```python
import torch

def _softmax(x, axis=-1):
    return torch.softmax(x, dim=axis)
def _gelu(x):
    return torch.nn.functional.gelu(x, approximate="tanh")
def _silu(x):
    return torch.nn.functional.silu(x)
def _sigmoid(x):
    return torch.sigmoid(x)
def _layernorm(x, axis=-1, eps=1e-5):
    m = x.mean(dim=axis, keepdim=True)
    v = x.var(dim=axis, keepdim=True, unbiased=False)
    return (x - m) / torch.sqrt(v + eps)
def _scatter_add(tgt, idx, src, axis=0):
    return tgt.clone().index_add_(axis, idx, src)
def _segment_sum(src, seg, n):
    out = torch.zeros((n,) + src.shape[1:], dtype=src.dtype)
    return out.index_add_(0, seg, src)
def _pad(x, pw):
    flat = tuple(v for pair in reversed(pw) for v in pair)
    return torch.nn.functional.pad(x, flat)

kernel_name = 'standalone__self_sup_conv_stem_wavlm__speech'

def setup_inputs():

    _w = (lambda a: (lambda t: t.to(torch.bfloat16) if t.is_floating_point() else t)(torch.as_tensor(a)))
    B, T, D, K = 2, 16000, 512, 31
    scale = (K * D) ** -0.5
    return {
        "x": _w((lambda *s: torch.randn(*s, dtype=torch.bfloat16) * 0.05)(B, T, D)),
        "w": _w((lambda *s: torch.randn(*s, dtype=torch.bfloat16) * 0.05)(K, D, D) * scale),
        "gn_gamma": _w((lambda *s: torch.randn(*s, dtype=torch.bfloat16) * 0.05)(D) * 0.1 + 1.0),
        "gn_beta": _w((lambda *s: torch.randn(*s, dtype=torch.bfloat16) * 0.05)(D) * 0.1),
    }

def reference(x, w, gn_gamma, gn_beta):
    B, T, D = x.shape
    K = w.shape[0]
    s = 2
    L = (T - K) // s + 1
    h = torch.einsum('blc,cd->bld', x[:, 0:s * L:s, :], w[0])
    for i in range(1, K):
        h = h + torch.einsum('blc,cd->bld', x[:, i:i + s * L:s, :], w[i])
    mu = torch.einsum('bld->bd', h)[:, None, :] / L
    var = torch.einsum('bld->bd', (h - mu) ** 2)[:, None, :] / L
    h = (h - mu) / torch.sqrt(var + 1e-5)
    h = h * gn_gamma[None, None, :] + gn_beta[None, None, :]
    return _gelu(h)
```

```cpp
#include <hip/hip_runtime.h>
#include <stdint.h>

typedef __attribute__((ext_vector_type(8))) short bf16x8;
typedef __attribute__((ext_vector_type(4))) short s16x4;
typedef __attribute__((ext_vector_type(4))) float f32x4;
typedef __attribute__((ext_vector_type(2))) float f32x2;
typedef __attribute__((ext_vector_type(2))) __bf16 bf16x2v;
typedef __attribute__((ext_vector_type(4))) unsigned int u32x4;
typedef __attribute__((ext_vector_type(2))) unsigned int u32x2;
constexpr int NH = 6, NST = 3;
typedef __attribute__((ext_vector_type(8))) unsigned int u32x8;
typedef __attribute__((ext_vector_type(16))) unsigned int u32x16;
typedef __attribute__((ext_vector_type(8))) _Float16 f16x8;
typedef __attribute__((ext_vector_type(2))) _Float16 f16x2;
typedef __attribute__((ext_vector_type(2))) unsigned short u16x2;
typedef __attribute__((address_space(1))) unsigned long long gu64;
typedef __attribute__((address_space(1))) unsigned gu32;
typedef __attribute__((address_space(3))) void lds_void;
typedef __attribute__((address_space(3))) s16x4 lds_s16x4;

constexpr int T_ = 16000, D_ = 512, L_ = 7985;
constexpr int HOP = 50, NBLK = 160, ROWS = 2 * NBLK;

constexpr unsigned WS_S = 0;
constexpr unsigned SBLK = 4u << 20;
constexpr unsigned sbase(unsigned sl) { return sl * 131072 + (sl >> 4) * 8192; }
constexpr unsigned FLG = 32u * SBLK;
constexpr unsigned WS_GRP = WS_S + FLG + 16 * 8192;
constexpr unsigned WS_D = WS_S + FLG + 20 * 8192;
constexpr unsigned WS_L = WS_D + ROWS * 1024;
constexpr unsigned WS_Y = 40u << 20;
constexpr unsigned WS_STATS = 129u << 20;
constexpr unsigned WS_FA = WS_STATS + (1u << 17);
constexpr unsigned WS_PUB = WS_STATS + (1u << 19);
static_assert(WS_L + D_ * D_ * 2 <= WS_STATS && FLG % (16 * 8192) == 0, "workspace layout");

#define RLX_AGENT __ATOMIC_RELAXED, __HIP_MEMORY_SCOPE_AGENT

constexpr double COS64[17] = {1.0, 0.99518472667219688624, 0.98078528040323044913, 0.95694033573220886494,
    0.92387953251128675613, 0.88192126434835502971, 0.83146961230254523708, 0.77301045336273696081,
    0.70710678118654752440, 0.63439328416364549822, 0.55557023301960222474, 0.47139673682599764856,
    0.38268343236508977173, 0.29028467725446236764, 0.19509032201612826785, 0.09801714032956060199, 0.0};
constexpr double c64(int k) { k &= 63; if (k > 32) k = 64 - k; return k <= 16 ? COS64[k] : -COS64[32 - k]; }
constexpr double s64(int k) { return c64(k + 48); }
constexpr unsigned short bf_rne(double v) {
  const unsigned u = __builtin_bit_cast(unsigned, (float)v);
  return (unsigned short)((u + 0x7fffu + ((u >> 16) & 1u)) >> 16);
}
constexpr double bf_val(unsigned short h) { return (double)__builtin_bit_cast(float, (unsigned)h << 16); }
constexpr unsigned short hilo(double v, int lo) { return lo ? bf_rne(v - bf_val(bf_rne(v))) : bf_rne(v); }
constexpr double R3 = 0.86602540378443864676;
constexpr double dft(int c, int t, double sgn) {
  return (c >> 1) == 0 ? ((c & t) & 1 ? -1.0 : 1.0)
                       : ((c & 1) ? -0.5 * c64((c >> 1) * t) - R3 * sgn * s64((c >> 1) * t) : c64((c >> 1) * t));
}
constexpr double idft(int c, int t) {
  return (c >> 1) == 0 ? ((c & t) & 1 ? -1.0 : 1.0) / 64.0
                       : ((c & 1) ? -s64((c >> 1) * t) / R3 : c64((c >> 1) * t) * (4.0 / 3.0)) / 32.0;
}
constexpr int kidx(int g, int j) { return j < 4 ? 4 * g + j : 12 + 4 * g + j; }
struct Tables {
  unsigned short F[4][2][2][64][8];
  unsigned short V[2][4][64][8];
  unsigned short G[4][2][2][64][8];
};
constexpr Tables make_tables() {
  Tables tb{};
  for (int l = 0; l < 64; ++l)
    for (int j = 0; j < 8; ++j) {
      const int g = l >> 4, n = l & 15;
      for (int ct = 0; ct < 4; ++ct) {
        for (int ks = 0; ks < 2; ++ks)
          for (int h = 0; h < 2; ++h) tb.F[ct][ks][h][l][j] = hilo(dft(16 * ct + n, 32 * ks + kidx(g, j), -1.0), h);
        for (int p = 0; p < 2; ++p) {
          const int tap = 4 * g + (j & 3);
          tb.V[p][ct][l][j] = (p == 1 && tap == 15) ? (unsigned short)0 : hilo(dft(16 * ct + n, tap, 1.0), j >> 2);
        }
      }
      for (int tt = 0; tt < 4; ++tt)
        for (int ks = 0; ks < 2; ++ks)
          for (int h = 0; h < 2; ++h) {
            const int c = 32 * ks + kidx(g, j);
            tb.G[tt][ks][h][l][j] = hilo(idft(c, 16 * tt + (tt < 3 ? n : n & 1)), h);
          }
    }
  return tb;
}
__device__ const __attribute__((aligned(16))) Tables TAB = make_tables();

__device__ __forceinline__ unsigned pk_bf16(float lo, float hi) {
  return __builtin_bit_cast(unsigned, __builtin_convertvector(f32x2{lo, hi}, bf16x2v));
}
__device__ __forceinline__ float bf_lo(unsigned v) { return __uint_as_float(v << 16); }
__device__ __forceinline__ float bf_hi(unsigned v) { return __uint_as_float(v & 0xffff0000u); }
__device__ __forceinline__ u32x4 pack8(f32x4 a, f32x4 b) {
  return u32x4{pk_bf16(a[0], a[1]), pk_bf16(a[2], a[3]), pk_bf16(b[0], b[1]), pk_bf16(b[2], b[3])};
}
__device__ __forceinline__ unsigned pk_f16(float lo, float hi) {
  return (__builtin_bit_cast(unsigned, __builtin_convertvector(f32x2{lo, hi}, f16x2)) + 0x40004u) & 0xfff8fff8u;
}
__device__ __forceinline__ u32x4 pack8h(f32x4 a, f32x4 b) {
  return u32x4{pk_f16(a[0], a[1]), pk_f16(a[2], a[3]), pk_f16(b[0], b[1]), pk_f16(b[2], b[3])};
}
__device__ __forceinline__ unsigned add_h(const bf16x8& a, const bf16x8& b, int d) {
  const unsigned x = __builtin_bit_cast(u32x4, a)[d], y = __builtin_bit_cast(u32x4, b)[d];
  unsigned r = __builtin_bit_cast(unsigned, __builtin_bit_cast(f16x2, x) + __builtin_bit_cast(f16x2, y));
  return r;
}
__device__ __forceinline__ unsigned amax(unsigned m, unsigned v) {
  return __builtin_bit_cast(unsigned, __builtin_elementwise_max(__builtin_bit_cast(u16x2, m), __builtin_bit_cast(u16x2, v & 0x7fff7fffu)));
}
struct Rng { f16x2 hi = {0, 0}, lo = {0, 0}; };
__device__ __forceinline__ void widen(Rng& r, unsigned a, unsigned b) {
  const f16x2 x = __builtin_bit_cast(f16x2, a), y = __builtin_bit_cast(f16x2, b);
  r.hi = __builtin_elementwise_maximum(__builtin_elementwise_maximum(r.hi, x), y);
  r.lo = __builtin_elementwise_minimum(__builtin_elementwise_minimum(r.lo, x), y);
}
__device__ __forceinline__ unsigned amax(const Rng& r) { return amax(amax(0, __builtin_bit_cast(unsigned, r.hi)), __builtin_bit_cast(unsigned, r.lo)); }
__device__ __forceinline__ unsigned expo(unsigned m) { return max(m & 0xffffu, m >> 16) >> 7; }
__device__ __forceinline__ unsigned wave_max(unsigned v) {
  unsigned m = 0;
#pragma unroll 1
  for (int l = 0; l < 64; ++l) m = max(m, (unsigned)__builtin_amdgcn_readlane((int)v, l));
  return m;
}
__device__ __forceinline__ void scale8(bf16x8& v, int e) {
  u32x4 u = __builtin_bit_cast(u32x4, v);
#pragma unroll
  for (int d = 0; d < 4; ++d)
    u[d] = __builtin_amdgcn_perm(__float_as_uint(__builtin_amdgcn_ldexpf(bf_hi(u[d]), e)), __float_as_uint(__builtin_amdgcn_ldexpf(bf_lo(u[d]), e)), 0x07060302u);
  v = __builtin_bit_cast(bf16x8, u);
}
__device__ __forceinline__ void restart(unsigned id) {
  asm volatile("s_waitcnt vmcnt(0)\n\ts_barrier\n\ts_branch conv_fft" :: "{s[0:1]}"(__builtin_amdgcn_kernarg_segment_ptr()), "{s2}"(id), "{v0}"(threadIdx.x) : "memory");
}
__device__ __forceinline__ s16x4 tr_read(const char* p) {
  return __builtin_amdgcn_ds_read_tr16_b64_v4i16((lds_s16x4*)p);
}
__device__ __forceinline__ bf16x8 cat(s16x4 a, s16x4 b) {
  const u32x2 x = __builtin_bit_cast(u32x2, a), y = __builtin_bit_cast(u32x2, b);
  return __builtin_bit_cast(bf16x8, u32x4{x[0], x[1], y[0], y[1]});
}
#define MFMA(a, b, c) __builtin_amdgcn_mfma_f32_16x16x32_bf16(a, b, c, 0, 0, 0)
#define MFMH(a, b, c) __builtin_amdgcn_mfma_f32_16x16x32_f16(__builtin_bit_cast(f16x8, a), __builtin_bit_cast(f16x8, b), c, 0, 0, 0)
#define SB0 __builtin_amdgcn_sched_barrier(0)
#define ZERO4 (f32x4{0.f, 0.f, 0.f, 0.f})
#define DMA16(POL, rs, lds, voff, soff) \
  asm volatile("s_mov_b32 m0, %0\n\ts_nop 0\n\tbuffer_load_dwordx4 %1, %2, %3 offen " POL " lds" \
               :: "s"(lds), "v"(voff), "s"(rs), "s"(soff) : "memory")
__device__ __forceinline__ void st16(const u32x4& v, const u32x4& rs, unsigned voff, unsigned soff) {
  asm volatile("buffer_store_dwordx4 %0, %1, %2, %3 offen sc1\n\ts_nop 1" :: "v"(v), "v"(voff), "s"(rs), "s"(soff) : "memory");
}
__device__ __forceinline__ void ld16(bf16x8& v, const void* p) {
  asm volatile("global_load_dwordx4 %0, %1, off" : "=v"(v) : "v"(p) : "memory");
}
template <int N> __device__ __forceinline__ void wait_vm() { asm volatile("s_waitcnt vmcnt(%0)" :: "i"(N) : "memory"); }
__device__ __forceinline__ u32x4 make_rsrc(const void* p, unsigned bytes) {
  const unsigned long long a = (unsigned long long)p;
  return u32x4{(unsigned)a, (unsigned)(a >> 32) & 0xffffu, bytes, 0x00020000u};
}
__device__ __forceinline__ void spin_flags(const char* p) {
  for (unsigned spins = 0; !__all(__hip_atomic_load((const gu32*)p, RLX_AGENT) != 0u) && spins < (1u << 21); ++spins)
    __builtin_amdgcn_s_sleep(3);
}

constexpr int GW = 10;
constexpr int NQ1 = GW / 2, NQW = 16 - NQ1;
constexpr int WSL = 16384, XSL = 20480, XRING = 3 * WSL;
constexpr int RAWB = 67 * 1024, XT1 = RAWB + NQ1 * 4096, SINK = XT1 + RAWB;
constexpr int RAWL = XRING + 3 * XSL;
constexpr int CORR = RAWL + 4096, CORD = RAWL + NQW * 4096;
constexpr int GAM = RAWL + 52224, CBUF = RAWL + 49152;

struct Ctx {
  char* smem;
  char* ws;
  const unsigned short *gamma, *beta;
  u32x4 rs;
  unsigned ldg, ldf, sg, sf;
  unsigned voff, voff5, crd;
  unsigned xbase, wbase;
  unsigned pwr, prd, poff;
  unsigned xo;
  unsigned lane4, fvo;
  unsigned id;
  unsigned tag[2];
  int es;
  int wv, vb;
  unsigned long long pc;
  unsigned pfo, pft;
};
__device__ __forceinline__ void code_touch(const Ctx& cx) {
  if ((cx.wv ^ (cx.vb >> 3)) & 3) return;
#pragma unroll
  for (int h = 0; h < 2; ++h)
    asm volatile("s_mov_b32 m0, %0\n\ts_nop 0\n\tglobal_load_lds_dword %1, %2" :: "s"(SINK + cx.wv * 256), "v"(min(__builtin_amdgcn_mbcnt_hi(~0u, __builtin_amdgcn_mbcnt_lo(~0u, cx.wv * 64u + h * 512u)) * 128u, cx.pfo)), "s"(cx.pc) : "memory");
}
__device__ __forceinline__ void x_store(const Ctx& cx, const u32x4& v, unsigned kb, unsigned blk) {
  st16(v, cx.rs, cx.xo, kb + (blk + (blk & 192u)) * 128u);
}
template <int NM>
__device__ __forceinline__ void stage_issue(const Ctx& cx, int kc) {
  const unsigned slot = (unsigned)(kc + 1) * (NM == 6 ? WSL : XSL);
  if (kc >= 0 || NM == 6 || !(cx.wv & 1))
    asm volatile("s_mov_b32 m0, %0\n\ts_nop 0\n\tbuffer_load_dwordx4 %1, %2, %3 offen sc1 lds\n\tbuffer_load_dwordx4 %1, %2, %3 offen offset:1024 sc1 lds\n\t"
                 "buffer_load_dwordx4 %1, %2, %3 offen offset:2048 sc1 lds\n\tbuffer_load_dwordx4 %1, %2, %3 offen offset:3072 sc1 lds"
                 :: "s"(cx.ldg + slot), "v"(cx.voff), "s"(cx.rs), "s"(cx.sg + kc * SBLK) : "memory");
  if (kc >= 0 && NM == 5) DMA16("sc1", cx.rs, cx.ldf + slot, cx.voff, cx.sf);
}
template <int V> struct IC { static constexpr int v = V; constexpr operator int() const { return V; } };
template <int A, int B, class F> __device__ __forceinline__ void sfor(F f) {
  if constexpr (A < B) { f(IC<A>{}); sfor<A + 1, B>(f); }
}
struct RQ { u32x4 h[NST + NH]; u32x2 raw[NQW]; bf16x8 vb; unsigned s[32], v[19]; };

__device__ __forceinline__ int run_bs(int vb, int t) { return 5 * ((vb >> 4) + 16 * ((vb ^ t) & 1)); }
__device__ __forceinline__ int run_q(int vb, int t) { return (((vb >> 1) & 1) + ((vb >> 2) & 2) + 2 * t) & 3; }
__device__ __forceinline__ unsigned x_addr(int vb, int t, int wv) {
  const int lane = threadIdx.x & 63, q = run_q(vb, t);
  const unsigned c = (unsigned)((lane & 15) ^ (((4 * wv + (lane >> 4)) & 7) << 1));
  return (unsigned)(2 * (HOP * run_bs(vb, t) + 4 * wv + (lane >> 4)) + ((t ^ q ^ (q >> 1)) & 1)) * 1024u + c * 16 + q * 256;
}
__device__ __forceinline__ u32x4 x_rsrc(const unsigned short* x, int vb) { return make_rsrc(x + (size_t)((vb >> 2) & 1) * T_ * D_, T_ * D_ * 2); }
__device__ __forceinline__ void issue_x(int t, int wv, unsigned a, const u32x4& rx, unsigned lds0, int j0 = 0, int j1 = 9) {
#pragma unroll
  for (int jj = 0; jj < 9; ++jj)
    if (jj >= j0 && jj < j1 && (jj < 8 || wv < 3)) DMA16("nt", rx, lds0 + t * XT1 + wv * 1024 + jj * 8192, a, jj * 65536u);
}
struct XR { s16x4 t[8]; };
constexpr int P0 = 3, P1 = 23;
constexpr int PG = 20, NFA = 1;
constexpr int TE = 4;
constexpr int NIT = 23, NS = 30 - NH - NST, NA = 7, NB = 13;
constexpr int ord(int p) { return p < 0 || p >= NIT ? -1 : p == 0 ? 0 : p < 6 ? p + 2 : p < 8 ? p - 5 : p; }
constexpr int st_w(int j) { return j < 2 ? j : j < 7 ? -1 : j < 15 ? j - 5 : -1; }
constexpr int st_x(int j) { return j < 7 ? j - 2 : j - 10; }
constexpr int st_pos(int j) { return st_w(j) >= 0 ? (j < 9 ? 0 : j < 13 ? 6 : 7) : j < 7 ? j - 1 : j - 7; }
constexpr int st_first(int p) {
  int j = p <= P0 ? 0 : p >= P1 ? NS : (p - P0) * NS / (P1 - P0);
  while (j > 0 && p <= NIT && st_pos(j - 1) + 2 > p - 1) --j;
  return j;
}
template <int NM, class F>
__device__ __forceinline__ void phase1(Ctx& cx, const unsigned short* __restrict__ x, const unsigned short* __restrict__ w,
                                       RQ& rqv, unsigned& xs, bf16x8& vb_, F setup) {
  u32x2 rq[NQW];
  static_assert(NQ1 == 5, "phase-1 wait counts assume five quanta");
  char* smem = cx.smem;
  const unsigned lds0 = (unsigned)(uintptr_t)(lds_void*)smem;
  const int idx = cx.vb >> 3, wv = cx.wv, lane = threadIdx.x & 63;
  const u32x4 rw = make_rsrc(w, 31 * D_ * D_ * 2);
  const int tap = threadIdx.x >> 5, pc = threadIdx.x & 31;
  const unsigned roff = (unsigned)(tap * (2 * D_ * D_ * 2) + pc * 8);
  const int ng = wv & 3, mg = wv >> 2;
  const int g = lane >> 4, qq = (lane >> 2) & 3, pp = lane & 3;
  u32x2 r1[NQ1];
  bf16x8 fb[2][2];
  unsigned long long tag;
  const char* rec = cx.ws + WS_PUB + ((cx.id & 7) * 8 + wv) * 16;
  u32x4 pr, wl;
  {
    const int vb = cx.vb;
    const int kr = (vb >> 7) * 32 + (vb & 4) * 4 + (idx & 15);
    auto raw_load = [&](u32x2& r, int q) {
      const unsigned so = (unsigned)(kr + 64 * (2 * (q & 3) + ((q >> 2) & 1)) + 512 * (((q >> 3) ^ q ^ (q >> 1)) & 1)) * 1024u + (unsigned)((vb + q) & 3) * 256u;
      asm volatile("buffer_load_dwordx2 %0, %1, %2, %3 offen nt" : "=v"(r) : "v"(roff), "s"(rw), "s"(so) : "memory");
    };
#pragma unroll
    for (int k = 0; k < NQ1; ++k) raw_load(r1[k], k);
    const unsigned xa1 = x_addr(vb, 1, wv);
    issue_x(1, wv, xa1, x_rsrc(x, vb), lds0, 0, TE);
    if (NM == 6 && (cx.id | wv) == 0) {
      unsigned g[2] = {~0u, ~0u};
#pragma unroll 1
      for (int k = -1; k < 16 && ((k & 1) || min(g[0], g[1]) > 440u); ++k) {
        unsigned long long t0, t1;
        unsigned d;
        asm volatile("s_memtime %0\n\ts_waitcnt lgkmcnt(0)\n\ts_load_dword %2, %3, 0x0\n\ts_waitcnt lgkmcnt(0)\n\ts_memtime %1\n\ts_waitcnt lgkmcnt(0)"
                     : "=&s"(t0), "=&s"(t1), "=&s"(d) : "s"(rec + 32768 + (k < 0 ? 128 : (k & 1) * 8192 + (k & 2) * 2048 + (k & 4) * 4096 + (k & 8) * 128)) : "memory");
        if (k >= 0) { if (k & 1) g[1] = min(g[1], (unsigned)(t1 - t0)); else g[0] = min(g[0], (unsigned)(t1 - t0)); }
      }
      const unsigned lo = cx.tag[0] | (((g[0] < g[1] ? 0u : 1u) ^ (cx.vb >> 2)) & 1u), hi = cx.tag[1];
      asm volatile("global_store_dwordx4 %0, %1, %2 sc1\n\ts_nop 1" :: "v"(lane * 16 + 2048), "v"(u32x4{lo, hi, lo, hi}), "s"(rec) : "memory");
    }
    {
#pragma unroll
      for (int i = 0; i < 4; ++i) ld16(fb[i >> 1][i & 1], TAB.F[ng][i >> 1][i & 1][lane]);
      ld16(vb_, TAB.V[0][ng][lane]);
      asm volatile("global_load_dwordx4 %0, %1, %2 sc1" : "=v"(pr) : "v"(0u), "s"(rec) : "memory");
    }
    if (NM == 5 && wv >= 6)
      asm volatile("buffer_load_dwordx4 %0, %1, %2, %3 offen nt" : "=v"(wl) : "v"((threadIdx.x & 127) * 16), "s"(rw), "s"(30 * D_ * D_ * 2 + vb * 2048) : "memory");
    if (cx.id == 0 && wv >= TE - 1)
      DMA16("nt", x_rsrc(x, vb), lds0 + XT1 + (wv + 1) * 8192, x_addr(vb, 1, 0), (wv + 1) * 65536u);
    if (NM == 5 || (cx.id | wv)) issue_x(1, wv, xa1, x_rsrc(x, vb), lds0, TE, 9);
#pragma unroll
    for (int k = 0; k < NQW; ++k) raw_load(rq[k], NQ1 + k);
    {
      const int bid = vb, xb = (bid >> 2) & 1, krow = kr;
      const int slot = (bid & 7) * 4 + (idx >> 3), b = (idx >> 2) & 1, nt = idx & 3;
      const int rt = 4 * g + qq;
      cx.lane4 = lane * 4;
      cx.fvo = NM == 5 ? (unsigned)((bid & 7) * 1024 + ng * 256 + lane * 4) : wv == 0 && lane < 8 ? (unsigned)(lane * 1024 + (bid >> 2) * 4) : 0x80000000u;
      cx.pwr = (unsigned)(RAWB + tap * 256 + (((pc >> 2) ^ (tap & 7)) << 5) + (pc & 3) * 8);
      cx.prd = (unsigned)(RAWB + rt * 256 + (((4 * mg) ^ (rt & 7)) << 5) + pp * 8);
      cx.poff = (unsigned)(sbase(8 * ng + ((lane >> 1) & 7)) + (krow >> 5) * SBLK + (krow & 24) * 512 + (krow & 7) * 128 +
                           mg * 2048 + (lane & 1) * 64 + g * 16);
      cx.xo = (unsigned)(sbase(8 * ng + ((lane >> 1) & 7)) + mg * SBLK + 65536 + xb * 8192 + (lane & 1) * 64 + g * 16);
      cx.xbase = (unsigned)(XRING + mg * 5 * 2048 + (lane & 8) * 128 + (lane & 7) * 64 + ((g ^ ((lane >> 1) & 2)) << 4));
      cx.wbase = (unsigned)((rt >> 3) * 4096 + ng * 1024 + (rt & 7) * 64 + (rt & 4) * 8 + pp * 8);
      const int pat = ((lane >> 2) & 7) * 128 + (lane >> 5) * 64 + (((lane & 3) ^ ((lane >> 3) & 2)) << 4);
      if (NM == 6) {
        cx.voff = (unsigned)(sbase(slot) + nt * 16384 + ng * 4096 + pat);
        cx.ldg = lds0 + ng * 4096;
        cx.ldf = lds0 + CORR + ng * 1024;
        cx.sg = SBLK;
        cx.sf = 0;
        const int kr8 = lane >> 3;
        cx.voff5 = (unsigned)(WS_L - WS_S + (8 * ng + kr8) * 1024 + (nt * 2 + xb) * 128 + (((lane & 7) ^ ((kr8 & 2) | (ng & 1) << 2)) << 4));
        cx.crd = (unsigned)(CORR + (8 * g + qq) * 128 + ((ng ^ ((qq >> 1) | (g & 1) << 1)) << 5) + pp * 8);
      } else {
        const int rg = (bid & 1) * 8 + (slot & 1) * 4 + (bid & 2) + (idx >> 4);
        cx.voff = (unsigned)(sbase(slot) + 65536 + b * 8192 + pat);
        cx.ldg = lds0 + XRING + (ng + (ng >> 1)) * 4096;
        cx.ldf = lds0 + XRING + 8192 + ng * 1024;
        cx.sg = (~ng & 1) * SBLK + (ng + (ng >> 1) * 3 + ((ng + 1) >> 2) * 2) * 4096;
        cx.sf = 16384 + ng * 1024;
        cx.voff5 = (unsigned)(WS_D - WS_S + (b * NBLK + 10 * rg + min(lane & 15, 9)) * 1024 + ng * 64 + g * 16);
      }
    }
    asm volatile("" : "+v"(cx.fvo), "+v"(cx.poff), "+v"(cx.xo), "+v"(cx.voff), "+v"(cx.voff5), "+v"(cx.lane4));
  }
  Rng gx, gw;
  auto scan = [&](int t) __attribute__((always_inline)) {
    u32x4 v[9];
#pragma unroll
    for (int j = 0; j < 9; ++j)
      if (j < 8 || NM == 6) v[j] = *reinterpret_cast<const u32x4*>(smem + t * XT1 + (j < 8 || wv < 3 ? j : 7) * 8192 + threadIdx.x * 16);
    SB0;
#pragma unroll
    for (int j = 0; j < 9; ++j)
      if (j < 8 || NM == 6) {
        widen(gx, v[j][0], v[j][1]);
        widen(gx, v[j][2], v[j][3]);
      }
    asm volatile("" : "+v"(gx.hi), "+v"(gx.lo));
    SB0;
  };
  if (NM == 6 && (cx.id | wv) == 0) wait_vm<NQW>();
  wait_vm<8 + 6 + NQW>();
  unsigned near;
  scan(0);
#pragma unroll
  for (int q = 0; q < NQ1; ++q) {
    asm volatile("" : "+v"(r1[q]));
    widen(gw, r1[q][0], r1[q][1]);
  }
  {
    wait_vm<NQW + 8 - TE>();
    unsigned d0, d1;
    for (unsigned spins = 0;; ++spins) {
      asm volatile("" : "+v"(pr));
      d0 = __builtin_amdgcn_readfirstlane(pr[0]);
      d1 = __builtin_amdgcn_readfirstlane(pr[1]);
      const unsigned d2 = __builtin_amdgcn_readfirstlane(pr[2]), d3 = __builtin_amdgcn_readfirstlane(pr[3]);
      if (__builtin_expect((d0 == ~d2 && d1 == ~d3) || spins > (1u << 21), 1)) break;
      __builtin_amdgcn_s_sleep(2);
      asm volatile("global_load_dwordx4 %0, %1, %2 sc1\n\ts_waitcnt vmcnt(0)" : "=v"(pr) : "v"(0u), "s"(rec) : "memory");
    }
    tag = d0 | (unsigned long long)d1 << 32;
    const unsigned vn = (cx.id + (d0 >> 1)) & 7u;
    if (__builtin_expect(vn != (cx.vb & 7u), 0)) restart(cx.id | 256u | vn << 10);
  }
  const unsigned ee = (cx.id & 512u ? cx.id : (unsigned)(tag >> 32)) >> 16, ex = ee & 255u, ew = ee >> 8;
  cx.es = 256 - (int)(ex + ew);
  char* ws;
  const int bid = cx.vb;
  bf16x8& vb = vb_;
  u32x4 f0, f1;
  char* fab;
  const u32x4* fa;
  u32x2 ov;
  unsigned pollv;
  const int rb[2] = {run_bs(bid, 0), run_bs(bid, 1)};
  unsigned xb0[4];
#pragma unroll
  for (int k = 0; k < 4; ++k) xb0[k] = (unsigned)((4 * g + qq) * 256 + (((2 * mg) ^ ((4 * g + qq + 2 * k) & 7)) << 5) + pp * 8);
#pragma unroll
  for (int q = 0; q < NQ1; ++q) *reinterpret_cast<u32x2*>(smem + cx.pwr + q * 4096) = r1[q];
  asm volatile("" : "+v"(fb[0][0]), "+v"(fb[0][1]), "+v"(fb[1][0]), "+v"(fb[1][1]), "+v"(vb));
  scale8(vb, 128 - (int)ew);
#pragma unroll
  for (int i = 0; i < 4; ++i) scale8(fb[i >> 1][i & 1], 128 - (int)ex);
  asm volatile("" : "+v"(fb[0][0]), "+v"(fb[0][1]), "+v"(fb[1][0]), "+v"(fb[1][1]), "+v"(vb));
  asm volatile("s_waitcnt lgkmcnt(0)" : "+v"(xb0[0]), "+v"(xb0[1]), "+v"(xb0[2]), "+v"(xb0[3]) :: "memory");
  wait_vm<NQW>();
  asm volatile("global_load_dwordx2 %0, %1, %2 sc1" : "=v"(ov) : "v"(2048u), "s"(rec) : "memory");
  __builtin_amdgcn_s_barrier();
  SB0;
  u32x4 hw[6][2], hx[20];
  f32x4 pend[2];
  auto rd = [&](int it, int i) __attribute__((always_inline)) {
    if (it < 3) return tr_read(smem + ((cx.prd ^ (unsigned)((i & 3) << 5)) + (2 * it + (i >> 2)) * 4096));
    const int n = it - 3;
    const unsigned base = (xb0[n % 5 & 3] ^ (unsigned)((4 * ((n / 5) & 1) + (i & 1)) << 5)) + n / 10 * XT1;
    return tr_read(smem + (base + n % 5 * (HOP * 256) + (i >> 1) * (16 * 256)));
  };
  auto dst = [&](int it) -> u32x4& { return it < 3 ? hw[it == 2 ? 4 : 2 * it + 1][1] : hx[it - 3]; };
  auto item = [&](int it, int nx, int pv, XR& xr, auto side) __attribute__((always_inline)) {
    const XR c = xr;
    const int nm = it == 2 ? 4 : 8, nr = nx == 2 ? 4 : 8;
    f32x4 r[8];
#pragma unroll
    for (int i = 0; i < 8; ++i) {
      if (i >= nm) break;
      const int s2 = i & 1, ks = (i >> 1) & 1;
      if (it < 3) r[i] = MFMA(cat(c.t[i], c.t[i]), vb, ZERO4);
      else r[i] = MFMA(cat(c.t[4 * ks + s2], c.t[4 * ks + 2 + s2]), fb[ks][i < 4], i < 2 ? ZERO4 : r[i - 2]);
      SB0;
      if (nx >= 0) {
#pragma unroll
        for (int j = 0; j < 8; ++j) if (j < nr && j * nm / nr == i) xr.t[j] = rd(nx, j);
      }
      if (pv >= 0 && (nm == 4 ? i >= 2 : i >= 1 && i <= 4)) {
        u32x4& d = dst(pv);
#pragma unroll
        for (int e = 0; e < 4; ++e)
          if ((nm == 4 ? 2 + (e >> 1) : 1 + e) == i) d[e] = pk_f16(pend[e >> 1][(e & 1) * 2], pend[e >> 1][(e & 1) * 2 + 1]);
        if (i == (nm == 4 ? 3 : 4)) asm volatile("" : "+v"(d));
      }
      if (it < 3 && i >= 3 && (i & 1)) {
        u32x4& d = hw[2 * it + ((i - 3) >> 2)][((i - 3) >> 1) & 1];
        d = pack8h(r[i - 3], r[i - 2]);
        asm volatile("" : "+v"(d));
      }
      side(i * (8 / nm));
      if (nm == 4) side(2 * i + 1);
      SB0;
    }
    pend[0] = r[nm - 2];
    pend[1] = r[nm - 1];
  };
  const unsigned so0 = (unsigned)(2 * run_q(bid, 0)) * SBLK, so1 = (unsigned)(16 + 2 * run_q(bid, 1)) * SBLK;
  auto store = [&](int j) __attribute__((always_inline)) {
    if (j == 0) {
      wait_vm<0>();
      for (unsigned spins = 0;; ++spins) {
        asm volatile("" : "+v"(ov));
        near = __builtin_amdgcn_readfirstlane(ov[0]) ^ (unsigned)tag;
        if (__builtin_expect((near < 2u && __builtin_amdgcn_readfirstlane(ov[1]) == (unsigned)(tag >> 32)) || spins > (1u << 21), 1)) break;
        __builtin_amdgcn_s_sleep(2);
        asm volatile("global_load_dwordx2 %0, %1, %2 sc1\n\ts_waitcnt vmcnt(0)" : "=v"(ov) : "v"(2048u), "s"(rec) : "memory");
      }
      ws = cx.ws += (near & 1u) * 8192;
      cx.rs = make_rsrc(ws + WS_S, WS_L - WS_S + D_ * D_ * 2);
      fab = ws + WS_FA + (cx.id & 512u) * 32;
      fa = reinterpret_cast<const u32x4*>(fab + (bid & 7) * 2048) + lane;
      if (NM == 6 && wv == 1) {
        const unsigned o = (lane < 32 ? WS_STATS : WS_S + FLG + 16384 - 512) + bid * 512 + lane * 16;
        const unsigned v = lane < 32 ? ~0u : 0u;
        asm volatile("global_store_dwordx4 %0, %1, %2 sc1\n\ts_nop 1" :: "v"(o), "v"(u32x4{v, v, v, v}), "s"(ws) : "memory");
        if (lane < 8) __hip_atomic_store((gu32*)(ws + WS_S + FLG + lane * 1024 + ((bid + 2) & 3) * 256 + (bid >> 2) * 4), 0u, RLX_AGENT);
      }
    }
    if (j == NB + 1 && NM == 6) {
      if (((bid >> 1) ^ (bid >> 3) ^ 1) & 1 && threadIdx.x < 160) {
        const int t = threadIdx.x >= 80, blk = (threadIdx.x >> 4) - 5 * t;
        const unsigned o = (unsigned)(t * XT1 + blk * (HOP * 256) + (((lane & 15) ^ ((blk & 3) << 2)) << 4));
        const u32x4 lo = *reinterpret_cast<const u32x4*>(smem + o), hi = *reinterpret_cast<const u32x4*>(smem + o + 64 * 256);
        u32x4 d;
#pragma unroll
        for (int e = 0; e < 4; ++e) d[e] = pk_bf16(bf_lo(hi[e]) - bf_lo(lo[e]), bf_hi(hi[e]) - bf_hi(lo[e]));
        st16(d, cx.rs, (unsigned)((((bid >> 2) & 1) * NBLK + (t ? rb[1] : rb[0]) + blk) * 1024 + run_q(bid, t) * 256 + (lane & 15) * 16), WS_D - WS_S);
      }
    }
    if (j == NB + 1 && NM == 5) {
      if (wv >= 6) {
        asm volatile("" : "+v"(wl));
        st16(wl, cx.rs, (threadIdx.x & 127) * 16, WS_L - WS_S + bid * 2048);
      }
    }
    const int wq = st_w(j), xn = st_x(j);
    if (wq >= 0) st16(hw[wq >> 1][wq & 1], cx.rs, cx.poff, (unsigned)(wq >> 1) * (2 * SBLK) + (unsigned)((bid + (wq >> 1)) & 3) * 16384u + (wq & 1) * 1024u);
    else x_store(cx, hx[xn], (xn < 10 ? so0 : so1) + ((xn / 5) & 1) * (8 * SBLK), (unsigned)(rb[xn / 10] + xn % 5));
    if (j == NA + NFA - 1) {
      scan(1);
#pragma unroll
      for (int k = 0; k < NQW; ++k) {
        asm volatile("" : "+v"(rq[k]));
        widen(gw, rq[k][0], rq[k][1]);
      }
      const unsigned mx = amax(gx), mw = amax(gw);
      if (__builtin_expect(__any(expo(mx) > ex + 7 || expo(mw) > ew + 9) && !(cx.id & 512u), 0)) {
        const u32x2 m = {wave_max(expo(mx)), wave_max(expo(mw))};
        asm volatile("global_atomic_umax %0, %1, %3 sc1\n\tglobal_atomic_umax %0, %2, %3 offset:4 sc1\n\ts_waitcnt vmcnt(0)"
                     :: "v"(lane * 16 + 1024), "v"(m[0]), "v"(m[1]), "s"(rec - ((cx.id & 7) * 8 + wv) * 16) : "memory");
      }
    }
    if (j == NA + NFA - 1 || j == NB + NFA - 1) {
      wait_vm<NFA>();
      __builtin_amdgcn_s_barrier();
      if (wv == 0 && lane < 8) {
        if (j < NB) __hip_atomic_store((gu64*)(fab + lane * 2048) + bid, tag, RLX_AGENT);
        else __hip_atomic_store((gu32*)(ws + WS_S + FLG + lane * 1024 + ((bid + 2) & 3) * 256 + (bid >> 2) * 4), 1u, RLX_AGENT);
      }
    }
    SB0;
  };
  XR xr;
#pragma unroll
  for (int j = 0; j < 8; ++j) xr.t[j] = rd(0, j);
  SB0;
  static_assert(st_first(NIT) == NS && PG >= 14 && st_first(PG - 2) >= NB + 2 && NA + NFA <= NB && NFA >= 1 && NH <= 7, "order of the hand-off steps");
  sfor<0, NIT>([&](auto p) __attribute__((always_inline)) {
    constexpr int j0 = st_first(p.v), j1 = st_first(p.v + 1);
    if (p >= 12 && p < PG) { if (!(bid & 1) && cx.id) __builtin_amdgcn_s_sleep(6); }
    if (p == PG - 2) {
      code_touch(cx);
      if (NM == 6 && wv == 0)
        asm volatile("global_load_dwordx4 %0, %2, off sc1\n\tglobal_load_dwordx4 %1, %2, off offset:1024 sc1" : "=&v"(f0), "=&v"(f1) : "v"(fa) : "memory");
    }
    if (p == PG) {
      if (NM == 6 && wv == 0) {
        wait_vm<st_first(PG) - st_first(PG - 2)>();
        for (unsigned spins = 0; spins < (1u << 21); ++spins) {
          asm volatile("" : "+v"(f0), "+v"(f1));
          const unsigned t0 = (unsigned)tag, t1 = (unsigned)(tag >> 32);
          if (__all(f0[0] == t0 && f0[1] == t1 && f0[2] == t0 && f0[3] == t1 && f1[0] == t0 && f1[1] == t1 && f1[2] == t0 && f1[3] == t1)) break;
          __builtin_amdgcn_s_sleep(2);
          asm volatile("global_load_dwordx4 %0, %2, off sc1\n\tglobal_load_dwordx4 %1, %2, off offset:1024 sc1\n\ts_waitcnt vmcnt(0)"
                       : "=&v"(f0), "=&v"(f1) : "v"(fa) : "memory");
        }
      }
      __builtin_amdgcn_s_barrier();
      stage_issue<NM>(cx, -1);
      asm volatile("global_load_dwordx2 %0, %1, %2 sc1" : "=v"(ov) : "v"(1024u), "s"(rec) : "memory");
      stage_issue<NM>(cx, 0);
      SB0;
    }
    if (p == NIT - 1 && NM == 5) {
      asm volatile("buffer_load_dword %0, %1, %2, %3 offen sc1" : "=v"(pollv) : "v"(cx.fvo), "s"(cx.rs), "s"(FLG) : "memory");
    }
    item(ord(p), ord(p + 1), ord(p - 1), xr, [&](int i) __attribute__((always_inline)) {
      if ((i * (j1 - j0) + 7) / 8 < ((i + 1) * (j1 - j0) + 7) / 8) store(j0 + (i * (j1 - j0) + 7) / 8);
    });
  });
  hx[19] = pack8h(pend[0], pend[1]);
  xs = so1 + (unsigned)rb[1];
#pragma unroll
  for (int e = 0; e < NST + NH; ++e) rqv.h[e] = hx[20 - NH - NST + e];
  asm volatile("" : "+v"(rqv.h[NST + NH - 1]));
  setup();
  wait_vm<NM == 5 ? NS - st_first(NIT - 1) : 10 - NM + NS - st_first(PG)>();
  {
    asm volatile("" : "+v"(ov));
    const unsigned ox = __builtin_amdgcn_readfirstlane(ov[0]), ow = __builtin_amdgcn_readfirstlane(ov[1]);
    if (__builtin_expect((ox | ow) && !(cx.id & 512u), 0))
      restart((cx.id & 255u) | 768u | (bid & 7u) << 10 | min(max(ex + 7, ox) - 7, 220u) << 16 | min(max(ew + 9, ow) - 9, 220u) << 24);
    if (NM == 6 && (cx.id & 255u) == 0 && wv == 0)
      asm volatile("global_store_dwordx4 %0, %1, %2 sc1\n\ts_nop 1" :: "v"(lane * 16), "v"(u32x4{0u, 0u, 0u, 0u}), "s"(rec) : "memory");
  }
  if (NM == 5) {
    asm volatile("" : "+v"(pollv));
    if (!__all(pollv != 0u)) spin_flags(ws + WS_S + FLG + cx.fvo);
  }
#pragma unroll
  for (int k = 0; k < NQW; ++k) rqv.raw[k] = rq[k];
}

#define LOOP0 \
"s_mov_b32 s91, 0x40004\n" \
"s_mov_b32 s92, 0xfff8fff8\n" \
"ds_read_b64_tr_b16 v[120:121], v241 offset:0\n" \
"ds_read_b64_tr_b16 v[122:123], v241 offset:8192\n" \
"ds_read_b64_tr_b16 v[124:125], v242 offset:0\n" \
"ds_read_b64_tr_b16 v[126:127], v242 offset:8192\n" \
"ds_read_b64_tr_b16 v[128:129], v241 offset:512\n" \
"ds_read_b64_tr_b16 v[130:131], v241 offset:8704\n" \
"ds_read_b64_tr_b16 v[132:133], v242 offset:512\n" \
"ds_read_b64_tr_b16 v[134:135], v242 offset:8704\n" \
"ds_read_b128 v[160:163], v240 offset:0\n" \
"ds_read_b128 v[164:167], v240 offset:512\n" \
"ds_read_b128 v[168:171], v240 offset:2048\n" \
"ds_read_b128 v[172:175], v240 offset:2560\n" \
"s_add_i32 m0, s44, 0x8000\n" \
"s_add_i32 s80, s46, 0x400000\n" \
"buffer_load_dwordx4 v232, s[36:39], s80 offen sc1 lds\n" \
"buffer_load_dwordx4 v232, s[36:39], s80 offen offset:1024 sc1 lds\n" \
"buffer_load_dwordx4 v232, s[36:39], s80 offen offset:2048 sc1 lds\n" \
"buffer_load_dwordx4 v232, s[36:39], s80 offen offset:3072 sc1 lds\n" \
"s_waitcnt vmcnt(8) lgkmcnt(0)\n" \
"s_barrier\n" \
"v_mfma_f32_16x16x32_f16 v[0:3], v[120:123], v[160:163], 0\n" \
"v_pk_add_f16 v136, v120, v128\n" \
"v_pk_add_f16 v137, v121, v129\n" \
"v_pk_add_f16 v138, v122, v130\n" \
"v_pk_add_f16 v139, v123, v131\n" \
"ds_read_b128 v[176:179], v240 offset:24576\n" \
"v_mfma_f32_16x16x32_f16 v[12:15], v[124:127], v[160:163], 0\n" \
"v_pk_add_f16 v160, v160, v164\n" \
"v_pk_add_f16 v161, v161, v165\n" \
"v_pk_add_f16 v140, v124, v132\n" \
"v_mfma_f32_16x16x32_f16 v[4:7], v[128:131], v[164:167], 0\n" \
"v_pk_add_f16 v162, v162, v166\n" \
"v_pk_add_f16 v163, v163, v167\n" \
"v_pk_add_f16 v141, v125, v133\n" \
"buffer_store_dwordx4 v[208:211], v235, s[36:39], s63 offen sc1\n" \
"v_mfma_f32_16x16x32_f16 v[16:19], v[132:135], v[164:167], 0\n" \
"v_pk_add_f16 v142, v126, v134\n" \
"v_pk_add_f16 v143, v127, v135\n" \
"ds_read_b128 v[180:183], v240 offset:25088\n" \
"s_add_i32 m0, s44, 0x0\n" \
"s_add_i32 s80, s46, 0x800000\n" \
"v_mfma_f32_16x16x32_f16 v[8:11], v[136:139], v[160:163], 0\n" \
"buffer_load_dwordx4 v232, s[36:39], s80 offen sc1 lds\n" \
"ds_write_b64 v246, v[96:97] offset:0\n" \
"v_mfma_f32_16x16x32_f16 v[20:23], v[140:143], v[160:163], 0\n" \
"buffer_store_dwordx4 v[72:75], v235, s[36:39], s60 offen sc1\n" \
"ds_read_b64_tr_b16 v[144:145], v241 offset:16384\n" \
"v_mfma_f32_16x16x32_f16 v[24:27], v[120:123], v[168:171], 0\n" \
"ds_read_b128 v[160:163], v240 offset:26624\n" \
"ds_write_b64 v246, v[98:99] offset:4096\n" \
"ds_write_b64 v246, v[100:101] offset:8192\n" \
"v_mfma_f32_16x16x32_f16 v[36:39], v[124:127], v[168:171], 0\n" \
"v_pk_add_f16 v168, v168, v172\n" \
"v_pk_add_f16 v169, v169, v173\n" \
"ds_read_b64_tr_b16 v[146:147], v241 offset:24576\n" \
"v_mfma_f32_16x16x32_f16 v[28:31], v[128:131], v[172:175], 0\n" \
"v_pk_add_f16 v170, v170, v174\n" \
"v_pk_add_f16 v171, v171, v175\n" \
"v_mfma_f32_16x16x32_f16 v[40:43], v[132:135], v[172:175], 0\n" \
"ds_read_b128 v[164:167], v240 offset:27136\n" \
"ds_write_b64 v246, v[102:103] offset:12288\n" \
"v_mfma_f32_16x16x32_f16 v[32:35], v[136:139], v[168:171], 0\n" \
"buffer_load_dwordx4 v232, s[36:39], s80 offen offset:1024 sc1 lds\n" \
"ds_read_b64_tr_b16 v[148:149], v242 offset:16384\n" \
"v_mfma_f32_16x16x32_f16 v[44:47], v[140:143], v[168:171], 0\n" \
"buffer_store_dwordx4 v[76:79], v235, s[36:39], s61 offen sc1\n" \
"ds_read_b64_tr_b16 v[150:151], v242 offset:24576\n" \
"s_waitcnt lgkmcnt(9)\n" \
"v_mfma_f32_16x16x32_f16 v[48:51], v[120:123], v[176:179], 0\n" \
"ds_read_b128 v[168:171], v240 offset:28672\n" \
"ds_write_b64 v246, v[104:105] offset:16384\n" \
"ds_write_b64 v246, v[106:107] offset:20480\n" \
"v_mfma_f32_16x16x32_f16 v[60:63], v[124:127], v[176:179], 0\n" \
"v_pk_add_f16 v176, v176, v180\n" \
"v_pk_add_f16 v177, v177, v181\n" \
"ds_read_b64_tr_b16 v[152:153], v241 offset:16896\n" \
"v_mfma_f32_16x16x32_f16 v[52:55], v[128:131], v[180:183], 0\n" \
"v_pk_add_f16 v178, v178, v182\n" \
"v_pk_add_f16 v179, v179, v183\n" \
"v_mfma_f32_16x16x32_f16 v[64:67], v[132:135], v[180:183], 0\n" \
"ds_read_b128 v[172:175], v240 offset:29184\n" \
"ds_write_b64 v246, v[108:109] offset:24576\n" \
"v_mfma_f32_16x16x32_f16 v[56:59], v[136:139], v[176:179], 0\n" \
"buffer_load_dwordx4 v232, s[36:39], s80 offen offset:2048 sc1 lds\n" \
"ds_write_b64 v246, v[110:111] offset:28672\n" \
"v_mfma_f32_16x16x32_f16 v[68:71], v[140:143], v[176:179], 0\n" \
"buffer_store_dwordx4 v[80:83], v235, s[36:39], s62 offen sc1\n" \
"ds_read_b64_tr_b16 v[154:155], v241 offset:25088\n" \
"s_waitcnt lgkmcnt(9)\n" \
"v_mfma_f32_16x16x32_f16 v[72:75], v[120:123], v[160:163], 0\n" \
"ds_read_b128 v[176:179], v240 offset:20480\n" \
"ds_write_b64 v246, v[112:113] offset:32768\n" \
"ds_read_b64_tr_b16 v[156:157], v242 offset:16896\n" \
"v_mfma_f32_16x16x32_f16 v[84:87], v[124:127], v[160:163], 0\n" \
"v_pk_add_f16 v160, v160, v164\n" \
"v_pk_add_f16 v161, v161, v165\n" \
"ds_read_b64_tr_b16 v[158:159], v242 offset:25088\n" \
"v_mfma_f32_16x16x32_f16 v[76:79], v[128:131], v[164:167], 0\n" \
"v_pk_add_f16 v162, v162, v166\n" \
"v_pk_add_f16 v163, v163, v167\n" \
"v_mfma_f32_16x16x32_f16 v[88:91], v[132:135], v[164:167], 0\n" \
"ds_read_b128 v[180:183], v240 offset:20992\n" \
"ds_write_b64 v246, v[114:115] offset:36864\n" \
"v_mfma_f32_16x16x32_f16 v[80:83], v[136:139], v[160:163], 0\n" \
"buffer_load_dwordx4 v232, s[36:39], s80 offen offset:3072 sc1 lds\n" \
"ds_write_b64 v246, v[116:117] offset:40960\n" \
"v_mfma_f32_16x16x32_f16 v[92:95], v[140:143], v[160:163], 0\n" \
"s_waitcnt lgkmcnt(9)\n" \
"v_mfma_f32_16x16x32_f16 v[96:99], v[120:123], v[168:171], 0\n" \
"v_pk_add_f16 v120, v144, v152\n" \
"v_pk_add_f16 v121, v145, v153\n" \
"ds_read_b128 v[160:163], v240 offset:22528\n" \
"v_mfma_f32_16x16x32_f16 v[108:111], v[124:127], v[168:171], 0\n" \
"v_pk_add_f16 v168, v168, v172\n" \
"v_pk_add_f16 v169, v169, v173\n" \
"ds_read_b128 v[164:167], v240 offset:23040\n" \
"v_mfma_f32_16x16x32_f16 v[100:103], v[128:131], v[172:175], 0\n" \
"v_pk_add_f16 v170, v170, v174\n" \
"v_pk_add_f16 v171, v171, v175\n" \
"v_mfma_f32_16x16x32_f16 v[112:115], v[132:135], v[172:175], 0\n" \
"s_waitcnt lgkmcnt(5)\n" \
"v_pk_add_f16 v122, v146, v154\n" \
"v_pk_add_f16 v123, v147, v155\n" \
"v_mfma_f32_16x16x32_f16 v[104:107], v[136:139], v[168:171], 0\n" \
"v_pk_add_f16 v124, v148, v156\n" \
"v_pk_add_f16 v125, v149, v157\n" \
"v_mfma_f32_16x16x32_f16 v[116:119], v[140:143], v[168:171], 0\n" \
"v_pk_add_f16 v126, v150, v158\n" \
"v_pk_add_f16 v127, v151, v159\n" \
"s_waitcnt vmcnt(8) lgkmcnt(0)\n" \
"s_barrier\n" \
"v_mfma_f32_16x16x32_f16 v[0:3], v[144:147], v[176:179], v[0:3]\n" \
"ds_read_b128 v[168:171], v240 offset:45056\n" \
"ds_read_b64_tr_b16 v[184:185], v236 offset:0\n" \
"ds_read_b64_tr_b16 v[186:187], v236 offset:0\n" \
"v_mfma_f32_16x16x32_f16 v[12:15], v[148:151], v[176:179], v[12:15]\n" \
"v_pk_add_f16 v176, v176, v180\n" \
"v_pk_add_f16 v177, v177, v181\n" \
"ds_read_b64_tr_b16 v[188:189], v237 offset:0\n" \
"v_mfma_f32_16x16x32_f16 v[4:7], v[152:155], v[180:183], v[4:7]\n" \
"v_pk_add_f16 v178, v178, v182\n" \
"v_pk_add_f16 v179, v179, v183\n" \
"buffer_store_dwordx4 v[212:215], v235, s[36:39], s64 offen sc1\n" \
"v_mfma_f32_16x16x32_f16 v[16:19], v[156:159], v[180:183], v[16:19]\n" \
"ds_read_b128 v[172:175], v240 offset:45568\n" \
"s_add_i32 m0, s44, 0x4000\n" \
"s_add_i32 s80, s46, 0xc00000\n" \
"ds_read_b64_tr_b16 v[190:191], v237 offset:0\n" \
"v_mfma_f32_16x16x32_f16 v[8:11], v[120:123], v[176:179], v[8:11]\n" \
"buffer_load_dwordx4 v232, s[36:39], s80 offen sc1 lds\n" \
"v_mfma_f32_16x16x32_f16 v[20:23], v[124:127], v[176:179], v[20:23]\n" \
"ds_read_b64_tr_b16 v[128:129], v241 offset:32768\n" \
"ds_read_b64_tr_b16 v[130:131], v241 offset:40960\n" \
"s_waitcnt lgkmcnt(5)\n" \
"v_mfma_f32_16x16x32_bf16 v[184:187], v[184:187], v[192:195], 0\n" \
"v_mfma_f32_16x16x32_f16 v[24:27], v[144:147], v[160:163], v[24:27]\n" \
"ds_read_b128 v[176:179], v240 offset:47104\n" \
"s_add_i32 s84, s52, 0x8004000\n" \
"s_waitcnt lgkmcnt(3)\n" \
"v_mfma_f32_16x16x32_bf16 v[188:191], v[188:191], v[192:195], 0\n" \
"v_mfma_f32_16x16x32_f16 v[36:39], v[148:151], v[160:163], v[36:39]\n" \
"v_pk_add_f16 v160, v160, v164\n" \
"v_pk_add_f16 v161, v161, v165\n" \
"buffer_store_dword v240, v243, s[36:39], s84 offen sc1\n" \
"v_mfma_f32_16x16x32_f16 v[28:31], v[152:155], v[164:167], v[28:31]\n" \
"v_pk_add_f16 v162, v162, v166\n" \
"v_pk_add_f16 v163, v163, v167\n" \
"ds_read_b64_tr_b16 v[132:133], v242 offset:32768\n" \
"v_mfma_f32_16x16x32_f16 v[40:43], v[156:159], v[164:167], v[40:43]\n" \
"v_cvt_pk_f16_f32 v184, v184, v185\n" \
"ds_read_b128 v[180:183], v240 offset:47616\n" \
"ds_read_b64_tr_b16 v[134:135], v242 offset:40960\n" \
"v_mfma_f32_16x16x32_f16 v[32:35], v[120:123], v[160:163], v[32:35]\n" \
"v_cvt_pk_f16_f32 v185, v186, v187\n" \
"v_cvt_pk_f16_f32 v186, v188, v189\n" \
"buffer_load_dwordx4 v232, s[36:39], s80 offen offset:1024 sc1 lds\n" \
"v_mfma_f32_16x16x32_f16 v[44:47], v[124:127], v[160:163], v[44:47]\n" \
"v_cvt_pk_f16_f32 v187, v190, v191\n" \
"v_add_u32 v184, s91, v184\n" \
"v_mfma_f32_16x16x32_f16 v[48:51], v[144:147], v[168:171], v[48:51]\n" \
"v_add_u32 v185, s91, v185\n" \
"v_add_u32 v186, s91, v186\n" \
"ds_read_b128 v[160:163], v240 offset:49152\n" \
"v_mfma_f32_16x16x32_f16 v[60:63], v[148:151], v[168:171], v[60:63]\n" \
"v_pk_add_f16 v168, v168, v172\n" \
"v_pk_add_f16 v169, v169, v173\n" \
"v_add_u32 v187, s91, v187\n" \
"v_and_b32 v184, s92, v184\n" \
"s_add_i32 s82, s49, 0x2800000\n" \
"v_mfma_f32_16x16x32_f16 v[52:55], v[152:155], v[172:175], v[52:55]\n" \
"v_pk_add_f16 v170, v170, v174\n" \
"v_pk_add_f16 v171, v171, v175\n" \
"v_and_b32 v185, s92, v185\n" \
"v_and_b32 v186, s92, v186\n" \
"v_and_b32 v187, s92, v187\n" \
"v_mfma_f32_16x16x32_f16 v[64:67], v[156:159], v[172:175], v[64:67]\n" \
"ds_read_b128 v[164:167], v240 offset:49664\n" \
"buffer_store_dwordx4 v[184:187], v234, s[36:39], s82 offen sc1\n" \
"ds_read_b64_tr_b16 v[136:137], v241 offset:33280\n" \
"v_mfma_f32_16x16x32_f16 v[56:59], v[120:123], v[168:171], v[56:59]\n" \
"buffer_load_dwordx4 v232, s[36:39], s80 offen offset:2048 sc1 lds\n" \
"ds_read_b64_tr_b16 v[184:185], v238 offset:0\n" \
"v_mfma_f32_16x16x32_f16 v[68:71], v[124:127], v[168:171], v[68:71]\n" \
"ds_read_b64_tr_b16 v[186:187], v238 offset:0\n" \
"ds_read_b64_tr_b16 v[188:189], v239 offset:0\n" \
"ds_read_b64_tr_b16 v[138:139], v241 offset:41472\n" \
"s_waitcnt lgkmcnt(6)\n" \
"v_mfma_f32_16x16x32_f16 v[72:75], v[144:147], v[176:179], v[72:75]\n" \
"ds_read_b128 v[168:171], v240 offset:40960\n" \
"ds_read_b64_tr_b16 v[190:191], v239 offset:0\n" \
"ds_read_b64_tr_b16 v[140:141], v242 offset:33280\n" \
"v_mfma_f32_16x16x32_f16 v[84:87], v[148:151], v[176:179], v[84:87]\n" \
"v_pk_add_f16 v176, v176, v180\n" \
"v_pk_add_f16 v177, v177, v181\n" \
"v_mfma_f32_16x16x32_f16 v[76:79], v[152:155], v[180:183], v[76:79]\n" \
"v_pk_add_f16 v178, v178, v182\n" \
"v_pk_add_f16 v179, v179, v183\n" \
"s_waitcnt lgkmcnt(5)\n" \
"v_mfma_f32_16x16x32_bf16 v[184:187], v[184:187], v[192:195], 0\n" \
"v_mfma_f32_16x16x32_f16 v[88:91], v[156:159], v[180:183], v[88:91]\n" \
"ds_read_b128 v[172:175], v240 offset:41472\n" \
"ds_read_b64_tr_b16 v[142:143], v242 offset:41472\n" \
"s_waitcnt lgkmcnt(3)\n" \
"v_mfma_f32_16x16x32_bf16 v[188:191], v[188:191], v[192:195], 0\n" \
"v_mfma_f32_16x16x32_f16 v[80:83], v[120:123], v[176:179], v[80:83]\n" \
"buffer_load_dwordx4 v232, s[36:39], s80 offen offset:3072 sc1 lds\n" \
"v_mfma_f32_16x16x32_f16 v[92:95], v[124:127], v[176:179], v[92:95]\n" \
"v_mfma_f32_16x16x32_f16 v[96:99], v[144:147], v[160:163], v[96:99]\n" \
"v_pk_add_f16 v144, v128, v136\n" \
"v_pk_add_f16 v145, v129, v137\n" \
"v_cvt_pk_f16_f32 v184, v184, v185\n" \
"v_cvt_pk_f16_f32 v185, v186, v187\n" \
"ds_read_b128 v[176:179], v240 offset:43008\n" \
"v_mfma_f32_16x16x32_f16 v[108:111], v[148:151], v[160:163], v[108:111]\n" \
"v_pk_add_f16 v160, v160, v164\n" \
"v_pk_add_f16 v161, v161, v165\n" \
"v_cvt_pk_f16_f32 v186, v188, v189\n" \
"v_cvt_pk_f16_f32 v187, v190, v191\n" \
"ds_read_b128 v[180:183], v240 offset:43520\n" \
"v_mfma_f32_16x16x32_f16 v[100:103], v[152:155], v[164:167], v[100:103]\n" \
"v_pk_add_f16 v162, v162, v166\n" \
"v_pk_add_f16 v163, v163, v167\n" \
"v_add_u32 v184, s91, v184\n" \
"v_add_u32 v185, s91, v185\n" \
"v_add_u32 v186, s91, v186\n" \
"v_mfma_f32_16x16x32_f16 v[112:115], v[156:159], v[164:167], v[112:115]\n" \
"v_pk_add_f16 v146, v130, v138\n" \
"v_pk_add_f16 v147, v131, v139\n" \
"v_add_u32 v187, s91, v187\n" \
"v_and_b32 v184, s92, v184\n" \
"v_mfma_f32_16x16x32_f16 v[104:107], v[120:123], v[160:163], v[104:107]\n" \
"s_waitcnt lgkmcnt(2)\n" \
"v_pk_add_f16 v148, v132, v140\n" \
"v_pk_add_f16 v149, v133, v141\n" \
"v_and_b32 v185, s92, v185\n" \
"v_and_b32 v186, s92, v186\n" \
"v_and_b32 v187, s92, v187\n" \
"v_mfma_f32_16x16x32_f16 v[116:119], v[124:127], v[160:163], v[116:119]\n" \
"v_pk_add_f16 v150, v134, v142\n" \
"v_pk_add_f16 v151, v135, v143\n" \
"buffer_store_dwordx4 v[184:187], v234, s[36:39], s82 offen offset:1024 sc1\n" \
"s_waitcnt vmcnt(8) lgkmcnt(0)\n" \
"s_barrier\n" \
"v_mfma_f32_16x16x32_f16 v[0:3], v[128:131], v[168:171], v[0:3]\n" \
"ds_read_b128 v[160:163], v240 offset:4096\n" \
"ds_read_b64_tr_b16 v[184:185], v236 offset:4096\n" \
"ds_read_b64_tr_b16 v[186:187], v236 offset:4096\n" \
"v_mfma_f32_16x16x32_f16 v[12:15], v[132:135], v[168:171], v[12:15]\n" \
"v_pk_add_f16 v168, v168, v172\n" \
"v_pk_add_f16 v169, v169, v173\n" \
"ds_read_b64_tr_b16 v[188:189], v237 offset:4096\n" \
"v_mfma_f32_16x16x32_f16 v[4:7], v[136:139], v[172:175], v[4:7]\n" \
"v_pk_add_f16 v170, v170, v174\n" \
"v_pk_add_f16 v171, v171, v175\n" \
"ds_read_b64_tr_b16 v[190:191], v237 offset:4096\n" \
"v_mfma_f32_16x16x32_f16 v[16:19], v[140:143], v[172:175], v[16:19]\n" \
"ds_read_b128 v[164:167], v240 offset:4608\n" \
"s_add_i32 m0, s44, 0x8000\n" \
"s_add_i32 s80, s46, 0x1000000\n" \
"v_mfma_f32_16x16x32_f16 v[8:11], v[144:147], v[168:171], v[8:11]\n" \
"buffer_load_dwordx4 v232, s[36:39], s80 offen sc1 lds\n" \
"s_waitcnt lgkmcnt(3)\n" \
"v_mfma_f32_16x16x32_bf16 v[184:187], v[184:187], v[192:195], 0\n" \
"v_mfma_f32_16x16x32_f16 v[20:23], v[148:151], v[168:171], v[20:23]\n" \
"ds_read_b64_tr_b16 v[120:121], v241 offset:0\n" \
"ds_read_b64_tr_b16 v[122:123], v241 offset:8192\n" \
"s_waitcnt lgkmcnt(3)\n" \
"v_mfma_f32_16x16x32_bf16 v[188:191], v[188:191], v[192:195], 0\n" \
"v_mfma_f32_16x16x32_f16 v[24:27], v[128:131], v[176:179], v[24:27]\n" \
"ds_read_b128 v[168:171], v240 offset:6144\n" \
"v_mfma_f32_16x16x32_f16 v[36:39], v[132:135], v[176:179], v[36:39]\n" \
"v_pk_add_f16 v176, v176, v180\n" \
"v_pk_add_f16 v177, v177, v181\n" \
"ds_read_b64_tr_b16 v[124:125], v242 offset:0\n" \
"v_mfma_f32_16x16x32_f16 v[28:31], v[136:139], v[180:183], v[28:31]\n" \
"v_pk_add_f16 v178, v178, v182\n" \
"v_pk_add_f16 v179, v179, v183\n" \
"v_cvt_pk_f16_f32 v184, v184, v185\n" \
"v_mfma_f32_16x16x32_f16 v[40:43], v[140:143], v[180:183], v[40:43]\n" \
"v_cvt_pk_f16_f32 v185, v186, v187\n" \
"v_cvt_pk_f16_f32 v186, v188, v189\n" \
"ds_read_b128 v[172:175], v240 offset:6656\n" \
"v_mfma_f32_16x16x32_f16 v[32:35], v[144:147], v[176:179], v[32:35]\n" \
"v_cvt_pk_f16_f32 v187, v190, v191\n" \
"v_add_u32 v184, s91, v184\n" \
"buffer_load_dwordx4 v232, s[36:39], s80 offen offset:1024 sc1 lds\n" \
"v_mfma_f32_16x16x32_f16 v[44:47], v[148:151], v[176:179], v[44:47]\n" \
"v_add_u32 v185, s91, v185\n" \
"v_add_u32 v186, s91, v186\n" \
"ds_read_b64_tr_b16 v[126:127], v242 offset:8192\n" \
"v_mfma_f32_16x16x32_f16 v[48:51], v[128:131], v[160:163], v[48:51]\n" \
"v_add_u32 v187, s91, v187\n" \
"v_and_b32 v184, s92, v184\n" \
"ds_read_b128 v[176:179], v240 offset:8192\n" \
"v_mfma_f32_16x16x32_f16 v[60:63], v[132:135], v[160:163], v[60:63]\n" \
"s_waitcnt lgkmcnt(4)\n" \
"v_pk_add_f16 v160, v160, v164\n" \
"v_pk_add_f16 v161, v161, v165\n" \
"v_and_b32 v185, s92, v185\n" \
"s_add_i32 s82, s50, 0x3000000\n" \
"v_mfma_f32_16x16x32_f16 v[52:55], v[136:139], v[164:167], v[52:55]\n" \
"v_pk_add_f16 v162, v162, v166\n" \
"v_pk_add_f16 v163, v163, v167\n" \
"v_and_b32 v186, s92, v186\n" \
"v_and_b32 v187, s92, v187\n" \
"v_mfma_f32_16x16x32_f16 v[64:67], v[140:143], v[164:167], v[64:67]\n" \
"ds_read_b128 v[180:183], v240 offset:8704\n" \
"buffer_store_dwordx4 v[184:187], v234, s[36:39], s82 offen sc1\n" \
"ds_read_b64_tr_b16 v[152:153], v241 offset:512\n" \
"v_mfma_f32_16x16x32_f16 v[56:59], v[144:147], v[160:163], v[56:59]\n" \
"buffer_load_dwordx4 v232, s[36:39], s80 offen offset:2048 sc1 lds\n" \
"v_mfma_f32_16x16x32_f16 v[68:71], v[148:151], v[160:163], v[68:71]\n" \
"ds_read_b64_tr_b16 v[154:155], v241 offset:8704\n" \
"ds_read_b64_tr_b16 v[156:157], v242 offset:512\n" \
"v_mfma_f32_16x16x32_f16 v[72:75], v[128:131], v[168:171], v[72:75]\n" \
"ds_read_b128 v[160:163], v240 offset:0\n" \
"ds_read_b64_tr_b16 v[158:159], v242 offset:8704\n" \
"v_mfma_f32_16x16x32_f16 v[84:87], v[132:135], v[168:171], v[84:87]\n" \
"s_waitcnt lgkmcnt(6)\n" \
"v_pk_add_f16 v168, v168, v172\n" \
"v_pk_add_f16 v169, v169, v173\n" \
"v_mfma_f32_16x16x32_f16 v[76:79], v[136:139], v[172:175], v[76:79]\n" \
"v_pk_add_f16 v170, v170, v174\n" \
"v_pk_add_f16 v171, v171, v175\n" \
"v_mfma_f32_16x16x32_f16 v[88:91], v[140:143], v[172:175], v[88:91]\n" \
"ds_read_b128 v[164:167], v240 offset:512\n" \
"v_mfma_f32_16x16x32_f16 v[80:83], v[144:147], v[168:171], v[80:83]\n" \
"buffer_load_dwordx4 v232, s[36:39], s80 offen offset:3072 sc1 lds\n" \
"v_mfma_f32_16x16x32_f16 v[92:95], v[148:151], v[168:171], v[92:95]\n" \
"v_mfma_f32_16x16x32_f16 v[96:99], v[128:131], v[176:179], v[96:99]\n" \
"s_waitcnt lgkmcnt(5)\n" \
"v_pk_add_f16 v128, v120, v152\n" \
"v_pk_add_f16 v129, v121, v153\n" \
"ds_read_b128 v[168:171], v240 offset:2048\n" \
"v_mfma_f32_16x16x32_f16 v[108:111], v[132:135], v[176:179], v[108:111]\n" \
"v_pk_add_f16 v176, v176, v180\n" \
"v_pk_add_f16 v177, v177, v181\n" \
"ds_read_b128 v[172:175], v240 offset:2560\n" \
"v_mfma_f32_16x16x32_f16 v[100:103], v[136:139], v[180:183], v[100:103]\n" \
"v_pk_add_f16 v178, v178, v182\n" \
"v_pk_add_f16 v179, v179, v183\n" \
"v_mfma_f32_16x16x32_f16 v[112:115], v[140:143], v[180:183], v[112:115]\n" \
"s_waitcnt lgkmcnt(3)\n" \
"v_pk_add_f16 v130, v122, v154\n" \
"v_pk_add_f16 v131, v123, v155\n" \
"v_mfma_f32_16x16x32_f16 v[104:107], v[144:147], v[176:179], v[104:107]\n" \
"v_pk_add_f16 v132, v124, v156\n" \
"v_pk_add_f16 v133, v125, v157\n" \
"v_mfma_f32_16x16x32_f16 v[116:119], v[148:151], v[176:179], v[116:119]\n" \
"v_pk_add_f16 v134, v126, v158\n" \
"v_pk_add_f16 v135, v127, v159\n" \
"s_waitcnt vmcnt(5) lgkmcnt(0)\n" \
"s_barrier\n" \
"v_mfma_f32_16x16x32_f16 v[0:3], v[120:123], v[160:163], v[0:3]\n" \
"ds_read_b128 v[176:179], v240 offset:24576\n" \
"ds_read_b64_tr_b16 v[184:185], v238 offset:4096\n" \
"ds_read_b64_tr_b16 v[186:187], v238 offset:4096\n" \
"v_mfma_f32_16x16x32_f16 v[12:15], v[124:127], v[160:163], v[12:15]\n" \
"v_pk_add_f16 v160, v160, v164\n" \
"v_pk_add_f16 v161, v161, v165\n" \
"ds_read_b64_tr_b16 v[188:189], v239 offset:4096\n" \
"v_mfma_f32_16x16x32_f16 v[4:7], v[152:155], v[164:167], v[4:7]\n" \
"v_pk_add_f16 v162, v162, v166\n" \
"v_pk_add_f16 v163, v163, v167\n" \
"ds_read_b64_tr_b16 v[190:191], v239 offset:4096\n" \
"v_mfma_f32_16x16x32_f16 v[16:19], v[156:159], v[164:167], v[16:19]\n" \
"ds_read_b128 v[180:183], v240 offset:25088\n" \
"s_add_i32 m0, s44, 0x0\n" \
"s_add_i32 s80, s46, 0x1400000\n" \
"v_mfma_f32_16x16x32_f16 v[8:11], v[128:131], v[160:163], v[8:11]\n" \
"buffer_load_dwordx4 v232, s[36:39], s80 offen sc1 lds\n" \
"s_waitcnt lgkmcnt(3)\n" \
"v_mfma_f32_16x16x32_bf16 v[184:187], v[184:187], v[192:195], 0\n" \
"v_mfma_f32_16x16x32_f16 v[20:23], v[132:135], v[160:163], v[20:23]\n" \
"ds_read_b64_tr_b16 v[136:137], v241 offset:16384\n" \
"ds_read_b64_tr_b16 v[138:139], v241 offset:24576\n" \
"s_waitcnt lgkmcnt(3)\n" \
"v_mfma_f32_16x16x32_bf16 v[188:191], v[188:191], v[192:195], 0\n" \
"v_mfma_f32_16x16x32_f16 v[24:27], v[120:123], v[168:171], v[24:27]\n" \
"ds_read_b128 v[160:163], v240 offset:26624\n" \
"s_add_i32 s84, s53, 0x8006000\n" \
"v_mfma_f32_16x16x32_f16 v[36:39], v[124:127], v[168:171], v[36:39]\n" \
"v_pk_add_f16 v168, v168, v172\n" \
"v_pk_add_f16 v169, v169, v173\n" \
"buffer_store_dword v240, v243, s[36:39], s84 offen sc1\n" \
"v_mfma_f32_16x16x32_f16 v[28:31], v[152:155], v[172:175], v[28:31]\n" \
"v_pk_add_f16 v170, v170, v174\n" \
"v_pk_add_f16 v171, v171, v175\n" \
"v_cvt_pk_f16_f32 v184, v184, v185\n" \
"ds_read_b64_tr_b16 v[140:141], v242 offset:16384\n" \
"v_mfma_f32_16x16x32_f16 v[40:43], v[156:159], v[172:175], v[40:43]\n" \
"v_cvt_pk_f16_f32 v185, v186, v187\n" \
"v_cvt_pk_f16_f32 v186, v188, v189\n" \
"ds_read_b128 v[164:167], v240 offset:27136\n" \
"v_mfma_f32_16x16x32_f16 v[32:35], v[128:131], v[168:171], v[32:35]\n" \
"v_cvt_pk_f16_f32 v187, v190, v191\n" \
"v_add_u32 v184, s91, v184\n" \
"buffer_load_dwordx4 v232, s[36:39], s80 offen offset:1024 sc1 lds\n" \
"v_mfma_f32_16x16x32_f16 v[44:47], v[132:135], v[168:171], v[44:47]\n" \
"v_add_u32 v185, s91, v185\n" \
"v_add_u32 v186, s91, v186\n" \
"ds_read_b64_tr_b16 v[142:143], v242 offset:24576\n" \
"v_mfma_f32_16x16x32_f16 v[48:51], v[120:123], v[176:179], v[48:51]\n" \
"v_add_u32 v187, s91, v187\n" \
"v_and_b32 v184, s92, v184\n" \
"ds_read_b128 v[168:171], v240 offset:28672\n" \
"v_mfma_f32_16x16x32_f16 v[60:63], v[124:127], v[176:179], v[60:63]\n" \
"s_waitcnt lgkmcnt(4)\n" \
"v_pk_add_f16 v176, v176, v180\n" \
"v_pk_add_f16 v177, v177, v181\n" \
"v_and_b32 v185, s92, v185\n" \
"v_mfma_f32_16x16x32_f16 v[52:55], v[152:155], v[180:183], v[52:55]\n" \
"v_pk_add_f16 v178, v178, v182\n" \
"v_pk_add_f16 v179, v179, v183\n" \
"v_and_b32 v186, s92, v186\n" \
"v_and_b32 v187, s92, v187\n" \
"v_mfma_f32_16x16x32_f16 v[64:67], v[156:159], v[180:183], v[64:67]\n" \
"ds_read_b128 v[172:175], v240 offset:29184\n" \
"buffer_store_dwordx4 v[184:187], v234, s[36:39], s82 offen offset:1024 sc1\n" \
"ds_read_b64_tr_b16 v[144:145], v241 offset:16896\n" \
"v_mfma_f32_16x16x32_f16 v[56:59], v[128:131], v[176:179], v[56:59]\n" \
"buffer_load_dwordx4 v232, s[36:39], s80 offen offset:2048 sc1 lds\n" \
"v_mfma_f32_16x16x32_f16 v[68:71], v[132:135], v[176:179], v[68:71]\n" \
"ds_read_b64_tr_b16 v[146:147], v241 offset:25088\n" \
"ds_read_b64_tr_b16 v[148:149], v242 offset:16896\n" \
"v_mfma_f32_16x16x32_f16 v[72:75], v[120:123], v[160:163], v[72:75]\n" \
"ds_read_b128 v[176:179], v240 offset:20480\n" \
"ds_read_b64_tr_b16 v[150:151], v242 offset:25088\n" \
"v_mfma_f32_16x16x32_f16 v[84:87], v[124:127], v[160:163], v[84:87]\n" \
"s_waitcnt lgkmcnt(6)\n" \
"v_pk_add_f16 v160, v160, v164\n" \
"v_pk_add_f16 v161, v161, v165\n" \
"v_mfma_f32_16x16x32_f16 v[76:79], v[152:155], v[164:167], v[76:79]\n" \
"v_pk_add_f16 v162, v162, v166\n" \
"v_pk_add_f16 v163, v163, v167\n" \
"v_mfma_f32_16x16x32_f16 v[88:91], v[156:159], v[164:167], v[88:91]\n" \
"ds_read_b128 v[180:183], v240 offset:20992\n" \
"v_mfma_f32_16x16x32_f16 v[80:83], v[128:131], v[160:163], v[80:83]\n" \
"buffer_load_dwordx4 v232, s[36:39], s80 offen offset:3072 sc1 lds\n" \
"v_mfma_f32_16x16x32_f16 v[92:95], v[132:135], v[160:163], v[92:95]\n" \
"v_mfma_f32_16x16x32_f16 v[96:99], v[120:123], v[168:171], v[96:99]\n" \
"s_waitcnt lgkmcnt(5)\n" \
"v_pk_add_f16 v120, v136, v144\n" \
"v_pk_add_f16 v121, v137, v145\n" \
"ds_read_b128 v[160:163], v240 offset:22528\n" \
"v_mfma_f32_16x16x32_f16 v[108:111], v[124:127], v[168:171], v[108:111]\n" \
"v_pk_add_f16 v168, v168, v172\n" \
"v_pk_add_f16 v169, v169, v173\n" \
"ds_read_b128 v[164:167], v240 offset:23040\n" \
"v_mfma_f32_16x16x32_f16 v[100:103], v[152:155], v[172:175], v[100:103]\n" \
"v_pk_add_f16 v170, v170, v174\n" \
"v_pk_add_f16 v171, v171, v175\n" \
"v_mfma_f32_16x16x32_f16 v[112:115], v[156:159], v[172:175], v[112:115]\n" \
"s_waitcnt lgkmcnt(3)\n" \
"v_pk_add_f16 v122, v138, v146\n" \
"v_pk_add_f16 v123, v139, v147\n" \
"v_mfma_f32_16x16x32_f16 v[104:107], v[128:131], v[168:171], v[104:107]\n" \
"v_pk_add_f16 v124, v140, v148\n" \
"v_pk_add_f16 v125, v141, v149\n" \
"v_mfma_f32_16x16x32_f16 v[116:119], v[132:135], v[168:171], v[116:119]\n" \
"v_pk_add_f16 v126, v142, v150\n" \
"v_pk_add_f16 v127, v143, v151\n" \
"s_waitcnt vmcnt(6) lgkmcnt(0)\n" \
"s_barrier\n" \
"v_mfma_f32_16x16x32_f16 v[0:3], v[136:139], v[176:179], v[0:3]\n" \
"ds_read_b128 v[168:171], v240 offset:45056\n" \
"ds_read_b64_tr_b16 v[184:185], v236 offset:8192\n" \
"ds_read_b64_tr_b16 v[186:187], v236 offset:8192\n" \
"v_mfma_f32_16x16x32_f16 v[12:15], v[140:143], v[176:179], v[12:15]\n" \
"v_pk_add_f16 v176, v176, v180\n" \
"v_pk_add_f16 v177, v177, v181\n" \
"ds_read_b64_tr_b16 v[188:189], v237 offset:8192\n" \
"ds_read_b64_tr_b16 v[190:191], v237 offset:8192\n" \
"v_mfma_f32_16x16x32_f16 v[4:7], v[144:147], v[180:183], v[4:7]\n" \
"v_pk_add_f16 v178, v178, v182\n" \
"v_pk_add_f16 v179, v179, v183\n" \
"buffer_store_dwordx4 v[216:219], v235, s[36:39], s65 offen sc1\n" \
"v_mfma_f32_16x16x32_f16 v[16:19], v[148:151], v[180:183], v[16:19]\n" \
"ds_read_b128 v[172:175], v240 offset:45568\n" \
"s_add_i32 m0, s44, 0x4000\n" \
"s_add_i32 s80, s46, 0x1800000\n" \
"v_mfma_f32_16x16x32_f16 v[8:11], v[120:123], v[176:179], v[8:11]\n" \
"buffer_load_dwordx4 v232, s[36:39], s80 offen sc1 lds\n" \
"s_waitcnt lgkmcnt(3)\n" \
"v_mfma_f32_16x16x32_bf16 v[184:187], v[184:187], v[192:195], 0\n" \
"v_mfma_f32_16x16x32_f16 v[20:23], v[124:127], v[176:179], v[20:23]\n" \
"ds_read_b64_tr_b16 v[128:129], v241 offset:32768\n" \
"ds_read_b64_tr_b16 v[130:131], v241 offset:40960\n" \
"s_waitcnt lgkmcnt(3)\n" \
"v_mfma_f32_16x16x32_bf16 v[188:191], v[188:191], v[192:195], 0\n" \
"v_mfma_f32_16x16x32_f16 v[24:27], v[136:139], v[160:163], v[24:27]\n" \
"ds_read_b128 v[176:179], v240 offset:47104\n" \
"v_mfma_f32_16x16x32_f16 v[36:39], v[140:143], v[160:163], v[36:39]\n" \
"v_pk_add_f16 v160, v160, v164\n" \
"v_pk_add_f16 v161, v161, v165\n" \
"ds_read_b64_tr_b16 v[132:133], v242 offset:32768\n" \
"v_mfma_f32_16x16x32_f16 v[28:31], v[144:147], v[164:167], v[28:31]\n" \
"v_pk_add_f16 v162, v162, v166\n" \
"v_pk_add_f16 v163, v163, v167\n" \
"v_cvt_pk_f16_f32 v184, v184, v185\n" \
"v_mfma_f32_16x16x32_f16 v[40:43], v[148:151], v[164:167], v[40:43]\n" \
"v_cvt_pk_f16_f32 v185, v186, v187\n" \
"v_cvt_pk_f16_f32 v186, v188, v189\n" \
"ds_read_b128 v[180:183], v240 offset:47616\n" \
"v_mfma_f32_16x16x32_f16 v[32:35], v[120:123], v[160:163], v[32:35]\n" \
"v_cvt_pk_f16_f32 v187, v190, v191\n" \
"v_add_u32 v184, s91, v184\n" \
"buffer_load_dwordx4 v232, s[36:39], s80 offen offset:1024 sc1 lds\n" \
"v_mfma_f32_16x16x32_f16 v[44:47], v[124:127], v[160:163], v[44:47]\n" \
"v_add_u32 v185, s91, v185\n" \
"v_add_u32 v186, s91, v186\n" \
"ds_read_b64_tr_b16 v[134:135], v242 offset:40960\n" \
"v_mfma_f32_16x16x32_f16 v[48:51], v[136:139], v[168:171], v[48:51]\n" \
"v_add_u32 v187, s91, v187\n" \
"v_and_b32 v184, s92, v184\n" \
"ds_read_b128 v[160:163], v240 offset:49152\n" \
"v_mfma_f32_16x16x32_f16 v[60:63], v[140:143], v[168:171], v[60:63]\n" \
"s_waitcnt lgkmcnt(4)\n" \
"v_pk_add_f16 v168, v168, v172\n" \
"v_pk_add_f16 v169, v169, v173\n" \
"v_and_b32 v185, s92, v185\n" \
"s_add_i32 s82, s51, 0x3800000\n" \
"v_mfma_f32_16x16x32_f16 v[52:55], v[144:147], v[172:175], v[52:55]\n" \
"v_pk_add_f16 v170, v170, v174\n" \
"v_pk_add_f16 v171, v171, v175\n" \
"v_and_b32 v186, s92, v186\n" \
"v_and_b32 v187, s92, v187\n" \
"v_mfma_f32_16x16x32_f16 v[64:67], v[148:151], v[172:175], v[64:67]\n" \
"ds_read_b128 v[164:167], v240 offset:49664\n" \
"buffer_store_dwordx4 v[184:187], v234, s[36:39], s82 offen sc1\n" \
"ds_read_b64_tr_b16 v[152:153], v241 offset:33280\n" \
"v_mfma_f32_16x16x32_f16 v[56:59], v[120:123], v[168:171], v[56:59]\n" \
"buffer_load_dwordx4 v232, s[36:39], s80 offen offset:2048 sc1 lds\n" \
"v_mfma_f32_16x16x32_f16 v[68:71], v[124:127], v[168:171], v[68:71]\n" \
"ds_read_b64_tr_b16 v[154:155], v241 offset:41472\n" \
"ds_read_b64_tr_b16 v[156:157], v242 offset:33280\n" \
"v_mfma_f32_16x16x32_f16 v[72:75], v[136:139], v[176:179], v[72:75]\n" \
"ds_read_b128 v[168:171], v240 offset:40960\n" \
"ds_read_b64_tr_b16 v[158:159], v242 offset:41472\n" \
"v_mfma_f32_16x16x32_f16 v[84:87], v[140:143], v[176:179], v[84:87]\n" \
"s_waitcnt lgkmcnt(6)\n" \
"v_pk_add_f16 v176, v176, v180\n" \
"v_pk_add_f16 v177, v177, v181\n" \
"v_mfma_f32_16x16x32_f16 v[76:79], v[144:147], v[180:183], v[76:79]\n" \
"v_pk_add_f16 v178, v178, v182\n" \
"v_pk_add_f16 v179, v179, v183\n" \
"v_mfma_f32_16x16x32_f16 v[88:91], v[148:151], v[180:183], v[88:91]\n" \
"ds_read_b128 v[172:175], v240 offset:41472\n" \
"v_mfma_f32_16x16x32_f16 v[80:83], v[120:123], v[176:179], v[80:83]\n" \
"buffer_load_dwordx4 v232, s[36:39], s80 offen offset:3072 sc1 lds\n" \
"v_mfma_f32_16x16x32_f16 v[92:95], v[124:127], v[176:179], v[92:95]\n" \
"v_mfma_f32_16x16x32_f16 v[96:99], v[136:139], v[160:163], v[96:99]\n" \
"s_waitcnt lgkmcnt(5)\n" \
"v_pk_add_f16 v136, v128, v152\n" \
"v_pk_add_f16 v137, v129, v153\n" \
"ds_read_b128 v[176:179], v240 offset:43008\n" \
"v_mfma_f32_16x16x32_f16 v[108:111], v[140:143], v[160:163], v[108:111]\n" \
"v_pk_add_f16 v160, v160, v164\n" \
"v_pk_add_f16 v161, v161, v165\n" \
"ds_read_b128 v[180:183], v240 offset:43520\n" \
"v_mfma_f32_16x16x32_f16 v[100:103], v[144:147], v[164:167], v[100:103]\n" \
"v_pk_add_f16 v162, v162, v166\n" \
"v_pk_add_f16 v163, v163, v167\n" \
"v_mfma_f32_16x16x32_f16 v[112:115], v[148:151], v[164:167], v[112:115]\n" \
"s_waitcnt lgkmcnt(3)\n" \
"v_pk_add_f16 v138, v130, v154\n" \
"v_pk_add_f16 v139, v131, v155\n" \
"v_mfma_f32_16x16x32_f16 v[104:107], v[120:123], v[160:163], v[104:107]\n" \
"v_pk_add_f16 v140, v132, v156\n" \
"v_pk_add_f16 v141, v133, v157\n" \
"v_mfma_f32_16x16x32_f16 v[116:119], v[124:127], v[160:163], v[116:119]\n" \
"v_pk_add_f16 v142, v134, v158\n" \
"v_pk_add_f16 v143, v135, v159\n" \
"s_waitcnt vmcnt(6) lgkmcnt(0)\n" \
"s_barrier\n" \
"v_mfma_f32_16x16x32_f16 v[0:3], v[128:131], v[168:171], v[0:3]\n" \
"ds_read_b128 v[160:163], v240 offset:4096\n" \
"ds_read_b64_tr_b16 v[184:185], v238 offset:8192\n" \
"ds_read_b64_tr_b16 v[186:187], v238 offset:8192\n" \
"v_mfma_f32_16x16x32_f16 v[12:15], v[132:135], v[168:171], v[12:15]\n" \
"v_pk_add_f16 v168, v168, v172\n" \
"v_pk_add_f16 v169, v169, v173\n" \
"ds_read_b64_tr_b16 v[188:189], v239 offset:8192\n" \
"v_mfma_f32_16x16x32_f16 v[4:7], v[152:155], v[172:175], v[4:7]\n" \
"v_pk_add_f16 v170, v170, v174\n" \
"v_pk_add_f16 v171, v171, v175\n" \
"ds_read_b64_tr_b16 v[190:191], v239 offset:8192\n" \
"v_mfma_f32_16x16x32_f16 v[16:19], v[156:159], v[172:175], v[16:19]\n" \
"ds_read_b128 v[164:167], v240 offset:4608\n" \
"s_add_i32 m0, s44, 0x8000\n" \
"s_add_i32 s80, s46, 0x1c00000\n" \
"v_mfma_f32_16x16x32_f16 v[8:11], v[136:139], v[168:171], v[8:11]\n" \
"buffer_load_dwordx4 v232, s[36:39], s80 offen sc1 lds\n" \
"s_waitcnt lgkmcnt(3)\n" \
"v_mfma_f32_16x16x32_bf16 v[184:187], v[184:187], v[192:195], 0\n" \
"v_mfma_f32_16x16x32_f16 v[20:23], v[140:143], v[168:171], v[20:23]\n" \
"ds_read_b64_tr_b16 v[120:121], v241 offset:0\n" \
"ds_read_b64_tr_b16 v[122:123], v241 offset:8192\n" \
"s_waitcnt lgkmcnt(3)\n" \
"v_mfma_f32_16x16x32_bf16 v[188:191], v[188:191], v[192:195], 0\n" \
"v_mfma_f32_16x16x32_f16 v[24:27], v[128:131], v[176:179], v[24:27]\n" \
"ds_read_b128 v[168:171], v240 offset:6144\n" \
"s_add_i32 s84, s54, 0x8008000\n" \
"v_mfma_f32_16x16x32_f16 v[36:39], v[132:135], v[176:179], v[36:39]\n" \
"v_pk_add_f16 v176, v176, v180\n" \
"v_pk_add_f16 v177, v177, v181\n" \
"buffer_store_dword v240, v243, s[36:39], s84 offen sc1\n" \
"v_mfma_f32_16x16x32_f16 v[28:31], v[152:155], v[180:183], v[28:31]\n" \
"v_pk_add_f16 v178, v178, v182\n" \
"v_pk_add_f16 v179, v179, v183\n" \
"v_cvt_pk_f16_f32 v184, v184, v185\n" \
"ds_read_b64_tr_b16 v[124:125], v242 offset:0\n" \
"v_mfma_f32_16x16x32_f16 v[40:43], v[156:159], v[180:183], v[40:43]\n" \
"v_cvt_pk_f16_f32 v185, v186, v187\n" \
"v_cvt_pk_f16_f32 v186, v188, v189\n" \
"ds_read_b128 v[172:175], v240 offset:6656\n" \
"v_mfma_f32_16x16x32_f16 v[32:35], v[136:139], v[176:179], v[32:35]\n" \
"v_cvt_pk_f16_f32 v187, v190, v191\n" \
"v_add_u32 v184, s91, v184\n" \
"buffer_load_dwordx4 v232, s[36:39], s80 offen offset:1024 sc1 lds\n" \
"v_mfma_f32_16x16x32_f16 v[44:47], v[140:143], v[176:179], v[44:47]\n" \
"v_add_u32 v185, s91, v185\n" \
"v_add_u32 v186, s91, v186\n" \
"ds_read_b64_tr_b16 v[126:127], v242 offset:8192\n" \
"v_mfma_f32_16x16x32_f16 v[48:51], v[128:131], v[160:163], v[48:51]\n" \
"v_add_u32 v187, s91, v187\n" \
"v_and_b32 v184, s92, v184\n" \
"ds_read_b128 v[176:179], v240 offset:8192\n" \
"v_mfma_f32_16x16x32_f16 v[60:63], v[132:135], v[160:163], v[60:63]\n" \
"s_waitcnt lgkmcnt(4)\n" \
"v_pk_add_f16 v160, v160, v164\n" \
"v_pk_add_f16 v161, v161, v165\n" \
"v_and_b32 v185, s92, v185\n" \
"v_mfma_f32_16x16x32_f16 v[52:55], v[152:155], v[164:167], v[52:55]\n" \
"v_pk_add_f16 v162, v162, v166\n" \
"v_pk_add_f16 v163, v163, v167\n" \
"v_and_b32 v186, s92, v186\n" \
"v_and_b32 v187, s92, v187\n" \
"v_mfma_f32_16x16x32_f16 v[64:67], v[156:159], v[164:167], v[64:67]\n" \
"ds_read_b128 v[180:183], v240 offset:8704\n" \
"buffer_store_dwordx4 v[184:187], v234, s[36:39], s82 offen offset:1024 sc1\n" \
"ds_read_b64_tr_b16 v[144:145], v241 offset:512\n" \
"v_mfma_f32_16x16x32_f16 v[56:59], v[136:139], v[160:163], v[56:59]\n" \
"buffer_load_dwordx4 v232, s[36:39], s80 offen offset:2048 sc1 lds\n" \
"v_mfma_f32_16x16x32_f16 v[68:71], v[140:143], v[160:163], v[68:71]\n" \
"ds_read_b64_tr_b16 v[146:147], v241 offset:8704\n" \
"ds_read_b64_tr_b16 v[148:149], v242 offset:512\n" \
"v_mfma_f32_16x16x32_f16 v[72:75], v[128:131], v[168:171], v[72:75]\n" \
"ds_read_b128 v[160:163], v240 offset:0\n" \
"ds_read_b64_tr_b16 v[150:151], v242 offset:8704\n" \
"v_mfma_f32_16x16x32_f16 v[84:87], v[132:135], v[168:171], v[84:87]\n" \
"s_waitcnt lgkmcnt(6)\n" \
"v_pk_add_f16 v168, v168, v172\n" \
"v_pk_add_f16 v169, v169, v173\n" \
"v_mfma_f32_16x16x32_f16 v[76:79], v[152:155], v[172:175], v[76:79]\n" \
"v_pk_add_f16 v170, v170, v174\n" \
"v_pk_add_f16 v171, v171, v175\n" \
"v_mfma_f32_16x16x32_f16 v[88:91], v[156:159], v[172:175], v[88:91]\n" \
"ds_read_b128 v[164:167], v240 offset:512\n" \
"v_mfma_f32_16x16x32_f16 v[80:83], v[136:139], v[168:171], v[80:83]\n" \
"buffer_load_dwordx4 v232, s[36:39], s80 offen offset:3072 sc1 lds\n" \
"v_mfma_f32_16x16x32_f16 v[92:95], v[140:143], v[168:171], v[92:95]\n" \
"v_mfma_f32_16x16x32_f16 v[96:99], v[128:131], v[176:179], v[96:99]\n" \
"s_waitcnt lgkmcnt(5)\n" \
"v_pk_add_f16 v128, v120, v144\n" \
"v_pk_add_f16 v129, v121, v145\n" \
"ds_read_b128 v[168:171], v240 offset:2048\n" \
"v_mfma_f32_16x16x32_f16 v[108:111], v[132:135], v[176:179], v[108:111]\n" \
"v_pk_add_f16 v176, v176, v180\n" \
"v_pk_add_f16 v177, v177, v181\n" \
"ds_read_b128 v[172:175], v240 offset:2560\n" \
"v_mfma_f32_16x16x32_f16 v[100:103], v[152:155], v[180:183], v[100:103]\n" \
"v_pk_add_f16 v178, v178, v182\n" \
"v_pk_add_f16 v179, v179, v183\n" \
"v_mfma_f32_16x16x32_f16 v[112:115], v[156:159], v[180:183], v[112:115]\n" \
"s_waitcnt lgkmcnt(3)\n" \
"v_pk_add_f16 v130, v122, v146\n" \
"v_pk_add_f16 v131, v123, v147\n" \
"v_mfma_f32_16x16x32_f16 v[104:107], v[136:139], v[176:179], v[104:107]\n" \
"v_pk_add_f16 v132, v124, v148\n" \
"v_pk_add_f16 v133, v125, v149\n" \
"v_mfma_f32_16x16x32_f16 v[116:119], v[140:143], v[176:179], v[116:119]\n" \
"v_pk_add_f16 v134, v126, v150\n" \
"v_pk_add_f16 v135, v127, v151\n" \
"s_waitcnt vmcnt(6) lgkmcnt(0)\n" \
"s_barrier\n" \
"v_mfma_f32_16x16x32_f16 v[0:3], v[120:123], v[160:163], v[0:3]\n" \
"ds_read_b128 v[176:179], v240 offset:24576\n" \
"s_mov_b32 s81, 0x8006000\n" \
"ds_read_b64_tr_b16 v[184:185], v236 offset:12288\n" \
"ds_read_b64_tr_b16 v[186:187], v236 offset:12288\n" \
"v_mfma_f32_16x16x32_f16 v[12:15], v[124:127], v[160:163], v[12:15]\n" \
"v_pk_add_f16 v160, v160, v164\n" \
"v_pk_add_f16 v161, v161, v165\n" \
"buffer_load_dword v253, v252, s[36:39], s81 offen sc1\n" \
"v_mfma_f32_16x16x32_f16 v[4:7], v[144:147], v[164:167], v[4:7]\n" \
"v_pk_add_f16 v162, v162, v166\n" \
"v_pk_add_f16 v163, v163, v167\n" \
"ds_read_b64_tr_b16 v[188:189], v237 offset:12288\n" \
"ds_read_b64_tr_b16 v[190:191], v237 offset:12288\n" \
"v_mfma_f32_16x16x32_f16 v[16:19], v[148:151], v[164:167], v[16:19]\n" \
"ds_read_b128 v[180:183], v240 offset:25088\n" \
"s_add_i32 m0, s44, 0x0\n" \
"s_add_i32 s80, s46, 0x2000000\n" \
"v_mfma_f32_16x16x32_f16 v[8:11], v[128:131], v[160:163], v[8:11]\n" \
"buffer_load_dwordx4 v232, s[36:39], s80 offen sc1 lds\n" \
"s_waitcnt lgkmcnt(3)\n" \
"v_mfma_f32_16x16x32_bf16 v[184:187], v[184:187], v[192:195], 0\n" \
"v_mfma_f32_16x16x32_f16 v[20:23], v[132:135], v[160:163], v[20:23]\n" \
"ds_read_b64_tr_b16 v[136:137], v241 offset:16384\n" \
"ds_read_b64_tr_b16 v[138:139], v241 offset:24576\n" \
"s_waitcnt lgkmcnt(3)\n" \
"v_mfma_f32_16x16x32_bf16 v[188:191], v[188:191], v[192:195], 0\n" \
"v_mfma_f32_16x16x32_f16 v[24:27], v[120:123], v[168:171], v[24:27]\n" \
"ds_read_b128 v[160:163], v240 offset:26624\n" \
"v_mfma_f32_16x16x32_f16 v[36:39], v[124:127], v[168:171], v[36:39]\n" \
"v_pk_add_f16 v168, v168, v172\n" \
"v_pk_add_f16 v169, v169, v173\n" \
"ds_read_b64_tr_b16 v[140:141], v242 offset:16384\n" \
"v_mfma_f32_16x16x32_f16 v[28:31], v[144:147], v[172:175], v[28:31]\n" \
"v_pk_add_f16 v170, v170, v174\n" \
"v_pk_add_f16 v171, v171, v175\n" \
"v_cvt_pk_f16_f32 v184, v184, v185\n" \
"v_mfma_f32_16x16x32_f16 v[40:43], v[148:151], v[172:175], v[40:43]\n" \
"v_cvt_pk_f16_f32 v185, v186, v187\n" \
"v_cvt_pk_f16_f32 v186, v188, v189\n" \
"ds_read_b128 v[164:167], v240 offset:27136\n" \
"v_mfma_f32_16x16x32_f16 v[32:35], v[128:131], v[168:171], v[32:35]\n" \
"v_cvt_pk_f16_f32 v187, v190, v191\n" \
"v_add_u32 v184, s91, v184\n" \
"buffer_load_dwordx4 v232, s[36:39], s80 offen offset:1024 sc1 lds\n" \
"v_mfma_f32_16x16x32_f16 v[44:47], v[132:135], v[168:171], v[44:47]\n" \
"v_add_u32 v185, s91, v185\n" \
"v_add_u32 v186, s91, v186\n" \
"ds_read_b64_tr_b16 v[142:143], v242 offset:24576\n" \
"v_mfma_f32_16x16x32_f16 v[48:51], v[120:123], v[176:179], v[48:51]\n" \
"v_add_u32 v187, s91, v187\n" \
"v_and_b32 v184, s92, v184\n" \
"ds_read_b128 v[168:171], v240 offset:28672\n" \
"v_mfma_f32_16x16x32_f16 v[60:63], v[124:127], v[176:179], v[60:63]\n" \
"s_waitcnt lgkmcnt(4)\n" \
"v_pk_add_f16 v176, v176, v180\n" \
"v_pk_add_f16 v177, v177, v181\n" \
"v_and_b32 v185, s92, v185\n" \
"s_add_i32 s82, s48, 0x4000000\n" \
"v_mfma_f32_16x16x32_f16 v[52:55], v[144:147], v[180:183], v[52:55]\n" \
"v_pk_add_f16 v178, v178, v182\n" \
"v_pk_add_f16 v179, v179, v183\n" \
"v_and_b32 v186, s92, v186\n" \
"v_and_b32 v187, s92, v187\n" \
"v_mfma_f32_16x16x32_f16 v[64:67], v[148:151], v[180:183], v[64:67]\n" \
"ds_read_b128 v[172:175], v240 offset:29184\n" \
"buffer_store_dwordx4 v[184:187], v234, s[36:39], s82 offen sc1\n" \
"ds_read_b64_tr_b16 v[152:153], v241 offset:16896\n" \
"v_mfma_f32_16x16x32_f16 v[56:59], v[128:131], v[176:179], v[56:59]\n" \
"buffer_load_dwordx4 v232, s[36:39], s80 offen offset:2048 sc1 lds\n" \
"v_mfma_f32_16x16x32_f16 v[68:71], v[132:135], v[176:179], v[68:71]\n" \
"ds_read_b64_tr_b16 v[154:155], v241 offset:25088\n" \
"ds_read_b64_tr_b16 v[156:157], v242 offset:16896\n" \
"v_mfma_f32_16x16x32_f16 v[72:75], v[120:123], v[160:163], v[72:75]\n" \
"ds_read_b128 v[176:179], v240 offset:20480\n" \
"ds_read_b64_tr_b16 v[158:159], v242 offset:25088\n" \
"v_mfma_f32_16x16x32_f16 v[84:87], v[124:127], v[160:163], v[84:87]\n" \
"s_waitcnt lgkmcnt(6)\n" \
"v_pk_add_f16 v160, v160, v164\n" \
"v_pk_add_f16 v161, v161, v165\n" \
"v_mfma_f32_16x16x32_f16 v[76:79], v[144:147], v[164:167], v[76:79]\n" \
"v_pk_add_f16 v162, v162, v166\n" \
"v_pk_add_f16 v163, v163, v167\n" \
"v_mfma_f32_16x16x32_f16 v[88:91], v[148:151], v[164:167], v[88:91]\n" \
"ds_read_b128 v[180:183], v240 offset:20992\n" \
"v_mfma_f32_16x16x32_f16 v[80:83], v[128:131], v[160:163], v[80:83]\n" \
"buffer_load_dwordx4 v232, s[36:39], s80 offen offset:3072 sc1 lds\n" \
"v_mfma_f32_16x16x32_f16 v[92:95], v[132:135], v[160:163], v[92:95]\n" \
"v_mfma_f32_16x16x32_f16 v[96:99], v[120:123], v[168:171], v[96:99]\n" \
"s_waitcnt lgkmcnt(5)\n" \
"v_pk_add_f16 v120, v136, v152\n" \
"v_pk_add_f16 v121, v137, v153\n" \
"ds_read_b128 v[160:163], v240 offset:22528\n" \
"v_mfma_f32_16x16x32_f16 v[108:111], v[124:127], v[168:171], v[108:111]\n" \
"v_pk_add_f16 v168, v168, v172\n" \
"v_pk_add_f16 v169, v169, v173\n" \
"ds_read_b128 v[164:167], v240 offset:23040\n" \
"v_mfma_f32_16x16x32_f16 v[100:103], v[144:147], v[172:175], v[100:103]\n" \
"v_pk_add_f16 v170, v170, v174\n" \
"v_pk_add_f16 v171, v171, v175\n" \
"v_mfma_f32_16x16x32_f16 v[112:115], v[148:151], v[172:175], v[112:115]\n" \
"s_waitcnt lgkmcnt(3)\n" \
"v_pk_add_f16 v122, v138, v154\n" \
"v_pk_add_f16 v123, v139, v155\n" \
"v_mfma_f32_16x16x32_f16 v[104:107], v[128:131], v[168:171], v[104:107]\n" \
"v_pk_add_f16 v124, v140, v156\n" \
"v_pk_add_f16 v125, v141, v157\n" \
"v_mfma_f32_16x16x32_f16 v[116:119], v[132:135], v[168:171], v[116:119]\n" \
"v_pk_add_f16 v126, v142, v158\n" \
"v_pk_add_f16 v127, v143, v159\n" \
"s_waitcnt vmcnt(5) lgkmcnt(0)\n" \
"s_barrier\n" \
"v_mfma_f32_16x16x32_f16 v[0:3], v[136:139], v[176:179], v[0:3]\n" \
"ds_read_b128 v[168:171], v240 offset:45056\n" \
"ds_read_b64_tr_b16 v[184:185], v238 offset:12288\n" \
"ds_read_b64_tr_b16 v[186:187], v238 offset:12288\n" \
"v_mfma_f32_16x16x32_f16 v[12:15], v[140:143], v[176:179], v[12:15]\n" \
"v_pk_add_f16 v176, v176, v180\n" \
"v_pk_add_f16 v177, v177, v181\n" \
"ds_read_b64_tr_b16 v[188:189], v239 offset:12288\n" \
"ds_read_b64_tr_b16 v[190:191], v239 offset:12288\n" \
"v_mfma_f32_16x16x32_f16 v[4:7], v[152:155], v[180:183], v[4:7]\n" \
"v_pk_add_f16 v178, v178, v182\n" \
"v_pk_add_f16 v179, v179, v183\n" \
"v_cmp_eq_u32 vcc, 0, v253\n" \
"s_cbranch_vccnz .Ls03_%=\n" \
".Lr03_%=:\n" \
"buffer_store_dwordx4 v[220:223], v235, s[36:39], s66 offen sc1\n" \
"v_mfma_f32_16x16x32_f16 v[16:19], v[156:159], v[180:183], v[16:19]\n" \
"ds_read_b128 v[172:175], v240 offset:45568\n" \
"s_add_i32 m0, s44, 0x4000\n" \
"s_add_i32 s80, s46, 0x2400000\n" \
"v_mfma_f32_16x16x32_f16 v[8:11], v[120:123], v[176:179], v[8:11]\n" \
"buffer_load_dwordx4 v232, s[36:39], s80 offen sc1 lds\n" \
"s_waitcnt lgkmcnt(3)\n" \
"v_mfma_f32_16x16x32_bf16 v[184:187], v[184:187], v[192:195], 0\n" \
"v_mfma_f32_16x16x32_f16 v[20:23], v[124:127], v[176:179], v[20:23]\n" \
"ds_read_b64_tr_b16 v[128:129], v241 offset:32768\n" \
"ds_read_b64_tr_b16 v[130:131], v241 offset:40960\n" \
"s_waitcnt lgkmcnt(3)\n" \
"v_mfma_f32_16x16x32_bf16 v[188:191], v[188:191], v[192:195], 0\n" \
"v_mfma_f32_16x16x32_f16 v[24:27], v[136:139], v[160:163], v[24:27]\n" \
"ds_read_b128 v[176:179], v240 offset:47104\n" \
"s_add_i32 s84, s55, 0x800a000\n" \
"v_mfma_f32_16x16x32_f16 v[36:39], v[140:143], v[160:163], v[36:39]\n" \
"v_pk_add_f16 v160, v160, v164\n" \
"v_pk_add_f16 v161, v161, v165\n" \
"buffer_store_dword v240, v243, s[36:39], s84 offen sc1\n" \
"v_mfma_f32_16x16x32_f16 v[28:31], v[152:155], v[164:167], v[28:31]\n" \
"v_pk_add_f16 v162, v162, v166\n" \
"v_pk_add_f16 v163, v163, v167\n" \
"v_cvt_pk_f16_f32 v184, v184, v185\n" \
"ds_read_b64_tr_b16 v[132:133], v242 offset:32768\n" \
"v_mfma_f32_16x16x32_f16 v[40:43], v[156:159], v[164:167], v[40:43]\n" \
"v_cvt_pk_f16_f32 v185, v186, v187\n" \
"v_cvt_pk_f16_f32 v186, v188, v189\n" \
"ds_read_b128 v[180:183], v240 offset:47616\n" \
"v_mfma_f32_16x16x32_f16 v[32:35], v[120:123], v[160:163], v[32:35]\n" \
"v_cvt_pk_f16_f32 v187, v190, v191\n" \
"v_add_u32 v184, s91, v184\n" \
"buffer_load_dwordx4 v232, s[36:39], s80 offen offset:1024 sc1 lds\n" \
"v_mfma_f32_16x16x32_f16 v[44:47], v[124:127], v[160:163], v[44:47]\n" \
"v_add_u32 v185, s91, v185\n" \
"v_add_u32 v186, s91, v186\n" \
"ds_read_b64_tr_b16 v[134:135], v242 offset:40960\n" \
"v_mfma_f32_16x16x32_f16 v[48:51], v[136:139], v[168:171], v[48:51]\n" \
"v_add_u32 v187, s91, v187\n" \
"v_and_b32 v184, s92, v184\n" \
"ds_read_b128 v[160:163], v240 offset:49152\n" \
"v_mfma_f32_16x16x32_f16 v[60:63], v[140:143], v[168:171], v[60:63]\n" \
"s_waitcnt lgkmcnt(4)\n" \
"v_pk_add_f16 v168, v168, v172\n" \
"v_pk_add_f16 v169, v169, v173\n" \
"v_and_b32 v185, s92, v185\n" \
"v_mfma_f32_16x16x32_f16 v[52:55], v[152:155], v[172:175], v[52:55]\n" \
"v_pk_add_f16 v170, v170, v174\n" \
"v_pk_add_f16 v171, v171, v175\n" \
"v_and_b32 v186, s92, v186\n" \
"v_and_b32 v187, s92, v187\n" \
"v_mfma_f32_16x16x32_f16 v[64:67], v[156:159], v[172:175], v[64:67]\n" \
"ds_read_b128 v[164:167], v240 offset:49664\n" \
"buffer_store_dwordx4 v[184:187], v234, s[36:39], s82 offen offset:1024 sc1\n" \
"ds_read_b64_tr_b16 v[144:145], v241 offset:33280\n" \
"v_mfma_f32_16x16x32_f16 v[56:59], v[120:123], v[168:171], v[56:59]\n" \
"buffer_load_dwordx4 v232, s[36:39], s80 offen offset:2048 sc1 lds\n" \
"v_mfma_f32_16x16x32_f16 v[68:71], v[124:127], v[168:171], v[68:71]\n" \
"ds_read_b64_tr_b16 v[146:147], v241 offset:41472\n" \
"ds_read_b64_tr_b16 v[148:149], v242 offset:33280\n" \
"v_mfma_f32_16x16x32_f16 v[72:75], v[136:139], v[176:179], v[72:75]\n" \
"ds_read_b128 v[168:171], v240 offset:40960\n" \
"ds_read_b64_tr_b16 v[150:151], v242 offset:41472\n" \
"v_mfma_f32_16x16x32_f16 v[84:87], v[140:143], v[176:179], v[84:87]\n" \
"s_waitcnt lgkmcnt(6)\n" \
"v_pk_add_f16 v176, v176, v180\n" \
"v_pk_add_f16 v177, v177, v181\n" \
"v_mfma_f32_16x16x32_f16 v[76:79], v[152:155], v[180:183], v[76:79]\n" \
"v_pk_add_f16 v178, v178, v182\n" \
"v_pk_add_f16 v179, v179, v183\n" \
"v_mfma_f32_16x16x32_f16 v[88:91], v[156:159], v[180:183], v[88:91]\n" \
"ds_read_b128 v[172:175], v240 offset:41472\n" \
"v_mfma_f32_16x16x32_f16 v[80:83], v[120:123], v[176:179], v[80:83]\n" \
"buffer_load_dwordx4 v232, s[36:39], s80 offen offset:3072 sc1 lds\n" \
"v_mfma_f32_16x16x32_f16 v[92:95], v[124:127], v[176:179], v[92:95]\n" \
"v_mfma_f32_16x16x32_f16 v[96:99], v[136:139], v[160:163], v[96:99]\n" \
"s_waitcnt lgkmcnt(5)\n" \
"v_pk_add_f16 v136, v128, v144\n" \
"v_pk_add_f16 v137, v129, v145\n" \
"ds_read_b128 v[176:179], v240 offset:43008\n" \
"v_mfma_f32_16x16x32_f16 v[108:111], v[140:143], v[160:163], v[108:111]\n" \
"v_pk_add_f16 v160, v160, v164\n" \
"v_pk_add_f16 v161, v161, v165\n" \
"ds_read_b128 v[180:183], v240 offset:43520\n" \
"v_mfma_f32_16x16x32_f16 v[100:103], v[152:155], v[164:167], v[100:103]\n" \
"v_pk_add_f16 v162, v162, v166\n" \
"v_pk_add_f16 v163, v163, v167\n" \
"v_mfma_f32_16x16x32_f16 v[112:115], v[156:159], v[164:167], v[112:115]\n" \
"s_waitcnt lgkmcnt(3)\n" \
"v_pk_add_f16 v138, v130, v146\n" \
"v_pk_add_f16 v139, v131, v147\n" \
"v_mfma_f32_16x16x32_f16 v[104:107], v[120:123], v[160:163], v[104:107]\n" \
"v_pk_add_f16 v140, v132, v148\n" \
"v_pk_add_f16 v141, v133, v149\n" \
"v_mfma_f32_16x16x32_f16 v[116:119], v[124:127], v[160:163], v[116:119]\n" \
"v_pk_add_f16 v142, v134, v150\n" \
"v_pk_add_f16 v143, v135, v151\n" \
"s_waitcnt vmcnt(7) lgkmcnt(0)\n" \
"s_barrier\n" \
"v_mfma_f32_16x16x32_f16 v[0:3], v[128:131], v[168:171], v[0:3]\n" \
"ds_read_b128 v[160:163], v240 offset:4096\n" \
"s_mov_b32 s81, 0x8008000\n" \
"ds_read_b64_tr_b16 v[184:185], v236 offset:16384\n" \
"ds_read_b64_tr_b16 v[186:187], v236 offset:16384\n" \
"v_mfma_f32_16x16x32_f16 v[12:15], v[132:135], v[168:171], v[12:15]\n" \
"v_pk_add_f16 v168, v168, v172\n" \
"v_pk_add_f16 v169, v169, v173\n" \
"buffer_load_dword v253, v252, s[36:39], s81 offen sc1\n" \
"v_mfma_f32_16x16x32_f16 v[4:7], v[144:147], v[172:175], v[4:7]\n" \
"v_pk_add_f16 v170, v170, v174\n" \
"v_pk_add_f16 v171, v171, v175\n" \
"ds_read_b64_tr_b16 v[188:189], v237 offset:16384\n" \
"ds_read_b64_tr_b16 v[190:191], v237 offset:16384\n" \
"v_mfma_f32_16x16x32_f16 v[16:19], v[148:151], v[172:175], v[16:19]\n" \
"ds_read_b128 v[164:167], v240 offset:4608\n" \
"s_add_i32 m0, s44, 0x8000\n" \
"s_add_i32 s80, s46, 0x2800000\n" \
"v_mfma_f32_16x16x32_f16 v[8:11], v[136:139], v[168:171], v[8:11]\n" \
"buffer_load_dwordx4 v232, s[36:39], s80 offen sc1 lds\n" \
"s_waitcnt lgkmcnt(3)\n" \
"v_mfma_f32_16x16x32_bf16 v[184:187], v[184:187], v[192:195], 0\n" \
"v_mfma_f32_16x16x32_f16 v[20:23], v[140:143], v[168:171], v[20:23]\n" \
"ds_read_b64_tr_b16 v[120:121], v241 offset:0\n" \
"ds_read_b64_tr_b16 v[122:123], v241 offset:8192\n" \
"s_waitcnt lgkmcnt(3)\n" \
"v_mfma_f32_16x16x32_bf16 v[188:191], v[188:191], v[192:195], 0\n" \
"v_mfma_f32_16x16x32_f16 v[24:27], v[128:131], v[176:179], v[24:27]\n" \
"ds_read_b128 v[168:171], v240 offset:6144\n" \
"v_mfma_f32_16x16x32_f16 v[36:39], v[132:135], v[176:179], v[36:39]\n" \
"v_pk_add_f16 v176, v176, v180\n" \
"v_pk_add_f16 v177, v177, v181\n" \
"ds_read_b64_tr_b16 v[124:125], v242 offset:0\n" \
"v_mfma_f32_16x16x32_f16 v[28:31], v[144:147], v[180:183], v[28:31]\n" \
"v_pk_add_f16 v178, v178, v182\n" \
"v_pk_add_f16 v179, v179, v183\n" \
"v_cvt_pk_f16_f32 v184, v184, v185\n" \
"v_mfma_f32_16x16x32_f16 v[40:43], v[148:151], v[180:183], v[40:43]\n" \
"v_cvt_pk_f16_f32 v185, v186, v187\n" \
"v_cvt_pk_f16_f32 v186, v188, v189\n" \
"ds_read_b128 v[172:175], v240 offset:6656\n" \
"v_mfma_f32_16x16x32_f16 v[32:35], v[136:139], v[176:179], v[32:35]\n" \
"v_cvt_pk_f16_f32 v187, v190, v191\n" \
"v_add_u32 v184, s91, v184\n" \
"buffer_load_dwordx4 v232, s[36:39], s80 offen offset:1024 sc1 lds\n" \
"v_mfma_f32_16x16x32_f16 v[44:47], v[140:143], v[176:179], v[44:47]\n" \
"v_add_u32 v185, s91, v185\n" \
"v_add_u32 v186, s91, v186\n" \
"ds_read_b64_tr_b16 v[126:127], v242 offset:8192\n" \
"v_mfma_f32_16x16x32_f16 v[48:51], v[128:131], v[160:163], v[48:51]\n" \
"v_add_u32 v187, s91, v187\n" \
"v_and_b32 v184, s92, v184\n" \
"ds_read_b128 v[176:179], v240 offset:8192\n" \
"v_mfma_f32_16x16x32_f16 v[60:63], v[132:135], v[160:163], v[60:63]\n" \
"s_waitcnt lgkmcnt(4)\n" \
"v_pk_add_f16 v160, v160, v164\n" \
"v_pk_add_f16 v161, v161, v165\n" \
"v_and_b32 v185, s92, v185\n" \
"s_add_i32 s82, s49, 0x4800000\n" \
"v_mfma_f32_16x16x32_f16 v[52:55], v[144:147], v[164:167], v[52:55]\n" \
"v_pk_add_f16 v162, v162, v166\n" \
"v_pk_add_f16 v163, v163, v167\n" \
"v_and_b32 v186, s92, v186\n" \
"v_and_b32 v187, s92, v187\n" \
"v_mfma_f32_16x16x32_f16 v[64:67], v[148:151], v[164:167], v[64:67]\n" \
"ds_read_b128 v[180:183], v240 offset:8704\n" \
"buffer_store_dwordx4 v[184:187], v234, s[36:39], s82 offen sc1\n" \
"ds_read_b64_tr_b16 v[152:153], v241 offset:512\n" \
"v_mfma_f32_16x16x32_f16 v[56:59], v[136:139], v[160:163], v[56:59]\n" \
"buffer_load_dwordx4 v232, s[36:39], s80 offen offset:2048 sc1 lds\n" \
"v_mfma_f32_16x16x32_f16 v[68:71], v[140:143], v[160:163], v[68:71]\n" \
"ds_read_b64_tr_b16 v[154:155], v241 offset:8704\n" \
"ds_read_b64_tr_b16 v[156:157], v242 offset:512\n" \
"v_mfma_f32_16x16x32_f16 v[72:75], v[128:131], v[168:171], v[72:75]\n" \
"ds_read_b128 v[160:163], v240 offset:0\n" \
"ds_read_b64_tr_b16 v[158:159], v242 offset:8704\n" \
"v_mfma_f32_16x16x32_f16 v[84:87], v[132:135], v[168:171], v[84:87]\n" \
"s_waitcnt lgkmcnt(6)\n" \
"v_pk_add_f16 v168, v168, v172\n" \
"v_pk_add_f16 v169, v169, v173\n" \
"v_mfma_f32_16x16x32_f16 v[76:79], v[144:147], v[172:175], v[76:79]\n" \
"v_pk_add_f16 v170, v170, v174\n" \
"v_pk_add_f16 v171, v171, v175\n" \
"v_mfma_f32_16x16x32_f16 v[88:91], v[148:151], v[172:175], v[88:91]\n" \
"ds_read_b128 v[164:167], v240 offset:512\n" \
"v_mfma_f32_16x16x32_f16 v[80:83], v[136:139], v[168:171], v[80:83]\n" \
"buffer_load_dwordx4 v232, s[36:39], s80 offen offset:3072 sc1 lds\n" \
"v_mfma_f32_16x16x32_f16 v[92:95], v[140:143], v[168:171], v[92:95]\n" \
"v_mfma_f32_16x16x32_f16 v[96:99], v[128:131], v[176:179], v[96:99]\n" \
"s_waitcnt lgkmcnt(5)\n" \
"v_pk_add_f16 v128, v120, v152\n" \
"v_pk_add_f16 v129, v121, v153\n" \
"ds_read_b128 v[168:171], v240 offset:2048\n" \
"v_mfma_f32_16x16x32_f16 v[108:111], v[132:135], v[176:179], v[108:111]\n" \
"v_pk_add_f16 v176, v176, v180\n" \
"v_pk_add_f16 v177, v177, v181\n" \
"ds_read_b128 v[172:175], v240 offset:2560\n" \
"v_mfma_f32_16x16x32_f16 v[100:103], v[144:147], v[180:183], v[100:103]\n" \
"v_pk_add_f16 v178, v178, v182\n" \
"v_pk_add_f16 v179, v179, v183\n" \
"v_mfma_f32_16x16x32_f16 v[112:115], v[148:151], v[180:183], v[112:115]\n" \
"s_waitcnt lgkmcnt(3)\n" \
"v_pk_add_f16 v130, v122, v154\n" \
"v_pk_add_f16 v131, v123, v155\n" \
"v_mfma_f32_16x16x32_f16 v[104:107], v[136:139], v[176:179], v[104:107]\n" \
"v_pk_add_f16 v132, v124, v156\n" \
"v_pk_add_f16 v133, v125, v157\n" \
"v_mfma_f32_16x16x32_f16 v[116:119], v[140:143], v[176:179], v[116:119]\n" \
"v_pk_add_f16 v134, v126, v158\n" \
"v_pk_add_f16 v135, v127, v159\n" \
"s_waitcnt vmcnt(5) lgkmcnt(0)\n" \
"s_barrier\n" \
"v_mfma_f32_16x16x32_f16 v[0:3], v[120:123], v[160:163], v[0:3]\n" \
"ds_read_b128 v[176:179], v240 offset:24576\n" \
"ds_read_b64_tr_b16 v[184:185], v238 offset:16384\n" \
"ds_read_b64_tr_b16 v[186:187], v238 offset:16384\n" \
"v_mfma_f32_16x16x32_f16 v[12:15], v[124:127], v[160:163], v[12:15]\n" \
"v_pk_add_f16 v160, v160, v164\n" \
"v_pk_add_f16 v161, v161, v165\n" \
"ds_read_b64_tr_b16 v[188:189], v239 offset:16384\n" \
"ds_read_b64_tr_b16 v[190:191], v239 offset:16384\n" \
"v_mfma_f32_16x16x32_f16 v[4:7], v[152:155], v[164:167], v[4:7]\n" \
"v_pk_add_f16 v162, v162, v166\n" \
"v_pk_add_f16 v163, v163, v167\n" \
"v_cmp_eq_u32 vcc, 0, v253\n" \
"s_cbranch_vccnz .Ls04_%=\n" \
".Lr04_%=:\n" \
"v_mfma_f32_16x16x32_f16 v[16:19], v[156:159], v[164:167], v[16:19]\n" \
"ds_read_b128 v[180:183], v240 offset:25088\n" \
"s_add_i32 m0, s44, 0x0\n" \
"s_add_i32 s80, s46, 0x2c00000\n" \
"v_mfma_f32_16x16x32_f16 v[8:11], v[128:131], v[160:163], v[8:11]\n" \
"buffer_load_dwordx4 v232, s[36:39], s80 offen sc1 lds\n" \
"s_waitcnt lgkmcnt(3)\n" \
"v_mfma_f32_16x16x32_bf16 v[184:187], v[184:187], v[192:195], 0\n" \
"v_mfma_f32_16x16x32_f16 v[20:23], v[132:135], v[160:163], v[20:23]\n" \
"ds_read_b64_tr_b16 v[136:137], v241 offset:16384\n" \
"ds_read_b64_tr_b16 v[138:139], v241 offset:24576\n" \
"s_waitcnt lgkmcnt(3)\n" \
"v_mfma_f32_16x16x32_bf16 v[188:191], v[188:191], v[192:195], 0\n" \
"v_mfma_f32_16x16x32_f16 v[24:27], v[120:123], v[168:171], v[24:27]\n" \
"ds_read_b128 v[160:163], v240 offset:26624\n" \
"s_add_i32 s84, s52, 0x800c000\n" \
"v_mfma_f32_16x16x32_f16 v[36:39], v[124:127], v[168:171], v[36:39]\n" \
"v_pk_add_f16 v168, v168, v172\n" \
"v_pk_add_f16 v169, v169, v173\n" \
"buffer_store_dword v240, v243, s[36:39], s84 offen sc1\n" \
"v_mfma_f32_16x16x32_f16 v[28:31], v[152:155], v[172:175], v[28:31]\n" \
"v_pk_add_f16 v170, v170, v174\n" \
"v_pk_add_f16 v171, v171, v175\n" \
"v_cvt_pk_f16_f32 v184, v184, v185\n" \
"ds_read_b64_tr_b16 v[140:141], v242 offset:16384\n" \
"v_mfma_f32_16x16x32_f16 v[40:43], v[156:159], v[172:175], v[40:43]\n" \
"v_cvt_pk_f16_f32 v185, v186, v187\n" \
"v_cvt_pk_f16_f32 v186, v188, v189\n" \
"ds_read_b128 v[164:167], v240 offset:27136\n" \
"v_mfma_f32_16x16x32_f16 v[32:35], v[128:131], v[168:171], v[32:35]\n" \
"v_cvt_pk_f16_f32 v187, v190, v191\n" \
"v_add_u32 v184, s91, v184\n" \
"buffer_load_dwordx4 v232, s[36:39], s80 offen offset:1024 sc1 lds\n" \
"v_mfma_f32_16x16x32_f16 v[44:47], v[132:135], v[168:171], v[44:47]\n" \
"v_add_u32 v185, s91, v185\n" \
"v_add_u32 v186, s91, v186\n" \
"ds_read_b64_tr_b16 v[142:143], v242 offset:24576\n" \
"v_mfma_f32_16x16x32_f16 v[48:51], v[120:123], v[176:179], v[48:51]\n" \
"v_add_u32 v187, s91, v187\n" \
"v_and_b32 v184, s92, v184\n" \
"ds_read_b128 v[168:171], v240 offset:28672\n" \
"v_mfma_f32_16x16x32_f16 v[60:63], v[124:127], v[176:179], v[60:63]\n" \
"s_waitcnt lgkmcnt(4)\n" \
"v_pk_add_f16 v176, v176, v180\n" \
"v_pk_add_f16 v177, v177, v181\n" \
"v_and_b32 v185, s92, v185\n" \
"v_mfma_f32_16x16x32_f16 v[52:55], v[152:155], v[180:183], v[52:55]\n" \
"v_pk_add_f16 v178, v178, v182\n" \
"v_pk_add_f16 v179, v179, v183\n" \
"v_and_b32 v186, s92, v186\n" \
"v_and_b32 v187, s92, v187\n" \
"v_mfma_f32_16x16x32_f16 v[64:67], v[156:159], v[180:183], v[64:67]\n" \
"ds_read_b128 v[172:175], v240 offset:29184\n" \
"buffer_store_dwordx4 v[184:187], v234, s[36:39], s82 offen offset:1024 sc1\n" \
"ds_read_b64_tr_b16 v[144:145], v241 offset:16896\n" \
"v_mfma_f32_16x16x32_f16 v[56:59], v[128:131], v[176:179], v[56:59]\n" \
"buffer_load_dwordx4 v232, s[36:39], s80 offen offset:2048 sc1 lds\n" \
"v_mfma_f32_16x16x32_f16 v[68:71], v[132:135], v[176:179], v[68:71]\n" \
"ds_read_b64_tr_b16 v[146:147], v241 offset:25088\n" \
"ds_read_b64_tr_b16 v[148:149], v242 offset:16896\n" \
"v_mfma_f32_16x16x32_f16 v[72:75], v[120:123], v[160:163], v[72:75]\n" \
"ds_read_b128 v[176:179], v240 offset:20480\n" \
"ds_read_b64_tr_b16 v[150:151], v242 offset:25088\n" \
"v_mfma_f32_16x16x32_f16 v[84:87], v[124:127], v[160:163], v[84:87]\n" \
"s_waitcnt lgkmcnt(6)\n" \
"v_pk_add_f16 v160, v160, v164\n" \
"v_pk_add_f16 v161, v161, v165\n" \
"v_mfma_f32_16x16x32_f16 v[76:79], v[152:155], v[164:167], v[76:79]\n" \
"v_pk_add_f16 v162, v162, v166\n" \
"v_pk_add_f16 v163, v163, v167\n" \
"v_mfma_f32_16x16x32_f16 v[88:91], v[156:159], v[164:167], v[88:91]\n" \
"ds_read_b128 v[180:183], v240 offset:20992\n" \
"v_mfma_f32_16x16x32_f16 v[80:83], v[128:131], v[160:163], v[80:83]\n" \
"buffer_load_dwordx4 v232, s[36:39], s80 offen offset:3072 sc1 lds\n" \
"v_mfma_f32_16x16x32_f16 v[92:95], v[132:135], v[160:163], v[92:95]\n" \
"v_mfma_f32_16x16x32_f16 v[96:99], v[120:123], v[168:171], v[96:99]\n" \
"s_waitcnt lgkmcnt(5)\n" \
"v_pk_add_f16 v120, v136, v144\n" \
"v_pk_add_f16 v121, v137, v145\n" \
"ds_read_b128 v[160:163], v240 offset:22528\n" \
"v_mfma_f32_16x16x32_f16 v[108:111], v[124:127], v[168:171], v[108:111]\n" \
"v_pk_add_f16 v168, v168, v172\n" \
"v_pk_add_f16 v169, v169, v173\n" \
"ds_read_b128 v[164:167], v240 offset:23040\n" \
"v_mfma_f32_16x16x32_f16 v[100:103], v[152:155], v[172:175], v[100:103]\n" \
"v_pk_add_f16 v170, v170, v174\n" \
"v_pk_add_f16 v171, v171, v175\n" \
"v_mfma_f32_16x16x32_f16 v[112:115], v[156:159], v[172:175], v[112:115]\n" \
"s_waitcnt lgkmcnt(3)\n" \
"v_pk_add_f16 v122, v138, v146\n" \
"v_pk_add_f16 v123, v139, v147\n" \
"v_mfma_f32_16x16x32_f16 v[104:107], v[128:131], v[168:171], v[104:107]\n" \
"v_pk_add_f16 v124, v140, v148\n" \
"v_pk_add_f16 v125, v141, v149\n" \
"v_mfma_f32_16x16x32_f16 v[116:119], v[132:135], v[168:171], v[116:119]\n" \
"v_pk_add_f16 v126, v142, v150\n" \
"v_pk_add_f16 v127, v143, v151\n" \
"s_waitcnt vmcnt(6) lgkmcnt(0)\n" \
"s_barrier\n" \
"v_mfma_f32_16x16x32_f16 v[0:3], v[136:139], v[176:179], v[0:3]\n" \
"ds_read_b128 v[168:171], v240 offset:45056\n" \
"s_mov_b32 s81, 0x800a000\n" \
"ds_read_b64_tr_b16 v[184:185], v236 offset:20480\n" \
"ds_read_b64_tr_b16 v[186:187], v236 offset:20480\n" \
"v_mfma_f32_16x16x32_f16 v[12:15], v[140:143], v[176:179], v[12:15]\n" \
"v_pk_add_f16 v176, v176, v180\n" \
"v_pk_add_f16 v177, v177, v181\n" \
"buffer_load_dword v253, v252, s[36:39], s81 offen sc1\n" \
"ds_read_b64_tr_b16 v[188:189], v237 offset:20480\n" \
"v_mfma_f32_16x16x32_f16 v[4:7], v[144:147], v[180:183], v[4:7]\n" \
"v_pk_add_f16 v178, v178, v182\n" \
"v_pk_add_f16 v179, v179, v183\n" \
"buffer_store_dwordx4 v[224:227], v235, s[36:39], s67 offen sc1\n" \
"ds_read_b64_tr_b16 v[190:191], v237 offset:20480\n" \
"v_mfma_f32_16x16x32_f16 v[16:19], v[148:151], v[180:183], v[16:19]\n" \
"ds_read_b128 v[172:175], v240 offset:45568\n" \
"s_add_i32 m0, s44, 0x4000\n" \
"s_add_i32 s80, s46, 0x3000000\n" \
"v_mfma_f32_16x16x32_f16 v[8:11], v[120:123], v[176:179], v[8:11]\n" \
"buffer_load_dwordx4 v232, s[36:39], s80 offen sc1 lds\n" \
"s_waitcnt lgkmcnt(3)\n" \
"v_mfma_f32_16x16x32_bf16 v[184:187], v[184:187], v[192:195], 0\n" \
"v_mfma_f32_16x16x32_f16 v[20:23], v[124:127], v[176:179], v[20:23]\n" \
"ds_read_b64_tr_b16 v[128:129], v241 offset:32768\n" \
"ds_read_b64_tr_b16 v[130:131], v241 offset:40960\n" \
"s_waitcnt lgkmcnt(3)\n" \
"v_mfma_f32_16x16x32_bf16 v[188:191], v[188:191], v[192:195], 0\n" \
"v_mfma_f32_16x16x32_f16 v[24:27], v[136:139], v[160:163], v[24:27]\n" \
"ds_read_b128 v[176:179], v240 offset:47104\n" \
"v_mfma_f32_16x16x32_f16 v[36:39], v[140:143], v[160:163], v[36:39]\n" \
"v_pk_add_f16 v160, v160, v164\n" \
"v_pk_add_f16 v161, v161, v165\n" \
"ds_read_b64_tr_b16 v[132:133], v242 offset:32768\n" \
"v_mfma_f32_16x16x32_f16 v[28:31], v[144:147], v[164:167], v[28:31]\n" \
"v_pk_add_f16 v162, v162, v166\n" \
"v_pk_add_f16 v163, v163, v167\n" \
"v_cvt_pk_f16_f32 v184, v184, v185\n" \
"v_mfma_f32_16x16x32_f16 v[40:43], v[148:151], v[164:167], v[40:43]\n" \
"v_cvt_pk_f16_f32 v185, v186, v187\n" \
"v_cvt_pk_f16_f32 v186, v188, v189\n" \
"ds_read_b128 v[180:183], v240 offset:47616\n" \
"v_mfma_f32_16x16x32_f16 v[32:35], v[120:123], v[160:163], v[32:35]\n" \
"v_cvt_pk_f16_f32 v187, v190, v191\n" \
"v_add_u32 v184, s91, v184\n" \
"buffer_load_dwordx4 v232, s[36:39], s80 offen offset:1024 sc1 lds\n" \
"v_mfma_f32_16x16x32_f16 v[44:47], v[124:127], v[160:163], v[44:47]\n" \
"v_add_u32 v185, s91, v185\n" \
"v_add_u32 v186, s91, v186\n" \
"ds_read_b64_tr_b16 v[134:135], v242 offset:40960\n" \
"v_mfma_f32_16x16x32_f16 v[48:51], v[136:139], v[168:171], v[48:51]\n" \
"v_add_u32 v187, s91, v187\n" \
"v_and_b32 v184, s92, v184\n" \
"ds_read_b128 v[160:163], v240 offset:49152\n" \
"v_mfma_f32_16x16x32_f16 v[60:63], v[140:143], v[168:171], v[60:63]\n" \
"s_waitcnt lgkmcnt(4)\n" \
"v_pk_add_f16 v168, v168, v172\n" \
"v_pk_add_f16 v169, v169, v173\n" \
"v_and_b32 v185, s92, v185\n" \
"s_add_i32 s82, s50, 0x5000000\n" \
"v_mfma_f32_16x16x32_f16 v[52:55], v[144:147], v[172:175], v[52:55]\n" \
"v_pk_add_f16 v170, v170, v174\n" \
"v_pk_add_f16 v171, v171, v175\n" \
"v_and_b32 v186, s92, v186\n" \
"v_and_b32 v187, s92, v187\n" \
"v_mfma_f32_16x16x32_f16 v[64:67], v[148:151], v[172:175], v[64:67]\n" \
"ds_read_b128 v[164:167], v240 offset:49664\n" \
"buffer_store_dwordx4 v[184:187], v234, s[36:39], s82 offen sc1\n" \
"ds_read_b64_tr_b16 v[152:153], v241 offset:33280\n" \
"v_mfma_f32_16x16x32_f16 v[56:59], v[120:123], v[168:171], v[56:59]\n" \
"buffer_load_dwordx4 v232, s[36:39], s80 offen offset:2048 sc1 lds\n" \
"v_mfma_f32_16x16x32_f16 v[68:71], v[124:127], v[168:171], v[68:71]\n" \
"ds_read_b64_tr_b16 v[154:155], v241 offset:41472\n" \
"ds_read_b64_tr_b16 v[156:157], v242 offset:33280\n" \
"v_mfma_f32_16x16x32_f16 v[72:75], v[136:139], v[176:179], v[72:75]\n" \
"ds_read_b128 v[168:171], v240 offset:40960\n" \
"ds_read_b64_tr_b16 v[158:159], v242 offset:41472\n" \
"v_mfma_f32_16x16x32_f16 v[84:87], v[140:143], v[176:179], v[84:87]\n" \
"s_waitcnt lgkmcnt(6)\n" \
"v_pk_add_f16 v176, v176, v180\n" \
"v_pk_add_f16 v177, v177, v181\n" \
"v_mfma_f32_16x16x32_f16 v[76:79], v[144:147], v[180:183], v[76:79]\n" \
"v_pk_add_f16 v178, v178, v182\n" \
"v_pk_add_f16 v179, v179, v183\n" \
"v_mfma_f32_16x16x32_f16 v[88:91], v[148:151], v[180:183], v[88:91]\n" \
"ds_read_b128 v[172:175], v240 offset:41472\n" \
"v_mfma_f32_16x16x32_f16 v[80:83], v[120:123], v[176:179], v[80:83]\n" \
"buffer_load_dwordx4 v232, s[36:39], s80 offen offset:3072 sc1 lds\n" \
"v_mfma_f32_16x16x32_f16 v[92:95], v[124:127], v[176:179], v[92:95]\n" \
"v_mfma_f32_16x16x32_f16 v[96:99], v[136:139], v[160:163], v[96:99]\n" \
"s_waitcnt lgkmcnt(5)\n" \
"v_pk_add_f16 v136, v128, v152\n" \
"v_pk_add_f16 v137, v129, v153\n" \
"ds_read_b128 v[176:179], v240 offset:43008\n" \
"v_mfma_f32_16x16x32_f16 v[108:111], v[140:143], v[160:163], v[108:111]\n" \
"v_pk_add_f16 v160, v160, v164\n" \
"v_pk_add_f16 v161, v161, v165\n" \
"ds_read_b128 v[180:183], v240 offset:43520\n" \
"v_mfma_f32_16x16x32_f16 v[100:103], v[144:147], v[164:167], v[100:103]\n" \
"v_pk_add_f16 v162, v162, v166\n" \
"v_pk_add_f16 v163, v163, v167\n" \
"v_mfma_f32_16x16x32_f16 v[112:115], v[148:151], v[164:167], v[112:115]\n" \
"s_waitcnt lgkmcnt(3)\n" \
"v_pk_add_f16 v138, v130, v154\n" \
"v_pk_add_f16 v139, v131, v155\n" \
"v_mfma_f32_16x16x32_f16 v[104:107], v[120:123], v[160:163], v[104:107]\n" \
"v_pk_add_f16 v140, v132, v156\n" \
"v_pk_add_f16 v141, v133, v157\n" \
"v_mfma_f32_16x16x32_f16 v[116:119], v[124:127], v[160:163], v[116:119]\n" \
"v_pk_add_f16 v142, v134, v158\n" \
"v_pk_add_f16 v143, v135, v159\n" \
"s_waitcnt vmcnt(6) lgkmcnt(0)\n" \
"s_barrier\n" \
"v_mfma_f32_16x16x32_f16 v[0:3], v[128:131], v[168:171], v[0:3]\n" \
"ds_read_b128 v[160:163], v240 offset:4096\n" \
"ds_read_b64_tr_b16 v[184:185], v238 offset:20480\n" \
"ds_read_b64_tr_b16 v[186:187], v238 offset:20480\n" \
"v_mfma_f32_16x16x32_f16 v[12:15], v[132:135], v[168:171], v[12:15]\n" \
"v_pk_add_f16 v168, v168, v172\n" \
"v_pk_add_f16 v169, v169, v173\n" \
"ds_read_b64_tr_b16 v[188:189], v239 offset:20480\n" \
"ds_read_b64_tr_b16 v[190:191], v239 offset:20480\n" \
"v_mfma_f32_16x16x32_f16 v[4:7], v[152:155], v[172:175], v[4:7]\n" \
"v_pk_add_f16 v170, v170, v174\n" \
"v_pk_add_f16 v171, v171, v175\n" \
"v_cmp_eq_u32 vcc, 0, v253\n" \
"s_cbranch_vccnz .Ls05_%=\n" \
".Lr05_%=:\n" \
"v_mfma_f32_16x16x32_f16 v[16:19], v[156:159], v[172:175], v[16:19]\n" \
"ds_read_b128 v[164:167], v240 offset:4608\n" \
"s_add_i32 m0, s44, 0x8000\n" \
"s_add_i32 s80, s46, 0x3400000\n" \
"v_mfma_f32_16x16x32_f16 v[8:11], v[136:139], v[168:171], v[8:11]\n" \
"buffer_load_dwordx4 v232, s[36:39], s80 offen sc1 lds\n" \
"s_waitcnt lgkmcnt(3)\n" \
"v_mfma_f32_16x16x32_bf16 v[184:187], v[184:187], v[192:195], 0\n" \
"v_mfma_f32_16x16x32_f16 v[20:23], v[140:143], v[168:171], v[20:23]\n" \
"ds_read_b64_tr_b16 v[120:121], v241 offset:0\n" \
"ds_read_b64_tr_b16 v[122:123], v241 offset:8192\n" \
"s_waitcnt lgkmcnt(3)\n" \
"v_mfma_f32_16x16x32_bf16 v[188:191], v[188:191], v[192:195], 0\n" \
"v_mfma_f32_16x16x32_f16 v[24:27], v[128:131], v[176:179], v[24:27]\n" \
"ds_read_b128 v[168:171], v240 offset:6144\n" \
"s_add_i32 s84, s53, 0x800e000\n" \
"v_mfma_f32_16x16x32_f16 v[36:39], v[132:135], v[176:179], v[36:39]\n" \
"v_pk_add_f16 v176, v176, v180\n" \
"v_pk_add_f16 v177, v177, v181\n" \
"buffer_store_dword v240, v243, s[36:39], s84 offen sc1\n" \
"v_mfma_f32_16x16x32_f16 v[28:31], v[152:155], v[180:183], v[28:31]\n" \
"v_pk_add_f16 v178, v178, v182\n" \
"v_pk_add_f16 v179, v179, v183\n" \
"v_cvt_pk_f16_f32 v184, v184, v185\n" \
"ds_read_b64_tr_b16 v[124:125], v242 offset:0\n" \
"v_mfma_f32_16x16x32_f16 v[40:43], v[156:159], v[180:183], v[40:43]\n" \
"v_cvt_pk_f16_f32 v185, v186, v187\n" \
"v_cvt_pk_f16_f32 v186, v188, v189\n" \
"ds_read_b128 v[172:175], v240 offset:6656\n" \
"v_mfma_f32_16x16x32_f16 v[32:35], v[136:139], v[176:179], v[32:35]\n" \
"v_cvt_pk_f16_f32 v187, v190, v191\n" \
"v_add_u32 v184, s91, v184\n" \
"buffer_load_dwordx4 v232, s[36:39], s80 offen offset:1024 sc1 lds\n" \
"v_mfma_f32_16x16x32_f16 v[44:47], v[140:143], v[176:179], v[44:47]\n" \
"v_add_u32 v185, s91, v185\n" \
"v_add_u32 v186, s91, v186\n" \
"ds_read_b64_tr_b16 v[126:127], v242 offset:8192\n" \
"v_mfma_f32_16x16x32_f16 v[48:51], v[128:131], v[160:163], v[48:51]\n" \
"v_add_u32 v187, s91, v187\n" \
"v_and_b32 v184, s92, v184\n" \
"ds_read_b128 v[176:179], v240 offset:8192\n" \
"v_mfma_f32_16x16x32_f16 v[60:63], v[132:135], v[160:163], v[60:63]\n" \
"s_waitcnt lgkmcnt(4)\n" \
"v_pk_add_f16 v160, v160, v164\n" \
"v_pk_add_f16 v161, v161, v165\n" \
"v_and_b32 v185, s92, v185\n" \
"v_mfma_f32_16x16x32_f16 v[52:55], v[152:155], v[164:167], v[52:55]\n" \
"v_pk_add_f16 v162, v162, v166\n" \
"v_pk_add_f16 v163, v163, v167\n" \
"v_and_b32 v186, s92, v186\n" \
"v_and_b32 v187, s92, v187\n" \
"v_mfma_f32_16x16x32_f16 v[64:67], v[156:159], v[164:167], v[64:67]\n" \
"ds_read_b128 v[180:183], v240 offset:8704\n" \
"buffer_store_dwordx4 v[184:187], v234, s[36:39], s82 offen offset:1024 sc1\n" \
"ds_read_b64_tr_b16 v[144:145], v241 offset:512\n" \
"v_mfma_f32_16x16x32_f16 v[56:59], v[136:139], v[160:163], v[56:59]\n" \
"buffer_load_dwordx4 v232, s[36:39], s80 offen offset:2048 sc1 lds\n" \
"v_mfma_f32_16x16x32_f16 v[68:71], v[140:143], v[160:163], v[68:71]\n" \
"ds_read_b64_tr_b16 v[146:147], v241 offset:8704\n" \
"ds_read_b64_tr_b16 v[148:149], v242 offset:512\n" \
"v_mfma_f32_16x16x32_f16 v[72:75], v[128:131], v[168:171], v[72:75]\n" \
"ds_read_b128 v[160:163], v240 offset:0\n" \
"ds_read_b64_tr_b16 v[150:151], v242 offset:8704\n" \
"v_mfma_f32_16x16x32_f16 v[84:87], v[132:135], v[168:171], v[84:87]\n" \
"s_waitcnt lgkmcnt(6)\n" \
"v_pk_add_f16 v168, v168, v172\n" \
"v_pk_add_f16 v169, v169, v173\n" \
"v_mfma_f32_16x16x32_f16 v[76:79], v[152:155], v[172:175], v[76:79]\n" \
"v_pk_add_f16 v170, v170, v174\n" \
"v_pk_add_f16 v171, v171, v175\n" \
"v_mfma_f32_16x16x32_f16 v[88:91], v[156:159], v[172:175], v[88:91]\n" \
"ds_read_b128 v[164:167], v240 offset:512\n" \
"v_mfma_f32_16x16x32_f16 v[80:83], v[136:139], v[168:171], v[80:83]\n" \
"buffer_load_dwordx4 v232, s[36:39], s80 offen offset:3072 sc1 lds\n" \
"v_mfma_f32_16x16x32_f16 v[92:95], v[140:143], v[168:171], v[92:95]\n" \
"v_mfma_f32_16x16x32_f16 v[96:99], v[128:131], v[176:179], v[96:99]\n" \
"s_waitcnt lgkmcnt(5)\n" \
"v_pk_add_f16 v128, v120, v144\n" \
"v_pk_add_f16 v129, v121, v145\n" \
"ds_read_b128 v[168:171], v240 offset:2048\n" \
"v_mfma_f32_16x16x32_f16 v[108:111], v[132:135], v[176:179], v[108:111]\n" \
"v_pk_add_f16 v176, v176, v180\n" \
"v_pk_add_f16 v177, v177, v181\n" \
"ds_read_b128 v[172:175], v240 offset:2560\n" \
"v_mfma_f32_16x16x32_f16 v[100:103], v[152:155], v[180:183], v[100:103]\n" \
"v_pk_add_f16 v178, v178, v182\n" \
"v_pk_add_f16 v179, v179, v183\n" \
"v_mfma_f32_16x16x32_f16 v[112:115], v[156:159], v[180:183], v[112:115]\n" \
"s_waitcnt lgkmcnt(3)\n" \
"v_pk_add_f16 v130, v122, v146\n" \
"v_pk_add_f16 v131, v123, v147\n" \
"v_mfma_f32_16x16x32_f16 v[104:107], v[136:139], v[176:179], v[104:107]\n" \
"v_pk_add_f16 v132, v124, v148\n" \
"v_pk_add_f16 v133, v125, v149\n" \
"v_mfma_f32_16x16x32_f16 v[116:119], v[140:143], v[176:179], v[116:119]\n" \
"v_pk_add_f16 v134, v126, v150\n" \
"v_pk_add_f16 v135, v127, v151\n" \
"s_waitcnt vmcnt(6) lgkmcnt(0)\n" \
"s_barrier\n" \
"v_mfma_f32_16x16x32_f16 v[0:3], v[120:123], v[160:163], v[0:3]\n" \
"ds_read_b128 v[176:179], v240 offset:24576\n" \
"ds_read_b64_tr_b16 v[184:185], v236 offset:24576\n" \
"ds_read_b64_tr_b16 v[186:187], v236 offset:24576\n" \
"v_mfma_f32_16x16x32_f16 v[12:15], v[124:127], v[160:163], v[12:15]\n" \
"v_pk_add_f16 v160, v160, v164\n" \
"v_pk_add_f16 v161, v161, v165\n" \
"ds_read_b64_tr_b16 v[188:189], v237 offset:24576\n" \
"v_mfma_f32_16x16x32_f16 v[4:7], v[144:147], v[164:167], v[4:7]\n" \
"v_pk_add_f16 v162, v162, v166\n" \
"v_pk_add_f16 v163, v163, v167\n" \
"ds_read_b64_tr_b16 v[190:191], v237 offset:24576\n" \
"v_mfma_f32_16x16x32_f16 v[16:19], v[148:151], v[164:167], v[16:19]\n" \
"ds_read_b128 v[180:183], v240 offset:25088\n" \
"s_add_i32 m0, s44, 0x0\n" \
"s_add_i32 s80, s46, 0x3800000\n" \
"v_mfma_f32_16x16x32_f16 v[8:11], v[128:131], v[160:163], v[8:11]\n" \
"buffer_load_dwordx4 v232, s[36:39], s80 offen sc1 lds\n" \
"s_waitcnt lgkmcnt(3)\n" \
"v_mfma_f32_16x16x32_bf16 v[184:187], v[184:187], v[192:195], 0\n" \
"v_mfma_f32_16x16x32_f16 v[20:23], v[132:135], v[160:163], v[20:23]\n" \
"ds_read_b64_tr_b16 v[136:137], v241 offset:16384\n" \
"ds_read_b64_tr_b16 v[138:139], v241 offset:24576\n" \
"s_waitcnt lgkmcnt(3)\n" \
"v_mfma_f32_16x16x32_bf16 v[188:191], v[188:191], v[192:195], 0\n" \
"v_mfma_f32_16x16x32_f16 v[24:27], v[120:123], v[168:171], v[24:27]\n" \
"ds_read_b128 v[160:163], v240 offset:26624\n" \
"v_mfma_f32_16x16x32_f16 v[36:39], v[124:127], v[168:171], v[36:39]\n" \
"v_pk_add_f16 v168, v168, v172\n" \
"v_pk_add_f16 v169, v169, v173\n" \
"ds_read_b64_tr_b16 v[140:141], v242 offset:16384\n" \
"v_mfma_f32_16x16x32_f16 v[28:31], v[144:147], v[172:175], v[28:31]\n" \
"v_pk_add_f16 v170, v170, v174\n" \
"v_pk_add_f16 v171, v171, v175\n" \
"v_cvt_pk_f16_f32 v184, v184, v185\n" \
"v_mfma_f32_16x16x32_f16 v[40:43], v[148:151], v[172:175], v[40:43]\n" \
"v_cvt_pk_f16_f32 v185, v186, v187\n" \
"v_cvt_pk_f16_f32 v186, v188, v189\n" \
"ds_read_b128 v[164:167], v240 offset:27136\n" \
"v_mfma_f32_16x16x32_f16 v[32:35], v[128:131], v[168:171], v[32:35]\n" \
"v_cvt_pk_f16_f32 v187, v190, v191\n" \
"v_add_u32 v184, s91, v184\n" \
"buffer_load_dwordx4 v232, s[36:39], s80 offen offset:1024 sc1 lds\n" \
"v_mfma_f32_16x16x32_f16 v[44:47], v[132:135], v[168:171], v[44:47]\n" \
"v_add_u32 v185, s91, v185\n" \
"v_add_u32 v186, s91, v186\n" \
"ds_read_b64_tr_b16 v[142:143], v242 offset:24576\n" \
"v_mfma_f32_16x16x32_f16 v[48:51], v[120:123], v[176:179], v[48:51]\n" \
"v_add_u32 v187, s91, v187\n" \
"v_and_b32 v184, s92, v184\n" \
"ds_read_b128 v[168:171], v240 offset:28672\n" \
"v_mfma_f32_16x16x32_f16 v[60:63], v[124:127], v[176:179], v[60:63]\n" \
"s_waitcnt lgkmcnt(4)\n" \
"v_pk_add_f16 v176, v176, v180\n" \
"v_pk_add_f16 v177, v177, v181\n" \
"v_and_b32 v185, s92, v185\n" \
"s_add_i32 s82, s51, 0x5800000\n" \
"v_mfma_f32_16x16x32_f16 v[52:55], v[144:147], v[180:183], v[52:55]\n" \
"v_pk_add_f16 v178, v178, v182\n" \
"v_pk_add_f16 v179, v179, v183\n" \
"v_and_b32 v186, s92, v186\n" \
"v_and_b32 v187, s92, v187\n" \
"v_mfma_f32_16x16x32_f16 v[64:67], v[148:151], v[180:183], v[64:67]\n" \
"ds_read_b128 v[172:175], v240 offset:29184\n" \
"buffer_store_dwordx4 v[184:187], v234, s[36:39], s82 offen sc1\n" \
"ds_read_b64_tr_b16 v[152:153], v241 offset:16896\n" \
"v_mfma_f32_16x16x32_f16 v[56:59], v[128:131], v[176:179], v[56:59]\n" \
"buffer_load_dwordx4 v232, s[36:39], s80 offen offset:2048 sc1 lds\n" \
"v_mfma_f32_16x16x32_f16 v[68:71], v[132:135], v[176:179], v[68:71]\n" \
"ds_read_b64_tr_b16 v[154:155], v241 offset:25088\n" \
"ds_read_b64_tr_b16 v[156:157], v242 offset:16896\n" \
"v_mfma_f32_16x16x32_f16 v[72:75], v[120:123], v[160:163], v[72:75]\n" \
"ds_read_b128 v[176:179], v240 offset:20480\n" \
"ds_read_b64_tr_b16 v[158:159], v242 offset:25088\n" \
"v_mfma_f32_16x16x32_f16 v[84:87], v[124:127], v[160:163], v[84:87]\n" \
"s_waitcnt lgkmcnt(6)\n" \
"v_pk_add_f16 v160, v160, v164\n" \
"v_pk_add_f16 v161, v161, v165\n" \
"v_mfma_f32_16x16x32_f16 v[76:79], v[144:147], v[164:167], v[76:79]\n" \
"v_pk_add_f16 v162, v162, v166\n" \
"v_pk_add_f16 v163, v163, v167\n" \
"v_mfma_f32_16x16x32_f16 v[88:91], v[148:151], v[164:167], v[88:91]\n" \
"ds_read_b128 v[180:183], v240 offset:20992\n" \
"v_mfma_f32_16x16x32_f16 v[80:83], v[128:131], v[160:163], v[80:83]\n" \
"buffer_load_dwordx4 v232, s[36:39], s80 offen offset:3072 sc1 lds\n" \
"v_mfma_f32_16x16x32_f16 v[92:95], v[132:135], v[160:163], v[92:95]\n" \
"v_mfma_f32_16x16x32_f16 v[96:99], v[120:123], v[168:171], v[96:99]\n" \
"s_waitcnt lgkmcnt(5)\n" \
"v_pk_add_f16 v120, v136, v152\n" \
"v_pk_add_f16 v121, v137, v153\n" \
"ds_read_b128 v[160:163], v240 offset:22528\n" \
"v_mfma_f32_16x16x32_f16 v[108:111], v[124:127], v[168:171], v[108:111]\n" \
"v_pk_add_f16 v168, v168, v172\n" \
"v_pk_add_f16 v169, v169, v173\n" \
"ds_read_b128 v[164:167], v240 offset:23040\n" \
"v_mfma_f32_16x16x32_f16 v[100:103], v[144:147], v[172:175], v[100:103]\n" \
"v_pk_add_f16 v170, v170, v174\n" \
"v_pk_add_f16 v171, v171, v175\n" \
"v_mfma_f32_16x16x32_f16 v[112:115], v[148:151], v[172:175], v[112:115]\n" \
"s_waitcnt lgkmcnt(3)\n" \
"v_pk_add_f16 v122, v138, v154\n" \
"v_pk_add_f16 v123, v139, v155\n" \
"v_mfma_f32_16x16x32_f16 v[104:107], v[128:131], v[168:171], v[104:107]\n" \
"v_pk_add_f16 v124, v140, v156\n" \
"v_pk_add_f16 v125, v141, v157\n" \
"v_mfma_f32_16x16x32_f16 v[116:119], v[132:135], v[168:171], v[116:119]\n" \
"v_pk_add_f16 v126, v142, v158\n" \
"v_pk_add_f16 v127, v143, v159\n" \
"s_waitcnt vmcnt(5) lgkmcnt(0)\n" \
"s_barrier\n" \
"v_mfma_f32_16x16x32_f16 v[0:3], v[136:139], v[176:179], v[0:3]\n" \
"ds_read_b128 v[168:171], v240 offset:45056\n" \
"ds_read_b64_tr_b16 v[184:185], v238 offset:24576\n" \
"ds_read_b64_tr_b16 v[186:187], v238 offset:24576\n" \
"v_mfma_f32_16x16x32_f16 v[12:15], v[140:143], v[176:179], v[12:15]\n" \
"v_pk_add_f16 v176, v176, v180\n" \
"v_pk_add_f16 v177, v177, v181\n" \
"ds_read_b64_tr_b16 v[188:189], v239 offset:24576\n" \
"ds_read_b64_tr_b16 v[190:191], v239 offset:24576\n" \
"v_mfma_f32_16x16x32_f16 v[4:7], v[152:155], v[180:183], v[4:7]\n" \
"v_pk_add_f16 v178, v178, v182\n" \
"v_pk_add_f16 v179, v179, v183\n" \
"buffer_store_dwordx4 v[228:231], v235, s[36:39], s68 offen sc1\n" \
"v_mfma_f32_16x16x32_f16 v[16:19], v[156:159], v[180:183], v[16:19]\n" \
"ds_read_b128 v[172:175], v240 offset:45568\n" \
"s_add_i32 m0, s44, 0x4000\n" \
"s_add_i32 s80, s46, 0x3c00000\n" \
"v_mfma_f32_16x16x32_f16 v[8:11], v[120:123], v[176:179], v[8:11]\n" \
"buffer_load_dwordx4 v232, s[36:39], s80 offen sc1 lds\n" \
"s_waitcnt lgkmcnt(3)\n" \
"v_mfma_f32_16x16x32_bf16 v[184:187], v[184:187], v[192:195], 0\n" \
"v_mfma_f32_16x16x32_f16 v[20:23], v[124:127], v[176:179], v[20:23]\n" \
"ds_read_b64_tr_b16 v[128:129], v241 offset:32768\n" \
"ds_read_b64_tr_b16 v[130:131], v241 offset:40960\n" \
"s_waitcnt lgkmcnt(3)\n" \
"v_mfma_f32_16x16x32_bf16 v[188:191], v[188:191], v[192:195], 0\n" \
"v_mfma_f32_16x16x32_f16 v[24:27], v[136:139], v[160:163], v[24:27]\n" \
"ds_read_b128 v[176:179], v240 offset:47104\n" \
"s_add_i32 s84, s54, 0x8010000\n" \
"v_mfma_f32_16x16x32_f16 v[36:39], v[140:143], v[160:163], v[36:39]\n" \
"v_pk_add_f16 v160, v160, v164\n" \
"v_pk_add_f16 v161, v161, v165\n" \
"buffer_store_dword v240, v243, s[36:39], s84 offen sc1\n" \
"v_mfma_f32_16x16x32_f16 v[28:31], v[152:155], v[164:167], v[28:31]\n" \
"v_pk_add_f16 v162, v162, v166\n" \
"v_pk_add_f16 v163, v163, v167\n" \
"v_cvt_pk_f16_f32 v184, v184, v185\n" \
"ds_read_b64_tr_b16 v[132:133], v242 offset:32768\n" \
"v_mfma_f32_16x16x32_f16 v[40:43], v[156:159], v[164:167], v[40:43]\n" \
"v_cvt_pk_f16_f32 v185, v186, v187\n" \
"v_cvt_pk_f16_f32 v186, v188, v189\n" \
"ds_read_b128 v[180:183], v240 offset:47616\n" \
"v_mfma_f32_16x16x32_f16 v[32:35], v[120:123], v[160:163], v[32:35]\n" \
"v_cvt_pk_f16_f32 v187, v190, v191\n" \
"v_add_u32 v184, s91, v184\n" \
"buffer_load_dwordx4 v232, s[36:39], s80 offen offset:1024 sc1 lds\n" \
"v_mfma_f32_16x16x32_f16 v[44:47], v[124:127], v[160:163], v[44:47]\n" \
"v_add_u32 v185, s91, v185\n" \
"v_add_u32 v186, s91, v186\n" \
"ds_read_b64_tr_b16 v[134:135], v242 offset:40960\n" \
"v_mfma_f32_16x16x32_f16 v[48:51], v[136:139], v[168:171], v[48:51]\n" \
"v_add_u32 v187, s91, v187\n" \
"v_and_b32 v184, s92, v184\n" \
"ds_read_b128 v[160:163], v240 offset:49152\n" \
"v_mfma_f32_16x16x32_f16 v[60:63], v[140:143], v[168:171], v[60:63]\n" \
"s_waitcnt lgkmcnt(4)\n" \
"v_pk_add_f16 v168, v168, v172\n" \
"v_pk_add_f16 v169, v169, v173\n" \
"v_and_b32 v185, s92, v185\n" \
"v_mfma_f32_16x16x32_f16 v[52:55], v[152:155], v[172:175], v[52:55]\n" \
"v_pk_add_f16 v170, v170, v174\n" \
"v_pk_add_f16 v171, v171, v175\n" \
"v_and_b32 v186, s92, v186\n" \
"v_and_b32 v187, s92, v187\n" \
"v_mfma_f32_16x16x32_f16 v[64:67], v[156:159], v[172:175], v[64:67]\n" \
"ds_read_b128 v[164:167], v240 offset:49664\n" \
"buffer_store_dwordx4 v[184:187], v234, s[36:39], s82 offen offset:1024 sc1\n" \
"ds_read_b64_tr_b16 v[144:145], v241 offset:33280\n" \
"v_mfma_f32_16x16x32_f16 v[56:59], v[120:123], v[168:171], v[56:59]\n" \
"buffer_load_dwordx4 v232, s[36:39], s80 offen offset:2048 sc1 lds\n" \
"v_mfma_f32_16x16x32_f16 v[68:71], v[124:127], v[168:171], v[68:71]\n" \
"ds_read_b64_tr_b16 v[146:147], v241 offset:41472\n" \
"ds_read_b64_tr_b16 v[148:149], v242 offset:33280\n" \
"v_mfma_f32_16x16x32_f16 v[72:75], v[136:139], v[176:179], v[72:75]\n" \
"ds_read_b128 v[168:171], v240 offset:40960\n" \
"ds_read_b64_tr_b16 v[150:151], v242 offset:41472\n" \
"v_mfma_f32_16x16x32_f16 v[84:87], v[140:143], v[176:179], v[84:87]\n" \
"s_waitcnt lgkmcnt(6)\n" \
"v_pk_add_f16 v176, v176, v180\n" \
"v_pk_add_f16 v177, v177, v181\n" \
"v_mfma_f32_16x16x32_f16 v[76:79], v[152:155], v[180:183], v[76:79]\n" \
"v_pk_add_f16 v178, v178, v182\n" \
"v_pk_add_f16 v179, v179, v183\n" \
"v_mfma_f32_16x16x32_f16 v[88:91], v[156:159], v[180:183], v[88:91]\n" \
"ds_read_b128 v[172:175], v240 offset:41472\n" \
"v_mfma_f32_16x16x32_f16 v[80:83], v[120:123], v[176:179], v[80:83]\n" \
"buffer_load_dwordx4 v232, s[36:39], s80 offen offset:3072 sc1 lds\n" \
"v_mfma_f32_16x16x32_f16 v[92:95], v[124:127], v[176:179], v[92:95]\n" \
"v_mfma_f32_16x16x32_f16 v[96:99], v[136:139], v[160:163], v[96:99]\n" \
"s_waitcnt lgkmcnt(5)\n" \
"v_pk_add_f16 v136, v128, v144\n" \
"v_pk_add_f16 v137, v129, v145\n" \
"ds_read_b128 v[176:179], v240 offset:43008\n" \
"v_mfma_f32_16x16x32_f16 v[108:111], v[140:143], v[160:163], v[108:111]\n" \
"v_pk_add_f16 v160, v160, v164\n" \
"v_pk_add_f16 v161, v161, v165\n" \
"ds_read_b128 v[180:183], v240 offset:43520\n" \
"v_mfma_f32_16x16x32_f16 v[100:103], v[152:155], v[164:167], v[100:103]\n" \
"v_pk_add_f16 v162, v162, v166\n" \
"v_pk_add_f16 v163, v163, v167\n" \
"v_mfma_f32_16x16x32_f16 v[112:115], v[156:159], v[164:167], v[112:115]\n" \
"s_waitcnt lgkmcnt(3)\n" \
"v_pk_add_f16 v138, v130, v146\n" \
"v_pk_add_f16 v139, v131, v147\n" \
"v_mfma_f32_16x16x32_f16 v[104:107], v[120:123], v[160:163], v[104:107]\n" \
"v_pk_add_f16 v140, v132, v148\n" \
"v_pk_add_f16 v141, v133, v149\n" \
"v_mfma_f32_16x16x32_f16 v[116:119], v[124:127], v[160:163], v[116:119]\n" \
"v_pk_add_f16 v142, v134, v150\n" \
"v_pk_add_f16 v143, v135, v151\n" \
"s_waitcnt vmcnt(7) lgkmcnt(0)\n" \
"s_barrier\n" \
"v_mfma_f32_16x16x32_f16 v[0:3], v[128:131], v[168:171], v[0:3]\n" \
"ds_read_b128 v[160:163], v240 offset:4096\n" \
"s_mov_b32 s81, 0x800e000\n" \
"ds_read_b64_tr_b16 v[184:185], v236 offset:28672\n" \
"ds_read_b64_tr_b16 v[186:187], v236 offset:28672\n" \
"s_add_i32 m0, s45, 0x0\n" \
"s_mov_b32 s83, 0x0\n" \
"v_mfma_f32_16x16x32_f16 v[12:15], v[132:135], v[168:171], v[12:15]\n" \
"v_pk_add_f16 v168, v168, v172\n" \
"v_pk_add_f16 v169, v169, v173\n" \
"buffer_load_dword v253, v252, s[36:39], s81 offen sc1\n" \
"buffer_load_dwordx4 v233, s[36:39], s83 offen sc1 lds\n" \
"v_mfma_f32_16x16x32_f16 v[4:7], v[144:147], v[172:175], v[4:7]\n" \
"v_pk_add_f16 v170, v170, v174\n" \
"v_pk_add_f16 v171, v171, v175\n" \
"ds_read_b64_tr_b16 v[188:189], v237 offset:28672\n" \
"ds_read_b64_tr_b16 v[190:191], v237 offset:28672\n" \
"v_mfma_f32_16x16x32_f16 v[16:19], v[148:151], v[172:175], v[16:19]\n" \
"ds_read_b128 v[164:167], v240 offset:4608\n" \
"s_add_i32 m0, s44, 0x8000\n" \
"s_add_i32 s80, s46, 0x4000000\n" \
"v_mfma_f32_16x16x32_f16 v[8:11], v[136:139], v[168:171], v[8:11]\n" \
"buffer_load_dwordx4 v232, s[36:39], s80 offen sc1 lds\n" \
"s_waitcnt lgkmcnt(3)\n" \
"v_mfma_f32_16x16x32_bf16 v[184:187], v[184:187], v[192:195], 0\n" \
"v_mfma_f32_16x16x32_f16 v[20:23], v[140:143], v[168:171], v[20:23]\n" \
"ds_read_b64_tr_b16 v[120:121], v241 offset:0\n" \
"ds_read_b64_tr_b16 v[122:123], v241 offset:8192\n" \
"s_waitcnt lgkmcnt(3)\n" \
"v_mfma_f32_16x16x32_bf16 v[188:191], v[188:191], v[192:195], 0\n" \
"v_mfma_f32_16x16x32_f16 v[24:27], v[128:131], v[176:179], v[24:27]\n" \
"ds_read_b128 v[168:171], v240 offset:6144\n" \
"v_mfma_f32_16x16x32_f16 v[36:39], v[132:135], v[176:179], v[36:39]\n" \
"v_pk_add_f16 v176, v176, v180\n" \
"v_pk_add_f16 v177, v177, v181\n" \
"ds_read_b64_tr_b16 v[124:125], v242 offset:0\n" \
"v_mfma_f32_16x16x32_f16 v[28:31], v[144:147], v[180:183], v[28:31]\n" \
"v_pk_add_f16 v178, v178, v182\n" \
"v_pk_add_f16 v179, v179, v183\n" \
"v_cvt_pk_f16_f32 v184, v184, v185\n" \
"v_mfma_f32_16x16x32_f16 v[40:43], v[148:151], v[180:183], v[40:43]\n" \
"v_cvt_pk_f16_f32 v185, v186, v187\n" \
"v_cvt_pk_f16_f32 v186, v188, v189\n" \
"ds_read_b128 v[172:175], v240 offset:6656\n" \
"v_mfma_f32_16x16x32_f16 v[32:35], v[136:139], v[176:179], v[32:35]\n" \
"v_cvt_pk_f16_f32 v187, v190, v191\n" \
"v_add_u32 v184, s91, v184\n" \
"buffer_load_dwordx4 v232, s[36:39], s80 offen offset:1024 sc1 lds\n" \
"v_mfma_f32_16x16x32_f16 v[44:47], v[140:143], v[176:179], v[44:47]\n" \
"v_add_u32 v185, s91, v185\n" \
"v_add_u32 v186, s91, v186\n" \
"ds_read_b64_tr_b16 v[126:127], v242 offset:8192\n" \
"v_mfma_f32_16x16x32_f16 v[48:51], v[128:131], v[160:163], v[48:51]\n" \
"v_add_u32 v187, s91, v187\n" \
"v_and_b32 v184, s92, v184\n" \
"ds_read_b128 v[176:179], v240 offset:8192\n" \
"v_mfma_f32_16x16x32_f16 v[60:63], v[132:135], v[160:163], v[60:63]\n" \
"s_waitcnt lgkmcnt(4)\n" \
"v_pk_add_f16 v160, v160, v164\n" \
"v_pk_add_f16 v161, v161, v165\n" \
"v_and_b32 v185, s92, v185\n" \
"s_add_i32 s82, s48, 0x6000000\n" \
"v_mfma_f32_16x16x32_f16 v[52:55], v[144:147], v[164:167], v[52:55]\n" \
"v_pk_add_f16 v162, v162, v166\n" \
"v_pk_add_f16 v163, v163, v167\n" \
"v_and_b32 v186, s92, v186\n" \
"v_and_b32 v187, s92, v187\n" \
"v_mfma_f32_16x16x32_f16 v[64:67], v[148:151], v[164:167], v[64:67]\n" \
"ds_read_b128 v[180:183], v240 offset:8704\n" \
"buffer_store_dwordx4 v[184:187], v234, s[36:39], s82 offen sc1\n" \
"ds_read_b64_tr_b16 v[152:153], v241 offset:512\n" \
"v_mfma_f32_16x16x32_f16 v[56:59], v[136:139], v[160:163], v[56:59]\n" \
"buffer_load_dwordx4 v232, s[36:39], s80 offen offset:2048 sc1 lds\n" \
"v_mfma_f32_16x16x32_f16 v[68:71], v[140:143], v[160:163], v[68:71]\n" \
"ds_read_b64_tr_b16 v[154:155], v241 offset:8704\n" \
"ds_read_b64_tr_b16 v[156:157], v242 offset:512\n" \
"v_mfma_f32_16x16x32_f16 v[72:75], v[128:131], v[168:171], v[72:75]\n" \
"ds_read_b128 v[160:163], v240 offset:0\n" \
"ds_read_b64_tr_b16 v[158:159], v242 offset:8704\n" \
"v_mfma_f32_16x16x32_f16 v[84:87], v[132:135], v[168:171], v[84:87]\n" \
"s_waitcnt lgkmcnt(6)\n" \
"v_pk_add_f16 v168, v168, v172\n" \
"v_pk_add_f16 v169, v169, v173\n" \
"v_mfma_f32_16x16x32_f16 v[76:79], v[144:147], v[172:175], v[76:79]\n" \
"v_pk_add_f16 v170, v170, v174\n" \
"v_pk_add_f16 v171, v171, v175\n" \
"v_mfma_f32_16x16x32_f16 v[88:91], v[148:151], v[172:175], v[88:91]\n" \
"ds_read_b128 v[164:167], v240 offset:512\n" \
"v_mfma_f32_16x16x32_f16 v[80:83], v[136:139], v[168:171], v[80:83]\n" \
"buffer_load_dwordx4 v232, s[36:39], s80 offen offset:3072 sc1 lds\n" \
"v_mfma_f32_16x16x32_f16 v[92:95], v[140:143], v[168:171], v[92:95]\n" \
"v_mfma_f32_16x16x32_f16 v[96:99], v[128:131], v[176:179], v[96:99]\n" \
"s_waitcnt lgkmcnt(5)\n" \
"v_pk_add_f16 v128, v120, v152\n" \
"v_pk_add_f16 v129, v121, v153\n" \
"ds_read_b128 v[168:171], v240 offset:2048\n" \
"s_add_i32 m0, s45, 0x1000\n" \
"s_mov_b32 s83, 0x8000\n" \
"v_mfma_f32_16x16x32_f16 v[108:111], v[132:135], v[176:179], v[108:111]\n" \
"v_pk_add_f16 v176, v176, v180\n" \
"v_pk_add_f16 v177, v177, v181\n" \
"ds_read_b128 v[172:175], v240 offset:2560\n" \
"buffer_load_dwordx4 v233, s[36:39], s83 offen sc1 lds\n" \
"v_mfma_f32_16x16x32_f16 v[100:103], v[144:147], v[180:183], v[100:103]\n" \
"v_pk_add_f16 v178, v178, v182\n" \
"v_pk_add_f16 v179, v179, v183\n" \
"v_mfma_f32_16x16x32_f16 v[112:115], v[148:151], v[180:183], v[112:115]\n" \
"s_waitcnt lgkmcnt(3)\n" \
"v_pk_add_f16 v130, v122, v154\n" \
"v_pk_add_f16 v131, v123, v155\n" \
"v_mfma_f32_16x16x32_f16 v[104:107], v[136:139], v[176:179], v[104:107]\n" \
"v_pk_add_f16 v132, v124, v156\n" \
"v_pk_add_f16 v133, v125, v157\n" \
"v_mfma_f32_16x16x32_f16 v[116:119], v[140:143], v[176:179], v[116:119]\n" \
"v_pk_add_f16 v134, v126, v158\n" \
"v_pk_add_f16 v135, v127, v159\n" \
"s_waitcnt vmcnt(7) lgkmcnt(0)\n" \
"s_barrier\n" \
"v_mfma_f32_16x16x32_f16 v[0:3], v[120:123], v[160:163], v[0:3]\n" \
"ds_read_b128 v[176:179], v240 offset:24576\n" \
"ds_read_b64_tr_b16 v[184:185], v238 offset:28672\n" \
"ds_read_b64_tr_b16 v[186:187], v238 offset:28672\n" \
"s_add_i32 m0, s45, 0x2000\n" \
"s_mov_b32 s83, 0x10000\n" \
"v_mfma_f32_16x16x32_f16 v[12:15], v[124:127], v[160:163], v[12:15]\n" \
"v_pk_add_f16 v160, v160, v164\n" \
"v_pk_add_f16 v161, v161, v165\n" \
"ds_read_b64_tr_b16 v[188:189], v239 offset:28672\n" \
"ds_read_b64_tr_b16 v[190:191], v239 offset:28672\n" \
"buffer_load_dwordx4 v233, s[36:39], s83 offen sc1 lds\n" \
"v_mfma_f32_16x16x32_f16 v[4:7], v[152:155], v[164:167], v[4:7]\n" \
"v_pk_add_f16 v162, v162, v166\n" \
"v_pk_add_f16 v163, v163, v167\n" \
"v_cmp_eq_u32 vcc, 0, v253\n" \
"s_cbranch_vccnz .Ls07_%=\n" \
".Lr07_%=:\n" \
"v_mfma_f32_16x16x32_f16 v[16:19], v[156:159], v[164:167], v[16:19]\n" \
"ds_read_b128 v[180:183], v240 offset:25088\n" \
"s_add_i32 m0, s44, 0x0\n" \
"s_add_i32 s80, s46, 0x4400000\n" \
"v_mfma_f32_16x16x32_f16 v[8:11], v[128:131], v[160:163], v[8:11]\n" \
"buffer_load_dwordx4 v232, s[36:39], s80 offen sc1 lds\n" \
"s_waitcnt lgkmcnt(3)\n" \
"v_mfma_f32_16x16x32_bf16 v[184:187], v[184:187], v[192:195], 0\n" \
"v_mfma_f32_16x16x32_f16 v[20:23], v[132:135], v[160:163], v[20:23]\n" \
"ds_read_b64_tr_b16 v[136:137], v241 offset:16384\n" \
"ds_read_b64_tr_b16 v[138:139], v241 offset:24576\n" \
"s_waitcnt lgkmcnt(3)\n" \
"v_mfma_f32_16x16x32_bf16 v[188:191], v[188:191], v[192:195], 0\n" \
"v_mfma_f32_16x16x32_f16 v[24:27], v[120:123], v[168:171], v[24:27]\n" \
"ds_read_b128 v[160:163], v240 offset:26624\n" \
"s_add_i32 s84, s55, 0x8012000\n" \
"v_mfma_f32_16x16x32_f16 v[36:39], v[124:127], v[168:171], v[36:39]\n" \
"v_pk_add_f16 v168, v168, v172\n" \
"v_pk_add_f16 v169, v169, v173\n" \
"buffer_store_dword v240, v243, s[36:39], s84 offen sc1\n" \
"v_mfma_f32_16x16x32_f16 v[28:31], v[152:155], v[172:175], v[28:31]\n" \
"v_pk_add_f16 v170, v170, v174\n" \
"v_pk_add_f16 v171, v171, v175\n" \
"v_cvt_pk_f16_f32 v184, v184, v185\n" \
"ds_read_b64_tr_b16 v[140:141], v242 offset:16384\n" \
"v_mfma_f32_16x16x32_f16 v[40:43], v[156:159], v[172:175], v[40:43]\n" \
"v_cvt_pk_f16_f32 v185, v186, v187\n" \
"v_cvt_pk_f16_f32 v186, v188, v189\n" \
"ds_read_b128 v[164:167], v240 offset:27136\n" \
"v_mfma_f32_16x16x32_f16 v[32:35], v[128:131], v[168:171], v[32:35]\n" \
"v_cvt_pk_f16_f32 v187, v190, v191\n" \
"v_add_u32 v184, s91, v184\n" \
"buffer_load_dwordx4 v232, s[36:39], s80 offen offset:1024 sc1 lds\n" \
"v_mfma_f32_16x16x32_f16 v[44:47], v[132:135], v[168:171], v[44:47]\n" \
"v_add_u32 v185, s91, v185\n" \
"v_add_u32 v186, s91, v186\n" \
"ds_read_b64_tr_b16 v[142:143], v242 offset:24576\n" \
"v_mfma_f32_16x16x32_f16 v[48:51], v[120:123], v[176:179], v[48:51]\n" \
"v_add_u32 v187, s91, v187\n" \
"v_and_b32 v184, s92, v184\n" \
"ds_read_b128 v[168:171], v240 offset:28672\n" \
"v_mfma_f32_16x16x32_f16 v[60:63], v[124:127], v[176:179], v[60:63]\n" \
"s_waitcnt lgkmcnt(4)\n" \
"v_pk_add_f16 v176, v176, v180\n" \
"v_pk_add_f16 v177, v177, v181\n" \
"v_and_b32 v185, s92, v185\n" \
"v_mfma_f32_16x16x32_f16 v[52:55], v[152:155], v[180:183], v[52:55]\n" \
"v_pk_add_f16 v178, v178, v182\n" \
"v_pk_add_f16 v179, v179, v183\n" \
"v_and_b32 v186, s92, v186\n" \
"v_and_b32 v187, s92, v187\n" \
"v_mfma_f32_16x16x32_f16 v[64:67], v[156:159], v[180:183], v[64:67]\n" \
"ds_read_b128 v[172:175], v240 offset:29184\n" \
"buffer_store_dwordx4 v[184:187], v234, s[36:39], s82 offen offset:1024 sc1\n" \
"ds_read_b64_tr_b16 v[144:145], v241 offset:16896\n" \
"v_mfma_f32_16x16x32_f16 v[56:59], v[128:131], v[176:179], v[56:59]\n" \
"buffer_load_dwordx4 v232, s[36:39], s80 offen offset:2048 sc1 lds\n" \
"v_mfma_f32_16x16x32_f16 v[68:71], v[132:135], v[176:179], v[68:71]\n" \
"ds_read_b64_tr_b16 v[146:147], v241 offset:25088\n" \
"ds_read_b64_tr_b16 v[148:149], v242 offset:16896\n" \
"v_mfma_f32_16x16x32_f16 v[72:75], v[120:123], v[160:163], v[72:75]\n" \
"ds_read_b128 v[176:179], v240 offset:20480\n" \
"ds_read_b64_tr_b16 v[150:151], v242 offset:25088\n" \
"v_mfma_f32_16x16x32_f16 v[84:87], v[124:127], v[160:163], v[84:87]\n" \
"s_waitcnt lgkmcnt(6)\n" \
"v_pk_add_f16 v160, v160, v164\n" \
"v_pk_add_f16 v161, v161, v165\n" \
"v_mfma_f32_16x16x32_f16 v[76:79], v[152:155], v[164:167], v[76:79]\n" \
"v_pk_add_f16 v162, v162, v166\n" \
"v_pk_add_f16 v163, v163, v167\n" \
"v_mfma_f32_16x16x32_f16 v[88:91], v[156:159], v[164:167], v[88:91]\n" \
"ds_read_b128 v[180:183], v240 offset:20992\n" \
"v_mfma_f32_16x16x32_f16 v[80:83], v[128:131], v[160:163], v[80:83]\n" \
"buffer_load_dwordx4 v232, s[36:39], s80 offen offset:3072 sc1 lds\n" \
"v_mfma_f32_16x16x32_f16 v[92:95], v[132:135], v[160:163], v[92:95]\n" \
"v_mfma_f32_16x16x32_f16 v[96:99], v[120:123], v[168:171], v[96:99]\n" \
"s_waitcnt lgkmcnt(5)\n" \
"v_pk_add_f16 v120, v136, v144\n" \
"v_pk_add_f16 v121, v137, v145\n" \
"ds_read_b128 v[160:163], v240 offset:22528\n" \
"s_add_i32 m0, s45, 0x3000\n" \
"s_mov_b32 s83, 0x18000\n" \
"v_mfma_f32_16x16x32_f16 v[108:111], v[124:127], v[168:171], v[108:111]\n" \
"v_pk_add_f16 v168, v168, v172\n" \
"v_pk_add_f16 v169, v169, v173\n" \
"ds_read_b128 v[164:167], v240 offset:23040\n" \
"buffer_load_dwordx4 v233, s[36:39], s83 offen sc1 lds\n" \
"v_mfma_f32_16x16x32_f16 v[100:103], v[152:155], v[172:175], v[100:103]\n" \
"v_pk_add_f16 v170, v170, v174\n" \
"v_pk_add_f16 v171, v171, v175\n" \
"v_mfma_f32_16x16x32_f16 v[112:115], v[156:159], v[172:175], v[112:115]\n" \
"s_waitcnt lgkmcnt(3)\n" \
"v_pk_add_f16 v122, v138, v146\n" \
"v_pk_add_f16 v123, v139, v147\n" \
"v_mfma_f32_16x16x32_f16 v[104:107], v[128:131], v[168:171], v[104:107]\n" \
"v_pk_add_f16 v124, v140, v148\n" \
"v_pk_add_f16 v125, v141, v149\n" \
"v_mfma_f32_16x16x32_f16 v[116:119], v[132:135], v[168:171], v[116:119]\n" \
"v_pk_add_f16 v126, v142, v150\n" \
"v_pk_add_f16 v127, v143, v151\n" \
"s_waitcnt vmcnt(8) lgkmcnt(0)\n" \
"s_barrier\n" \
"v_mfma_f32_16x16x32_f16 v[0:3], v[136:139], v[176:179], v[0:3]\n" \
"ds_read_b128 v[168:171], v240 offset:45056\n" \
"s_mov_b32 s81, 0x8010000\n" \
"ds_read_b64_tr_b16 v[184:185], v236 offset:32768\n" \
"ds_read_b64_tr_b16 v[186:187], v236 offset:32768\n" \
"s_add_i32 m0, s45, 0x4000\n" \
"s_mov_b32 s83, 0x20000\n" \
"v_mfma_f32_16x16x32_f16 v[12:15], v[140:143], v[176:179], v[12:15]\n" \
"v_pk_add_f16 v176, v176, v180\n" \
"v_pk_add_f16 v177, v177, v181\n" \
"buffer_load_dword v253, v252, s[36:39], s81 offen sc1\n" \
"buffer_load_dwordx4 v233, s[36:39], s83 offen sc1 lds\n" \
"v_mfma_f32_16x16x32_f16 v[4:7], v[144:147], v[180:183], v[4:7]\n" \
"v_pk_add_f16 v178, v178, v182\n" \
"v_pk_add_f16 v179, v179, v183\n" \
"ds_read_b64_tr_b16 v[188:189], v237 offset:32768\n" \
"ds_read_b64_tr_b16 v[190:191], v237 offset:32768\n" \
"v_mfma_f32_16x16x32_f16 v[16:19], v[148:151], v[180:183], v[16:19]\n" \
"ds_read_b128 v[172:175], v240 offset:45568\n" \
"s_add_i32 m0, s44, 0x4000\n" \
"s_add_i32 s80, s46, 0x4800000\n" \
"v_mfma_f32_16x16x32_f16 v[8:11], v[120:123], v[176:179], v[8:11]\n" \
"buffer_load_dwordx4 v232, s[36:39], s80 offen sc1 lds\n" \
"s_waitcnt lgkmcnt(3)\n" \
"v_mfma_f32_16x16x32_bf16 v[184:187], v[184:187], v[192:195], 0\n" \
"v_mfma_f32_16x16x32_f16 v[20:23], v[124:127], v[176:179], v[20:23]\n" \
"ds_read_b128 v[196:199], v249 offset:0\n" \
"ds_read_b64_tr_b16 v[200:201], v247 offset:0\n" \
"s_waitcnt lgkmcnt(3)\n" \
"v_mfma_f32_16x16x32_bf16 v[188:191], v[188:191], v[192:195], 0\n" \
"v_mfma_f32_16x16x32_f16 v[24:27], v[136:139], v[160:163], v[24:27]\n" \
"ds_read_b128 v[176:179], v240 offset:47104\n" \
"ds_read_b64_tr_b16 v[128:129], v241 offset:32768\n" \
"ds_read_b64_tr_b16 v[130:131], v241 offset:40960\n" \
"v_mfma_f32_16x16x32_f16 v[36:39], v[140:143], v[160:163], v[36:39]\n" \
"v_pk_add_f16 v160, v160, v164\n" \
"v_pk_add_f16 v161, v161, v165\n" \
"ds_read_b64_tr_b16 v[202:203], v247 offset:512\n" \
"ds_read_b64_tr_b16 v[132:133], v242 offset:32768\n" \
"v_mfma_f32_16x16x32_f16 v[28:31], v[144:147], v[164:167], v[28:31]\n" \
"v_pk_add_f16 v162, v162, v166\n" \
"v_pk_add_f16 v163, v163, v167\n" \
"v_cvt_pk_f16_f32 v184, v184, v185\n" \
"v_mfma_f32_16x16x32_f16 v[40:43], v[148:151], v[164:167], v[40:43]\n" \
"v_cvt_pk_f16_f32 v185, v186, v187\n" \
"v_cvt_pk_f16_f32 v186, v188, v189\n" \
"ds_read_b128 v[180:183], v240 offset:47616\n" \
"v_mfma_f32_16x16x32_f16 v[32:35], v[120:123], v[160:163], v[32:35]\n" \
"v_cvt_pk_f16_f32 v187, v190, v191\n" \
"v_add_u32 v184, s91, v184\n" \
"buffer_load_dwordx4 v232, s[36:39], s80 offen offset:1024 sc1 lds\n" \
"v_mfma_f32_16x16x32_f16 v[44:47], v[124:127], v[160:163], v[44:47]\n" \
"v_add_u32 v185, s91, v185\n" \
"v_add_u32 v186, s91, v186\n" \
"ds_read_b64_tr_b16 v[134:135], v242 offset:40960\n" \
"v_mfma_f32_16x16x32_f16 v[48:51], v[136:139], v[168:171], v[48:51]\n" \
"v_add_u32 v187, s91, v187\n" \
"v_and_b32 v184, s92, v184\n" \
"ds_read_b128 v[160:163], v240 offset:49152\n" \
"v_mfma_f32_16x16x32_f16 v[60:63], v[140:143], v[168:171], v[60:63]\n" \
"s_waitcnt lgkmcnt(5)\n" \
"v_pk_add_f16 v168, v168, v172\n" \
"v_pk_add_f16 v169, v169, v173\n" \
"v_and_b32 v185, s92, v185\n" \
"s_add_i32 s82, s49, 0x6800000\n" \
"s_waitcnt lgkmcnt(3)\n" \
"v_mfma_f32_16x16x32_bf16 v[204:207], v[200:203], v[196:199], 0\n" \
"v_mfma_f32_16x16x32_f16 v[52:55], v[144:147], v[172:175], v[52:55]\n" \
"v_pk_add_f16 v170, v170, v174\n" \
"v_pk_add_f16 v171, v171, v175\n" \
"v_and_b32 v186, s92, v186\n" \
"v_and_b32 v187, s92, v187\n" \
"v_mfma_f32_16x16x32_f16 v[64:67], v[148:151], v[172:175], v[64:67]\n" \
"ds_read_b128 v[164:167], v240 offset:49664\n" \
"buffer_store_dwordx4 v[184:187], v234, s[36:39], s82 offen sc1\n" \
"ds_read_b64_tr_b16 v[152:153], v241 offset:33280\n" \
"v_mfma_f32_16x16x32_f16 v[56:59], v[120:123], v[168:171], v[56:59]\n" \
"buffer_load_dwordx4 v232, s[36:39], s80 offen offset:2048 sc1 lds\n" \
"ds_read_b64_tr_b16 v[154:155], v241 offset:41472\n" \
"v_mfma_f32_16x16x32_f16 v[68:71], v[124:127], v[168:171], v[68:71]\n" \
"ds_read_b128 v[196:199], v249 offset:1024\n" \
"ds_read_b64_tr_b16 v[200:201], v247 offset:4096\n" \
"ds_read_b64_tr_b16 v[156:157], v242 offset:33280\n" \
"v_mfma_f32_16x16x32_f16 v[72:75], v[136:139], v[176:179], v[72:75]\n" \
"ds_read_b128 v[168:171], v240 offset:40960\n" \
"ds_read_b64_tr_b16 v[202:203], v247 offset:4608\n" \
"v_mfma_f32_16x16x32_f16 v[84:87], v[140:143], v[176:179], v[84:87]\n" \
"s_waitcnt lgkmcnt(8)\n" \
"v_pk_add_f16 v176, v176, v180\n" \
"v_pk_add_f16 v177, v177, v181\n" \
"v_mfma_f32_16x16x32_f16 v[76:79], v[144:147], v[180:183], v[76:79]\n" \
"v_pk_add_f16 v178, v178, v182\n" \
"v_pk_add_f16 v179, v179, v183\n" \
"v_mfma_f32_16x16x32_f16 v[88:91], v[148:151], v[180:183], v[88:91]\n" \
"ds_read_b128 v[172:175], v240 offset:41472\n" \
"ds_read_b64_tr_b16 v[158:159], v242 offset:41472\n" \
"v_mfma_f32_16x16x32_f16 v[80:83], v[120:123], v[176:179], v[80:83]\n" \
"buffer_load_dwordx4 v232, s[36:39], s80 offen offset:3072 sc1 lds\n" \
"v_mfma_f32_16x16x32_f16 v[92:95], v[124:127], v[176:179], v[92:95]\n" \
"v_mfma_f32_16x16x32_f16 v[96:99], v[136:139], v[160:163], v[96:99]\n" \
"s_waitcnt lgkmcnt(8)\n" \
"v_pk_add_f16 v136, v128, v152\n" \
"v_pk_add_f16 v137, v129, v153\n" \
"ds_read_b128 v[176:179], v240 offset:43008\n" \
"s_add_i32 m0, s45, 0x5000\n" \
"s_mov_b32 s83, 0x28000\n" \
"s_waitcnt lgkmcnt(3)\n" \
"v_mfma_f32_16x16x32_bf16 v[204:207], v[200:203], v[196:199], v[204:207]\n" \
"v_mfma_f32_16x16x32_f16 v[108:111], v[140:143], v[160:163], v[108:111]\n" \
"v_pk_add_f16 v160, v160, v164\n" \
"v_pk_add_f16 v161, v161, v165\n" \
"ds_read_b128 v[180:183], v240 offset:43520\n" \
"buffer_load_dwordx4 v233, s[36:39], s83 offen sc1 lds\n" \
"v_mfma_f32_16x16x32_f16 v[100:103], v[144:147], v[164:167], v[100:103]\n" \
"v_pk_add_f16 v162, v162, v166\n" \
"v_pk_add_f16 v163, v163, v167\n" \
"v_mfma_f32_16x16x32_f16 v[112:115], v[148:151], v[164:167], v[112:115]\n" \
"v_pk_add_f16 v138, v130, v154\n" \
"v_pk_add_f16 v139, v131, v155\n" \
"v_mfma_f32_16x16x32_f16 v[104:107], v[120:123], v[160:163], v[104:107]\n" \
"v_pk_add_f16 v140, v132, v156\n" \
"v_pk_add_f16 v141, v133, v157\n" \
"v_mfma_f32_16x16x32_f16 v[116:119], v[124:127], v[160:163], v[116:119]\n" \
"s_waitcnt lgkmcnt(2)\n" \
"v_pk_add_f16 v142, v134, v158\n" \
"v_pk_add_f16 v143, v135, v159\n" \
"s_waitcnt vmcnt(7) lgkmcnt(0)\n" \
"s_barrier\n" \
"v_mfma_f32_16x16x32_f16 v[0:3], v[128:131], v[168:171], v[0:3]\n" \
"ds_read_b128 v[160:163], v240 offset:4096\n" \
"ds_read_b64_tr_b16 v[184:185], v238 offset:32768\n" \
"ds_read_b64_tr_b16 v[186:187], v238 offset:32768\n" \
"s_add_i32 m0, s45, 0x0\n" \
"s_mov_b32 s83, 0x30000\n" \
"v_mfma_f32_16x16x32_f16 v[12:15], v[132:135], v[168:171], v[12:15]\n" \
"v_pk_add_f16 v168, v168, v172\n" \
"v_pk_add_f16 v169, v169, v173\n" \
"ds_read_b64_tr_b16 v[188:189], v239 offset:32768\n" \
"ds_read_b64_tr_b16 v[190:191], v239 offset:32768\n" \
"buffer_load_dwordx4 v233, s[36:39], s83 offen sc1 lds\n" \
"v_mfma_f32_16x16x32_f16 v[4:7], v[152:155], v[172:175], v[4:7]\n" \
"v_pk_add_f16 v170, v170, v174\n" \
"v_pk_add_f16 v171, v171, v175\n" \
"v_cmp_eq_u32 vcc, 0, v253\n" \
"s_cbranch_vccnz .Ls08_%=\n" \
".Lr08_%=:\n" \
"v_mfma_f32_16x16x32_f16 v[16:19], v[156:159], v[172:175], v[16:19]\n" \
"ds_read_b128 v[164:167], v240 offset:4608\n" \
"s_add_i32 m0, s44, 0x8000\n" \
"s_add_i32 s80, s46, 0x4c00000\n" \
"v_mfma_f32_16x16x32_f16 v[8:11], v[136:139], v[168:171], v[8:11]\n" \
"buffer_load_dwordx4 v232, s[36:39], s80 offen sc1 lds\n" \
"ds_read_b64_tr_b16 v[120:121], v241 offset:0\n" \
"s_waitcnt lgkmcnt(4)\n" \
"v_mfma_f32_16x16x32_bf16 v[184:187], v[184:187], v[192:195], 0\n" \
"v_mfma_f32_16x16x32_f16 v[20:23], v[140:143], v[168:171], v[20:23]\n" \
"ds_read_b128 v[196:199], v249 offset:2048\n" \
"ds_read_b64_tr_b16 v[200:201], v247 offset:8192\n" \
"s_waitcnt lgkmcnt(4)\n" \
"v_mfma_f32_16x16x32_bf16 v[188:191], v[188:191], v[192:195], 0\n" \
"v_mfma_f32_16x16x32_f16 v[24:27], v[128:131], v[176:179], v[24:27]\n" \
"ds_read_b128 v[168:171], v240 offset:6144\n" \
"s_add_i32 s84, s52, 0x8014000\n" \
"ds_read_b64_tr_b16 v[122:123], v241 offset:8192\n" \
"v_mfma_f32_16x16x32_f16 v[36:39], v[132:135], v[176:179], v[36:39]\n" \
"v_pk_add_f16 v176, v176, v180\n" \
"v_pk_add_f16 v177, v177, v181\n" \
"buffer_store_dword v240, v243, s[36:39], s84 offen sc1\n" \
"v_mfma_f32_16x16x32_f16 v[28:31], v[152:155], v[180:183], v[28:31]\n" \
"v_pk_add_f16 v178, v178, v182\n" \
"v_pk_add_f16 v179, v179, v183\n" \
"v_cvt_pk_f16_f32 v184, v184, v185\n" \
"ds_read_b64_tr_b16 v[202:203], v247 offset:8704\n" \
"v_mfma_f32_16x16x32_f16 v[40:43], v[156:159], v[180:183], v[40:43]\n" \
"v_cvt_pk_f16_f32 v185, v186, v187\n" \
"v_cvt_pk_f16_f32 v186, v188, v189\n" \
"ds_read_b128 v[172:175], v240 offset:6656\n" \
"ds_read_b64_tr_b16 v[124:125], v242 offset:0\n" \
"v_mfma_f32_16x16x32_f16 v[32:35], v[136:139], v[176:179], v[32:35]\n" \
"v_cvt_pk_f16_f32 v187, v190, v191\n" \
"v_add_u32 v184, s91, v184\n" \
"buffer_load_dwordx4 v232, s[36:39], s80 offen offset:1024 sc1 lds\n" \
"v_mfma_f32_16x16x32_f16 v[44:47], v[140:143], v[176:179], v[44:47]\n" \
"v_add_u32 v185, s91, v185\n" \
"v_add_u32 v186, s91, v186\n" \
"ds_read_b64_tr_b16 v[126:127], v242 offset:8192\n" \
"v_mfma_f32_16x16x32_f16 v[48:51], v[128:131], v[160:163], v[48:51]\n" \
"v_add_u32 v187, s91, v187\n" \
"v_and_b32 v184, s92, v184\n" \
"ds_read_b128 v[176:179], v240 offset:8192\n" \
"v_mfma_f32_16x16x32_f16 v[60:63], v[132:135], v[160:163], v[60:63]\n" \
"s_waitcnt lgkmcnt(5)\n" \
"v_pk_add_f16 v160, v160, v164\n" \
"v_pk_add_f16 v161, v161, v165\n" \
"v_and_b32 v185, s92, v185\n" \
"s_waitcnt lgkmcnt(4)\n" \
"v_mfma_f32_16x16x32_bf16 v[204:207], v[200:203], v[196:199], v[204:207]\n" \
"v_mfma_f32_16x16x32_f16 v[52:55], v[152:155], v[164:167], v[52:55]\n" \
"v_pk_add_f16 v162, v162, v166\n" \
"v_pk_add_f16 v163, v163, v167\n" \
"v_and_b32 v186, s92, v186\n" \
"v_and_b32 v187, s92, v187\n" \
"v_mfma_f32_16x16x32_f16 v[64:67], v[156:159], v[164:167], v[64:67]\n" \
"ds_read_b128 v[180:183], v240 offset:8704\n" \
"buffer_store_dwordx4 v[184:187], v234, s[36:39], s82 offen offset:1024 sc1\n" \
"ds_read_b64_tr_b16 v[144:145], v241 offset:512\n" \
"v_mfma_f32_16x16x32_f16 v[56:59], v[136:139], v[160:163], v[56:59]\n" \
"buffer_load_dwordx4 v232, s[36:39], s80 offen offset:2048 sc1 lds\n" \
"ds_read_b64_tr_b16 v[146:147], v241 offset:8704\n" \
"v_mfma_f32_16x16x32_f16 v[68:71], v[140:143], v[160:163], v[68:71]\n" \
"ds_read_b128 v[196:199], v249 offset:3072\n" \
"ds_read_b64_tr_b16 v[200:201], v247 offset:12288\n" \
"ds_read_b64_tr_b16 v[148:149], v242 offset:512\n" \
"v_mfma_f32_16x16x32_f16 v[72:75], v[128:131], v[168:171], v[72:75]\n" \
"ds_read_b128 v[160:163], v240 offset:0\n" \
"ds_read_b64_tr_b16 v[202:203], v247 offset:12800\n" \
"v_mfma_f32_16x16x32_f16 v[84:87], v[132:135], v[168:171], v[84:87]\n" \
"s_waitcnt lgkmcnt(8)\n" \
"v_pk_add_f16 v168, v168, v172\n" \
"v_pk_add_f16 v169, v169, v173\n" \
"v_mfma_f32_16x16x32_f16 v[76:79], v[152:155], v[172:175], v[76:79]\n" \
"v_pk_add_f16 v170, v170, v174\n" \
"v_pk_add_f16 v171, v171, v175\n" \
"v_mfma_f32_16x16x32_f16 v[88:91], v[156:159], v[172:175], v[88:91]\n" \
"ds_read_b128 v[164:167], v240 offset:512\n" \
"ds_read_b64_tr_b16 v[150:151], v242 offset:8704\n" \
"v_mfma_f32_16x16x32_f16 v[80:83], v[136:139], v[168:171], v[80:83]\n" \
"buffer_load_dwordx4 v232, s[36:39], s80 offen offset:3072 sc1 lds\n" \
"v_mfma_f32_16x16x32_f16 v[92:95], v[140:143], v[168:171], v[92:95]\n" \
"v_mfma_f32_16x16x32_f16 v[96:99], v[128:131], v[176:179], v[96:99]\n" \
"s_waitcnt lgkmcnt(8)\n" \
"v_pk_add_f16 v128, v120, v144\n" \
"v_pk_add_f16 v129, v121, v145\n" \
"ds_read_b128 v[168:171], v240 offset:2048\n" \
"s_add_i32 m0, s45, 0x1000\n" \
"s_mov_b32 s83, 0x38000\n" \
"s_waitcnt lgkmcnt(3)\n" \
"v_mfma_f32_16x16x32_bf16 v[204:207], v[200:203], v[196:199], v[204:207]\n" \
"v_mfma_f32_16x16x32_f16 v[108:111], v[132:135], v[176:179], v[108:111]\n" \
"v_pk_add_f16 v176, v176, v180\n" \
"v_pk_add_f16 v177, v177, v181\n" \
"ds_read_b128 v[172:175], v240 offset:2560\n" \
"buffer_load_dwordx4 v233, s[36:39], s83 offen sc1 lds\n" \
"v_mfma_f32_16x16x32_f16 v[100:103], v[152:155], v[180:183], v[100:103]\n" \
"v_pk_add_f16 v178, v178, v182\n" \
"v_pk_add_f16 v179, v179, v183\n" \
"v_mfma_f32_16x16x32_f16 v[112:115], v[156:159], v[180:183], v[112:115]\n" \
"v_pk_add_f16 v130, v122, v146\n" \
"v_pk_add_f16 v131, v123, v147\n" \
"v_mfma_f32_16x16x32_f16 v[104:107], v[136:139], v[176:179], v[104:107]\n" \
"v_pk_add_f16 v132, v124, v148\n" \
"v_pk_add_f16 v133, v125, v149\n" \
"v_mfma_f32_16x16x32_f16 v[116:119], v[140:143], v[176:179], v[116:119]\n" \
"s_waitcnt lgkmcnt(2)\n" \
"v_pk_add_f16 v134, v126, v150\n" \
"v_pk_add_f16 v135, v127, v151\n" \
"s_waitcnt vmcnt(8) lgkmcnt(0)\n" \
"s_barrier\n" \
"v_mfma_f32_16x16x32_f16 v[0:3], v[120:123], v[160:163], v[0:3]\n" \
"ds_read_b128 v[176:179], v240 offset:24576\n" \
"s_mov_b32 s81, 0x8012000\n" \
"ds_read_b64_tr_b16 v[184:185], v236 offset:36864\n" \
"ds_read_b64_tr_b16 v[186:187], v236 offset:36864\n" \
"s_add_i32 m0, s45, 0x2000\n" \
"s_mov_b32 s83, 0x40000\n" \
"v_mfma_f32_16x16x32_f16 v[12:15], v[124:127], v[160:163], v[12:15]\n" \
"v_pk_add_f16 v160, v160, v164\n" \
"v_pk_add_f16 v161, v161, v165\n" \
"buffer_load_dword v253, v252, s[36:39], s81 offen sc1\n" \
"buffer_load_dwordx4 v233, s[36:39], s83 offen sc1 lds\n" \
"v_mfma_f32_16x16x32_f16 v[4:7], v[144:147], v[164:167], v[4:7]\n" \
"v_pk_add_f16 v162, v162, v166\n" \
"v_pk_add_f16 v163, v163, v167\n" \
"ds_read_b64_tr_b16 v[188:189], v237 offset:36864\n" \
"ds_read_b64_tr_b16 v[190:191], v237 offset:36864\n" \
"v_mfma_f32_16x16x32_f16 v[16:19], v[148:151], v[164:167], v[16:19]\n" \
"ds_read_b128 v[180:183], v240 offset:25088\n" \
"s_add_i32 m0, s44, 0x0\n" \
"s_add_i32 s80, s46, 0x5000000\n" \
"v_mfma_f32_16x16x32_f16 v[8:11], v[128:131], v[160:163], v[8:11]\n" \
"buffer_load_dwordx4 v232, s[36:39], s80 offen sc1 lds\n" \
"s_waitcnt lgkmcnt(3)\n" \
"v_mfma_f32_16x16x32_bf16 v[184:187], v[184:187], v[192:195], 0\n" \
"v_mfma_f32_16x16x32_f16 v[20:23], v[132:135], v[160:163], v[20:23]\n" \
"ds_read_b128 v[196:199], v249 offset:4096\n" \
"ds_read_b64_tr_b16 v[200:201], v247 offset:16384\n" \
"s_waitcnt lgkmcnt(3)\n" \
"v_mfma_f32_16x16x32_bf16 v[188:191], v[188:191], v[192:195], 0\n" \
"v_mfma_f32_16x16x32_f16 v[24:27], v[120:123], v[168:171], v[24:27]\n" \
"ds_read_b128 v[160:163], v240 offset:26624\n" \
"ds_read_b64_tr_b16 v[136:137], v241 offset:16384\n" \
"ds_read_b64_tr_b16 v[138:139], v241 offset:24576\n" \
"v_mfma_f32_16x16x32_f16 v[36:39], v[124:127], v[168:171], v[36:39]\n" \
"v_pk_add_f16 v168, v168, v172\n" \
"v_pk_add_f16 v169, v169, v173\n" \
"ds_read_b64_tr_b16 v[202:203], v247 offset:16896\n" \
"ds_read_b64_tr_b16 v[140:141], v242 offset:16384\n" \
"v_mfma_f32_16x16x32_f16 v[28:31], v[144:147], v[172:175], v[28:31]\n" \
"v_pk_add_f16 v170, v170, v174\n" \
"v_pk_add_f16 v171, v171, v175\n" \
"v_cvt_pk_f16_f32 v184, v184, v185\n" \
"v_mfma_f32_16x16x32_f16 v[40:43], v[148:151], v[172:175], v[40:43]\n" \
"v_cvt_pk_f16_f32 v185, v186, v187\n" \
"v_cvt_pk_f16_f32 v186, v188, v189\n" \
"ds_read_b128 v[164:167], v240 offset:27136\n" \
"v_mfma_f32_16x16x32_f16 v[32:35], v[128:131], v[168:171], v[32:35]\n" \
"v_cvt_pk_f16_f32 v187, v190, v191\n" \
"v_add_u32 v184, s91, v184\n" \
"buffer_load_dwordx4 v232, s[36:39], s80 offen offset:1024 sc1 lds\n" \
"v_mfma_f32_16x16x32_f16 v[44:47], v[132:135], v[168:171], v[44:47]\n" \
"v_add_u32 v185, s91, v185\n" \
"v_add_u32 v186, s91, v186\n" \
"ds_read_b64_tr_b16 v[142:143], v242 offset:24576\n" \
"v_mfma_f32_16x16x32_f16 v[48:51], v[120:123], v[176:179], v[48:51]\n" \
"v_add_u32 v187, s91, v187\n" \
"v_and_b32 v184, s92, v184\n" \
"ds_read_b128 v[168:171], v240 offset:28672\n" \
"v_mfma_f32_16x16x32_f16 v[60:63], v[124:127], v[176:179], v[60:63]\n" \
"s_waitcnt lgkmcnt(5)\n" \
"v_pk_add_f16 v176, v176, v180\n" \
"v_pk_add_f16 v177, v177, v181\n" \
"v_and_b32 v185, s92, v185\n" \
"s_add_i32 s82, s50, 0x7000000\n" \
"s_waitcnt lgkmcnt(3)\n" \
"v_mfma_f32_16x16x32_bf16 v[204:207], v[200:203], v[196:199], v[204:207]\n" \
"v_mfma_f32_16x16x32_f16 v[52:55], v[144:147], v[180:183], v[52:55]\n" \
"v_pk_add_f16 v178, v178, v182\n" \
"v_pk_add_f16 v179, v179, v183\n" \
"v_and_b32 v186, s92, v186\n" \
"v_and_b32 v187, s92, v187\n" \
"v_mfma_f32_16x16x32_f16 v[64:67], v[148:151], v[180:183], v[64:67]\n" \
"ds_read_b128 v[172:175], v240 offset:29184\n" \
"buffer_store_dwordx4 v[184:187], v234, s[36:39], s82 offen sc1\n" \
"ds_read_b64_tr_b16 v[152:153], v241 offset:16896\n" \
"v_mfma_f32_16x16x32_f16 v[56:59], v[128:131], v[176:179], v[56:59]\n" \
"buffer_load_dwordx4 v232, s[36:39], s80 offen offset:2048 sc1 lds\n" \
"ds_read_b64_tr_b16 v[154:155], v241 offset:25088\n" \
"v_mfma_f32_16x16x32_f16 v[68:71], v[132:135], v[176:179], v[68:71]\n" \
"ds_read_b128 v[196:199], v249 offset:5120\n" \
"ds_read_b64_tr_b16 v[200:201], v247 offset:20480\n" \
"ds_read_b64_tr_b16 v[156:157], v242 offset:16896\n" \
"v_mfma_f32_16x16x32_f16 v[72:75], v[120:123], v[160:163], v[72:75]\n" \
"ds_read_b128 v[176:179], v240 offset:20480\n" \
"ds_read_b64_tr_b16 v[202:203], v247 offset:20992\n" \
"v_mfma_f32_16x16x32_f16 v[84:87], v[124:127], v[160:163], v[84:87]\n" \
"s_waitcnt lgkmcnt(8)\n" \
"v_pk_add_f16 v160, v160, v164\n" \
"v_pk_add_f16 v161, v161, v165\n" \
"v_mfma_f32_16x16x32_f16 v[76:79], v[144:147], v[164:167], v[76:79]\n" \
"v_pk_add_f16 v162, v162, v166\n" \
"v_pk_add_f16 v163, v163, v167\n" \
"v_mfma_f32_16x16x32_f16 v[88:91], v[148:151], v[164:167], v[88:91]\n" \
"ds_read_b128 v[180:183], v240 offset:20992\n" \
"ds_read_b64_tr_b16 v[158:159], v242 offset:25088\n" \
"v_mfma_f32_16x16x32_f16 v[80:83], v[128:131], v[160:163], v[80:83]\n" \
"buffer_load_dwordx4 v232, s[36:39], s80 offen offset:3072 sc1 lds\n" \
"v_mfma_f32_16x16x32_f16 v[92:95], v[132:135], v[160:163], v[92:95]\n" \
"v_mfma_f32_16x16x32_f16 v[96:99], v[120:123], v[168:171], v[96:99]\n" \
"s_waitcnt lgkmcnt(8)\n" \
"v_pk_add_f16 v120, v136, v152\n" \
"v_pk_add_f16 v121, v137, v153\n" \
"ds_read_b128 v[160:163], v240 offset:22528\n" \
"s_add_i32 m0, s45, 0x3000\n" \
"s_mov_b32 s83, 0x48000\n" \
"s_waitcnt lgkmcnt(3)\n" \
"v_mfma_f32_16x16x32_bf16 v[204:207], v[200:203], v[196:199], v[204:207]\n" \
"v_mfma_f32_16x16x32_f16 v[108:111], v[124:127], v[168:171], v[108:111]\n" \
"v_pk_add_f16 v168, v168, v172\n" \
"v_pk_add_f16 v169, v169, v173\n" \
"ds_read_b128 v[164:167], v240 offset:23040\n" \
"buffer_load_dwordx4 v233, s[36:39], s83 offen sc1 lds\n" \
"v_mfma_f32_16x16x32_f16 v[100:103], v[144:147], v[172:175], v[100:103]\n" \
"v_pk_add_f16 v170, v170, v174\n" \
"v_pk_add_f16 v171, v171, v175\n" \
"v_mfma_f32_16x16x32_f16 v[112:115], v[148:151], v[172:175], v[112:115]\n" \
"v_pk_add_f16 v122, v138, v154\n" \
"v_pk_add_f16 v123, v139, v155\n" \
"v_mfma_f32_16x16x32_f16 v[104:107], v[128:131], v[168:171], v[104:107]\n" \
"v_pk_add_f16 v124, v140, v156\n" \
"v_pk_add_f16 v125, v141, v157\n" \
"v_mfma_f32_16x16x32_f16 v[116:119], v[132:135], v[168:171], v[116:119]\n" \
"s_waitcnt lgkmcnt(2)\n" \
"v_pk_add_f16 v126, v142, v158\n" \
"v_pk_add_f16 v127, v143, v159\n" \
"s_waitcnt vmcnt(7) lgkmcnt(0)\n" \
"s_barrier\n" \
"v_mfma_f32_16x16x32_f16 v[0:3], v[136:139], v[176:179], v[0:3]\n" \
"ds_read_b128 v[168:171], v240 offset:45056\n" \
"ds_read_b64_tr_b16 v[184:185], v238 offset:36864\n" \
"ds_read_b64_tr_b16 v[186:187], v238 offset:36864\n" \
"s_add_i32 m0, s45, 0x4000\n" \
"s_mov_b32 s83, 0x50000\n" \
"v_mfma_f32_16x16x32_f16 v[12:15], v[140:143], v[176:179], v[12:15]\n" \
"v_pk_add_f16 v176, v176, v180\n" \
"v_pk_add_f16 v177, v177, v181\n" \
"ds_read_b64_tr_b16 v[188:189], v239 offset:36864\n" \
"ds_read_b64_tr_b16 v[190:191], v239 offset:36864\n" \
"buffer_load_dwordx4 v233, s[36:39], s83 offen sc1 lds\n" \
"v_mfma_f32_16x16x32_f16 v[4:7], v[152:155], v[180:183], v[4:7]\n" \
"v_pk_add_f16 v178, v178, v182\n" \
"v_pk_add_f16 v179, v179, v183\n" \
"v_cmp_eq_u32 vcc, 0, v253\n" \
"s_cbranch_vccnz .Ls09_%=\n" \
".Lr09_%=:\n" \
"v_mfma_f32_16x16x32_f16 v[16:19], v[156:159], v[180:183], v[16:19]\n" \
"ds_read_b128 v[172:175], v240 offset:45568\n" \
"s_add_i32 m0, s44, 0x4000\n" \
"s_add_i32 s80, s46, 0x5400000\n" \
"v_mfma_f32_16x16x32_f16 v[8:11], v[120:123], v[176:179], v[8:11]\n" \
"buffer_load_dwordx4 v232, s[36:39], s80 offen sc1 lds\n" \
"ds_read_b64_tr_b16 v[128:129], v241 offset:32768\n" \
"s_waitcnt lgkmcnt(4)\n" \
"v_mfma_f32_16x16x32_bf16 v[184:187], v[184:187], v[192:195], 0\n" \
"v_mfma_f32_16x16x32_f16 v[20:23], v[124:127], v[176:179], v[20:23]\n" \
"ds_read_b128 v[196:199], v249 offset:6144\n" \
"ds_read_b64_tr_b16 v[200:201], v247 offset:0\n" \
"s_waitcnt lgkmcnt(4)\n" \
"v_mfma_f32_16x16x32_bf16 v[188:191], v[188:191], v[192:195], 0\n" \
"v_mfma_f32_16x16x32_f16 v[24:27], v[136:139], v[160:163], v[24:27]\n" \
"ds_read_b128 v[176:179], v240 offset:47104\n" \
"s_add_i32 s84, s53, 0x8016000\n" \
"ds_read_b64_tr_b16 v[130:131], v241 offset:40960\n" \
"v_mfma_f32_16x16x32_f16 v[36:39], v[140:143], v[160:163], v[36:39]\n" \
"v_pk_add_f16 v160, v160, v164\n" \
"v_pk_add_f16 v161, v161, v165\n" \
"buffer_store_dword v240, v243, s[36:39], s84 offen sc1\n" \
"v_mfma_f32_16x16x32_f16 v[28:31], v[152:155], v[164:167], v[28:31]\n" \
"v_pk_add_f16 v162, v162, v166\n" \
"v_pk_add_f16 v163, v163, v167\n" \
"v_cvt_pk_f16_f32 v184, v184, v185\n" \
"ds_read_b64_tr_b16 v[202:203], v247 offset:512\n" \
"v_mfma_f32_16x16x32_f16 v[40:43], v[156:159], v[164:167], v[40:43]\n" \
"v_cvt_pk_f16_f32 v185, v186, v187\n" \
"v_cvt_pk_f16_f32 v186, v188, v189\n" \
"ds_read_b128 v[180:183], v240 offset:47616\n" \
"ds_read_b64_tr_b16 v[132:133], v242 offset:32768\n" \
"v_mfma_f32_16x16x32_f16 v[32:35], v[120:123], v[160:163], v[32:35]\n" \
"v_cvt_pk_f16_f32 v187, v190, v191\n" \
"v_add_u32 v184, s91, v184\n" \
"buffer_load_dwordx4 v232, s[36:39], s80 offen offset:1024 sc1 lds\n" \
"v_mfma_f32_16x16x32_f16 v[44:47], v[124:127], v[160:163], v[44:47]\n" \
"v_add_u32 v185, s91, v185\n" \
"v_add_u32 v186, s91, v186\n" \
"ds_read_b64_tr_b16 v[134:135], v242 offset:40960\n" \
"v_mfma_f32_16x16x32_f16 v[48:51], v[136:139], v[168:171], v[48:51]\n" \
"v_add_u32 v187, s91, v187\n" \
"v_and_b32 v184, s92, v184\n" \
"ds_read_b128 v[160:163], v240 offset:49152\n" \
"v_mfma_f32_16x16x32_f16 v[60:63], v[140:143], v[168:171], v[60:63]\n" \
"s_waitcnt lgkmcnt(5)\n" \
"v_pk_add_f16 v168, v168, v172\n" \
"v_pk_add_f16 v169, v169, v173\n" \
"v_and_b32 v185, s92, v185\n" \
"s_waitcnt lgkmcnt(4)\n" \
"v_mfma_f32_16x16x32_bf16 v[204:207], v[200:203], v[196:199], v[204:207]\n" \
"v_mfma_f32_16x16x32_f16 v[52:55], v[152:155], v[172:175], v[52:55]\n" \
"v_pk_add_f16 v170, v170, v174\n" \
"v_pk_add_f16 v171, v171, v175\n" \
"v_and_b32 v186, s92, v186\n" \
"v_and_b32 v187, s92, v187\n" \
"v_mfma_f32_16x16x32_f16 v[64:67], v[156:159], v[172:175], v[64:67]\n" \
"ds_read_b128 v[164:167], v240 offset:49664\n" \
"buffer_store_dwordx4 v[184:187], v234, s[36:39], s82 offen offset:1024 sc1\n" \
"ds_read_b64_tr_b16 v[144:145], v241 offset:33280\n" \
"v_mfma_f32_16x16x32_f16 v[56:59], v[120:123], v[168:171], v[56:59]\n" \
"buffer_load_dwordx4 v232, s[36:39], s80 offen offset:2048 sc1 lds\n" \
"ds_read_b64_tr_b16 v[146:147], v241 offset:41472\n" \
"v_mfma_f32_16x16x32_f16 v[68:71], v[124:127], v[168:171], v[68:71]\n" \
"ds_read_b128 v[196:199], v249 offset:7168\n" \
"ds_read_b64_tr_b16 v[200:201], v247 offset:4096\n" \
"ds_read_b64_tr_b16 v[148:149], v242 offset:33280\n" \
"v_mfma_f32_16x16x32_f16 v[72:75], v[136:139], v[176:179], v[72:75]\n" \
"ds_read_b128 v[168:171], v240 offset:40960\n" \
"ds_read_b64_tr_b16 v[202:203], v247 offset:4608\n" \
"v_mfma_f32_16x16x32_f16 v[84:87], v[140:143], v[176:179], v[84:87]\n" \
"s_waitcnt lgkmcnt(8)\n" \
"v_pk_add_f16 v176, v176, v180\n" \
"v_pk_add_f16 v177, v177, v181\n" \
"v_mfma_f32_16x16x32_f16 v[76:79], v[152:155], v[180:183], v[76:79]\n" \
"v_pk_add_f16 v178, v178, v182\n" \
"v_pk_add_f16 v179, v179, v183\n" \
"v_mfma_f32_16x16x32_f16 v[88:91], v[156:159], v[180:183], v[88:91]\n" \
"ds_read_b128 v[172:175], v240 offset:41472\n" \
"ds_read_b64_tr_b16 v[150:151], v242 offset:41472\n" \
"v_mfma_f32_16x16x32_f16 v[80:83], v[120:123], v[176:179], v[80:83]\n" \
"buffer_load_dwordx4 v232, s[36:39], s80 offen offset:3072 sc1 lds\n" \
"v_mfma_f32_16x16x32_f16 v[92:95], v[124:127], v[176:179], v[92:95]\n" \
"v_mfma_f32_16x16x32_f16 v[96:99], v[136:139], v[160:163], v[96:99]\n" \
"s_waitcnt lgkmcnt(8)\n" \
"v_pk_add_f16 v136, v128, v144\n" \
"v_pk_add_f16 v137, v129, v145\n" \
"ds_read_b128 v[176:179], v240 offset:43008\n" \
"s_add_i32 m0, s45, 0x5000\n" \
"s_mov_b32 s83, 0x58000\n" \
"s_waitcnt lgkmcnt(3)\n" \
"v_mfma_f32_16x16x32_bf16 v[204:207], v[200:203], v[196:199], v[204:207]\n" \
"v_mfma_f32_16x16x32_f16 v[108:111], v[140:143], v[160:163], v[108:111]\n" \
"v_pk_add_f16 v160, v160, v164\n" \
"v_pk_add_f16 v161, v161, v165\n" \
"ds_read_b128 v[180:183], v240 offset:43520\n" \
"buffer_load_dwordx4 v233, s[36:39], s83 offen sc1 lds\n" \
"v_mfma_f32_16x16x32_f16 v[100:103], v[152:155], v[164:167], v[100:103]\n" \
"v_pk_add_f16 v162, v162, v166\n" \
"v_pk_add_f16 v163, v163, v167\n" \
"v_mfma_f32_16x16x32_f16 v[112:115], v[156:159], v[164:167], v[112:115]\n" \
"v_pk_add_f16 v138, v130, v146\n" \
"v_pk_add_f16 v139, v131, v147\n" \
"v_mfma_f32_16x16x32_f16 v[104:107], v[120:123], v[160:163], v[104:107]\n" \
"v_pk_add_f16 v140, v132, v148\n" \
"v_pk_add_f16 v141, v133, v149\n" \
"v_mfma_f32_16x16x32_f16 v[116:119], v[124:127], v[160:163], v[116:119]\n" \
"s_waitcnt lgkmcnt(2)\n" \
"v_pk_add_f16 v142, v134, v150\n" \
"v_pk_add_f16 v143, v135, v151\n" \
"s_waitcnt vmcnt(8) lgkmcnt(0)\n" \
"s_barrier\n" \
"v_mfma_f32_16x16x32_f16 v[0:3], v[128:131], v[168:171], v[0:3]\n" \
"ds_read_b128 v[160:163], v240 offset:4096\n" \
"ds_read_b64_tr_b16 v[184:185], v236 offset:40960\n" \
"ds_read_b64_tr_b16 v[186:187], v236 offset:40960\n" \
"s_add_i32 m0, s45, 0x0\n" \
"s_mov_b32 s83, 0x60000\n" \
"v_mfma_f32_16x16x32_f16 v[12:15], v[132:135], v[168:171], v[12:15]\n" \
"v_pk_add_f16 v168, v168, v172\n" \
"v_pk_add_f16 v169, v169, v173\n" \
"ds_read_b64_tr_b16 v[188:189], v237 offset:40960\n" \
"buffer_load_dwordx4 v233, s[36:39], s83 offen sc1 lds\n" \
"v_mfma_f32_16x16x32_f16 v[4:7], v[144:147], v[172:175], v[4:7]\n" \
"v_pk_add_f16 v170, v170, v174\n" \
"v_pk_add_f16 v171, v171, v175\n" \
"ds_read_b64_tr_b16 v[190:191], v237 offset:40960\n" \
"v_mfma_f32_16x16x32_f16 v[16:19], v[148:151], v[172:175], v[16:19]\n" \
"ds_read_b128 v[164:167], v240 offset:4608\n" \
"s_add_i32 m0, s44, 0x8000\n" \
"s_add_i32 s80, s46, 0x5800000\n" \
"v_mfma_f32_16x16x32_f16 v[8:11], v[136:139], v[168:171], v[8:11]\n" \
"buffer_load_dwordx4 v232, s[36:39], s80 offen sc1 lds\n" \
"v_mfma_f32_16x16x32_f16 v[20:23], v[140:143], v[168:171], v[20:23]\n" \
"ds_read_b128 v[196:199], v249 offset:0\n" \
"ds_read_b64_tr_b16 v[200:201], v247 offset:8192\n" \
"s_waitcnt lgkmcnt(5)\n" \
"v_mfma_f32_16x16x32_bf16 v[184:187], v[184:187], v[192:195], 0\n" \
"v_mfma_f32_16x16x32_f16 v[24:27], v[128:131], v[176:179], v[24:27]\n" \
"ds_read_b128 v[168:171], v240 offset:6144\n" \
"ds_read_b64_tr_b16 v[120:121], v241 offset:0\n" \
"ds_read_b64_tr_b16 v[122:123], v241 offset:8192\n" \
"s_waitcnt lgkmcnt(6)\n" \
"v_mfma_f32_16x16x32_bf16 v[188:191], v[188:191], v[192:195], 0\n" \
"v_mfma_f32_16x16x32_f16 v[36:39], v[132:135], v[176:179], v[36:39]\n" \
"v_pk_add_f16 v176, v176, v180\n" \
"v_pk_add_f16 v177, v177, v181\n" \
"ds_read_b64_tr_b16 v[202:203], v247 offset:8704\n" \
"v_mfma_f32_16x16x32_f16 v[28:31], v[144:147], v[180:183], v[28:31]\n" \
"v_pk_add_f16 v178, v178, v182\n" \
"v_pk_add_f16 v179, v179, v183\n" \
"ds_read_b64_tr_b16 v[124:125], v242 offset:0\n" \
"v_mfma_f32_16x16x32_f16 v[40:43], v[148:151], v[180:183], v[40:43]\n" \
"v_cvt_pk_f16_f32 v184, v184, v185\n" \
"ds_read_b128 v[172:175], v240 offset:6656\n" \
"ds_read_b64_tr_b16 v[126:127], v242 offset:8192\n" \
"v_mfma_f32_16x16x32_f16 v[32:35], v[136:139], v[176:179], v[32:35]\n" \
"v_cvt_pk_f16_f32 v185, v186, v187\n" \
"v_cvt_pk_f16_f32 v186, v188, v189\n" \
"buffer_load_dwordx4 v232, s[36:39], s80 offen offset:1024 sc1 lds\n" \
"v_mfma_f32_16x16x32_f16 v[44:47], v[140:143], v[176:179], v[44:47]\n" \
"v_cvt_pk_f16_f32 v187, v190, v191\n" \
"v_add_u32 v184, s91, v184\n" \
"v_mfma_f32_16x16x32_f16 v[48:51], v[128:131], v[160:163], v[48:51]\n" \
"v_add_u32 v185, s91, v185\n" \
"v_add_u32 v186, s91, v186\n" \
"ds_read_b128 v[176:179], v240 offset:8192\n" \
"v_mfma_f32_16x16x32_f16 v[60:63], v[132:135], v[160:163], v[60:63]\n" \
"s_waitcnt lgkmcnt(5)\n" \
"v_pk_add_f16 v160, v160, v164\n" \
"v_pk_add_f16 v161, v161, v165\n" \
"v_add_u32 v187, s91, v187\n" \
"v_and_b32 v184, s92, v184\n" \
"s_add_i32 s82, s51, 0x7800000\n" \
"s_waitcnt lgkmcnt(4)\n" \
"v_mfma_f32_16x16x32_bf16 v[204:207], v[200:203], v[196:199], v[204:207]\n" \
"v_mfma_f32_16x16x32_f16 v[52:55], v[144:147], v[164:167], v[52:55]\n" \
"v_pk_add_f16 v162, v162, v166\n" \
"v_pk_add_f16 v163, v163, v167\n" \
"v_and_b32 v185, s92, v185\n" \
"v_and_b32 v186, s92, v186\n" \
"v_and_b32 v187, s92, v187\n" \
"v_mfma_f32_16x16x32_f16 v[64:67], v[148:151], v[164:167], v[64:67]\n" \
"ds_read_b128 v[180:183], v240 offset:8704\n" \
"buffer_store_dwordx4 v[184:187], v234, s[36:39], s82 offen sc1\n" \
"ds_read_b64_tr_b16 v[152:153], v241 offset:512\n" \
"v_mfma_f32_16x16x32_f16 v[56:59], v[136:139], v[160:163], v[56:59]\n" \
"buffer_load_dwordx4 v232, s[36:39], s80 offen offset:2048 sc1 lds\n" \
"ds_read_b64_tr_b16 v[184:185], v238 offset:40960\n" \
"ds_read_b64_tr_b16 v[154:155], v241 offset:8704\n" \
"v_mfma_f32_16x16x32_f16 v[68:71], v[140:143], v[160:163], v[68:71]\n" \
"ds_read_b64_tr_b16 v[186:187], v238 offset:40960\n" \
"ds_read_b64_tr_b16 v[188:189], v239 offset:40960\n" \
"ds_read_b128 v[196:199], v249 offset:1024\n" \
"ds_read_b64_tr_b16 v[156:157], v242 offset:512\n" \
"v_mfma_f32_16x16x32_f16 v[72:75], v[128:131], v[168:171], v[72:75]\n" \
"ds_read_b128 v[160:163], v240 offset:0\n" \
"ds_read_b64_tr_b16 v[190:191], v239 offset:40960\n" \
"ds_read_b64_tr_b16 v[200:201], v247 offset:12288\n" \
"v_mfma_f32_16x16x32_f16 v[84:87], v[132:135], v[168:171], v[84:87]\n" \
"s_waitcnt lgkmcnt(11)\n" \
"v_pk_add_f16 v168, v168, v172\n" \
"v_pk_add_f16 v169, v169, v173\n" \
"ds_read_b64_tr_b16 v[202:203], v247 offset:12800\n" \
"v_mfma_f32_16x16x32_f16 v[76:79], v[144:147], v[172:175], v[76:79]\n" \
"v_pk_add_f16 v170, v170, v174\n" \
"v_pk_add_f16 v171, v171, v175\n" \
"s_waitcnt lgkmcnt(7)\n" \
"v_mfma_f32_16x16x32_bf16 v[184:187], v[184:187], v[192:195], 0\n" \
"v_mfma_f32_16x16x32_f16 v[88:91], v[148:151], v[172:175], v[88:91]\n" \
"ds_read_b128 v[164:167], v240 offset:512\n" \
"ds_read_b64_tr_b16 v[158:159], v242 offset:8704\n" \
"s_waitcnt lgkmcnt(4)\n" \
"v_mfma_f32_16x16x32_bf16 v[188:191], v[188:191], v[192:195], 0\n" \
"v_mfma_f32_16x16x32_f16 v[80:83], v[136:139], v[168:171], v[80:83]\n" \
"buffer_load_dwordx4 v232, s[36:39], s80 offen offset:3072 sc1 lds\n" \
"v_mfma_f32_16x16x32_f16 v[92:95], v[140:143], v[168:171], v[92:95]\n" \
"v_mfma_f32_16x16x32_f16 v[96:99], v[128:131], v[176:179], v[96:99]\n" \
"v_pk_add_f16 v128, v120, v152\n" \
"v_pk_add_f16 v129, v121, v153\n" \
"v_cvt_pk_f16_f32 v184, v184, v185\n" \
"v_cvt_pk_f16_f32 v185, v186, v187\n" \
"ds_read_b128 v[168:171], v240 offset:2048\n" \
"s_add_i32 m0, s45, 0x1000\n" \
"s_mov_b32 s83, 0x68000\n" \
"s_waitcnt lgkmcnt(3)\n" \
"v_mfma_f32_16x16x32_bf16 v[204:207], v[200:203], v[196:199], v[204:207]\n" \
"v_mfma_f32_16x16x32_f16 v[108:111], v[132:135], v[176:179], v[108:111]\n" \
"v_pk_add_f16 v176, v176, v180\n" \
"v_pk_add_f16 v177, v177, v181\n" \
"v_cvt_pk_f16_f32 v186, v188, v189\n" \
"v_cvt_pk_f16_f32 v187, v190, v191\n" \
"ds_read_b128 v[172:175], v240 offset:2560\n" \
"buffer_load_dwordx4 v233, s[36:39], s83 offen sc1 lds\n" \
"v_mfma_f32_16x16x32_f16 v[100:103], v[144:147], v[180:183], v[100:103]\n" \
"v_pk_add_f16 v178, v178, v182\n" \
"v_pk_add_f16 v179, v179, v183\n" \
"v_add_u32 v184, s91, v184\n" \
"v_add_u32 v185, s91, v185\n" \
"v_add_u32 v186, s91, v186\n" \
"v_mfma_f32_16x16x32_f16 v[112:115], v[148:151], v[180:183], v[112:115]\n" \
"v_pk_add_f16 v130, v122, v154\n" \
"v_pk_add_f16 v131, v123, v155\n" \
"v_add_u32 v187, s91, v187\n" \
"v_and_b32 v184, s92, v184\n" \
"v_mfma_f32_16x16x32_f16 v[104:107], v[136:139], v[176:179], v[104:107]\n" \
"v_pk_add_f16 v132, v124, v156\n" \
"v_pk_add_f16 v133, v125, v157\n" \
"v_and_b32 v185, s92, v185\n" \
"v_and_b32 v186, s92, v186\n" \
"v_and_b32 v187, s92, v187\n" \
"v_mfma_f32_16x16x32_f16 v[116:119], v[140:143], v[176:179], v[116:119]\n" \
"s_waitcnt lgkmcnt(1)\n" \
"v_pk_add_f16 v134, v126, v158\n" \
"v_pk_add_f16 v135, v127, v159\n" \
"buffer_store_dwordx4 v[184:187], v234, s[36:39], s82 offen offset:1024 sc1\n" \
"s_waitcnt vmcnt(8) lgkmcnt(0)\n" \
"s_barrier\n" \
"v_mfma_f32_16x16x32_f16 v[0:3], v[120:123], v[160:163], v[0:3]\n" \
"ds_read_b128 v[176:179], v240 offset:24576\n" \
"s_add_i32 m0, s45, 0x2000\n" \
"s_mov_b32 s83, 0x70000\n" \
"v_mfma_f32_16x16x32_f16 v[12:15], v[124:127], v[160:163], v[12:15]\n" \
"v_pk_add_f16 v160, v160, v164\n" \
"v_pk_add_f16 v161, v161, v165\n" \
"buffer_load_dwordx4 v233, s[36:39], s83 offen sc1 lds\n" \
"v_mfma_f32_16x16x32_f16 v[4:7], v[152:155], v[164:167], v[4:7]\n" \
"v_pk_add_f16 v162, v162, v166\n" \
"v_pk_add_f16 v163, v163, v167\n" \
"v_mfma_f32_16x16x32_f16 v[16:19], v[156:159], v[164:167], v[16:19]\n" \
"ds_read_b128 v[180:183], v240 offset:25088\n" \
"s_add_i32 m0, s44, 0x0\n" \
"s_add_i32 s80, s46, 0x5c00000\n" \
"v_mfma_f32_16x16x32_f16 v[8:11], v[128:131], v[160:163], v[8:11]\n" \
"buffer_load_dwordx4 v232, s[36:39], s80 offen sc1 lds\n" \
"ds_read_b64_tr_b16 v[136:137], v241 offset:16384\n" \
"v_mfma_f32_16x16x32_f16 v[20:23], v[132:135], v[160:163], v[20:23]\n" \
"ds_read_b128 v[196:199], v249 offset:2048\n" \
"ds_read_b64_tr_b16 v[200:201], v247 offset:16384\n" \
"v_mfma_f32_16x16x32_f16 v[24:27], v[120:123], v[168:171], v[24:27]\n" \
"ds_read_b128 v[160:163], v240 offset:26624\n" \
"s_add_i32 s84, s54, 0x8018000\n" \
"ds_read_b64_tr_b16 v[138:139], v241 offset:24576\n" \
"v_mfma_f32_16x16x32_f16 v[36:39], v[124:127], v[168:171], v[36:39]\n" \
"v_pk_add_f16 v168, v168, v172\n" \
"v_pk_add_f16 v169, v169, v173\n" \
"buffer_store_dword v240, v243, s[36:39], s84 offen sc1\n" \
"v_mfma_f32_16x16x32_f16 v[28:31], v[152:155], v[172:175], v[28:31]\n" \
"v_pk_add_f16 v170, v170, v174\n" \
"v_pk_add_f16 v171, v171, v175\n" \
"v_mfma_f32_16x16x32_f16 v[40:43], v[156:159], v[172:175], v[40:43]\n" \
"ds_read_b128 v[164:167], v240 offset:27136\n" \
"ds_read_b64_tr_b16 v[202:203], v247 offset:16896\n" \
"v_mfma_f32_16x16x32_f16 v[32:35], v[128:131], v[168:171], v[32:35]\n" \
"buffer_load_dwordx4 v232, s[36:39], s80 offen offset:1024 sc1 lds\n" \
"ds_read_b64_tr_b16 v[140:141], v242 offset:16384\n" \
"v_mfma_f32_16x16x32_f16 v[44:47], v[132:135], v[168:171], v[44:47]\n" \
"ds_read_b64_tr_b16 v[142:143], v242 offset:24576\n" \
"s_waitcnt lgkmcnt(9)\n" \
"v_mfma_f32_16x16x32_f16 v[48:51], v[120:123], v[176:179], v[48:51]\n" \
"ds_read_b128 v[168:171], v240 offset:28672\n" \
"ds_read_b64_tr_b16 v[144:145], v241 offset:16896\n" \
"v_mfma_f32_16x16x32_f16 v[60:63], v[124:127], v[176:179], v[60:63]\n" \
"v_pk_add_f16 v176, v176, v180\n" \
"v_pk_add_f16 v177, v177, v181\n" \
"s_waitcnt lgkmcnt(4)\n" \
"v_mfma_f32_16x16x32_bf16 v[204:207], v[200:203], v[196:199], v[204:207]\n" \
"v_mfma_f32_16x16x32_f16 v[52:55], v[152:155], v[180:183], v[52:55]\n" \
"v_pk_add_f16 v178, v178, v182\n" \
"v_pk_add_f16 v179, v179, v183\n" \
"v_mfma_f32_16x16x32_f16 v[64:67], v[156:159], v[180:183], v[64:67]\n" \
"ds_read_b128 v[172:175], v240 offset:29184\n" \
"ds_read_b64_tr_b16 v[146:147], v241 offset:25088\n" \
"v_mfma_f32_16x16x32_f16 v[56:59], v[128:131], v[176:179], v[56:59]\n" \
"buffer_load_dwordx4 v232, s[36:39], s80 offen offset:2048 sc1 lds\n" \
"ds_read_b64_tr_b16 v[148:149], v242 offset:16896\n" \
"v_mfma_f32_16x16x32_f16 v[68:71], v[132:135], v[176:179], v[68:71]\n" \
"ds_read_b128 v[196:199], v249 offset:3072\n" \
"ds_read_b64_tr_b16 v[200:201], v247 offset:20480\n" \
"v_mfma_f32_16x16x32_f16 v[72:75], v[120:123], v[160:163], v[72:75]\n" \
"ds_read_b128 v[176:179], v240 offset:20480\n" \
"ds_read_b64_tr_b16 v[202:203], v247 offset:20992\n" \
"v_mfma_f32_16x16x32_f16 v[84:87], v[124:127], v[160:163], v[84:87]\n" \
"v_pk_add_f16 v160, v160, v164\n" \
"v_pk_add_f16 v161, v161, v165\n" \
"v_mfma_f32_16x16x32_f16 v[76:79], v[152:155], v[164:167], v[76:79]\n" \
"v_pk_add_f16 v162, v162, v166\n" \
"v_pk_add_f16 v163, v163, v167\n" \
"v_mfma_f32_16x16x32_f16 v[88:91], v[156:159], v[164:167], v[88:91]\n" \
"ds_read_b128 v[180:183], v240 offset:20992\n" \
"ds_read_b64_tr_b16 v[150:151], v242 offset:25088\n" \
"v_mfma_f32_16x16x32_f16 v[80:83], v[128:131], v[160:163], v[80:83]\n" \
"buffer_load_dwordx4 v232, s[36:39], s80 offen offset:3072 sc1 lds\n" \
"v_mfma_f32_16x16x32_f16 v[92:95], v[132:135], v[160:163], v[92:95]\n" \
"s_waitcnt lgkmcnt(9)\n" \
"v_mfma_f32_16x16x32_f16 v[96:99], v[120:123], v[168:171], v[96:99]\n" \
"v_pk_add_f16 v120, v136, v144\n" \
"v_pk_add_f16 v121, v137, v145\n" \
"ds_read_b128 v[160:163], v240 offset:22528\n" \
"s_add_i32 m0, s45, 0x3000\n" \
"s_mov_b32 s83, 0x78000\n" \
"s_waitcnt lgkmcnt(3)\n" \
"v_mfma_f32_16x16x32_bf16 v[204:207], v[200:203], v[196:199], v[204:207]\n" \
"v_mfma_f32_16x16x32_f16 v[108:111], v[124:127], v[168:171], v[108:111]\n" \
"v_pk_add_f16 v168, v168, v172\n" \
"v_pk_add_f16 v169, v169, v173\n" \
"ds_read_b128 v[164:167], v240 offset:23040\n" \
"buffer_load_dwordx4 v233, s[36:39], s83 offen sc1 lds\n" \
"v_mfma_f32_16x16x32_f16 v[100:103], v[152:155], v[172:175], v[100:103]\n" \
"v_pk_add_f16 v170, v170, v174\n" \
"v_pk_add_f16 v171, v171, v175\n" \
"v_mfma_f32_16x16x32_f16 v[112:115], v[156:159], v[172:175], v[112:115]\n" \
"v_pk_add_f16 v122, v138, v146\n" \
"v_pk_add_f16 v123, v139, v147\n" \
"v_mfma_f32_16x16x32_f16 v[104:107], v[128:131], v[168:171], v[104:107]\n" \
"v_pk_add_f16 v124, v140, v148\n" \
"v_pk_add_f16 v125, v141, v149\n" \
"v_mfma_f32_16x16x32_f16 v[116:119], v[132:135], v[168:171], v[116:119]\n" \
"s_waitcnt lgkmcnt(2)\n" \
"v_pk_add_f16 v126, v142, v150\n" \
"v_pk_add_f16 v127, v143, v151\n" \
"s_waitcnt vmcnt(7) lgkmcnt(0)\n" \
"s_barrier\n" \
"v_mfma_f32_16x16x32_f16 v[0:3], v[136:139], v[176:179], v[0:3]\n" \
"ds_read_b128 v[168:171], v240 offset:45056\n" \
"s_mov_b32 s81, 0x8016000\n" \
"v_mfma_f32_16x16x32_f16 v[12:15], v[140:143], v[176:179], v[12:15]\n" \
"v_pk_add_f16 v176, v176, v180\n" \
"v_pk_add_f16 v177, v177, v181\n" \
"buffer_load_dword v253, v252, s[36:39], s81 offen sc1\n" \
"v_mfma_f32_16x16x32_f16 v[4:7], v[144:147], v[180:183], v[4:7]\n" \
"v_pk_add_f16 v178, v178, v182\n" \
"v_pk_add_f16 v179, v179, v183\n" \
"v_mfma_f32_16x16x32_f16 v[16:19], v[148:151], v[180:183], v[16:19]\n" \
"ds_read_b128 v[172:175], v240 offset:45568\n" \
"s_add_i32 m0, s44, 0x4000\n" \
"s_add_i32 s80, s46, 0x6000000\n" \
"v_mfma_f32_16x16x32_f16 v[8:11], v[120:123], v[176:179], v[8:11]\n" \
"buffer_load_dwordx4 v232, s[36:39], s80 offen sc1 lds\n" \
"ds_read_b64_tr_b16 v[128:129], v241 offset:32768\n" \
"v_mfma_f32_16x16x32_f16 v[20:23], v[124:127], v[176:179], v[20:23]\n" \
"ds_read_b128 v[196:199], v249 offset:4096\n" \
"ds_read_b64_tr_b16 v[200:201], v247 offset:0\n" \
"v_mfma_f32_16x16x32_f16 v[24:27], v[136:139], v[160:163], v[24:27]\n" \
"ds_read_b128 v[176:179], v240 offset:47104\n" \
"s_add_i32 s84, s55, 0x801a000\n" \
"ds_read_b64_tr_b16 v[130:131], v241 offset:40960\n" \
"v_mfma_f32_16x16x32_f16 v[36:39], v[140:143], v[160:163], v[36:39]\n" \
"v_pk_add_f16 v160, v160, v164\n" \
"v_pk_add_f16 v161, v161, v165\n" \
"buffer_store_dword v240, v243, s[36:39], s84 offen sc1\n" \
"v_mfma_f32_16x16x32_f16 v[28:31], v[144:147], v[164:167], v[28:31]\n" \
"v_pk_add_f16 v162, v162, v166\n" \
"v_pk_add_f16 v163, v163, v167\n" \
"v_mfma_f32_16x16x32_f16 v[40:43], v[148:151], v[164:167], v[40:43]\n" \
"ds_read_b128 v[180:183], v240 offset:47616\n" \
"ds_read_b64_tr_b16 v[202:203], v247 offset:512\n" \
"v_mfma_f32_16x16x32_f16 v[32:35], v[120:123], v[160:163], v[32:35]\n" \
"buffer_load_dwordx4 v232, s[36:39], s80 offen offset:1024 sc1 lds\n" \
"ds_read_b64_tr_b16 v[132:133], v242 offset:32768\n" \
"v_mfma_f32_16x16x32_f16 v[44:47], v[124:127], v[160:163], v[44:47]\n" \
"ds_read_b64_tr_b16 v[134:135], v242 offset:40960\n" \
"s_waitcnt lgkmcnt(9)\n" \
"v_mfma_f32_16x16x32_f16 v[48:51], v[136:139], v[168:171], v[48:51]\n" \
"ds_read_b128 v[160:163], v240 offset:49152\n" \
"ds_read_b64_tr_b16 v[152:153], v241 offset:33280\n" \
"v_mfma_f32_16x16x32_f16 v[60:63], v[140:143], v[168:171], v[60:63]\n" \
"v_pk_add_f16 v168, v168, v172\n" \
"v_pk_add_f16 v169, v169, v173\n" \
"s_waitcnt lgkmcnt(4)\n" \
"v_mfma_f32_16x16x32_bf16 v[204:207], v[200:203], v[196:199], v[204:207]\n" \
"v_mfma_f32_16x16x32_f16 v[52:55], v[144:147], v[172:175], v[52:55]\n" \
"v_pk_add_f16 v170, v170, v174\n" \
"v_pk_add_f16 v171, v171, v175\n" \
"v_mfma_f32_16x16x32_f16 v[64:67], v[148:151], v[172:175], v[64:67]\n" \
"ds_read_b128 v[164:167], v240 offset:49664\n" \
"ds_read_b64_tr_b16 v[154:155], v241 offset:41472\n" \
"v_mfma_f32_16x16x32_f16 v[56:59], v[120:123], v[168:171], v[56:59]\n" \
"buffer_load_dwordx4 v232, s[36:39], s80 offen offset:2048 sc1 lds\n" \
"ds_read_b64_tr_b16 v[156:157], v242 offset:33280\n" \
"v_mfma_f32_16x16x32_f16 v[68:71], v[124:127], v[168:171], v[68:71]\n" \
"ds_read_b128 v[196:199], v249 offset:5120\n" \
"ds_read_b64_tr_b16 v[200:201], v247 offset:4096\n" \
"v_mfma_f32_16x16x32_f16 v[72:75], v[136:139], v[176:179], v[72:75]\n" \
"ds_read_b128 v[168:171], v240 offset:40960\n" \
"ds_read_b64_tr_b16 v[202:203], v247 offset:4608\n" \
"v_mfma_f32_16x16x32_f16 v[84:87], v[140:143], v[176:179], v[84:87]\n" \
"v_pk_add_f16 v176, v176, v180\n" \
"v_pk_add_f16 v177, v177, v181\n" \
"v_mfma_f32_16x16x32_f16 v[76:79], v[144:147], v[180:183], v[76:79]\n" \
"v_pk_add_f16 v178, v178, v182\n" \
"v_pk_add_f16 v179, v179, v183\n" \
"v_mfma_f32_16x16x32_f16 v[88:91], v[148:151], v[180:183], v[88:91]\n" \
"ds_read_b128 v[172:175], v240 offset:41472\n" \
"ds_read_b64_tr_b16 v[158:159], v242 offset:41472\n" \
"v_mfma_f32_16x16x32_f16 v[80:83], v[120:123], v[176:179], v[80:83]\n" \
"buffer_load_dwordx4 v232, s[36:39], s80 offen offset:3072 sc1 lds\n" \
"v_mfma_f32_16x16x32_f16 v[92:95], v[124:127], v[176:179], v[92:95]\n" \
"s_waitcnt lgkmcnt(9)\n" \
"v_mfma_f32_16x16x32_f16 v[96:99], v[136:139], v[160:163], v[96:99]\n" \
"v_pk_add_f16 v136, v128, v152\n" \
"v_pk_add_f16 v137, v129, v153\n" \
"ds_read_b128 v[176:179], v240 offset:43008\n" \
"s_waitcnt lgkmcnt(3)\n" \
"v_mfma_f32_16x16x32_bf16 v[204:207], v[200:203], v[196:199], v[204:207]\n" \
"v_mfma_f32_16x16x32_f16 v[108:111], v[140:143], v[160:163], v[108:111]\n" \
"v_pk_add_f16 v160, v160, v164\n" \
"v_pk_add_f16 v161, v161, v165\n" \
"ds_read_b128 v[180:183], v240 offset:43520\n" \
"v_mfma_f32_16x16x32_f16 v[100:103], v[144:147], v[164:167], v[100:103]\n" \
"v_pk_add_f16 v162, v162, v166\n" \
"v_pk_add_f16 v163, v163, v167\n" \
"v_mfma_f32_16x16x32_f16 v[112:115], v[148:151], v[164:167], v[112:115]\n" \
"v_pk_add_f16 v138, v130, v154\n" \
"v_pk_add_f16 v139, v131, v155\n" \
"v_mfma_f32_16x16x32_f16 v[104:107], v[120:123], v[160:163], v[104:107]\n" \
"v_pk_add_f16 v140, v132, v156\n" \
"v_pk_add_f16 v141, v133, v157\n" \
"v_mfma_f32_16x16x32_f16 v[116:119], v[124:127], v[160:163], v[116:119]\n" \
"s_waitcnt lgkmcnt(2)\n" \
"v_pk_add_f16 v142, v134, v158\n" \
"v_pk_add_f16 v143, v135, v159\n" \
"s_waitcnt vmcnt(5) lgkmcnt(0)\n" \
"s_barrier\n" \
"v_mfma_f32_16x16x32_f16 v[0:3], v[128:131], v[168:171], v[0:3]\n" \
"ds_read_b128 v[160:163], v240 offset:4096\n" \
"v_mfma_f32_16x16x32_f16 v[12:15], v[132:135], v[168:171], v[12:15]\n" \
"v_pk_add_f16 v168, v168, v172\n" \
"v_pk_add_f16 v169, v169, v173\n" \
"v_mfma_f32_16x16x32_f16 v[4:7], v[152:155], v[172:175], v[4:7]\n" \
"v_pk_add_f16 v170, v170, v174\n" \
"v_pk_add_f16 v171, v171, v175\n" \
"v_cmp_eq_u32 vcc, 0, v253\n" \
"s_cbranch_vccnz .Ls011_%=\n" \
".Lr011_%=:\n" \
"v_mfma_f32_16x16x32_f16 v[16:19], v[156:159], v[172:175], v[16:19]\n" \
"ds_read_b128 v[164:167], v240 offset:4608\n" \
"s_add_i32 m0, s44, 0x8000\n" \
"s_add_i32 s80, s46, 0x6400000\n" \
"v_mfma_f32_16x16x32_f16 v[8:11], v[136:139], v[168:171], v[8:11]\n" \
"buffer_load_dwordx4 v232, s[36:39], s80 offen sc1 lds\n" \
"v_mfma_f32_16x16x32_f16 v[20:23], v[140:143], v[168:171], v[20:23]\n" \
"ds_read_b128 v[196:199], v249 offset:6144\n" \
"ds_read_b64_tr_b16 v[200:201], v247 offset:8192\n" \
"v_mfma_f32_16x16x32_f16 v[24:27], v[128:131], v[176:179], v[24:27]\n" \
"ds_read_b128 v[168:171], v240 offset:6144\n" \
"ds_read_b64_tr_b16 v[120:121], v241 offset:0\n" \
"ds_read_b64_tr_b16 v[122:123], v241 offset:8192\n" \
"v_mfma_f32_16x16x32_f16 v[36:39], v[132:135], v[176:179], v[36:39]\n" \
"v_pk_add_f16 v176, v176, v180\n" \
"v_pk_add_f16 v177, v177, v181\n" \
"v_mfma_f32_16x16x32_f16 v[28:31], v[152:155], v[180:183], v[28:31]\n" \
"v_pk_add_f16 v178, v178, v182\n" \
"v_pk_add_f16 v179, v179, v183\n" \
"v_mfma_f32_16x16x32_f16 v[40:43], v[156:159], v[180:183], v[40:43]\n" \
"ds_read_b128 v[172:175], v240 offset:6656\n" \
"ds_read_b64_tr_b16 v[202:203], v247 offset:8704\n" \
"v_mfma_f32_16x16x32_f16 v[32:35], v[136:139], v[176:179], v[32:35]\n" \
"buffer_load_dwordx4 v232, s[36:39], s80 offen offset:1024 sc1 lds\n" \
"ds_read_b64_tr_b16 v[124:125], v242 offset:0\n" \
"v_mfma_f32_16x16x32_f16 v[44:47], v[140:143], v[176:179], v[44:47]\n" \
"ds_read_b64_tr_b16 v[126:127], v242 offset:8192\n" \
"s_waitcnt lgkmcnt(9)\n" \
"v_mfma_f32_16x16x32_f16 v[48:51], v[128:131], v[160:163], v[48:51]\n" \
"ds_read_b128 v[176:179], v240 offset:8192\n" \
"ds_read_b64_tr_b16 v[144:145], v241 offset:512\n" \
"v_mfma_f32_16x16x32_f16 v[60:63], v[132:135], v[160:163], v[60:63]\n" \
"v_pk_add_f16 v160, v160, v164\n" \
"v_pk_add_f16 v161, v161, v165\n" \
"s_waitcnt lgkmcnt(4)\n" \
"v_mfma_f32_16x16x32_bf16 v[204:207], v[200:203], v[196:199], v[204:207]\n" \
"v_mfma_f32_16x16x32_f16 v[52:55], v[152:155], v[164:167], v[52:55]\n" \
"v_pk_add_f16 v162, v162, v166\n" \
"v_pk_add_f16 v163, v163, v167\n" \
"v_mfma_f32_16x16x32_f16 v[64:67], v[156:159], v[164:167], v[64:67]\n" \
"ds_read_b128 v[180:183], v240 offset:8704\n" \
"ds_read_b64_tr_b16 v[146:147], v241 offset:8704\n" \
"v_mfma_f32_16x16x32_f16 v[56:59], v[136:139], v[160:163], v[56:59]\n" \
"buffer_load_dwordx4 v232, s[36:39], s80 offen offset:2048 sc1 lds\n" \
"ds_read_b64_tr_b16 v[148:149], v242 offset:512\n" \
"v_mfma_f32_16x16x32_f16 v[68:71], v[140:143], v[160:163], v[68:71]\n" \
"ds_read_b128 v[196:199], v249 offset:7168\n" \
"ds_read_b64_tr_b16 v[200:201], v247 offset:12288\n" \
"v_mfma_f32_16x16x32_f16 v[72:75], v[128:131], v[168:171], v[72:75]\n" \
"ds_read_b128 v[160:163], v240 offset:0\n" \
"ds_read_b64_tr_b16 v[202:203], v247 offset:12800\n" \
"v_mfma_f32_16x16x32_f16 v[84:87], v[132:135], v[168:171], v[84:87]\n" \
"v_pk_add_f16 v168, v168, v172\n" \
"v_pk_add_f16 v169, v169, v173\n" \
"v_mfma_f32_16x16x32_f16 v[76:79], v[152:155], v[172:175], v[76:79]\n" \
"v_pk_add_f16 v170, v170, v174\n" \
"v_pk_add_f16 v171, v171, v175\n" \
"v_mfma_f32_16x16x32_f16 v[88:91], v[156:159], v[172:175], v[88:91]\n" \
"ds_read_b128 v[164:167], v240 offset:512\n" \
"ds_read_b64_tr_b16 v[150:151], v242 offset:8704\n" \
"v_mfma_f32_16x16x32_f16 v[80:83], v[136:139], v[168:171], v[80:83]\n" \
"buffer_load_dwordx4 v232, s[36:39], s80 offen offset:3072 sc1 lds\n" \
"v_mfma_f32_16x16x32_f16 v[92:95], v[140:143], v[168:171], v[92:95]\n" \
"s_waitcnt lgkmcnt(9)\n" \
"v_mfma_f32_16x16x32_f16 v[96:99], v[128:131], v[176:179], v[96:99]\n" \
"v_pk_add_f16 v128, v120, v144\n" \
"v_pk_add_f16 v129, v121, v145\n" \
"ds_read_b128 v[168:171], v240 offset:2048\n" \
"s_waitcnt lgkmcnt(3)\n" \
"v_mfma_f32_16x16x32_bf16 v[204:207], v[200:203], v[196:199], v[204:207]\n" \
"v_mfma_f32_16x16x32_f16 v[108:111], v[132:135], v[176:179], v[108:111]\n" \
"v_pk_add_f16 v176, v176, v180\n" \
"v_pk_add_f16 v177, v177, v181\n" \
"ds_read_b128 v[172:175], v240 offset:2560\n" \
"v_mfma_f32_16x16x32_f16 v[100:103], v[152:155], v[180:183], v[100:103]\n" \
"v_pk_add_f16 v178, v178, v182\n" \
"v_pk_add_f16 v179, v179, v183\n" \
"v_mfma_f32_16x16x32_f16 v[112:115], v[156:159], v[180:183], v[112:115]\n" \
"v_pk_add_f16 v130, v122, v146\n" \
"v_pk_add_f16 v131, v123, v147\n" \
"v_mfma_f32_16x16x32_f16 v[104:107], v[136:139], v[176:179], v[104:107]\n" \
"v_pk_add_f16 v132, v124, v148\n" \
"v_pk_add_f16 v133, v125, v149\n" \
"v_mfma_f32_16x16x32_f16 v[116:119], v[140:143], v[176:179], v[116:119]\n" \
"s_waitcnt lgkmcnt(2)\n" \
"v_pk_add_f16 v134, v126, v150\n" \
"v_pk_add_f16 v135, v127, v151\n" \
"s_waitcnt vmcnt(4) lgkmcnt(0)\n" \
"s_barrier\n" \
"v_mfma_f32_16x16x32_f16 v[0:3], v[120:123], v[160:163], v[0:3]\n" \
"ds_read_b128 v[176:179], v240 offset:24576\n" \
"s_mov_b32 s81, 0x8018000\n" \
"v_mfma_f32_16x16x32_f16 v[12:15], v[124:127], v[160:163], v[12:15]\n" \
"v_pk_add_f16 v160, v160, v164\n" \
"v_pk_add_f16 v161, v161, v165\n" \
"buffer_load_dword v253, v252, s[36:39], s81 offen sc1\n" \
"v_mfma_f32_16x16x32_f16 v[4:7], v[144:147], v[164:167], v[4:7]\n" \
"v_pk_add_f16 v162, v162, v166\n" \
"v_pk_add_f16 v163, v163, v167\n" \
"v_mfma_f32_16x16x32_f16 v[16:19], v[148:151], v[164:167], v[16:19]\n" \
"ds_read_b128 v[180:183], v240 offset:25088\n" \
"s_add_i32 m0, s44, 0x0\n" \
"s_add_i32 s80, s46, 0x6800000\n" \
"v_mfma_f32_16x16x32_f16 v[8:11], v[128:131], v[160:163], v[8:11]\n" \
"buffer_load_dwordx4 v232, s[36:39], s80 offen sc1 lds\n" \
"v_mfma_f32_16x16x32_f16 v[20:23], v[132:135], v[160:163], v[20:23]\n" \
"ds_write_b128 v248, v[204:207]\n" \
"ds_read_b64_tr_b16 v[136:137], v241 offset:16384\n" \
"v_mfma_f32_16x16x32_f16 v[24:27], v[120:123], v[168:171], v[24:27]\n" \
"ds_read_b128 v[160:163], v240 offset:26624\n" \
"ds_read_b64_tr_b16 v[138:139], v241 offset:24576\n" \
"v_mfma_f32_16x16x32_f16 v[36:39], v[124:127], v[168:171], v[36:39]\n" \
"v_pk_add_f16 v168, v168, v172\n" \
"v_pk_add_f16 v169, v169, v173\n" \
"v_mfma_f32_16x16x32_f16 v[28:31], v[144:147], v[172:175], v[28:31]\n" \
"v_pk_add_f16 v170, v170, v174\n" \
"v_pk_add_f16 v171, v171, v175\n" \
"v_mfma_f32_16x16x32_f16 v[40:43], v[148:151], v[172:175], v[40:43]\n" \
"ds_read_b128 v[164:167], v240 offset:27136\n" \
"ds_read_b64_tr_b16 v[140:141], v242 offset:16384\n" \
"v_mfma_f32_16x16x32_f16 v[32:35], v[128:131], v[168:171], v[32:35]\n" \
"buffer_load_dwordx4 v232, s[36:39], s80 offen offset:1024 sc1 lds\n" \
"v_mfma_f32_16x16x32_f16 v[44:47], v[132:135], v[168:171], v[44:47]\n" \
"ds_read_b64_tr_b16 v[142:143], v242 offset:24576\n" \
"s_waitcnt lgkmcnt(7)\n" \
"v_mfma_f32_16x16x32_f16 v[48:51], v[120:123], v[176:179], v[48:51]\n" \
"ds_read_b128 v[168:171], v240 offset:28672\n" \
"ds_read_b64_tr_b16 v[152:153], v241 offset:16896\n" \
"v_mfma_f32_16x16x32_f16 v[60:63], v[124:127], v[176:179], v[60:63]\n" \
"v_pk_add_f16 v176, v176, v180\n" \
"v_pk_add_f16 v177, v177, v181\n" \
"v_mfma_f32_16x16x32_f16 v[52:55], v[144:147], v[180:183], v[52:55]\n" \
"v_pk_add_f16 v178, v178, v182\n" \
"v_pk_add_f16 v179, v179, v183\n" \
"v_mfma_f32_16x16x32_f16 v[64:67], v[148:151], v[180:183], v[64:67]\n" \
"ds_read_b128 v[172:175], v240 offset:29184\n" \
"v_mfma_f32_16x16x32_f16 v[56:59], v[128:131], v[176:179], v[56:59]\n" \
"buffer_load_dwordx4 v232, s[36:39], s80 offen offset:2048 sc1 lds\n" \
"v_mfma_f32_16x16x32_f16 v[68:71], v[132:135], v[176:179], v[68:71]\n" \
"ds_read_b64_tr_b16 v[154:155], v241 offset:25088\n" \
"ds_read_b64_tr_b16 v[156:157], v242 offset:16896\n" \
"s_waitcnt lgkmcnt(8)\n" \
"v_mfma_f32_16x16x32_f16 v[72:75], v[120:123], v[160:163], v[72:75]\n" \
"ds_read_b128 v[176:179], v240 offset:20480\n" \
"ds_read_b64_tr_b16 v[158:159], v242 offset:25088\n" \
"v_mfma_f32_16x16x32_f16 v[84:87], v[124:127], v[160:163], v[84:87]\n" \
"s_waitcnt lgkmcnt(8)\n" \
"v_pk_add_f16 v160, v160, v164\n" \
"v_pk_add_f16 v161, v161, v165\n" \
"v_mfma_f32_16x16x32_f16 v[76:79], v[144:147], v[164:167], v[76:79]\n" \
"v_pk_add_f16 v162, v162, v166\n" \
"v_pk_add_f16 v163, v163, v167\n" \
"v_mfma_f32_16x16x32_f16 v[88:91], v[148:151], v[164:167], v[88:91]\n" \
"ds_read_b128 v[180:183], v240 offset:20992\n" \
"v_mfma_f32_16x16x32_f16 v[80:83], v[128:131], v[160:163], v[80:83]\n" \
"buffer_load_dwordx4 v232, s[36:39], s80 offen offset:3072 sc1 lds\n" \
"v_mfma_f32_16x16x32_f16 v[92:95], v[132:135], v[160:163], v[92:95]\n" \
"s_waitcnt lgkmcnt(6)\n" \
"v_mfma_f32_16x16x32_f16 v[96:99], v[120:123], v[168:171], v[96:99]\n" \
"v_pk_add_f16 v120, v136, v152\n" \
"v_pk_add_f16 v121, v137, v153\n" \
"ds_read_b128 v[160:163], v240 offset:22528\n" \
"v_mfma_f32_16x16x32_f16 v[108:111], v[124:127], v[168:171], v[108:111]\n" \
"s_waitcnt lgkmcnt(5)\n" \
"v_pk_add_f16 v168, v168, v172\n" \
"v_pk_add_f16 v169, v169, v173\n" \
"ds_read_b128 v[164:167], v240 offset:23040\n" \
"v_mfma_f32_16x16x32_f16 v[100:103], v[144:147], v[172:175], v[100:103]\n" \
"v_pk_add_f16 v170, v170, v174\n" \
"v_pk_add_f16 v171, v171, v175\n" \
"v_mfma_f32_16x16x32_f16 v[112:115], v[148:151], v[172:175], v[112:115]\n" \
"v_pk_add_f16 v122, v138, v154\n" \
"v_pk_add_f16 v123, v139, v155\n" \
"v_mfma_f32_16x16x32_f16 v[104:107], v[128:131], v[168:171], v[104:107]\n" \
"s_waitcnt lgkmcnt(3)\n" \
"v_pk_add_f16 v124, v140, v156\n" \
"v_pk_add_f16 v125, v141, v157\n" \
"v_mfma_f32_16x16x32_f16 v[116:119], v[132:135], v[168:171], v[116:119]\n" \
"v_pk_add_f16 v126, v142, v158\n" \
"v_pk_add_f16 v127, v143, v159\n" \
".Lcf_t:\n" \
"s_waitcnt vmcnt(4) lgkmcnt(0)\n" \
"s_barrier\n" \
"v_mfma_f32_16x16x32_f16 v[0:3], v[136:139], v[176:179], v[0:3]\n" \
"ds_read_b128 v[168:171], v240 offset:45056\n" \
"v_mfma_f32_16x16x32_f16 v[12:15], v[140:143], v[176:179], v[12:15]\n" \
"v_pk_add_f16 v176, v176, v180\n" \
"v_pk_add_f16 v177, v177, v181\n" \
"v_mfma_f32_16x16x32_f16 v[4:7], v[152:155], v[180:183], v[4:7]\n" \
"v_pk_add_f16 v178, v178, v182\n" \
"v_pk_add_f16 v179, v179, v183\n" \
"v_cmp_eq_u32 vcc, 0, v253\n" \
"s_cbranch_vccnz .Ls012_%=\n" \
".Lr012_%=:\n" \
"s_mov_b32 s81, 0x801a000\n" \
"v_mfma_f32_16x16x32_f16 v[16:19], v[156:159], v[180:183], v[16:19]\n" \
"ds_read_b128 v[172:175], v240 offset:45568\n" \
"buffer_load_dword v253, v252, s[36:39], s81 offen sc1\n" \
"s_add_i32 m0, s44, 0x4000\n" \
"s_add_i32 s80, s46, 0x6c00000\n" \
"v_mfma_f32_16x16x32_f16 v[8:11], v[120:123], v[176:179], v[8:11]\n" \
"buffer_load_dwordx4 v232, s[36:39], s80 offen sc1 lds\n" \
"v_mfma_f32_16x16x32_f16 v[20:23], v[124:127], v[176:179], v[20:23]\n" \
"ds_read_b64_tr_b16 v[128:129], v241 offset:32768\n" \
"ds_read_b64_tr_b16 v[130:131], v241 offset:40960\n" \
"v_mfma_f32_16x16x32_f16 v[24:27], v[136:139], v[160:163], v[24:27]\n" \
"ds_read_b128 v[176:179], v240 offset:47104\n" \
"v_mfma_f32_16x16x32_f16 v[36:39], v[140:143], v[160:163], v[36:39]\n" \
"v_pk_add_f16 v160, v160, v164\n" \
"v_pk_add_f16 v161, v161, v165\n" \
"v_mfma_f32_16x16x32_f16 v[28:31], v[152:155], v[164:167], v[28:31]\n" \
"v_pk_add_f16 v162, v162, v166\n" \
"v_pk_add_f16 v163, v163, v167\n" \
"v_mfma_f32_16x16x32_f16 v[40:43], v[156:159], v[164:167], v[40:43]\n" \
"ds_read_b128 v[180:183], v240 offset:47616\n" \
"ds_read_b64_tr_b16 v[132:133], v242 offset:32768\n" \
"v_mfma_f32_16x16x32_f16 v[32:35], v[120:123], v[160:163], v[32:35]\n" \
"buffer_load_dwordx4 v232, s[36:39], s80 offen offset:1024 sc1 lds\n" \
"v_mfma_f32_16x16x32_f16 v[44:47], v[124:127], v[160:163], v[44:47]\n" \
"ds_read_b64_tr_b16 v[134:135], v242 offset:40960\n" \
"s_waitcnt lgkmcnt(6)\n" \
"v_mfma_f32_16x16x32_f16 v[48:51], v[136:139], v[168:171], v[48:51]\n" \
"ds_read_b128 v[160:163], v240 offset:49152\n" \
"ds_read_b64_tr_b16 v[144:145], v241 offset:33280\n" \
"v_mfma_f32_16x16x32_f16 v[60:63], v[140:143], v[168:171], v[60:63]\n" \
"v_pk_add_f16 v168, v168, v172\n" \
"v_pk_add_f16 v169, v169, v173\n" \
"v_mfma_f32_16x16x32_f16 v[52:55], v[152:155], v[172:175], v[52:55]\n" \
"v_pk_add_f16 v170, v170, v174\n" \
"v_pk_add_f16 v171, v171, v175\n" \
"v_mfma_f32_16x16x32_f16 v[64:67], v[156:159], v[172:175], v[64:67]\n" \
"ds_read_b128 v[164:167], v240 offset:49664\n" \
"v_mfma_f32_16x16x32_f16 v[56:59], v[120:123], v[168:171], v[56:59]\n" \
"buffer_load_dwordx4 v232, s[36:39], s80 offen offset:2048 sc1 lds\n" \
"v_mfma_f32_16x16x32_f16 v[68:71], v[124:127], v[168:171], v[68:71]\n" \
"ds_read_b64_tr_b16 v[146:147], v241 offset:41472\n" \
"ds_read_b64_tr_b16 v[148:149], v242 offset:33280\n" \
"s_waitcnt lgkmcnt(8)\n" \
"v_mfma_f32_16x16x32_f16 v[72:75], v[136:139], v[176:179], v[72:75]\n" \
"ds_read_b128 v[168:171], v240 offset:40960\n" \
"ds_read_b64_tr_b16 v[150:151], v242 offset:41472\n" \
"v_mfma_f32_16x16x32_f16 v[84:87], v[140:143], v[176:179], v[84:87]\n" \
"s_waitcnt lgkmcnt(8)\n" \
"v_pk_add_f16 v176, v176, v180\n" \
"v_pk_add_f16 v177, v177, v181\n" \
"v_mfma_f32_16x16x32_f16 v[76:79], v[152:155], v[180:183], v[76:79]\n" \
"v_pk_add_f16 v178, v178, v182\n" \
"v_pk_add_f16 v179, v179, v183\n" \
"v_mfma_f32_16x16x32_f16 v[88:91], v[156:159], v[180:183], v[88:91]\n" \
"ds_read_b128 v[172:175], v240 offset:41472\n" \
"v_mfma_f32_16x16x32_f16 v[80:83], v[120:123], v[176:179], v[80:83]\n" \
"buffer_load_dwordx4 v232, s[36:39], s80 offen offset:3072 sc1 lds\n" \
"v_mfma_f32_16x16x32_f16 v[92:95], v[124:127], v[176:179], v[92:95]\n" \
"s_waitcnt lgkmcnt(6)\n" \
"v_mfma_f32_16x16x32_f16 v[96:99], v[136:139], v[160:163], v[96:99]\n" \
"v_pk_add_f16 v136, v128, v144\n" \
"v_pk_add_f16 v137, v129, v145\n" \
"ds_read_b128 v[176:179], v240 offset:43008\n" \
"v_mfma_f32_16x16x32_f16 v[108:111], v[140:143], v[160:163], v[108:111]\n" \
"s_waitcnt lgkmcnt(5)\n" \
"v_pk_add_f16 v160, v160, v164\n" \
"v_pk_add_f16 v161, v161, v165\n" \
"ds_read_b128 v[180:183], v240 offset:43520\n" \
"v_mfma_f32_16x16x32_f16 v[100:103], v[152:155], v[164:167], v[100:103]\n" \
"v_pk_add_f16 v162, v162, v166\n" \
"v_pk_add_f16 v163, v163, v167\n" \
"v_mfma_f32_16x16x32_f16 v[112:115], v[156:159], v[164:167], v[112:115]\n" \
"v_pk_add_f16 v138, v130, v146\n" \
"v_pk_add_f16 v139, v131, v147\n" \
"v_mfma_f32_16x16x32_f16 v[104:107], v[120:123], v[160:163], v[104:107]\n" \
"s_waitcnt lgkmcnt(3)\n" \
"v_pk_add_f16 v140, v132, v148\n" \
"v_pk_add_f16 v141, v133, v149\n" \
"v_mfma_f32_16x16x32_f16 v[116:119], v[124:127], v[160:163], v[116:119]\n" \
"v_pk_add_f16 v142, v134, v150\n" \
"v_pk_add_f16 v143, v135, v151\n" \
"s_waitcnt vmcnt(4) lgkmcnt(0)\n" \
"s_barrier\n" \
"v_mfma_f32_16x16x32_f16 v[0:3], v[128:131], v[168:171], v[0:3]\n" \
"ds_read_b128 v[160:163], v240 offset:4096\n" \
"v_mfma_f32_16x16x32_f16 v[12:15], v[132:135], v[168:171], v[12:15]\n" \
"v_pk_add_f16 v168, v168, v172\n" \
"v_pk_add_f16 v169, v169, v173\n" \
"s_add_i32 m0, s44, 0x8000\n" \
"s_add_i32 s80, s46, 0x7000000\n" \
"v_mfma_f32_16x16x32_f16 v[4:7], v[144:147], v[172:175], v[4:7]\n" \
"v_pk_add_f16 v170, v170, v174\n" \
"v_pk_add_f16 v171, v171, v175\n" \
"buffer_load_dwordx4 v232, s[36:39], s80 offen sc1 lds\n" \
"v_mfma_f32_16x16x32_f16 v[16:19], v[148:151], v[172:175], v[16:19]\n" \
"ds_read_b128 v[164:167], v240 offset:4608\n" \
"v_mfma_f32_16x16x32_f16 v[8:11], v[136:139], v[168:171], v[8:11]\n" \
"buffer_load_dwordx4 v232, s[36:39], s80 offen offset:1024 sc1 lds\n" \
"v_mfma_f32_16x16x32_f16 v[20:23], v[140:143], v[168:171], v[20:23]\n" \
"ds_read_b64_tr_b16 v[120:121], v241 offset:0\n" \
"ds_read_b64_tr_b16 v[122:123], v241 offset:8192\n" \
"v_mfma_f32_16x16x32_f16 v[24:27], v[128:131], v[176:179], v[24:27]\n" \
"ds_read_b128 v[168:171], v240 offset:6144\n" \
"buffer_load_dwordx4 v232, s[36:39], s80 offen offset:2048 sc1 lds\n" \
"v_mfma_f32_16x16x32_f16 v[36:39], v[132:135], v[176:179], v[36:39]\n" \
"v_pk_add_f16 v176, v176, v180\n" \
"v_pk_add_f16 v177, v177, v181\n" \
"ds_read_b64_tr_b16 v[124:125], v242 offset:0\n" \
"v_mfma_f32_16x16x32_f16 v[28:31], v[144:147], v[180:183], v[28:31]\n" \
"v_pk_add_f16 v178, v178, v182\n" \
"v_pk_add_f16 v179, v179, v183\n" \
"v_mfma_f32_16x16x32_f16 v[40:43], v[148:151], v[180:183], v[40:43]\n" \
"ds_read_b128 v[172:175], v240 offset:6656\n" \
"buffer_load_dwordx4 v232, s[36:39], s80 offen offset:3072 sc1 lds\n" \
"v_mfma_f32_16x16x32_f16 v[32:35], v[136:139], v[176:179], v[32:35]\n" \
"v_cmp_eq_u32 vcc, 0, v253\n" \
"s_cbranch_vccnz .Ls013_%=\n" \
".Lr013_%=:\n" \
"v_mfma_f32_16x16x32_f16 v[44:47], v[140:143], v[176:179], v[44:47]\n" \
"s_add_i32 m0, s44, 0x1b000\n" \
"s_add_i32 s80, s46, 0x7400000\n" \
"ds_read_b64_tr_b16 v[126:127], v242 offset:8192\n" \
"s_waitcnt lgkmcnt(6)\n" \
"v_mfma_f32_16x16x32_f16 v[48:51], v[128:131], v[160:163], v[48:51]\n" \
"ds_read_b128 v[176:179], v240 offset:8192\n" \
"buffer_load_dwordx4 v232, s[36:39], s80 offen sc1 lds\n" \
"v_mfma_f32_16x16x32_f16 v[60:63], v[132:135], v[160:163], v[60:63]\n" \
"v_pk_add_f16 v160, v160, v164\n" \
"v_pk_add_f16 v161, v161, v165\n" \
"v_mfma_f32_16x16x32_f16 v[52:55], v[144:147], v[164:167], v[52:55]\n" \
"v_pk_add_f16 v162, v162, v166\n" \
"v_pk_add_f16 v163, v163, v167\n" \
"buffer_load_dwordx4 v232, s[36:39], s80 offen offset:1024 sc1 lds\n" \
"v_mfma_f32_16x16x32_f16 v[64:67], v[148:151], v[164:167], v[64:67]\n" \
"ds_read_b128 v[180:183], v240 offset:8704\n" \
"ds_read_b64_tr_b16 v[152:153], v241 offset:512\n" \
"v_mfma_f32_16x16x32_f16 v[56:59], v[136:139], v[160:163], v[56:59]\n" \
"buffer_load_dwordx4 v232, s[36:39], s80 offen offset:2048 sc1 lds\n" \
"v_mfma_f32_16x16x32_f16 v[68:71], v[140:143], v[160:163], v[68:71]\n" \
"ds_read_b64_tr_b16 v[154:155], v241 offset:8704\n" \
"ds_read_b64_tr_b16 v[156:157], v242 offset:512\n" \
"s_waitcnt lgkmcnt(6)\n" \
"v_mfma_f32_16x16x32_f16 v[72:75], v[128:131], v[168:171], v[72:75]\n" \
"ds_read_b128 v[160:163], v240 offset:0\n" \
"buffer_load_dwordx4 v232, s[36:39], s80 offen offset:3072 sc1 lds\n" \
"v_mfma_f32_16x16x32_f16 v[84:87], v[132:135], v[168:171], v[84:87]\n" \
"v_pk_add_f16 v168, v168, v172\n" \
"v_pk_add_f16 v169, v169, v173\n" \
"ds_read_b64_tr_b16 v[158:159], v242 offset:8704\n" \
"v_mfma_f32_16x16x32_f16 v[76:79], v[144:147], v[172:175], v[76:79]\n" \
"v_pk_add_f16 v170, v170, v174\n" \
"v_pk_add_f16 v171, v171, v175\n" \
"s_add_i32 m0, s44, 0x1f000\n" \
"s_add_i32 s80, s46, 0x7800000\n" \
"v_mfma_f32_16x16x32_f16 v[88:91], v[148:151], v[172:175], v[88:91]\n" \
"ds_read_b128 v[164:167], v240 offset:512\n" \
"buffer_load_dwordx4 v232, s[36:39], s80 offen sc1 lds\n" \
"v_mfma_f32_16x16x32_f16 v[80:83], v[136:139], v[168:171], v[80:83]\n" \
"v_mfma_f32_16x16x32_f16 v[92:95], v[140:143], v[168:171], v[92:95]\n" \
"buffer_load_dwordx4 v232, s[36:39], s80 offen offset:1024 sc1 lds\n" \
"s_waitcnt lgkmcnt(5)\n" \
"v_mfma_f32_16x16x32_f16 v[96:99], v[128:131], v[176:179], v[96:99]\n" \
"v_pk_add_f16 v128, v120, v152\n" \
"v_pk_add_f16 v129, v121, v153\n" \
"ds_read_b128 v[168:171], v240 offset:2048\n" \
"v_mfma_f32_16x16x32_f16 v[108:111], v[132:135], v[176:179], v[108:111]\n" \
"v_pk_add_f16 v176, v176, v180\n" \
"v_pk_add_f16 v177, v177, v181\n" \
"ds_read_b128 v[172:175], v240 offset:2560\n" \
"buffer_load_dwordx4 v232, s[36:39], s80 offen offset:2048 sc1 lds\n" \
"v_mfma_f32_16x16x32_f16 v[100:103], v[144:147], v[180:183], v[100:103]\n" \
"v_pk_add_f16 v178, v178, v182\n" \
"v_pk_add_f16 v179, v179, v183\n" \
"v_mfma_f32_16x16x32_f16 v[112:115], v[148:151], v[180:183], v[112:115]\n" \
"s_waitcnt lgkmcnt(3)\n" \
"v_pk_add_f16 v130, v122, v154\n" \
"v_pk_add_f16 v131, v123, v155\n" \
"buffer_load_dwordx4 v232, s[36:39], s80 offen offset:3072 sc1 lds\n" \
"v_mfma_f32_16x16x32_f16 v[104:107], v[136:139], v[176:179], v[104:107]\n" \
"v_pk_add_f16 v132, v124, v156\n" \
"v_pk_add_f16 v133, v125, v157\n" \
"v_mfma_f32_16x16x32_f16 v[116:119], v[140:143], v[176:179], v[116:119]\n" \
"v_pk_add_f16 v134, v126, v158\n" \
"v_pk_add_f16 v135, v127, v159\n" \
"s_waitcnt vmcnt(12) lgkmcnt(0)\n" \
"s_barrier\n" \
"v_mfma_f32_16x16x32_f16 v[0:3], v[120:123], v[160:163], v[0:3]\n" \
"ds_read_b128 v[176:179], v240 offset:24576\n" \
"v_mfma_f32_16x16x32_f16 v[12:15], v[124:127], v[160:163], v[12:15]\n" \
"v_pk_add_f16 v160, v160, v164\n" \
"v_pk_add_f16 v161, v161, v165\n" \
"v_mfma_f32_16x16x32_f16 v[4:7], v[152:155], v[164:167], v[4:7]\n" \
"v_pk_add_f16 v162, v162, v166\n" \
"v_pk_add_f16 v163, v163, v167\n" \
"v_add_u32 v252, s44, v244\n" \
"v_mfma_f32_16x16x32_f16 v[16:19], v[156:159], v[164:167], v[16:19]\n" \
"ds_read_b128 v[180:183], v240 offset:25088\n" \
"s_mov_b32 m0, s44\n" \
"v_mfma_f32_16x16x32_f16 v[8:11], v[128:131], v[160:163], v[8:11]\n" \
"global_load_lds_dwordx4 v252, s[76:77]\n" \
"v_mfma_f32_16x16x32_f16 v[20:23], v[132:135], v[160:163], v[20:23]\n" \
"ds_read_b64_tr_b16 v[136:137], v241 offset:16384\n" \
"ds_read_b64_tr_b16 v[138:139], v241 offset:24576\n" \
"v_mfma_f32_16x16x32_f16 v[24:27], v[120:123], v[168:171], v[24:27]\n" \
"ds_read_b128 v[160:163], v240 offset:26624\n" \
"v_mfma_f32_16x16x32_f16 v[36:39], v[124:127], v[168:171], v[36:39]\n" \
"v_pk_add_f16 v168, v168, v172\n" \
"v_pk_add_f16 v169, v169, v173\n" \
"v_mfma_f32_16x16x32_f16 v[28:31], v[152:155], v[172:175], v[28:31]\n" \
"v_pk_add_f16 v170, v170, v174\n" \
"v_pk_add_f16 v171, v171, v175\n" \
"v_mfma_f32_16x16x32_f16 v[40:43], v[156:159], v[172:175], v[40:43]\n" \
"ds_read_b128 v[164:167], v240 offset:27136\n" \
"ds_read_b64_tr_b16 v[140:141], v242 offset:16384\n" \
"v_mfma_f32_16x16x32_f16 v[32:35], v[128:131], v[168:171], v[32:35]\n" \
"global_load_lds_dwordx4 v252, s[76:77] offset:1024\n" \
"v_mfma_f32_16x16x32_f16 v[44:47], v[132:135], v[168:171], v[44:47]\n" \
"ds_read_b64_tr_b16 v[142:143], v242 offset:24576\n" \
"s_waitcnt lgkmcnt(7)\n" \
"v_mfma_f32_16x16x32_f16 v[48:51], v[120:123], v[176:179], v[48:51]\n" \
"ds_read_b128 v[168:171], v240 offset:28672\n" \
"ds_read_b64_tr_b16 v[144:145], v241 offset:16896\n" \
"v_mfma_f32_16x16x32_f16 v[60:63], v[124:127], v[176:179], v[60:63]\n" \
"s_waitcnt lgkmcnt(8)\n" \
"v_pk_add_f16 v176, v176, v180\n" \
"v_pk_add_f16 v177, v177, v181\n" \
"v_mfma_f32_16x16x32_f16 v[52:55], v[152:155], v[180:183], v[52:55]\n" \
"v_pk_add_f16 v178, v178, v182\n" \
"v_pk_add_f16 v179, v179, v183\n" \
"v_mfma_f32_16x16x32_f16 v[64:67], v[156:159], v[180:183], v[64:67]\n" \
"ds_read_b128 v[172:175], v240 offset:29184\n" \
"v_mfma_f32_16x16x32_f16 v[56:59], v[128:131], v[176:179], v[56:59]\n" \
"global_load_lds_dwordx4 v252, s[76:77] offset:2048\n" \
"v_mfma_f32_16x16x32_f16 v[68:71], v[132:135], v[176:179], v[68:71]\n" \
"ds_read_b64_tr_b16 v[146:147], v241 offset:25088\n" \
"ds_read_b64_tr_b16 v[148:149], v242 offset:16896\n" \
"s_waitcnt lgkmcnt(7)\n" \
"v_mfma_f32_16x16x32_f16 v[72:75], v[120:123], v[160:163], v[72:75]\n" \
"ds_read_b128 v[176:179], v240 offset:20480\n" \
"ds_read_b64_tr_b16 v[150:151], v242 offset:25088\n" \
"v_mfma_f32_16x16x32_f16 v[84:87], v[124:127], v[160:163], v[84:87]\n" \
"v_pk_add_f16 v160, v160, v164\n" \
"v_pk_add_f16 v161, v161, v165\n" \
"v_mfma_f32_16x16x32_f16 v[76:79], v[152:155], v[164:167], v[76:79]\n" \
"v_pk_add_f16 v162, v162, v166\n" \
"v_pk_add_f16 v163, v163, v167\n" \
"v_add_u32 v243, 0x1b000, v241\n" \
"v_mfma_f32_16x16x32_f16 v[88:91], v[156:159], v[164:167], v[88:91]\n" \
"v_xor_b32 v244, 32, v243\n" \
"ds_read_b128 v[180:183], v240 offset:20992\n" \
"v_mfma_f32_16x16x32_f16 v[80:83], v[128:131], v[160:163], v[80:83]\n" \
"global_load_lds_dwordx4 v252, s[76:77] offset:3072\n" \
"v_mfma_f32_16x16x32_f16 v[92:95], v[132:135], v[160:163], v[92:95]\n" \
"v_add_u32 v246, 0x17000, v240\n" \
"s_waitcnt lgkmcnt(5)\n" \
"v_mfma_f32_16x16x32_f16 v[96:99], v[120:123], v[168:171], v[96:99]\n" \
"v_pk_add_f16 v120, v136, v144\n" \
"v_pk_add_f16 v121, v137, v145\n" \
"ds_read_b128 v[160:163], v240 offset:22528\n" \
"v_mfma_f32_16x16x32_f16 v[108:111], v[124:127], v[168:171], v[108:111]\n" \
"v_pk_add_f16 v168, v168, v172\n" \
"v_pk_add_f16 v169, v169, v173\n" \
"ds_read_b128 v[164:167], v240 offset:23040\n" \
"v_mfma_f32_16x16x32_f16 v[100:103], v[152:155], v[172:175], v[100:103]\n" \
"v_pk_add_f16 v170, v170, v174\n" \
"v_pk_add_f16 v171, v171, v175\n" \
"v_mfma_f32_16x16x32_f16 v[112:115], v[156:159], v[172:175], v[112:115]\n" \
"s_waitcnt lgkmcnt(3)\n" \
"v_pk_add_f16 v122, v138, v146\n" \
"v_pk_add_f16 v123, v139, v147\n" \
"v_mfma_f32_16x16x32_f16 v[104:107], v[128:131], v[168:171], v[104:107]\n" \
"v_pk_add_f16 v124, v140, v148\n" \
"v_pk_add_f16 v125, v141, v149\n" \
"v_mfma_f32_16x16x32_f16 v[116:119], v[132:135], v[168:171], v[116:119]\n" \
"v_pk_add_f16 v126, v142, v150\n" \
"v_pk_add_f16 v127, v143, v151\n" \
"s_waitcnt vmcnt(4) lgkmcnt(0)\n" \
"s_barrier\n" \
"v_mfma_f32_16x16x32_f16 v[0:3], v[136:139], v[176:179], v[0:3]\n" \
"ds_read_b128 v[168:171], v240 offset:40960\n" \
"ds_read_b64_tr_b16 v[128:129], v241 offset:32768\n" \
"ds_read_b64_tr_b16 v[130:131], v241 offset:40960\n" \
"v_mfma_f32_16x16x32_f16 v[12:15], v[140:143], v[176:179], v[12:15]\n" \
"v_pk_add_f16 v176, v176, v180\n" \
"v_pk_add_f16 v177, v177, v181\n" \
"ds_read_b64_tr_b16 v[132:133], v242 offset:32768\n" \
"v_mfma_f32_16x16x32_f16 v[4:7], v[144:147], v[180:183], v[4:7]\n" \
"v_pk_add_f16 v178, v178, v182\n" \
"v_pk_add_f16 v179, v179, v183\n" \
"v_mfma_f32_16x16x32_f16 v[16:19], v[148:151], v[180:183], v[16:19]\n" \
"ds_read_b128 v[172:175], v240 offset:41472\n" \
"ds_read_b64_tr_b16 v[134:135], v242 offset:40960\n" \
"ds_read_b64_tr_b16 v[152:153], v241 offset:33280\n" \
"v_mfma_f32_16x16x32_f16 v[8:11], v[120:123], v[176:179], v[8:11]\n" \
"ds_read_b64_tr_b16 v[154:155], v241 offset:41472\n" \
"v_mfma_f32_16x16x32_f16 v[20:23], v[124:127], v[176:179], v[20:23]\n" \
"ds_read_b64_tr_b16 v[156:157], v242 offset:33280\n" \
"ds_read_b64_tr_b16 v[158:159], v242 offset:41472\n" \
"v_mfma_f32_16x16x32_f16 v[24:27], v[136:139], v[160:163], v[24:27]\n" \
"ds_read_b128 v[176:179], v246 offset:0\n" \
"ds_read_b64_tr_b16 v[184:185], v243 offset:0\n" \
"ds_read_b64_tr_b16 v[186:187], v243 offset:8192\n" \
"v_mfma_f32_16x16x32_f16 v[36:39], v[140:143], v[160:163], v[36:39]\n" \
"v_pk_add_f16 v160, v160, v164\n" \
"v_pk_add_f16 v161, v161, v165\n" \
"ds_read_b64_tr_b16 v[188:189], v244 offset:0\n" \
"v_mfma_f32_16x16x32_f16 v[28:31], v[144:147], v[164:167], v[28:31]\n" \
"v_pk_add_f16 v162, v162, v166\n" \
"v_pk_add_f16 v163, v163, v167\n" \
"v_mfma_f32_16x16x32_f16 v[40:43], v[148:151], v[164:167], v[40:43]\n" \
"ds_read_b128 v[180:183], v246 offset:512\n" \
"ds_read_b64_tr_b16 v[190:191], v244 offset:8192\n" \
"ds_read_b64_tr_b16 v[192:193], v243 offset:512\n" \
"v_mfma_f32_16x16x32_f16 v[32:35], v[120:123], v[160:163], v[32:35]\n" \
"ds_read_b64_tr_b16 v[194:195], v243 offset:8704\n" \
"v_mfma_f32_16x16x32_f16 v[44:47], v[124:127], v[160:163], v[44:47]\n" \
"s_waitcnt lgkmcnt(11)\n" \
"v_pk_add_f16 v232, v128, v152\n" \
"v_pk_add_f16 v233, v129, v153\n" \
"ds_read_b64_tr_b16 v[196:197], v244 offset:512\n" \
"ds_read_b64_tr_b16 v[198:199], v244 offset:8704\n" \
"v_mfma_f32_16x16x32_f16 v[0:3], v[128:131], v[168:171], v[0:3]\n" \
"s_waitcnt lgkmcnt(10)\n" \
"v_pk_add_f16 v234, v130, v154\n" \
"ds_read_b128 v[160:163], v246 offset:8192\n" \
"ds_read_b64_tr_b16 v[208:209], v243 offset:16384\n" \
"ds_read_b64_tr_b16 v[210:211], v243 offset:24576\n" \
"v_mfma_f32_16x16x32_f16 v[12:15], v[132:135], v[168:171], v[12:15]\n" \
"v_pk_add_f16 v168, v168, v172\n" \
"v_pk_add_f16 v169, v169, v173\n" \
"v_pk_add_f16 v235, v131, v155\n" \
"v_pk_add_f16 v236, v132, v156\n" \
"ds_read_b64_tr_b16 v[212:213], v244 offset:16384\n" \
"v_mfma_f32_16x16x32_f16 v[4:7], v[152:155], v[172:175], v[4:7]\n" \
"v_pk_add_f16 v170, v170, v174\n" \
"v_pk_add_f16 v171, v171, v175\n" \
"v_pk_add_f16 v237, v133, v157\n" \
"v_pk_add_f16 v238, v134, v158\n" \
"v_pk_add_f16 v239, v135, v159\n" \
"v_mfma_f32_16x16x32_f16 v[16:19], v[156:159], v[172:175], v[16:19]\n" \
"s_waitcnt lgkmcnt(6)\n" \
"v_pk_add_f16 v200, v184, v192\n" \
"ds_read_b128 v[164:167], v246 offset:8704\n" \
"ds_read_b64_tr_b16 v[214:215], v244 offset:24576\n" \
"ds_read_b64_tr_b16 v[216:217], v243 offset:16896\n" \
"v_mfma_f32_16x16x32_f16 v[8:11], v[232:235], v[168:171], v[8:11]\n" \
"v_pk_add_f16 v201, v185, v193\n" \
"v_pk_add_f16 v202, v186, v194\n" \
"ds_read_b64_tr_b16 v[218:219], v243 offset:25088\n" \
"v_mfma_f32_16x16x32_f16 v[20:23], v[236:239], v[168:171], v[20:23]\n" \
"ds_read_b64_tr_b16 v[220:221], v244 offset:16896\n" \
"ds_read_b64_tr_b16 v[222:223], v244 offset:25088\n" \
"v_mfma_f32_16x16x32_f16 v[0:3], v[184:187], v[176:179], v[0:3]\n" \
"v_pk_add_f16 v203, v187, v195\n" \
"s_waitcnt lgkmcnt(7)\n" \
"v_pk_add_f16 v204, v188, v196\n" \
"ds_read_b128 v[168:171], v240 offset:43008\n" \
"v_mfma_f32_16x16x32_f16 v[12:15], v[188:191], v[176:179], v[12:15]\n" \
"v_pk_add_f16 v176, v176, v180\n" \
"v_pk_add_f16 v177, v177, v181\n" \
"v_pk_add_f16 v205, v189, v197\n" \
"v_mfma_f32_16x16x32_f16 v[4:7], v[192:195], v[180:183], v[4:7]\n" \
"v_pk_add_f16 v178, v178, v182\n" \
"v_pk_add_f16 v179, v179, v183\n" \
"v_pk_add_f16 v206, v190, v198\n" \
"v_pk_add_f16 v207, v191, v199\n" \
"v_mfma_f32_16x16x32_f16 v[16:19], v[196:199], v[180:183], v[16:19]\n" \
"s_waitcnt lgkmcnt(4)\n" \
"v_pk_add_f16 v224, v208, v216\n" \
"ds_read_b128 v[172:175], v240 offset:43520\n" \
"v_mfma_f32_16x16x32_f16 v[8:11], v[200:203], v[176:179], v[8:11]\n" \
"v_pk_add_f16 v225, v209, v217\n" \
"v_mfma_f32_16x16x32_f16 v[20:23], v[204:207], v[176:179], v[20:23]\n" \
"s_waitcnt lgkmcnt(4)\n" \
"v_pk_add_f16 v226, v210, v218\n" \
"v_pk_add_f16 v227, v211, v219\n" \
"v_mfma_f32_16x16x32_f16 v[0:3], v[208:211], v[160:163], v[0:3]\n" \
"s_waitcnt lgkmcnt(2)\n" \
"v_pk_add_f16 v228, v212, v220\n" \
"v_pk_add_f16 v229, v213, v221\n" \
"ds_read_b128 v[176:179], v246 offset:2048\n" \
"v_mfma_f32_16x16x32_f16 v[12:15], v[212:215], v[160:163], v[12:15]\n" \
"v_pk_add_f16 v160, v160, v164\n" \
"v_pk_add_f16 v161, v161, v165\n" \
"v_pk_add_f16 v230, v214, v222\n" \
"v_mfma_f32_16x16x32_f16 v[4:7], v[216:219], v[164:167], v[4:7]\n" \
"v_pk_add_f16 v162, v162, v166\n" \
"v_pk_add_f16 v163, v163, v167\n" \
"v_pk_add_f16 v231, v215, v223\n" \
"v_mfma_f32_16x16x32_f16 v[16:19], v[220:223], v[164:167], v[16:19]\n" \
"ds_read_b128 v[180:183], v246 offset:2560\n" \
"s_mov_b32 vcc_lo, 0xaaaaaaaa\n" \
"v_mfma_f32_16x16x32_f16 v[8:11], v[224:227], v[160:163], v[8:11]\n" \
"s_mov_b32 vcc_hi, 0xaaaaaaaa\n" \
"v_mfma_f32_16x16x32_f16 v[20:23], v[228:231], v[160:163], v[20:23]\n" \
"v_add_u32 v241, 0xffff7000, v240\n" \
"s_waitcnt lgkmcnt(2)\n" \
"v_mfma_f32_16x16x32_f16 v[24:27], v[128:131], v[168:171], v[24:27]\n" \
"ds_read_b128 v[160:163], v246 offset:10240\n" \
"v_mfma_f32_16x16x32_f16 v[36:39], v[132:135], v[168:171], v[36:39]\n" \
"v_pk_add_f16 v168, v168, v172\n" \
"v_pk_add_f16 v169, v169, v173\n" \
"v_mfma_f32_16x16x32_f16 v[28:31], v[152:155], v[172:175], v[28:31]\n" \
"v_pk_add_f16 v170, v170, v174\n" \
"v_pk_add_f16 v171, v171, v175\n" \
"v_add_f32 v252, v4, v8\n" \
"v_add_f32 v253, v5, v9\n" \
"v_add_f32 v254, v6, v10\n" \
"v_add_f32 v255, v7, v11\n" \
"v_mfma_f32_16x16x32_f16 v[40:43], v[156:159], v[172:175], v[40:43]\n" \
"ds_read_b128 v[164:167], v246 offset:10752\n" \
"v_fmac_f32 v4, s56, v8\n" \
"v_fmac_f32 v5, s56, v9\n" \
"v_fmac_f32 v6, s56, v10\n" \
"v_fmac_f32 v7, s56, v11\n" \
"v_mfma_f32_16x16x32_f16 v[32:35], v[232:235], v[168:171], v[32:35]\n" \
"v_fmac_f32 v0, s57, v252\n" \
"v_fmac_f32 v1, s57, v253\n" \
"v_fmac_f32 v2, s57, v254\n" \
"v_fmac_f32 v3, s57, v255\n" \
"v_add_f32 v252, v16, v20\n" \
"v_mfma_f32_16x16x32_f16 v[44:47], v[236:239], v[168:171], v[44:47]\n" \
"v_add_f32 v253, v17, v21\n" \
"v_add_f32 v254, v18, v22\n" \
"v_add_f32 v255, v19, v23\n" \
"v_fmac_f32 v16, s56, v20\n" \
"s_waitcnt lgkmcnt(1)\n" \
"v_mfma_f32_16x16x32_f16 v[24:27], v[184:187], v[176:179], v[24:27]\n" \
"ds_read_b128 v[168:171], v240 offset:45056\n" \
"v_fmac_f32 v17, s56, v21\n" \
"v_fmac_f32 v18, s56, v22\n" \
"v_fmac_f32 v19, s56, v23\n" \
"v_fmac_f32 v12, s57, v252\n" \
"v_fmac_f32 v13, s57, v253\n" \
"v_mfma_f32_16x16x32_f16 v[36:39], v[188:191], v[176:179], v[36:39]\n" \
"v_pk_add_f16 v176, v176, v180\n" \
"v_pk_add_f16 v177, v177, v181\n" \
"v_fmac_f32 v14, s57, v254\n" \
"v_fmac_f32 v15, s57, v255\n" \
"v_cvt_pk_bf16_f32 v8, v0, v1\n" \
"v_cvt_pk_bf16_f32 v20, v4, v5\n" \
"v_mfma_f32_16x16x32_f16 v[28:31], v[192:195], v[180:183], v[28:31]\n" \
"v_pk_add_f16 v178, v178, v182\n" \
"v_pk_add_f16 v179, v179, v183\n" \
"v_cvt_pk_bf16_f32 v9, v2, v3\n" \
"v_cvt_pk_bf16_f32 v21, v6, v7\n" \
"v_cvt_pk_bf16_f32 v10, v12, v13\n" \
"v_cvt_pk_bf16_f32 v22, v16, v17\n" \
"v_cvt_pk_bf16_f32 v11, v14, v15\n" \
"v_mfma_f32_16x16x32_f16 v[40:43], v[196:199], v[180:183], v[40:43]\n" \
"ds_read_b128 v[172:175], v240 offset:45568\n" \
"v_cvt_pk_bf16_f32 v23, v18, v19\n" \
"s_not_b64 vcc, vcc\n" \
"v_cndmask_b32_dpp v0, v20, v8, vcc quad_perm:[1,0,3,2] row_mask:0xf bank_mask:0xf\n" \
"v_cndmask_b32_dpp v1, v21, v9, vcc quad_perm:[1,0,3,2] row_mask:0xf bank_mask:0xf\n" \
"v_mfma_f32_16x16x32_f16 v[32:35], v[200:203], v[176:179], v[32:35]\n" \
"s_waitcnt vmcnt(0)\n" \
"s_barrier\n" \
"v_cndmask_b32_dpp v2, v22, v10, vcc quad_perm:[1,0,3,2] row_mask:0xf bank_mask:0xf\n" \
"v_cndmask_b32_dpp v3, v23, v11, vcc quad_perm:[1,0,3,2] row_mask:0xf bank_mask:0xf\n" \
"buffer_store_dwordx4 v[0:3], v245, s[40:43], s69 offen sc1\n" \
"s_not_b64 vcc, vcc\n" \
"s_add_i32 s85, s69, 0x10000\n" \
"v_mfma_f32_16x16x32_f16 v[44:47], v[204:207], v[176:179], v[44:47]\n" \
"v_cndmask_b32_dpp v12, v8, v20, vcc quad_perm:[1,0,3,2] row_mask:0xf bank_mask:0xf\n" \
"v_cndmask_b32_dpp v13, v9, v21, vcc quad_perm:[1,0,3,2] row_mask:0xf bank_mask:0xf\n" \
"v_cndmask_b32_dpp v14, v10, v22, vcc quad_perm:[1,0,3,2] row_mask:0xf bank_mask:0xf\n" \
"v_cndmask_b32_dpp v15, v11, v23, vcc quad_perm:[1,0,3,2] row_mask:0xf bank_mask:0xf\n" \
"buffer_store_dwordx4 v[12:15], v245, s[40:43], s85 offen sc1\n" \
"v_mfma_f32_16x16x32_f16 v[24:27], v[208:211], v[160:163], v[24:27]\n" \
"ds_read_b128 v[176:179], v240 offset:4096\n" \
"v_mfma_f32_16x16x32_f16 v[36:39], v[212:215], v[160:163], v[36:39]\n" \
"s_waitcnt lgkmcnt(2)\n" \
"v_pk_add_f16 v160, v160, v164\n" \
"v_pk_add_f16 v161, v161, v165\n" \
"v_mfma_f32_16x16x32_f16 v[28:31], v[216:219], v[164:167], v[28:31]\n" \
"v_pk_add_f16 v162, v162, v166\n" \
"v_pk_add_f16 v163, v163, v167\n" \
"v_mfma_f32_16x16x32_f16 v[40:43], v[220:223], v[164:167], v[40:43]\n" \
"ds_read_b128 v[180:183], v240 offset:4608\n" \
"v_mfma_f32_16x16x32_f16 v[32:35], v[224:227], v[160:163], v[32:35]\n" \
"v_mfma_f32_16x16x32_f16 v[44:47], v[228:231], v[160:163], v[44:47]\n" \
"v_mfma_f32_16x16x32_f16 v[48:51], v[136:139], v[168:171], v[48:51]\n" \
"ds_read_b128 v[160:163], v240 offset:24576\n" \
"v_mfma_f32_16x16x32_f16 v[60:63], v[140:143], v[168:171], v[60:63]\n" \
"s_waitcnt lgkmcnt(3)\n" \
"v_pk_add_f16 v168, v168, v172\n" \
"v_pk_add_f16 v169, v169, v173\n" \
"v_mfma_f32_16x16x32_f16 v[52:55], v[144:147], v[172:175], v[52:55]\n" \
"v_pk_add_f16 v170, v170, v174\n" \
"v_pk_add_f16 v171, v171, v175\n" \
"v_add_f32 v252, v28, v32\n" \
"v_add_f32 v253, v29, v33\n" \
"v_mfma_f32_16x16x32_f16 v[64:67], v[148:151], v[172:175], v[64:67]\n" \
"ds_read_b128 v[164:167], v240 offset:25088\n" \
"v_add_f32 v254, v30, v34\n" \
"v_add_f32 v255, v31, v35\n" \
"v_fmac_f32 v28, s56, v32\n" \
"v_mfma_f32_16x16x32_f16 v[56:59], v[120:123], v[168:171], v[56:59]\n" \
"v_fmac_f32 v29, s56, v33\n" \
"v_fmac_f32 v30, s56, v34\n" \
"v_fmac_f32 v31, s56, v35\n" \
"v_mfma_f32_16x16x32_f16 v[68:71], v[124:127], v[168:171], v[68:71]\n" \
"v_fmac_f32 v24, s57, v252\n" \
"v_fmac_f32 v25, s57, v253\n" \
"v_fmac_f32 v26, s57, v254\n" \
"s_waitcnt lgkmcnt(2)\n" \
"v_mfma_f32_16x16x32_f16 v[48:51], v[128:131], v[176:179], v[48:51]\n" \
"ds_read_b128 v[168:171], v241 offset:4096\n" \
"v_fmac_f32 v27, s57, v255\n" \
"v_add_f32 v252, v40, v44\n" \
"v_add_f32 v253, v41, v45\n" \
"v_mfma_f32_16x16x32_f16 v[60:63], v[132:135], v[176:179], v[60:63]\n" \
"v_pk_add_f16 v176, v176, v180\n" \
"v_pk_add_f16 v177, v177, v181\n" \
"v_add_f32 v254, v42, v46\n" \
"v_add_f32 v255, v43, v47\n" \
"v_mfma_f32_16x16x32_f16 v[52:55], v[152:155], v[180:183], v[52:55]\n" \
"v_pk_add_f16 v178, v178, v182\n" \
"v_pk_add_f16 v179, v179, v183\n" \
"v_fmac_f32 v40, s56, v44\n" \
"v_fmac_f32 v41, s56, v45\n" \
"v_fmac_f32 v42, s56, v46\n" \
"v_mfma_f32_16x16x32_f16 v[64:67], v[156:159], v[180:183], v[64:67]\n" \
"ds_read_b128 v[172:175], v241 offset:4608\n" \
"v_fmac_f32 v43, s56, v47\n" \
"v_fmac_f32 v36, s57, v252\n" \
"v_fmac_f32 v37, s57, v253\n" \
"v_mfma_f32_16x16x32_f16 v[56:59], v[232:235], v[176:179], v[56:59]\n" \
"v_fmac_f32 v38, s57, v254\n" \
"v_fmac_f32 v39, s57, v255\n" \
"v_cvt_pk_bf16_f32 v32, v24, v25\n" \
"v_mfma_f32_16x16x32_f16 v[68:71], v[236:239], v[176:179], v[68:71]\n" \
"v_cvt_pk_bf16_f32 v44, v28, v29\n" \
"v_cvt_pk_bf16_f32 v33, v26, v27\n" \
"v_cvt_pk_bf16_f32 v45, v30, v31\n" \
"s_waitcnt lgkmcnt(1)\n" \
"v_mfma_f32_16x16x32_f16 v[48:51], v[184:187], v[160:163], v[48:51]\n" \
"ds_read_b128 v[176:179], v240 offset:47104\n" \
"v_cvt_pk_bf16_f32 v34, v36, v37\n" \
"v_cvt_pk_bf16_f32 v46, v40, v41\n" \
"v_mfma_f32_16x16x32_f16 v[60:63], v[188:191], v[160:163], v[60:63]\n" \
"v_pk_add_f16 v160, v160, v164\n" \
"v_pk_add_f16 v161, v161, v165\n" \
"v_cvt_pk_bf16_f32 v35, v38, v39\n" \
"v_cvt_pk_bf16_f32 v47, v42, v43\n" \
"s_not_b64 vcc, vcc\n" \
"v_mfma_f32_16x16x32_f16 v[52:55], v[192:195], v[164:167], v[52:55]\n" \
"v_pk_add_f16 v162, v162, v166\n" \
"v_pk_add_f16 v163, v163, v167\n" \
"v_cndmask_b32_dpp v24, v44, v32, vcc quad_perm:[1,0,3,2] row_mask:0xf bank_mask:0xf\n" \
"v_cndmask_b32_dpp v25, v45, v33, vcc quad_perm:[1,0,3,2] row_mask:0xf bank_mask:0xf\n" \
"v_cndmask_b32_dpp v26, v46, v34, vcc quad_perm:[1,0,3,2] row_mask:0xf bank_mask:0xf\n" \
"v_mfma_f32_16x16x32_f16 v[64:67], v[196:199], v[164:167], v[64:67]\n" \
"ds_read_b128 v[180:183], v240 offset:47616\n" \
"v_cndmask_b32_dpp v27, v47, v35, vcc quad_perm:[1,0,3,2] row_mask:0xf bank_mask:0xf\n" \
"buffer_store_dwordx4 v[24:27], v245, s[40:43], s70 offen sc1\n" \
"s_not_b64 vcc, vcc\n" \
"v_mfma_f32_16x16x32_f16 v[56:59], v[200:203], v[160:163], v[56:59]\n" \
"s_add_i32 s85, s70, 0x10000\n" \
"v_cndmask_b32_dpp v36, v32, v44, vcc quad_perm:[1,0,3,2] row_mask:0xf bank_mask:0xf\n" \
"v_cndmask_b32_dpp v37, v33, v45, vcc quad_perm:[1,0,3,2] row_mask:0xf bank_mask:0xf\n" \
"v_mfma_f32_16x16x32_f16 v[68:71], v[204:207], v[160:163], v[68:71]\n" \
"v_cndmask_b32_dpp v38, v34, v46, vcc quad_perm:[1,0,3,2] row_mask:0xf bank_mask:0xf\n" \
"v_cndmask_b32_dpp v39, v35, v47, vcc quad_perm:[1,0,3,2] row_mask:0xf bank_mask:0xf\n" \
"buffer_store_dwordx4 v[36:39], v245, s[40:43], s85 offen sc1\n" \
"v_mfma_f32_16x16x32_f16 v[48:51], v[208:211], v[168:171], v[48:51]\n" \
"ds_read_b128 v[160:163], v240 offset:6144\n" \
"v_mfma_f32_16x16x32_f16 v[60:63], v[212:215], v[168:171], v[60:63]\n" \
"s_waitcnt lgkmcnt(2)\n" \
"v_pk_add_f16 v168, v168, v172\n" \
"v_pk_add_f16 v169, v169, v173\n" \
"v_mfma_f32_16x16x32_f16 v[52:55], v[216:219], v[172:175], v[52:55]\n" \
"v_pk_add_f16 v170, v170, v174\n" \
"v_pk_add_f16 v171, v171, v175\n" \
"v_mfma_f32_16x16x32_f16 v[64:67], v[220:223], v[172:175], v[64:67]\n" \
"ds_read_b128 v[164:167], v240 offset:6656\n" \
"v_mfma_f32_16x16x32_f16 v[56:59], v[224:227], v[168:171], v[56:59]\n" \
"v_mfma_f32_16x16x32_f16 v[68:71], v[228:231], v[168:171], v[68:71]\n" \
"v_mfma_f32_16x16x32_f16 v[72:75], v[136:139], v[176:179], v[72:75]\n" \
"ds_read_b128 v[168:171], v240 offset:26624\n" \
"v_mfma_f32_16x16x32_f16 v[84:87], v[140:143], v[176:179], v[84:87]\n" \
"s_waitcnt lgkmcnt(3)\n" \
"v_pk_add_f16 v176, v176, v180\n" \
"v_pk_add_f16 v177, v177, v181\n" \
"v_mfma_f32_16x16x32_f16 v[76:79], v[144:147], v[180:183], v[76:79]\n" \
"v_pk_add_f16 v178, v178, v182\n" \
"v_pk_add_f16 v179, v179, v183\n" \
"v_add_f32 v252, v52, v56\n" \
"v_add_f32 v253, v53, v57\n" \
"v_mfma_f32_16x16x32_f16 v[88:91], v[148:151], v[180:183], v[88:91]\n" \
"ds_read_b128 v[172:175], v240 offset:27136\n" \
"v_add_f32 v254, v54, v58\n" \
"v_add_f32 v255, v55, v59\n" \
"v_mfma_f32_16x16x32_f16 v[80:83], v[120:123], v[176:179], v[80:83]\n" \
"v_fmac_f32 v52, s56, v56\n" \
"v_fmac_f32 v53, s56, v57\n" \
"v_mfma_f32_16x16x32_f16 v[92:95], v[124:127], v[176:179], v[92:95]\n" \
"v_fmac_f32 v54, s56, v58\n" \
"v_fmac_f32 v55, s56, v59\n" \
"s_waitcnt lgkmcnt(2)\n" \
"v_mfma_f32_16x16x32_f16 v[72:75], v[128:131], v[160:163], v[72:75]\n" \
"ds_read_b128 v[176:179], v241 offset:6144\n" \
"v_fmac_f32 v48, s57, v252\n" \
"v_fmac_f32 v49, s57, v253\n" \
"v_mfma_f32_16x16x32_f16 v[84:87], v[132:135], v[160:163], v[84:87]\n" \
"v_pk_add_f16 v160, v160, v164\n" \
"v_pk_add_f16 v161, v161, v165\n" \
"v_fmac_f32 v50, s57, v254\n" \
"v_fmac_f32 v51, s57, v255\n" \
"v_mfma_f32_16x16x32_f16 v[76:79], v[152:155], v[164:167], v[76:79]\n" \
"v_pk_add_f16 v162, v162, v166\n" \
"v_pk_add_f16 v163, v163, v167\n" \
"v_add_f32 v252, v64, v68\n" \
"v_add_f32 v253, v65, v69\n" \
"v_mfma_f32_16x16x32_f16 v[88:91], v[156:159], v[164:167], v[88:91]\n" \
"ds_read_b128 v[180:183], v241 offset:6656\n" \
"v_add_f32 v254, v66, v70\n" \
"v_add_f32 v255, v67, v71\n" \
"v_mfma_f32_16x16x32_f16 v[80:83], v[232:235], v[160:163], v[80:83]\n" \
"v_fmac_f32 v64, s56, v68\n" \
"v_fmac_f32 v65, s56, v69\n" \
"v_mfma_f32_16x16x32_f16 v[92:95], v[236:239], v[160:163], v[92:95]\n" \
"v_fmac_f32 v66, s56, v70\n" \
"v_fmac_f32 v67, s56, v71\n" \
"s_waitcnt lgkmcnt(2)\n" \
"v_mfma_f32_16x16x32_f16 v[72:75], v[184:187], v[168:171], v[72:75]\n" \
"ds_read_b128 v[160:163], v240 offset:49152\n" \
"v_fmac_f32 v60, s57, v252\n" \
"v_fmac_f32 v61, s57, v253\n" \
"v_mfma_f32_16x16x32_f16 v[84:87], v[188:191], v[168:171], v[84:87]\n" \
"v_pk_add_f16 v168, v168, v172\n" \
"v_pk_add_f16 v169, v169, v173\n" \
"v_fmac_f32 v62, s57, v254\n" \
"v_fmac_f32 v63, s57, v255\n" \
"v_mfma_f32_16x16x32_f16 v[76:79], v[192:195], v[172:175], v[76:79]\n" \
"v_pk_add_f16 v170, v170, v174\n" \
"v_pk_add_f16 v171, v171, v175\n" \
"v_cvt_pk_bf16_f32 v56, v48, v49\n" \
"v_cvt_pk_bf16_f32 v68, v52, v53\n" \
"v_mfma_f32_16x16x32_f16 v[88:91], v[196:199], v[172:175], v[88:91]\n" \
"ds_read_b128 v[164:167], v240 offset:49664\n" \
"v_cvt_pk_bf16_f32 v57, v50, v51\n" \
"v_cvt_pk_bf16_f32 v69, v54, v55\n" \
"v_mfma_f32_16x16x32_f16 v[80:83], v[200:203], v[168:171], v[80:83]\n" \
"v_cvt_pk_bf16_f32 v58, v60, v61\n" \
"v_cvt_pk_bf16_f32 v70, v64, v65\n" \
"v_mfma_f32_16x16x32_f16 v[92:95], v[204:207], v[168:171], v[92:95]\n" \
"v_cvt_pk_bf16_f32 v59, v62, v63\n" \
"v_cvt_pk_bf16_f32 v71, v66, v67\n" \
"s_waitcnt lgkmcnt(2)\n" \
"v_mfma_f32_16x16x32_f16 v[72:75], v[208:211], v[176:179], v[72:75]\n" \
"ds_read_b128 v[168:171], v240 offset:8192\n" \
"s_not_b64 vcc, vcc\n" \
"v_cndmask_b32_dpp v48, v68, v56, vcc quad_perm:[1,0,3,2] row_mask:0xf bank_mask:0xf\n" \
"v_mfma_f32_16x16x32_f16 v[84:87], v[212:215], v[176:179], v[84:87]\n" \
"v_pk_add_f16 v176, v176, v180\n" \
"v_pk_add_f16 v177, v177, v181\n" \
"v_cndmask_b32_dpp v49, v69, v57, vcc quad_perm:[1,0,3,2] row_mask:0xf bank_mask:0xf\n" \
"v_cndmask_b32_dpp v50, v70, v58, vcc quad_perm:[1,0,3,2] row_mask:0xf bank_mask:0xf\n" \
"v_mfma_f32_16x16x32_f16 v[76:79], v[216:219], v[180:183], v[76:79]\n" \
"v_pk_add_f16 v178, v178, v182\n" \
"v_pk_add_f16 v179, v179, v183\n" \
"v_cndmask_b32_dpp v51, v71, v59, vcc quad_perm:[1,0,3,2] row_mask:0xf bank_mask:0xf\n" \
"buffer_store_dwordx4 v[48:51], v245, s[40:43], s71 offen sc1\n" \
"v_mfma_f32_16x16x32_f16 v[88:91], v[220:223], v[180:183], v[88:91]\n" \
"ds_read_b128 v[172:175], v240 offset:8704\n" \
"s_not_b64 vcc, vcc\n" \
"s_add_i32 s85, s71, 0x10000\n" \
"v_mfma_f32_16x16x32_f16 v[80:83], v[224:227], v[176:179], v[80:83]\n" \
"v_cndmask_b32_dpp v60, v56, v68, vcc quad_perm:[1,0,3,2] row_mask:0xf bank_mask:0xf\n" \
"v_cndmask_b32_dpp v61, v57, v69, vcc quad_perm:[1,0,3,2] row_mask:0xf bank_mask:0xf\n" \
"v_mfma_f32_16x16x32_f16 v[92:95], v[228:231], v[176:179], v[92:95]\n" \
"v_cndmask_b32_dpp v62, v58, v70, vcc quad_perm:[1,0,3,2] row_mask:0xf bank_mask:0xf\n" \
"v_cndmask_b32_dpp v63, v59, v71, vcc quad_perm:[1,0,3,2] row_mask:0xf bank_mask:0xf\n" \
"buffer_store_dwordx4 v[60:63], v245, s[40:43], s85 offen sc1\n" \
"s_waitcnt lgkmcnt(1)\n" \
"v_mfma_f32_16x16x32_f16 v[96:99], v[136:139], v[160:163], v[96:99]\n" \
"ds_read_b128 v[176:179], v240 offset:28672\n" \
"v_mfma_f32_16x16x32_f16 v[108:111], v[140:143], v[160:163], v[108:111]\n" \
"v_pk_add_f16 v160, v160, v164\n" \
"v_pk_add_f16 v161, v161, v165\n" \
"v_mfma_f32_16x16x32_f16 v[100:103], v[144:147], v[164:167], v[100:103]\n" \
"v_pk_add_f16 v162, v162, v166\n" \
"v_pk_add_f16 v163, v163, v167\n" \
"v_add_f32 v252, v76, v80\n" \
"v_add_f32 v253, v77, v81\n" \
"v_mfma_f32_16x16x32_f16 v[112:115], v[148:151], v[164:167], v[112:115]\n" \
"ds_read_b128 v[180:183], v240 offset:29184\n" \
"v_add_f32 v254, v78, v82\n" \
"v_add_f32 v255, v79, v83\n" \
"v_mfma_f32_16x16x32_f16 v[104:107], v[120:123], v[160:163], v[104:107]\n" \
"v_fmac_f32 v76, s56, v80\n" \
"v_fmac_f32 v77, s56, v81\n" \
"v_mfma_f32_16x16x32_f16 v[116:119], v[124:127], v[160:163], v[116:119]\n" \
"v_fmac_f32 v78, s56, v82\n" \
"v_fmac_f32 v79, s56, v83\n" \
"v_mfma_f32_16x16x32_f16 v[96:99], v[128:131], v[168:171], v[96:99]\n" \
"ds_read_b128 v[160:163], v241 offset:8192\n" \
"v_fmac_f32 v72, s57, v252\n" \
"v_fmac_f32 v73, s57, v253\n" \
"v_mfma_f32_16x16x32_f16 v[108:111], v[132:135], v[168:171], v[108:111]\n" \
"s_waitcnt lgkmcnt(2)\n" \
"v_pk_add_f16 v168, v168, v172\n" \
"v_pk_add_f16 v169, v169, v173\n" \
"v_fmac_f32 v74, s57, v254\n" \
"v_fmac_f32 v75, s57, v255\n" \
"v_mfma_f32_16x16x32_f16 v[100:103], v[152:155], v[172:175], v[100:103]\n" \
"v_pk_add_f16 v170, v170, v174\n" \
"v_pk_add_f16 v171, v171, v175\n" \
"v_add_f32 v252, v88, v92\n" \
"v_add_f32 v253, v89, v93\n" \
"v_mfma_f32_16x16x32_f16 v[112:115], v[156:159], v[172:175], v[112:115]\n" \
"ds_read_b128 v[164:167], v241 offset:8704\n" \
"v_add_f32 v254, v90, v94\n" \
"v_add_f32 v255, v91, v95\n" \
"v_mfma_f32_16x16x32_f16 v[104:107], v[232:235], v[168:171], v[104:107]\n" \
"v_fmac_f32 v88, s56, v92\n" \
"v_fmac_f32 v89, s56, v93\n" \
"v_mfma_f32_16x16x32_f16 v[116:119], v[236:239], v[168:171], v[116:119]\n" \
"v_fmac_f32 v90, s56, v94\n" \
"v_fmac_f32 v91, s56, v95\n" \
"v_mfma_f32_16x16x32_f16 v[96:99], v[184:187], v[176:179], v[96:99]\n" \
"v_fmac_f32 v84, s57, v252\n" \
"v_fmac_f32 v85, s57, v253\n" \
"v_fmac_f32 v86, s57, v254\n" \
"v_mfma_f32_16x16x32_f16 v[108:111], v[188:191], v[176:179], v[108:111]\n" \
"s_waitcnt lgkmcnt(1)\n" \
"v_pk_add_f16 v176, v176, v180\n" \
"v_pk_add_f16 v177, v177, v181\n" \
"v_fmac_f32 v87, s57, v255\n" \
"v_mfma_f32_16x16x32_f16 v[100:103], v[192:195], v[180:183], v[100:103]\n" \
"v_pk_add_f16 v178, v178, v182\n" \
"v_pk_add_f16 v179, v179, v183\n" \
"v_cvt_pk_bf16_f32 v80, v72, v73\n" \
"v_cvt_pk_bf16_f32 v92, v76, v77\n" \
"v_mfma_f32_16x16x32_f16 v[112:115], v[196:199], v[180:183], v[112:115]\n" \
"v_cvt_pk_bf16_f32 v81, v74, v75\n" \
"v_cvt_pk_bf16_f32 v93, v78, v79\n" \
"v_mfma_f32_16x16x32_f16 v[104:107], v[200:203], v[176:179], v[104:107]\n" \
"v_cvt_pk_bf16_f32 v82, v84, v85\n" \
"v_cvt_pk_bf16_f32 v94, v88, v89\n" \
"v_mfma_f32_16x16x32_f16 v[116:119], v[204:207], v[176:179], v[116:119]\n" \
"v_cvt_pk_bf16_f32 v83, v86, v87\n" \
"v_cvt_pk_bf16_f32 v95, v90, v91\n" \
"v_mfma_f32_16x16x32_f16 v[96:99], v[208:211], v[160:163], v[96:99]\n" \
"s_not_b64 vcc, vcc\n" \
"v_cndmask_b32_dpp v72, v92, v80, vcc quad_perm:[1,0,3,2] row_mask:0xf bank_mask:0xf\n" \
"v_cndmask_b32_dpp v73, v93, v81, vcc quad_perm:[1,0,3,2] row_mask:0xf bank_mask:0xf\n" \
"v_mfma_f32_16x16x32_f16 v[108:111], v[212:215], v[160:163], v[108:111]\n" \
"s_waitcnt lgkmcnt(0)\n" \
"v_pk_add_f16 v160, v160, v164\n" \
"v_pk_add_f16 v161, v161, v165\n" \
"v_cndmask_b32_dpp v74, v94, v82, vcc quad_perm:[1,0,3,2] row_mask:0xf bank_mask:0xf\n" \
"v_mfma_f32_16x16x32_f16 v[100:103], v[216:219], v[164:167], v[100:103]\n" \
"v_pk_add_f16 v162, v162, v166\n" \
"v_pk_add_f16 v163, v163, v167\n" \
"v_cndmask_b32_dpp v75, v95, v83, vcc quad_perm:[1,0,3,2] row_mask:0xf bank_mask:0xf\n" \
"buffer_store_dwordx4 v[72:75], v245, s[40:43], s72 offen sc1\n" \
"v_mfma_f32_16x16x32_f16 v[112:115], v[220:223], v[164:167], v[112:115]\n" \
"s_not_b64 vcc, vcc\n" \
"s_add_i32 s85, s72, 0x10000\n" \
"v_mfma_f32_16x16x32_f16 v[104:107], v[224:227], v[160:163], v[104:107]\n" \
"v_cndmask_b32_dpp v84, v80, v92, vcc quad_perm:[1,0,3,2] row_mask:0xf bank_mask:0xf\n" \
"v_cndmask_b32_dpp v85, v81, v93, vcc quad_perm:[1,0,3,2] row_mask:0xf bank_mask:0xf\n" \
"v_mfma_f32_16x16x32_f16 v[116:119], v[228:231], v[160:163], v[116:119]\n" \
"v_cndmask_b32_dpp v86, v82, v94, vcc quad_perm:[1,0,3,2] row_mask:0xf bank_mask:0xf\n" \
"v_cndmask_b32_dpp v87, v83, v95, vcc quad_perm:[1,0,3,2] row_mask:0xf bank_mask:0xf\n" \
"buffer_store_dwordx4 v[84:87], v245, s[40:43], s85 offen sc1\n" \
"s_nop 7\n" \
"v_add_f32 v252, v100, v104\n" \
"v_add_f32 v253, v101, v105\n" \
"v_add_f32 v254, v102, v106\n" \
"v_add_f32 v255, v103, v107\n" \
"v_fmac_f32 v100, s56, v104\n" \
"v_fmac_f32 v101, s56, v105\n" \
"v_fmac_f32 v102, s56, v106\n" \
"v_fmac_f32 v103, s56, v107\n" \
"v_fmac_f32 v96, s57, v252\n" \
"v_fmac_f32 v97, s57, v253\n" \
"v_fmac_f32 v98, s57, v254\n" \
"v_fmac_f32 v99, s57, v255\n" \
"v_add_f32 v252, v112, v116\n" \
"v_add_f32 v253, v113, v117\n" \
"v_add_f32 v254, v114, v118\n" \
"v_add_f32 v255, v115, v119\n" \
"v_fmac_f32 v112, s56, v116\n" \
"v_fmac_f32 v113, s56, v117\n" \
"v_fmac_f32 v114, s56, v118\n" \
"v_fmac_f32 v115, s56, v119\n" \
"v_fmac_f32 v108, s57, v252\n" \
"v_fmac_f32 v109, s57, v253\n" \
"v_fmac_f32 v110, s57, v254\n" \
"v_fmac_f32 v111, s57, v255\n" \
"v_cvt_pk_bf16_f32 v104, v96, v97\n" \
"v_cvt_pk_bf16_f32 v116, v100, v101\n" \
"v_cvt_pk_bf16_f32 v105, v98, v99\n" \
"v_cvt_pk_bf16_f32 v117, v102, v103\n" \
"v_cvt_pk_bf16_f32 v106, v108, v109\n" \
"v_cvt_pk_bf16_f32 v118, v112, v113\n" \
"v_cvt_pk_bf16_f32 v107, v110, v111\n" \
"v_cvt_pk_bf16_f32 v119, v114, v115\n" \
"s_not_b64 vcc, vcc\n" \
"v_cndmask_b32_dpp v96, v116, v104, vcc quad_perm:[1,0,3,2] row_mask:0xf bank_mask:0xf\n" \
"v_cndmask_b32_dpp v97, v117, v105, vcc quad_perm:[1,0,3,2] row_mask:0xf bank_mask:0xf\n" \
"v_cndmask_b32_dpp v98, v118, v106, vcc quad_perm:[1,0,3,2] row_mask:0xf bank_mask:0xf\n" \
"v_cndmask_b32_dpp v99, v119, v107, vcc quad_perm:[1,0,3,2] row_mask:0xf bank_mask:0xf\n" \
"buffer_store_dwordx4 v[96:99], v245, s[40:43], s73 offen sc1\n" \
"s_not_b64 vcc, vcc\n" \
"s_add_i32 s85, s73, 0x10000\n" \
"v_cndmask_b32_dpp v108, v104, v116, vcc quad_perm:[1,0,3,2] row_mask:0xf bank_mask:0xf\n" \
"v_cndmask_b32_dpp v109, v105, v117, vcc quad_perm:[1,0,3,2] row_mask:0xf bank_mask:0xf\n" \
"v_cndmask_b32_dpp v110, v106, v118, vcc quad_perm:[1,0,3,2] row_mask:0xf bank_mask:0xf\n" \
"v_cndmask_b32_dpp v111, v107, v119, vcc quad_perm:[1,0,3,2] row_mask:0xf bank_mask:0xf\n" \
"buffer_store_dwordx4 v[108:111], v245, s[40:43], s85 offen sc1\n" \
"s_waitcnt vmcnt(0)\n" \
"s_branch .Lcf_e\n" \
".Ls03_%=:\n" \
"s_mov_b32 s87, 0x200000\n" \
"s_mov_b32 s86, 0x8006000\n" \
".Lt03_%=:\n" \
"s_sleep 3\n" \
"buffer_load_dword v253, v252, s[36:39], s86 offen sc1\n" \
"s_waitcnt vmcnt(0)\n" \
"s_sub_u32 s87, s87, 1\n" \
"s_cbranch_scc1 .Lr03_%=\n" \
"v_cmp_eq_u32 vcc, 0, v253\n" \
"s_cbranch_vccnz .Lt03_%=\n" \
"s_branch .Lr03_%=\n" \
".Ls04_%=:\n" \
"s_mov_b32 s87, 0x200000\n" \
"s_mov_b32 s86, 0x8008000\n" \
".Lt04_%=:\n" \
"s_sleep 3\n" \
"buffer_load_dword v253, v252, s[36:39], s86 offen sc1\n" \
"s_waitcnt vmcnt(0)\n" \
"s_sub_u32 s87, s87, 1\n" \
"s_cbranch_scc1 .Lr04_%=\n" \
"v_cmp_eq_u32 vcc, 0, v253\n" \
"s_cbranch_vccnz .Lt04_%=\n" \
"s_branch .Lr04_%=\n" \
".Ls05_%=:\n" \
"s_mov_b32 s87, 0x200000\n" \
"s_mov_b32 s86, 0x800a000\n" \
".Lt05_%=:\n" \
"s_sleep 3\n" \
"buffer_load_dword v253, v252, s[36:39], s86 offen sc1\n" \
"s_waitcnt vmcnt(0)\n" \
"s_sub_u32 s87, s87, 1\n" \
"s_cbranch_scc1 .Lr05_%=\n" \
"v_cmp_eq_u32 vcc, 0, v253\n" \
"s_cbranch_vccnz .Lt05_%=\n" \
"s_branch .Lr05_%=\n" \
".Ls07_%=:\n" \
"s_mov_b32 s87, 0x200000\n" \
"s_mov_b32 s86, 0x800e000\n" \
".Lt07_%=:\n" \
"s_sleep 3\n" \
"buffer_load_dword v253, v252, s[36:39], s86 offen sc1\n" \
"s_waitcnt vmcnt(0)\n" \
"s_sub_u32 s87, s87, 1\n" \
"s_cbranch_scc1 .Lr07_%=\n" \
"v_cmp_eq_u32 vcc, 0, v253\n" \
"s_cbranch_vccnz .Lt07_%=\n" \
"s_branch .Lr07_%=\n" \
".Ls08_%=:\n" \
"s_mov_b32 s87, 0x200000\n" \
"s_mov_b32 s86, 0x8010000\n" \
".Lt08_%=:\n" \
"s_sleep 3\n" \
"buffer_load_dword v253, v252, s[36:39], s86 offen sc1\n" \
"s_waitcnt vmcnt(0)\n" \
"s_sub_u32 s87, s87, 1\n" \
"s_cbranch_scc1 .Lr08_%=\n" \
"v_cmp_eq_u32 vcc, 0, v253\n" \
"s_cbranch_vccnz .Lt08_%=\n" \
"s_branch .Lr08_%=\n" \
".Ls09_%=:\n" \
"s_mov_b32 s87, 0x200000\n" \
"s_mov_b32 s86, 0x8012000\n" \
".Lt09_%=:\n" \
"s_sleep 3\n" \
"buffer_load_dword v253, v252, s[36:39], s86 offen sc1\n" \
"s_waitcnt vmcnt(0)\n" \
"s_sub_u32 s87, s87, 1\n" \
"s_cbranch_scc1 .Lr09_%=\n" \
"v_cmp_eq_u32 vcc, 0, v253\n" \
"s_cbranch_vccnz .Lt09_%=\n" \
"s_branch .Lr09_%=\n" \
".Ls011_%=:\n" \
"s_mov_b32 s87, 0x200000\n" \
"s_mov_b32 s86, 0x8016000\n" \
".Lt011_%=:\n" \
"s_sleep 3\n" \
"buffer_load_dword v253, v252, s[36:39], s86 offen sc1\n" \
"s_waitcnt vmcnt(0)\n" \
"s_sub_u32 s87, s87, 1\n" \
"s_cbranch_scc1 .Lr011_%=\n" \
"v_cmp_eq_u32 vcc, 0, v253\n" \
"s_cbranch_vccnz .Lt011_%=\n" \
"s_branch .Lr011_%=\n" \
".Ls012_%=:\n" \
"s_mov_b32 s87, 0x200000\n" \
"s_mov_b32 s86, 0x8018000\n" \
".Lt012_%=:\n" \
"s_sleep 3\n" \
"buffer_load_dword v253, v252, s[36:39], s86 offen sc1\n" \
"s_waitcnt vmcnt(0)\n" \
"s_sub_u32 s87, s87, 1\n" \
"s_cbranch_scc1 .Lr012_%=\n" \
"v_cmp_eq_u32 vcc, 0, v253\n" \
"s_cbranch_vccnz .Lt012_%=\n" \
"s_branch .Lr012_%=\n" \
".Ls013_%=:\n" \
"s_mov_b32 s87, 0x200000\n" \
"s_mov_b32 s86, 0x801a000\n" \
".Lt013_%=:\n" \
"s_sleep 3\n" \
"buffer_load_dword v253, v252, s[36:39], s86 offen sc1\n" \
"s_waitcnt vmcnt(0)\n" \
"s_sub_u32 s87, s87, 1\n" \
"s_cbranch_scc1 .Lr013_%=\n" \
"v_cmp_eq_u32 vcc, 0, v253\n" \
"s_cbranch_vccnz .Lt013_%=\n" \
"s_branch .Lr013_%=\n" \
""
#define LOOP1 \
"s_mov_b32 s91, 0x40004\n" \
"s_mov_b32 s92, 0xfff8fff8\n" \
"ds_read_b64_tr_b16 v[120:121], v241 offset:0\n" \
"ds_read_b64_tr_b16 v[122:123], v241 offset:8192\n" \
"ds_read_b64_tr_b16 v[124:125], v242 offset:0\n" \
"ds_read_b64_tr_b16 v[126:127], v242 offset:8192\n" \
"ds_read_b64_tr_b16 v[128:129], v241 offset:512\n" \
"ds_read_b64_tr_b16 v[130:131], v241 offset:8704\n" \
"ds_read_b64_tr_b16 v[132:133], v242 offset:512\n" \
"ds_read_b64_tr_b16 v[134:135], v242 offset:8704\n" \
"ds_read_b128 v[160:163], v240 offset:2048\n" \
"ds_read_b128 v[164:167], v240 offset:2560\n" \
"ds_read_b128 v[168:171], v240 offset:4096\n" \
"ds_read_b128 v[172:175], v240 offset:4608\n" \
"s_add_i32 m0, s44, 0xa000\n" \
"s_add_i32 s80, s46, 0x400000\n" \
"buffer_load_dwordx4 v232, s[36:39], s80 offen sc1 lds\n" \
"buffer_load_dwordx4 v232, s[36:39], s80 offen offset:1024 sc1 lds\n" \
"buffer_load_dwordx4 v232, s[36:39], s80 offen offset:2048 sc1 lds\n" \
"buffer_load_dwordx4 v232, s[36:39], s80 offen offset:3072 sc1 lds\n" \
"s_add_i32 m0, s45, 0xa000\n" \
"s_add_i32 s81, s47, 0x400000\n" \
"buffer_load_dwordx4 v232, s[36:39], s81 offen sc1 lds\n" \
"s_mov_b32 s90, s53\n" \
"s_setprio 1\n" \
"s_waitcnt vmcnt(9) lgkmcnt(0)\n" \
"s_memrealtime s[86:87]\n" \
"s_waitcnt lgkmcnt(0)\n" \
"s_add_i32 s75, s86, s90\n" \
"s_add_i32 s75, s75, 14\n" \
"s_barrier\n" \
"v_mfma_f32_16x16x32_f16 v[0:3], v[120:123], v[160:163], 0\n" \
"v_pk_add_f16 v136, v120, v128\n" \
"v_pk_add_f16 v137, v121, v129\n" \
"v_pk_add_f16 v138, v122, v130\n" \
"v_pk_add_f16 v139, v123, v131\n" \
"ds_read_b128 v[176:179], v240 offset:26624\n" \
"v_mfma_f32_16x16x32_f16 v[12:15], v[124:127], v[160:163], 0\n" \
"v_pk_add_f16 v160, v160, v164\n" \
"v_pk_add_f16 v161, v161, v165\n" \
"v_pk_add_f16 v140, v124, v132\n" \
"v_mfma_f32_16x16x32_f16 v[4:7], v[128:131], v[164:167], 0\n" \
"v_pk_add_f16 v162, v162, v166\n" \
"v_pk_add_f16 v163, v163, v167\n" \
"v_pk_add_f16 v141, v125, v133\n" \
"buffer_store_dwordx4 v[208:211], v235, s[36:39], s63 offen sc1\n" \
"v_mfma_f32_16x16x32_f16 v[16:19], v[132:135], v[164:167], 0\n" \
"v_pk_add_f16 v142, v126, v134\n" \
"v_pk_add_f16 v143, v127, v135\n" \
"ds_read_b128 v[180:183], v240 offset:27136\n" \
"s_add_i32 m0, s44, 0x0\n" \
"s_add_i32 s80, s46, 0x800000\n" \
"v_mfma_f32_16x16x32_f16 v[8:11], v[136:139], v[160:163], 0\n" \
"buffer_load_dwordx4 v232, s[36:39], s80 offen sc1 lds\n" \
"ds_write_b64 v246, v[96:97] offset:0\n" \
"v_mfma_f32_16x16x32_f16 v[20:23], v[140:143], v[160:163], 0\n" \
"buffer_store_dwordx4 v[72:75], v235, s[36:39], s60 offen sc1\n" \
"ds_read_b64_tr_b16 v[144:145], v241 offset:16384\n" \
"v_mfma_f32_16x16x32_f16 v[24:27], v[120:123], v[168:171], 0\n" \
"ds_read_b128 v[160:163], v240 offset:28672\n" \
"ds_write_b64 v246, v[98:99] offset:4096\n" \
"ds_write_b64 v246, v[100:101] offset:8192\n" \
"v_mfma_f32_16x16x32_f16 v[36:39], v[124:127], v[168:171], 0\n" \
"v_pk_add_f16 v168, v168, v172\n" \
"v_pk_add_f16 v169, v169, v173\n" \
"ds_read_b64_tr_b16 v[146:147], v241 offset:24576\n" \
"v_mfma_f32_16x16x32_f16 v[28:31], v[128:131], v[172:175], 0\n" \
"v_pk_add_f16 v170, v170, v174\n" \
"v_pk_add_f16 v171, v171, v175\n" \
"v_mfma_f32_16x16x32_f16 v[40:43], v[132:135], v[172:175], 0\n" \
"ds_read_b128 v[164:167], v240 offset:29184\n" \
"ds_write_b64 v246, v[102:103] offset:12288\n" \
"v_mfma_f32_16x16x32_f16 v[32:35], v[136:139], v[168:171], 0\n" \
"buffer_load_dwordx4 v232, s[36:39], s80 offen offset:1024 sc1 lds\n" \
"ds_read_b64_tr_b16 v[148:149], v242 offset:16384\n" \
"v_mfma_f32_16x16x32_f16 v[44:47], v[140:143], v[168:171], 0\n" \
"buffer_store_dwordx4 v[76:79], v235, s[36:39], s61 offen sc1\n" \
"ds_read_b64_tr_b16 v[150:151], v242 offset:24576\n" \
"s_waitcnt lgkmcnt(9)\n" \
"v_mfma_f32_16x16x32_f16 v[48:51], v[120:123], v[176:179], 0\n" \
"ds_read_b128 v[168:171], v240 offset:20480\n" \
"ds_write_b64 v246, v[104:105] offset:16384\n" \
"ds_write_b64 v246, v[106:107] offset:20480\n" \
"v_mfma_f32_16x16x32_f16 v[60:63], v[124:127], v[176:179], 0\n" \
"v_pk_add_f16 v176, v176, v180\n" \
"v_pk_add_f16 v177, v177, v181\n" \
"ds_read_b64_tr_b16 v[152:153], v241 offset:16896\n" \
"v_mfma_f32_16x16x32_f16 v[52:55], v[128:131], v[180:183], 0\n" \
"v_pk_add_f16 v178, v178, v182\n" \
"v_pk_add_f16 v179, v179, v183\n" \
"v_mfma_f32_16x16x32_f16 v[64:67], v[132:135], v[180:183], 0\n" \
"ds_read_b128 v[172:175], v240 offset:20992\n" \
"ds_write_b64 v246, v[108:109] offset:24576\n" \
"v_mfma_f32_16x16x32_f16 v[56:59], v[136:139], v[176:179], 0\n" \
"buffer_load_dwordx4 v232, s[36:39], s80 offen offset:2048 sc1 lds\n" \
"ds_write_b64 v246, v[110:111] offset:28672\n" \
"v_mfma_f32_16x16x32_f16 v[68:71], v[140:143], v[176:179], 0\n" \
"s_setprio 0\n" \
"buffer_store_dwordx4 v[80:83], v235, s[36:39], s62 offen sc1\n" \
"ds_read_b64_tr_b16 v[154:155], v241 offset:25088\n" \
"s_waitcnt lgkmcnt(9)\n" \
"v_mfma_f32_16x16x32_f16 v[72:75], v[120:123], v[160:163], 0\n" \
"ds_read_b128 v[176:179], v240 offset:22528\n" \
"ds_write_b64 v246, v[112:113] offset:32768\n" \
"ds_read_b64_tr_b16 v[156:157], v242 offset:16896\n" \
"v_mfma_f32_16x16x32_f16 v[84:87], v[124:127], v[160:163], 0\n" \
"v_pk_add_f16 v160, v160, v164\n" \
"v_pk_add_f16 v161, v161, v165\n" \
"ds_read_b64_tr_b16 v[158:159], v242 offset:25088\n" \
"v_mfma_f32_16x16x32_f16 v[76:79], v[128:131], v[164:167], 0\n" \
"v_pk_add_f16 v162, v162, v166\n" \
"v_pk_add_f16 v163, v163, v167\n" \
"v_mfma_f32_16x16x32_f16 v[88:91], v[132:135], v[164:167], 0\n" \
"ds_read_b128 v[180:183], v240 offset:23040\n" \
"ds_write_b64 v246, v[114:115] offset:36864\n" \
"v_mfma_f32_16x16x32_f16 v[80:83], v[136:139], v[160:163], 0\n" \
"buffer_load_dwordx4 v232, s[36:39], s80 offen offset:3072 sc1 lds\n" \
"ds_write_b64 v246, v[116:117] offset:40960\n" \
"v_mfma_f32_16x16x32_f16 v[92:95], v[140:143], v[160:163], 0\n" \
"s_waitcnt lgkmcnt(8)\n" \
"v_mfma_f32_16x16x32_f16 v[96:99], v[120:123], v[168:171], 0\n" \
"v_pk_add_f16 v120, v144, v152\n" \
"v_pk_add_f16 v121, v145, v153\n" \
"ds_read_b128 v[160:163], v240 offset:24576\n" \
"v_mfma_f32_16x16x32_f16 v[108:111], v[124:127], v[168:171], 0\n" \
"v_pk_add_f16 v168, v168, v172\n" \
"v_pk_add_f16 v169, v169, v173\n" \
"ds_read_b128 v[164:167], v240 offset:25088\n" \
"v_mfma_f32_16x16x32_f16 v[100:103], v[128:131], v[172:175], 0\n" \
"v_pk_add_f16 v170, v170, v174\n" \
"v_pk_add_f16 v171, v171, v175\n" \
"s_add_i32 m0, s45, 0x0\n" \
"s_add_i32 s81, s47, 0x800000\n" \
"v_mfma_f32_16x16x32_f16 v[112:115], v[132:135], v[172:175], 0\n" \
"s_waitcnt lgkmcnt(5)\n" \
"v_pk_add_f16 v122, v146, v154\n" \
"v_pk_add_f16 v123, v147, v155\n" \
"buffer_load_dwordx4 v232, s[36:39], s81 offen sc1 lds\n" \
"v_mfma_f32_16x16x32_f16 v[104:107], v[136:139], v[168:171], 0\n" \
"v_pk_add_f16 v124, v148, v156\n" \
"v_pk_add_f16 v125, v149, v157\n" \
"v_mfma_f32_16x16x32_f16 v[116:119], v[140:143], v[168:171], 0\n" \
"s_setprio 1\n" \
"v_pk_add_f16 v126, v150, v158\n" \
"v_pk_add_f16 v127, v151, v159\n" \
"s_waitcnt vmcnt(9) lgkmcnt(0)\n" \
"s_cmp_le_u32 s90, 88\n" \
"s_cbranch_scc1 .Lp1_%=\n" \
".Lq1_%=:\n" \
"s_memrealtime s[86:87]\n" \
"s_waitcnt lgkmcnt(0)\n" \
"s_sub_i32 s87, s86, s75\n" \
"s_cmp_lt_i32 s87, 0\n" \
"s_cbranch_scc1 .Lq1_%=\n" \
"s_add_i32 s75, s86, s90\n" \
"s_add_i32 s75, s75, 8\n" \
"s_sub_i32 s90, s90, 14\n" \
".Lp1_%=:\n" \
"s_barrier\n" \
"v_mfma_f32_16x16x32_f16 v[0:3], v[144:147], v[176:179], v[0:3]\n" \
"ds_read_b128 v[168:171], v240 offset:47104\n" \
"ds_read_b64_tr_b16 v[184:185], v236 offset:0\n" \
"ds_read_b64_tr_b16 v[186:187], v236 offset:0\n" \
"v_mfma_f32_16x16x32_f16 v[12:15], v[148:151], v[176:179], v[12:15]\n" \
"v_pk_add_f16 v176, v176, v180\n" \
"v_pk_add_f16 v177, v177, v181\n" \
"ds_read_b64_tr_b16 v[188:189], v237 offset:0\n" \
"v_mfma_f32_16x16x32_f16 v[4:7], v[152:155], v[180:183], v[4:7]\n" \
"v_pk_add_f16 v178, v178, v182\n" \
"v_pk_add_f16 v179, v179, v183\n" \
"buffer_store_dwordx4 v[212:215], v235, s[36:39], s64 offen sc1\n" \
"v_mfma_f32_16x16x32_f16 v[16:19], v[156:159], v[180:183], v[16:19]\n" \
"ds_read_b128 v[172:175], v240 offset:47616\n" \
"s_add_i32 m0, s44, 0x5000\n" \
"s_add_i32 s80, s46, 0xc00000\n" \
"ds_read_b64_tr_b16 v[190:191], v237 offset:0\n" \
"v_mfma_f32_16x16x32_f16 v[8:11], v[120:123], v[176:179], v[8:11]\n" \
"buffer_load_dwordx4 v232, s[36:39], s80 offen sc1 lds\n" \
"v_mfma_f32_16x16x32_f16 v[20:23], v[124:127], v[176:179], v[20:23]\n" \
"ds_read_b64_tr_b16 v[128:129], v241 offset:32768\n" \
"ds_read_b64_tr_b16 v[130:131], v241 offset:40960\n" \
"s_waitcnt lgkmcnt(5)\n" \
"v_mfma_f32_16x16x32_bf16 v[184:187], v[184:187], v[192:195], 0\n" \
"v_mfma_f32_16x16x32_f16 v[24:27], v[144:147], v[160:163], v[24:27]\n" \
"ds_read_b128 v[176:179], v240 offset:49152\n" \
"s_waitcnt lgkmcnt(3)\n" \
"v_mfma_f32_16x16x32_bf16 v[188:191], v[188:191], v[192:195], 0\n" \
"v_mfma_f32_16x16x32_f16 v[36:39], v[148:151], v[160:163], v[36:39]\n" \
"v_pk_add_f16 v160, v160, v164\n" \
"v_pk_add_f16 v161, v161, v165\n" \
"ds_read_b64_tr_b16 v[132:133], v242 offset:32768\n" \
"v_mfma_f32_16x16x32_f16 v[28:31], v[152:155], v[164:167], v[28:31]\n" \
"v_pk_add_f16 v162, v162, v166\n" \
"v_pk_add_f16 v163, v163, v167\n" \
"v_mfma_f32_16x16x32_f16 v[40:43], v[156:159], v[164:167], v[40:43]\n" \
"v_cvt_pk_f16_f32 v184, v184, v185\n" \
"ds_read_b128 v[180:183], v240 offset:49664\n" \
"ds_read_b64_tr_b16 v[134:135], v242 offset:40960\n" \
"v_mfma_f32_16x16x32_f16 v[32:35], v[120:123], v[160:163], v[32:35]\n" \
"v_cvt_pk_f16_f32 v185, v186, v187\n" \
"v_cvt_pk_f16_f32 v186, v188, v189\n" \
"buffer_load_dwordx4 v232, s[36:39], s80 offen offset:1024 sc1 lds\n" \
"v_mfma_f32_16x16x32_f16 v[44:47], v[124:127], v[160:163], v[44:47]\n" \
"v_cvt_pk_f16_f32 v187, v190, v191\n" \
"v_add_u32 v184, s91, v184\n" \
"v_mfma_f32_16x16x32_f16 v[48:51], v[144:147], v[168:171], v[48:51]\n" \
"v_add_u32 v185, s91, v185\n" \
"v_add_u32 v186, s91, v186\n" \
"ds_read_b128 v[160:163], v240 offset:40960\n" \
"v_mfma_f32_16x16x32_f16 v[60:63], v[148:151], v[168:171], v[60:63]\n" \
"v_pk_add_f16 v168, v168, v172\n" \
"v_pk_add_f16 v169, v169, v173\n" \
"v_add_u32 v187, s91, v187\n" \
"v_and_b32 v184, s92, v184\n" \
"s_add_i32 s82, s49, 0x2800000\n" \
"v_mfma_f32_16x16x32_f16 v[52:55], v[152:155], v[172:175], v[52:55]\n" \
"v_pk_add_f16 v170, v170, v174\n" \
"v_pk_add_f16 v171, v171, v175\n" \
"v_and_b32 v185, s92, v185\n" \
"v_and_b32 v186, s92, v186\n" \
"v_and_b32 v187, s92, v187\n" \
"v_mfma_f32_16x16x32_f16 v[64:67], v[156:159], v[172:175], v[64:67]\n" \
"ds_read_b128 v[164:167], v240 offset:41472\n" \
"buffer_store_dwordx4 v[184:187], v234, s[36:39], s82 offen sc1\n" \
"ds_read_b64_tr_b16 v[136:137], v241 offset:33280\n" \
"v_mfma_f32_16x16x32_f16 v[56:59], v[120:123], v[168:171], v[56:59]\n" \
"buffer_load_dwordx4 v232, s[36:39], s80 offen offset:2048 sc1 lds\n" \
"ds_read_b64_tr_b16 v[184:185], v238 offset:0\n" \
"v_mfma_f32_16x16x32_f16 v[68:71], v[124:127], v[168:171], v[68:71]\n" \
"s_setprio 0\n" \
"ds_read_b64_tr_b16 v[186:187], v238 offset:0\n" \
"ds_read_b64_tr_b16 v[188:189], v239 offset:0\n" \
"ds_read_b64_tr_b16 v[138:139], v241 offset:41472\n" \
"s_waitcnt lgkmcnt(6)\n" \
"v_mfma_f32_16x16x32_f16 v[72:75], v[144:147], v[176:179], v[72:75]\n" \
"ds_read_b128 v[168:171], v240 offset:43008\n" \
"ds_read_b64_tr_b16 v[190:191], v239 offset:0\n" \
"ds_read_b64_tr_b16 v[140:141], v242 offset:33280\n" \
"v_mfma_f32_16x16x32_f16 v[84:87], v[148:151], v[176:179], v[84:87]\n" \
"v_pk_add_f16 v176, v176, v180\n" \
"v_pk_add_f16 v177, v177, v181\n" \
"v_mfma_f32_16x16x32_f16 v[76:79], v[152:155], v[180:183], v[76:79]\n" \
"v_pk_add_f16 v178, v178, v182\n" \
"v_pk_add_f16 v179, v179, v183\n" \
"s_waitcnt lgkmcnt(5)\n" \
"v_mfma_f32_16x16x32_bf16 v[184:187], v[184:187], v[192:195], 0\n" \
"v_mfma_f32_16x16x32_f16 v[88:91], v[156:159], v[180:183], v[88:91]\n" \
"ds_read_b128 v[172:175], v240 offset:43520\n" \
"ds_read_b64_tr_b16 v[142:143], v242 offset:41472\n" \
"s_waitcnt lgkmcnt(3)\n" \
"v_mfma_f32_16x16x32_bf16 v[188:191], v[188:191], v[192:195], 0\n" \
"v_mfma_f32_16x16x32_f16 v[80:83], v[120:123], v[176:179], v[80:83]\n" \
"buffer_load_dwordx4 v232, s[36:39], s80 offen offset:3072 sc1 lds\n" \
"v_mfma_f32_16x16x32_f16 v[92:95], v[124:127], v[176:179], v[92:95]\n" \
"v_mfma_f32_16x16x32_f16 v[96:99], v[144:147], v[160:163], v[96:99]\n" \
"v_pk_add_f16 v144, v128, v136\n" \
"v_pk_add_f16 v145, v129, v137\n" \
"v_cvt_pk_f16_f32 v184, v184, v185\n" \
"v_cvt_pk_f16_f32 v185, v186, v187\n" \
"ds_read_b128 v[176:179], v240 offset:45056\n" \
"v_mfma_f32_16x16x32_f16 v[108:111], v[148:151], v[160:163], v[108:111]\n" \
"v_pk_add_f16 v160, v160, v164\n" \
"v_pk_add_f16 v161, v161, v165\n" \
"v_cvt_pk_f16_f32 v186, v188, v189\n" \
"v_cvt_pk_f16_f32 v187, v190, v191\n" \
"ds_read_b128 v[180:183], v240 offset:45568\n" \
"v_mfma_f32_16x16x32_f16 v[100:103], v[152:155], v[164:167], v[100:103]\n" \
"v_pk_add_f16 v162, v162, v166\n" \
"v_pk_add_f16 v163, v163, v167\n" \
"v_add_u32 v184, s91, v184\n" \
"v_add_u32 v185, s91, v185\n" \
"v_add_u32 v186, s91, v186\n" \
"s_add_i32 m0, s45, 0x5000\n" \
"s_add_i32 s81, s47, 0xc00000\n" \
"v_mfma_f32_16x16x32_f16 v[112:115], v[156:159], v[164:167], v[112:115]\n" \
"v_pk_add_f16 v146, v130, v138\n" \
"v_pk_add_f16 v147, v131, v139\n" \
"v_add_u32 v187, s91, v187\n" \
"v_and_b32 v184, s92, v184\n" \
"buffer_load_dwordx4 v232, s[36:39], s81 offen sc1 lds\n" \
"v_mfma_f32_16x16x32_f16 v[104:107], v[120:123], v[160:163], v[104:107]\n" \
"s_waitcnt lgkmcnt(2)\n" \
"v_pk_add_f16 v148, v132, v140\n" \
"v_pk_add_f16 v149, v133, v141\n" \
"v_and_b32 v185, s92, v185\n" \
"v_and_b32 v186, s92, v186\n" \
"v_and_b32 v187, s92, v187\n" \
"v_mfma_f32_16x16x32_f16 v[116:119], v[124:127], v[160:163], v[116:119]\n" \
"s_setprio 1\n" \
"v_pk_add_f16 v150, v134, v142\n" \
"v_pk_add_f16 v151, v135, v143\n" \
"buffer_store_dwordx4 v[184:187], v234, s[36:39], s82 offen offset:1024 sc1\n" \
"s_waitcnt vmcnt(8) lgkmcnt(0)\n" \
"s_cmp_le_u32 s90, 88\n" \
"s_cbranch_scc1 .Lp2_%=\n" \
".Lq2_%=:\n" \
"s_memrealtime s[86:87]\n" \
"s_waitcnt lgkmcnt(0)\n" \
"s_sub_i32 s87, s86, s75\n" \
"s_cmp_lt_i32 s87, 0\n" \
"s_cbranch_scc1 .Lq2_%=\n" \
"s_add_i32 s75, s86, s90\n" \
"s_add_i32 s75, s75, 0\n" \
"s_sub_i32 s90, s90, 14\n" \
".Lp2_%=:\n" \
"s_barrier\n" \
"v_mfma_f32_16x16x32_f16 v[0:3], v[128:131], v[168:171], v[0:3]\n" \
"ds_read_b128 v[160:163], v240 offset:6144\n" \
"ds_read_b64_tr_b16 v[184:185], v236 offset:4096\n" \
"ds_read_b64_tr_b16 v[186:187], v236 offset:4096\n" \
"v_mfma_f32_16x16x32_f16 v[12:15], v[132:135], v[168:171], v[12:15]\n" \
"v_pk_add_f16 v168, v168, v172\n" \
"v_pk_add_f16 v169, v169, v173\n" \
"ds_read_b64_tr_b16 v[188:189], v237 offset:4096\n" \
"v_mfma_f32_16x16x32_f16 v[4:7], v[136:139], v[172:175], v[4:7]\n" \
"v_pk_add_f16 v170, v170, v174\n" \
"v_pk_add_f16 v171, v171, v175\n" \
"ds_read_b64_tr_b16 v[190:191], v237 offset:4096\n" \
"v_mfma_f32_16x16x32_f16 v[16:19], v[140:143], v[172:175], v[16:19]\n" \
"ds_read_b128 v[164:167], v240 offset:6656\n" \
"s_add_i32 m0, s44, 0xa000\n" \
"s_add_i32 s80, s46, 0x1000000\n" \
"v_mfma_f32_16x16x32_f16 v[8:11], v[144:147], v[168:171], v[8:11]\n" \
"buffer_load_dwordx4 v232, s[36:39], s80 offen sc1 lds\n" \
"s_waitcnt lgkmcnt(3)\n" \
"v_mfma_f32_16x16x32_bf16 v[184:187], v[184:187], v[192:195], 0\n" \
"v_mfma_f32_16x16x32_f16 v[20:23], v[148:151], v[168:171], v[20:23]\n" \
"ds_read_b64_tr_b16 v[120:121], v241 offset:0\n" \
"ds_read_b64_tr_b16 v[122:123], v241 offset:8192\n" \
"s_waitcnt lgkmcnt(3)\n" \
"v_mfma_f32_16x16x32_bf16 v[188:191], v[188:191], v[192:195], 0\n" \
"v_mfma_f32_16x16x32_f16 v[24:27], v[128:131], v[176:179], v[24:27]\n" \
"ds_read_b128 v[168:171], v240 offset:8192\n" \
"v_mfma_f32_16x16x32_f16 v[36:39], v[132:135], v[176:179], v[36:39]\n" \
"v_pk_add_f16 v176, v176, v180\n" \
"v_pk_add_f16 v177, v177, v181\n" \
"ds_read_b64_tr_b16 v[124:125], v242 offset:0\n" \
"v_mfma_f32_16x16x32_f16 v[28:31], v[136:139], v[180:183], v[28:31]\n" \
"v_pk_add_f16 v178, v178, v182\n" \
"v_pk_add_f16 v179, v179, v183\n" \
"v_cvt_pk_f16_f32 v184, v184, v185\n" \
"v_mfma_f32_16x16x32_f16 v[40:43], v[140:143], v[180:183], v[40:43]\n" \
"v_cvt_pk_f16_f32 v185, v186, v187\n" \
"v_cvt_pk_f16_f32 v186, v188, v189\n" \
"ds_read_b128 v[172:175], v240 offset:8704\n" \
"v_mfma_f32_16x16x32_f16 v[32:35], v[144:147], v[176:179], v[32:35]\n" \
"v_cvt_pk_f16_f32 v187, v190, v191\n" \
"v_add_u32 v184, s91, v184\n" \
"buffer_load_dwordx4 v232, s[36:39], s80 offen offset:1024 sc1 lds\n" \
"v_mfma_f32_16x16x32_f16 v[44:47], v[148:151], v[176:179], v[44:47]\n" \
"v_add_u32 v185, s91, v185\n" \
"v_add_u32 v186, s91, v186\n" \
"ds_read_b64_tr_b16 v[126:127], v242 offset:8192\n" \
"v_mfma_f32_16x16x32_f16 v[48:51], v[128:131], v[160:163], v[48:51]\n" \
"v_add_u32 v187, s91, v187\n" \
"v_and_b32 v184, s92, v184\n" \
"ds_read_b128 v[176:179], v240 offset:0\n" \
"v_mfma_f32_16x16x32_f16 v[60:63], v[132:135], v[160:163], v[60:63]\n" \
"s_waitcnt lgkmcnt(4)\n" \
"v_pk_add_f16 v160, v160, v164\n" \
"v_pk_add_f16 v161, v161, v165\n" \
"v_and_b32 v185, s92, v185\n" \
"s_add_i32 s82, s50, 0x3000000\n" \
"v_mfma_f32_16x16x32_f16 v[52:55], v[136:139], v[164:167], v[52:55]\n" \
"v_pk_add_f16 v162, v162, v166\n" \
"v_pk_add_f16 v163, v163, v167\n" \
"v_and_b32 v186, s92, v186\n" \
"v_and_b32 v187, s92, v187\n" \
"v_mfma_f32_16x16x32_f16 v[64:67], v[140:143], v[164:167], v[64:67]\n" \
"ds_read_b128 v[180:183], v240 offset:512\n" \
"buffer_store_dwordx4 v[184:187], v234, s[36:39], s82 offen sc1\n" \
"ds_read_b64_tr_b16 v[152:153], v241 offset:512\n" \
"v_mfma_f32_16x16x32_f16 v[56:59], v[144:147], v[160:163], v[56:59]\n" \
"buffer_load_dwordx4 v232, s[36:39], s80 offen offset:2048 sc1 lds\n" \
"v_mfma_f32_16x16x32_f16 v[68:71], v[148:151], v[160:163], v[68:71]\n" \
"s_setprio 0\n" \
"ds_read_b64_tr_b16 v[154:155], v241 offset:8704\n" \
"ds_read_b64_tr_b16 v[156:157], v242 offset:512\n" \
"v_mfma_f32_16x16x32_f16 v[72:75], v[128:131], v[168:171], v[72:75]\n" \
"ds_read_b128 v[160:163], v240 offset:2048\n" \
"ds_read_b64_tr_b16 v[158:159], v242 offset:8704\n" \
"v_mfma_f32_16x16x32_f16 v[84:87], v[132:135], v[168:171], v[84:87]\n" \
"s_waitcnt lgkmcnt(6)\n" \
"v_pk_add_f16 v168, v168, v172\n" \
"v_pk_add_f16 v169, v169, v173\n" \
"v_mfma_f32_16x16x32_f16 v[76:79], v[136:139], v[172:175], v[76:79]\n" \
"v_pk_add_f16 v170, v170, v174\n" \
"v_pk_add_f16 v171, v171, v175\n" \
"v_mfma_f32_16x16x32_f16 v[88:91], v[140:143], v[172:175], v[88:91]\n" \
"ds_read_b128 v[164:167], v240 offset:2560\n" \
"v_mfma_f32_16x16x32_f16 v[80:83], v[144:147], v[168:171], v[80:83]\n" \
"buffer_load_dwordx4 v232, s[36:39], s80 offen offset:3072 sc1 lds\n" \
"v_mfma_f32_16x16x32_f16 v[92:95], v[148:151], v[168:171], v[92:95]\n" \
"v_mfma_f32_16x16x32_f16 v[96:99], v[128:131], v[176:179], v[96:99]\n" \
"s_waitcnt lgkmcnt(5)\n" \
"v_pk_add_f16 v128, v120, v152\n" \
"v_pk_add_f16 v129, v121, v153\n" \
"ds_read_b128 v[168:171], v240 offset:4096\n" \
"v_mfma_f32_16x16x32_f16 v[108:111], v[132:135], v[176:179], v[108:111]\n" \
"v_pk_add_f16 v176, v176, v180\n" \
"v_pk_add_f16 v177, v177, v181\n" \
"ds_read_b128 v[172:175], v240 offset:4608\n" \
"v_mfma_f32_16x16x32_f16 v[100:103], v[136:139], v[180:183], v[100:103]\n" \
"v_pk_add_f16 v178, v178, v182\n" \
"v_pk_add_f16 v179, v179, v183\n" \
"s_add_i32 m0, s45, 0xa000\n" \
"s_add_i32 s81, s47, 0x1000000\n" \
"v_mfma_f32_16x16x32_f16 v[112:115], v[140:143], v[180:183], v[112:115]\n" \
"s_waitcnt lgkmcnt(3)\n" \
"v_pk_add_f16 v130, v122, v154\n" \
"v_pk_add_f16 v131, v123, v155\n" \
"buffer_load_dwordx4 v232, s[36:39], s81 offen sc1 lds\n" \
"v_mfma_f32_16x16x32_f16 v[104:107], v[144:147], v[176:179], v[104:107]\n" \
"v_pk_add_f16 v132, v124, v156\n" \
"v_pk_add_f16 v133, v125, v157\n" \
"v_mfma_f32_16x16x32_f16 v[116:119], v[148:151], v[176:179], v[116:119]\n" \
"s_setprio 1\n" \
"v_pk_add_f16 v134, v126, v158\n" \
"v_pk_add_f16 v135, v127, v159\n" \
"s_waitcnt vmcnt(6) lgkmcnt(0)\n" \
"s_cmp_le_u32 s90, 88\n" \
"s_cbranch_scc1 .Lp3_%=\n" \
".Lq3_%=:\n" \
"s_memrealtime s[86:87]\n" \
"s_waitcnt lgkmcnt(0)\n" \
"s_sub_i32 s87, s86, s75\n" \
"s_cmp_lt_i32 s87, 0\n" \
"s_cbranch_scc1 .Lq3_%=\n" \
"s_add_i32 s75, s86, s90\n" \
"s_add_i32 s75, s75, 8\n" \
"s_sub_i32 s90, s90, 14\n" \
".Lp3_%=:\n" \
"s_barrier\n" \
"v_mfma_f32_16x16x32_f16 v[0:3], v[120:123], v[160:163], v[0:3]\n" \
"ds_read_b128 v[176:179], v240 offset:26624\n" \
"ds_read_b64_tr_b16 v[184:185], v238 offset:4096\n" \
"ds_read_b64_tr_b16 v[186:187], v238 offset:4096\n" \
"v_mfma_f32_16x16x32_f16 v[12:15], v[124:127], v[160:163], v[12:15]\n" \
"v_pk_add_f16 v160, v160, v164\n" \
"v_pk_add_f16 v161, v161, v165\n" \
"ds_read_b64_tr_b16 v[188:189], v239 offset:4096\n" \
"v_mfma_f32_16x16x32_f16 v[4:7], v[152:155], v[164:167], v[4:7]\n" \
"v_pk_add_f16 v162, v162, v166\n" \
"v_pk_add_f16 v163, v163, v167\n" \
"ds_read_b64_tr_b16 v[190:191], v239 offset:4096\n" \
"v_mfma_f32_16x16x32_f16 v[16:19], v[156:159], v[164:167], v[16:19]\n" \
"ds_read_b128 v[180:183], v240 offset:27136\n" \
"s_add_i32 m0, s44, 0x0\n" \
"s_add_i32 s80, s46, 0x1400000\n" \
"v_mfma_f32_16x16x32_f16 v[8:11], v[128:131], v[160:163], v[8:11]\n" \
"buffer_load_dwordx4 v232, s[36:39], s80 offen sc1 lds\n" \
"s_waitcnt lgkmcnt(3)\n" \
"v_mfma_f32_16x16x32_bf16 v[184:187], v[184:187], v[192:195], 0\n" \
"v_mfma_f32_16x16x32_f16 v[20:23], v[132:135], v[160:163], v[20:23]\n" \
"ds_read_b64_tr_b16 v[136:137], v241 offset:16384\n" \
"ds_read_b64_tr_b16 v[138:139], v241 offset:24576\n" \
"s_waitcnt lgkmcnt(3)\n" \
"v_mfma_f32_16x16x32_bf16 v[188:191], v[188:191], v[192:195], 0\n" \
"v_mfma_f32_16x16x32_f16 v[24:27], v[120:123], v[168:171], v[24:27]\n" \
"ds_read_b128 v[160:163], v240 offset:28672\n" \
"v_mfma_f32_16x16x32_f16 v[36:39], v[124:127], v[168:171], v[36:39]\n" \
"v_pk_add_f16 v168, v168, v172\n" \
"v_pk_add_f16 v169, v169, v173\n" \
"ds_read_b64_tr_b16 v[140:141], v242 offset:16384\n" \
"v_mfma_f32_16x16x32_f16 v[28:31], v[152:155], v[172:175], v[28:31]\n" \
"v_pk_add_f16 v170, v170, v174\n" \
"v_pk_add_f16 v171, v171, v175\n" \
"v_cvt_pk_f16_f32 v184, v184, v185\n" \
"v_mfma_f32_16x16x32_f16 v[40:43], v[156:159], v[172:175], v[40:43]\n" \
"v_cvt_pk_f16_f32 v185, v186, v187\n" \
"v_cvt_pk_f16_f32 v186, v188, v189\n" \
"ds_read_b128 v[164:167], v240 offset:29184\n" \
"v_mfma_f32_16x16x32_f16 v[32:35], v[128:131], v[168:171], v[32:35]\n" \
"v_cvt_pk_f16_f32 v187, v190, v191\n" \
"v_add_u32 v184, s91, v184\n" \
"buffer_load_dwordx4 v232, s[36:39], s80 offen offset:1024 sc1 lds\n" \
"v_mfma_f32_16x16x32_f16 v[44:47], v[132:135], v[168:171], v[44:47]\n" \
"v_add_u32 v185, s91, v185\n" \
"v_add_u32 v186, s91, v186\n" \
"ds_read_b64_tr_b16 v[142:143], v242 offset:24576\n" \
"v_mfma_f32_16x16x32_f16 v[48:51], v[120:123], v[176:179], v[48:51]\n" \
"v_add_u32 v187, s91, v187\n" \
"v_and_b32 v184, s92, v184\n" \
"ds_read_b128 v[168:171], v240 offset:20480\n" \
"v_mfma_f32_16x16x32_f16 v[60:63], v[124:127], v[176:179], v[60:63]\n" \
"s_waitcnt lgkmcnt(4)\n" \
"v_pk_add_f16 v176, v176, v180\n" \
"v_pk_add_f16 v177, v177, v181\n" \
"v_and_b32 v185, s92, v185\n" \
"v_mfma_f32_16x16x32_f16 v[52:55], v[152:155], v[180:183], v[52:55]\n" \
"v_pk_add_f16 v178, v178, v182\n" \
"v_pk_add_f16 v179, v179, v183\n" \
"v_and_b32 v186, s92, v186\n" \
"v_and_b32 v187, s92, v187\n" \
"v_mfma_f32_16x16x32_f16 v[64:67], v[156:159], v[180:183], v[64:67]\n" \
"ds_read_b128 v[172:175], v240 offset:20992\n" \
"buffer_store_dwordx4 v[184:187], v234, s[36:39], s82 offen offset:1024 sc1\n" \
"ds_read_b64_tr_b16 v[144:145], v241 offset:16896\n" \
"v_mfma_f32_16x16x32_f16 v[56:59], v[128:131], v[176:179], v[56:59]\n" \
"buffer_load_dwordx4 v232, s[36:39], s80 offen offset:2048 sc1 lds\n" \
"v_mfma_f32_16x16x32_f16 v[68:71], v[132:135], v[176:179], v[68:71]\n" \
"s_setprio 0\n" \
"ds_read_b64_tr_b16 v[146:147], v241 offset:25088\n" \
"ds_read_b64_tr_b16 v[148:149], v242 offset:16896\n" \
"v_mfma_f32_16x16x32_f16 v[72:75], v[120:123], v[160:163], v[72:75]\n" \
"ds_read_b128 v[176:179], v240 offset:22528\n" \
"ds_read_b64_tr_b16 v[150:151], v242 offset:25088\n" \
"v_mfma_f32_16x16x32_f16 v[84:87], v[124:127], v[160:163], v[84:87]\n" \
"s_waitcnt lgkmcnt(6)\n" \
"v_pk_add_f16 v160, v160, v164\n" \
"v_pk_add_f16 v161, v161, v165\n" \
"v_mfma_f32_16x16x32_f16 v[76:79], v[152:155], v[164:167], v[76:79]\n" \
"v_pk_add_f16 v162, v162, v166\n" \
"v_pk_add_f16 v163, v163, v167\n" \
"v_mfma_f32_16x16x32_f16 v[88:91], v[156:159], v[164:167], v[88:91]\n" \
"ds_read_b128 v[180:183], v240 offset:23040\n" \
"v_mfma_f32_16x16x32_f16 v[80:83], v[128:131], v[160:163], v[80:83]\n" \
"buffer_load_dwordx4 v232, s[36:39], s80 offen offset:3072 sc1 lds\n" \
"v_mfma_f32_16x16x32_f16 v[92:95], v[132:135], v[160:163], v[92:95]\n" \
"v_mfma_f32_16x16x32_f16 v[96:99], v[120:123], v[168:171], v[96:99]\n" \
"s_waitcnt lgkmcnt(5)\n" \
"v_pk_add_f16 v120, v136, v144\n" \
"v_pk_add_f16 v121, v137, v145\n" \
"ds_read_b128 v[160:163], v240 offset:24576\n" \
"v_mfma_f32_16x16x32_f16 v[108:111], v[124:127], v[168:171], v[108:111]\n" \
"v_pk_add_f16 v168, v168, v172\n" \
"v_pk_add_f16 v169, v169, v173\n" \
"ds_read_b128 v[164:167], v240 offset:25088\n" \
"v_mfma_f32_16x16x32_f16 v[100:103], v[152:155], v[172:175], v[100:103]\n" \
"v_pk_add_f16 v170, v170, v174\n" \
"v_pk_add_f16 v171, v171, v175\n" \
"s_add_i32 m0, s45, 0x0\n" \
"s_add_i32 s81, s47, 0x1400000\n" \
"v_mfma_f32_16x16x32_f16 v[112:115], v[156:159], v[172:175], v[112:115]\n" \
"s_waitcnt lgkmcnt(3)\n" \
"v_pk_add_f16 v122, v138, v146\n" \
"v_pk_add_f16 v123, v139, v147\n" \
"buffer_load_dwordx4 v232, s[36:39], s81 offen sc1 lds\n" \
"v_mfma_f32_16x16x32_f16 v[104:107], v[128:131], v[168:171], v[104:107]\n" \
"v_pk_add_f16 v124, v140, v148\n" \
"v_pk_add_f16 v125, v141, v149\n" \
"v_mfma_f32_16x16x32_f16 v[116:119], v[132:135], v[168:171], v[116:119]\n" \
"s_setprio 1\n" \
"v_pk_add_f16 v126, v142, v150\n" \
"v_pk_add_f16 v127, v143, v151\n" \
"s_waitcnt vmcnt(6) lgkmcnt(0)\n" \
"s_barrier\n" \
"v_mfma_f32_16x16x32_f16 v[0:3], v[136:139], v[176:179], v[0:3]\n" \
"ds_read_b128 v[168:171], v240 offset:47104\n" \
"ds_read_b64_tr_b16 v[184:185], v236 offset:8192\n" \
"ds_read_b64_tr_b16 v[186:187], v236 offset:8192\n" \
"v_mfma_f32_16x16x32_f16 v[12:15], v[140:143], v[176:179], v[12:15]\n" \
"v_pk_add_f16 v176, v176, v180\n" \
"v_pk_add_f16 v177, v177, v181\n" \
"s_mov_b32 s84, 0x8004000\n" \
"ds_read_b64_tr_b16 v[188:189], v237 offset:8192\n" \
"ds_read_b64_tr_b16 v[190:191], v237 offset:8192\n" \
"v_mfma_f32_16x16x32_f16 v[4:7], v[144:147], v[180:183], v[4:7]\n" \
"v_pk_add_f16 v178, v178, v182\n" \
"v_pk_add_f16 v179, v179, v183\n" \
"buffer_load_dword v253, v252, s[36:39], s84 offen sc1\n" \
"buffer_store_dwordx4 v[216:219], v235, s[36:39], s65 offen sc1\n" \
"v_mfma_f32_16x16x32_f16 v[16:19], v[148:151], v[180:183], v[16:19]\n" \
"ds_read_b128 v[172:175], v240 offset:47616\n" \
"s_add_i32 m0, s44, 0x5000\n" \
"s_add_i32 s80, s46, 0x1800000\n" \
"v_mfma_f32_16x16x32_f16 v[8:11], v[120:123], v[176:179], v[8:11]\n" \
"buffer_load_dwordx4 v232, s[36:39], s80 offen sc1 lds\n" \
"s_waitcnt lgkmcnt(3)\n" \
"v_mfma_f32_16x16x32_bf16 v[184:187], v[184:187], v[192:195], 0\n" \
"v_mfma_f32_16x16x32_f16 v[20:23], v[124:127], v[176:179], v[20:23]\n" \
"ds_read_b64_tr_b16 v[128:129], v241 offset:32768\n" \
"ds_read_b64_tr_b16 v[130:131], v241 offset:40960\n" \
"s_waitcnt lgkmcnt(3)\n" \
"v_mfma_f32_16x16x32_bf16 v[188:191], v[188:191], v[192:195], 0\n" \
"v_mfma_f32_16x16x32_f16 v[24:27], v[136:139], v[160:163], v[24:27]\n" \
"ds_read_b128 v[176:179], v240 offset:49152\n" \
"v_mfma_f32_16x16x32_f16 v[36:39], v[140:143], v[160:163], v[36:39]\n" \
"v_pk_add_f16 v160, v160, v164\n" \
"v_pk_add_f16 v161, v161, v165\n" \
"ds_read_b64_tr_b16 v[132:133], v242 offset:32768\n" \
"v_mfma_f32_16x16x32_f16 v[28:31], v[144:147], v[164:167], v[28:31]\n" \
"v_pk_add_f16 v162, v162, v166\n" \
"v_pk_add_f16 v163, v163, v167\n" \
"v_cvt_pk_f16_f32 v184, v184, v185\n" \
"v_mfma_f32_16x16x32_f16 v[40:43], v[148:151], v[164:167], v[40:43]\n" \
"v_cvt_pk_f16_f32 v185, v186, v187\n" \
"v_cvt_pk_f16_f32 v186, v188, v189\n" \
"ds_read_b128 v[180:183], v240 offset:49664\n" \
"v_mfma_f32_16x16x32_f16 v[32:35], v[120:123], v[160:163], v[32:35]\n" \
"v_cvt_pk_f16_f32 v187, v190, v191\n" \
"v_add_u32 v184, s91, v184\n" \
"buffer_load_dwordx4 v232, s[36:39], s80 offen offset:1024 sc1 lds\n" \
"v_mfma_f32_16x16x32_f16 v[44:47], v[124:127], v[160:163], v[44:47]\n" \
"v_add_u32 v185, s91, v185\n" \
"v_add_u32 v186, s91, v186\n" \
"ds_read_b64_tr_b16 v[134:135], v242 offset:40960\n" \
"v_mfma_f32_16x16x32_f16 v[48:51], v[136:139], v[168:171], v[48:51]\n" \
"v_add_u32 v187, s91, v187\n" \
"v_and_b32 v184, s92, v184\n" \
"ds_read_b128 v[160:163], v240 offset:40960\n" \
"v_mfma_f32_16x16x32_f16 v[60:63], v[140:143], v[168:171], v[60:63]\n" \
"s_waitcnt lgkmcnt(4)\n" \
"v_pk_add_f16 v168, v168, v172\n" \
"v_pk_add_f16 v169, v169, v173\n" \
"v_and_b32 v185, s92, v185\n" \
"s_add_i32 s82, s51, 0x3800000\n" \
"v_mfma_f32_16x16x32_f16 v[52:55], v[144:147], v[172:175], v[52:55]\n" \
"v_pk_add_f16 v170, v170, v174\n" \
"v_pk_add_f16 v171, v171, v175\n" \
"v_and_b32 v186, s92, v186\n" \
"v_and_b32 v187, s92, v187\n" \
"v_mfma_f32_16x16x32_f16 v[64:67], v[148:151], v[172:175], v[64:67]\n" \
"ds_read_b128 v[164:167], v240 offset:41472\n" \
"buffer_store_dwordx4 v[184:187], v234, s[36:39], s82 offen sc1\n" \
"ds_read_b64_tr_b16 v[152:153], v241 offset:33280\n" \
"v_mfma_f32_16x16x32_f16 v[56:59], v[120:123], v[168:171], v[56:59]\n" \
"buffer_load_dwordx4 v232, s[36:39], s80 offen offset:2048 sc1 lds\n" \
"v_mfma_f32_16x16x32_f16 v[68:71], v[124:127], v[168:171], v[68:71]\n" \
"s_setprio 0\n" \
"ds_read_b64_tr_b16 v[154:155], v241 offset:41472\n" \
"ds_read_b64_tr_b16 v[156:157], v242 offset:33280\n" \
"v_mfma_f32_16x16x32_f16 v[72:75], v[136:139], v[176:179], v[72:75]\n" \
"ds_read_b128 v[168:171], v240 offset:43008\n" \
"ds_read_b64_tr_b16 v[158:159], v242 offset:41472\n" \
"v_mfma_f32_16x16x32_f16 v[84:87], v[140:143], v[176:179], v[84:87]\n" \
"s_waitcnt lgkmcnt(6)\n" \
"v_pk_add_f16 v176, v176, v180\n" \
"v_pk_add_f16 v177, v177, v181\n" \
"v_mfma_f32_16x16x32_f16 v[76:79], v[144:147], v[180:183], v[76:79]\n" \
"v_pk_add_f16 v178, v178, v182\n" \
"v_pk_add_f16 v179, v179, v183\n" \
"v_mfma_f32_16x16x32_f16 v[88:91], v[148:151], v[180:183], v[88:91]\n" \
"ds_read_b128 v[172:175], v240 offset:43520\n" \
"v_mfma_f32_16x16x32_f16 v[80:83], v[120:123], v[176:179], v[80:83]\n" \
"buffer_load_dwordx4 v232, s[36:39], s80 offen offset:3072 sc1 lds\n" \
"v_mfma_f32_16x16x32_f16 v[92:95], v[124:127], v[176:179], v[92:95]\n" \
"v_mfma_f32_16x16x32_f16 v[96:99], v[136:139], v[160:163], v[96:99]\n" \
"s_waitcnt lgkmcnt(5)\n" \
"v_pk_add_f16 v136, v128, v152\n" \
"v_pk_add_f16 v137, v129, v153\n" \
"ds_read_b128 v[176:179], v240 offset:45056\n" \
"v_mfma_f32_16x16x32_f16 v[108:111], v[140:143], v[160:163], v[108:111]\n" \
"v_pk_add_f16 v160, v160, v164\n" \
"v_pk_add_f16 v161, v161, v165\n" \
"ds_read_b128 v[180:183], v240 offset:45568\n" \
"v_mfma_f32_16x16x32_f16 v[100:103], v[144:147], v[164:167], v[100:103]\n" \
"v_pk_add_f16 v162, v162, v166\n" \
"v_pk_add_f16 v163, v163, v167\n" \
"s_waitcnt vmcnt(6)\n" \
"v_cmp_eq_u32 vcc, 0, v253\n" \
"s_cbranch_vccnz .Ls12_%=\n" \
".Lr12_%=:\n" \
"s_add_i32 m0, s45, 0x5000\n" \
"s_add_i32 s81, s47, 0x1800000\n" \
"v_mfma_f32_16x16x32_f16 v[112:115], v[148:151], v[164:167], v[112:115]\n" \
"s_waitcnt lgkmcnt(3)\n" \
"v_pk_add_f16 v138, v130, v154\n" \
"v_pk_add_f16 v139, v131, v155\n" \
"buffer_load_dwordx4 v232, s[36:39], s81 offen sc1 lds\n" \
"v_mfma_f32_16x16x32_f16 v[104:107], v[120:123], v[160:163], v[104:107]\n" \
"v_pk_add_f16 v140, v132, v156\n" \
"v_pk_add_f16 v141, v133, v157\n" \
"v_mfma_f32_16x16x32_f16 v[116:119], v[124:127], v[160:163], v[116:119]\n" \
"s_setprio 1\n" \
"v_pk_add_f16 v142, v134, v158\n" \
"v_pk_add_f16 v143, v135, v159\n" \
"s_waitcnt vmcnt(7) lgkmcnt(0)\n" \
"s_barrier\n" \
"v_mfma_f32_16x16x32_f16 v[0:3], v[128:131], v[168:171], v[0:3]\n" \
"ds_read_b128 v[160:163], v240 offset:6144\n" \
"ds_read_b64_tr_b16 v[184:185], v238 offset:8192\n" \
"ds_read_b64_tr_b16 v[186:187], v238 offset:8192\n" \
"v_mfma_f32_16x16x32_f16 v[12:15], v[132:135], v[168:171], v[12:15]\n" \
"v_pk_add_f16 v168, v168, v172\n" \
"v_pk_add_f16 v169, v169, v173\n" \
"ds_read_b64_tr_b16 v[188:189], v239 offset:8192\n" \
"v_mfma_f32_16x16x32_f16 v[4:7], v[152:155], v[172:175], v[4:7]\n" \
"v_pk_add_f16 v170, v170, v174\n" \
"v_pk_add_f16 v171, v171, v175\n" \
"ds_read_b64_tr_b16 v[190:191], v239 offset:8192\n" \
"v_mfma_f32_16x16x32_f16 v[16:19], v[156:159], v[172:175], v[16:19]\n" \
"ds_read_b128 v[164:167], v240 offset:6656\n" \
"s_add_i32 m0, s44, 0xa000\n" \
"s_add_i32 s80, s46, 0x1c00000\n" \
"v_mfma_f32_16x16x32_f16 v[8:11], v[136:139], v[168:171], v[8:11]\n" \
"buffer_load_dwordx4 v232, s[36:39], s80 offen sc1 lds\n" \
"s_waitcnt lgkmcnt(3)\n" \
"v_mfma_f32_16x16x32_bf16 v[184:187], v[184:187], v[192:195], 0\n" \
"v_mfma_f32_16x16x32_f16 v[20:23], v[140:143], v[168:171], v[20:23]\n" \
"ds_read_b64_tr_b16 v[120:121], v241 offset:0\n" \
"ds_read_b64_tr_b16 v[122:123], v241 offset:8192\n" \
"s_waitcnt lgkmcnt(3)\n" \
"v_mfma_f32_16x16x32_bf16 v[188:191], v[188:191], v[192:195], 0\n" \
"v_mfma_f32_16x16x32_f16 v[24:27], v[128:131], v[176:179], v[24:27]\n" \
"ds_read_b128 v[168:171], v240 offset:8192\n" \
"v_mfma_f32_16x16x32_f16 v[36:39], v[132:135], v[176:179], v[36:39]\n" \
"v_pk_add_f16 v176, v176, v180\n" \
"v_pk_add_f16 v177, v177, v181\n" \
"ds_read_b64_tr_b16 v[124:125], v242 offset:0\n" \
"v_mfma_f32_16x16x32_f16 v[28:31], v[152:155], v[180:183], v[28:31]\n" \
"v_pk_add_f16 v178, v178, v182\n" \
"v_pk_add_f16 v179, v179, v183\n" \
"v_cvt_pk_f16_f32 v184, v184, v185\n" \
"v_mfma_f32_16x16x32_f16 v[40:43], v[156:159], v[180:183], v[40:43]\n" \
"v_cvt_pk_f16_f32 v185, v186, v187\n" \
"v_cvt_pk_f16_f32 v186, v188, v189\n" \
"ds_read_b128 v[172:175], v240 offset:8704\n" \
"v_mfma_f32_16x16x32_f16 v[32:35], v[136:139], v[176:179], v[32:35]\n" \
"v_cvt_pk_f16_f32 v187, v190, v191\n" \
"v_add_u32 v184, s91, v184\n" \
"buffer_load_dwordx4 v232, s[36:39], s80 offen offset:1024 sc1 lds\n" \
"v_mfma_f32_16x16x32_f16 v[44:47], v[140:143], v[176:179], v[44:47]\n" \
"v_add_u32 v185, s91, v185\n" \
"v_add_u32 v186, s91, v186\n" \
"ds_read_b64_tr_b16 v[126:127], v242 offset:8192\n" \
"v_mfma_f32_16x16x32_f16 v[48:51], v[128:131], v[160:163], v[48:51]\n" \
"v_add_u32 v187, s91, v187\n" \
"v_and_b32 v184, s92, v184\n" \
"ds_read_b128 v[176:179], v240 offset:0\n" \
"v_mfma_f32_16x16x32_f16 v[60:63], v[132:135], v[160:163], v[60:63]\n" \
"s_waitcnt lgkmcnt(4)\n" \
"v_pk_add_f16 v160, v160, v164\n" \
"v_pk_add_f16 v161, v161, v165\n" \
"v_and_b32 v185, s92, v185\n" \
"v_mfma_f32_16x16x32_f16 v[52:55], v[152:155], v[164:167], v[52:55]\n" \
"v_pk_add_f16 v162, v162, v166\n" \
"v_pk_add_f16 v163, v163, v167\n" \
"v_and_b32 v186, s92, v186\n" \
"v_and_b32 v187, s92, v187\n" \
"v_mfma_f32_16x16x32_f16 v[64:67], v[156:159], v[164:167], v[64:67]\n" \
"ds_read_b128 v[180:183], v240 offset:512\n" \
"buffer_store_dwordx4 v[184:187], v234, s[36:39], s82 offen offset:1024 sc1\n" \
"ds_read_b64_tr_b16 v[144:145], v241 offset:512\n" \
"v_mfma_f32_16x16x32_f16 v[56:59], v[136:139], v[160:163], v[56:59]\n" \
"buffer_load_dwordx4 v232, s[36:39], s80 offen offset:2048 sc1 lds\n" \
"v_mfma_f32_16x16x32_f16 v[68:71], v[140:143], v[160:163], v[68:71]\n" \
"s_setprio 0\n" \
"ds_read_b64_tr_b16 v[146:147], v241 offset:8704\n" \
"ds_read_b64_tr_b16 v[148:149], v242 offset:512\n" \
"v_mfma_f32_16x16x32_f16 v[72:75], v[128:131], v[168:171], v[72:75]\n" \
"ds_read_b128 v[160:163], v240 offset:2048\n" \
"ds_read_b64_tr_b16 v[150:151], v242 offset:8704\n" \
"v_mfma_f32_16x16x32_f16 v[84:87], v[132:135], v[168:171], v[84:87]\n" \
"s_waitcnt lgkmcnt(6)\n" \
"v_pk_add_f16 v168, v168, v172\n" \
"v_pk_add_f16 v169, v169, v173\n" \
"v_mfma_f32_16x16x32_f16 v[76:79], v[152:155], v[172:175], v[76:79]\n" \
"v_pk_add_f16 v170, v170, v174\n" \
"v_pk_add_f16 v171, v171, v175\n" \
"v_mfma_f32_16x16x32_f16 v[88:91], v[156:159], v[172:175], v[88:91]\n" \
"ds_read_b128 v[164:167], v240 offset:2560\n" \
"v_mfma_f32_16x16x32_f16 v[80:83], v[136:139], v[168:171], v[80:83]\n" \
"buffer_load_dwordx4 v232, s[36:39], s80 offen offset:3072 sc1 lds\n" \
"v_mfma_f32_16x16x32_f16 v[92:95], v[140:143], v[168:171], v[92:95]\n" \
"v_mfma_f32_16x16x32_f16 v[96:99], v[128:131], v[176:179], v[96:99]\n" \
"s_waitcnt lgkmcnt(5)\n" \
"v_pk_add_f16 v128, v120, v144\n" \
"v_pk_add_f16 v129, v121, v145\n" \
"ds_read_b128 v[168:171], v240 offset:4096\n" \
"v_mfma_f32_16x16x32_f16 v[108:111], v[132:135], v[176:179], v[108:111]\n" \
"v_pk_add_f16 v176, v176, v180\n" \
"v_pk_add_f16 v177, v177, v181\n" \
"ds_read_b128 v[172:175], v240 offset:4608\n" \
"v_mfma_f32_16x16x32_f16 v[100:103], v[152:155], v[180:183], v[100:103]\n" \
"v_pk_add_f16 v178, v178, v182\n" \
"v_pk_add_f16 v179, v179, v183\n" \
"s_add_i32 m0, s45, 0xa000\n" \
"s_add_i32 s81, s47, 0x1c00000\n" \
"v_mfma_f32_16x16x32_f16 v[112:115], v[156:159], v[180:183], v[112:115]\n" \
"s_waitcnt lgkmcnt(3)\n" \
"v_pk_add_f16 v130, v122, v146\n" \
"v_pk_add_f16 v131, v123, v147\n" \
"buffer_load_dwordx4 v232, s[36:39], s81 offen sc1 lds\n" \
"v_mfma_f32_16x16x32_f16 v[104:107], v[136:139], v[176:179], v[104:107]\n" \
"v_pk_add_f16 v132, v124, v148\n" \
"v_pk_add_f16 v133, v125, v149\n" \
"v_mfma_f32_16x16x32_f16 v[116:119], v[140:143], v[176:179], v[116:119]\n" \
"s_setprio 1\n" \
"v_pk_add_f16 v134, v126, v150\n" \
"v_pk_add_f16 v135, v127, v151\n" \
"s_waitcnt vmcnt(6) lgkmcnt(0)\n" \
"s_barrier\n" \
"v_mfma_f32_16x16x32_f16 v[0:3], v[120:123], v[160:163], v[0:3]\n" \
"ds_read_b128 v[176:179], v240 offset:26624\n" \
"ds_read_b64_tr_b16 v[184:185], v236 offset:12288\n" \
"ds_read_b64_tr_b16 v[186:187], v236 offset:12288\n" \
"v_mfma_f32_16x16x32_f16 v[12:15], v[124:127], v[160:163], v[12:15]\n" \
"v_pk_add_f16 v160, v160, v164\n" \
"v_pk_add_f16 v161, v161, v165\n" \
"ds_read_b64_tr_b16 v[188:189], v237 offset:12288\n" \
"v_mfma_f32_16x16x32_f16 v[4:7], v[144:147], v[164:167], v[4:7]\n" \
"v_pk_add_f16 v162, v162, v166\n" \
"v_pk_add_f16 v163, v163, v167\n" \
"ds_read_b64_tr_b16 v[190:191], v237 offset:12288\n" \
"v_mfma_f32_16x16x32_f16 v[16:19], v[148:151], v[164:167], v[16:19]\n" \
"ds_read_b128 v[180:183], v240 offset:27136\n" \
"s_add_i32 m0, s44, 0x0\n" \
"s_add_i32 s80, s46, 0x2000000\n" \
"v_mfma_f32_16x16x32_f16 v[8:11], v[128:131], v[160:163], v[8:11]\n" \
"buffer_load_dwordx4 v232, s[36:39], s80 offen sc1 lds\n" \
"s_waitcnt lgkmcnt(3)\n" \
"v_mfma_f32_16x16x32_bf16 v[184:187], v[184:187], v[192:195], 0\n" \
"v_mfma_f32_16x16x32_f16 v[20:23], v[132:135], v[160:163], v[20:23]\n" \
"ds_read_b64_tr_b16 v[136:137], v241 offset:16384\n" \
"ds_read_b64_tr_b16 v[138:139], v241 offset:24576\n" \
"s_waitcnt lgkmcnt(3)\n" \
"v_mfma_f32_16x16x32_bf16 v[188:191], v[188:191], v[192:195], 0\n" \
"v_mfma_f32_16x16x32_f16 v[24:27], v[120:123], v[168:171], v[24:27]\n" \
"ds_read_b128 v[160:163], v240 offset:28672\n" \
"v_mfma_f32_16x16x32_f16 v[36:39], v[124:127], v[168:171], v[36:39]\n" \
"v_pk_add_f16 v168, v168, v172\n" \
"v_pk_add_f16 v169, v169, v173\n" \
"ds_read_b64_tr_b16 v[140:141], v242 offset:16384\n" \
"v_mfma_f32_16x16x32_f16 v[28:31], v[144:147], v[172:175], v[28:31]\n" \
"v_pk_add_f16 v170, v170, v174\n" \
"v_pk_add_f16 v171, v171, v175\n" \
"v_cvt_pk_f16_f32 v184, v184, v185\n" \
"v_mfma_f32_16x16x32_f16 v[40:43], v[148:151], v[172:175], v[40:43]\n" \
"v_cvt_pk_f16_f32 v185, v186, v187\n" \
"v_cvt_pk_f16_f32 v186, v188, v189\n" \
"ds_read_b128 v[164:167], v240 offset:29184\n" \
"v_mfma_f32_16x16x32_f16 v[32:35], v[128:131], v[168:171], v[32:35]\n" \
"v_cvt_pk_f16_f32 v187, v190, v191\n" \
"v_add_u32 v184, s91, v184\n" \
"buffer_load_dwordx4 v232, s[36:39], s80 offen offset:1024 sc1 lds\n" \
"v_mfma_f32_16x16x32_f16 v[44:47], v[132:135], v[168:171], v[44:47]\n" \
"v_add_u32 v185, s91, v185\n" \
"v_add_u32 v186, s91, v186\n" \
"ds_read_b64_tr_b16 v[142:143], v242 offset:24576\n" \
"v_mfma_f32_16x16x32_f16 v[48:51], v[120:123], v[176:179], v[48:51]\n" \
"v_add_u32 v187, s91, v187\n" \
"v_and_b32 v184, s92, v184\n" \
"ds_read_b128 v[168:171], v240 offset:20480\n" \
"v_mfma_f32_16x16x32_f16 v[60:63], v[124:127], v[176:179], v[60:63]\n" \
"s_waitcnt lgkmcnt(4)\n" \
"v_pk_add_f16 v176, v176, v180\n" \
"v_pk_add_f16 v177, v177, v181\n" \
"v_and_b32 v185, s92, v185\n" \
"s_add_i32 s82, s48, 0x4000000\n" \
"v_mfma_f32_16x16x32_f16 v[52:55], v[144:147], v[180:183], v[52:55]\n" \
"v_pk_add_f16 v178, v178, v182\n" \
"v_pk_add_f16 v179, v179, v183\n" \
"v_and_b32 v186, s92, v186\n" \
"v_and_b32 v187, s92, v187\n" \
"v_mfma_f32_16x16x32_f16 v[64:67], v[148:151], v[180:183], v[64:67]\n" \
"ds_read_b128 v[172:175], v240 offset:20992\n" \
"buffer_store_dwordx4 v[184:187], v234, s[36:39], s82 offen sc1\n" \
"ds_read_b64_tr_b16 v[152:153], v241 offset:16896\n" \
"v_mfma_f32_16x16x32_f16 v[56:59], v[128:131], v[176:179], v[56:59]\n" \
"buffer_load_dwordx4 v232, s[36:39], s80 offen offset:2048 sc1 lds\n" \
"v_mfma_f32_16x16x32_f16 v[68:71], v[132:135], v[176:179], v[68:71]\n" \
"s_setprio 0\n" \
"ds_read_b64_tr_b16 v[154:155], v241 offset:25088\n" \
"ds_read_b64_tr_b16 v[156:157], v242 offset:16896\n" \
"v_mfma_f32_16x16x32_f16 v[72:75], v[120:123], v[160:163], v[72:75]\n" \
"ds_read_b128 v[176:179], v240 offset:22528\n" \
"ds_read_b64_tr_b16 v[158:159], v242 offset:25088\n" \
"v_mfma_f32_16x16x32_f16 v[84:87], v[124:127], v[160:163], v[84:87]\n" \
"s_waitcnt lgkmcnt(6)\n" \
"v_pk_add_f16 v160, v160, v164\n" \
"v_pk_add_f16 v161, v161, v165\n" \
"v_mfma_f32_16x16x32_f16 v[76:79], v[144:147], v[164:167], v[76:79]\n" \
"v_pk_add_f16 v162, v162, v166\n" \
"v_pk_add_f16 v163, v163, v167\n" \
"v_mfma_f32_16x16x32_f16 v[88:91], v[148:151], v[164:167], v[88:91]\n" \
"ds_read_b128 v[180:183], v240 offset:23040\n" \
"v_mfma_f32_16x16x32_f16 v[80:83], v[128:131], v[160:163], v[80:83]\n" \
"buffer_load_dwordx4 v232, s[36:39], s80 offen offset:3072 sc1 lds\n" \
"v_mfma_f32_16x16x32_f16 v[92:95], v[132:135], v[160:163], v[92:95]\n" \
"v_mfma_f32_16x16x32_f16 v[96:99], v[120:123], v[168:171], v[96:99]\n" \
"s_waitcnt lgkmcnt(5)\n" \
"v_pk_add_f16 v120, v136, v152\n" \
"v_pk_add_f16 v121, v137, v153\n" \
"ds_read_b128 v[160:163], v240 offset:24576\n" \
"v_mfma_f32_16x16x32_f16 v[108:111], v[124:127], v[168:171], v[108:111]\n" \
"v_pk_add_f16 v168, v168, v172\n" \
"v_pk_add_f16 v169, v169, v173\n" \
"ds_read_b128 v[164:167], v240 offset:25088\n" \
"v_mfma_f32_16x16x32_f16 v[100:103], v[144:147], v[172:175], v[100:103]\n" \
"v_pk_add_f16 v170, v170, v174\n" \
"v_pk_add_f16 v171, v171, v175\n" \
"s_add_i32 m0, s45, 0x0\n" \
"s_add_i32 s81, s47, 0x2000000\n" \
"v_mfma_f32_16x16x32_f16 v[112:115], v[148:151], v[172:175], v[112:115]\n" \
"s_waitcnt lgkmcnt(3)\n" \
"v_pk_add_f16 v122, v138, v154\n" \
"v_pk_add_f16 v123, v139, v155\n" \
"buffer_load_dwordx4 v232, s[36:39], s81 offen sc1 lds\n" \
"v_mfma_f32_16x16x32_f16 v[104:107], v[128:131], v[168:171], v[104:107]\n" \
"v_pk_add_f16 v124, v140, v156\n" \
"v_pk_add_f16 v125, v141, v157\n" \
"v_mfma_f32_16x16x32_f16 v[116:119], v[132:135], v[168:171], v[116:119]\n" \
"s_setprio 1\n" \
"v_pk_add_f16 v126, v142, v158\n" \
"v_pk_add_f16 v127, v143, v159\n" \
"s_waitcnt vmcnt(6) lgkmcnt(0)\n" \
"s_barrier\n" \
"v_mfma_f32_16x16x32_f16 v[0:3], v[136:139], v[176:179], v[0:3]\n" \
"ds_read_b128 v[168:171], v240 offset:47104\n" \
"ds_read_b64_tr_b16 v[184:185], v238 offset:12288\n" \
"ds_read_b64_tr_b16 v[186:187], v238 offset:12288\n" \
"v_mfma_f32_16x16x32_f16 v[12:15], v[140:143], v[176:179], v[12:15]\n" \
"v_pk_add_f16 v176, v176, v180\n" \
"v_pk_add_f16 v177, v177, v181\n" \
"ds_read_b64_tr_b16 v[188:189], v239 offset:12288\n" \
"ds_read_b64_tr_b16 v[190:191], v239 offset:12288\n" \
"v_mfma_f32_16x16x32_f16 v[4:7], v[152:155], v[180:183], v[4:7]\n" \
"v_pk_add_f16 v178, v178, v182\n" \
"v_pk_add_f16 v179, v179, v183\n" \
"buffer_store_dwordx4 v[220:223], v235, s[36:39], s66 offen sc1\n" \
"v_mfma_f32_16x16x32_f16 v[16:19], v[156:159], v[180:183], v[16:19]\n" \
"ds_read_b128 v[172:175], v240 offset:47616\n" \
"s_add_i32 m0, s44, 0x5000\n" \
"s_add_i32 s80, s46, 0x2400000\n" \
"v_mfma_f32_16x16x32_f16 v[8:11], v[120:123], v[176:179], v[8:11]\n" \
"buffer_load_dwordx4 v232, s[36:39], s80 offen sc1 lds\n" \
"s_waitcnt lgkmcnt(3)\n" \
"v_mfma_f32_16x16x32_bf16 v[184:187], v[184:187], v[192:195], 0\n" \
"v_mfma_f32_16x16x32_f16 v[20:23], v[124:127], v[176:179], v[20:23]\n" \
"ds_read_b64_tr_b16 v[128:129], v241 offset:32768\n" \
"ds_read_b64_tr_b16 v[130:131], v241 offset:40960\n" \
"s_waitcnt lgkmcnt(3)\n" \
"v_mfma_f32_16x16x32_bf16 v[188:191], v[188:191], v[192:195], 0\n" \
"v_mfma_f32_16x16x32_f16 v[24:27], v[136:139], v[160:163], v[24:27]\n" \
"ds_read_b128 v[176:179], v240 offset:49152\n" \
"v_mfma_f32_16x16x32_f16 v[36:39], v[140:143], v[160:163], v[36:39]\n" \
"v_pk_add_f16 v160, v160, v164\n" \
"v_pk_add_f16 v161, v161, v165\n" \
"ds_read_b64_tr_b16 v[132:133], v242 offset:32768\n" \
"v_mfma_f32_16x16x32_f16 v[28:31], v[152:155], v[164:167], v[28:31]\n" \
"v_pk_add_f16 v162, v162, v166\n" \
"v_pk_add_f16 v163, v163, v167\n" \
"v_cvt_pk_f16_f32 v184, v184, v185\n" \
"v_mfma_f32_16x16x32_f16 v[40:43], v[156:159], v[164:167], v[40:43]\n" \
"v_cvt_pk_f16_f32 v185, v186, v187\n" \
"v_cvt_pk_f16_f32 v186, v188, v189\n" \
"ds_read_b128 v[180:183], v240 offset:49664\n" \
"v_mfma_f32_16x16x32_f16 v[32:35], v[120:123], v[160:163], v[32:35]\n" \
"v_cvt_pk_f16_f32 v187, v190, v191\n" \
"v_add_u32 v184, s91, v184\n" \
"buffer_load_dwordx4 v232, s[36:39], s80 offen offset:1024 sc1 lds\n" \
"v_mfma_f32_16x16x32_f16 v[44:47], v[124:127], v[160:163], v[44:47]\n" \
"v_add_u32 v185, s91, v185\n" \
"v_add_u32 v186, s91, v186\n" \
"ds_read_b64_tr_b16 v[134:135], v242 offset:40960\n" \
"v_mfma_f32_16x16x32_f16 v[48:51], v[136:139], v[168:171], v[48:51]\n" \
"v_add_u32 v187, s91, v187\n" \
"v_and_b32 v184, s92, v184\n" \
"ds_read_b128 v[160:163], v240 offset:40960\n" \
"v_mfma_f32_16x16x32_f16 v[60:63], v[140:143], v[168:171], v[60:63]\n" \
"s_waitcnt lgkmcnt(4)\n" \
"v_pk_add_f16 v168, v168, v172\n" \
"v_pk_add_f16 v169, v169, v173\n" \
"v_and_b32 v185, s92, v185\n" \
"v_mfma_f32_16x16x32_f16 v[52:55], v[152:155], v[172:175], v[52:55]\n" \
"v_pk_add_f16 v170, v170, v174\n" \
"v_pk_add_f16 v171, v171, v175\n" \
"v_and_b32 v186, s92, v186\n" \
"v_and_b32 v187, s92, v187\n" \
"v_mfma_f32_16x16x32_f16 v[64:67], v[156:159], v[172:175], v[64:67]\n" \
"ds_read_b128 v[164:167], v240 offset:41472\n" \
"buffer_store_dwordx4 v[184:187], v234, s[36:39], s82 offen offset:1024 sc1\n" \
"ds_read_b64_tr_b16 v[144:145], v241 offset:33280\n" \
"v_mfma_f32_16x16x32_f16 v[56:59], v[120:123], v[168:171], v[56:59]\n" \
"buffer_load_dwordx4 v232, s[36:39], s80 offen offset:2048 sc1 lds\n" \
"v_mfma_f32_16x16x32_f16 v[68:71], v[124:127], v[168:171], v[68:71]\n" \
"s_setprio 0\n" \
"ds_read_b64_tr_b16 v[146:147], v241 offset:41472\n" \
"ds_read_b64_tr_b16 v[148:149], v242 offset:33280\n" \
"v_mfma_f32_16x16x32_f16 v[72:75], v[136:139], v[176:179], v[72:75]\n" \
"ds_read_b128 v[168:171], v240 offset:43008\n" \
"ds_read_b64_tr_b16 v[150:151], v242 offset:41472\n" \
"v_mfma_f32_16x16x32_f16 v[84:87], v[140:143], v[176:179], v[84:87]\n" \
"s_waitcnt lgkmcnt(6)\n" \
"v_pk_add_f16 v176, v176, v180\n" \
"v_pk_add_f16 v177, v177, v181\n" \
"v_mfma_f32_16x16x32_f16 v[76:79], v[152:155], v[180:183], v[76:79]\n" \
"v_pk_add_f16 v178, v178, v182\n" \
"v_pk_add_f16 v179, v179, v183\n" \
"v_mfma_f32_16x16x32_f16 v[88:91], v[156:159], v[180:183], v[88:91]\n" \
"ds_read_b128 v[172:175], v240 offset:43520\n" \
"v_mfma_f32_16x16x32_f16 v[80:83], v[120:123], v[176:179], v[80:83]\n" \
"buffer_load_dwordx4 v232, s[36:39], s80 offen offset:3072 sc1 lds\n" \
"v_mfma_f32_16x16x32_f16 v[92:95], v[124:127], v[176:179], v[92:95]\n" \
"v_mfma_f32_16x16x32_f16 v[96:99], v[136:139], v[160:163], v[96:99]\n" \
"s_waitcnt lgkmcnt(5)\n" \
"v_pk_add_f16 v136, v128, v144\n" \
"v_pk_add_f16 v137, v129, v145\n" \
"ds_read_b128 v[176:179], v240 offset:45056\n" \
"v_mfma_f32_16x16x32_f16 v[108:111], v[140:143], v[160:163], v[108:111]\n" \
"v_pk_add_f16 v160, v160, v164\n" \
"v_pk_add_f16 v161, v161, v165\n" \
"ds_read_b128 v[180:183], v240 offset:45568\n" \
"v_mfma_f32_16x16x32_f16 v[100:103], v[152:155], v[164:167], v[100:103]\n" \
"v_pk_add_f16 v162, v162, v166\n" \
"v_pk_add_f16 v163, v163, v167\n" \
"s_add_i32 m0, s45, 0x5000\n" \
"s_add_i32 s81, s47, 0x2400000\n" \
"v_mfma_f32_16x16x32_f16 v[112:115], v[156:159], v[164:167], v[112:115]\n" \
"s_waitcnt lgkmcnt(3)\n" \
"v_pk_add_f16 v138, v130, v146\n" \
"v_pk_add_f16 v139, v131, v147\n" \
"buffer_load_dwordx4 v232, s[36:39], s81 offen sc1 lds\n" \
"v_mfma_f32_16x16x32_f16 v[104:107], v[120:123], v[160:163], v[104:107]\n" \
"v_pk_add_f16 v140, v132, v148\n" \
"v_pk_add_f16 v141, v133, v149\n" \
"v_mfma_f32_16x16x32_f16 v[116:119], v[124:127], v[160:163], v[116:119]\n" \
"s_setprio 1\n" \
"v_pk_add_f16 v142, v134, v150\n" \
"v_pk_add_f16 v143, v135, v151\n" \
"s_waitcnt vmcnt(7) lgkmcnt(0)\n" \
"s_barrier\n" \
"v_mfma_f32_16x16x32_f16 v[0:3], v[128:131], v[168:171], v[0:3]\n" \
"ds_read_b128 v[160:163], v240 offset:6144\n" \
"ds_read_b64_tr_b16 v[184:185], v236 offset:16384\n" \
"ds_read_b64_tr_b16 v[186:187], v236 offset:16384\n" \
"v_mfma_f32_16x16x32_f16 v[12:15], v[132:135], v[168:171], v[12:15]\n" \
"v_pk_add_f16 v168, v168, v172\n" \
"v_pk_add_f16 v169, v169, v173\n" \
"ds_read_b64_tr_b16 v[188:189], v237 offset:16384\n" \
"v_mfma_f32_16x16x32_f16 v[4:7], v[144:147], v[172:175], v[4:7]\n" \
"v_pk_add_f16 v170, v170, v174\n" \
"v_pk_add_f16 v171, v171, v175\n" \
"ds_read_b64_tr_b16 v[190:191], v237 offset:16384\n" \
"v_mfma_f32_16x16x32_f16 v[16:19], v[148:151], v[172:175], v[16:19]\n" \
"ds_read_b128 v[164:167], v240 offset:6656\n" \
"s_add_i32 m0, s44, 0xa000\n" \
"s_add_i32 s80, s46, 0x2800000\n" \
"v_mfma_f32_16x16x32_f16 v[8:11], v[136:139], v[168:171], v[8:11]\n" \
"buffer_load_dwordx4 v232, s[36:39], s80 offen sc1 lds\n" \
"s_waitcnt lgkmcnt(3)\n" \
"v_mfma_f32_16x16x32_bf16 v[184:187], v[184:187], v[192:195], 0\n" \
"v_mfma_f32_16x16x32_f16 v[20:23], v[140:143], v[168:171], v[20:23]\n" \
"ds_read_b64_tr_b16 v[120:121], v241 offset:0\n" \
"ds_read_b64_tr_b16 v[122:123], v241 offset:8192\n" \
"s_waitcnt lgkmcnt(3)\n" \
"v_mfma_f32_16x16x32_bf16 v[188:191], v[188:191], v[192:195], 0\n" \
"v_mfma_f32_16x16x32_f16 v[24:27], v[128:131], v[176:179], v[24:27]\n" \
"ds_read_b128 v[168:171], v240 offset:8192\n" \
"v_mfma_f32_16x16x32_f16 v[36:39], v[132:135], v[176:179], v[36:39]\n" \
"v_pk_add_f16 v176, v176, v180\n" \
"v_pk_add_f16 v177, v177, v181\n" \
"ds_read_b64_tr_b16 v[124:125], v242 offset:0\n" \
"v_mfma_f32_16x16x32_f16 v[28:31], v[144:147], v[180:183], v[28:31]\n" \
"v_pk_add_f16 v178, v178, v182\n" \
"v_pk_add_f16 v179, v179, v183\n" \
"v_cvt_pk_f16_f32 v184, v184, v185\n" \
"v_mfma_f32_16x16x32_f16 v[40:43], v[148:151], v[180:183], v[40:43]\n" \
"v_cvt_pk_f16_f32 v185, v186, v187\n" \
"v_cvt_pk_f16_f32 v186, v188, v189\n" \
"ds_read_b128 v[172:175], v240 offset:8704\n" \
"v_mfma_f32_16x16x32_f16 v[32:35], v[136:139], v[176:179], v[32:35]\n" \
"v_cvt_pk_f16_f32 v187, v190, v191\n" \
"v_add_u32 v184, s91, v184\n" \
"buffer_load_dwordx4 v232, s[36:39], s80 offen offset:1024 sc1 lds\n" \
"v_mfma_f32_16x16x32_f16 v[44:47], v[140:143], v[176:179], v[44:47]\n" \
"v_add_u32 v185, s91, v185\n" \
"v_add_u32 v186, s91, v186\n" \
"ds_read_b64_tr_b16 v[126:127], v242 offset:8192\n" \
"v_mfma_f32_16x16x32_f16 v[48:51], v[128:131], v[160:163], v[48:51]\n" \
"v_add_u32 v187, s91, v187\n" \
"v_and_b32 v184, s92, v184\n" \
"ds_read_b128 v[176:179], v240 offset:0\n" \
"v_mfma_f32_16x16x32_f16 v[60:63], v[132:135], v[160:163], v[60:63]\n" \
"s_waitcnt lgkmcnt(4)\n" \
"v_pk_add_f16 v160, v160, v164\n" \
"v_pk_add_f16 v161, v161, v165\n" \
"v_and_b32 v185, s92, v185\n" \
"s_add_i32 s82, s49, 0x4800000\n" \
"v_mfma_f32_16x16x32_f16 v[52:55], v[144:147], v[164:167], v[52:55]\n" \
"v_pk_add_f16 v162, v162, v166\n" \
"v_pk_add_f16 v163, v163, v167\n" \
"v_and_b32 v186, s92, v186\n" \
"v_and_b32 v187, s92, v187\n" \
"v_mfma_f32_16x16x32_f16 v[64:67], v[148:151], v[164:167], v[64:67]\n" \
"ds_read_b128 v[180:183], v240 offset:512\n" \
"buffer_store_dwordx4 v[184:187], v234, s[36:39], s82 offen sc1\n" \
"ds_read_b64_tr_b16 v[152:153], v241 offset:512\n" \
"v_mfma_f32_16x16x32_f16 v[56:59], v[136:139], v[160:163], v[56:59]\n" \
"buffer_load_dwordx4 v232, s[36:39], s80 offen offset:2048 sc1 lds\n" \
"v_mfma_f32_16x16x32_f16 v[68:71], v[140:143], v[160:163], v[68:71]\n" \
"s_setprio 0\n" \
"ds_read_b64_tr_b16 v[154:155], v241 offset:8704\n" \
"ds_read_b64_tr_b16 v[156:157], v242 offset:512\n" \
"v_mfma_f32_16x16x32_f16 v[72:75], v[128:131], v[168:171], v[72:75]\n" \
"ds_read_b128 v[160:163], v240 offset:2048\n" \
"ds_read_b64_tr_b16 v[158:159], v242 offset:8704\n" \
"v_mfma_f32_16x16x32_f16 v[84:87], v[132:135], v[168:171], v[84:87]\n" \
"s_waitcnt lgkmcnt(6)\n" \
"v_pk_add_f16 v168, v168, v172\n" \
"v_pk_add_f16 v169, v169, v173\n" \
"v_mfma_f32_16x16x32_f16 v[76:79], v[144:147], v[172:175], v[76:79]\n" \
"v_pk_add_f16 v170, v170, v174\n" \
"v_pk_add_f16 v171, v171, v175\n" \
"v_mfma_f32_16x16x32_f16 v[88:91], v[148:151], v[172:175], v[88:91]\n" \
"ds_read_b128 v[164:167], v240 offset:2560\n" \
"v_mfma_f32_16x16x32_f16 v[80:83], v[136:139], v[168:171], v[80:83]\n" \
"buffer_load_dwordx4 v232, s[36:39], s80 offen offset:3072 sc1 lds\n" \
"v_mfma_f32_16x16x32_f16 v[92:95], v[140:143], v[168:171], v[92:95]\n" \
"v_mfma_f32_16x16x32_f16 v[96:99], v[128:131], v[176:179], v[96:99]\n" \
"s_waitcnt lgkmcnt(5)\n" \
"v_pk_add_f16 v128, v120, v152\n" \
"v_pk_add_f16 v129, v121, v153\n" \
"ds_read_b128 v[168:171], v240 offset:4096\n" \
"v_mfma_f32_16x16x32_f16 v[108:111], v[132:135], v[176:179], v[108:111]\n" \
"v_pk_add_f16 v176, v176, v180\n" \
"v_pk_add_f16 v177, v177, v181\n" \
"ds_read_b128 v[172:175], v240 offset:4608\n" \
"v_mfma_f32_16x16x32_f16 v[100:103], v[144:147], v[180:183], v[100:103]\n" \
"v_pk_add_f16 v178, v178, v182\n" \
"v_pk_add_f16 v179, v179, v183\n" \
"s_add_i32 m0, s45, 0xa000\n" \
"s_add_i32 s81, s47, 0x2800000\n" \
"v_mfma_f32_16x16x32_f16 v[112:115], v[148:151], v[180:183], v[112:115]\n" \
"s_waitcnt lgkmcnt(3)\n" \
"v_pk_add_f16 v130, v122, v154\n" \
"v_pk_add_f16 v131, v123, v155\n" \
"buffer_load_dwordx4 v232, s[36:39], s81 offen sc1 lds\n" \
"v_mfma_f32_16x16x32_f16 v[104:107], v[136:139], v[176:179], v[104:107]\n" \
"v_pk_add_f16 v132, v124, v156\n" \
"v_pk_add_f16 v133, v125, v157\n" \
"v_mfma_f32_16x16x32_f16 v[116:119], v[140:143], v[176:179], v[116:119]\n" \
"s_setprio 1\n" \
"v_pk_add_f16 v134, v126, v158\n" \
"v_pk_add_f16 v135, v127, v159\n" \
"s_waitcnt vmcnt(6) lgkmcnt(0)\n" \
"s_barrier\n" \
"v_mfma_f32_16x16x32_f16 v[0:3], v[120:123], v[160:163], v[0:3]\n" \
"ds_read_b128 v[176:179], v240 offset:26624\n" \
"ds_read_b64_tr_b16 v[184:185], v238 offset:16384\n" \
"ds_read_b64_tr_b16 v[186:187], v238 offset:16384\n" \
"v_mfma_f32_16x16x32_f16 v[12:15], v[124:127], v[160:163], v[12:15]\n" \
"v_pk_add_f16 v160, v160, v164\n" \
"v_pk_add_f16 v161, v161, v165\n" \
"ds_read_b64_tr_b16 v[188:189], v239 offset:16384\n" \
"v_mfma_f32_16x16x32_f16 v[4:7], v[152:155], v[164:167], v[4:7]\n" \
"v_pk_add_f16 v162, v162, v166\n" \
"v_pk_add_f16 v163, v163, v167\n" \
"ds_read_b64_tr_b16 v[190:191], v239 offset:16384\n" \
"v_mfma_f32_16x16x32_f16 v[16:19], v[156:159], v[164:167], v[16:19]\n" \
"ds_read_b128 v[180:183], v240 offset:27136\n" \
"s_add_i32 m0, s44, 0x0\n" \
"s_add_i32 s80, s46, 0x2c00000\n" \
"v_mfma_f32_16x16x32_f16 v[8:11], v[128:131], v[160:163], v[8:11]\n" \
"buffer_load_dwordx4 v232, s[36:39], s80 offen sc1 lds\n" \
"s_waitcnt lgkmcnt(3)\n" \
"v_mfma_f32_16x16x32_bf16 v[184:187], v[184:187], v[192:195], 0\n" \
"v_mfma_f32_16x16x32_f16 v[20:23], v[132:135], v[160:163], v[20:23]\n" \
"ds_read_b64_tr_b16 v[136:137], v241 offset:16384\n" \
"ds_read_b64_tr_b16 v[138:139], v241 offset:24576\n" \
"s_waitcnt lgkmcnt(3)\n" \
"v_mfma_f32_16x16x32_bf16 v[188:191], v[188:191], v[192:195], 0\n" \
"v_mfma_f32_16x16x32_f16 v[24:27], v[120:123], v[168:171], v[24:27]\n" \
"ds_read_b128 v[160:163], v240 offset:28672\n" \
"v_mfma_f32_16x16x32_f16 v[36:39], v[124:127], v[168:171], v[36:39]\n" \
"v_pk_add_f16 v168, v168, v172\n" \
"v_pk_add_f16 v169, v169, v173\n" \
"ds_read_b64_tr_b16 v[140:141], v242 offset:16384\n" \
"v_mfma_f32_16x16x32_f16 v[28:31], v[152:155], v[172:175], v[28:31]\n" \
"v_pk_add_f16 v170, v170, v174\n" \
"v_pk_add_f16 v171, v171, v175\n" \
"v_cvt_pk_f16_f32 v184, v184, v185\n" \
"v_mfma_f32_16x16x32_f16 v[40:43], v[156:159], v[172:175], v[40:43]\n" \
"v_cvt_pk_f16_f32 v185, v186, v187\n" \
"v_cvt_pk_f16_f32 v186, v188, v189\n" \
"ds_read_b128 v[164:167], v240 offset:29184\n" \
"v_mfma_f32_16x16x32_f16 v[32:35], v[128:131], v[168:171], v[32:35]\n" \
"v_cvt_pk_f16_f32 v187, v190, v191\n" \
"v_add_u32 v184, s91, v184\n" \
"buffer_load_dwordx4 v232, s[36:39], s80 offen offset:1024 sc1 lds\n" \
"v_mfma_f32_16x16x32_f16 v[44:47], v[132:135], v[168:171], v[44:47]\n" \
"v_add_u32 v185, s91, v185\n" \
"v_add_u32 v186, s91, v186\n" \
"ds_read_b64_tr_b16 v[142:143], v242 offset:24576\n" \
"v_mfma_f32_16x16x32_f16 v[48:51], v[120:123], v[176:179], v[48:51]\n" \
"v_add_u32 v187, s91, v187\n" \
"v_and_b32 v184, s92, v184\n" \
"ds_read_b128 v[168:171], v240 offset:20480\n" \
"v_mfma_f32_16x16x32_f16 v[60:63], v[124:127], v[176:179], v[60:63]\n" \
"s_waitcnt lgkmcnt(4)\n" \
"v_pk_add_f16 v176, v176, v180\n" \
"v_pk_add_f16 v177, v177, v181\n" \
"v_and_b32 v185, s92, v185\n" \
"v_mfma_f32_16x16x32_f16 v[52:55], v[152:155], v[180:183], v[52:55]\n" \
"v_pk_add_f16 v178, v178, v182\n" \
"v_pk_add_f16 v179, v179, v183\n" \
"v_and_b32 v186, s92, v186\n" \
"v_and_b32 v187, s92, v187\n" \
"v_mfma_f32_16x16x32_f16 v[64:67], v[156:159], v[180:183], v[64:67]\n" \
"ds_read_b128 v[172:175], v240 offset:20992\n" \
"buffer_store_dwordx4 v[184:187], v234, s[36:39], s82 offen offset:1024 sc1\n" \
"ds_read_b64_tr_b16 v[144:145], v241 offset:16896\n" \
"v_mfma_f32_16x16x32_f16 v[56:59], v[128:131], v[176:179], v[56:59]\n" \
"buffer_load_dwordx4 v232, s[36:39], s80 offen offset:2048 sc1 lds\n" \
"v_mfma_f32_16x16x32_f16 v[68:71], v[132:135], v[176:179], v[68:71]\n" \
"s_setprio 0\n" \
"ds_read_b64_tr_b16 v[146:147], v241 offset:25088\n" \
"ds_read_b64_tr_b16 v[148:149], v242 offset:16896\n" \
"v_mfma_f32_16x16x32_f16 v[72:75], v[120:123], v[160:163], v[72:75]\n" \
"ds_read_b128 v[176:179], v240 offset:22528\n" \
"ds_read_b64_tr_b16 v[150:151], v242 offset:25088\n" \
"v_mfma_f32_16x16x32_f16 v[84:87], v[124:127], v[160:163], v[84:87]\n" \
"s_waitcnt lgkmcnt(6)\n" \
"v_pk_add_f16 v160, v160, v164\n" \
"v_pk_add_f16 v161, v161, v165\n" \
"v_mfma_f32_16x16x32_f16 v[76:79], v[152:155], v[164:167], v[76:79]\n" \
"v_pk_add_f16 v162, v162, v166\n" \
"v_pk_add_f16 v163, v163, v167\n" \
"v_mfma_f32_16x16x32_f16 v[88:91], v[156:159], v[164:167], v[88:91]\n" \
"ds_read_b128 v[180:183], v240 offset:23040\n" \
"v_mfma_f32_16x16x32_f16 v[80:83], v[128:131], v[160:163], v[80:83]\n" \
"buffer_load_dwordx4 v232, s[36:39], s80 offen offset:3072 sc1 lds\n" \
"v_mfma_f32_16x16x32_f16 v[92:95], v[132:135], v[160:163], v[92:95]\n" \
"v_mfma_f32_16x16x32_f16 v[96:99], v[120:123], v[168:171], v[96:99]\n" \
"s_waitcnt lgkmcnt(5)\n" \
"v_pk_add_f16 v120, v136, v144\n" \
"v_pk_add_f16 v121, v137, v145\n" \
"ds_read_b128 v[160:163], v240 offset:24576\n" \
"v_mfma_f32_16x16x32_f16 v[108:111], v[124:127], v[168:171], v[108:111]\n" \
"v_pk_add_f16 v168, v168, v172\n" \
"v_pk_add_f16 v169, v169, v173\n" \
"ds_read_b128 v[164:167], v240 offset:25088\n" \
"v_mfma_f32_16x16x32_f16 v[100:103], v[152:155], v[172:175], v[100:103]\n" \
"v_pk_add_f16 v170, v170, v174\n" \
"v_pk_add_f16 v171, v171, v175\n" \
"s_add_i32 m0, s45, 0x0\n" \
"s_add_i32 s81, s47, 0x2c00000\n" \
"v_mfma_f32_16x16x32_f16 v[112:115], v[156:159], v[172:175], v[112:115]\n" \
"s_waitcnt lgkmcnt(3)\n" \
"v_pk_add_f16 v122, v138, v146\n" \
"v_pk_add_f16 v123, v139, v147\n" \
"buffer_load_dwordx4 v232, s[36:39], s81 offen sc1 lds\n" \
"v_mfma_f32_16x16x32_f16 v[104:107], v[128:131], v[168:171], v[104:107]\n" \
"v_pk_add_f16 v124, v140, v148\n" \
"v_pk_add_f16 v125, v141, v149\n" \
"v_mfma_f32_16x16x32_f16 v[116:119], v[132:135], v[168:171], v[116:119]\n" \
"s_setprio 1\n" \
"v_pk_add_f16 v126, v142, v150\n" \
"v_pk_add_f16 v127, v143, v151\n" \
"s_waitcnt vmcnt(6) lgkmcnt(0)\n" \
"s_barrier\n" \
"v_mfma_f32_16x16x32_f16 v[0:3], v[136:139], v[176:179], v[0:3]\n" \
"ds_read_b128 v[168:171], v240 offset:47104\n" \
"ds_read_b64_tr_b16 v[184:185], v236 offset:20480\n" \
"ds_read_b64_tr_b16 v[186:187], v236 offset:20480\n" \
"v_mfma_f32_16x16x32_f16 v[12:15], v[140:143], v[176:179], v[12:15]\n" \
"v_pk_add_f16 v176, v176, v180\n" \
"v_pk_add_f16 v177, v177, v181\n" \
"ds_read_b64_tr_b16 v[188:189], v237 offset:20480\n" \
"ds_read_b64_tr_b16 v[190:191], v237 offset:20480\n" \
"v_mfma_f32_16x16x32_f16 v[4:7], v[144:147], v[180:183], v[4:7]\n" \
"v_pk_add_f16 v178, v178, v182\n" \
"v_pk_add_f16 v179, v179, v183\n" \
"buffer_store_dwordx4 v[224:227], v235, s[36:39], s67 offen sc1\n" \
"v_mfma_f32_16x16x32_f16 v[16:19], v[148:151], v[180:183], v[16:19]\n" \
"ds_read_b128 v[172:175], v240 offset:47616\n" \
"s_add_i32 m0, s44, 0x5000\n" \
"s_add_i32 s80, s46, 0x3000000\n" \
"v_mfma_f32_16x16x32_f16 v[8:11], v[120:123], v[176:179], v[8:11]\n" \
"buffer_load_dwordx4 v232, s[36:39], s80 offen sc1 lds\n" \
"s_waitcnt lgkmcnt(3)\n" \
"v_mfma_f32_16x16x32_bf16 v[184:187], v[184:187], v[192:195], 0\n" \
"v_mfma_f32_16x16x32_f16 v[20:23], v[124:127], v[176:179], v[20:23]\n" \
"ds_read_b64_tr_b16 v[128:129], v241 offset:32768\n" \
"ds_read_b64_tr_b16 v[130:131], v241 offset:40960\n" \
"s_waitcnt lgkmcnt(3)\n" \
"v_mfma_f32_16x16x32_bf16 v[188:191], v[188:191], v[192:195], 0\n" \
"v_mfma_f32_16x16x32_f16 v[24:27], v[136:139], v[160:163], v[24:27]\n" \
"ds_read_b128 v[176:179], v240 offset:49152\n" \
"v_mfma_f32_16x16x32_f16 v[36:39], v[140:143], v[160:163], v[36:39]\n" \
"v_pk_add_f16 v160, v160, v164\n" \
"v_pk_add_f16 v161, v161, v165\n" \
"ds_read_b64_tr_b16 v[132:133], v242 offset:32768\n" \
"v_mfma_f32_16x16x32_f16 v[28:31], v[144:147], v[164:167], v[28:31]\n" \
"v_pk_add_f16 v162, v162, v166\n" \
"v_pk_add_f16 v163, v163, v167\n" \
"v_cvt_pk_f16_f32 v184, v184, v185\n" \
"v_mfma_f32_16x16x32_f16 v[40:43], v[148:151], v[164:167], v[40:43]\n" \
"v_cvt_pk_f16_f32 v185, v186, v187\n" \
"v_cvt_pk_f16_f32 v186, v188, v189\n" \
"ds_read_b128 v[180:183], v240 offset:49664\n" \
"v_mfma_f32_16x16x32_f16 v[32:35], v[120:123], v[160:163], v[32:35]\n" \
"v_cvt_pk_f16_f32 v187, v190, v191\n" \
"v_add_u32 v184, s91, v184\n" \
"buffer_load_dwordx4 v232, s[36:39], s80 offen offset:1024 sc1 lds\n" \
"v_mfma_f32_16x16x32_f16 v[44:47], v[124:127], v[160:163], v[44:47]\n" \
"v_add_u32 v185, s91, v185\n" \
"v_add_u32 v186, s91, v186\n" \
"ds_read_b64_tr_b16 v[134:135], v242 offset:40960\n" \
"v_mfma_f32_16x16x32_f16 v[48:51], v[136:139], v[168:171], v[48:51]\n" \
"v_add_u32 v187, s91, v187\n" \
"v_and_b32 v184, s92, v184\n" \
"ds_read_b128 v[160:163], v240 offset:40960\n" \
"v_mfma_f32_16x16x32_f16 v[60:63], v[140:143], v[168:171], v[60:63]\n" \
"s_waitcnt lgkmcnt(4)\n" \
"v_pk_add_f16 v168, v168, v172\n" \
"v_pk_add_f16 v169, v169, v173\n" \
"v_and_b32 v185, s92, v185\n" \
"s_add_i32 s82, s50, 0x5000000\n" \
"v_mfma_f32_16x16x32_f16 v[52:55], v[144:147], v[172:175], v[52:55]\n" \
"v_pk_add_f16 v170, v170, v174\n" \
"v_pk_add_f16 v171, v171, v175\n" \
"v_and_b32 v186, s92, v186\n" \
"v_and_b32 v187, s92, v187\n" \
"v_mfma_f32_16x16x32_f16 v[64:67], v[148:151], v[172:175], v[64:67]\n" \
"ds_read_b128 v[164:167], v240 offset:41472\n" \
"buffer_store_dwordx4 v[184:187], v234, s[36:39], s82 offen sc1\n" \
"ds_read_b64_tr_b16 v[152:153], v241 offset:33280\n" \
"v_mfma_f32_16x16x32_f16 v[56:59], v[120:123], v[168:171], v[56:59]\n" \
"buffer_load_dwordx4 v232, s[36:39], s80 offen offset:2048 sc1 lds\n" \
"v_mfma_f32_16x16x32_f16 v[68:71], v[124:127], v[168:171], v[68:71]\n" \
"s_setprio 0\n" \
"ds_read_b64_tr_b16 v[154:155], v241 offset:41472\n" \
"ds_read_b64_tr_b16 v[156:157], v242 offset:33280\n" \
"v_mfma_f32_16x16x32_f16 v[72:75], v[136:139], v[176:179], v[72:75]\n" \
"ds_read_b128 v[168:171], v240 offset:43008\n" \
"ds_read_b64_tr_b16 v[158:159], v242 offset:41472\n" \
"v_mfma_f32_16x16x32_f16 v[84:87], v[140:143], v[176:179], v[84:87]\n" \
"s_waitcnt lgkmcnt(6)\n" \
"v_pk_add_f16 v176, v176, v180\n" \
"v_pk_add_f16 v177, v177, v181\n" \
"v_mfma_f32_16x16x32_f16 v[76:79], v[144:147], v[180:183], v[76:79]\n" \
"v_pk_add_f16 v178, v178, v182\n" \
"v_pk_add_f16 v179, v179, v183\n" \
"v_mfma_f32_16x16x32_f16 v[88:91], v[148:151], v[180:183], v[88:91]\n" \
"ds_read_b128 v[172:175], v240 offset:43520\n" \
"v_mfma_f32_16x16x32_f16 v[80:83], v[120:123], v[176:179], v[80:83]\n" \
"buffer_load_dwordx4 v232, s[36:39], s80 offen offset:3072 sc1 lds\n" \
"v_mfma_f32_16x16x32_f16 v[92:95], v[124:127], v[176:179], v[92:95]\n" \
"v_mfma_f32_16x16x32_f16 v[96:99], v[136:139], v[160:163], v[96:99]\n" \
"s_waitcnt lgkmcnt(5)\n" \
"v_pk_add_f16 v136, v128, v152\n" \
"v_pk_add_f16 v137, v129, v153\n" \
"ds_read_b128 v[176:179], v240 offset:45056\n" \
"v_mfma_f32_16x16x32_f16 v[108:111], v[140:143], v[160:163], v[108:111]\n" \
"v_pk_add_f16 v160, v160, v164\n" \
"v_pk_add_f16 v161, v161, v165\n" \
"ds_read_b128 v[180:183], v240 offset:45568\n" \
"v_mfma_f32_16x16x32_f16 v[100:103], v[144:147], v[164:167], v[100:103]\n" \
"v_pk_add_f16 v162, v162, v166\n" \
"v_pk_add_f16 v163, v163, v167\n" \
"s_add_i32 m0, s45, 0x5000\n" \
"s_add_i32 s81, s47, 0x3000000\n" \
"v_mfma_f32_16x16x32_f16 v[112:115], v[148:151], v[164:167], v[112:115]\n" \
"s_waitcnt lgkmcnt(3)\n" \
"v_pk_add_f16 v138, v130, v154\n" \
"v_pk_add_f16 v139, v131, v155\n" \
"buffer_load_dwordx4 v232, s[36:39], s81 offen sc1 lds\n" \
"v_mfma_f32_16x16x32_f16 v[104:107], v[120:123], v[160:163], v[104:107]\n" \
"v_pk_add_f16 v140, v132, v156\n" \
"v_pk_add_f16 v141, v133, v157\n" \
"v_mfma_f32_16x16x32_f16 v[116:119], v[124:127], v[160:163], v[116:119]\n" \
"s_setprio 1\n" \
"v_pk_add_f16 v142, v134, v158\n" \
"v_pk_add_f16 v143, v135, v159\n" \
"s_waitcnt vmcnt(7) lgkmcnt(0)\n" \
"s_barrier\n" \
"v_mfma_f32_16x16x32_f16 v[0:3], v[128:131], v[168:171], v[0:3]\n" \
"ds_read_b128 v[160:163], v240 offset:6144\n" \
"ds_read_b64_tr_b16 v[184:185], v238 offset:20480\n" \
"ds_read_b64_tr_b16 v[186:187], v238 offset:20480\n" \
"v_mfma_f32_16x16x32_f16 v[12:15], v[132:135], v[168:171], v[12:15]\n" \
"v_pk_add_f16 v168, v168, v172\n" \
"v_pk_add_f16 v169, v169, v173\n" \
"ds_read_b64_tr_b16 v[188:189], v239 offset:20480\n" \
"v_mfma_f32_16x16x32_f16 v[4:7], v[152:155], v[172:175], v[4:7]\n" \
"v_pk_add_f16 v170, v170, v174\n" \
"v_pk_add_f16 v171, v171, v175\n" \
"ds_read_b64_tr_b16 v[190:191], v239 offset:20480\n" \
"v_mfma_f32_16x16x32_f16 v[16:19], v[156:159], v[172:175], v[16:19]\n" \
"ds_read_b128 v[164:167], v240 offset:6656\n" \
"s_add_i32 m0, s44, 0xa000\n" \
"s_add_i32 s80, s46, 0x3400000\n" \
"v_mfma_f32_16x16x32_f16 v[8:11], v[136:139], v[168:171], v[8:11]\n" \
"buffer_load_dwordx4 v232, s[36:39], s80 offen sc1 lds\n" \
"s_waitcnt lgkmcnt(3)\n" \
"v_mfma_f32_16x16x32_bf16 v[184:187], v[184:187], v[192:195], 0\n" \
"v_mfma_f32_16x16x32_f16 v[20:23], v[140:143], v[168:171], v[20:23]\n" \
"ds_read_b64_tr_b16 v[120:121], v241 offset:0\n" \
"ds_read_b64_tr_b16 v[122:123], v241 offset:8192\n" \
"s_waitcnt lgkmcnt(3)\n" \
"v_mfma_f32_16x16x32_bf16 v[188:191], v[188:191], v[192:195], 0\n" \
"v_mfma_f32_16x16x32_f16 v[24:27], v[128:131], v[176:179], v[24:27]\n" \
"ds_read_b128 v[168:171], v240 offset:8192\n" \
"v_mfma_f32_16x16x32_f16 v[36:39], v[132:135], v[176:179], v[36:39]\n" \
"v_pk_add_f16 v176, v176, v180\n" \
"v_pk_add_f16 v177, v177, v181\n" \
"ds_read_b64_tr_b16 v[124:125], v242 offset:0\n" \
"v_mfma_f32_16x16x32_f16 v[28:31], v[152:155], v[180:183], v[28:31]\n" \
"v_pk_add_f16 v178, v178, v182\n" \
"v_pk_add_f16 v179, v179, v183\n" \
"v_cvt_pk_f16_f32 v184, v184, v185\n" \
"v_mfma_f32_16x16x32_f16 v[40:43], v[156:159], v[180:183], v[40:43]\n" \
"v_cvt_pk_f16_f32 v185, v186, v187\n" \
"v_cvt_pk_f16_f32 v186, v188, v189\n" \
"ds_read_b128 v[172:175], v240 offset:8704\n" \
"v_mfma_f32_16x16x32_f16 v[32:35], v[136:139], v[176:179], v[32:35]\n" \
"v_cvt_pk_f16_f32 v187, v190, v191\n" \
"v_add_u32 v184, s91, v184\n" \
"buffer_load_dwordx4 v232, s[36:39], s80 offen offset:1024 sc1 lds\n" \
"v_mfma_f32_16x16x32_f16 v[44:47], v[140:143], v[176:179], v[44:47]\n" \
"v_add_u32 v185, s91, v185\n" \
"v_add_u32 v186, s91, v186\n" \
"ds_read_b64_tr_b16 v[126:127], v242 offset:8192\n" \
"v_mfma_f32_16x16x32_f16 v[48:51], v[128:131], v[160:163], v[48:51]\n" \
"v_add_u32 v187, s91, v187\n" \
"v_and_b32 v184, s92, v184\n" \
"ds_read_b128 v[176:179], v240 offset:0\n" \
"v_mfma_f32_16x16x32_f16 v[60:63], v[132:135], v[160:163], v[60:63]\n" \
"s_waitcnt lgkmcnt(4)\n" \
"v_pk_add_f16 v160, v160, v164\n" \
"v_pk_add_f16 v161, v161, v165\n" \
"v_and_b32 v185, s92, v185\n" \
"v_mfma_f32_16x16x32_f16 v[52:55], v[152:155], v[164:167], v[52:55]\n" \
"v_pk_add_f16 v162, v162, v166\n" \
"v_pk_add_f16 v163, v163, v167\n" \
"v_and_b32 v186, s92, v186\n" \
"v_and_b32 v187, s92, v187\n" \
"v_mfma_f32_16x16x32_f16 v[64:67], v[156:159], v[164:167], v[64:67]\n" \
"ds_read_b128 v[180:183], v240 offset:512\n" \
"buffer_store_dwordx4 v[184:187], v234, s[36:39], s82 offen offset:1024 sc1\n" \
"ds_read_b64_tr_b16 v[144:145], v241 offset:512\n" \
"v_mfma_f32_16x16x32_f16 v[56:59], v[136:139], v[160:163], v[56:59]\n" \
"buffer_load_dwordx4 v232, s[36:39], s80 offen offset:2048 sc1 lds\n" \
"v_mfma_f32_16x16x32_f16 v[68:71], v[140:143], v[160:163], v[68:71]\n" \
"s_setprio 0\n" \
"ds_read_b64_tr_b16 v[146:147], v241 offset:8704\n" \
"ds_read_b64_tr_b16 v[148:149], v242 offset:512\n" \
"v_mfma_f32_16x16x32_f16 v[72:75], v[128:131], v[168:171], v[72:75]\n" \
"ds_read_b128 v[160:163], v240 offset:2048\n" \
"ds_read_b64_tr_b16 v[150:151], v242 offset:8704\n" \
"v_mfma_f32_16x16x32_f16 v[84:87], v[132:135], v[168:171], v[84:87]\n" \
"s_waitcnt lgkmcnt(6)\n" \
"v_pk_add_f16 v168, v168, v172\n" \
"v_pk_add_f16 v169, v169, v173\n" \
"v_mfma_f32_16x16x32_f16 v[76:79], v[152:155], v[172:175], v[76:79]\n" \
"v_pk_add_f16 v170, v170, v174\n" \
"v_pk_add_f16 v171, v171, v175\n" \
"v_mfma_f32_16x16x32_f16 v[88:91], v[156:159], v[172:175], v[88:91]\n" \
"ds_read_b128 v[164:167], v240 offset:2560\n" \
"v_mfma_f32_16x16x32_f16 v[80:83], v[136:139], v[168:171], v[80:83]\n" \
"buffer_load_dwordx4 v232, s[36:39], s80 offen offset:3072 sc1 lds\n" \
"v_mfma_f32_16x16x32_f16 v[92:95], v[140:143], v[168:171], v[92:95]\n" \
"v_mfma_f32_16x16x32_f16 v[96:99], v[128:131], v[176:179], v[96:99]\n" \
"s_waitcnt lgkmcnt(5)\n" \
"v_pk_add_f16 v128, v120, v144\n" \
"v_pk_add_f16 v129, v121, v145\n" \
"ds_read_b128 v[168:171], v240 offset:4096\n" \
"v_mfma_f32_16x16x32_f16 v[108:111], v[132:135], v[176:179], v[108:111]\n" \
"v_pk_add_f16 v176, v176, v180\n" \
"v_pk_add_f16 v177, v177, v181\n" \
"ds_read_b128 v[172:175], v240 offset:4608\n" \
"v_mfma_f32_16x16x32_f16 v[100:103], v[152:155], v[180:183], v[100:103]\n" \
"v_pk_add_f16 v178, v178, v182\n" \
"v_pk_add_f16 v179, v179, v183\n" \
"s_add_i32 m0, s45, 0xa000\n" \
"s_add_i32 s81, s47, 0x3400000\n" \
"v_mfma_f32_16x16x32_f16 v[112:115], v[156:159], v[180:183], v[112:115]\n" \
"s_waitcnt lgkmcnt(3)\n" \
"v_pk_add_f16 v130, v122, v146\n" \
"v_pk_add_f16 v131, v123, v147\n" \
"buffer_load_dwordx4 v232, s[36:39], s81 offen sc1 lds\n" \
"v_mfma_f32_16x16x32_f16 v[104:107], v[136:139], v[176:179], v[104:107]\n" \
"v_pk_add_f16 v132, v124, v148\n" \
"v_pk_add_f16 v133, v125, v149\n" \
"v_mfma_f32_16x16x32_f16 v[116:119], v[140:143], v[176:179], v[116:119]\n" \
"s_setprio 1\n" \
"v_pk_add_f16 v134, v126, v150\n" \
"v_pk_add_f16 v135, v127, v151\n" \
"s_waitcnt vmcnt(6) lgkmcnt(0)\n" \
"s_barrier\n" \
"v_mfma_f32_16x16x32_f16 v[0:3], v[120:123], v[160:163], v[0:3]\n" \
"ds_read_b128 v[176:179], v240 offset:26624\n" \
"ds_read_b64_tr_b16 v[184:185], v236 offset:24576\n" \
"ds_read_b64_tr_b16 v[186:187], v236 offset:24576\n" \
"v_mfma_f32_16x16x32_f16 v[12:15], v[124:127], v[160:163], v[12:15]\n" \
"v_pk_add_f16 v160, v160, v164\n" \
"v_pk_add_f16 v161, v161, v165\n" \
"s_mov_b32 s84, 0x800c000\n" \
"ds_read_b64_tr_b16 v[188:189], v237 offset:24576\n" \
"v_mfma_f32_16x16x32_f16 v[4:7], v[144:147], v[164:167], v[4:7]\n" \
"v_pk_add_f16 v162, v162, v166\n" \
"v_pk_add_f16 v163, v163, v167\n" \
"buffer_load_dword v253, v252, s[36:39], s84 offen sc1\n" \
"ds_read_b64_tr_b16 v[190:191], v237 offset:24576\n" \
"v_mfma_f32_16x16x32_f16 v[16:19], v[148:151], v[164:167], v[16:19]\n" \
"ds_read_b128 v[180:183], v240 offset:27136\n" \
"s_add_i32 m0, s44, 0x0\n" \
"s_add_i32 s80, s46, 0x3800000\n" \
"v_mfma_f32_16x16x32_f16 v[8:11], v[128:131], v[160:163], v[8:11]\n" \
"buffer_load_dwordx4 v232, s[36:39], s80 offen sc1 lds\n" \
"s_waitcnt lgkmcnt(3)\n" \
"v_mfma_f32_16x16x32_bf16 v[184:187], v[184:187], v[192:195], 0\n" \
"v_mfma_f32_16x16x32_f16 v[20:23], v[132:135], v[160:163], v[20:23]\n" \
"ds_read_b64_tr_b16 v[136:137], v241 offset:16384\n" \
"ds_read_b64_tr_b16 v[138:139], v241 offset:24576\n" \
"s_waitcnt lgkmcnt(3)\n" \
"v_mfma_f32_16x16x32_bf16 v[188:191], v[188:191], v[192:195], 0\n" \
"v_mfma_f32_16x16x32_f16 v[24:27], v[120:123], v[168:171], v[24:27]\n" \
"ds_read_b128 v[160:163], v240 offset:28672\n" \
"v_mfma_f32_16x16x32_f16 v[36:39], v[124:127], v[168:171], v[36:39]\n" \
"v_pk_add_f16 v168, v168, v172\n" \
"v_pk_add_f16 v169, v169, v173\n" \
"ds_read_b64_tr_b16 v[140:141], v242 offset:16384\n" \
"v_mfma_f32_16x16x32_f16 v[28:31], v[144:147], v[172:175], v[28:31]\n" \
"v_pk_add_f16 v170, v170, v174\n" \
"v_pk_add_f16 v171, v171, v175\n" \
"v_cvt_pk_f16_f32 v184, v184, v185\n" \
"v_mfma_f32_16x16x32_f16 v[40:43], v[148:151], v[172:175], v[40:43]\n" \
"v_cvt_pk_f16_f32 v185, v186, v187\n" \
"v_cvt_pk_f16_f32 v186, v188, v189\n" \
"ds_read_b128 v[164:167], v240 offset:29184\n" \
"v_mfma_f32_16x16x32_f16 v[32:35], v[128:131], v[168:171], v[32:35]\n" \
"v_cvt_pk_f16_f32 v187, v190, v191\n" \
"v_add_u32 v184, s91, v184\n" \
"buffer_load_dwordx4 v232, s[36:39], s80 offen offset:1024 sc1 lds\n" \
"v_mfma_f32_16x16x32_f16 v[44:47], v[132:135], v[168:171], v[44:47]\n" \
"v_add_u32 v185, s91, v185\n" \
"v_add_u32 v186, s91, v186\n" \
"ds_read_b64_tr_b16 v[142:143], v242 offset:24576\n" \
"v_mfma_f32_16x16x32_f16 v[48:51], v[120:123], v[176:179], v[48:51]\n" \
"v_add_u32 v187, s91, v187\n" \
"v_and_b32 v184, s92, v184\n" \
"ds_read_b128 v[168:171], v240 offset:20480\n" \
"v_mfma_f32_16x16x32_f16 v[60:63], v[124:127], v[176:179], v[60:63]\n" \
"s_waitcnt lgkmcnt(4)\n" \
"v_pk_add_f16 v176, v176, v180\n" \
"v_pk_add_f16 v177, v177, v181\n" \
"v_and_b32 v185, s92, v185\n" \
"s_add_i32 s82, s51, 0x5800000\n" \
"v_mfma_f32_16x16x32_f16 v[52:55], v[144:147], v[180:183], v[52:55]\n" \
"v_pk_add_f16 v178, v178, v182\n" \
"v_pk_add_f16 v179, v179, v183\n" \
"v_and_b32 v186, s92, v186\n" \
"v_and_b32 v187, s92, v187\n" \
"v_mfma_f32_16x16x32_f16 v[64:67], v[148:151], v[180:183], v[64:67]\n" \
"ds_read_b128 v[172:175], v240 offset:20992\n" \
"buffer_store_dwordx4 v[184:187], v234, s[36:39], s82 offen sc1\n" \
"ds_read_b64_tr_b16 v[152:153], v241 offset:16896\n" \
"v_mfma_f32_16x16x32_f16 v[56:59], v[128:131], v[176:179], v[56:59]\n" \
"buffer_load_dwordx4 v232, s[36:39], s80 offen offset:2048 sc1 lds\n" \
"v_mfma_f32_16x16x32_f16 v[68:71], v[132:135], v[176:179], v[68:71]\n" \
"s_setprio 0\n" \
"ds_read_b64_tr_b16 v[154:155], v241 offset:25088\n" \
"ds_read_b64_tr_b16 v[156:157], v242 offset:16896\n" \
"v_mfma_f32_16x16x32_f16 v[72:75], v[120:123], v[160:163], v[72:75]\n" \
"ds_read_b128 v[176:179], v240 offset:22528\n" \
"ds_read_b64_tr_b16 v[158:159], v242 offset:25088\n" \
"v_mfma_f32_16x16x32_f16 v[84:87], v[124:127], v[160:163], v[84:87]\n" \
"s_waitcnt lgkmcnt(6)\n" \
"v_pk_add_f16 v160, v160, v164\n" \
"v_pk_add_f16 v161, v161, v165\n" \
"v_mfma_f32_16x16x32_f16 v[76:79], v[144:147], v[164:167], v[76:79]\n" \
"v_pk_add_f16 v162, v162, v166\n" \
"v_pk_add_f16 v163, v163, v167\n" \
"v_mfma_f32_16x16x32_f16 v[88:91], v[148:151], v[164:167], v[88:91]\n" \
"ds_read_b128 v[180:183], v240 offset:23040\n" \
"v_mfma_f32_16x16x32_f16 v[80:83], v[128:131], v[160:163], v[80:83]\n" \
"buffer_load_dwordx4 v232, s[36:39], s80 offen offset:3072 sc1 lds\n" \
"v_mfma_f32_16x16x32_f16 v[92:95], v[132:135], v[160:163], v[92:95]\n" \
"v_mfma_f32_16x16x32_f16 v[96:99], v[120:123], v[168:171], v[96:99]\n" \
"s_waitcnt lgkmcnt(5)\n" \
"v_pk_add_f16 v120, v136, v152\n" \
"v_pk_add_f16 v121, v137, v153\n" \
"ds_read_b128 v[160:163], v240 offset:24576\n" \
"v_mfma_f32_16x16x32_f16 v[108:111], v[124:127], v[168:171], v[108:111]\n" \
"v_pk_add_f16 v168, v168, v172\n" \
"v_pk_add_f16 v169, v169, v173\n" \
"ds_read_b128 v[164:167], v240 offset:25088\n" \
"v_mfma_f32_16x16x32_f16 v[100:103], v[144:147], v[172:175], v[100:103]\n" \
"v_pk_add_f16 v170, v170, v174\n" \
"v_pk_add_f16 v171, v171, v175\n" \
"s_waitcnt vmcnt(5)\n" \
"v_cmp_eq_u32 vcc, 0, v253\n" \
"s_cbranch_vccnz .Ls16_%=\n" \
".Lr16_%=:\n" \
"s_add_i32 m0, s45, 0x0\n" \
"s_add_i32 s81, s47, 0x3800000\n" \
"v_mfma_f32_16x16x32_f16 v[112:115], v[148:151], v[172:175], v[112:115]\n" \
"s_waitcnt lgkmcnt(3)\n" \
"v_pk_add_f16 v122, v138, v154\n" \
"v_pk_add_f16 v123, v139, v155\n" \
"buffer_load_dwordx4 v232, s[36:39], s81 offen sc1 lds\n" \
"v_mfma_f32_16x16x32_f16 v[104:107], v[128:131], v[168:171], v[104:107]\n" \
"v_pk_add_f16 v124, v140, v156\n" \
"v_pk_add_f16 v125, v141, v157\n" \
"v_mfma_f32_16x16x32_f16 v[116:119], v[132:135], v[168:171], v[116:119]\n" \
"s_setprio 1\n" \
"v_pk_add_f16 v126, v142, v158\n" \
"v_pk_add_f16 v127, v143, v159\n" \
"s_waitcnt vmcnt(6) lgkmcnt(0)\n" \
"s_barrier\n" \
"v_mfma_f32_16x16x32_f16 v[0:3], v[136:139], v[176:179], v[0:3]\n" \
"ds_read_b128 v[168:171], v240 offset:47104\n" \
"ds_read_b64_tr_b16 v[184:185], v238 offset:24576\n" \
"ds_read_b64_tr_b16 v[186:187], v238 offset:24576\n" \
"v_mfma_f32_16x16x32_f16 v[12:15], v[140:143], v[176:179], v[12:15]\n" \
"v_pk_add_f16 v176, v176, v180\n" \
"v_pk_add_f16 v177, v177, v181\n" \
"ds_read_b64_tr_b16 v[188:189], v239 offset:24576\n" \
"ds_read_b64_tr_b16 v[190:191], v239 offset:24576\n" \
"v_mfma_f32_16x16x32_f16 v[4:7], v[152:155], v[180:183], v[4:7]\n" \
"v_pk_add_f16 v178, v178, v182\n" \
"v_pk_add_f16 v179, v179, v183\n" \
"buffer_store_dwordx4 v[228:231], v235, s[36:39], s68 offen sc1\n" \
"v_mfma_f32_16x16x32_f16 v[16:19], v[156:159], v[180:183], v[16:19]\n" \
"ds_read_b128 v[172:175], v240 offset:47616\n" \
"s_add_i32 m0, s44, 0x5000\n" \
"s_add_i32 s80, s46, 0x3c00000\n" \
"v_mfma_f32_16x16x32_f16 v[8:11], v[120:123], v[176:179], v[8:11]\n" \
"buffer_load_dwordx4 v232, s[36:39], s80 offen sc1 lds\n" \
"s_waitcnt lgkmcnt(3)\n" \
"v_mfma_f32_16x16x32_bf16 v[184:187], v[184:187], v[192:195], 0\n" \
"v_mfma_f32_16x16x32_f16 v[20:23], v[124:127], v[176:179], v[20:23]\n" \
"ds_read_b64_tr_b16 v[128:129], v241 offset:32768\n" \
"ds_read_b64_tr_b16 v[130:131], v241 offset:40960\n" \
"s_waitcnt lgkmcnt(3)\n" \
"v_mfma_f32_16x16x32_bf16 v[188:191], v[188:191], v[192:195], 0\n" \
"v_mfma_f32_16x16x32_f16 v[24:27], v[136:139], v[160:163], v[24:27]\n" \
"ds_read_b128 v[176:179], v240 offset:49152\n" \
"v_mfma_f32_16x16x32_f16 v[36:39], v[140:143], v[160:163], v[36:39]\n" \
"v_pk_add_f16 v160, v160, v164\n" \
"v_pk_add_f16 v161, v161, v165\n" \
"ds_read_b64_tr_b16 v[132:133], v242 offset:32768\n" \
"v_mfma_f32_16x16x32_f16 v[28:31], v[152:155], v[164:167], v[28:31]\n" \
"v_pk_add_f16 v162, v162, v166\n" \
"v_pk_add_f16 v163, v163, v167\n" \
"v_cvt_pk_f16_f32 v184, v184, v185\n" \
"v_mfma_f32_16x16x32_f16 v[40:43], v[156:159], v[164:167], v[40:43]\n" \
"v_cvt_pk_f16_f32 v185, v186, v187\n" \
"v_cvt_pk_f16_f32 v186, v188, v189\n" \
"ds_read_b128 v[180:183], v240 offset:49664\n" \
"v_mfma_f32_16x16x32_f16 v[32:35], v[120:123], v[160:163], v[32:35]\n" \
"v_cvt_pk_f16_f32 v187, v190, v191\n" \
"v_add_u32 v184, s91, v184\n" \
"buffer_load_dwordx4 v232, s[36:39], s80 offen offset:1024 sc1 lds\n" \
"v_mfma_f32_16x16x32_f16 v[44:47], v[124:127], v[160:163], v[44:47]\n" \
"v_add_u32 v185, s91, v185\n" \
"v_add_u32 v186, s91, v186\n" \
"ds_read_b64_tr_b16 v[134:135], v242 offset:40960\n" \
"v_mfma_f32_16x16x32_f16 v[48:51], v[136:139], v[168:171], v[48:51]\n" \
"v_add_u32 v187, s91, v187\n" \
"v_and_b32 v184, s92, v184\n" \
"ds_read_b128 v[160:163], v240 offset:40960\n" \
"v_mfma_f32_16x16x32_f16 v[60:63], v[140:143], v[168:171], v[60:63]\n" \
"s_waitcnt lgkmcnt(4)\n" \
"v_pk_add_f16 v168, v168, v172\n" \
"v_pk_add_f16 v169, v169, v173\n" \
"v_and_b32 v185, s92, v185\n" \
"v_mfma_f32_16x16x32_f16 v[52:55], v[152:155], v[172:175], v[52:55]\n" \
"v_pk_add_f16 v170, v170, v174\n" \
"v_pk_add_f16 v171, v171, v175\n" \
"v_and_b32 v186, s92, v186\n" \
"v_and_b32 v187, s92, v187\n" \
"v_mfma_f32_16x16x32_f16 v[64:67], v[156:159], v[172:175], v[64:67]\n" \
"ds_read_b128 v[164:167], v240 offset:41472\n" \
"buffer_store_dwordx4 v[184:187], v234, s[36:39], s82 offen offset:1024 sc1\n" \
"ds_read_b64_tr_b16 v[144:145], v241 offset:33280\n" \
"v_mfma_f32_16x16x32_f16 v[56:59], v[120:123], v[168:171], v[56:59]\n" \
"buffer_load_dwordx4 v232, s[36:39], s80 offen offset:2048 sc1 lds\n" \
"v_mfma_f32_16x16x32_f16 v[68:71], v[124:127], v[168:171], v[68:71]\n" \
"s_setprio 0\n" \
"ds_read_b64_tr_b16 v[146:147], v241 offset:41472\n" \
"ds_read_b64_tr_b16 v[148:149], v242 offset:33280\n" \
"v_mfma_f32_16x16x32_f16 v[72:75], v[136:139], v[176:179], v[72:75]\n" \
"ds_read_b128 v[168:171], v240 offset:43008\n" \
"ds_read_b64_tr_b16 v[150:151], v242 offset:41472\n" \
"v_mfma_f32_16x16x32_f16 v[84:87], v[140:143], v[176:179], v[84:87]\n" \
"s_waitcnt lgkmcnt(6)\n" \
"v_pk_add_f16 v176, v176, v180\n" \
"v_pk_add_f16 v177, v177, v181\n" \
"v_mfma_f32_16x16x32_f16 v[76:79], v[152:155], v[180:183], v[76:79]\n" \
"v_pk_add_f16 v178, v178, v182\n" \
"v_pk_add_f16 v179, v179, v183\n" \
"v_mfma_f32_16x16x32_f16 v[88:91], v[156:159], v[180:183], v[88:91]\n" \
"ds_read_b128 v[172:175], v240 offset:43520\n" \
"v_mfma_f32_16x16x32_f16 v[80:83], v[120:123], v[176:179], v[80:83]\n" \
"buffer_load_dwordx4 v232, s[36:39], s80 offen offset:3072 sc1 lds\n" \
"v_mfma_f32_16x16x32_f16 v[92:95], v[124:127], v[176:179], v[92:95]\n" \
"v_mfma_f32_16x16x32_f16 v[96:99], v[136:139], v[160:163], v[96:99]\n" \
"s_waitcnt lgkmcnt(5)\n" \
"v_pk_add_f16 v136, v128, v144\n" \
"v_pk_add_f16 v137, v129, v145\n" \
"ds_read_b128 v[176:179], v240 offset:45056\n" \
"v_mfma_f32_16x16x32_f16 v[108:111], v[140:143], v[160:163], v[108:111]\n" \
"v_pk_add_f16 v160, v160, v164\n" \
"v_pk_add_f16 v161, v161, v165\n" \
"ds_read_b128 v[180:183], v240 offset:45568\n" \
"v_mfma_f32_16x16x32_f16 v[100:103], v[152:155], v[164:167], v[100:103]\n" \
"v_pk_add_f16 v162, v162, v166\n" \
"v_pk_add_f16 v163, v163, v167\n" \
"s_add_i32 m0, s45, 0x5000\n" \
"s_add_i32 s81, s47, 0x3c00000\n" \
"v_mfma_f32_16x16x32_f16 v[112:115], v[156:159], v[164:167], v[112:115]\n" \
"s_waitcnt lgkmcnt(3)\n" \
"v_pk_add_f16 v138, v130, v146\n" \
"v_pk_add_f16 v139, v131, v147\n" \
"buffer_load_dwordx4 v232, s[36:39], s81 offen sc1 lds\n" \
"v_mfma_f32_16x16x32_f16 v[104:107], v[120:123], v[160:163], v[104:107]\n" \
"v_pk_add_f16 v140, v132, v148\n" \
"v_pk_add_f16 v141, v133, v149\n" \
"v_mfma_f32_16x16x32_f16 v[116:119], v[124:127], v[160:163], v[116:119]\n" \
"s_setprio 1\n" \
"v_pk_add_f16 v142, v134, v150\n" \
"v_pk_add_f16 v143, v135, v151\n" \
"s_waitcnt vmcnt(7) lgkmcnt(0)\n" \
"s_barrier\n" \
"v_mfma_f32_16x16x32_f16 v[0:3], v[128:131], v[168:171], v[0:3]\n" \
"ds_read_b128 v[160:163], v240 offset:6144\n" \
"ds_read_b64_tr_b16 v[184:185], v236 offset:28672\n" \
"ds_read_b64_tr_b16 v[186:187], v236 offset:28672\n" \
"s_add_i32 m0, s54, 0x0\n" \
"s_mov_b32 s83, 0x0\n" \
"v_mfma_f32_16x16x32_f16 v[12:15], v[132:135], v[168:171], v[12:15]\n" \
"v_pk_add_f16 v168, v168, v172\n" \
"v_pk_add_f16 v169, v169, v173\n" \
"ds_read_b64_tr_b16 v[188:189], v237 offset:28672\n" \
"buffer_load_dwordx4 v233, s[36:39], s83 offen sc1 lds\n" \
"v_mfma_f32_16x16x32_f16 v[4:7], v[144:147], v[172:175], v[4:7]\n" \
"v_pk_add_f16 v170, v170, v174\n" \
"v_pk_add_f16 v171, v171, v175\n" \
"ds_read_b64_tr_b16 v[190:191], v237 offset:28672\n" \
"v_mfma_f32_16x16x32_f16 v[16:19], v[148:151], v[172:175], v[16:19]\n" \
"ds_read_b128 v[164:167], v240 offset:6656\n" \
"s_add_i32 m0, s44, 0xa000\n" \
"s_add_i32 s80, s46, 0x4000000\n" \
"v_mfma_f32_16x16x32_f16 v[8:11], v[136:139], v[168:171], v[8:11]\n" \
"buffer_load_dwordx4 v232, s[36:39], s80 offen sc1 lds\n" \
"s_waitcnt lgkmcnt(3)\n" \
"v_mfma_f32_16x16x32_bf16 v[184:187], v[184:187], v[192:195], 0\n" \
"v_mfma_f32_16x16x32_f16 v[20:23], v[140:143], v[168:171], v[20:23]\n" \
"ds_read_b64_tr_b16 v[120:121], v241 offset:0\n" \
"ds_read_b64_tr_b16 v[122:123], v241 offset:8192\n" \
"s_waitcnt lgkmcnt(3)\n" \
"v_mfma_f32_16x16x32_bf16 v[188:191], v[188:191], v[192:195], 0\n" \
"v_mfma_f32_16x16x32_f16 v[24:27], v[128:131], v[176:179], v[24:27]\n" \
"ds_read_b128 v[168:171], v240 offset:8192\n" \
"v_mfma_f32_16x16x32_f16 v[36:39], v[132:135], v[176:179], v[36:39]\n" \
"v_pk_add_f16 v176, v176, v180\n" \
"v_pk_add_f16 v177, v177, v181\n" \
"ds_read_b64_tr_b16 v[124:125], v242 offset:0\n" \
"v_mfma_f32_16x16x32_f16 v[28:31], v[144:147], v[180:183], v[28:31]\n" \
"v_pk_add_f16 v178, v178, v182\n" \
"v_pk_add_f16 v179, v179, v183\n" \
"v_cvt_pk_f16_f32 v184, v184, v185\n" \
"v_mfma_f32_16x16x32_f16 v[40:43], v[148:151], v[180:183], v[40:43]\n" \
"v_cvt_pk_f16_f32 v185, v186, v187\n" \
"v_cvt_pk_f16_f32 v186, v188, v189\n" \
"ds_read_b128 v[172:175], v240 offset:8704\n" \
"v_mfma_f32_16x16x32_f16 v[32:35], v[136:139], v[176:179], v[32:35]\n" \
"v_cvt_pk_f16_f32 v187, v190, v191\n" \
"v_add_u32 v184, s91, v184\n" \
"buffer_load_dwordx4 v232, s[36:39], s80 offen offset:1024 sc1 lds\n" \
"v_mfma_f32_16x16x32_f16 v[44:47], v[140:143], v[176:179], v[44:47]\n" \
"v_add_u32 v185, s91, v185\n" \
"v_add_u32 v186, s91, v186\n" \
"ds_read_b64_tr_b16 v[126:127], v242 offset:8192\n" \
"v_mfma_f32_16x16x32_f16 v[48:51], v[128:131], v[160:163], v[48:51]\n" \
"v_add_u32 v187, s91, v187\n" \
"v_and_b32 v184, s92, v184\n" \
"ds_read_b128 v[176:179], v240 offset:0\n" \
"v_mfma_f32_16x16x32_f16 v[60:63], v[132:135], v[160:163], v[60:63]\n" \
"s_waitcnt lgkmcnt(4)\n" \
"v_pk_add_f16 v160, v160, v164\n" \
"v_pk_add_f16 v161, v161, v165\n" \
"v_and_b32 v185, s92, v185\n" \
"s_add_i32 s82, s48, 0x6000000\n" \
"v_mfma_f32_16x16x32_f16 v[52:55], v[144:147], v[164:167], v[52:55]\n" \
"v_pk_add_f16 v162, v162, v166\n" \
"v_pk_add_f16 v163, v163, v167\n" \
"v_and_b32 v186, s92, v186\n" \
"v_and_b32 v187, s92, v187\n" \
"v_mfma_f32_16x16x32_f16 v[64:67], v[148:151], v[164:167], v[64:67]\n" \
"ds_read_b128 v[180:183], v240 offset:512\n" \
"buffer_store_dwordx4 v[184:187], v234, s[36:39], s82 offen sc1\n" \
"ds_read_b64_tr_b16 v[152:153], v241 offset:512\n" \
"v_mfma_f32_16x16x32_f16 v[56:59], v[136:139], v[160:163], v[56:59]\n" \
"buffer_load_dwordx4 v232, s[36:39], s80 offen offset:2048 sc1 lds\n" \
"v_mfma_f32_16x16x32_f16 v[68:71], v[140:143], v[160:163], v[68:71]\n" \
"s_setprio 0\n" \
"ds_read_b64_tr_b16 v[154:155], v241 offset:8704\n" \
"ds_read_b64_tr_b16 v[156:157], v242 offset:512\n" \
"v_mfma_f32_16x16x32_f16 v[72:75], v[128:131], v[168:171], v[72:75]\n" \
"ds_read_b128 v[160:163], v240 offset:2048\n" \
"ds_read_b64_tr_b16 v[158:159], v242 offset:8704\n" \
"v_mfma_f32_16x16x32_f16 v[84:87], v[132:135], v[168:171], v[84:87]\n" \
"s_waitcnt lgkmcnt(6)\n" \
"v_pk_add_f16 v168, v168, v172\n" \
"v_pk_add_f16 v169, v169, v173\n" \
"v_mfma_f32_16x16x32_f16 v[76:79], v[144:147], v[172:175], v[76:79]\n" \
"v_pk_add_f16 v170, v170, v174\n" \
"v_pk_add_f16 v171, v171, v175\n" \
"v_mfma_f32_16x16x32_f16 v[88:91], v[148:151], v[172:175], v[88:91]\n" \
"ds_read_b128 v[164:167], v240 offset:2560\n" \
"v_mfma_f32_16x16x32_f16 v[80:83], v[136:139], v[168:171], v[80:83]\n" \
"buffer_load_dwordx4 v232, s[36:39], s80 offen offset:3072 sc1 lds\n" \
"v_mfma_f32_16x16x32_f16 v[92:95], v[140:143], v[168:171], v[92:95]\n" \
"v_mfma_f32_16x16x32_f16 v[96:99], v[128:131], v[176:179], v[96:99]\n" \
"s_waitcnt lgkmcnt(5)\n" \
"v_pk_add_f16 v128, v120, v152\n" \
"v_pk_add_f16 v129, v121, v153\n" \
"ds_read_b128 v[168:171], v240 offset:4096\n" \
"v_mfma_f32_16x16x32_f16 v[108:111], v[132:135], v[176:179], v[108:111]\n" \
"v_pk_add_f16 v176, v176, v180\n" \
"v_pk_add_f16 v177, v177, v181\n" \
"ds_read_b128 v[172:175], v240 offset:4608\n" \
"v_mfma_f32_16x16x32_f16 v[100:103], v[144:147], v[180:183], v[100:103]\n" \
"v_pk_add_f16 v178, v178, v182\n" \
"v_pk_add_f16 v179, v179, v183\n" \
"s_add_i32 m0, s45, 0xa000\n" \
"s_add_i32 s81, s47, 0x4000000\n" \
"v_mfma_f32_16x16x32_f16 v[112:115], v[148:151], v[180:183], v[112:115]\n" \
"s_waitcnt lgkmcnt(3)\n" \
"v_pk_add_f16 v130, v122, v154\n" \
"v_pk_add_f16 v131, v123, v155\n" \
"buffer_load_dwordx4 v232, s[36:39], s81 offen sc1 lds\n" \
"v_mfma_f32_16x16x32_f16 v[104:107], v[136:139], v[176:179], v[104:107]\n" \
"v_pk_add_f16 v132, v124, v156\n" \
"v_pk_add_f16 v133, v125, v157\n" \
"v_mfma_f32_16x16x32_f16 v[116:119], v[140:143], v[176:179], v[116:119]\n" \
"s_setprio 1\n" \
"v_pk_add_f16 v134, v126, v158\n" \
"v_pk_add_f16 v135, v127, v159\n" \
"s_waitcnt vmcnt(7) lgkmcnt(0)\n" \
"s_barrier\n" \
"v_mfma_f32_16x16x32_f16 v[0:3], v[120:123], v[160:163], v[0:3]\n" \
"ds_read_b128 v[176:179], v240 offset:26624\n" \
"ds_read_b64_tr_b16 v[184:185], v238 offset:28672\n" \
"ds_read_b64_tr_b16 v[186:187], v238 offset:28672\n" \
"v_mfma_f32_16x16x32_f16 v[12:15], v[124:127], v[160:163], v[12:15]\n" \
"v_pk_add_f16 v160, v160, v164\n" \
"v_pk_add_f16 v161, v161, v165\n" \
"ds_read_b64_tr_b16 v[188:189], v239 offset:28672\n" \
"v_mfma_f32_16x16x32_f16 v[4:7], v[152:155], v[164:167], v[4:7]\n" \
"v_pk_add_f16 v162, v162, v166\n" \
"v_pk_add_f16 v163, v163, v167\n" \
"ds_read_b64_tr_b16 v[190:191], v239 offset:28672\n" \
"v_mfma_f32_16x16x32_f16 v[16:19], v[156:159], v[164:167], v[16:19]\n" \
"ds_read_b128 v[180:183], v240 offset:27136\n" \
"s_add_i32 m0, s44, 0x0\n" \
"s_add_i32 s80, s46, 0x4400000\n" \
"v_mfma_f32_16x16x32_f16 v[8:11], v[128:131], v[160:163], v[8:11]\n" \
"buffer_load_dwordx4 v232, s[36:39], s80 offen sc1 lds\n" \
"s_waitcnt lgkmcnt(3)\n" \
"v_mfma_f32_16x16x32_bf16 v[184:187], v[184:187], v[192:195], 0\n" \
"v_mfma_f32_16x16x32_f16 v[20:23], v[132:135], v[160:163], v[20:23]\n" \
"ds_read_b64_tr_b16 v[136:137], v241 offset:16384\n" \
"ds_read_b64_tr_b16 v[138:139], v241 offset:24576\n" \
"s_waitcnt lgkmcnt(3)\n" \
"v_mfma_f32_16x16x32_bf16 v[188:191], v[188:191], v[192:195], 0\n" \
"v_mfma_f32_16x16x32_f16 v[24:27], v[120:123], v[168:171], v[24:27]\n" \
"ds_read_b128 v[160:163], v240 offset:28672\n" \
"v_mfma_f32_16x16x32_f16 v[36:39], v[124:127], v[168:171], v[36:39]\n" \
"v_pk_add_f16 v168, v168, v172\n" \
"v_pk_add_f16 v169, v169, v173\n" \
"ds_read_b64_tr_b16 v[140:141], v242 offset:16384\n" \
"v_mfma_f32_16x16x32_f16 v[28:31], v[152:155], v[172:175], v[28:31]\n" \
"v_pk_add_f16 v170, v170, v174\n" \
"v_pk_add_f16 v171, v171, v175\n" \
"v_cvt_pk_f16_f32 v184, v184, v185\n" \
"v_mfma_f32_16x16x32_f16 v[40:43], v[156:159], v[172:175], v[40:43]\n" \
"v_cvt_pk_f16_f32 v185, v186, v187\n" \
"v_cvt_pk_f16_f32 v186, v188, v189\n" \
"ds_read_b128 v[164:167], v240 offset:29184\n" \
"v_mfma_f32_16x16x32_f16 v[32:35], v[128:131], v[168:171], v[32:35]\n" \
"v_cvt_pk_f16_f32 v187, v190, v191\n" \
"v_add_u32 v184, s91, v184\n" \
"buffer_load_dwordx4 v232, s[36:39], s80 offen offset:1024 sc1 lds\n" \
"v_mfma_f32_16x16x32_f16 v[44:47], v[132:135], v[168:171], v[44:47]\n" \
"v_add_u32 v185, s91, v185\n" \
"v_add_u32 v186, s91, v186\n" \
"ds_read_b64_tr_b16 v[142:143], v242 offset:24576\n" \
"v_mfma_f32_16x16x32_f16 v[48:51], v[120:123], v[176:179], v[48:51]\n" \
"v_add_u32 v187, s91, v187\n" \
"v_and_b32 v184, s92, v184\n" \
"ds_read_b128 v[168:171], v240 offset:20480\n" \
"v_mfma_f32_16x16x32_f16 v[60:63], v[124:127], v[176:179], v[60:63]\n" \
"s_waitcnt lgkmcnt(4)\n" \
"v_pk_add_f16 v176, v176, v180\n" \
"v_pk_add_f16 v177, v177, v181\n" \
"v_and_b32 v185, s92, v185\n" \
"v_mfma_f32_16x16x32_f16 v[52:55], v[152:155], v[180:183], v[52:55]\n" \
"v_pk_add_f16 v178, v178, v182\n" \
"v_pk_add_f16 v179, v179, v183\n" \
"v_and_b32 v186, s92, v186\n" \
"v_and_b32 v187, s92, v187\n" \
"v_mfma_f32_16x16x32_f16 v[64:67], v[156:159], v[180:183], v[64:67]\n" \
"ds_read_b128 v[172:175], v240 offset:20992\n" \
"buffer_store_dwordx4 v[184:187], v234, s[36:39], s82 offen offset:1024 sc1\n" \
"ds_read_b64_tr_b16 v[144:145], v241 offset:16896\n" \
"v_mfma_f32_16x16x32_f16 v[56:59], v[128:131], v[176:179], v[56:59]\n" \
"buffer_load_dwordx4 v232, s[36:39], s80 offen offset:2048 sc1 lds\n" \
"v_mfma_f32_16x16x32_f16 v[68:71], v[132:135], v[176:179], v[68:71]\n" \
"s_setprio 0\n" \
"ds_read_b64_tr_b16 v[146:147], v241 offset:25088\n" \
"ds_read_b64_tr_b16 v[148:149], v242 offset:16896\n" \
"v_mfma_f32_16x16x32_f16 v[72:75], v[120:123], v[160:163], v[72:75]\n" \
"ds_read_b128 v[176:179], v240 offset:22528\n" \
"ds_read_b64_tr_b16 v[150:151], v242 offset:25088\n" \
"v_mfma_f32_16x16x32_f16 v[84:87], v[124:127], v[160:163], v[84:87]\n" \
"s_waitcnt lgkmcnt(6)\n" \
"v_pk_add_f16 v160, v160, v164\n" \
"v_pk_add_f16 v161, v161, v165\n" \
"v_mfma_f32_16x16x32_f16 v[76:79], v[152:155], v[164:167], v[76:79]\n" \
"v_pk_add_f16 v162, v162, v166\n" \
"v_pk_add_f16 v163, v163, v167\n" \
"v_mfma_f32_16x16x32_f16 v[88:91], v[156:159], v[164:167], v[88:91]\n" \
"ds_read_b128 v[180:183], v240 offset:23040\n" \
"v_mfma_f32_16x16x32_f16 v[80:83], v[128:131], v[160:163], v[80:83]\n" \
"buffer_load_dwordx4 v232, s[36:39], s80 offen offset:3072 sc1 lds\n" \
"v_mfma_f32_16x16x32_f16 v[92:95], v[132:135], v[160:163], v[92:95]\n" \
"v_mfma_f32_16x16x32_f16 v[96:99], v[120:123], v[168:171], v[96:99]\n" \
"s_waitcnt lgkmcnt(5)\n" \
"v_pk_add_f16 v120, v136, v144\n" \
"v_pk_add_f16 v121, v137, v145\n" \
"ds_read_b128 v[160:163], v240 offset:24576\n" \
"v_mfma_f32_16x16x32_f16 v[108:111], v[124:127], v[168:171], v[108:111]\n" \
"v_pk_add_f16 v168, v168, v172\n" \
"v_pk_add_f16 v169, v169, v173\n" \
"ds_read_b128 v[164:167], v240 offset:25088\n" \
"v_mfma_f32_16x16x32_f16 v[100:103], v[152:155], v[172:175], v[100:103]\n" \
"v_pk_add_f16 v170, v170, v174\n" \
"v_pk_add_f16 v171, v171, v175\n" \
"s_add_i32 m0, s45, 0x0\n" \
"s_add_i32 s81, s47, 0x4400000\n" \
"v_mfma_f32_16x16x32_f16 v[112:115], v[156:159], v[172:175], v[112:115]\n" \
"s_waitcnt lgkmcnt(3)\n" \
"v_pk_add_f16 v122, v138, v146\n" \
"v_pk_add_f16 v123, v139, v147\n" \
"buffer_load_dwordx4 v232, s[36:39], s81 offen sc1 lds\n" \
"v_mfma_f32_16x16x32_f16 v[104:107], v[128:131], v[168:171], v[104:107]\n" \
"v_pk_add_f16 v124, v140, v148\n" \
"v_pk_add_f16 v125, v141, v149\n" \
"v_mfma_f32_16x16x32_f16 v[116:119], v[132:135], v[168:171], v[116:119]\n" \
"s_setprio 1\n" \
"v_pk_add_f16 v126, v142, v150\n" \
"v_pk_add_f16 v127, v143, v151\n" \
"s_waitcnt vmcnt(6) lgkmcnt(0)\n" \
"s_barrier\n" \
"v_mfma_f32_16x16x32_f16 v[0:3], v[136:139], v[176:179], v[0:3]\n" \
"ds_read_b128 v[168:171], v240 offset:47104\n" \
"ds_read_b64_tr_b16 v[184:185], v236 offset:32768\n" \
"ds_read_b64_tr_b16 v[186:187], v236 offset:32768\n" \
"s_add_i32 m0, s54, 0x1000\n" \
"s_mov_b32 s83, 0x100\n" \
"v_mfma_f32_16x16x32_f16 v[12:15], v[140:143], v[176:179], v[12:15]\n" \
"v_pk_add_f16 v176, v176, v180\n" \
"v_pk_add_f16 v177, v177, v181\n" \
"ds_read_b64_tr_b16 v[188:189], v237 offset:32768\n" \
"buffer_load_dwordx4 v233, s[36:39], s83 offen sc1 lds\n" \
"v_mfma_f32_16x16x32_f16 v[4:7], v[144:147], v[180:183], v[4:7]\n" \
"v_pk_add_f16 v178, v178, v182\n" \
"v_pk_add_f16 v179, v179, v183\n" \
"ds_read_b64_tr_b16 v[190:191], v237 offset:32768\n" \
"v_mfma_f32_16x16x32_f16 v[16:19], v[148:151], v[180:183], v[16:19]\n" \
"ds_read_b128 v[172:175], v240 offset:47616\n" \
"s_add_i32 m0, s44, 0x5000\n" \
"s_add_i32 s80, s46, 0x4800000\n" \
"v_mfma_f32_16x16x32_f16 v[8:11], v[120:123], v[176:179], v[8:11]\n" \
"buffer_load_dwordx4 v232, s[36:39], s80 offen sc1 lds\n" \
"s_waitcnt lgkmcnt(3)\n" \
"v_mfma_f32_16x16x32_bf16 v[184:187], v[184:187], v[192:195], 0\n" \
"v_mfma_f32_16x16x32_f16 v[20:23], v[124:127], v[176:179], v[20:23]\n" \
"ds_read_b64_tr_b16 v[128:129], v241 offset:32768\n" \
"ds_read_b64_tr_b16 v[130:131], v241 offset:40960\n" \
"s_waitcnt lgkmcnt(3)\n" \
"v_mfma_f32_16x16x32_bf16 v[188:191], v[188:191], v[192:195], 0\n" \
"v_mfma_f32_16x16x32_f16 v[24:27], v[136:139], v[160:163], v[24:27]\n" \
"ds_read_b128 v[176:179], v240 offset:49152\n" \
"v_mfma_f32_16x16x32_f16 v[36:39], v[140:143], v[160:163], v[36:39]\n" \
"v_pk_add_f16 v160, v160, v164\n" \
"v_pk_add_f16 v161, v161, v165\n" \
"ds_read_b64_tr_b16 v[132:133], v242 offset:32768\n" \
"v_mfma_f32_16x16x32_f16 v[28:31], v[144:147], v[164:167], v[28:31]\n" \
"v_pk_add_f16 v162, v162, v166\n" \
"v_pk_add_f16 v163, v163, v167\n" \
"v_cvt_pk_f16_f32 v184, v184, v185\n" \
"v_mfma_f32_16x16x32_f16 v[40:43], v[148:151], v[164:167], v[40:43]\n" \
"v_cvt_pk_f16_f32 v185, v186, v187\n" \
"v_cvt_pk_f16_f32 v186, v188, v189\n" \
"ds_read_b128 v[180:183], v240 offset:49664\n" \
"v_mfma_f32_16x16x32_f16 v[32:35], v[120:123], v[160:163], v[32:35]\n" \
"v_cvt_pk_f16_f32 v187, v190, v191\n" \
"v_add_u32 v184, s91, v184\n" \
"buffer_load_dwordx4 v232, s[36:39], s80 offen offset:1024 sc1 lds\n" \
"v_mfma_f32_16x16x32_f16 v[44:47], v[124:127], v[160:163], v[44:47]\n" \
"v_add_u32 v185, s91, v185\n" \
"v_add_u32 v186, s91, v186\n" \
"ds_read_b64_tr_b16 v[134:135], v242 offset:40960\n" \
"v_mfma_f32_16x16x32_f16 v[48:51], v[136:139], v[168:171], v[48:51]\n" \
"v_add_u32 v187, s91, v187\n" \
"v_and_b32 v184, s92, v184\n" \
"ds_read_b128 v[160:163], v240 offset:40960\n" \
"v_mfma_f32_16x16x32_f16 v[60:63], v[140:143], v[168:171], v[60:63]\n" \
"s_waitcnt lgkmcnt(4)\n" \
"v_pk_add_f16 v168, v168, v172\n" \
"v_pk_add_f16 v169, v169, v173\n" \
"v_and_b32 v185, s92, v185\n" \
"s_add_i32 s82, s49, 0x6800000\n" \
"v_mfma_f32_16x16x32_f16 v[52:55], v[144:147], v[172:175], v[52:55]\n" \
"v_pk_add_f16 v170, v170, v174\n" \
"v_pk_add_f16 v171, v171, v175\n" \
"v_and_b32 v186, s92, v186\n" \
"v_and_b32 v187, s92, v187\n" \
"v_mfma_f32_16x16x32_f16 v[64:67], v[148:151], v[172:175], v[64:67]\n" \
"ds_read_b128 v[164:167], v240 offset:41472\n" \
"buffer_store_dwordx4 v[184:187], v234, s[36:39], s82 offen sc1\n" \
"ds_read_b64_tr_b16 v[152:153], v241 offset:33280\n" \
"v_mfma_f32_16x16x32_f16 v[56:59], v[120:123], v[168:171], v[56:59]\n" \
"buffer_load_dwordx4 v232, s[36:39], s80 offen offset:2048 sc1 lds\n" \
"v_mfma_f32_16x16x32_f16 v[68:71], v[124:127], v[168:171], v[68:71]\n" \
"s_setprio 0\n" \
"ds_read_b64_tr_b16 v[154:155], v241 offset:41472\n" \
"ds_read_b64_tr_b16 v[156:157], v242 offset:33280\n" \
"v_mfma_f32_16x16x32_f16 v[72:75], v[136:139], v[176:179], v[72:75]\n" \
"ds_read_b128 v[168:171], v240 offset:43008\n" \
"ds_read_b64_tr_b16 v[158:159], v242 offset:41472\n" \
"v_mfma_f32_16x16x32_f16 v[84:87], v[140:143], v[176:179], v[84:87]\n" \
"s_waitcnt lgkmcnt(6)\n" \
"v_pk_add_f16 v176, v176, v180\n" \
"v_pk_add_f16 v177, v177, v181\n" \
"v_mfma_f32_16x16x32_f16 v[76:79], v[144:147], v[180:183], v[76:79]\n" \
"v_pk_add_f16 v178, v178, v182\n" \
"v_pk_add_f16 v179, v179, v183\n" \
"v_mfma_f32_16x16x32_f16 v[88:91], v[148:151], v[180:183], v[88:91]\n" \
"ds_read_b128 v[172:175], v240 offset:43520\n" \
"v_mfma_f32_16x16x32_f16 v[80:83], v[120:123], v[176:179], v[80:83]\n" \
"buffer_load_dwordx4 v232, s[36:39], s80 offen offset:3072 sc1 lds\n" \
"v_mfma_f32_16x16x32_f16 v[92:95], v[124:127], v[176:179], v[92:95]\n" \
"v_mfma_f32_16x16x32_f16 v[96:99], v[136:139], v[160:163], v[96:99]\n" \
"s_waitcnt lgkmcnt(5)\n" \
"v_pk_add_f16 v136, v128, v152\n" \
"v_pk_add_f16 v137, v129, v153\n" \
"ds_read_b128 v[176:179], v240 offset:45056\n" \
"v_mfma_f32_16x16x32_f16 v[108:111], v[140:143], v[160:163], v[108:111]\n" \
"v_pk_add_f16 v160, v160, v164\n" \
"v_pk_add_f16 v161, v161, v165\n" \
"ds_read_b128 v[180:183], v240 offset:45568\n" \
"v_mfma_f32_16x16x32_f16 v[100:103], v[144:147], v[164:167], v[100:103]\n" \
"v_pk_add_f16 v162, v162, v166\n" \
"v_pk_add_f16 v163, v163, v167\n" \
"s_add_i32 m0, s45, 0x5000\n" \
"s_add_i32 s81, s47, 0x4800000\n" \
"v_mfma_f32_16x16x32_f16 v[112:115], v[148:151], v[164:167], v[112:115]\n" \
"s_waitcnt lgkmcnt(3)\n" \
"v_pk_add_f16 v138, v130, v154\n" \
"v_pk_add_f16 v139, v131, v155\n" \
"buffer_load_dwordx4 v232, s[36:39], s81 offen sc1 lds\n" \
"v_mfma_f32_16x16x32_f16 v[104:107], v[120:123], v[160:163], v[104:107]\n" \
"v_pk_add_f16 v140, v132, v156\n" \
"v_pk_add_f16 v141, v133, v157\n" \
"v_mfma_f32_16x16x32_f16 v[116:119], v[124:127], v[160:163], v[116:119]\n" \
"s_setprio 1\n" \
"v_pk_add_f16 v142, v134, v158\n" \
"v_pk_add_f16 v143, v135, v159\n" \
"s_waitcnt vmcnt(7) lgkmcnt(0)\n" \
"s_barrier\n" \
"v_mfma_f32_16x16x32_f16 v[0:3], v[128:131], v[168:171], v[0:3]\n" \
"ds_read_b128 v[160:163], v240 offset:6144\n" \
"ds_read_b64_tr_b16 v[184:185], v238 offset:32768\n" \
"ds_read_b64_tr_b16 v[186:187], v238 offset:32768\n" \
"v_mfma_f32_16x16x32_f16 v[12:15], v[132:135], v[168:171], v[12:15]\n" \
"v_pk_add_f16 v168, v168, v172\n" \
"v_pk_add_f16 v169, v169, v173\n" \
"ds_read_b64_tr_b16 v[188:189], v239 offset:32768\n" \
"v_mfma_f32_16x16x32_f16 v[4:7], v[152:155], v[172:175], v[4:7]\n" \
"v_pk_add_f16 v170, v170, v174\n" \
"v_pk_add_f16 v171, v171, v175\n" \
"ds_read_b64_tr_b16 v[190:191], v239 offset:32768\n" \
"v_mfma_f32_16x16x32_f16 v[16:19], v[156:159], v[172:175], v[16:19]\n" \
"ds_read_b128 v[164:167], v240 offset:6656\n" \
"s_add_i32 m0, s44, 0xa000\n" \
"s_add_i32 s80, s46, 0x4c00000\n" \
"v_mfma_f32_16x16x32_f16 v[8:11], v[136:139], v[168:171], v[8:11]\n" \
"buffer_load_dwordx4 v232, s[36:39], s80 offen sc1 lds\n" \
"s_waitcnt lgkmcnt(3)\n" \
"v_mfma_f32_16x16x32_bf16 v[184:187], v[184:187], v[192:195], 0\n" \
"v_mfma_f32_16x16x32_f16 v[20:23], v[140:143], v[168:171], v[20:23]\n" \
"ds_read_b64_tr_b16 v[120:121], v241 offset:0\n" \
"ds_read_b64_tr_b16 v[122:123], v241 offset:8192\n" \
"s_waitcnt lgkmcnt(3)\n" \
"v_mfma_f32_16x16x32_bf16 v[188:191], v[188:191], v[192:195], 0\n" \
"v_mfma_f32_16x16x32_f16 v[24:27], v[128:131], v[176:179], v[24:27]\n" \
"ds_read_b128 v[168:171], v240 offset:8192\n" \
"v_mfma_f32_16x16x32_f16 v[36:39], v[132:135], v[176:179], v[36:39]\n" \
"v_pk_add_f16 v176, v176, v180\n" \
"v_pk_add_f16 v177, v177, v181\n" \
"ds_read_b64_tr_b16 v[124:125], v242 offset:0\n" \
"v_mfma_f32_16x16x32_f16 v[28:31], v[152:155], v[180:183], v[28:31]\n" \
"v_pk_add_f16 v178, v178, v182\n" \
"v_pk_add_f16 v179, v179, v183\n" \
"v_cvt_pk_f16_f32 v184, v184, v185\n" \
"v_mfma_f32_16x16x32_f16 v[40:43], v[156:159], v[180:183], v[40:43]\n" \
"v_cvt_pk_f16_f32 v185, v186, v187\n" \
"v_cvt_pk_f16_f32 v186, v188, v189\n" \
"ds_read_b128 v[172:175], v240 offset:8704\n" \
"v_mfma_f32_16x16x32_f16 v[32:35], v[136:139], v[176:179], v[32:35]\n" \
"v_cvt_pk_f16_f32 v187, v190, v191\n" \
"v_add_u32 v184, s91, v184\n" \
"buffer_load_dwordx4 v232, s[36:39], s80 offen offset:1024 sc1 lds\n" \
"v_mfma_f32_16x16x32_f16 v[44:47], v[140:143], v[176:179], v[44:47]\n" \
"v_add_u32 v185, s91, v185\n" \
"v_add_u32 v186, s91, v186\n" \
"ds_read_b64_tr_b16 v[126:127], v242 offset:8192\n" \
"v_mfma_f32_16x16x32_f16 v[48:51], v[128:131], v[160:163], v[48:51]\n" \
"v_add_u32 v187, s91, v187\n" \
"v_and_b32 v184, s92, v184\n" \
"ds_read_b128 v[176:179], v240 offset:0\n" \
"v_mfma_f32_16x16x32_f16 v[60:63], v[132:135], v[160:163], v[60:63]\n" \
"s_waitcnt lgkmcnt(4)\n" \
"v_pk_add_f16 v160, v160, v164\n" \
"v_pk_add_f16 v161, v161, v165\n" \
"v_and_b32 v185, s92, v185\n" \
"v_mfma_f32_16x16x32_f16 v[52:55], v[152:155], v[164:167], v[52:55]\n" \
"v_pk_add_f16 v162, v162, v166\n" \
"v_pk_add_f16 v163, v163, v167\n" \
"v_and_b32 v186, s92, v186\n" \
"v_and_b32 v187, s92, v187\n" \
"v_mfma_f32_16x16x32_f16 v[64:67], v[156:159], v[164:167], v[64:67]\n" \
"ds_read_b128 v[180:183], v240 offset:512\n" \
"buffer_store_dwordx4 v[184:187], v234, s[36:39], s82 offen offset:1024 sc1\n" \
"ds_read_b64_tr_b16 v[144:145], v241 offset:512\n" \
"v_mfma_f32_16x16x32_f16 v[56:59], v[136:139], v[160:163], v[56:59]\n" \
"buffer_load_dwordx4 v232, s[36:39], s80 offen offset:2048 sc1 lds\n" \
"v_mfma_f32_16x16x32_f16 v[68:71], v[140:143], v[160:163], v[68:71]\n" \
"s_setprio 0\n" \
"ds_read_b64_tr_b16 v[146:147], v241 offset:8704\n" \
"ds_read_b64_tr_b16 v[148:149], v242 offset:512\n" \
"v_mfma_f32_16x16x32_f16 v[72:75], v[128:131], v[168:171], v[72:75]\n" \
"ds_read_b128 v[160:163], v240 offset:2048\n" \
"ds_read_b64_tr_b16 v[150:151], v242 offset:8704\n" \
"v_mfma_f32_16x16x32_f16 v[84:87], v[132:135], v[168:171], v[84:87]\n" \
"s_waitcnt lgkmcnt(6)\n" \
"v_pk_add_f16 v168, v168, v172\n" \
"v_pk_add_f16 v169, v169, v173\n" \
"v_mfma_f32_16x16x32_f16 v[76:79], v[152:155], v[172:175], v[76:79]\n" \
"v_pk_add_f16 v170, v170, v174\n" \
"v_pk_add_f16 v171, v171, v175\n" \
"v_mfma_f32_16x16x32_f16 v[88:91], v[156:159], v[172:175], v[88:91]\n" \
"ds_read_b128 v[164:167], v240 offset:2560\n" \
"v_mfma_f32_16x16x32_f16 v[80:83], v[136:139], v[168:171], v[80:83]\n" \
"buffer_load_dwordx4 v232, s[36:39], s80 offen offset:3072 sc1 lds\n" \
"v_mfma_f32_16x16x32_f16 v[92:95], v[140:143], v[168:171], v[92:95]\n" \
"v_mfma_f32_16x16x32_f16 v[96:99], v[128:131], v[176:179], v[96:99]\n" \
"s_waitcnt lgkmcnt(5)\n" \
"v_pk_add_f16 v128, v120, v144\n" \
"v_pk_add_f16 v129, v121, v145\n" \
"ds_read_b128 v[168:171], v240 offset:4096\n" \
"v_mfma_f32_16x16x32_f16 v[108:111], v[132:135], v[176:179], v[108:111]\n" \
"v_pk_add_f16 v176, v176, v180\n" \
"v_pk_add_f16 v177, v177, v181\n" \
"ds_read_b128 v[172:175], v240 offset:4608\n" \
"v_mfma_f32_16x16x32_f16 v[100:103], v[152:155], v[180:183], v[100:103]\n" \
"v_pk_add_f16 v178, v178, v182\n" \
"v_pk_add_f16 v179, v179, v183\n" \
"s_add_i32 m0, s45, 0xa000\n" \
"s_add_i32 s81, s47, 0x4c00000\n" \
"v_mfma_f32_16x16x32_f16 v[112:115], v[156:159], v[180:183], v[112:115]\n" \
"s_waitcnt lgkmcnt(3)\n" \
"v_pk_add_f16 v130, v122, v146\n" \
"v_pk_add_f16 v131, v123, v147\n" \
"buffer_load_dwordx4 v232, s[36:39], s81 offen sc1 lds\n" \
"v_mfma_f32_16x16x32_f16 v[104:107], v[136:139], v[176:179], v[104:107]\n" \
"v_pk_add_f16 v132, v124, v148\n" \
"v_pk_add_f16 v133, v125, v149\n" \
"v_mfma_f32_16x16x32_f16 v[116:119], v[140:143], v[176:179], v[116:119]\n" \
"s_setprio 1\n" \
"v_pk_add_f16 v134, v126, v150\n" \
"v_pk_add_f16 v135, v127, v151\n" \
"s_waitcnt vmcnt(6) lgkmcnt(0)\n" \
"s_barrier\n" \
"v_mfma_f32_16x16x32_f16 v[0:3], v[120:123], v[160:163], v[0:3]\n" \
"ds_read_b128 v[176:179], v240 offset:26624\n" \
"ds_read_b64_tr_b16 v[184:185], v236 offset:36864\n" \
"ds_read_b64_tr_b16 v[186:187], v236 offset:36864\n" \
"s_add_i32 m0, s54, 0x0\n" \
"s_mov_b32 s83, 0x200\n" \
"v_mfma_f32_16x16x32_f16 v[12:15], v[124:127], v[160:163], v[12:15]\n" \
"v_pk_add_f16 v160, v160, v164\n" \
"v_pk_add_f16 v161, v161, v165\n" \
"ds_read_b64_tr_b16 v[188:189], v237 offset:36864\n" \
"buffer_load_dwordx4 v233, s[36:39], s83 offen sc1 lds\n" \
"v_mfma_f32_16x16x32_f16 v[4:7], v[144:147], v[164:167], v[4:7]\n" \
"v_pk_add_f16 v162, v162, v166\n" \
"v_pk_add_f16 v163, v163, v167\n" \
"ds_read_b64_tr_b16 v[190:191], v237 offset:36864\n" \
"v_mfma_f32_16x16x32_f16 v[16:19], v[148:151], v[164:167], v[16:19]\n" \
"ds_read_b128 v[180:183], v240 offset:27136\n" \
"s_add_i32 m0, s44, 0x0\n" \
"s_add_i32 s80, s46, 0x5000000\n" \
"v_mfma_f32_16x16x32_f16 v[8:11], v[128:131], v[160:163], v[8:11]\n" \
"buffer_load_dwordx4 v232, s[36:39], s80 offen sc1 lds\n" \
"s_waitcnt lgkmcnt(3)\n" \
"v_mfma_f32_16x16x32_bf16 v[184:187], v[184:187], v[192:195], 0\n" \
"v_mfma_f32_16x16x32_f16 v[20:23], v[132:135], v[160:163], v[20:23]\n" \
"ds_read_b64_tr_b16 v[136:137], v241 offset:16384\n" \
"ds_read_b64_tr_b16 v[138:139], v241 offset:24576\n" \
"s_waitcnt lgkmcnt(3)\n" \
"v_mfma_f32_16x16x32_bf16 v[188:191], v[188:191], v[192:195], 0\n" \
"v_mfma_f32_16x16x32_f16 v[24:27], v[120:123], v[168:171], v[24:27]\n" \
"ds_read_b128 v[160:163], v240 offset:28672\n" \
"v_mfma_f32_16x16x32_f16 v[36:39], v[124:127], v[168:171], v[36:39]\n" \
"v_pk_add_f16 v168, v168, v172\n" \
"v_pk_add_f16 v169, v169, v173\n" \
"ds_read_b64_tr_b16 v[140:141], v242 offset:16384\n" \
"v_mfma_f32_16x16x32_f16 v[28:31], v[144:147], v[172:175], v[28:31]\n" \
"v_pk_add_f16 v170, v170, v174\n" \
"v_pk_add_f16 v171, v171, v175\n" \
"v_cvt_pk_f16_f32 v184, v184, v185\n" \
"v_mfma_f32_16x16x32_f16 v[40:43], v[148:151], v[172:175], v[40:43]\n" \
"v_cvt_pk_f16_f32 v185, v186, v187\n" \
"v_cvt_pk_f16_f32 v186, v188, v189\n" \
"ds_read_b128 v[164:167], v240 offset:29184\n" \
"v_mfma_f32_16x16x32_f16 v[32:35], v[128:131], v[168:171], v[32:35]\n" \
"v_cvt_pk_f16_f32 v187, v190, v191\n" \
"v_add_u32 v184, s91, v184\n" \
"buffer_load_dwordx4 v232, s[36:39], s80 offen offset:1024 sc1 lds\n" \
"v_mfma_f32_16x16x32_f16 v[44:47], v[132:135], v[168:171], v[44:47]\n" \
"v_add_u32 v185, s91, v185\n" \
"v_add_u32 v186, s91, v186\n" \
"ds_read_b64_tr_b16 v[142:143], v242 offset:24576\n" \
"v_mfma_f32_16x16x32_f16 v[48:51], v[120:123], v[176:179], v[48:51]\n" \
"v_add_u32 v187, s91, v187\n" \
"v_and_b32 v184, s92, v184\n" \
"ds_read_b128 v[168:171], v240 offset:20480\n" \
"v_mfma_f32_16x16x32_f16 v[60:63], v[124:127], v[176:179], v[60:63]\n" \
"s_waitcnt lgkmcnt(4)\n" \
"v_pk_add_f16 v176, v176, v180\n" \
"v_pk_add_f16 v177, v177, v181\n" \
"v_and_b32 v185, s92, v185\n" \
"s_add_i32 s82, s50, 0x7000000\n" \
"v_mfma_f32_16x16x32_f16 v[52:55], v[144:147], v[180:183], v[52:55]\n" \
"v_pk_add_f16 v178, v178, v182\n" \
"v_pk_add_f16 v179, v179, v183\n" \
"v_and_b32 v186, s92, v186\n" \
"v_and_b32 v187, s92, v187\n" \
"v_mfma_f32_16x16x32_f16 v[64:67], v[148:151], v[180:183], v[64:67]\n" \
"ds_read_b128 v[172:175], v240 offset:20992\n" \
"buffer_store_dwordx4 v[184:187], v234, s[36:39], s82 offen sc1\n" \
"ds_read_b64_tr_b16 v[152:153], v241 offset:16896\n" \
"v_mfma_f32_16x16x32_f16 v[56:59], v[128:131], v[176:179], v[56:59]\n" \
"buffer_load_dwordx4 v232, s[36:39], s80 offen offset:2048 sc1 lds\n" \
"v_mfma_f32_16x16x32_f16 v[68:71], v[132:135], v[176:179], v[68:71]\n" \
"s_setprio 0\n" \
"ds_read_b64_tr_b16 v[154:155], v241 offset:25088\n" \
"ds_read_b64_tr_b16 v[156:157], v242 offset:16896\n" \
"v_mfma_f32_16x16x32_f16 v[72:75], v[120:123], v[160:163], v[72:75]\n" \
"ds_read_b128 v[176:179], v240 offset:22528\n" \
"ds_read_b64_tr_b16 v[158:159], v242 offset:25088\n" \
"v_mfma_f32_16x16x32_f16 v[84:87], v[124:127], v[160:163], v[84:87]\n" \
"s_waitcnt lgkmcnt(6)\n" \
"v_pk_add_f16 v160, v160, v164\n" \
"v_pk_add_f16 v161, v161, v165\n" \
"v_mfma_f32_16x16x32_f16 v[76:79], v[144:147], v[164:167], v[76:79]\n" \
"v_pk_add_f16 v162, v162, v166\n" \
"v_pk_add_f16 v163, v163, v167\n" \
"v_mfma_f32_16x16x32_f16 v[88:91], v[148:151], v[164:167], v[88:91]\n" \
"ds_read_b128 v[180:183], v240 offset:23040\n" \
"v_mfma_f32_16x16x32_f16 v[80:83], v[128:131], v[160:163], v[80:83]\n" \
"buffer_load_dwordx4 v232, s[36:39], s80 offen offset:3072 sc1 lds\n" \
"v_mfma_f32_16x16x32_f16 v[92:95], v[132:135], v[160:163], v[92:95]\n" \
"v_mfma_f32_16x16x32_f16 v[96:99], v[120:123], v[168:171], v[96:99]\n" \
"s_waitcnt lgkmcnt(5)\n" \
"v_pk_add_f16 v120, v136, v152\n" \
"v_pk_add_f16 v121, v137, v153\n" \
"ds_read_b128 v[160:163], v240 offset:24576\n" \
"v_mfma_f32_16x16x32_f16 v[108:111], v[124:127], v[168:171], v[108:111]\n" \
"v_pk_add_f16 v168, v168, v172\n" \
"v_pk_add_f16 v169, v169, v173\n" \
"ds_read_b128 v[164:167], v240 offset:25088\n" \
"v_mfma_f32_16x16x32_f16 v[100:103], v[144:147], v[172:175], v[100:103]\n" \
"v_pk_add_f16 v170, v170, v174\n" \
"v_pk_add_f16 v171, v171, v175\n" \
"s_add_i32 m0, s45, 0x0\n" \
"s_add_i32 s81, s47, 0x5000000\n" \
"v_mfma_f32_16x16x32_f16 v[112:115], v[148:151], v[172:175], v[112:115]\n" \
"s_waitcnt lgkmcnt(3)\n" \
"v_pk_add_f16 v122, v138, v154\n" \
"v_pk_add_f16 v123, v139, v155\n" \
"buffer_load_dwordx4 v232, s[36:39], s81 offen sc1 lds\n" \
"v_mfma_f32_16x16x32_f16 v[104:107], v[128:131], v[168:171], v[104:107]\n" \
"v_pk_add_f16 v124, v140, v156\n" \
"v_pk_add_f16 v125, v141, v157\n" \
"v_mfma_f32_16x16x32_f16 v[116:119], v[132:135], v[168:171], v[116:119]\n" \
"s_setprio 1\n" \
"v_pk_add_f16 v126, v142, v158\n" \
"v_pk_add_f16 v127, v143, v159\n" \
"s_waitcnt vmcnt(7) lgkmcnt(0)\n" \
"s_barrier\n" \
"v_mfma_f32_16x16x32_f16 v[0:3], v[136:139], v[176:179], v[0:3]\n" \
"ds_read_b128 v[168:171], v240 offset:47104\n" \
"ds_read_b64_tr_b16 v[184:185], v238 offset:36864\n" \
"ds_read_b64_tr_b16 v[186:187], v238 offset:36864\n" \
"v_mfma_f32_16x16x32_f16 v[12:15], v[140:143], v[176:179], v[12:15]\n" \
"v_pk_add_f16 v176, v176, v180\n" \
"v_pk_add_f16 v177, v177, v181\n" \
"ds_read_b64_tr_b16 v[188:189], v239 offset:36864\n" \
"v_mfma_f32_16x16x32_f16 v[4:7], v[152:155], v[180:183], v[4:7]\n" \
"v_pk_add_f16 v178, v178, v182\n" \
"v_pk_add_f16 v179, v179, v183\n" \
"ds_read_b64_tr_b16 v[190:191], v239 offset:36864\n" \
"v_mfma_f32_16x16x32_f16 v[16:19], v[156:159], v[180:183], v[16:19]\n" \
"ds_read_b128 v[172:175], v240 offset:47616\n" \
"s_add_i32 m0, s44, 0x5000\n" \
"s_add_i32 s80, s46, 0x5400000\n" \
"v_mfma_f32_16x16x32_f16 v[8:11], v[120:123], v[176:179], v[8:11]\n" \
"buffer_load_dwordx4 v232, s[36:39], s80 offen sc1 lds\n" \
"s_waitcnt lgkmcnt(3)\n" \
"v_mfma_f32_16x16x32_bf16 v[184:187], v[184:187], v[192:195], 0\n" \
"v_mfma_f32_16x16x32_f16 v[20:23], v[124:127], v[176:179], v[20:23]\n" \
"ds_read_b64_tr_b16 v[128:129], v241 offset:32768\n" \
"ds_read_b64_tr_b16 v[130:131], v241 offset:40960\n" \
"s_waitcnt lgkmcnt(3)\n" \
"v_mfma_f32_16x16x32_bf16 v[188:191], v[188:191], v[192:195], 0\n" \
"v_mfma_f32_16x16x32_f16 v[24:27], v[136:139], v[160:163], v[24:27]\n" \
"ds_read_b128 v[176:179], v240 offset:49152\n" \
"v_mfma_f32_16x16x32_f16 v[36:39], v[140:143], v[160:163], v[36:39]\n" \
"v_pk_add_f16 v160, v160, v164\n" \
"v_pk_add_f16 v161, v161, v165\n" \
"ds_read_b64_tr_b16 v[132:133], v242 offset:32768\n" \
"v_mfma_f32_16x16x32_f16 v[28:31], v[152:155], v[164:167], v[28:31]\n" \
"v_pk_add_f16 v162, v162, v166\n" \
"v_pk_add_f16 v163, v163, v167\n" \
"v_cvt_pk_f16_f32 v184, v184, v185\n" \
"v_mfma_f32_16x16x32_f16 v[40:43], v[156:159], v[164:167], v[40:43]\n" \
"v_cvt_pk_f16_f32 v185, v186, v187\n" \
"v_cvt_pk_f16_f32 v186, v188, v189\n" \
"ds_read_b128 v[180:183], v240 offset:49664\n" \
"v_mfma_f32_16x16x32_f16 v[32:35], v[120:123], v[160:163], v[32:35]\n" \
"v_cvt_pk_f16_f32 v187, v190, v191\n" \
"v_add_u32 v184, s91, v184\n" \
"buffer_load_dwordx4 v232, s[36:39], s80 offen offset:1024 sc1 lds\n" \
"v_mfma_f32_16x16x32_f16 v[44:47], v[124:127], v[160:163], v[44:47]\n" \
"v_add_u32 v185, s91, v185\n" \
"v_add_u32 v186, s91, v186\n" \
"ds_read_b64_tr_b16 v[134:135], v242 offset:40960\n" \
"v_mfma_f32_16x16x32_f16 v[48:51], v[136:139], v[168:171], v[48:51]\n" \
"v_add_u32 v187, s91, v187\n" \
"v_and_b32 v184, s92, v184\n" \
"ds_read_b128 v[160:163], v240 offset:40960\n" \
"v_mfma_f32_16x16x32_f16 v[60:63], v[140:143], v[168:171], v[60:63]\n" \
"s_waitcnt lgkmcnt(4)\n" \
"v_pk_add_f16 v168, v168, v172\n" \
"v_pk_add_f16 v169, v169, v173\n" \
"v_and_b32 v185, s92, v185\n" \
"v_mfma_f32_16x16x32_f16 v[52:55], v[152:155], v[172:175], v[52:55]\n" \
"v_pk_add_f16 v170, v170, v174\n" \
"v_pk_add_f16 v171, v171, v175\n" \
"v_and_b32 v186, s92, v186\n" \
"v_and_b32 v187, s92, v187\n" \
"v_mfma_f32_16x16x32_f16 v[64:67], v[156:159], v[172:175], v[64:67]\n" \
"ds_read_b128 v[164:167], v240 offset:41472\n" \
"buffer_store_dwordx4 v[184:187], v234, s[36:39], s82 offen offset:1024 sc1\n" \
"ds_read_b64_tr_b16 v[144:145], v241 offset:33280\n" \
"v_mfma_f32_16x16x32_f16 v[56:59], v[120:123], v[168:171], v[56:59]\n" \
"buffer_load_dwordx4 v232, s[36:39], s80 offen offset:2048 sc1 lds\n" \
"v_mfma_f32_16x16x32_f16 v[68:71], v[124:127], v[168:171], v[68:71]\n" \
"s_setprio 0\n" \
"ds_read_b64_tr_b16 v[146:147], v241 offset:41472\n" \
"ds_read_b64_tr_b16 v[148:149], v242 offset:33280\n" \
"v_mfma_f32_16x16x32_f16 v[72:75], v[136:139], v[176:179], v[72:75]\n" \
"ds_read_b128 v[168:171], v240 offset:43008\n" \
"ds_read_b64_tr_b16 v[150:151], v242 offset:41472\n" \
"v_mfma_f32_16x16x32_f16 v[84:87], v[140:143], v[176:179], v[84:87]\n" \
"s_waitcnt lgkmcnt(6)\n" \
"v_pk_add_f16 v176, v176, v180\n" \
"v_pk_add_f16 v177, v177, v181\n" \
"v_mfma_f32_16x16x32_f16 v[76:79], v[152:155], v[180:183], v[76:79]\n" \
"v_pk_add_f16 v178, v178, v182\n" \
"v_pk_add_f16 v179, v179, v183\n" \
"v_mfma_f32_16x16x32_f16 v[88:91], v[156:159], v[180:183], v[88:91]\n" \
"ds_read_b128 v[172:175], v240 offset:43520\n" \
"v_mfma_f32_16x16x32_f16 v[80:83], v[120:123], v[176:179], v[80:83]\n" \
"buffer_load_dwordx4 v232, s[36:39], s80 offen offset:3072 sc1 lds\n" \
"v_mfma_f32_16x16x32_f16 v[92:95], v[124:127], v[176:179], v[92:95]\n" \
"v_mfma_f32_16x16x32_f16 v[96:99], v[136:139], v[160:163], v[96:99]\n" \
"s_waitcnt lgkmcnt(5)\n" \
"v_pk_add_f16 v136, v128, v144\n" \
"v_pk_add_f16 v137, v129, v145\n" \
"ds_read_b128 v[176:179], v240 offset:45056\n" \
"v_mfma_f32_16x16x32_f16 v[108:111], v[140:143], v[160:163], v[108:111]\n" \
"v_pk_add_f16 v160, v160, v164\n" \
"v_pk_add_f16 v161, v161, v165\n" \
"ds_read_b128 v[180:183], v240 offset:45568\n" \
"v_mfma_f32_16x16x32_f16 v[100:103], v[152:155], v[164:167], v[100:103]\n" \
"v_pk_add_f16 v162, v162, v166\n" \
"v_pk_add_f16 v163, v163, v167\n" \
"s_add_i32 m0, s45, 0x5000\n" \
"s_add_i32 s81, s47, 0x5400000\n" \
"v_mfma_f32_16x16x32_f16 v[112:115], v[156:159], v[164:167], v[112:115]\n" \
"s_waitcnt lgkmcnt(3)\n" \
"v_pk_add_f16 v138, v130, v146\n" \
"v_pk_add_f16 v139, v131, v147\n" \
"buffer_load_dwordx4 v232, s[36:39], s81 offen sc1 lds\n" \
"v_mfma_f32_16x16x32_f16 v[104:107], v[120:123], v[160:163], v[104:107]\n" \
"v_pk_add_f16 v140, v132, v148\n" \
"v_pk_add_f16 v141, v133, v149\n" \
"v_mfma_f32_16x16x32_f16 v[116:119], v[124:127], v[160:163], v[116:119]\n" \
"s_setprio 1\n" \
"v_pk_add_f16 v142, v134, v150\n" \
"v_pk_add_f16 v143, v135, v151\n" \
"s_waitcnt vmcnt(6) lgkmcnt(0)\n" \
"s_barrier\n" \
"v_mfma_f32_16x16x32_f16 v[0:3], v[128:131], v[168:171], v[0:3]\n" \
"ds_read_b128 v[160:163], v240 offset:6144\n" \
"ds_read_b64_tr_b16 v[184:185], v236 offset:40960\n" \
"ds_read_b64_tr_b16 v[186:187], v236 offset:40960\n" \
"s_add_i32 m0, s54, 0x1000\n" \
"s_mov_b32 s83, 0x300\n" \
"v_mfma_f32_16x16x32_f16 v[12:15], v[132:135], v[168:171], v[12:15]\n" \
"v_pk_add_f16 v168, v168, v172\n" \
"v_pk_add_f16 v169, v169, v173\n" \
"s_mov_b32 s84, 0x8014000\n" \
"buffer_load_dwordx4 v233, s[36:39], s83 offen sc1 lds\n" \
"v_mfma_f32_16x16x32_f16 v[4:7], v[144:147], v[172:175], v[4:7]\n" \
"v_pk_add_f16 v170, v170, v174\n" \
"v_pk_add_f16 v171, v171, v175\n" \
"buffer_load_dword v253, v252, s[36:39], s84 offen sc1\n" \
"v_mfma_f32_16x16x32_f16 v[16:19], v[148:151], v[172:175], v[16:19]\n" \
"ds_read_b128 v[164:167], v240 offset:6656\n" \
"s_add_i32 m0, s44, 0xa000\n" \
"s_add_i32 s80, s46, 0x5800000\n" \
"ds_read_b64_tr_b16 v[188:189], v237 offset:40960\n" \
"ds_read_b64_tr_b16 v[190:191], v237 offset:40960\n" \
"v_mfma_f32_16x16x32_f16 v[8:11], v[136:139], v[168:171], v[8:11]\n" \
"buffer_load_dwordx4 v232, s[36:39], s80 offen sc1 lds\n" \
"v_mfma_f32_16x16x32_f16 v[20:23], v[140:143], v[168:171], v[20:23]\n" \
"ds_read_b64_tr_b16 v[120:121], v241 offset:0\n" \
"ds_read_b64_tr_b16 v[122:123], v241 offset:8192\n" \
"s_waitcnt lgkmcnt(5)\n" \
"v_mfma_f32_16x16x32_bf16 v[184:187], v[184:187], v[192:195], 0\n" \
"v_mfma_f32_16x16x32_f16 v[24:27], v[128:131], v[176:179], v[24:27]\n" \
"ds_read_b128 v[168:171], v240 offset:8192\n" \
"s_waitcnt lgkmcnt(3)\n" \
"v_mfma_f32_16x16x32_bf16 v[188:191], v[188:191], v[192:195], 0\n" \
"v_mfma_f32_16x16x32_f16 v[36:39], v[132:135], v[176:179], v[36:39]\n" \
"v_pk_add_f16 v176, v176, v180\n" \
"v_pk_add_f16 v177, v177, v181\n" \
"ds_read_b64_tr_b16 v[124:125], v242 offset:0\n" \
"v_mfma_f32_16x16x32_f16 v[28:31], v[144:147], v[180:183], v[28:31]\n" \
"v_pk_add_f16 v178, v178, v182\n" \
"v_pk_add_f16 v179, v179, v183\n" \
"v_mfma_f32_16x16x32_f16 v[40:43], v[148:151], v[180:183], v[40:43]\n" \
"v_cvt_pk_f16_f32 v184, v184, v185\n" \
"ds_read_b128 v[172:175], v240 offset:8704\n" \
"ds_read_b64_tr_b16 v[126:127], v242 offset:8192\n" \
"v_mfma_f32_16x16x32_f16 v[32:35], v[136:139], v[176:179], v[32:35]\n" \
"v_cvt_pk_f16_f32 v185, v186, v187\n" \
"v_cvt_pk_f16_f32 v186, v188, v189\n" \
"buffer_load_dwordx4 v232, s[36:39], s80 offen offset:1024 sc1 lds\n" \
"v_mfma_f32_16x16x32_f16 v[44:47], v[140:143], v[176:179], v[44:47]\n" \
"v_cvt_pk_f16_f32 v187, v190, v191\n" \
"v_add_u32 v184, s91, v184\n" \
"v_mfma_f32_16x16x32_f16 v[48:51], v[128:131], v[160:163], v[48:51]\n" \
"v_add_u32 v185, s91, v185\n" \
"v_add_u32 v186, s91, v186\n" \
"ds_read_b128 v[176:179], v240 offset:0\n" \
"v_mfma_f32_16x16x32_f16 v[60:63], v[132:135], v[160:163], v[60:63]\n" \
"v_pk_add_f16 v160, v160, v164\n" \
"v_pk_add_f16 v161, v161, v165\n" \
"v_add_u32 v187, s91, v187\n" \
"v_and_b32 v184, s92, v184\n" \
"s_add_i32 s82, s51, 0x7800000\n" \
"v_mfma_f32_16x16x32_f16 v[52:55], v[144:147], v[164:167], v[52:55]\n" \
"v_pk_add_f16 v162, v162, v166\n" \
"v_pk_add_f16 v163, v163, v167\n" \
"v_and_b32 v185, s92, v185\n" \
"v_and_b32 v186, s92, v186\n" \
"v_and_b32 v187, s92, v187\n" \
"v_mfma_f32_16x16x32_f16 v[64:67], v[148:151], v[164:167], v[64:67]\n" \
"ds_read_b128 v[180:183], v240 offset:512\n" \
"buffer_store_dwordx4 v[184:187], v234, s[36:39], s82 offen sc1\n" \
"ds_read_b64_tr_b16 v[152:153], v241 offset:512\n" \
"v_mfma_f32_16x16x32_f16 v[56:59], v[136:139], v[160:163], v[56:59]\n" \
"buffer_load_dwordx4 v232, s[36:39], s80 offen offset:2048 sc1 lds\n" \
"ds_read_b64_tr_b16 v[184:185], v238 offset:40960\n" \
"v_mfma_f32_16x16x32_f16 v[68:71], v[140:143], v[160:163], v[68:71]\n" \
"s_setprio 0\n" \
"ds_read_b64_tr_b16 v[186:187], v238 offset:40960\n" \
"ds_read_b64_tr_b16 v[188:189], v239 offset:40960\n" \
"ds_read_b64_tr_b16 v[154:155], v241 offset:8704\n" \
"s_waitcnt lgkmcnt(6)\n" \
"v_mfma_f32_16x16x32_f16 v[72:75], v[128:131], v[168:171], v[72:75]\n" \
"ds_read_b128 v[160:163], v240 offset:2048\n" \
"ds_read_b64_tr_b16 v[190:191], v239 offset:40960\n" \
"ds_read_b64_tr_b16 v[156:157], v242 offset:512\n" \
"v_mfma_f32_16x16x32_f16 v[84:87], v[132:135], v[168:171], v[84:87]\n" \
"v_pk_add_f16 v168, v168, v172\n" \
"v_pk_add_f16 v169, v169, v173\n" \
"v_mfma_f32_16x16x32_f16 v[76:79], v[144:147], v[172:175], v[76:79]\n" \
"v_pk_add_f16 v170, v170, v174\n" \
"v_pk_add_f16 v171, v171, v175\n" \
"s_waitcnt lgkmcnt(5)\n" \
"v_mfma_f32_16x16x32_bf16 v[184:187], v[184:187], v[192:195], 0\n" \
"v_mfma_f32_16x16x32_f16 v[88:91], v[148:151], v[172:175], v[88:91]\n" \
"ds_read_b128 v[164:167], v240 offset:2560\n" \
"ds_read_b64_tr_b16 v[158:159], v242 offset:8704\n" \
"s_waitcnt lgkmcnt(3)\n" \
"v_mfma_f32_16x16x32_bf16 v[188:191], v[188:191], v[192:195], 0\n" \
"v_mfma_f32_16x16x32_f16 v[80:83], v[136:139], v[168:171], v[80:83]\n" \
"buffer_load_dwordx4 v232, s[36:39], s80 offen offset:3072 sc1 lds\n" \
"v_mfma_f32_16x16x32_f16 v[92:95], v[140:143], v[168:171], v[92:95]\n" \
"v_mfma_f32_16x16x32_f16 v[96:99], v[128:131], v[176:179], v[96:99]\n" \
"v_pk_add_f16 v128, v120, v152\n" \
"v_pk_add_f16 v129, v121, v153\n" \
"v_cvt_pk_f16_f32 v184, v184, v185\n" \
"v_cvt_pk_f16_f32 v185, v186, v187\n" \
"ds_read_b128 v[168:171], v240 offset:4096\n" \
"v_mfma_f32_16x16x32_f16 v[108:111], v[132:135], v[176:179], v[108:111]\n" \
"v_pk_add_f16 v176, v176, v180\n" \
"v_pk_add_f16 v177, v177, v181\n" \
"v_cvt_pk_f16_f32 v186, v188, v189\n" \
"v_cvt_pk_f16_f32 v187, v190, v191\n" \
"v_add_u32 v184, s91, v184\n" \
"v_add_u32 v185, s91, v185\n" \
"v_add_u32 v186, s91, v186\n" \
"ds_read_b128 v[172:175], v240 offset:4608\n" \
"v_mfma_f32_16x16x32_f16 v[100:103], v[144:147], v[180:183], v[100:103]\n" \
"v_pk_add_f16 v178, v178, v182\n" \
"v_pk_add_f16 v179, v179, v183\n" \
"s_waitcnt vmcnt(5)\n" \
"v_cmp_eq_u32 vcc, 0, v253\n" \
"s_cbranch_vccnz .Ls110_%=\n" \
".Lr110_%=:\n" \
"s_add_i32 m0, s45, 0xa000\n" \
"s_add_i32 s81, s47, 0x5800000\n" \
"v_mfma_f32_16x16x32_f16 v[112:115], v[148:151], v[180:183], v[112:115]\n" \
"v_pk_add_f16 v130, v122, v154\n" \
"v_pk_add_f16 v131, v123, v155\n" \
"v_add_u32 v187, s91, v187\n" \
"v_and_b32 v184, s92, v184\n" \
"buffer_load_dwordx4 v232, s[36:39], s81 offen sc1 lds\n" \
"v_mfma_f32_16x16x32_f16 v[104:107], v[136:139], v[176:179], v[104:107]\n" \
"s_waitcnt lgkmcnt(1)\n" \
"v_pk_add_f16 v132, v124, v156\n" \
"v_pk_add_f16 v133, v125, v157\n" \
"v_and_b32 v185, s92, v185\n" \
"v_and_b32 v186, s92, v186\n" \
"v_and_b32 v187, s92, v187\n" \
"v_mfma_f32_16x16x32_f16 v[116:119], v[140:143], v[176:179], v[116:119]\n" \
"s_setprio 1\n" \
"v_pk_add_f16 v134, v126, v158\n" \
"v_pk_add_f16 v135, v127, v159\n" \
"buffer_store_dwordx4 v[184:187], v234, s[36:39], s82 offen offset:1024 sc1\n" \
"s_waitcnt vmcnt(7) lgkmcnt(0)\n" \
"s_barrier\n" \
"v_mfma_f32_16x16x32_f16 v[0:3], v[120:123], v[160:163], v[0:3]\n" \
"ds_read_b128 v[176:179], v240 offset:26624\n" \
"v_mfma_f32_16x16x32_f16 v[12:15], v[124:127], v[160:163], v[12:15]\n" \
"v_pk_add_f16 v160, v160, v164\n" \
"v_pk_add_f16 v161, v161, v165\n" \
"v_mfma_f32_16x16x32_f16 v[4:7], v[152:155], v[164:167], v[4:7]\n" \
"v_pk_add_f16 v162, v162, v166\n" \
"v_pk_add_f16 v163, v163, v167\n" \
"v_mfma_f32_16x16x32_f16 v[16:19], v[156:159], v[164:167], v[16:19]\n" \
"ds_read_b128 v[180:183], v240 offset:27136\n" \
"s_add_i32 m0, s44, 0x0\n" \
"s_add_i32 s80, s46, 0x5c00000\n" \
"v_mfma_f32_16x16x32_f16 v[8:11], v[128:131], v[160:163], v[8:11]\n" \
"buffer_load_dwordx4 v232, s[36:39], s80 offen sc1 lds\n" \
"v_mfma_f32_16x16x32_f16 v[20:23], v[132:135], v[160:163], v[20:23]\n" \
"ds_read_b64_tr_b16 v[136:137], v241 offset:16384\n" \
"ds_read_b64_tr_b16 v[138:139], v241 offset:24576\n" \
"v_mfma_f32_16x16x32_f16 v[24:27], v[120:123], v[168:171], v[24:27]\n" \
"ds_read_b128 v[160:163], v240 offset:28672\n" \
"v_mfma_f32_16x16x32_f16 v[36:39], v[124:127], v[168:171], v[36:39]\n" \
"v_pk_add_f16 v168, v168, v172\n" \
"v_pk_add_f16 v169, v169, v173\n" \
"v_mfma_f32_16x16x32_f16 v[28:31], v[152:155], v[172:175], v[28:31]\n" \
"v_pk_add_f16 v170, v170, v174\n" \
"v_pk_add_f16 v171, v171, v175\n" \
"v_mfma_f32_16x16x32_f16 v[40:43], v[156:159], v[172:175], v[40:43]\n" \
"ds_read_b128 v[164:167], v240 offset:29184\n" \
"ds_read_b64_tr_b16 v[140:141], v242 offset:16384\n" \
"v_mfma_f32_16x16x32_f16 v[32:35], v[128:131], v[168:171], v[32:35]\n" \
"buffer_load_dwordx4 v232, s[36:39], s80 offen offset:1024 sc1 lds\n" \
"v_mfma_f32_16x16x32_f16 v[44:47], v[132:135], v[168:171], v[44:47]\n" \
"ds_read_b64_tr_b16 v[142:143], v242 offset:24576\n" \
"s_waitcnt lgkmcnt(7)\n" \
"v_mfma_f32_16x16x32_f16 v[48:51], v[120:123], v[176:179], v[48:51]\n" \
"ds_read_b128 v[168:171], v240 offset:20480\n" \
"ds_read_b64_tr_b16 v[144:145], v241 offset:16896\n" \
"v_mfma_f32_16x16x32_f16 v[60:63], v[124:127], v[176:179], v[60:63]\n" \
"s_waitcnt lgkmcnt(8)\n" \
"v_pk_add_f16 v176, v176, v180\n" \
"v_pk_add_f16 v177, v177, v181\n" \
"v_mfma_f32_16x16x32_f16 v[52:55], v[152:155], v[180:183], v[52:55]\n" \
"v_pk_add_f16 v178, v178, v182\n" \
"v_pk_add_f16 v179, v179, v183\n" \
"v_mfma_f32_16x16x32_f16 v[64:67], v[156:159], v[180:183], v[64:67]\n" \
"ds_read_b128 v[172:175], v240 offset:20992\n" \
"v_mfma_f32_16x16x32_f16 v[56:59], v[128:131], v[176:179], v[56:59]\n" \
"buffer_load_dwordx4 v232, s[36:39], s80 offen offset:2048 sc1 lds\n" \
"v_mfma_f32_16x16x32_f16 v[68:71], v[132:135], v[176:179], v[68:71]\n" \
"s_setprio 0\n" \
"ds_read_b64_tr_b16 v[146:147], v241 offset:25088\n" \
"ds_read_b64_tr_b16 v[148:149], v242 offset:16896\n" \
"s_waitcnt lgkmcnt(6)\n" \
"v_mfma_f32_16x16x32_f16 v[72:75], v[120:123], v[160:163], v[72:75]\n" \
"ds_read_b128 v[176:179], v240 offset:22528\n" \
"ds_read_b64_tr_b16 v[150:151], v242 offset:25088\n" \
"v_mfma_f32_16x16x32_f16 v[84:87], v[124:127], v[160:163], v[84:87]\n" \
"v_pk_add_f16 v160, v160, v164\n" \
"v_pk_add_f16 v161, v161, v165\n" \
"v_mfma_f32_16x16x32_f16 v[76:79], v[152:155], v[164:167], v[76:79]\n" \
"v_pk_add_f16 v162, v162, v166\n" \
"v_pk_add_f16 v163, v163, v167\n" \
"v_mfma_f32_16x16x32_f16 v[88:91], v[156:159], v[164:167], v[88:91]\n" \
"ds_read_b128 v[180:183], v240 offset:23040\n" \
"v_mfma_f32_16x16x32_f16 v[80:83], v[128:131], v[160:163], v[80:83]\n" \
"buffer_load_dwordx4 v232, s[36:39], s80 offen offset:3072 sc1 lds\n" \
"v_mfma_f32_16x16x32_f16 v[92:95], v[132:135], v[160:163], v[92:95]\n" \
"s_waitcnt lgkmcnt(6)\n" \
"v_mfma_f32_16x16x32_f16 v[96:99], v[120:123], v[168:171], v[96:99]\n" \
"v_pk_add_f16 v120, v136, v144\n" \
"v_pk_add_f16 v121, v137, v145\n" \
"ds_read_b128 v[160:163], v240 offset:24576\n" \
"v_mfma_f32_16x16x32_f16 v[108:111], v[124:127], v[168:171], v[108:111]\n" \
"s_waitcnt lgkmcnt(6)\n" \
"v_pk_add_f16 v168, v168, v172\n" \
"v_pk_add_f16 v169, v169, v173\n" \
"ds_read_b128 v[164:167], v240 offset:25088\n" \
"v_mfma_f32_16x16x32_f16 v[100:103], v[152:155], v[172:175], v[100:103]\n" \
"v_pk_add_f16 v170, v170, v174\n" \
"v_pk_add_f16 v171, v171, v175\n" \
"s_add_i32 m0, s45, 0x0\n" \
"s_add_i32 s81, s47, 0x5c00000\n" \
"v_mfma_f32_16x16x32_f16 v[112:115], v[156:159], v[172:175], v[112:115]\n" \
"s_waitcnt lgkmcnt(3)\n" \
"v_pk_add_f16 v122, v138, v146\n" \
"v_pk_add_f16 v123, v139, v147\n" \
"buffer_load_dwordx4 v232, s[36:39], s81 offen sc1 lds\n" \
"v_mfma_f32_16x16x32_f16 v[104:107], v[128:131], v[168:171], v[104:107]\n" \
"v_pk_add_f16 v124, v140, v148\n" \
"v_pk_add_f16 v125, v141, v149\n" \
"v_mfma_f32_16x16x32_f16 v[116:119], v[132:135], v[168:171], v[116:119]\n" \
"s_setprio 1\n" \
"v_pk_add_f16 v126, v142, v150\n" \
"v_pk_add_f16 v127, v143, v151\n" \
"s_waitcnt vmcnt(5) lgkmcnt(0)\n" \
"s_barrier\n" \
"v_mfma_f32_16x16x32_f16 v[0:3], v[136:139], v[176:179], v[0:3]\n" \
"ds_read_b128 v[168:171], v240 offset:47104\n" \
"v_mfma_f32_16x16x32_f16 v[12:15], v[140:143], v[176:179], v[12:15]\n" \
"v_pk_add_f16 v176, v176, v180\n" \
"v_pk_add_f16 v177, v177, v181\n" \
"v_mfma_f32_16x16x32_f16 v[4:7], v[144:147], v[180:183], v[4:7]\n" \
"v_pk_add_f16 v178, v178, v182\n" \
"v_pk_add_f16 v179, v179, v183\n" \
"v_mfma_f32_16x16x32_f16 v[16:19], v[148:151], v[180:183], v[16:19]\n" \
"ds_read_b128 v[172:175], v240 offset:47616\n" \
"s_add_i32 m0, s44, 0x5000\n" \
"s_add_i32 s80, s46, 0x6000000\n" \
"v_mfma_f32_16x16x32_f16 v[8:11], v[120:123], v[176:179], v[8:11]\n" \
"buffer_load_dwordx4 v232, s[36:39], s80 offen sc1 lds\n" \
"v_mfma_f32_16x16x32_f16 v[20:23], v[124:127], v[176:179], v[20:23]\n" \
"ds_read_b64_tr_b16 v[128:129], v241 offset:32768\n" \
"ds_read_b64_tr_b16 v[130:131], v241 offset:40960\n" \
"v_mfma_f32_16x16x32_f16 v[24:27], v[136:139], v[160:163], v[24:27]\n" \
"ds_read_b128 v[176:179], v240 offset:49152\n" \
"v_mfma_f32_16x16x32_f16 v[36:39], v[140:143], v[160:163], v[36:39]\n" \
"v_pk_add_f16 v160, v160, v164\n" \
"v_pk_add_f16 v161, v161, v165\n" \
"v_mfma_f32_16x16x32_f16 v[28:31], v[144:147], v[164:167], v[28:31]\n" \
"v_pk_add_f16 v162, v162, v166\n" \
"v_pk_add_f16 v163, v163, v167\n" \
"v_mfma_f32_16x16x32_f16 v[40:43], v[148:151], v[164:167], v[40:43]\n" \
"ds_read_b128 v[180:183], v240 offset:49664\n" \
"ds_read_b64_tr_b16 v[132:133], v242 offset:32768\n" \
"v_mfma_f32_16x16x32_f16 v[32:35], v[120:123], v[160:163], v[32:35]\n" \
"buffer_load_dwordx4 v232, s[36:39], s80 offen offset:1024 sc1 lds\n" \
"v_mfma_f32_16x16x32_f16 v[44:47], v[124:127], v[160:163], v[44:47]\n" \
"ds_read_b64_tr_b16 v[134:135], v242 offset:40960\n" \
"s_waitcnt lgkmcnt(7)\n" \
"v_mfma_f32_16x16x32_f16 v[48:51], v[136:139], v[168:171], v[48:51]\n" \
"ds_read_b128 v[160:163], v240 offset:40960\n" \
"ds_read_b64_tr_b16 v[152:153], v241 offset:33280\n" \
"v_mfma_f32_16x16x32_f16 v[60:63], v[140:143], v[168:171], v[60:63]\n" \
"s_waitcnt lgkmcnt(8)\n" \
"v_pk_add_f16 v168, v168, v172\n" \
"v_pk_add_f16 v169, v169, v173\n" \
"v_mfma_f32_16x16x32_f16 v[52:55], v[144:147], v[172:175], v[52:55]\n" \
"v_pk_add_f16 v170, v170, v174\n" \
"v_pk_add_f16 v171, v171, v175\n" \
"v_mfma_f32_16x16x32_f16 v[64:67], v[148:151], v[172:175], v[64:67]\n" \
"ds_read_b128 v[164:167], v240 offset:41472\n" \
"v_mfma_f32_16x16x32_f16 v[56:59], v[120:123], v[168:171], v[56:59]\n" \
"buffer_load_dwordx4 v232, s[36:39], s80 offen offset:2048 sc1 lds\n" \
"v_mfma_f32_16x16x32_f16 v[68:71], v[124:127], v[168:171], v[68:71]\n" \
"s_setprio 0\n" \
"ds_read_b64_tr_b16 v[154:155], v241 offset:41472\n" \
"ds_read_b64_tr_b16 v[156:157], v242 offset:33280\n" \
"s_waitcnt lgkmcnt(6)\n" \
"v_mfma_f32_16x16x32_f16 v[72:75], v[136:139], v[176:179], v[72:75]\n" \
"ds_read_b128 v[168:171], v240 offset:43008\n" \
"ds_read_b64_tr_b16 v[158:159], v242 offset:41472\n" \
"v_mfma_f32_16x16x32_f16 v[84:87], v[140:143], v[176:179], v[84:87]\n" \
"v_pk_add_f16 v176, v176, v180\n" \
"v_pk_add_f16 v177, v177, v181\n" \
"v_mfma_f32_16x16x32_f16 v[76:79], v[144:147], v[180:183], v[76:79]\n" \
"v_pk_add_f16 v178, v178, v182\n" \
"v_pk_add_f16 v179, v179, v183\n" \
"v_mfma_f32_16x16x32_f16 v[88:91], v[148:151], v[180:183], v[88:91]\n" \
"ds_read_b128 v[172:175], v240 offset:43520\n" \
"v_mfma_f32_16x16x32_f16 v[80:83], v[120:123], v[176:179], v[80:83]\n" \
"buffer_load_dwordx4 v232, s[36:39], s80 offen offset:3072 sc1 lds\n" \
"v_mfma_f32_16x16x32_f16 v[92:95], v[124:127], v[176:179], v[92:95]\n" \
"s_waitcnt lgkmcnt(6)\n" \
"v_mfma_f32_16x16x32_f16 v[96:99], v[136:139], v[160:163], v[96:99]\n" \
"v_pk_add_f16 v136, v128, v152\n" \
"v_pk_add_f16 v137, v129, v153\n" \
"ds_read_b128 v[176:179], v240 offset:45056\n" \
"v_mfma_f32_16x16x32_f16 v[108:111], v[140:143], v[160:163], v[108:111]\n" \
"s_waitcnt lgkmcnt(6)\n" \
"v_pk_add_f16 v160, v160, v164\n" \
"v_pk_add_f16 v161, v161, v165\n" \
"ds_read_b128 v[180:183], v240 offset:45568\n" \
"v_mfma_f32_16x16x32_f16 v[100:103], v[144:147], v[164:167], v[100:103]\n" \
"v_pk_add_f16 v162, v162, v166\n" \
"v_pk_add_f16 v163, v163, v167\n" \
"s_add_i32 m0, s45, 0x5000\n" \
"s_add_i32 s81, s47, 0x6000000\n" \
"v_mfma_f32_16x16x32_f16 v[112:115], v[148:151], v[164:167], v[112:115]\n" \
"s_waitcnt lgkmcnt(3)\n" \
"v_pk_add_f16 v138, v130, v154\n" \
"v_pk_add_f16 v139, v131, v155\n" \
"buffer_load_dwordx4 v232, s[36:39], s81 offen sc1 lds\n" \
"v_mfma_f32_16x16x32_f16 v[104:107], v[120:123], v[160:163], v[104:107]\n" \
"v_pk_add_f16 v140, v132, v156\n" \
"v_pk_add_f16 v141, v133, v157\n" \
"v_mfma_f32_16x16x32_f16 v[116:119], v[124:127], v[160:163], v[116:119]\n" \
"s_setprio 1\n" \
"v_pk_add_f16 v142, v134, v158\n" \
"v_pk_add_f16 v143, v135, v159\n" \
"s_waitcnt vmcnt(5) lgkmcnt(0)\n" \
"s_barrier\n" \
"v_mfma_f32_16x16x32_f16 v[0:3], v[128:131], v[168:171], v[0:3]\n" \
"ds_read_b128 v[160:163], v240 offset:6144\n" \
"v_mfma_f32_16x16x32_f16 v[12:15], v[132:135], v[168:171], v[12:15]\n" \
"v_pk_add_f16 v168, v168, v172\n" \
"v_pk_add_f16 v169, v169, v173\n" \
"v_mfma_f32_16x16x32_f16 v[4:7], v[152:155], v[172:175], v[4:7]\n" \
"v_pk_add_f16 v170, v170, v174\n" \
"v_pk_add_f16 v171, v171, v175\n" \
"v_mfma_f32_16x16x32_f16 v[16:19], v[156:159], v[172:175], v[16:19]\n" \
"ds_read_b128 v[164:167], v240 offset:6656\n" \
"s_add_i32 m0, s44, 0xa000\n" \
"s_add_i32 s80, s46, 0x6400000\n" \
"v_mfma_f32_16x16x32_f16 v[8:11], v[136:139], v[168:171], v[8:11]\n" \
"buffer_load_dwordx4 v232, s[36:39], s80 offen sc1 lds\n" \
"v_mfma_f32_16x16x32_f16 v[20:23], v[140:143], v[168:171], v[20:23]\n" \
"v_lshlrev_b32 v254, 3, v244\n" \
"ds_read_b64_tr_b16 v[120:121], v241 offset:0\n" \
"ds_read_b64_tr_b16 v[122:123], v241 offset:8192\n" \
"v_mfma_f32_16x16x32_f16 v[24:27], v[128:131], v[176:179], v[24:27]\n" \
"ds_read_b128 v[168:171], v240 offset:8192\n" \
"v_mfma_f32_16x16x32_f16 v[36:39], v[132:135], v[176:179], v[36:39]\n" \
"v_pk_add_f16 v176, v176, v180\n" \
"v_pk_add_f16 v177, v177, v181\n" \
"v_mfma_f32_16x16x32_f16 v[28:31], v[152:155], v[180:183], v[28:31]\n" \
"v_pk_add_f16 v178, v178, v182\n" \
"v_pk_add_f16 v179, v179, v183\n" \
"v_mfma_f32_16x16x32_f16 v[40:43], v[156:159], v[180:183], v[40:43]\n" \
"ds_read_b128 v[172:175], v240 offset:8704\n" \
"ds_read_b64_tr_b16 v[124:125], v242 offset:0\n" \
"v_mfma_f32_16x16x32_f16 v[32:35], v[136:139], v[176:179], v[32:35]\n" \
"buffer_load_dwordx4 v232, s[36:39], s80 offen offset:1024 sc1 lds\n" \
"v_mfma_f32_16x16x32_f16 v[44:47], v[140:143], v[176:179], v[44:47]\n" \
"ds_read_b64_tr_b16 v[126:127], v242 offset:8192\n" \
"s_mov_b64 exec, s[58:59]\n" \
"global_load_dword v255, v254, s[78:79]\n" \
"s_mov_b64 exec, -1\n" \
"s_waitcnt lgkmcnt(6)\n" \
"v_mfma_f32_16x16x32_f16 v[48:51], v[128:131], v[160:163], v[48:51]\n" \
"ds_read_b128 v[176:179], v240 offset:0\n" \
"ds_read_b64_tr_b16 v[144:145], v241 offset:512\n" \
"v_mfma_f32_16x16x32_f16 v[60:63], v[132:135], v[160:163], v[60:63]\n" \
"v_pk_add_f16 v160, v160, v164\n" \
"v_pk_add_f16 v161, v161, v165\n" \
"v_mfma_f32_16x16x32_f16 v[52:55], v[152:155], v[164:167], v[52:55]\n" \
"v_pk_add_f16 v162, v162, v166\n" \
"v_pk_add_f16 v163, v163, v167\n" \
"v_mfma_f32_16x16x32_f16 v[64:67], v[156:159], v[164:167], v[64:67]\n" \
"ds_read_b128 v[180:183], v240 offset:512\n" \
"v_mfma_f32_16x16x32_f16 v[56:59], v[136:139], v[160:163], v[56:59]\n" \
"buffer_load_dwordx4 v232, s[36:39], s80 offen offset:2048 sc1 lds\n" \
"v_mfma_f32_16x16x32_f16 v[68:71], v[140:143], v[160:163], v[68:71]\n" \
"s_setprio 0\n" \
"ds_read_b64_tr_b16 v[146:147], v241 offset:8704\n" \
"ds_read_b64_tr_b16 v[148:149], v242 offset:512\n" \
"s_waitcnt lgkmcnt(6)\n" \
"v_mfma_f32_16x16x32_f16 v[72:75], v[128:131], v[168:171], v[72:75]\n" \
"ds_read_b128 v[160:163], v240 offset:2048\n" \
"ds_read_b64_tr_b16 v[150:151], v242 offset:8704\n" \
"v_mfma_f32_16x16x32_f16 v[84:87], v[132:135], v[168:171], v[84:87]\n" \
"v_pk_add_f16 v168, v168, v172\n" \
"v_pk_add_f16 v169, v169, v173\n" \
"v_mfma_f32_16x16x32_f16 v[76:79], v[152:155], v[172:175], v[76:79]\n" \
"v_pk_add_f16 v170, v170, v174\n" \
"v_pk_add_f16 v171, v171, v175\n" \
"v_mfma_f32_16x16x32_f16 v[88:91], v[156:159], v[172:175], v[88:91]\n" \
"ds_read_b128 v[164:167], v240 offset:2560\n" \
"v_mfma_f32_16x16x32_f16 v[80:83], v[136:139], v[168:171], v[80:83]\n" \
"buffer_load_dwordx4 v232, s[36:39], s80 offen offset:3072 sc1 lds\n" \
"v_mfma_f32_16x16x32_f16 v[92:95], v[140:143], v[168:171], v[92:95]\n" \
"s_waitcnt lgkmcnt(6)\n" \
"v_mfma_f32_16x16x32_f16 v[96:99], v[128:131], v[176:179], v[96:99]\n" \
"v_pk_add_f16 v128, v120, v144\n" \
"v_pk_add_f16 v129, v121, v145\n" \
"ds_read_b128 v[168:171], v240 offset:4096\n" \
"v_mfma_f32_16x16x32_f16 v[108:111], v[132:135], v[176:179], v[108:111]\n" \
"s_waitcnt lgkmcnt(6)\n" \
"v_pk_add_f16 v176, v176, v180\n" \
"v_pk_add_f16 v177, v177, v181\n" \
"ds_read_b128 v[172:175], v240 offset:4608\n" \
"v_mfma_f32_16x16x32_f16 v[100:103], v[152:155], v[180:183], v[100:103]\n" \
"v_pk_add_f16 v178, v178, v182\n" \
"v_pk_add_f16 v179, v179, v183\n" \
"s_add_i32 m0, s45, 0xa000\n" \
"s_add_i32 s81, s47, 0x6400000\n" \
"v_mfma_f32_16x16x32_f16 v[112:115], v[156:159], v[180:183], v[112:115]\n" \
"s_waitcnt lgkmcnt(3)\n" \
"v_pk_add_f16 v130, v122, v146\n" \
"v_pk_add_f16 v131, v123, v147\n" \
"buffer_load_dwordx4 v232, s[36:39], s81 offen sc1 lds\n" \
"v_mfma_f32_16x16x32_f16 v[104:107], v[136:139], v[176:179], v[104:107]\n" \
"v_pk_add_f16 v132, v124, v148\n" \
"v_pk_add_f16 v133, v125, v149\n" \
"v_mfma_f32_16x16x32_f16 v[116:119], v[140:143], v[176:179], v[116:119]\n" \
"s_setprio 1\n" \
"v_pk_add_f16 v134, v126, v150\n" \
"v_pk_add_f16 v135, v127, v151\n" \
"s_waitcnt vmcnt(6) lgkmcnt(0)\n" \
"s_barrier\n" \
"v_mfma_f32_16x16x32_f16 v[0:3], v[120:123], v[160:163], v[0:3]\n" \
"ds_read_b128 v[176:179], v240 offset:26624\n" \
"v_mfma_f32_16x16x32_f16 v[12:15], v[124:127], v[160:163], v[12:15]\n" \
"v_pk_add_f16 v160, v160, v164\n" \
"v_pk_add_f16 v161, v161, v165\n" \
"v_mfma_f32_16x16x32_f16 v[4:7], v[144:147], v[164:167], v[4:7]\n" \
"v_pk_add_f16 v162, v162, v166\n" \
"v_pk_add_f16 v163, v163, v167\n" \
"v_mfma_f32_16x16x32_f16 v[16:19], v[148:151], v[164:167], v[16:19]\n" \
"ds_read_b128 v[180:183], v240 offset:27136\n" \
"s_add_i32 m0, s44, 0x0\n" \
"s_add_i32 s80, s46, 0x6800000\n" \
"v_mfma_f32_16x16x32_f16 v[8:11], v[128:131], v[160:163], v[8:11]\n" \
"buffer_load_dwordx4 v232, s[36:39], s80 offen sc1 lds\n" \
"v_mfma_f32_16x16x32_f16 v[20:23], v[132:135], v[160:163], v[20:23]\n" \
"ds_read_b64_tr_b16 v[136:137], v241 offset:16384\n" \
"ds_read_b64_tr_b16 v[138:139], v241 offset:24576\n" \
"v_mfma_f32_16x16x32_f16 v[24:27], v[120:123], v[168:171], v[24:27]\n" \
"ds_read_b128 v[160:163], v240 offset:28672\n" \
"v_mfma_f32_16x16x32_f16 v[36:39], v[124:127], v[168:171], v[36:39]\n" \
"v_pk_add_f16 v168, v168, v172\n" \
"v_pk_add_f16 v169, v169, v173\n" \
"v_mfma_f32_16x16x32_f16 v[28:31], v[144:147], v[172:175], v[28:31]\n" \
"v_pk_add_f16 v170, v170, v174\n" \
"v_pk_add_f16 v171, v171, v175\n" \
"v_mfma_f32_16x16x32_f16 v[40:43], v[148:151], v[172:175], v[40:43]\n" \
"ds_read_b128 v[164:167], v240 offset:29184\n" \
"ds_read_b64_tr_b16 v[140:141], v242 offset:16384\n" \
"v_mfma_f32_16x16x32_f16 v[32:35], v[128:131], v[168:171], v[32:35]\n" \
"buffer_load_dwordx4 v232, s[36:39], s80 offen offset:1024 sc1 lds\n" \
"v_mfma_f32_16x16x32_f16 v[44:47], v[132:135], v[168:171], v[44:47]\n" \
"ds_read_b64_tr_b16 v[142:143], v242 offset:24576\n" \
"s_waitcnt lgkmcnt(7)\n" \
"v_mfma_f32_16x16x32_f16 v[48:51], v[120:123], v[176:179], v[48:51]\n" \
"ds_read_b128 v[168:171], v240 offset:20480\n" \
"ds_read_b64_tr_b16 v[152:153], v241 offset:16896\n" \
"v_mfma_f32_16x16x32_f16 v[60:63], v[124:127], v[176:179], v[60:63]\n" \
"s_waitcnt lgkmcnt(8)\n" \
"v_pk_add_f16 v176, v176, v180\n" \
"v_pk_add_f16 v177, v177, v181\n" \
"v_mfma_f32_16x16x32_f16 v[52:55], v[144:147], v[180:183], v[52:55]\n" \
"v_pk_add_f16 v178, v178, v182\n" \
"v_pk_add_f16 v179, v179, v183\n" \
"v_mfma_f32_16x16x32_f16 v[64:67], v[148:151], v[180:183], v[64:67]\n" \
"ds_read_b128 v[172:175], v240 offset:20992\n" \
"v_mfma_f32_16x16x32_f16 v[56:59], v[128:131], v[176:179], v[56:59]\n" \
"buffer_load_dwordx4 v232, s[36:39], s80 offen offset:2048 sc1 lds\n" \
"v_mfma_f32_16x16x32_f16 v[68:71], v[132:135], v[176:179], v[68:71]\n" \
"s_setprio 0\n" \
"ds_read_b64_tr_b16 v[154:155], v241 offset:25088\n" \
"ds_read_b64_tr_b16 v[156:157], v242 offset:16896\n" \
"s_waitcnt lgkmcnt(6)\n" \
"v_mfma_f32_16x16x32_f16 v[72:75], v[120:123], v[160:163], v[72:75]\n" \
"ds_read_b128 v[176:179], v240 offset:22528\n" \
"ds_read_b64_tr_b16 v[158:159], v242 offset:25088\n" \
"v_mfma_f32_16x16x32_f16 v[84:87], v[124:127], v[160:163], v[84:87]\n" \
"v_pk_add_f16 v160, v160, v164\n" \
"v_pk_add_f16 v161, v161, v165\n" \
"v_mfma_f32_16x16x32_f16 v[76:79], v[144:147], v[164:167], v[76:79]\n" \
"v_pk_add_f16 v162, v162, v166\n" \
"v_pk_add_f16 v163, v163, v167\n" \
"v_mfma_f32_16x16x32_f16 v[88:91], v[148:151], v[164:167], v[88:91]\n" \
"ds_read_b128 v[180:183], v240 offset:23040\n" \
"v_mfma_f32_16x16x32_f16 v[80:83], v[128:131], v[160:163], v[80:83]\n" \
"buffer_load_dwordx4 v232, s[36:39], s80 offen offset:3072 sc1 lds\n" \
"v_mfma_f32_16x16x32_f16 v[92:95], v[132:135], v[160:163], v[92:95]\n" \
"s_waitcnt lgkmcnt(6)\n" \
"v_mfma_f32_16x16x32_f16 v[96:99], v[120:123], v[168:171], v[96:99]\n" \
"v_pk_add_f16 v120, v136, v152\n" \
"v_pk_add_f16 v121, v137, v153\n" \
"ds_read_b128 v[160:163], v240 offset:24576\n" \
"v_mfma_f32_16x16x32_f16 v[108:111], v[124:127], v[168:171], v[108:111]\n" \
"s_waitcnt lgkmcnt(6)\n" \
"v_pk_add_f16 v168, v168, v172\n" \
"v_pk_add_f16 v169, v169, v173\n" \
"ds_read_b128 v[164:167], v240 offset:25088\n" \
"v_mfma_f32_16x16x32_f16 v[100:103], v[144:147], v[172:175], v[100:103]\n" \
"v_pk_add_f16 v170, v170, v174\n" \
"v_pk_add_f16 v171, v171, v175\n" \
"s_add_i32 m0, s45, 0x0\n" \
"s_add_i32 s81, s47, 0x6800000\n" \
"v_mfma_f32_16x16x32_f16 v[112:115], v[148:151], v[172:175], v[112:115]\n" \
"s_waitcnt lgkmcnt(3)\n" \
"v_pk_add_f16 v122, v138, v154\n" \
"v_pk_add_f16 v123, v139, v155\n" \
"buffer_load_dwordx4 v232, s[36:39], s81 offen sc1 lds\n" \
"v_mfma_f32_16x16x32_f16 v[104:107], v[128:131], v[168:171], v[104:107]\n" \
"v_pk_add_f16 v124, v140, v156\n" \
"v_pk_add_f16 v125, v141, v157\n" \
"v_mfma_f32_16x16x32_f16 v[116:119], v[132:135], v[168:171], v[116:119]\n" \
"s_setprio 1\n" \
"v_pk_add_f16 v126, v142, v158\n" \
"v_pk_add_f16 v127, v143, v159\n" \
"s_waitcnt vmcnt(5) lgkmcnt(0)\n" \
"s_barrier\n" \
"v_mfma_f32_16x16x32_f16 v[0:3], v[136:139], v[176:179], v[0:3]\n" \
"ds_read_b128 v[168:171], v240 offset:47104\n" \
"v_mfma_f32_16x16x32_f16 v[12:15], v[140:143], v[176:179], v[12:15]\n" \
"v_pk_add_f16 v176, v176, v180\n" \
"v_pk_add_f16 v177, v177, v181\n" \
"v_mfma_f32_16x16x32_f16 v[4:7], v[152:155], v[180:183], v[4:7]\n" \
"v_pk_add_f16 v178, v178, v182\n" \
"v_pk_add_f16 v179, v179, v183\n" \
"s_add_i32 m0, s44, 0x5000\n" \
"s_add_i32 s80, s46, 0x6c00000\n" \
"v_mfma_f32_16x16x32_f16 v[16:19], v[156:159], v[180:183], v[16:19]\n" \
"ds_read_b128 v[172:175], v240 offset:47616\n" \
"buffer_load_dwordx4 v232, s[36:39], s80 offen sc1 lds\n" \
"v_mfma_f32_16x16x32_f16 v[8:11], v[120:123], v[176:179], v[8:11]\n" \
"ds_read_b64_tr_b16 v[128:129], v241 offset:32768\n" \
"v_mfma_f32_16x16x32_f16 v[20:23], v[124:127], v[176:179], v[20:23]\n" \
"ds_read_b64_tr_b16 v[130:131], v241 offset:40960\n" \
"v_mfma_f32_16x16x32_f16 v[24:27], v[136:139], v[160:163], v[24:27]\n" \
"ds_read_b128 v[176:179], v240 offset:49152\n" \
"buffer_load_dwordx4 v232, s[36:39], s80 offen offset:1024 sc1 lds\n" \
"v_mfma_f32_16x16x32_f16 v[36:39], v[140:143], v[160:163], v[36:39]\n" \
"v_pk_add_f16 v160, v160, v164\n" \
"v_pk_add_f16 v161, v161, v165\n" \
"v_mfma_f32_16x16x32_f16 v[28:31], v[152:155], v[164:167], v[28:31]\n" \
"v_pk_add_f16 v162, v162, v166\n" \
"v_pk_add_f16 v163, v163, v167\n" \
"v_mfma_f32_16x16x32_f16 v[40:43], v[156:159], v[164:167], v[40:43]\n" \
"ds_read_b128 v[180:183], v240 offset:49664\n" \
"buffer_load_dwordx4 v232, s[36:39], s80 offen offset:2048 sc1 lds\n" \
"v_mfma_f32_16x16x32_f16 v[32:35], v[120:123], v[160:163], v[32:35]\n" \
"ds_read_b64_tr_b16 v[132:133], v242 offset:32768\n" \
"v_mfma_f32_16x16x32_f16 v[44:47], v[124:127], v[160:163], v[44:47]\n" \
"ds_read_b64_tr_b16 v[134:135], v242 offset:40960\n" \
"s_waitcnt lgkmcnt(7)\n" \
"v_mfma_f32_16x16x32_f16 v[48:51], v[136:139], v[168:171], v[48:51]\n" \
"ds_read_b128 v[160:163], v240 offset:40960\n" \
"buffer_load_dwordx4 v232, s[36:39], s80 offen offset:3072 sc1 lds\n" \
"v_mfma_f32_16x16x32_f16 v[60:63], v[140:143], v[168:171], v[60:63]\n" \
"s_waitcnt lgkmcnt(6)\n" \
"v_pk_add_f16 v168, v168, v172\n" \
"v_pk_add_f16 v169, v169, v173\n" \
"ds_read_b64_tr_b16 v[144:145], v241 offset:33280\n" \
"v_mfma_f32_16x16x32_f16 v[52:55], v[152:155], v[172:175], v[52:55]\n" \
"v_pk_add_f16 v170, v170, v174\n" \
"v_pk_add_f16 v171, v171, v175\n" \
"s_add_i32 m0, s45, 0x5000\n" \
"s_add_i32 s81, s47, 0x6c00000\n" \
"v_mfma_f32_16x16x32_f16 v[64:67], v[156:159], v[172:175], v[64:67]\n" \
"ds_read_b128 v[164:167], v240 offset:41472\n" \
"buffer_load_dwordx4 v232, s[36:39], s81 offen sc1 lds\n" \
"v_mfma_f32_16x16x32_f16 v[56:59], v[120:123], v[168:171], v[56:59]\n" \
"ds_read_b64_tr_b16 v[146:147], v241 offset:41472\n" \
"ds_read_b64_tr_b16 v[148:149], v242 offset:33280\n" \
"v_mfma_f32_16x16x32_f16 v[68:71], v[124:127], v[168:171], v[68:71]\n" \
"s_setprio 0\n" \
"s_add_i32 m0, s52, 0x0\n" \
"s_add_i32 s80, s55, 0x0\n" \
"s_waitcnt lgkmcnt(5)\n" \
"v_mfma_f32_16x16x32_f16 v[72:75], v[136:139], v[176:179], v[72:75]\n" \
"ds_read_b128 v[168:171], v240 offset:43008\n" \
"buffer_load_dwordx4 v232, s[36:39], s80 offen sc1 lds\n" \
"v_mfma_f32_16x16x32_f16 v[84:87], v[140:143], v[176:179], v[84:87]\n" \
"v_pk_add_f16 v176, v176, v180\n" \
"v_pk_add_f16 v177, v177, v181\n" \
"ds_read_b64_tr_b16 v[150:151], v242 offset:41472\n" \
"v_mfma_f32_16x16x32_f16 v[76:79], v[152:155], v[180:183], v[76:79]\n" \
"v_pk_add_f16 v178, v178, v182\n" \
"v_pk_add_f16 v179, v179, v183\n" \
"v_mfma_f32_16x16x32_f16 v[88:91], v[156:159], v[180:183], v[88:91]\n" \
"ds_read_b128 v[172:175], v240 offset:43520\n" \
"buffer_load_dwordx4 v232, s[36:39], s80 offen offset:1024 sc1 lds\n" \
"v_mfma_f32_16x16x32_f16 v[80:83], v[120:123], v[176:179], v[80:83]\n" \
"v_mfma_f32_16x16x32_f16 v[92:95], v[124:127], v[176:179], v[92:95]\n" \
"s_waitcnt lgkmcnt(5)\n" \
"v_mfma_f32_16x16x32_f16 v[96:99], v[136:139], v[160:163], v[96:99]\n" \
"v_pk_add_f16 v136, v128, v144\n" \
"v_pk_add_f16 v137, v129, v145\n" \
"ds_read_b128 v[176:179], v240 offset:45056\n" \
"buffer_load_dwordx4 v232, s[36:39], s80 offen offset:2048 sc1 lds\n" \
"v_mfma_f32_16x16x32_f16 v[108:111], v[140:143], v[160:163], v[108:111]\n" \
"v_pk_add_f16 v160, v160, v164\n" \
"v_pk_add_f16 v161, v161, v165\n" \
"ds_read_b128 v[180:183], v240 offset:45568\n" \
"v_mfma_f32_16x16x32_f16 v[100:103], v[152:155], v[164:167], v[100:103]\n" \
"v_pk_add_f16 v162, v162, v166\n" \
"v_pk_add_f16 v163, v163, v167\n" \
"v_mfma_f32_16x16x32_f16 v[112:115], v[156:159], v[164:167], v[112:115]\n" \
"s_waitcnt lgkmcnt(4)\n" \
"v_pk_add_f16 v138, v130, v146\n" \
"v_pk_add_f16 v139, v131, v147\n" \
"buffer_load_dwordx4 v232, s[36:39], s80 offen offset:3072 sc1 lds\n" \
"v_mfma_f32_16x16x32_f16 v[104:107], v[120:123], v[160:163], v[104:107]\n" \
"v_pk_add_f16 v140, v132, v148\n" \
"v_pk_add_f16 v141, v133, v149\n" \
"v_mfma_f32_16x16x32_f16 v[116:119], v[124:127], v[160:163], v[116:119]\n" \
"s_setprio 1\n" \
"s_waitcnt lgkmcnt(2)\n" \
"v_pk_add_f16 v142, v134, v150\n" \
"v_pk_add_f16 v143, v135, v151\n" \
"s_waitcnt vmcnt(9) lgkmcnt(0)\n" \
"s_barrier\n" \
"v_mfma_f32_16x16x32_f16 v[0:3], v[128:131], v[168:171], v[0:3]\n" \
"ds_read_b128 v[160:163], v240 offset:6144\n" \
"v_mfma_f32_16x16x32_f16 v[12:15], v[132:135], v[168:171], v[12:15]\n" \
"v_pk_add_f16 v168, v168, v172\n" \
"v_pk_add_f16 v169, v169, v173\n" \
"v_mfma_f32_16x16x32_f16 v[4:7], v[144:147], v[172:175], v[4:7]\n" \
"v_pk_add_f16 v170, v170, v174\n" \
"v_pk_add_f16 v171, v171, v175\n" \
"v_mfma_f32_16x16x32_f16 v[16:19], v[148:151], v[172:175], v[16:19]\n" \
"ds_read_b128 v[164:167], v240 offset:6656\n" \
"s_add_i32 m0, s44, 0xa000\n" \
"s_add_i32 s80, s46, 0x7000000\n" \
"v_mfma_f32_16x16x32_f16 v[8:11], v[136:139], v[168:171], v[8:11]\n" \
"buffer_load_dwordx4 v232, s[36:39], s80 offen sc1 lds\n" \
"v_mfma_f32_16x16x32_f16 v[20:23], v[140:143], v[168:171], v[20:23]\n" \
"ds_read_b64_tr_b16 v[120:121], v241 offset:0\n" \
"ds_read_b64_tr_b16 v[122:123], v241 offset:8192\n" \
"v_mfma_f32_16x16x32_f16 v[24:27], v[128:131], v[176:179], v[24:27]\n" \
"ds_read_b128 v[168:171], v240 offset:8192\n" \
"v_mfma_f32_16x16x32_f16 v[36:39], v[132:135], v[176:179], v[36:39]\n" \
"v_pk_add_f16 v176, v176, v180\n" \
"v_pk_add_f16 v177, v177, v181\n" \
"v_mfma_f32_16x16x32_f16 v[28:31], v[144:147], v[180:183], v[28:31]\n" \
"v_pk_add_f16 v178, v178, v182\n" \
"v_pk_add_f16 v179, v179, v183\n" \
"v_mfma_f32_16x16x32_f16 v[40:43], v[148:151], v[180:183], v[40:43]\n" \
"ds_read_b128 v[172:175], v240 offset:8704\n" \
"ds_read_b64_tr_b16 v[124:125], v242 offset:0\n" \
"v_mfma_f32_16x16x32_f16 v[32:35], v[136:139], v[176:179], v[32:35]\n" \
"buffer_load_dwordx4 v232, s[36:39], s80 offen offset:1024 sc1 lds\n" \
"v_mfma_f32_16x16x32_f16 v[44:47], v[140:143], v[176:179], v[44:47]\n" \
"ds_read_b64_tr_b16 v[126:127], v242 offset:8192\n" \
"s_waitcnt lgkmcnt(7)\n" \
"v_mfma_f32_16x16x32_f16 v[48:51], v[128:131], v[160:163], v[48:51]\n" \
"ds_read_b128 v[176:179], v240 offset:0\n" \
"ds_read_b64_tr_b16 v[152:153], v241 offset:512\n" \
"v_mfma_f32_16x16x32_f16 v[60:63], v[132:135], v[160:163], v[60:63]\n" \
"s_waitcnt lgkmcnt(8)\n" \
"v_pk_add_f16 v160, v160, v164\n" \
"v_pk_add_f16 v161, v161, v165\n" \
"v_mfma_f32_16x16x32_f16 v[52:55], v[144:147], v[164:167], v[52:55]\n" \
"v_pk_add_f16 v162, v162, v166\n" \
"v_pk_add_f16 v163, v163, v167\n" \
"v_mfma_f32_16x16x32_f16 v[64:67], v[148:151], v[164:167], v[64:67]\n" \
"ds_read_b128 v[180:183], v240 offset:512\n" \
"v_mfma_f32_16x16x32_f16 v[56:59], v[136:139], v[160:163], v[56:59]\n" \
"buffer_load_dwordx4 v232, s[36:39], s80 offen offset:2048 sc1 lds\n" \
"v_mfma_f32_16x16x32_f16 v[68:71], v[140:143], v[160:163], v[68:71]\n" \
"s_setprio 0\n" \
"ds_read_b64_tr_b16 v[154:155], v241 offset:8704\n" \
"ds_read_b64_tr_b16 v[156:157], v242 offset:512\n" \
"s_waitcnt lgkmcnt(6)\n" \
"v_mfma_f32_16x16x32_f16 v[72:75], v[128:131], v[168:171], v[72:75]\n" \
"ds_read_b128 v[160:163], v240 offset:2048\n" \
"ds_read_b64_tr_b16 v[158:159], v242 offset:8704\n" \
"v_mfma_f32_16x16x32_f16 v[84:87], v[132:135], v[168:171], v[84:87]\n" \
"v_pk_add_f16 v168, v168, v172\n" \
"v_pk_add_f16 v169, v169, v173\n" \
"v_mfma_f32_16x16x32_f16 v[76:79], v[144:147], v[172:175], v[76:79]\n" \
"v_pk_add_f16 v170, v170, v174\n" \
"v_pk_add_f16 v171, v171, v175\n" \
"v_mfma_f32_16x16x32_f16 v[88:91], v[148:151], v[172:175], v[88:91]\n" \
"ds_read_b128 v[164:167], v240 offset:2560\n" \
"v_mfma_f32_16x16x32_f16 v[80:83], v[136:139], v[168:171], v[80:83]\n" \
"buffer_load_dwordx4 v232, s[36:39], s80 offen offset:3072 sc1 lds\n" \
"v_mfma_f32_16x16x32_f16 v[92:95], v[140:143], v[168:171], v[92:95]\n" \
"s_waitcnt lgkmcnt(6)\n" \
"v_mfma_f32_16x16x32_f16 v[96:99], v[128:131], v[176:179], v[96:99]\n" \
"v_pk_add_f16 v128, v120, v152\n" \
"v_pk_add_f16 v129, v121, v153\n" \
"ds_read_b128 v[168:171], v240 offset:4096\n" \
"v_mfma_f32_16x16x32_f16 v[108:111], v[132:135], v[176:179], v[108:111]\n" \
"s_waitcnt lgkmcnt(6)\n" \
"v_pk_add_f16 v176, v176, v180\n" \
"v_pk_add_f16 v177, v177, v181\n" \
"ds_read_b128 v[172:175], v240 offset:4608\n" \
"v_mfma_f32_16x16x32_f16 v[100:103], v[144:147], v[180:183], v[100:103]\n" \
"v_pk_add_f16 v178, v178, v182\n" \
"v_pk_add_f16 v179, v179, v183\n" \
"s_add_i32 m0, s45, 0xa000\n" \
"s_add_i32 s81, s47, 0x7000000\n" \
"v_mfma_f32_16x16x32_f16 v[112:115], v[148:151], v[180:183], v[112:115]\n" \
"s_waitcnt lgkmcnt(3)\n" \
"v_pk_add_f16 v130, v122, v154\n" \
"v_pk_add_f16 v131, v123, v155\n" \
"buffer_load_dwordx4 v232, s[36:39], s81 offen sc1 lds\n" \
"v_mfma_f32_16x16x32_f16 v[104:107], v[136:139], v[176:179], v[104:107]\n" \
"v_pk_add_f16 v132, v124, v156\n" \
"v_pk_add_f16 v133, v125, v157\n" \
"v_mfma_f32_16x16x32_f16 v[116:119], v[140:143], v[176:179], v[116:119]\n" \
"s_setprio 1\n" \
"v_pk_add_f16 v134, v126, v158\n" \
"v_pk_add_f16 v135, v127, v159\n" \
"s_waitcnt vmcnt(9) lgkmcnt(0)\n" \
"s_barrier\n" \
"v_mfma_f32_16x16x32_f16 v[0:3], v[120:123], v[160:163], v[0:3]\n" \
"ds_read_b128 v[176:179], v240 offset:26624\n" \
"v_mfma_f32_16x16x32_f16 v[12:15], v[124:127], v[160:163], v[12:15]\n" \
"v_pk_add_f16 v160, v160, v164\n" \
"v_pk_add_f16 v161, v161, v165\n" \
"v_mfma_f32_16x16x32_f16 v[4:7], v[152:155], v[164:167], v[4:7]\n" \
"v_pk_add_f16 v162, v162, v166\n" \
"v_pk_add_f16 v163, v163, v167\n" \
"v_mfma_f32_16x16x32_f16 v[16:19], v[156:159], v[164:167], v[16:19]\n" \
"ds_read_b128 v[180:183], v240 offset:27136\n" \
"s_add_i32 m0, s44, 0x0\n" \
"s_add_i32 s80, s46, 0x7400000\n" \
"s_bitcmp1_b32 s46, 22\n" \
"s_cselect_b32 s80, 0x80000000, s80\n" \
"v_mfma_f32_16x16x32_f16 v[8:11], v[128:131], v[160:163], v[8:11]\n" \
"buffer_load_dwordx4 v232, s[36:39], s80 offen sc1 lds\n" \
"v_mfma_f32_16x16x32_f16 v[20:23], v[132:135], v[160:163], v[20:23]\n" \
"ds_read_b64_tr_b16 v[136:137], v241 offset:16384\n" \
"ds_read_b64_tr_b16 v[138:139], v241 offset:24576\n" \
"v_mfma_f32_16x16x32_f16 v[24:27], v[120:123], v[168:171], v[24:27]\n" \
"ds_read_b128 v[160:163], v240 offset:28672\n" \
"v_mfma_f32_16x16x32_f16 v[36:39], v[124:127], v[168:171], v[36:39]\n" \
"v_pk_add_f16 v168, v168, v172\n" \
"v_pk_add_f16 v169, v169, v173\n" \
"v_mfma_f32_16x16x32_f16 v[28:31], v[152:155], v[172:175], v[28:31]\n" \
"v_pk_add_f16 v170, v170, v174\n" \
"v_pk_add_f16 v171, v171, v175\n" \
"v_mfma_f32_16x16x32_f16 v[40:43], v[156:159], v[172:175], v[40:43]\n" \
"ds_read_b128 v[164:167], v240 offset:29184\n" \
"ds_read_b64_tr_b16 v[140:141], v242 offset:16384\n" \
"v_mfma_f32_16x16x32_f16 v[32:35], v[128:131], v[168:171], v[32:35]\n" \
"buffer_load_dwordx4 v232, s[36:39], s80 offen offset:1024 sc1 lds\n" \
"v_mfma_f32_16x16x32_f16 v[44:47], v[132:135], v[168:171], v[44:47]\n" \
"ds_read_b64_tr_b16 v[142:143], v242 offset:24576\n" \
"s_waitcnt lgkmcnt(6)\n" \
"v_mfma_f32_16x16x32_f16 v[48:51], v[120:123], v[176:179], v[48:51]\n" \
"ds_read_b128 v[168:171], v240 offset:20480\n" \
"ds_read_b64_tr_b16 v[144:145], v241 offset:16896\n" \
"v_mfma_f32_16x16x32_f16 v[60:63], v[124:127], v[176:179], v[60:63]\n" \
"v_pk_add_f16 v176, v176, v180\n" \
"v_pk_add_f16 v177, v177, v181\n" \
"v_mfma_f32_16x16x32_f16 v[52:55], v[152:155], v[180:183], v[52:55]\n" \
"v_pk_add_f16 v178, v178, v182\n" \
"v_pk_add_f16 v179, v179, v183\n" \
"v_mfma_f32_16x16x32_f16 v[64:67], v[156:159], v[180:183], v[64:67]\n" \
"ds_read_b128 v[172:175], v240 offset:20992\n" \
"v_mfma_f32_16x16x32_f16 v[56:59], v[128:131], v[176:179], v[56:59]\n" \
"buffer_load_dwordx4 v232, s[36:39], s80 offen offset:2048 sc1 lds\n" \
"v_mfma_f32_16x16x32_f16 v[68:71], v[132:135], v[176:179], v[68:71]\n" \
"s_setprio 0\n" \
"ds_read_b64_tr_b16 v[146:147], v241 offset:25088\n" \
"ds_read_b64_tr_b16 v[148:149], v242 offset:16896\n" \
"s_waitcnt lgkmcnt(7)\n" \
"v_mfma_f32_16x16x32_f16 v[72:75], v[120:123], v[160:163], v[72:75]\n" \
"ds_read_b128 v[176:179], v240 offset:22528\n" \
"ds_read_b64_tr_b16 v[150:151], v242 offset:25088\n" \
"v_mfma_f32_16x16x32_f16 v[84:87], v[124:127], v[160:163], v[84:87]\n" \
"v_pk_add_f16 v160, v160, v164\n" \
"v_pk_add_f16 v161, v161, v165\n" \
"v_mfma_f32_16x16x32_f16 v[76:79], v[152:155], v[164:167], v[76:79]\n" \
"v_pk_add_f16 v162, v162, v166\n" \
"v_pk_add_f16 v163, v163, v167\n" \
"v_add_u32 v243, 0x1b000, v241\n" \
"v_mfma_f32_16x16x32_f16 v[88:91], v[156:159], v[164:167], v[88:91]\n" \
"v_xor_b32 v244, 32, v243\n" \
"ds_read_b128 v[180:183], v240 offset:23040\n" \
"v_mfma_f32_16x16x32_f16 v[80:83], v[128:131], v[160:163], v[80:83]\n" \
"buffer_load_dwordx4 v232, s[36:39], s80 offen offset:3072 sc1 lds\n" \
"v_mfma_f32_16x16x32_f16 v[92:95], v[132:135], v[160:163], v[92:95]\n" \
"v_add_u32 v246, 0x15000, v240\n" \
"s_waitcnt lgkmcnt(5)\n" \
"v_mfma_f32_16x16x32_f16 v[96:99], v[120:123], v[168:171], v[96:99]\n" \
"v_pk_add_f16 v120, v136, v144\n" \
"v_pk_add_f16 v121, v137, v145\n" \
"ds_read_b128 v[160:163], v240 offset:24576\n" \
"v_mfma_f32_16x16x32_f16 v[108:111], v[124:127], v[168:171], v[108:111]\n" \
"v_pk_add_f16 v168, v168, v172\n" \
"v_pk_add_f16 v169, v169, v173\n" \
"ds_read_b128 v[164:167], v240 offset:25088\n" \
"v_mfma_f32_16x16x32_f16 v[100:103], v[152:155], v[172:175], v[100:103]\n" \
"v_pk_add_f16 v170, v170, v174\n" \
"v_pk_add_f16 v171, v171, v175\n" \
"s_add_i32 m0, s45, 0x0\n" \
"s_add_i32 s81, s47, 0x7400000\n" \
"v_mfma_f32_16x16x32_f16 v[112:115], v[156:159], v[172:175], v[112:115]\n" \
"s_waitcnt lgkmcnt(3)\n" \
"v_pk_add_f16 v122, v138, v146\n" \
"v_pk_add_f16 v123, v139, v147\n" \
"buffer_load_dwordx4 v232, s[36:39], s81 offen sc1 lds\n" \
"v_mfma_f32_16x16x32_f16 v[104:107], v[128:131], v[168:171], v[104:107]\n" \
"v_pk_add_f16 v124, v140, v148\n" \
"v_pk_add_f16 v125, v141, v149\n" \
"s_mov_b32 m0, 0x27c00\n" \
"v_mfma_f32_16x16x32_f16 v[116:119], v[132:135], v[168:171], v[116:119]\n" \
"v_pk_add_f16 v126, v142, v150\n" \
"v_pk_add_f16 v127, v143, v151\n" \
"global_load_lds_dword v[248:249], off\n" \
"s_waitcnt vmcnt(6) lgkmcnt(0)\n" \
"s_barrier\n" \
"v_mfma_f32_16x16x32_f16 v[0:3], v[136:139], v[176:179], v[0:3]\n" \
"ds_read_b128 v[168:171], v240 offset:43008\n" \
"s_add_i32 m0, s44, 0x5000\n" \
"s_add_i32 s80, s46, 0x7800000\n" \
"s_bitcmp1_b32 s46, 22\n" \
"s_cselect_b32 s80, 0x80000000, s80\n" \
"ds_read_b64_tr_b16 v[128:129], v241 offset:32768\n" \
"v_mfma_f32_16x16x32_f16 v[12:15], v[140:143], v[176:179], v[12:15]\n" \
"v_pk_add_f16 v176, v176, v180\n" \
"v_pk_add_f16 v177, v177, v181\n" \
"buffer_load_dwordx4 v232, s[36:39], s80 offen sc1 lds\n" \
"ds_read_b64_tr_b16 v[130:131], v241 offset:40960\n" \
"v_mfma_f32_16x16x32_f16 v[4:7], v[144:147], v[180:183], v[4:7]\n" \
"v_pk_add_f16 v178, v178, v182\n" \
"v_pk_add_f16 v179, v179, v183\n" \
"buffer_load_dwordx4 v232, s[36:39], s80 offen offset:1024 sc1 lds\n" \
"ds_read_b64_tr_b16 v[132:133], v242 offset:32768\n" \
"v_mfma_f32_16x16x32_f16 v[16:19], v[148:151], v[180:183], v[16:19]\n" \
"ds_read_b128 v[172:175], v240 offset:43520\n" \
"buffer_load_dwordx4 v232, s[36:39], s80 offen offset:2048 sc1 lds\n" \
"ds_read_b64_tr_b16 v[134:135], v242 offset:40960\n" \
"ds_read_b64_tr_b16 v[152:153], v241 offset:33280\n" \
"v_mfma_f32_16x16x32_f16 v[8:11], v[120:123], v[176:179], v[8:11]\n" \
"buffer_load_dwordx4 v232, s[36:39], s80 offen offset:3072 sc1 lds\n" \
"ds_read_b64_tr_b16 v[154:155], v241 offset:41472\n" \
"v_mfma_f32_16x16x32_f16 v[20:23], v[124:127], v[176:179], v[20:23]\n" \
"s_add_i32 m0, s45, 0x5000\n" \
"s_add_i32 s81, s47, 0x7800000\n" \
"ds_read_b64_tr_b16 v[156:157], v242 offset:33280\n" \
"ds_read_b64_tr_b16 v[158:159], v242 offset:41472\n" \
"v_mfma_f32_16x16x32_f16 v[24:27], v[136:139], v[160:163], v[24:27]\n" \
"ds_read_b128 v[176:179], v246 offset:2048\n" \
"buffer_load_dwordx4 v232, s[36:39], s81 offen sc1 lds\n" \
"ds_read_b64_tr_b16 v[184:185], v243 offset:0\n" \
"v_mfma_f32_16x16x32_f16 v[36:39], v[140:143], v[160:163], v[36:39]\n" \
"v_pk_add_f16 v160, v160, v164\n" \
"v_pk_add_f16 v161, v161, v165\n" \
"ds_read_b64_tr_b16 v[186:187], v243 offset:8192\n" \
"ds_read_b64_tr_b16 v[188:189], v244 offset:0\n" \
"v_mfma_f32_16x16x32_f16 v[28:31], v[144:147], v[164:167], v[28:31]\n" \
"v_pk_add_f16 v162, v162, v166\n" \
"v_pk_add_f16 v163, v163, v167\n" \
"s_add_i32 m0, s44, 0xffff7000\n" \
"s_add_i32 s80, s46, 0x7c00000\n" \
"s_bitcmp0_b32 s46, 22\n" \
"s_cselect_b64 exec, -1, 0\n" \
"buffer_load_dwordx4 v232, s[36:39], s80 offen sc1 lds\n" \
"buffer_load_dwordx4 v232, s[36:39], s80 offen offset:1024 sc1 lds\n" \
"buffer_load_dwordx4 v232, s[36:39], s80 offen offset:2048 sc1 lds\n" \
"buffer_load_dwordx4 v232, s[36:39], s80 offen offset:3072 sc1 lds\n" \
"s_mov_b64 exec, -1\n" \
"v_mfma_f32_16x16x32_f16 v[40:43], v[148:151], v[164:167], v[40:43]\n" \
"ds_read_b128 v[180:183], v246 offset:2560\n" \
"s_add_i32 m0, s45, 0xffff7000\n" \
"s_add_i32 s81, s47, 0x7c00000\n" \
"ds_read_b64_tr_b16 v[190:191], v244 offset:8192\n" \
"ds_read_b64_tr_b16 v[192:193], v243 offset:512\n" \
"v_mfma_f32_16x16x32_f16 v[32:35], v[120:123], v[160:163], v[32:35]\n" \
"buffer_load_dwordx4 v232, s[36:39], s81 offen sc1 lds\n" \
"ds_read_b64_tr_b16 v[194:195], v243 offset:8704\n" \
"v_mfma_f32_16x16x32_f16 v[44:47], v[124:127], v[160:163], v[44:47]\n" \
"s_waitcnt lgkmcnt(5)\n" \
"v_pk_add_f16 v232, v128, v152\n" \
"v_pk_add_f16 v233, v129, v153\n" \
"ds_read_b64_tr_b16 v[196:197], v244 offset:512\n" \
"ds_read_b64_tr_b16 v[198:199], v244 offset:8704\n" \
"v_mfma_f32_16x16x32_f16 v[0:3], v[128:131], v[168:171], v[0:3]\n" \
"v_pk_add_f16 v234, v130, v154\n" \
"ds_read_b128 v[160:163], v246 offset:10240\n" \
"ds_read_b64_tr_b16 v[208:209], v243 offset:16384\n" \
"ds_read_b64_tr_b16 v[210:211], v243 offset:24576\n" \
"v_mfma_f32_16x16x32_f16 v[12:15], v[132:135], v[168:171], v[12:15]\n" \
"v_pk_add_f16 v168, v168, v172\n" \
"v_pk_add_f16 v169, v169, v173\n" \
"v_pk_add_f16 v235, v131, v155\n" \
"v_pk_add_f16 v236, v132, v156\n" \
"ds_read_b64_tr_b16 v[212:213], v244 offset:16384\n" \
"v_mfma_f32_16x16x32_f16 v[4:7], v[152:155], v[172:175], v[4:7]\n" \
"v_pk_add_f16 v170, v170, v174\n" \
"v_pk_add_f16 v171, v171, v175\n" \
"v_pk_add_f16 v237, v133, v157\n" \
"v_pk_add_f16 v238, v134, v158\n" \
"v_pk_add_f16 v239, v135, v159\n" \
"v_mfma_f32_16x16x32_f16 v[16:19], v[156:159], v[172:175], v[16:19]\n" \
"s_waitcnt lgkmcnt(6)\n" \
"v_pk_add_f16 v200, v184, v192\n" \
"ds_read_b128 v[164:167], v246 offset:10752\n" \
"ds_read_b64_tr_b16 v[214:215], v244 offset:24576\n" \
"ds_read_b64_tr_b16 v[216:217], v243 offset:16896\n" \
"v_mfma_f32_16x16x32_f16 v[8:11], v[232:235], v[168:171], v[8:11]\n" \
"v_pk_add_f16 v201, v185, v193\n" \
"v_pk_add_f16 v202, v186, v194\n" \
"ds_read_b64_tr_b16 v[218:219], v243 offset:25088\n" \
"v_mfma_f32_16x16x32_f16 v[20:23], v[236:239], v[168:171], v[20:23]\n" \
"ds_read_b64_tr_b16 v[220:221], v244 offset:16896\n" \
"ds_read_b64_tr_b16 v[222:223], v244 offset:25088\n" \
"v_mfma_f32_16x16x32_f16 v[0:3], v[184:187], v[176:179], v[0:3]\n" \
"v_pk_add_f16 v203, v187, v195\n" \
"s_waitcnt lgkmcnt(7)\n" \
"v_pk_add_f16 v204, v188, v196\n" \
"ds_read_b128 v[168:171], v240 offset:45056\n" \
"v_mfma_f32_16x16x32_f16 v[12:15], v[188:191], v[176:179], v[12:15]\n" \
"v_pk_add_f16 v176, v176, v180\n" \
"v_pk_add_f16 v177, v177, v181\n" \
"v_pk_add_f16 v205, v189, v197\n" \
"v_mfma_f32_16x16x32_f16 v[4:7], v[192:195], v[180:183], v[4:7]\n" \
"v_pk_add_f16 v178, v178, v182\n" \
"v_pk_add_f16 v179, v179, v183\n" \
"v_pk_add_f16 v206, v190, v198\n" \
"v_pk_add_f16 v207, v191, v199\n" \
"v_mfma_f32_16x16x32_f16 v[16:19], v[196:199], v[180:183], v[16:19]\n" \
"s_waitcnt lgkmcnt(4)\n" \
"v_pk_add_f16 v224, v208, v216\n" \
"ds_read_b128 v[172:175], v240 offset:45568\n" \
"v_mfma_f32_16x16x32_f16 v[8:11], v[200:203], v[176:179], v[8:11]\n" \
"v_pk_add_f16 v225, v209, v217\n" \
"v_mfma_f32_16x16x32_f16 v[20:23], v[204:207], v[176:179], v[20:23]\n" \
"s_waitcnt lgkmcnt(4)\n" \
"v_pk_add_f16 v226, v210, v218\n" \
"v_pk_add_f16 v227, v211, v219\n" \
"v_mfma_f32_16x16x32_f16 v[0:3], v[208:211], v[160:163], v[0:3]\n" \
"s_waitcnt lgkmcnt(2)\n" \
"v_pk_add_f16 v228, v212, v220\n" \
"v_pk_add_f16 v229, v213, v221\n" \
"ds_read_b128 v[176:179], v246 offset:4096\n" \
"v_mfma_f32_16x16x32_f16 v[12:15], v[212:215], v[160:163], v[12:15]\n" \
"v_pk_add_f16 v160, v160, v164\n" \
"v_pk_add_f16 v161, v161, v165\n" \
"v_pk_add_f16 v230, v214, v222\n" \
"v_mfma_f32_16x16x32_f16 v[4:7], v[216:219], v[164:167], v[4:7]\n" \
"v_pk_add_f16 v162, v162, v166\n" \
"v_pk_add_f16 v163, v163, v167\n" \
"v_pk_add_f16 v231, v215, v223\n" \
"v_mfma_f32_16x16x32_f16 v[16:19], v[220:223], v[164:167], v[16:19]\n" \
"ds_read_b128 v[180:183], v246 offset:4608\n" \
"s_mov_b32 vcc_lo, 0xaaaaaaaa\n" \
"v_mfma_f32_16x16x32_f16 v[8:11], v[224:227], v[160:163], v[8:11]\n" \
"s_mov_b32 vcc_hi, 0xaaaaaaaa\n" \
"v_mfma_f32_16x16x32_f16 v[20:23], v[228:231], v[160:163], v[20:23]\n" \
"v_add_u32 v241, 0xffff7000, v240\n" \
"s_waitcnt lgkmcnt(2)\n" \
"v_mfma_f32_16x16x32_f16 v[24:27], v[128:131], v[168:171], v[24:27]\n" \
"ds_read_b128 v[160:163], v246 offset:12288\n" \
"v_mfma_f32_16x16x32_f16 v[36:39], v[132:135], v[168:171], v[36:39]\n" \
"v_pk_add_f16 v168, v168, v172\n" \
"v_pk_add_f16 v169, v169, v173\n" \
"v_mfma_f32_16x16x32_f16 v[28:31], v[152:155], v[172:175], v[28:31]\n" \
"v_pk_add_f16 v170, v170, v174\n" \
"v_pk_add_f16 v171, v171, v175\n" \
"v_add_f32 v252, v4, v8\n" \
"v_add_f32 v253, v5, v9\n" \
"v_add_f32 v254, v6, v10\n" \
"v_add_f32 v255, v7, v11\n" \
"v_mfma_f32_16x16x32_f16 v[40:43], v[156:159], v[172:175], v[40:43]\n" \
"ds_read_b128 v[164:167], v246 offset:12800\n" \
"v_fmac_f32 v4, s56, v8\n" \
"v_fmac_f32 v5, s56, v9\n" \
"v_fmac_f32 v6, s56, v10\n" \
"v_fmac_f32 v7, s56, v11\n" \
"v_mfma_f32_16x16x32_f16 v[32:35], v[232:235], v[168:171], v[32:35]\n" \
"v_fmac_f32 v0, s57, v252\n" \
"v_fmac_f32 v1, s57, v253\n" \
"v_fmac_f32 v2, s57, v254\n" \
"v_fmac_f32 v3, s57, v255\n" \
"v_add_f32 v252, v16, v20\n" \
"v_mfma_f32_16x16x32_f16 v[44:47], v[236:239], v[168:171], v[44:47]\n" \
"v_add_f32 v253, v17, v21\n" \
"v_add_f32 v254, v18, v22\n" \
"v_add_f32 v255, v19, v23\n" \
"v_fmac_f32 v16, s56, v20\n" \
"s_waitcnt lgkmcnt(1)\n" \
"v_mfma_f32_16x16x32_f16 v[24:27], v[184:187], v[176:179], v[24:27]\n" \
"ds_read_b128 v[168:171], v240 offset:47104\n" \
"v_fmac_f32 v17, s56, v21\n" \
"v_fmac_f32 v18, s56, v22\n" \
"v_fmac_f32 v19, s56, v23\n" \
"v_fmac_f32 v12, s57, v252\n" \
"v_fmac_f32 v13, s57, v253\n" \
"v_mfma_f32_16x16x32_f16 v[36:39], v[188:191], v[176:179], v[36:39]\n" \
"v_pk_add_f16 v176, v176, v180\n" \
"v_pk_add_f16 v177, v177, v181\n" \
"v_fmac_f32 v14, s57, v254\n" \
"v_fmac_f32 v15, s57, v255\n" \
"v_cvt_pk_bf16_f32 v8, v0, v1\n" \
"v_cvt_pk_bf16_f32 v20, v4, v5\n" \
"v_mfma_f32_16x16x32_f16 v[28:31], v[192:195], v[180:183], v[28:31]\n" \
"v_pk_add_f16 v178, v178, v182\n" \
"v_pk_add_f16 v179, v179, v183\n" \
"v_cvt_pk_bf16_f32 v9, v2, v3\n" \
"v_cvt_pk_bf16_f32 v21, v6, v7\n" \
"v_cvt_pk_bf16_f32 v10, v12, v13\n" \
"v_cvt_pk_bf16_f32 v22, v16, v17\n" \
"v_cvt_pk_bf16_f32 v11, v14, v15\n" \
"v_mfma_f32_16x16x32_f16 v[40:43], v[196:199], v[180:183], v[40:43]\n" \
"ds_read_b128 v[172:175], v240 offset:47616\n" \
"v_cvt_pk_bf16_f32 v23, v18, v19\n" \
"s_not_b64 vcc, vcc\n" \
"v_cndmask_b32_dpp v0, v20, v8, vcc quad_perm:[1,0,3,2] row_mask:0xf bank_mask:0xf\n" \
"v_cndmask_b32_dpp v1, v21, v9, vcc quad_perm:[1,0,3,2] row_mask:0xf bank_mask:0xf\n" \
"v_mfma_f32_16x16x32_f16 v[32:35], v[200:203], v[176:179], v[32:35]\n" \
"s_waitcnt vmcnt(0)\n" \
"s_barrier\n" \
"v_cndmask_b32_dpp v2, v22, v10, vcc quad_perm:[1,0,3,2] row_mask:0xf bank_mask:0xf\n" \
"v_cndmask_b32_dpp v3, v23, v11, vcc quad_perm:[1,0,3,2] row_mask:0xf bank_mask:0xf\n" \
"buffer_store_dwordx4 v[0:3], v245, s[40:43], s69 offen sc1\n" \
"s_not_b64 vcc, vcc\n" \
"s_add_i32 s85, s69, 0x10000\n" \
"v_mfma_f32_16x16x32_f16 v[44:47], v[204:207], v[176:179], v[44:47]\n" \
"v_cndmask_b32_dpp v12, v8, v20, vcc quad_perm:[1,0,3,2] row_mask:0xf bank_mask:0xf\n" \
"v_cndmask_b32_dpp v13, v9, v21, vcc quad_perm:[1,0,3,2] row_mask:0xf bank_mask:0xf\n" \
"v_cndmask_b32_dpp v14, v10, v22, vcc quad_perm:[1,0,3,2] row_mask:0xf bank_mask:0xf\n" \
"v_cndmask_b32_dpp v15, v11, v23, vcc quad_perm:[1,0,3,2] row_mask:0xf bank_mask:0xf\n" \
"buffer_store_dwordx4 v[12:15], v245, s[40:43], s85 offen sc1\n" \
"v_mfma_f32_16x16x32_f16 v[24:27], v[208:211], v[160:163], v[24:27]\n" \
"ds_read_b128 v[176:179], v240 offset:6144\n" \
"v_mfma_f32_16x16x32_f16 v[36:39], v[212:215], v[160:163], v[36:39]\n" \
"s_waitcnt lgkmcnt(2)\n" \
"v_pk_add_f16 v160, v160, v164\n" \
"v_pk_add_f16 v161, v161, v165\n" \
"v_mfma_f32_16x16x32_f16 v[28:31], v[216:219], v[164:167], v[28:31]\n" \
"v_pk_add_f16 v162, v162, v166\n" \
"v_pk_add_f16 v163, v163, v167\n" \
"v_mfma_f32_16x16x32_f16 v[40:43], v[220:223], v[164:167], v[40:43]\n" \
"ds_read_b128 v[180:183], v240 offset:6656\n" \
"v_mfma_f32_16x16x32_f16 v[32:35], v[224:227], v[160:163], v[32:35]\n" \
"v_mfma_f32_16x16x32_f16 v[44:47], v[228:231], v[160:163], v[44:47]\n" \
"v_mfma_f32_16x16x32_f16 v[48:51], v[136:139], v[168:171], v[48:51]\n" \
"ds_read_b128 v[160:163], v240 offset:26624\n" \
"v_mfma_f32_16x16x32_f16 v[60:63], v[140:143], v[168:171], v[60:63]\n" \
"s_waitcnt lgkmcnt(3)\n" \
"v_pk_add_f16 v168, v168, v172\n" \
"v_pk_add_f16 v169, v169, v173\n" \
"v_mfma_f32_16x16x32_f16 v[52:55], v[144:147], v[172:175], v[52:55]\n" \
"v_pk_add_f16 v170, v170, v174\n" \
"v_pk_add_f16 v171, v171, v175\n" \
"v_add_f32 v252, v28, v32\n" \
"v_add_f32 v253, v29, v33\n" \
"v_mfma_f32_16x16x32_f16 v[64:67], v[148:151], v[172:175], v[64:67]\n" \
"ds_read_b128 v[164:167], v240 offset:27136\n" \
"v_add_f32 v254, v30, v34\n" \
"v_add_f32 v255, v31, v35\n" \
"v_fmac_f32 v28, s56, v32\n" \
"v_mfma_f32_16x16x32_f16 v[56:59], v[120:123], v[168:171], v[56:59]\n" \
"v_fmac_f32 v29, s56, v33\n" \
"v_fmac_f32 v30, s56, v34\n" \
"v_fmac_f32 v31, s56, v35\n" \
"v_mfma_f32_16x16x32_f16 v[68:71], v[124:127], v[168:171], v[68:71]\n" \
"v_fmac_f32 v24, s57, v252\n" \
"v_fmac_f32 v25, s57, v253\n" \
"v_fmac_f32 v26, s57, v254\n" \
"s_waitcnt lgkmcnt(2)\n" \
"v_mfma_f32_16x16x32_f16 v[48:51], v[128:131], v[176:179], v[48:51]\n" \
"ds_read_b128 v[168:171], v241 offset:6144\n" \
"v_fmac_f32 v27, s57, v255\n" \
"v_add_f32 v252, v40, v44\n" \
"v_add_f32 v253, v41, v45\n" \
"v_mfma_f32_16x16x32_f16 v[60:63], v[132:135], v[176:179], v[60:63]\n" \
"v_pk_add_f16 v176, v176, v180\n" \
"v_pk_add_f16 v177, v177, v181\n" \
"v_add_f32 v254, v42, v46\n" \
"v_add_f32 v255, v43, v47\n" \
"v_mfma_f32_16x16x32_f16 v[52:55], v[152:155], v[180:183], v[52:55]\n" \
"v_pk_add_f16 v178, v178, v182\n" \
"v_pk_add_f16 v179, v179, v183\n" \
"v_fmac_f32 v40, s56, v44\n" \
"v_fmac_f32 v41, s56, v45\n" \
"v_fmac_f32 v42, s56, v46\n" \
"v_mfma_f32_16x16x32_f16 v[64:67], v[156:159], v[180:183], v[64:67]\n" \
"ds_read_b128 v[172:175], v241 offset:6656\n" \
"v_fmac_f32 v43, s56, v47\n" \
"v_fmac_f32 v36, s57, v252\n" \
"v_fmac_f32 v37, s57, v253\n" \
"v_mfma_f32_16x16x32_f16 v[56:59], v[232:235], v[176:179], v[56:59]\n" \
"v_fmac_f32 v38, s57, v254\n" \
"v_fmac_f32 v39, s57, v255\n" \
"v_cvt_pk_bf16_f32 v32, v24, v25\n" \
"v_mfma_f32_16x16x32_f16 v[68:71], v[236:239], v[176:179], v[68:71]\n" \
"v_cvt_pk_bf16_f32 v44, v28, v29\n" \
"v_cvt_pk_bf16_f32 v33, v26, v27\n" \
"v_cvt_pk_bf16_f32 v45, v30, v31\n" \
"s_waitcnt lgkmcnt(1)\n" \
"v_mfma_f32_16x16x32_f16 v[48:51], v[184:187], v[160:163], v[48:51]\n" \
"ds_read_b128 v[176:179], v240 offset:49152\n" \
"v_cvt_pk_bf16_f32 v34, v36, v37\n" \
"v_cvt_pk_bf16_f32 v46, v40, v41\n" \
"v_mfma_f32_16x16x32_f16 v[60:63], v[188:191], v[160:163], v[60:63]\n" \
"v_pk_add_f16 v160, v160, v164\n" \
"v_pk_add_f16 v161, v161, v165\n" \
"v_cvt_pk_bf16_f32 v35, v38, v39\n" \
"v_cvt_pk_bf16_f32 v47, v42, v43\n" \
"s_not_b64 vcc, vcc\n" \
"v_mfma_f32_16x16x32_f16 v[52:55], v[192:195], v[164:167], v[52:55]\n" \
"v_pk_add_f16 v162, v162, v166\n" \
"v_pk_add_f16 v163, v163, v167\n" \
"v_cndmask_b32_dpp v24, v44, v32, vcc quad_perm:[1,0,3,2] row_mask:0xf bank_mask:0xf\n" \
"v_cndmask_b32_dpp v25, v45, v33, vcc quad_perm:[1,0,3,2] row_mask:0xf bank_mask:0xf\n" \
"v_cndmask_b32_dpp v26, v46, v34, vcc quad_perm:[1,0,3,2] row_mask:0xf bank_mask:0xf\n" \
"v_mfma_f32_16x16x32_f16 v[64:67], v[196:199], v[164:167], v[64:67]\n" \
"ds_read_b128 v[180:183], v240 offset:49664\n" \
"v_cndmask_b32_dpp v27, v47, v35, vcc quad_perm:[1,0,3,2] row_mask:0xf bank_mask:0xf\n" \
"buffer_store_dwordx4 v[24:27], v245, s[40:43], s70 offen sc1\n" \
"s_not_b64 vcc, vcc\n" \
"v_mfma_f32_16x16x32_f16 v[56:59], v[200:203], v[160:163], v[56:59]\n" \
"s_add_i32 s85, s70, 0x10000\n" \
"v_cndmask_b32_dpp v36, v32, v44, vcc quad_perm:[1,0,3,2] row_mask:0xf bank_mask:0xf\n" \
"v_cndmask_b32_dpp v37, v33, v45, vcc quad_perm:[1,0,3,2] row_mask:0xf bank_mask:0xf\n" \
"v_mfma_f32_16x16x32_f16 v[68:71], v[204:207], v[160:163], v[68:71]\n" \
"v_cndmask_b32_dpp v38, v34, v46, vcc quad_perm:[1,0,3,2] row_mask:0xf bank_mask:0xf\n" \
"v_cndmask_b32_dpp v39, v35, v47, vcc quad_perm:[1,0,3,2] row_mask:0xf bank_mask:0xf\n" \
"buffer_store_dwordx4 v[36:39], v245, s[40:43], s85 offen sc1\n" \
"v_mfma_f32_16x16x32_f16 v[48:51], v[208:211], v[168:171], v[48:51]\n" \
"ds_read_b128 v[160:163], v240 offset:8192\n" \
"v_mfma_f32_16x16x32_f16 v[60:63], v[212:215], v[168:171], v[60:63]\n" \
"s_waitcnt lgkmcnt(2)\n" \
"v_pk_add_f16 v168, v168, v172\n" \
"v_pk_add_f16 v169, v169, v173\n" \
"v_mfma_f32_16x16x32_f16 v[52:55], v[216:219], v[172:175], v[52:55]\n" \
"v_pk_add_f16 v170, v170, v174\n" \
"v_pk_add_f16 v171, v171, v175\n" \
"v_mfma_f32_16x16x32_f16 v[64:67], v[220:223], v[172:175], v[64:67]\n" \
"ds_read_b128 v[164:167], v240 offset:8704\n" \
"v_mfma_f32_16x16x32_f16 v[56:59], v[224:227], v[168:171], v[56:59]\n" \
"v_mfma_f32_16x16x32_f16 v[68:71], v[228:231], v[168:171], v[68:71]\n" \
"v_mfma_f32_16x16x32_f16 v[72:75], v[136:139], v[176:179], v[72:75]\n" \
"ds_read_b128 v[168:171], v240 offset:28672\n" \
"v_mfma_f32_16x16x32_f16 v[84:87], v[140:143], v[176:179], v[84:87]\n" \
"s_waitcnt lgkmcnt(3)\n" \
"v_pk_add_f16 v176, v176, v180\n" \
"v_pk_add_f16 v177, v177, v181\n" \
"v_mfma_f32_16x16x32_f16 v[76:79], v[144:147], v[180:183], v[76:79]\n" \
"v_pk_add_f16 v178, v178, v182\n" \
"v_pk_add_f16 v179, v179, v183\n" \
"v_add_f32 v252, v52, v56\n" \
"v_add_f32 v253, v53, v57\n" \
"v_mfma_f32_16x16x32_f16 v[88:91], v[148:151], v[180:183], v[88:91]\n" \
"ds_read_b128 v[172:175], v240 offset:29184\n" \
"v_add_f32 v254, v54, v58\n" \
"v_add_f32 v255, v55, v59\n" \
"v_mfma_f32_16x16x32_f16 v[80:83], v[120:123], v[176:179], v[80:83]\n" \
"v_fmac_f32 v52, s56, v56\n" \
"v_fmac_f32 v53, s56, v57\n" \
"v_mfma_f32_16x16x32_f16 v[92:95], v[124:127], v[176:179], v[92:95]\n" \
"v_fmac_f32 v54, s56, v58\n" \
"v_fmac_f32 v55, s56, v59\n" \
"s_waitcnt lgkmcnt(2)\n" \
"v_mfma_f32_16x16x32_f16 v[72:75], v[128:131], v[160:163], v[72:75]\n" \
"ds_read_b128 v[176:179], v241 offset:8192\n" \
"v_fmac_f32 v48, s57, v252\n" \
"v_fmac_f32 v49, s57, v253\n" \
"v_mfma_f32_16x16x32_f16 v[84:87], v[132:135], v[160:163], v[84:87]\n" \
"v_pk_add_f16 v160, v160, v164\n" \
"v_pk_add_f16 v161, v161, v165\n" \
"v_fmac_f32 v50, s57, v254\n" \
"v_fmac_f32 v51, s57, v255\n" \
"v_mfma_f32_16x16x32_f16 v[76:79], v[152:155], v[164:167], v[76:79]\n" \
"v_pk_add_f16 v162, v162, v166\n" \
"v_pk_add_f16 v163, v163, v167\n" \
"v_add_f32 v252, v64, v68\n" \
"v_add_f32 v253, v65, v69\n" \
"v_mfma_f32_16x16x32_f16 v[88:91], v[156:159], v[164:167], v[88:91]\n" \
"ds_read_b128 v[180:183], v241 offset:8704\n" \
"v_add_f32 v254, v66, v70\n" \
"v_add_f32 v255, v67, v71\n" \
"v_mfma_f32_16x16x32_f16 v[80:83], v[232:235], v[160:163], v[80:83]\n" \
"v_fmac_f32 v64, s56, v68\n" \
"v_fmac_f32 v65, s56, v69\n" \
"v_mfma_f32_16x16x32_f16 v[92:95], v[236:239], v[160:163], v[92:95]\n" \
"v_fmac_f32 v66, s56, v70\n" \
"v_fmac_f32 v67, s56, v71\n" \
"s_waitcnt lgkmcnt(2)\n" \
"v_mfma_f32_16x16x32_f16 v[72:75], v[184:187], v[168:171], v[72:75]\n" \
"ds_read_b128 v[160:163], v240 offset:40960\n" \
"v_fmac_f32 v60, s57, v252\n" \
"v_fmac_f32 v61, s57, v253\n" \
"v_mfma_f32_16x16x32_f16 v[84:87], v[188:191], v[168:171], v[84:87]\n" \
"v_pk_add_f16 v168, v168, v172\n" \
"v_pk_add_f16 v169, v169, v173\n" \
"v_fmac_f32 v62, s57, v254\n" \
"v_fmac_f32 v63, s57, v255\n" \
"v_mfma_f32_16x16x32_f16 v[76:79], v[192:195], v[172:175], v[76:79]\n" \
"v_pk_add_f16 v170, v170, v174\n" \
"v_pk_add_f16 v171, v171, v175\n" \
"v_cvt_pk_bf16_f32 v56, v48, v49\n" \
"v_cvt_pk_bf16_f32 v68, v52, v53\n" \
"v_mfma_f32_16x16x32_f16 v[88:91], v[196:199], v[172:175], v[88:91]\n" \
"ds_read_b128 v[164:167], v240 offset:41472\n" \
"v_cvt_pk_bf16_f32 v57, v50, v51\n" \
"v_cvt_pk_bf16_f32 v69, v54, v55\n" \
"v_mfma_f32_16x16x32_f16 v[80:83], v[200:203], v[168:171], v[80:83]\n" \
"v_cvt_pk_bf16_f32 v58, v60, v61\n" \
"v_cvt_pk_bf16_f32 v70, v64, v65\n" \
"v_mfma_f32_16x16x32_f16 v[92:95], v[204:207], v[168:171], v[92:95]\n" \
"v_cvt_pk_bf16_f32 v59, v62, v63\n" \
"v_cvt_pk_bf16_f32 v71, v66, v67\n" \
"s_waitcnt lgkmcnt(2)\n" \
"v_mfma_f32_16x16x32_f16 v[72:75], v[208:211], v[176:179], v[72:75]\n" \
"ds_read_b128 v[168:171], v240 offset:0\n" \
"s_not_b64 vcc, vcc\n" \
"v_cndmask_b32_dpp v48, v68, v56, vcc quad_perm:[1,0,3,2] row_mask:0xf bank_mask:0xf\n" \
"v_mfma_f32_16x16x32_f16 v[84:87], v[212:215], v[176:179], v[84:87]\n" \
"v_pk_add_f16 v176, v176, v180\n" \
"v_pk_add_f16 v177, v177, v181\n" \
"v_cndmask_b32_dpp v49, v69, v57, vcc quad_perm:[1,0,3,2] row_mask:0xf bank_mask:0xf\n" \
"v_cndmask_b32_dpp v50, v70, v58, vcc quad_perm:[1,0,3,2] row_mask:0xf bank_mask:0xf\n" \
"v_mfma_f32_16x16x32_f16 v[76:79], v[216:219], v[180:183], v[76:79]\n" \
"v_pk_add_f16 v178, v178, v182\n" \
"v_pk_add_f16 v179, v179, v183\n" \
"v_cndmask_b32_dpp v51, v71, v59, vcc quad_perm:[1,0,3,2] row_mask:0xf bank_mask:0xf\n" \
"buffer_store_dwordx4 v[48:51], v245, s[40:43], s71 offen sc1\n" \
"v_mfma_f32_16x16x32_f16 v[88:91], v[220:223], v[180:183], v[88:91]\n" \
"ds_read_b128 v[172:175], v240 offset:512\n" \
"s_not_b64 vcc, vcc\n" \
"s_add_i32 s85, s71, 0x10000\n" \
"v_mfma_f32_16x16x32_f16 v[80:83], v[224:227], v[176:179], v[80:83]\n" \
"v_cndmask_b32_dpp v60, v56, v68, vcc quad_perm:[1,0,3,2] row_mask:0xf bank_mask:0xf\n" \
"v_cndmask_b32_dpp v61, v57, v69, vcc quad_perm:[1,0,3,2] row_mask:0xf bank_mask:0xf\n" \
"v_mfma_f32_16x16x32_f16 v[92:95], v[228:231], v[176:179], v[92:95]\n" \
"v_cndmask_b32_dpp v62, v58, v70, vcc quad_perm:[1,0,3,2] row_mask:0xf bank_mask:0xf\n" \
"v_cndmask_b32_dpp v63, v59, v71, vcc quad_perm:[1,0,3,2] row_mask:0xf bank_mask:0xf\n" \
"buffer_store_dwordx4 v[60:63], v245, s[40:43], s85 offen sc1\n" \
"s_waitcnt lgkmcnt(1)\n" \
"v_mfma_f32_16x16x32_f16 v[96:99], v[136:139], v[160:163], v[96:99]\n" \
"ds_read_b128 v[176:179], v240 offset:20480\n" \
"v_mfma_f32_16x16x32_f16 v[108:111], v[140:143], v[160:163], v[108:111]\n" \
"v_pk_add_f16 v160, v160, v164\n" \
"v_pk_add_f16 v161, v161, v165\n" \
"v_mfma_f32_16x16x32_f16 v[100:103], v[144:147], v[164:167], v[100:103]\n" \
"v_pk_add_f16 v162, v162, v166\n" \
"v_pk_add_f16 v163, v163, v167\n" \
"v_add_f32 v252, v76, v80\n" \
"v_add_f32 v253, v77, v81\n" \
"v_mfma_f32_16x16x32_f16 v[112:115], v[148:151], v[164:167], v[112:115]\n" \
"ds_read_b128 v[180:183], v240 offset:20992\n" \
"v_add_f32 v254, v78, v82\n" \
"v_add_f32 v255, v79, v83\n" \
"v_mfma_f32_16x16x32_f16 v[104:107], v[120:123], v[160:163], v[104:107]\n" \
"v_fmac_f32 v76, s56, v80\n" \
"v_fmac_f32 v77, s56, v81\n" \
"v_mfma_f32_16x16x32_f16 v[116:119], v[124:127], v[160:163], v[116:119]\n" \
"v_fmac_f32 v78, s56, v82\n" \
"v_fmac_f32 v79, s56, v83\n" \
"v_mfma_f32_16x16x32_f16 v[96:99], v[128:131], v[168:171], v[96:99]\n" \
"ds_read_b128 v[160:163], v241 offset:0\n" \
"v_fmac_f32 v72, s57, v252\n" \
"v_fmac_f32 v73, s57, v253\n" \
"v_mfma_f32_16x16x32_f16 v[108:111], v[132:135], v[168:171], v[108:111]\n" \
"s_waitcnt lgkmcnt(2)\n" \
"v_pk_add_f16 v168, v168, v172\n" \
"v_pk_add_f16 v169, v169, v173\n" \
"v_fmac_f32 v74, s57, v254\n" \
"v_fmac_f32 v75, s57, v255\n" \
"v_mfma_f32_16x16x32_f16 v[100:103], v[152:155], v[172:175], v[100:103]\n" \
"v_pk_add_f16 v170, v170, v174\n" \
"v_pk_add_f16 v171, v171, v175\n" \
"v_add_f32 v252, v88, v92\n" \
"v_add_f32 v253, v89, v93\n" \
"v_mfma_f32_16x16x32_f16 v[112:115], v[156:159], v[172:175], v[112:115]\n" \
"ds_read_b128 v[164:167], v241 offset:512\n" \
"v_add_f32 v254, v90, v94\n" \
"v_add_f32 v255, v91, v95\n" \
"v_mfma_f32_16x16x32_f16 v[104:107], v[232:235], v[168:171], v[104:107]\n" \
"v_fmac_f32 v88, s56, v92\n" \
"v_fmac_f32 v89, s56, v93\n" \
"v_mfma_f32_16x16x32_f16 v[116:119], v[236:239], v[168:171], v[116:119]\n" \
"v_fmac_f32 v90, s56, v94\n" \
"v_fmac_f32 v91, s56, v95\n" \
"v_mfma_f32_16x16x32_f16 v[96:99], v[184:187], v[176:179], v[96:99]\n" \
"v_fmac_f32 v84, s57, v252\n" \
"v_fmac_f32 v85, s57, v253\n" \
"v_fmac_f32 v86, s57, v254\n" \
"v_mfma_f32_16x16x32_f16 v[108:111], v[188:191], v[176:179], v[108:111]\n" \
"s_waitcnt lgkmcnt(1)\n" \
"v_pk_add_f16 v176, v176, v180\n" \
"v_pk_add_f16 v177, v177, v181\n" \
"v_fmac_f32 v87, s57, v255\n" \
"v_mfma_f32_16x16x32_f16 v[100:103], v[192:195], v[180:183], v[100:103]\n" \
"v_pk_add_f16 v178, v178, v182\n" \
"v_pk_add_f16 v179, v179, v183\n" \
"v_cvt_pk_bf16_f32 v80, v72, v73\n" \
"v_cvt_pk_bf16_f32 v92, v76, v77\n" \
"v_mfma_f32_16x16x32_f16 v[112:115], v[196:199], v[180:183], v[112:115]\n" \
"v_cvt_pk_bf16_f32 v81, v74, v75\n" \
"v_cvt_pk_bf16_f32 v93, v78, v79\n" \
"v_mfma_f32_16x16x32_f16 v[104:107], v[200:203], v[176:179], v[104:107]\n" \
"v_cvt_pk_bf16_f32 v82, v84, v85\n" \
"v_cvt_pk_bf16_f32 v94, v88, v89\n" \
"v_mfma_f32_16x16x32_f16 v[116:119], v[204:207], v[176:179], v[116:119]\n" \
"v_cvt_pk_bf16_f32 v83, v86, v87\n" \
"v_cvt_pk_bf16_f32 v95, v90, v91\n" \
"v_mfma_f32_16x16x32_f16 v[96:99], v[208:211], v[160:163], v[96:99]\n" \
"s_not_b64 vcc, vcc\n" \
"v_cndmask_b32_dpp v72, v92, v80, vcc quad_perm:[1,0,3,2] row_mask:0xf bank_mask:0xf\n" \
"v_cndmask_b32_dpp v73, v93, v81, vcc quad_perm:[1,0,3,2] row_mask:0xf bank_mask:0xf\n" \
"v_mfma_f32_16x16x32_f16 v[108:111], v[212:215], v[160:163], v[108:111]\n" \
"s_waitcnt lgkmcnt(0)\n" \
"v_pk_add_f16 v160, v160, v164\n" \
"v_pk_add_f16 v161, v161, v165\n" \
"v_cndmask_b32_dpp v74, v94, v82, vcc quad_perm:[1,0,3,2] row_mask:0xf bank_mask:0xf\n" \
"v_mfma_f32_16x16x32_f16 v[100:103], v[216:219], v[164:167], v[100:103]\n" \
"v_pk_add_f16 v162, v162, v166\n" \
"v_pk_add_f16 v163, v163, v167\n" \
"v_cndmask_b32_dpp v75, v95, v83, vcc quad_perm:[1,0,3,2] row_mask:0xf bank_mask:0xf\n" \
"buffer_store_dwordx4 v[72:75], v245, s[40:43], s72 offen sc1\n" \
"v_mfma_f32_16x16x32_f16 v[112:115], v[220:223], v[164:167], v[112:115]\n" \
"s_not_b64 vcc, vcc\n" \
"s_add_i32 s85, s72, 0x10000\n" \
"v_mfma_f32_16x16x32_f16 v[104:107], v[224:227], v[160:163], v[104:107]\n" \
"v_cndmask_b32_dpp v84, v80, v92, vcc quad_perm:[1,0,3,2] row_mask:0xf bank_mask:0xf\n" \
"v_cndmask_b32_dpp v85, v81, v93, vcc quad_perm:[1,0,3,2] row_mask:0xf bank_mask:0xf\n" \
"v_mfma_f32_16x16x32_f16 v[116:119], v[228:231], v[160:163], v[116:119]\n" \
"v_cndmask_b32_dpp v86, v82, v94, vcc quad_perm:[1,0,3,2] row_mask:0xf bank_mask:0xf\n" \
"v_cndmask_b32_dpp v87, v83, v95, vcc quad_perm:[1,0,3,2] row_mask:0xf bank_mask:0xf\n" \
"buffer_store_dwordx4 v[84:87], v245, s[40:43], s85 offen sc1\n" \
"s_nop 7\n" \
"v_add_f32 v252, v100, v104\n" \
"v_add_f32 v253, v101, v105\n" \
"v_add_f32 v254, v102, v106\n" \
"v_add_f32 v255, v103, v107\n" \
"v_fmac_f32 v100, s56, v104\n" \
"v_fmac_f32 v101, s56, v105\n" \
"v_fmac_f32 v102, s56, v106\n" \
"v_fmac_f32 v103, s56, v107\n" \
"v_fmac_f32 v96, s57, v252\n" \
"v_fmac_f32 v97, s57, v253\n" \
"v_fmac_f32 v98, s57, v254\n" \
"v_fmac_f32 v99, s57, v255\n" \
"v_add_f32 v252, v112, v116\n" \
"v_add_f32 v253, v113, v117\n" \
"v_add_f32 v254, v114, v118\n" \
"v_add_f32 v255, v115, v119\n" \
"v_fmac_f32 v112, s56, v116\n" \
"v_fmac_f32 v113, s56, v117\n" \
"v_fmac_f32 v114, s56, v118\n" \
"v_fmac_f32 v115, s56, v119\n" \
"v_fmac_f32 v108, s57, v252\n" \
"v_fmac_f32 v109, s57, v253\n" \
"v_fmac_f32 v110, s57, v254\n" \
"v_fmac_f32 v111, s57, v255\n" \
"v_cvt_pk_bf16_f32 v104, v96, v97\n" \
"v_cvt_pk_bf16_f32 v116, v100, v101\n" \
"v_cvt_pk_bf16_f32 v105, v98, v99\n" \
"v_cvt_pk_bf16_f32 v117, v102, v103\n" \
"v_cvt_pk_bf16_f32 v106, v108, v109\n" \
"v_cvt_pk_bf16_f32 v118, v112, v113\n" \
"v_cvt_pk_bf16_f32 v107, v110, v111\n" \
"v_cvt_pk_bf16_f32 v119, v114, v115\n" \
"s_not_b64 vcc, vcc\n" \
"v_cndmask_b32_dpp v96, v116, v104, vcc quad_perm:[1,0,3,2] row_mask:0xf bank_mask:0xf\n" \
"v_cndmask_b32_dpp v97, v117, v105, vcc quad_perm:[1,0,3,2] row_mask:0xf bank_mask:0xf\n" \
"v_cndmask_b32_dpp v98, v118, v106, vcc quad_perm:[1,0,3,2] row_mask:0xf bank_mask:0xf\n" \
"v_cndmask_b32_dpp v99, v119, v107, vcc quad_perm:[1,0,3,2] row_mask:0xf bank_mask:0xf\n" \
"buffer_store_dwordx4 v[96:99], v245, s[40:43], s73 offen sc1\n" \
"s_not_b64 vcc, vcc\n" \
"s_add_i32 s85, s73, 0x10000\n" \
"v_cndmask_b32_dpp v108, v104, v116, vcc quad_perm:[1,0,3,2] row_mask:0xf bank_mask:0xf\n" \
"v_cndmask_b32_dpp v109, v105, v117, vcc quad_perm:[1,0,3,2] row_mask:0xf bank_mask:0xf\n" \
"v_cndmask_b32_dpp v110, v106, v118, vcc quad_perm:[1,0,3,2] row_mask:0xf bank_mask:0xf\n" \
"v_cndmask_b32_dpp v111, v107, v119, vcc quad_perm:[1,0,3,2] row_mask:0xf bank_mask:0xf\n" \
"buffer_store_dwordx4 v[108:111], v245, s[40:43], s85 offen sc1\n" \
"s_waitcnt vmcnt(0)\n" \
"s_branch .Lcf_e\n" \
".Ls12_%=:\n" \
"s_mov_b32 s87, 0x200000\n" \
"s_mov_b32 s86, 0x8004000\n" \
".Lt12_%=:\n" \
"s_sleep 3\n" \
"buffer_load_dword v253, v252, s[36:39], s86 offen sc1\n" \
"s_waitcnt vmcnt(0)\n" \
"s_sub_u32 s87, s87, 1\n" \
"s_cbranch_scc1 .Lr12_%=\n" \
"v_cmp_eq_u32 vcc, 0, v253\n" \
"s_cbranch_vccnz .Lt12_%=\n" \
"s_branch .Lr12_%=\n" \
".Ls16_%=:\n" \
"s_mov_b32 s87, 0x200000\n" \
"s_mov_b32 s86, 0x800c000\n" \
".Lt16_%=:\n" \
"s_sleep 3\n" \
"buffer_load_dword v253, v252, s[36:39], s86 offen sc1\n" \
"s_waitcnt vmcnt(0)\n" \
"s_sub_u32 s87, s87, 1\n" \
"s_cbranch_scc1 .Lr16_%=\n" \
"v_cmp_eq_u32 vcc, 0, v253\n" \
"s_cbranch_vccnz .Lt16_%=\n" \
"s_branch .Lr16_%=\n" \
".Ls110_%=:\n" \
"s_mov_b32 s87, 0x200000\n" \
"s_mov_b32 s86, 0x8014000\n" \
".Lt110_%=:\n" \
"s_sleep 3\n" \
"buffer_load_dword v253, v252, s[36:39], s86 offen sc1\n" \
"s_waitcnt vmcnt(0)\n" \
"s_sub_u32 s87, s87, 1\n" \
"s_cbranch_scc1 .Lr110_%=\n" \
"v_cmp_eq_u32 vcc, 0, v253\n" \
"s_cbranch_vccnz .Lt110_%=\n" \
"s_branch .Lr110_%=\n" \
""
#define CLOBBERS "v0", "v1", "v2", "v3", "v4", "v5", "v6", "v7", "v8", "v9", "v10", "v11", "v12", "v13", "v14", "v15", "v16", "v17", "v18", "v19", "v20", "v21", "v22", "v23", "v24", "v25", "v26", "v27", "v28", "v29", "v30", "v31", "v32", "v33", "v34", "v35", "v36", "v37", "v38", "v39", "v40", "v41", "v42", "v43", "v44", "v45", "v46", "v47", "v48", "v49", "v50", "v51", "v52", "v53", "v54", "v55", "v56", "v57", "v58", "v59", "v60", "v61", "v62", "v63", "v64", "v65", "v66", "v67", "v68", "v69", "v70", "v71", "v84", "v85", "v86", "v87", "v88", "v89", "v90", "v91", "v92", "v93", "v94", "v95", "v120", "v121", "v122", "v123", "v124", "v125", "v126", "v127", "v128", "v129", "v130", "v131", "v132", "v133", "v134", "v135", "v136", "v137", "v138", "v139", "v140", "v141", "v142", "v143", "v144", "v145", "v146", "v147", "v148", "v149", "v150", "v151", "v152", "v153", "v154", "v155", "v156", "v157", "v158", "v159", "v160", "v161", "v162", "v163", "v164", "v165", "v166", "v167", "v168", "v169", "v170", "v171", "v172", "v173", "v174", "v175", "v176", "v177", "v178", "v179", "v180", "v181", "v182", "v183", "v184", "v185", "v186", "v187", "v188", "v189", "v190", "v191", "v196", "v197", "v198", "v199", "v200", "v201", "v202", "v203", "v204", "v205", "v206", "v207", "v253", "v254", "v255", "s80", "s81", "s82", "s83", "s84", "s85", "s86", "s87", "s88", "s89", "s90", "s91", "s92", "s75"
template <int NM>
__device__ __forceinline__ void run1(Ctx& cx, const unsigned short* __restrict__ x, const unsigned short* __restrict__ w, RQ& rq) {
  unsigned xs;
  phase1<NM>(cx, x, w, rq, xs, rq.vb, [&]() __attribute__((always_inline)) {
  static_assert(NS - st_first(PG) == 4 && NH == 6 && NST == 3 && NQW == 11 && RAWL == 110592, "constants of the generated code");
  const unsigned bid = cx.vb, idx = bid >> 3, slot = (bid & 7) * 4 + (idx >> 3), lane = cx.lane4 >> 2, h = (idx >> 2) & 1, wv = cx.wv;
  const unsigned lds0 = (unsigned)(uintptr_t)(lds_void*)cx.smem;
  unsigned own = ((wv ^ idx) & 3) - 1u;
  asm volatile("s_ashr_i32 %0, %0, 31" : "+s"(own) :: "scc");
  unsigned* sa = rq.s, *sb = rq.s + 16;
  sa[0] = cx.ldg; sa[1] = cx.ldf; sa[2] = cx.sg; sa[3] = cx.sf;
#pragma unroll
  for (int j = 0; j < 4; ++j) {
    sa[4 + j] = ((bid + j) & 3) * 16384;
    sa[8 + j] = ((bid + j) & 3) * 256;
  }
  if (NM == 5) {
    sa[8] = lds0 + RAWL + 32768 + (wv & 1) * 8192 + (wv & 2) * 2048;
    sa[11] = (30 + (wv & 1)) * SBLK + (wv & 2) * 10240;
    sa[9] = (137u - 14u + (bid & 1) * (125u - 137u)) * ((wv + 1) >> 3);
    sa[10] = lds0 + CORD + (wv & 3) * 1024;
  }
  sa[12] = min(slot, 1u) * 0xbf800000u;
  sa[13] = min(slot, 1u) * 0xbf000000u;
  sa[14] = sa[15] = own;
#pragma unroll
  for (int e = 0; e < NST + NH; ++e) {
    const unsigned n = 10 - NH - NST + e, blk = (xs & 255u) + n % 5;
    sb[e] = (xs & ~255u) + (n / 5) * (8 * SBLK) + (blk + (blk & 192u)) * 128u;
  }
#pragma unroll
  for (int m = 0; m < 5; ++m) {
    const unsigned t = NM == 5 ? (m + 1) % 5 : m;
    sb[9 + m] = t * (16 * 65536) + ((h * 10 + (wv >> 2) * 5 + (wv & 2) + t) & 2) * 4096;
  }
  sb[14] = 0;
#pragma unroll
  for (int i = 0; i < 31; ++i) rq.s[i] = __builtin_amdgcn_readfirstlane(rq.s[i]);
  const unsigned prd = cx.prd + (RAWL - RAWB);
  const unsigned yo = (h * NBLK + (wv >> 2) * 80 + (lane & 14)) * 65536 + ((idx & 3) * 4 + (wv & 1)) * 4096 + (lane >> 4) * 16 + (2 * slot + (lane & 1)) * 64;
  unsigned* v = rq.v;
  v[0] = cx.voff; v[1] = cx.voff5; v[2] = cx.poff; v[3] = cx.xo; v[4] = prd; v[5] = prd ^ 32u; v[6] = prd ^ 64u; v[7] = prd ^ 96u;
  v[8] = cx.xbase; v[9] = cx.wbase; v[10] = cx.wbase ^ 32u; v[11] = cx.fvo; v[12] = lane * 16; v[13] = yo; v[14] = cx.pwr + (RAWL - RAWB);
  if (NM == 6) {
    v[15] = cx.crd;
    v[16] = CBUF + min(lane & 15, 10u) * 272 + wv * 64 + (cx.lane4 >> 6) * 16;
    v[17] = CORD + lane * 16;
    v[18] = (bid & 7) * 1024 + (idx & 3) * 256 + lane * 4;
  } else {
    const unsigned co = (((bid >> 3) & 3) * 2 + ((bid >> 2) & 1)) * 128u + (cx.lane4 & 127u);
    const unsigned long long p = (unsigned long long)((const char*)(cx.lane4 < 128 ? cx.gamma : cx.beta) + co);
    v[15] = 0u;
    v[16] = (unsigned)p;
    v[17] = (unsigned)(p >> 32);
    v[18] = cx.fvo;
  }
  rq.s[31] = __builtin_amdgcn_readfirstlane(wv & 2 ? cx.pft + (wv & 1) * 8192 : cx.pfo - 8192 - (wv & 1) * 8192);
#pragma unroll
  for (int i = 0; i < 32; ++i) asm volatile("" : "+s"(rq.s[i]));
#pragma unroll
  for (int i = 0; i < 19; ++i) asm volatile("" : "+v"(rq.v[i]));
  SB0;
  });
}

template <int CTRL>
__device__ __forceinline__ float bfly(float a, float b, bool hi) {
  const float give = hi ? a : b, keep = hi ? b : a;
  return keep + __builtin_bit_cast(float, __builtin_amdgcn_update_dpp(0, __builtin_bit_cast(int, give), CTRL, 0xf, 0xf, false));
}
template <int CTRL>
__device__ __forceinline__ float dpp_add(float v) {
  asm("" : "+v"(v));
  return v + __builtin_bit_cast(float, __builtin_amdgcn_update_dpp(0, __builtin_bit_cast(int, v), CTRL, 0xf, 0xf, true));
}
__device__ __forceinline__ void row16_sum(float (&v)[8]) {
#pragma unroll
  for (int r = 0; r < 8; ++r) v[r] = dpp_add<0x128>(v[r]);
  SB0;
#pragma unroll
  for (int r = 0; r < 8; ++r) v[r] = dpp_add<0x124>(v[r]);
  SB0;
#pragma unroll
  for (int r = 0; r < 8; ++r) v[r] = dpp_add<0x122>(v[r]);
  SB0;
#pragma unroll
  for (int r = 0; r < 8; ++r) v[r] = dpp_add<0x121>(v[r]);
#pragma unroll
  for (int r = 0; r < 8; ++r) asm("" : "+v"(v[r]));
  SB0;
}

extern "C" __global__ __launch_bounds__(512, 2) void conv_fft(char* ws, const unsigned short* __restrict__ x,
                                                   const unsigned short* __restrict__ w,
                                                   const unsigned short* __restrict__ gamma,
                                                   const unsigned short* __restrict__ beta,
                                                   unsigned short* __restrict__ out) {
  __shared__ __attribute__((aligned(16))) char smem[160 * 1024];
  const unsigned lds0 = (unsigned)(uintptr_t)(lds_void*)smem;
  const int wv = __builtin_amdgcn_readfirstlane(threadIdx.x >> 6);
  Ctx cx;
  {
    unsigned len;
    asm volatile("s_getpc_b64 %0\n.Lcf_a:\n\ts_mov_b32 %1, .Lcf_b-.Lcf_a\n\ts_mov_b32 %2, .Lcf_t-.Lcf_a" : "=s"(cx.pc), "=s"(len), "=s"(cx.pft));
    cx.wv = wv;
    cx.pfo = len;
    unsigned xcc;
    asm volatile("s_getreg_b32 %0, hwreg(HW_REG_XCC_ID, 0, 3)" : "=s"(xcc));
    unsigned id = blockIdx.x;
    asm volatile("" : "+s"(id));
    cx.id = id;
    cx.vb = (int)((id & 248u) | (id & 256u ? (id >> 10) & 7u : xcc));
    const char* pb = ws + WS_PUB;
    u32x4 sx, sw;
    if ((id | wv) == 0) {
      const unsigned l = threadIdx.x;
      const u32x4 z = {0u, 0u, 0u, 0u};
      {
#pragma unroll
        for (int k = 0; k < 2; ++k)
          asm volatile("global_store_dwordx4 %0, %1, %2 sc1" :: "v"(32768 + (l & 7) * 16 + (l & 8) * 512 + (l & 16) * 1024 + (l & 32) * 32 + k * 8192), "v"(z), "s"(pb) : "memory");
      }
      {
        const unsigned so = l * (31u * 1024u + 16u) + (4u << 20);
        asm volatile("global_store_dwordx4 %2, %3, %4 sc1\n\tglobal_load_dwordx4 %0, %5, %6\n\tglobal_load_dwordx4 %1, %7, %8"
                     : "=&v"(sx), "=&v"(sw) : "v"(l * 16 + 1024), "v"(z), "s"(pb), "v"(so + (l & 3u) * (8u << 20)), "s"(x), "v"(so + (l & 1u) * (6u << 20)), "s"(w) : "memory");
      }
    }
    if (id) code_touch(cx);
    SB0;
    issue_x(0, wv, x_addr(cx.vb, 0, wv), x_rsrc(x, cx.vb), lds0);
    SB0;
    if ((id | wv) == 0) {
      wait_vm<9>();
      asm volatile("" : "+v"(sx), "+v"(sw));
      unsigned m = expo(amax(amax(amax(amax(0, sx[0]), sx[1]), sx[2]), sx[3])) | expo(amax(amax(amax(amax(0, sw[0]), sw[1]), sw[2]), sw[3])) << 16;
      m = amax(m, (unsigned)__builtin_amdgcn_update_dpp(0, (int)m, 0x128, 0xf, 0xf, false));
      m = amax(m, (unsigned)__builtin_amdgcn_update_dpp(0, (int)m, 0x124, 0xf, 0xf, false));
      m = amax(m, (unsigned)__builtin_amdgcn_update_dpp(0, (int)m, 0x122, 0xf, 0xf, false));
      m = amax(m, (unsigned)__builtin_amdgcn_update_dpp(0, (int)m, 0x121, 0xf, 0xf, false));
      unsigned ex = 28, ew = 28;
#pragma unroll
      for (int l = 0; l < 64; l += 16) {
        const unsigned v = (unsigned)__builtin_amdgcn_readlane((int)m, l);
        ex = max(ex + 2, v & 255u) - 2;
        ew = max(ew + 3, v >> 16) - 3;
      }
      const unsigned long long tg = __builtin_amdgcn_s_memrealtime();
      const unsigned lo = (unsigned)tg << 4 | xcc << 1, hi = ((unsigned)(tg >> 28) & 0xffffu) | min(ex, 220u) << 16 | min(ew, 220u) << 24;
      asm volatile("global_store_dwordx4 %0, %1, %2 sc1\n\ts_nop 1" :: "v"(threadIdx.x * 16), "v"(u32x4{lo, hi, ~lo, ~hi}), "s"(pb) : "memory");
      cx.tag[0] = lo;
      cx.tag[1] = hi;
    }
  }
  cx.smem = smem;
  cx.ws = ws;
  cx.gamma = gamma;
  cx.beta = beta;
  bf16x8 gb[16];
  {
    RQ rq;
    if (wv < 4) run1<6>(cx, x, w, rq); else run1<5>(cx, x, w, rq);
    const unsigned long long tpc = cx.pc + rq.s[31];
    __builtin_amdgcn_s_barrier();
    asm volatile("s_cmp_gt_u32 %[wv], 3\n\ts_cbranch_scc1 .Lcf_1\n" LOOP0 ".Lcf_1:\n" LOOP1 ".Lcf_e:"
      :
        "+{v232}"(rq.v[0]), "+{v233}"(rq.v[1]), "+{v234}"(rq.v[2]),
        "+{v235}"(rq.v[3]), "+{v236}"(rq.v[4]), "+{v237}"(rq.v[5]), "+{v238}"(rq.v[6]), "+{v239}"(rq.v[7]), "+{v[72:75]}"(rq.h[0]),
        "+{v[76:79]}"(rq.h[1]), "+{v[80:83]}"(rq.h[2]), "+{v[96:97]}"(rq.raw[0]), "+{v[98:99]}"(rq.raw[1]), "+{v[100:101]}"(rq.raw[2]),
        "+{v[102:103]}"(rq.raw[3]), "+{v[104:105]}"(rq.raw[4]), "+{v[106:107]}"(rq.raw[5]), "+{v[108:109]}"(rq.raw[6]), "+{v[110:111]}"(rq.raw[7]),
        "+{v[112:113]}"(rq.raw[8]), "+{v[114:115]}"(rq.raw[9]), "+{v[116:117]}"(rq.raw[10]), "+{v241}"(rq.v[9]), "+{v243}"(rq.v[11]), "+{v244}"(rq.v[12]), "+{v252}"(rq.v[18]), "+{v[192:195]}"(rq.vb),
        "+{v[208:211]}"(rq.h[3]), "+{v[212:215]}"(rq.h[4]), "+{v[216:219]}"(rq.h[5]), "+{v[220:223]}"(rq.h[6]), "+{v[224:227]}"(rq.h[7]), "+{v[228:231]}"(rq.h[8]),
        "+{v246}"(rq.v[14]), "+{v247}"(rq.v[15])
      :
        "{v240}"(rq.v[8]), "{v242}"(rq.v[10]), "{v245}"(rq.v[13]),
        "{v248}"(rq.v[16]), "{v249}"(rq.v[17]),
            "{s44}"(rq.s[0]), "{s45}"(rq.s[1]),
        "{s46}"(rq.s[2]), "{s47}"(rq.s[3]), "{s48}"(rq.s[4]), "{s49}"(rq.s[5]), "{s50}"(rq.s[6]), "{s51}"(rq.s[7]), "{s52}"(rq.s[8]), "{s53}"(rq.s[9]),
        "{s54}"(rq.s[10]), "{s55}"(rq.s[11]), "{s56}"(rq.s[12]), "{s57}"(rq.s[13]), "{s58}"(rq.s[14]), "{s59}"(rq.s[15]), "{s60}"(rq.s[16]),
        "{s61}"(rq.s[17]), "{s62}"(rq.s[18]), "{s63}"(rq.s[19]), "{s64}"(rq.s[20]), "{s65}"(rq.s[21]), "{s66}"(rq.s[22]), "{s67}"(rq.s[23]),
        "{s68}"(rq.s[24]), "{s69}"(rq.s[25]), "{s70}"(rq.s[26]), "{s71}"(rq.s[27]), "{s72}"(rq.s[28]), "{s73}"(rq.s[29]),
        "{s[36:39]}"(cx.rs), "{s[40:43]}"(make_rsrc(cx.ws + WS_Y, ROWS * 65536)), "{s[76:77]}"(&TAB.G[0][0][0][0][0]), "{s[78:79]}"(tpc), [wv] "s"(wv)
      : "memory", "vcc", "scc", "v118", "v119", CLOBBERS);
  }
  ws = cx.ws;
  const int bid = cx.vb, idx = bid >> 3;
  const int slot = (bid & 7) * 4 + (idx >> 3), b = (idx >> 2) & 1, nt = idx & 3;
  const int lane = (int)__builtin_amdgcn_mbcnt_hi(~0u, __builtin_amdgcn_mbcnt_lo(~0u, 0u));
  __builtin_amdgcn_s_barrier();
  gu32* fl = (gu32*)(ws + WS_GRP + nt * 256);
  if (wv == 1 && lane < 8) __hip_atomic_store(fl + (lane + (lane & 4)) * 256 + b * 32 + slot, 1u, RLX_AGENT);
  const int c64i = nt * 2 + ((bid >> 2) & 1), rg = (bid & 1) * 8 + (slot & 1) * 4 + (bid & 2) + (idx >> 4);
  const int hw = wv >> 2, wp = hw ? 7 - wv : wv, cg = c64i * 2 + hw;
  const int bs = 10 * rg;
  const int ntk = wp < 2 ? 3 : 2;
  const int g = lane >> 4, qq = (lane >> 2) & 3, pp = lane & 3, fr = lane & 15;
  const int ch0 = cg * 32 + 8 * g;
  const int fi = b * 32 + (lane & 31);
  const unsigned cnt = lds0 + 8 * 12288 + 3072 + hw * 8;
  if (wv == 2) asm volatile("ds_write_b32 %0, %1\n\ts_waitcnt lgkmcnt(0)" :: "v"(cnt + (lane & 3) * 4), "v"(0u) : "memory");
#pragma unroll
  for (int i = 0; i < 16; ++i) gb[i] = *reinterpret_cast<const bf16x8*>(smem + i * 1024 + lane * 16);
  const int l0 = HOP * (bs + wp) + fr, le = (fr >> 1) < ntk ? HOP * (bs + wp + 4 * (fr >> 1)) + 48 + (fr & 1) : 1 << 20;
  const int lend = min(HOP * (bs + 10), L_);
  f32x4 nk, rk;
  float rn = 1.0f / (float)(lend - HOP * bs);
#pragma unroll
  for (int k = 0; k < 4; ++k) {
    nk[k] = (float)(2 * HOP + min(max(lend - HOP * (bs + k + 8), 0), HOP));
    rk[k] = 1.0f / nk[k];
  }
  asm volatile("" : "+v"(nk), "+v"(rk), "+v"(rn));
  f32x4 cin[2];
#pragma unroll
    for (int s = 0; s < 2; ++s) {
      cin[s] = *reinterpret_cast<const f32x4*>(smem + CBUF + min(wp + 4 * (fr >> 1), 10) * 272 + hw * 128 + g * 32 + s * 16);
#pragma unroll
      for (int r = 0; r < 4; ++r) cin[s][r] = fr & 1 ? __builtin_amdgcn_ldexpf(cin[s][r], cx.es) : 0.f;
    }
    asm volatile("" : "+v"(cin[0]), "+v"(cin[1]));
#pragma unroll
  for (int i = 0; i < 16; i += 4) asm volatile("" : "+v"(gb[i]), "+v"(gb[i + 1]), "+v"(gb[i + 2]), "+v"(gb[i + 3]));
  SB0;
  if (wv == 0) spin_flags((const char*)(fl + ((bid & 7) + (bid & 4)) * 256 + (fi ^ (fi == b * 32 + slot))));
  __builtin_amdgcn_s_barrier();
  char* my = smem + wv * 12288;
  {
    const u32x4 rs = make_rsrc(ws + WS_Y, ROWS * 65536);
    const unsigned vo = (unsigned)((lane >> 2) * 64 + (((lane & 3) ^ ((lane >> 3) & 2)) << 4));
#pragma unroll
    for (int i = 0; i < 3; ++i) {
      const unsigned yr = (unsigned)(b * NBLK + bs + wp + 4 * i);
      const unsigned so = i < ntk ? (yr * 16 + (cg ^ ((yr >> 4) & 2))) * 4096 : 0xf0000000u;
#pragma unroll
      for (int j = 0; j < 4; ++j) DMA16("sc1", rs, lds0 + wv * 12288 + i * 4096 + j * 1024, vo, so + j * 1024);
    }
  }
  f32x4 y[24], npv[2];
  float s1[8], s2[8];
  {
    bf16x8 a[12];
    const char* src = my + (4 * g + qq) * 64 + ((g & 1) << 5) + pp * 8;
    auto rd = [&](int q) __attribute__((always_inline)) {
      const char* p = reinterpret_cast<const char*>(smem + (((unsigned)(src - smem)) ^ (unsigned)((q & 2) << 4)) + (q >> 2) * 4096 + (q & 1) * 2048);
      a[q] = cat(tr_read(p), tr_read(p + 1024));
    };
    auto sums = [&](int cp, int q) __attribute__((always_inline)) {
      const int e = (cp & 4) + q;
      if ((cp & 3) == 3) {
        if (cp >= 8) y[cp & 7][q] = (fr >> 1) == (cp >> 3) ? y[cp][q] : y[cp & 7][q];
        return;
      }
      float d = y[cp][q];
      if (cp >= 16 && l0 + 8 * HOP + 16 * (cp & 3) >= lend) d = 0.f;
      if (cp == 0 || cp == 4) { s1[e] = d; s2[e] = d * d; }
      else { s1[e] += d; s2[e] = __builtin_fmaf(d, d, s2[e]); }
      asm volatile("" : "+v"(s1[e]), "+v"(s2[e]));
    };
    wait_vm<8>();
    SB0;
    u32x2 pv[2];
#pragma unroll
    for (int s = 0; s < 2; ++s) pv[s] = *reinterpret_cast<const u32x2*>(my + s * 32 + g * 8);
    SB0;
#pragma unroll
    for (int q = 0; q < 4; ++q) { rd(q); SB0; }
#pragma unroll
    for (int s = 0; s < 2; ++s) {
      npv[s] = f32x4{bf_lo(pv[s][0]), bf_hi(pv[s][0]), bf_lo(pv[s][1]), bf_hi(pv[s][1])} * (-1.0f / 64.0f);
      cin[s] += npv[s];
    }
    SB0;
    auto chain = [&](auto cc) __attribute__((always_inline)) {
      constexpr int c = cc.v, is = c >> 2, tt = c & 3;
      if (c == 7) wait_vm<4>();
      if (c == 15) wait_vm<0>();
#pragma unroll
      for (int q = 0; q < 4; ++q) {
        if (c < 24) y[c % 24] = MFMA(a[(2 * is + (q >> 1)) % 12], gb[4 * tt + (q ^ 1)], q ? y[c % 24] : tt == 3 ? cin[is & 1] : npv[is & 1]);
        SB0;
        if (c == 7 || c == 15) rd((c == 7 ? 4 : 8) + q);
        if (c >= 2) sums(c - 2, q);
        SB0;
      }
    };
    sfor<0, 16>(chain);
    if (ntk == 3) {
      sfor<16, 26>(chain);
    } else {
#pragma unroll
      for (int q = 0; q < 8; ++q) sums(14 + (q >> 2), q & 3);
    }
#pragma unroll
    for (int e = 0; e < 8; ++e) {
      const float d = le < lend ? y[3 + (e & 4)][e & 3] : 0.f;
      s1[e] += d;
      s2[e] = __builtin_fmaf(d, d, s2[e]);
    }
  }
  float* red = reinterpret_cast<float*>(smem + 8 * 12288);
  {
    float v[8];
#pragma unroll
    for (int j = 0; j < 8; ++j) v[j] = bfly<0x140>(s1[j], s2[j], fr & 8);
#pragma unroll
    for (int j = 0; j < 4; ++j) v[j] = bfly<0x141>(v[j], v[j + 4], fr & 4);
#pragma unroll
    for (int j = 0; j < 2; ++j) v[j] = bfly<0x4E>(v[j], v[j + 2], fr & 2);
    red[((fr >> 3) * 8 + 2 * wp + hw) * 32 + 8 * g + (fr & 7)] = bfly<0xB1>(v[0], v[1], fr & 1);
    if (fr < 2) *reinterpret_cast<f32x4*>(red + (16 + 2 * wp + hw) * 32 + 8 * g + 4 * fr) = fr ? npv[1] : npv[0];
  }
  unsigned arr = 1u;
  if (lane == 0) asm volatile("ds_add_rtn_u32 %0, %1, %0\n\ts_waitcnt lgkmcnt(0)" : "+v"(arr) : "v"(cnt) : "memory");
  unsigned long long* stats = reinterpret_cast<unsigned long long*>(ws + WS_STATS);
  if (__builtin_amdgcn_readfirstlane(arr) == 3u && lane < 32) {
    const int tid = hw * 32 + lane;
    float mk[4], sm = 0.f, m2 = 0.f;
#pragma unroll
    for (int k = 0; k < 4; ++k) {
      const float* e = red + 64 * k + tid;
      const float dm = e[0] * rk[k];
      mk[k] = dm - e[512];
      m2 += fmaxf(e[256] - e[0] * dm, 0.f);
      sm += nk[k] * mk[k];
    }
    const float mean = sm * rn;
#pragma unroll
    for (int k = 0; k < 4; ++k) m2 += nk[k] * (mk[k] - mean) * (mk[k] - mean);
    unsigned long long pk = (unsigned long long)__float_as_uint(mean) | ((unsigned long long)__float_as_uint(m2) << 32);
    if (pk == ~0ull) pk = ~1ull;
    __hip_atomic_store((gu64*)(stats + (b * 16 + rg + (rg & 12) + (c64i & 1) * 4 + b * 16) * 256 + nt * 64 + tid), pk, RLX_AGENT);
  }
  const u32x4 gam = *reinterpret_cast<const u32x4*>(smem + GAM + hw * 64 + g * 16);
  const u32x4 bet = *reinterpret_cast<const u32x4*>(smem + GAM + 128 + hw * 64 + g * 16);
  f32x4 ga[2], be[2];
#pragma unroll
  for (int r = 0; r < 8; ++r) {
    ga[r >> 2][r & 3] = (r & 1) ? bf_hi(gam[r >> 1]) : bf_lo(gam[r >> 1]);
    be[r >> 2][r & 3] = (r & 1) ? bf_hi(bet[r >> 1]) : bf_lo(bet[r >> 1]);
  }
  const float eps = fmaxf(__builtin_amdgcn_ldexpf(1e-5f, 2 * cx.es), 1e-30f);
  float na = fr < 15 ? 500.f : 485.f;
  asm volatile("" : "+v"(ga[0]), "+v"(ga[1]), "+v"(be[0]), "+v"(be[1]), "+v"(na));
  unsigned long long* gl = reinterpret_cast<unsigned long long*>(smem + 8 * 12288 + 4096);
  {
    unsigned long long pk[2];
    const int row = 4 * wp + (lane >> 5), col = hw * 32 + (lane & 31);
    for (unsigned spins = 0;; ++spins) {
#pragma unroll
      for (int r = 0; r < 2; ++r)
        pk[r] = __hip_atomic_load((gu64*)(stats + (b * 32 + row + 2 * r + 4 * wp + (c64i & 1) * 4) * 256 + nt * 64 + col), RLX_AGENT);
      if (__all(pk[0] != ~0ull && pk[1] != ~0ull) || spins > (1u << 20)) break;
      __builtin_amdgcn_s_sleep(1);
    }
    gl[row * 66 + col] = pk[0];
    gl[(row + 2) * 66 + col] = pk[1];
    if (lane == 0) asm volatile("ds_add_u32 %0, %1" :: "v"(cnt + 4), "v"(1u) : "memory");
    unsigned c;
    do asm volatile("ds_read_b32 %0, %1\n\ts_waitcnt lgkmcnt(0)" : "=v"(c) : "v"(cnt + 4) : "memory");
    while (__builtin_amdgcn_readfirstlane(c) < 4u);
  }
  {
    u32x4 pk[4];
#pragma unroll
    for (int r = 0; r < 4; ++r)
      pk[r] = *reinterpret_cast<const u32x4*>(gl + fr * 66 + hw * 32 + 8 * g + 2 * r);
    float mu[8], vs[8];
#pragma unroll
    for (int r = 0; r < 8; ++r) mu[r] = na * __uint_as_float(pk[r >> 1][(r & 1) * 2]);
    row16_sum(mu);
#pragma unroll
    for (int r = 0; r < 8; ++r) {
      mu[r] *= 1.0f / (float)L_;
      const float da = __uint_as_float(pk[r >> 1][(r & 1) * 2]) - mu[r];
      vs[r] = __uint_as_float(pk[r >> 1][(r & 1) * 2 + 1]) + na * da * da;
    }
    row16_sum(vs);
#pragma unroll
    for (int r = 0; r < 8; ++r) {
      const float mean = mu[r], var = vs[r] * (1.0f / (float)L_);
      const float rstd = __builtin_amdgcn_rsqf(var + eps);
      const float gg = ga[r >> 2][r & 3] * rstd;
      ga[r >> 2][r & 3] = gg;
      be[r >> 2][r & 3] -= (mean + npv[r >> 2][r & 3]) * gg;
    }
  }
  {
    const float k1 = -2.0f * 0.7978845608028654f * 1.4426950408889634f, k2 = k1 * 0.044715f;
    f32x4 kk1 = {k1, k1, k1, k1}, kk2 = {k2, k2, k2, k2};
    asm volatile("" : "+v"(kk1), "+v"(kk2));
    const u32x4 ro = make_rsrc(out + (size_t)b * L_ * D_, lend * (D_ * 2));
    const unsigned oo = (unsigned)((l0 * D_ + ch0) * 2);
#pragma unroll
    for (int i = 0; i < 3; ++i)
      if (i < 2 || ntk == 3)
#pragma unroll
      for (int tt = 0; tt < 4; ++tt) {
        if (tt == 3 && i) break;
        f32x4 o[2];
#pragma unroll
        for (int s = 0; s < 2; ++s) {
          const f32x4 z = y[8 * i + 4 * s + tt] * ga[s] + be[s];
          const f32x4 a = z * (z * z * kk2 + kk1);
          f32x4 e;
#pragma unroll
          for (int r = 0; r < 4; ++r) e[r] = __builtin_amdgcn_exp2f(a[r]);
          e += 1.0f;
#pragma unroll
          for (int r = 0; r < 4; ++r) e[r] = __builtin_amdgcn_rcpf(e[r]);
          o[s] = z * e;
        }
        st16(pack8(o[0], o[1]), ro, tt < 3 ? oo : (unsigned)((le * D_ + ch0) * 2), tt < 3 ? (4 * HOP * i + 16 * tt) * (D_ * 2) : 0);
      }
  }
  asm volatile(".Lcf_b:\n\ts_nop 0" ::: "memory");
}

extern "C" void kernel_launch(void* const* d_in, const int* in_sizes, int n_in,
                              void* d_out, int out_size, void* d_ws, size_t ws_size,
                              hipStream_t stream) {
  const unsigned short* x = (const unsigned short*)d_in[0];
  const unsigned short* w = (const unsigned short*)d_in[1];
  const unsigned short* gamma = (const unsigned short*)d_in[2];
  const unsigned short* beta = (const unsigned short*)d_in[3];
  char* ws = (char*)(((uintptr_t)d_ws + (2u << 20) - 1) & ~(uintptr_t)((2u << 20) - 1));
  conv_fft<<<256, 512, 0, stream>>>(ws, x, w, gamma, beta, (unsigned short*)d_out);
}
```

```cpp
#include <hip/hip_runtime.h>
#include <stdint.h>

typedef __attribute__((ext_vector_type(8))) short bf16x8;
typedef __attribute__((ext_vector_type(4))) short s16x4;
typedef __attribute__((ext_vector_type(4))) float f32x4;
typedef __attribute__((ext_vector_type(2))) float f32x2;
typedef __attribute__((ext_vector_type(2))) __bf16 bf16x2v;
typedef __attribute__((ext_vector_type(4))) unsigned int u32x4;
typedef __attribute__((ext_vector_type(2))) unsigned int u32x2;
constexpr int NH = 6, NST = 3;
typedef __attribute__((ext_vector_type(8))) unsigned int u32x8;
typedef __attribute__((ext_vector_type(16))) unsigned int u32x16;
typedef __attribute__((ext_vector_type(8))) _Float16 f16x8;
typedef __attribute__((ext_vector_type(2))) _Float16 f16x2;
typedef __attribute__((ext_vector_type(2))) unsigned short u16x2;
typedef __attribute__((address_space(1))) unsigned long long gu64;
typedef __attribute__((address_space(1))) unsigned gu32;
typedef __attribute__((address_space(3))) void lds_void;
typedef __attribute__((address_space(3))) s16x4 lds_s16x4;

constexpr unsigned long long bytes(unsigned a, unsigned b = 0, unsigned c = 0, unsigned d = 0, unsigned e = 0, unsigned f = 0, unsigned g = 0, unsigned h = 0) {
  return a | b << 8 | c << 16 | d << 24 | (unsigned long long)(e | f << 8 | g << 16 | h << 24) << 32;
}
constexpr int T_ = 16000, D_ = 512, L_ = 7985;
constexpr int HOP = 50, NBLK = 160, ROWS = 2 * NBLK;

constexpr unsigned WS_S = 0;
constexpr unsigned SBLK = 4u << 20;
constexpr unsigned sbase(unsigned sl) { return sl * 131072 + (sl >> 4) * 8192; }
constexpr unsigned FLG = 32u * SBLK;
constexpr unsigned WS_GRP = WS_S + FLG + 16 * 8192;
constexpr unsigned WS_D = WS_S + FLG + 20 * 8192;
constexpr unsigned WS_L = WS_D + ROWS * 1024;
constexpr unsigned WS_Y = 40u << 20;
constexpr unsigned WS_STATS = 129u << 20;
constexpr unsigned WS_FA = WS_STATS + (1u << 17);
constexpr unsigned WS_PUB = WS_STATS + (1u << 19);
static_assert(WS_L + D_ * D_ * 2 <= WS_STATS && FLG % (16 * 8192) == 0, "workspace layout");

#define RLX_AGENT __ATOMIC_RELAXED, __HIP_MEMORY_SCOPE_AGENT

constexpr double COS64[17] = {1.0, 0.99518472667219688624, 0.98078528040323044913, 0.95694033573220886494,
    0.92387953251128675613, 0.88192126434835502971, 0.83146961230254523708, 0.77301045336273696081,
    0.70710678118654752440, 0.63439328416364549822, 0.55557023301960222474, 0.47139673682599764856,
    0.38268343236508977173, 0.29028467725446236764, 0.19509032201612826785, 0.09801714032956060199, 0.0};
constexpr double c64(int k) { k &= 63; if (k > 32) k = 64 - k; return k <= 16 ? COS64[k] : -COS64[32 - k]; }
constexpr double s64(int k) { return c64(k + 48); }
constexpr unsigned short bf_rne(double v) {
  const unsigned u = __builtin_bit_cast(unsigned, (float)v);
  return (unsigned short)((u + 0x7fffu + ((u >> 16) & 1u)) >> 16);
}
constexpr double R3 = 0.86602540378443864676;
constexpr double dft(int c, int t, double sgn) {
  return (c >> 1) == 0 ? ((c & t) & 1 ? -1.0 : 1.0)
                       : ((c & 1) ? -0.5 * c64((c >> 1) * t) - R3 * sgn * s64((c >> 1) * t) : c64((c >> 1) * t));
}
constexpr double idft(int c, int t) {
  return (c >> 1) == 0 ? ((c & t) & 1 ? -1.0 : 1.0) / 64.0
                       : ((c & 1) ? -s64((c >> 1) * t) / R3 : c64((c >> 1) * t) * (4.0 / 3.0)) / 32.0;
}
constexpr int kidx(int g, int j) { return j < 4 ? 4 * g + j : 12 + 4 * g + j; }
struct Tables {
  unsigned short G[4][2][64][8];
  unsigned short F[4][2][64][8];
  unsigned short V[4][64][4];
};
constexpr Tables make_tables() {
  Tables tb{};
  for (int l = 0; l < 64; ++l)
    for (int j = 0; j < 8; ++j)
      for (int t = 0; t < 4; ++t) {
        const int g = l >> 4, n = l & 15;
        for (int ks = 0; ks < 2; ++ks) {
          tb.F[t][ks][l][j] = bf_rne(dft(16 * t + n, 32 * ks + kidx(g, j), -1.0));
          tb.G[t][ks][l][j] = bf_rne(idft(32 * ks + kidx(g, j), 16 * t + (t < 3 ? n : n & 1)));
        }
        tb.V[t][l][j & 3] = bf_rne(dft(16 * t + n, 4 * g + (j & 3), 1.0));
      }
  return tb;
}
__device__ const __attribute__((aligned(16))) Tables TAB = make_tables();

__device__ __forceinline__ unsigned pk_bf16(float lo, float hi) {
  return __builtin_bit_cast(unsigned, __builtin_convertvector(f32x2{lo, hi}, bf16x2v));
}
__device__ __forceinline__ float bf_lo(unsigned v) { return __uint_as_float(v << 16); }
__device__ __forceinline__ float bf_hi(unsigned v) { return __uint_as_float(v & 0xffff0000u); }
__device__ __forceinline__ u32x4 pack8(f32x4 a, f32x4 b) {
  return u32x4{pk_bf16(a[0], a[1]), pk_bf16(a[2], a[3]), pk_bf16(b[0], b[1]), pk_bf16(b[2], b[3])};
}
__device__ __forceinline__ unsigned pk_f16(float lo, float hi) {
  return (__builtin_bit_cast(unsigned, __builtin_convertvector(f32x2{lo, hi}, f16x2)) + 0x40004u) & 0xfff8fff8u;
}
__device__ __forceinline__ u32x4 pack8h(f32x4 a, f32x4 b) {
  return u32x4{pk_f16(a[0], a[1]), pk_f16(a[2], a[3]), pk_f16(b[0], b[1]), pk_f16(b[2], b[3])};
}
__device__ __forceinline__ unsigned add_h(const bf16x8& a, const bf16x8& b, int d) {
  const unsigned x = __builtin_bit_cast(u32x4, a)[d], y = __builtin_bit_cast(u32x4, b)[d];
  unsigned r = __builtin_bit_cast(unsigned, __builtin_bit_cast(f16x2, x) + __builtin_bit_cast(f16x2, y));
  return r;
}
__device__ __forceinline__ unsigned amax(unsigned m, unsigned v) {
  return __builtin_bit_cast(unsigned, __builtin_elementwise_max(__builtin_bit_cast(u16x2, m), __builtin_bit_cast(u16x2, v & 0x7fff7fffu)));
}
struct Rng { f16x2 hi = {0, 0}, lo = {0, 0}; };
__device__ __forceinline__ void widen(Rng& r, unsigned a, unsigned b) {
  const f16x2 x = __builtin_bit_cast(f16x2, a), y = __builtin_bit_cast(f16x2, b);
  r.hi = __builtin_elementwise_maximum(__builtin_elementwise_maximum(r.hi, x), y);
  r.lo = __builtin_elementwise_minimum(__builtin_elementwise_minimum(r.lo, x), y);
}
__device__ __forceinline__ unsigned amax(const Rng& r) { return amax(amax(0, __builtin_bit_cast(unsigned, r.hi)), __builtin_bit_cast(unsigned, r.lo)); }
__device__ __forceinline__ unsigned expo(unsigned m) { return max(m & 0xffffu, m >> 16) >> 7; }
__device__ __forceinline__ unsigned wave_max(unsigned v) {
  unsigned m = 0;
#pragma unroll 1
  for (int l = 0; l < 64; ++l) m = max(m, (unsigned)__builtin_amdgcn_readlane((int)v, l));
  return m;
}
__device__ __forceinline__ unsigned scale2(unsigned u, int e) {
  return __builtin_amdgcn_perm(__float_as_uint(__builtin_amdgcn_ldexpf(bf_hi(u), e)), __float_as_uint(__builtin_amdgcn_ldexpf(bf_lo(u), e)), 0x07060302u);
}
__device__ __forceinline__ void restart(unsigned id) {
  asm volatile("s_waitcnt vmcnt(0)\n\ts_barrier\n\ts_branch conv_fft" :: "{s[0:1]}"(__builtin_amdgcn_kernarg_segment_ptr()), "{s2}"(id), "{v0}"(threadIdx.x) : "memory");
}
__device__ __forceinline__ s16x4 tr_read(const char* p) {
  return __builtin_amdgcn_ds_read_tr16_b64_v4i16((lds_s16x4*)p);
}
__device__ __forceinline__ bf16x8 cat(s16x4 a, s16x4 b) {
  const u32x2 x = __builtin_bit_cast(u32x2, a), y = __builtin_bit_cast(u32x2, b);
  return __builtin_bit_cast(bf16x8, u32x4{x[0], x[1], y[0], y[1]});
}
#define MFMA(a, b, c) __builtin_amdgcn_mfma_f32_16x16x32_bf16(a, b, c, 0, 0, 0)
#define MFMS(a, b) __builtin_amdgcn_mfma_f32_16x16x16bf16_1k(a, __builtin_bit_cast(s16x4, b), ZERO4, 0, 0, 0)
#define SB0 __builtin_amdgcn_sched_barrier(0)
#define ZERO4 (f32x4{0.f, 0.f, 0.f, 0.f})
#define DMA16(POL, rs, lds, voff, soff) \
  asm volatile("s_mov_b32 m0, %0\n\ts_nop 0\n\tbuffer_load_dwordx4 %1, %2, %3 offen " POL " lds" \
               :: "s"(lds), "v"(voff), "s"(rs), "s"(soff) : "memory")
__device__ __forceinline__ void st16(const u32x4& v, const u32x4& rs, unsigned voff, unsigned soff) {
  asm volatile("buffer_store_dwordx4 %0, %1, %2, %3 offen sc1\n\ts_nop 1" :: "v"(v), "v"(voff), "s"(rs), "s"(soff) : "memory");
}
template <int N> __device__ __forceinline__ void wait_vm() { asm volatile("s_waitcnt vmcnt(%0)" :: "i"(N) : "memory"); }
__device__ __forceinline__ u32x4 make_rsrc(const void* p, unsigned bytes) {
  const unsigned long long a = (unsigned long long)p;
  return u32x4{(unsigned)a, (unsigned)(a >> 32) & 0xffffu, bytes, 0x00020000u};
}
__device__ __forceinline__ void spin_flags(const char* p) {
  for (unsigned spins = 0; !__all(__hip_atomic_load((const gu32*)p, RLX_AGENT) != 0u) && spins < (1u << 21); ++spins)
    __builtin_amdgcn_s_sleep(3);
}

constexpr int GW = 10;
constexpr int NQ1 = GW / 2, NQW = 16 - NQ1;
constexpr int WSL = 16384, XSL = 20480, XRING = 3 * WSL;
constexpr int RAWB = 67 * 1024, XT1 = RAWB + NQ1 * 4096, SINK = XT1 + RAWB;
constexpr int RAWL = XRING + 3 * XSL;
constexpr int CORR = RAWL + 4096, CORD = RAWL + NQW * 4096;
constexpr int GAM = RAWL + 52224, CBUF = RAWL + 49152;

struct Ctx {
  char* smem;
  char* ws;
  const unsigned short *gamma, *beta;
  u32x4 rs;
  unsigned ldg, ldf, sg, sf;
  unsigned voff, voff5, crd;
  unsigned xbase, wbase;
  unsigned pwr, prd, poff;
  unsigned xo;
  unsigned lane4, fvo;
  unsigned id;
  unsigned tag[2];
  int es;
  int wv, vb;
  unsigned long long pc;
  unsigned pfo, pft;
};
__device__ __forceinline__ void code_touch(const Ctx& cx) {
  if ((cx.wv ^ (cx.vb >> 3)) & 3) return;
#pragma unroll
  for (int h = 0; h < 2; ++h)
    asm volatile("s_mov_b32 m0, %0\n\ts_nop 0\n\tglobal_load_lds_dword %1, %2" :: "s"(SINK + cx.wv * 256), "v"(min(__builtin_amdgcn_mbcnt_hi(~0u, __builtin_amdgcn_mbcnt_lo(~0u, cx.wv * 64u + h * 512u)) * 128u, cx.pfo)), "s"(cx.pc) : "memory");
}
__device__ __forceinline__ void x_store(const Ctx& cx, const u32x4& v, unsigned kb, unsigned blk) {
  st16(v, cx.rs, cx.xo, kb + (blk + (blk & 192u)) * 128u);
}
template <int NM>
__device__ __forceinline__ void stage_issue(const Ctx& cx, int kc) {
  const unsigned slot = (unsigned)(kc + 1) * (NM == 6 ? WSL : XSL);
  if (kc >= 0 || NM == 6 || !(cx.wv & 1))
    asm volatile("s_mov_b32 m0, %0\n\ts_nop 0\n\tbuffer_load_dwordx4 %1, %2, %3 offen sc1 lds\n\tbuffer_load_dwordx4 %1, %2, %3 offen offset:1024 sc1 lds\n\t"
                 "buffer_load_dwordx4 %1, %2, %3 offen offset:2048 sc1 lds\n\tbuffer_load_dwordx4 %1, %2, %3 offen offset:3072 sc1 lds"
                 :: "s"(cx.ldg + slot), "v"(cx.voff), "s"(cx.rs), "s"(cx.sg + kc * SBLK) : "memory");
  if (kc >= 0 && NM == 5) DMA16("sc1", cx.rs, cx.ldf + slot, cx.voff, cx.sf);
}
template <int V> struct IC { static constexpr int v = V; constexpr operator int() const { return V; } };
template <int A, int B, class F> __device__ __forceinline__ void sfor(F f) {
  if constexpr (A < B) { f(IC<A>{}); sfor<A + 1, B>(f); }
}
struct RQ { u32x4 h[NST + NH]; u32x2 raw[NQW]; u32x2 vb; unsigned s[32], v[19]; };

__device__ __forceinline__ int run_bs(int vb, int t) { return 5 * ((vb >> 4) + 16 * ((vb ^ t) & 1)); }
__device__ __forceinline__ int run_q(int vb, int t) { return (((vb >> 1) & 1) + ((vb >> 2) & 2) + 2 * t) & 3; }
__device__ __forceinline__ unsigned x_addr(int vb, int t, int wv) {
  const int lane = threadIdx.x & 63, q = run_q(vb, t);
  const unsigned c = (unsigned)((lane & 15) ^ (((4 * wv + (lane >> 4)) & 7) << 1));
  return (unsigned)(2 * (HOP * run_bs(vb, t) + 4 * wv + (lane >> 4)) + ((t ^ q ^ (q >> 1)) & 1)) * 1024u + c * 16 + q * 256;
}
__device__ __forceinline__ u32x4 x_rsrc(const unsigned short* x, int vb) { return make_rsrc(x + (size_t)((vb >> 2) & 1) * T_ * D_, T_ * D_ * 2); }
__device__ __forceinline__ void issue_x(int t, int wv, unsigned a, const u32x4& rx, unsigned lds0, int j0 = 0, int j1 = 9) {
#pragma unroll
  for (int jj = 0; jj < 9; ++jj)
    if (jj >= j0 && jj < j1 && (jj < 8 || wv < 3)) DMA16("nt", rx, lds0 + t * XT1 + wv * 1024 + jj * 8192, a, jj * 65536u);
}
struct XR { s16x4 t[8]; };
constexpr int P0 = 3, P1 = 23;
constexpr int PG = 20, NFA = 1;
constexpr int TE = 4;
constexpr int NIT = 23, NS = 30 - NH - NST, NA = 7, NB = 13;
constexpr int ord(int p) { return p < 0 || p >= NIT ? -1 : p == 0 ? 0 : p < 6 ? p + 2 : p < 8 ? p - 5 : p; }
constexpr int st_w(int j) { return j < 2 ? j : j < 7 ? -1 : j < 15 ? j - 5 : -1; }
constexpr int st_x(int j) { return j < 7 ? j - 2 : j - 10; }
constexpr int st_pos(int j) { return st_w(j) >= 0 ? (j < 9 ? 0 : j < 13 ? 6 : 7) : j < 7 ? j - 1 : j - 7; }
constexpr int st_first(int p) {
  int j = p <= P0 ? 0 : p >= P1 ? NS : (p - P0) * NS / (P1 - P0);
  while (j > 0 && p <= NIT && st_pos(j - 1) + 2 > p - 1) --j;
  return j;
}
template <int NM, class F>
__device__ __forceinline__ void phase1(Ctx& cx, const unsigned short* __restrict__ x, const unsigned short* __restrict__ w,
                                       RQ& rqv, unsigned& xs, u32x2& vt, F setup) {
  u32x2 rq[NQW];
  static_assert(NQ1 == 5, "phase-1 wait counts assume five quanta");
  char* smem = cx.smem;
  const unsigned lds0 = (unsigned)(uintptr_t)(lds_void*)smem;
  const int idx = cx.vb >> 3, wv = cx.wv, lane = threadIdx.x & 63;
  const u32x4 rw = make_rsrc(w, 31 * D_ * D_ * 2);
  const int tap = threadIdx.x >> 5, pc = threadIdx.x & 31;
  const unsigned roff = (unsigned)(tap * (2 * D_ * D_ * 2) + pc * 8);
  const int ng = wv & 3, mg = wv >> 2;
  const int g = lane >> 4, qq = (lane >> 2) & 3, pp = lane & 3;
  u32x2 r1[NQ1];
  u32x4 fb[2];
  unsigned long long tag;
  const char* rec = cx.ws + WS_PUB + ((cx.id & 7) * 8 + wv) * 16;
  u32x4 pr, wl;
  {
    const int vb = cx.vb;
    const int kr = (vb >> 7) * 32 + (vb & 4) * 4 + (idx & 15);
    auto raw_load = [&](u32x2& r, int q) {
      const unsigned so = (unsigned)(kr + 64 * (2 * (q & 3) + ((q >> 2) & 1)) + 512 * (((q >> 3) ^ q ^ (q >> 1)) & 1)) * 1024u + (unsigned)((vb + q) & 3) * 256u;
      asm volatile("buffer_load_dwordx2 %0, %1, %2, %3 offen nt" : "=v"(r) : "v"(roff), "s"(rw), "s"(so) : "memory");
    };
#pragma unroll
    for (int k = 0; k < NQ1; ++k) raw_load(r1[k], k);
    const unsigned xa1 = x_addr(vb, 1, wv);
    issue_x(1, wv, xa1, x_rsrc(x, vb), lds0, 0, TE);
    if (NM == 6 && (cx.id | wv) == 0) {
      unsigned g[2] = {~0u, ~0u};
#pragma unroll 1
      for (int k = -1; k < 16 && ((k & 1) || min(g[0], g[1]) > 440u); ++k) {
        unsigned long long t0, t1;
        unsigned d;
        asm volatile("s_memtime %0\n\ts_waitcnt lgkmcnt(0)\n\ts_load_dword %2, %3, 0x0\n\ts_waitcnt lgkmcnt(0)\n\ts_memtime %1\n\ts_waitcnt lgkmcnt(0)"
                     : "=&s"(t0), "=&s"(t1), "=&s"(d) : "s"(rec + 32768 + (k < 0 ? 128 : (k & 1) * 8192 + (k & 2) * 2048 + (k & 4) * 4096 + (k & 8) * 128)) : "memory");
        if (k >= 0) { if (k & 1) g[1] = min(g[1], (unsigned)(t1 - t0)); else g[0] = min(g[0], (unsigned)(t1 - t0)); }
      }
      const unsigned lo = cx.tag[0] | (((g[0] < g[1] ? 0u : 1u) ^ (cx.vb >> 2)) & 1u), hi = cx.tag[1];
      asm volatile("global_store_dwordx4 %0, %1, %2 sc1\n\ts_nop 1" :: "v"(lane * 16 + 2048), "v"(u32x4{lo, hi, lo, hi}), "s"(rec) : "memory");
    }
    {
#pragma unroll
      for (int i = 0; i < 2; ++i) asm volatile("global_load_dwordx4 %0, %1, off" : "=v"(fb[i]) : "v"(TAB.F[ng][i][lane]) : "memory");
      asm volatile("global_load_dwordx2 %0, %1, off" : "=v"(vt) : "v"(TAB.V[ng][lane]) : "memory");
      asm volatile("global_load_dwordx4 %0, %1, %2 sc1" : "=v"(pr) : "v"(0u), "s"(rec) : "memory");
    }
    if (NM == 5 && wv >= 6)
      asm volatile("buffer_load_dwordx4 %0, %1, %2, %3 offen nt" : "=v"(wl) : "v"((threadIdx.x & 127) * 16), "s"(rw), "s"(30 * D_ * D_ * 2 + vb * 2048) : "memory");
    if (cx.id == 0 && wv >= TE - 1)
      DMA16("nt", x_rsrc(x, vb), lds0 + XT1 + (wv + 1) * 8192, x_addr(vb, 1, 0), (wv + 1) * 65536u);
    if (NM == 5 || (cx.id | wv)) issue_x(1, wv, xa1, x_rsrc(x, vb), lds0, TE, 9);
#pragma unroll
    for (int k = 0; k < NQW; ++k) raw_load(rq[k], NQ1 + k);
    {
      const int bid = vb, xb = (bid >> 2) & 1, krow = kr;
      const int slot = (bid & 7) * 4 + (idx >> 3), b = (idx >> 2) & 1, nt = idx & 3;
      const int rt = 4 * g + qq;
      cx.lane4 = lane * 4;
      cx.fvo = NM == 5 ? (unsigned)((bid & 7) * 1024 + ng * 256 + lane * 4) : wv == 0 && lane < 8 ? (unsigned)(lane * 1024 + (bid >> 2) * 4) : 0x80000000u;
      cx.pwr = (unsigned)(RAWB + tap * 256 + (((pc >> 2) ^ (tap & 7)) << 5) + (pc & 3) * 8);
      cx.prd = (unsigned)(RAWB + rt * 256 + (((4 * mg) ^ (rt & 7)) << 5) + pp * 8);
      cx.poff = (unsigned)(sbase(8 * ng + ((lane >> 1) & 7)) + (krow >> 5) * SBLK + (krow & 24) * 512 + (krow & 7) * 128 +
                           mg * 2048 + (lane & 1) * 64 + g * 16);
      cx.xo = (unsigned)(sbase(8 * ng + ((lane >> 1) & 7)) + mg * SBLK + 65536 + xb * 8192 + (lane & 1) * 64 + g * 16);
      cx.xbase = (unsigned)(XRING + mg * 5 * 2048 + (lane & 8) * 128 + (lane & 7) * 64 + ((g ^ ((lane >> 1) & 2)) << 4));
      cx.wbase = (unsigned)((rt >> 3) * 4096 + ng * 1024 + (rt & 7) * 64 + (rt & 4) * 8 + pp * 8);
      const int pat = ((lane >> 2) & 7) * 128 + (lane >> 5) * 64 + (((lane & 3) ^ ((lane >> 3) & 2)) << 4);
      if (NM == 6) {
        cx.voff = (unsigned)(sbase(slot) + nt * 16384 + ng * 4096 + pat);
        cx.ldg = lds0 + ng * 4096;
        cx.ldf = lds0 + CORR + ng * 1024;
        cx.sg = SBLK;
        cx.sf = 0;
        const int kr8 = lane >> 3;
        cx.voff5 = (unsigned)(WS_L - WS_S + (8 * ng + kr8) * 1024 + (nt * 2 + xb) * 128 + (((lane & 7) ^ ((kr8 & 2) | (ng & 1) << 2)) << 4));
        cx.crd = (unsigned)(CORR + (8 * g + qq) * 128 + ((ng ^ ((qq >> 1) | (g & 1) << 1)) << 5) + pp * 8);
      } else {
        const int rg = (bid & 1) * 8 + (slot & 1) * 4 + (bid & 2) + (idx >> 4);
        cx.voff = (unsigned)(sbase(slot) + 65536 + b * 8192 + pat);
        cx.ldg = lds0 + XRING + (ng + (ng >> 1)) * 4096;
        cx.ldf = lds0 + XRING + 8192 + ng * 1024;
        cx.sg = (~ng & 1) * SBLK + (ng + (ng >> 1) * 3 + ((ng + 1) >> 2) * 2) * 4096;
        cx.sf = 16384 + ng * 1024;
        cx.voff5 = (unsigned)(WS_D - WS_S + (b * NBLK + 10 * rg + min(lane & 15, 9)) * 1024 + ng * 64 + g * 16);
      }
    }
    asm volatile("" : "+v"(cx.fvo), "+v"(cx.poff), "+v"(cx.xo), "+v"(cx.voff), "+v"(cx.voff5), "+v"(cx.lane4));
  }
  Rng gx, gw;
  auto scan = [&](int t) __attribute__((always_inline)) {
    u32x4 v[9];
#pragma unroll
    for (int j = 0; j < 9; ++j)
      if (j < 8 || NM == 6) v[j] = *reinterpret_cast<const u32x4*>(smem + t * XT1 + (j < 8 || wv < 3 ? j : 7) * 8192 + threadIdx.x * 16);
    SB0;
#pragma unroll
    for (int j = 0; j < 9; ++j)
      if (j < 8 || NM == 6) {
        widen(gx, v[j][0], v[j][1]);
        widen(gx, v[j][2], v[j][3]);
      }
    asm volatile("" : "+v"(gx.hi), "+v"(gx.lo));
    SB0;
  };
  if (NM == 6 && (cx.id | wv) == 0) wait_vm<NQW>();
  wait_vm<8 + 4 + NQW>();
  unsigned near;
  scan(0);
#pragma unroll
  for (int q = 0; q < NQ1; ++q) {
    asm volatile("" : "+v"(r1[q]));
    widen(gw, r1[q][0], r1[q][1]);
  }
  {
    wait_vm<NQW + 8 - TE>();
    unsigned d0, d1;
    for (unsigned spins = 0;; ++spins) {
      asm volatile("" : "+v"(pr));
      d0 = __builtin_amdgcn_readfirstlane(pr[0]);
      d1 = __builtin_amdgcn_readfirstlane(pr[1]);
      const unsigned d2 = __builtin_amdgcn_readfirstlane(pr[2]), d3 = __builtin_amdgcn_readfirstlane(pr[3]);
      if (__builtin_expect((d0 == ~d2 && d1 == ~d3) || spins > (1u << 21), 1)) break;
      __builtin_amdgcn_s_sleep(2);
      asm volatile("global_load_dwordx4 %0, %1, %2 sc1\n\ts_waitcnt vmcnt(0)" : "=v"(pr) : "v"(0u), "s"(rec) : "memory");
    }
    tag = d0 | (unsigned long long)d1 << 32;
    const unsigned vn = (cx.id + (d0 >> 1)) & 7u;
    if (__builtin_expect(vn != (cx.vb & 7u), 0)) restart(cx.id | 256u | vn << 10);
  }
  const unsigned ee = (cx.id & 512u ? cx.id : (unsigned)(tag >> 32)) >> 16, ex = ee & 255u, ew = ee >> 8;
  cx.es = 256 - (int)(ex + ew);
  char* ws;
  const int bid = cx.vb;
  u32x4 f0, f1;
  char* fab;
  const u32x4* fa;
  u32x2 ov;
  unsigned pollv;
  const int rb[2] = {run_bs(bid, 0), run_bs(bid, 1)};
  unsigned xb0[4];
#pragma unroll
  for (int k = 0; k < 4; ++k) xb0[k] = (unsigned)((4 * g + qq) * 256 + (((2 * mg) ^ ((4 * g + qq + 2 * k) & 7)) << 5) + pp * 8);
#pragma unroll
  for (int q = 0; q < NQ1; ++q) *reinterpret_cast<u32x2*>(smem + cx.pwr + q * 4096) = r1[q];
  asm volatile("" : "+v"(fb[0]), "+v"(fb[1]), "+v"(vt));
#pragma unroll
  for (int i = 0; i < 2; ++i) vt[i] = scale2(vt[i], 128 - (int)ew);
#pragma unroll
  for (int i = 0; i < 8; ++i) fb[i >> 2][i & 3] = scale2(fb[i >> 2][i & 3], 128 - (int)ex);
  asm volatile("" : "+v"(fb[0]), "+v"(fb[1]), "+v"(vt));
  asm volatile("s_waitcnt lgkmcnt(0)" : "+v"(xb0[0]), "+v"(xb0[1]), "+v"(xb0[2]), "+v"(xb0[3]) :: "memory");
  wait_vm<NQW>();
  asm volatile("global_load_dwordx2 %0, %1, %2 sc1" : "=v"(ov) : "v"(2048u), "s"(rec) : "memory");
  __builtin_amdgcn_s_barrier();
  SB0;
  u32x4 hw[6][2], hx[20];
  f32x4 pend[2];
  auto rd = [&](int it, int i) __attribute__((always_inline)) {
    if (it < 3) return tr_read(smem + ((cx.prd ^ (unsigned)((i & 3) << 5)) + (2 * it + (i >> 2)) * 4096));
    const int n = it - 3;
    const unsigned base = (xb0[n % 5 & 3] ^ (unsigned)((4 * ((n / 5) & 1) + (i & 1)) << 5)) + n / 10 * XT1;
    return tr_read(smem + (base + n % 5 * (HOP * 256) + (i >> 1) * (16 * 256)));
  };
  auto dst = [&](int it) -> u32x4& { return it < 3 ? hw[it == 2 ? 4 : 2 * it + 1][1] : hx[it - 3]; };
  auto item = [&](int it, int nx, int pv, XR& xr, auto side) __attribute__((always_inline)) {
    const XR c = xr;
    const int nm = it < 2 ? 8 : 4, nr = nx == 2 ? 4 : 8;
    f32x4 r[8];
#pragma unroll
    for (int i = 0; i < 8; ++i) {
      if (i >= nm) break;
      const int s2 = i & 1, ks = (i >> 1) & 1;
      if (it < 3) r[i] = MFMS(c.t[i], vt);
      else r[i] = MFMA(cat(c.t[4 * ks + s2], c.t[4 * ks + 2 + s2]), __builtin_bit_cast(bf16x8, fb[ks]), i < 2 ? ZERO4 : r[i - 2]);
      SB0;
      if (nx >= 0) {
#pragma unroll
        for (int j = 0; j < 8; ++j) if (j < nr && j * nm / nr == i) xr.t[j] = rd(nx, j);
      }
      if (pv >= 0 && (nm == 4 ? i >= 2 : i >= 1 && i <= 4)) {
        u32x4& d = dst(pv);
#pragma unroll
        for (int e = 0; e < 4; ++e)
          if ((nm == 4 ? 2 + (e >> 1) : 1 + e) == i) d[e] = pk_f16(pend[e >> 1][(e & 1) * 2], pend[e >> 1][(e & 1) * 2 + 1]);
        if (i == (nm == 4 ? 3 : 4)) asm volatile("" : "+v"(d));
      }
      if (it < 3 && i >= 3 && (i & 1)) {
        u32x4& d = hw[2 * it + ((i - 3) >> 2)][((i - 3) >> 1) & 1];
        d = pack8h(r[i - 3], r[i - 2]);
        asm volatile("" : "+v"(d));
      }
      side(i * (8 / nm));
      if (nm == 4) side(2 * i + 1);
      SB0;
    }
    pend[0] = r[nm - 2];
    pend[1] = r[nm - 1];
  };
  const unsigned so0 = (unsigned)(2 * run_q(bid, 0)) * SBLK, so1 = (unsigned)(16 + 2 * run_q(bid, 1)) * SBLK;
  auto store = [&](int j) __attribute__((always_inline)) {
    if (j == 0) {
      wait_vm<0>();
      for (unsigned spins = 0;; ++spins) {
        asm volatile("" : "+v"(ov));
        near = __builtin_amdgcn_readfirstlane(ov[0]) ^ (unsigned)tag;
        if (__builtin_expect((near < 2u && __builtin_amdgcn_readfirstlane(ov[1]) == (unsigned)(tag >> 32)) || spins > (1u << 21), 1)) break;
        __builtin_amdgcn_s_sleep(2);
        asm volatile("global_load_dwordx2 %0, %1, %2 sc1\n\ts_waitcnt vmcnt(0)" : "=v"(ov) : "v"(2048u), "s"(rec) : "memory");
      }
      ws = cx.ws += (near & 1u) * 8192;
      cx.rs = make_rsrc(ws + WS_S, WS_L - WS_S + D_ * D_ * 2);
      fab = ws + WS_FA + (cx.id & 512u) * 32;
      fa = reinterpret_cast<const u32x4*>(fab + (bid & 7) * 2048) + lane;
      if (NM == 6 && wv == 1) {
        const unsigned o = (lane < 32 ? WS_STATS : WS_S + FLG + 16384 - 512) + bid * 512 + lane * 16;
        const unsigned v = lane < 32 ? ~0u : 0u;
        asm volatile("global_store_dwordx4 %0, %1, %2 sc1\n\ts_nop 1" :: "v"(o), "v"(u32x4{v, v, v, v}), "s"(ws) : "memory");
        if (lane < 8) __hip_atomic_store((gu32*)(ws + WS_S + FLG + lane * 1024 + ((bid + 2) & 3) * 256 + (bid >> 2) * 4), 0u, RLX_AGENT);
      }
    }
    if (j == NB + 1 && NM == 6) {
      if (((bid >> 1) ^ (bid >> 3) ^ 1) & 1 && threadIdx.x < 160) {
        const int t = threadIdx.x >= 80, blk = (threadIdx.x >> 4) - 5 * t;
        const unsigned o = (unsigned)(t * XT1 + blk * (HOP * 256) + (((lane & 15) ^ ((blk & 3) << 2)) << 4));
        const u32x4 lo = *reinterpret_cast<const u32x4*>(smem + o), hi = *reinterpret_cast<const u32x4*>(smem + o + 64 * 256);
        u32x4 d;
#pragma unroll
        for (int e = 0; e < 4; ++e) d[e] = pk_bf16(bf_lo(hi[e]) - bf_lo(lo[e]), bf_hi(hi[e]) - bf_hi(lo[e]));
        st16(d, cx.rs, (unsigned)((((bid >> 2) & 1) * NBLK + (t ? rb[1] : rb[0]) + blk) * 1024 + run_q(bid, t) * 256 + (lane & 15) * 16), WS_D - WS_S);
      }
    }
    if (j == NB + 1 && NM == 5) {
      if (wv >= 6) {
        asm volatile("" : "+v"(wl));
        st16(wl, cx.rs, (threadIdx.x & 127) * 16, WS_L - WS_S + bid * 2048);
      }
    }
    const int wq = st_w(j), xn = st_x(j);
    if (wq >= 0) st16(hw[wq >> 1][wq & 1], cx.rs, cx.poff, (unsigned)(wq >> 1) * (2 * SBLK) + (unsigned)((bid + (wq >> 1)) & 3) * 16384u + (wq & 1) * 1024u);
    else x_store(cx, hx[xn], (xn < 10 ? so0 : so1) + ((xn / 5) & 1) * (8 * SBLK), (unsigned)(rb[xn / 10] + xn % 5));
    if (j == NA + NFA - 1) {
      scan(1);
#pragma unroll
      for (int k = 0; k < NQW; ++k) {
        asm volatile("" : "+v"(rq[k]));
        widen(gw, rq[k][0], rq[k][1]);
      }
      const unsigned mx = amax(gx), mw = amax(gw);
      if (__builtin_expect(__any(expo(mx) > ex + 7 || expo(mw) > ew + 9) && !(cx.id & 512u), 0)) {
        const u32x2 m = {wave_max(expo(mx)), wave_max(expo(mw))};
        asm volatile("global_atomic_umax %0, %1, %3 sc1\n\tglobal_atomic_umax %0, %2, %3 offset:4 sc1\n\ts_waitcnt vmcnt(0)"
                     :: "v"(lane * 16 + 1024), "v"(m[0]), "v"(m[1]), "s"(rec - ((cx.id & 7) * 8 + wv) * 16) : "memory");
      }
    }
    if (j == NA + NFA - 1 || j == NB + NFA - 1) {
      wait_vm<NFA>();
      __builtin_amdgcn_s_barrier();
      if (wv == 0 && lane < 8) {
        if (j < NB) __hip_atomic_store((gu64*)(fab + lane * 2048) + bid, tag, RLX_AGENT);
        else __hip_atomic_store((gu32*)(ws + WS_S + FLG + lane * 1024 + ((bid + 2) & 3) * 256 + (bid >> 2) * 4), 1u, RLX_AGENT);
      }
    }
    SB0;
  };
  XR xr;
#pragma unroll
  for (int j = 0; j < 8; ++j) xr.t[j] = rd(0, j);
  SB0;
  static_assert(st_first(NIT) == NS && PG >= 14 && st_first(PG - 2) >= NB + 2 && NA + NFA <= NB && NFA >= 1 && NH <= 7, "order of the hand-off steps");
  sfor<0, NIT>([&](auto p) __attribute__((always_inline)) {
    constexpr int j0 = st_first(p.v), j1 = st_first(p.v + 1);
    if (p >= 12 && p < PG) { if (!(bid & 1) && cx.id) __builtin_amdgcn_s_sleep(3); }
    if (p == PG - 2) {
      code_touch(cx);
      if (NM == 6 && wv == 0)
        asm volatile("global_load_dwordx4 %0, %2, off sc1\n\tglobal_load_dwordx4 %1, %2, off offset:1024 sc1" : "=&v"(f0), "=&v"(f1) : "v"(fa) : "memory");
    }
    if (p == PG) {
      if (NM == 6 && wv == 0) {
        wait_vm<st_first(PG) - st_first(PG - 2)>();
        for (unsigned spins = 0; spins < (1u << 21); ++spins) {
          asm volatile("" : "+v"(f0), "+v"(f1));
          const unsigned t0 = (unsigned)tag, t1 = (unsigned)(tag >> 32);
          if (__all(f0[0] == t0 && f0[1] == t1 && f0[2] == t0 && f0[3] == t1 && f1[0] == t0 && f1[1] == t1 && f1[2] == t0 && f1[3] == t1)) break;
          __builtin_amdgcn_s_sleep(2);
          asm volatile("global_load_dwordx4 %0, %2, off sc1\n\tglobal_load_dwordx4 %1, %2, off offset:1024 sc1\n\ts_waitcnt vmcnt(0)"
                       : "=&v"(f0), "=&v"(f1) : "v"(fa) : "memory");
        }
      }
      __builtin_amdgcn_s_barrier();
      stage_issue<NM>(cx, -1);
      asm volatile("global_load_dwordx2 %0, %1, %2 sc1" : "=v"(ov) : "v"(1024u), "s"(rec) : "memory");
      stage_issue<NM>(cx, 0);
      SB0;
    }
    if (p == NIT - 1 && NM == 5) {
      asm volatile("buffer_load_dword %0, %1, %2, %3 offen sc1" : "=v"(pollv) : "v"(cx.fvo), "s"(cx.rs), "s"(FLG) : "memory");
    }
    item(ord(p), ord(p + 1), ord(p - 1), xr, [&](int i) __attribute__((always_inline)) {
      if ((i * (j1 - j0) + 7) / 8 < ((i + 1) * (j1 - j0) + 7) / 8) store(j0 + (i * (j1 - j0) + 7) / 8);
    });
  });
  hx[19] = pack8h(pend[0], pend[1]);
  xs = so1 + (unsigned)rb[1];
#pragma unroll
  for (int e = 0; e < NST + NH; ++e) rqv.h[e] = hx[20 - NH - NST + e];
  asm volatile("" : "+v"(rqv.h[NST + NH - 1]));
  setup();
  wait_vm<NM == 5 ? NS - st_first(NIT - 1) : 10 - NM + NS - st_first(PG)>();
  {
    asm volatile("" : "+v"(ov));
    const unsigned ox = __builtin_amdgcn_readfirstlane(ov[0]), ow = __builtin_amdgcn_readfirstlane(ov[1]);
    if (__builtin_expect((ox | ow) && !(cx.id & 512u), 0))
      restart((cx.id & 255u) | 768u | (bid & 7u) << 10 | min(max(ex + 7, ox) - 7, 220u) << 16 | min(max(ew + 9, ow) - 9, 220u) << 24);
    if (NM == 6 && (cx.id & 255u) == 0 && wv == 0)
      asm volatile("global_store_dwordx4 %0, %1, %2 sc1\n\ts_nop 1" :: "v"(lane * 16), "v"(u32x4{0u, 0u, 0u, 0u}), "s"(rec) : "memory");
  }
  if (NM == 5) {
    asm volatile("" : "+v"(pollv));
    if (!__all(pollv != 0u)) spin_flags(ws + WS_S + FLG + cx.fvo);
  }
#pragma unroll
  for (int k = 0; k < NQW; ++k) rqv.raw[k] = rq[k];
}

#define LOOP0 \
"s_mov_b32 s91, 0x40004\n" \
"s_mov_b32 s92, 0xfff8fff8\n" \
"ds_read_b64_tr_b16 v[120:121], v241 offset:0\n" \
"ds_read_b64_tr_b16 v[122:123], v241 offset:8192\n" \
"ds_read_b64_tr_b16 v[124:125], v242 offset:0\n" \
"ds_read_b64_tr_b16 v[126:127], v242 offset:8192\n" \
"ds_read_b64_tr_b16 v[128:129], v241 offset:512\n" \
"ds_read_b64_tr_b16 v[130:131], v241 offset:8704\n" \
"ds_read_b64_tr_b16 v[132:133], v242 offset:512\n" \
"ds_read_b64_tr_b16 v[134:135], v242 offset:8704\n" \
"ds_read_b128 v[160:163], v240 offset:0\n" \
"ds_read_b128 v[164:167], v240 offset:512\n" \
"ds_read_b128 v[168:171], v240 offset:2048\n" \
"ds_read_b128 v[172:175], v240 offset:2560\n" \
"s_add_i32 m0, s44, 0x8000\n" \
"s_add_i32 s80, s46, 0x400000\n" \
"buffer_load_dwordx4 v232, s[36:39], s80 offen sc1 lds\n" \
"buffer_load_dwordx4 v232, s[36:39], s80 offen offset:1024 sc1 lds\n" \
"buffer_load_dwordx4 v232, s[36:39], s80 offen offset:2048 sc1 lds\n" \
"buffer_load_dwordx4 v232, s[36:39], s80 offen offset:3072 sc1 lds\n" \
"s_waitcnt vmcnt(8) lgkmcnt(0)\n" \
"s_barrier\n" \
"v_mfma_f32_16x16x32_f16 v[0:3], v[120:123], v[160:163], 0\n" \
"v_pk_add_f16 v136, v120, v128\n" \
"v_pk_add_f16 v137, v121, v129\n" \
"v_pk_add_f16 v138, v122, v130\n" \
"v_pk_add_f16 v139, v123, v131\n" \
"ds_read_b128 v[176:179], v240 offset:24576\n" \
"v_mfma_f32_16x16x32_f16 v[12:15], v[124:127], v[160:163], 0\n" \
"v_pk_add_f16 v160, v160, v164\n" \
"v_pk_add_f16 v161, v161, v165\n" \
"v_pk_add_f16 v140, v124, v132\n" \
"v_mfma_f32_16x16x32_f16 v[4:7], v[128:131], v[164:167], 0\n" \
"v_pk_add_f16 v162, v162, v166\n" \
"v_pk_add_f16 v163, v163, v167\n" \
"v_pk_add_f16 v141, v125, v133\n" \
"buffer_store_dwordx4 v[208:211], v235, s[36:39], s63 offen sc1\n" \
"v_mfma_f32_16x16x32_f16 v[16:19], v[132:135], v[164:167], 0\n" \
"v_pk_add_f16 v142, v126, v134\n" \
"v_pk_add_f16 v143, v127, v135\n" \
"ds_read_b128 v[180:183], v240 offset:25088\n" \
"s_add_i32 m0, s44, 0x0\n" \
"s_add_i32 s80, s46, 0x800000\n" \
"v_mfma_f32_16x16x32_f16 v[8:11], v[136:139], v[160:163], 0\n" \
"buffer_load_dwordx4 v232, s[36:39], s80 offen sc1 lds\n" \
"ds_write_b64 v246, v[96:97] offset:0\n" \
"v_mfma_f32_16x16x32_f16 v[20:23], v[140:143], v[160:163], 0\n" \
"buffer_store_dwordx4 v[72:75], v235, s[36:39], s60 offen sc1\n" \
"ds_read_b64_tr_b16 v[144:145], v241 offset:16384\n" \
"v_mfma_f32_16x16x32_f16 v[24:27], v[120:123], v[168:171], 0\n" \
"ds_read_b128 v[160:163], v240 offset:26624\n" \
"ds_write_b64 v246, v[98:99] offset:4096\n" \
"ds_write_b64 v246, v[100:101] offset:8192\n" \
"v_mfma_f32_16x16x32_f16 v[36:39], v[124:127], v[168:171], 0\n" \
"v_pk_add_f16 v168, v168, v172\n" \
"v_pk_add_f16 v169, v169, v173\n" \
"ds_read_b64_tr_b16 v[146:147], v241 offset:24576\n" \
"v_mfma_f32_16x16x32_f16 v[28:31], v[128:131], v[172:175], 0\n" \
"v_pk_add_f16 v170, v170, v174\n" \
"v_pk_add_f16 v171, v171, v175\n" \
"v_mfma_f32_16x16x32_f16 v[40:43], v[132:135], v[172:175], 0\n" \
"ds_read_b128 v[164:167], v240 offset:27136\n" \
"ds_write_b64 v246, v[102:103] offset:12288\n" \
"v_mfma_f32_16x16x32_f16 v[32:35], v[136:139], v[168:171], 0\n" \
"buffer_load_dwordx4 v232, s[36:39], s80 offen offset:1024 sc1 lds\n" \
"ds_read_b64_tr_b16 v[148:149], v242 offset:16384\n" \
"v_mfma_f32_16x16x32_f16 v[44:47], v[140:143], v[168:171], 0\n" \
"buffer_store_dwordx4 v[76:79], v235, s[36:39], s61 offen sc1\n" \
"ds_read_b64_tr_b16 v[150:151], v242 offset:24576\n" \
"s_waitcnt lgkmcnt(9)\n" \
"v_mfma_f32_16x16x32_f16 v[48:51], v[120:123], v[176:179], 0\n" \
"ds_read_b128 v[168:171], v240 offset:28672\n" \
"ds_write_b64 v246, v[104:105] offset:16384\n" \
"ds_write_b64 v246, v[106:107] offset:20480\n" \
"v_mfma_f32_16x16x32_f16 v[60:63], v[124:127], v[176:179], 0\n" \
"v_pk_add_f16 v176, v176, v180\n" \
"v_pk_add_f16 v177, v177, v181\n" \
"ds_read_b64_tr_b16 v[152:153], v241 offset:16896\n" \
"v_mfma_f32_16x16x32_f16 v[52:55], v[128:131], v[180:183], 0\n" \
"v_pk_add_f16 v178, v178, v182\n" \
"v_pk_add_f16 v179, v179, v183\n" \
"v_mfma_f32_16x16x32_f16 v[64:67], v[132:135], v[180:183], 0\n" \
"ds_read_b128 v[172:175], v240 offset:29184\n" \
"ds_write_b64 v246, v[108:109] offset:24576\n" \
"v_mfma_f32_16x16x32_f16 v[56:59], v[136:139], v[176:179], 0\n" \
"buffer_load_dwordx4 v232, s[36:39], s80 offen offset:2048 sc1 lds\n" \
"ds_write_b64 v246, v[110:111] offset:28672\n" \
"v_mfma_f32_16x16x32_f16 v[68:71], v[140:143], v[176:179], 0\n" \
"buffer_store_dwordx4 v[80:83], v235, s[36:39], s62 offen sc1\n" \
"ds_read_b64_tr_b16 v[154:155], v241 offset:25088\n" \
"s_waitcnt lgkmcnt(9)\n" \
"v_mfma_f32_16x16x32_f16 v[72:75], v[120:123], v[160:163], 0\n" \
"ds_read_b128 v[176:179], v240 offset:20480\n" \
"ds_write_b64 v246, v[112:113] offset:32768\n" \
"ds_read_b64_tr_b16 v[156:157], v242 offset:16896\n" \
"v_mfma_f32_16x16x32_f16 v[84:87], v[124:127], v[160:163], 0\n" \
"v_pk_add_f16 v160, v160, v164\n" \
"v_pk_add_f16 v161, v161, v165\n" \
"ds_read_b64_tr_b16 v[158:159], v242 offset:25088\n" \
"v_mfma_f32_16x16x32_f16 v[76:79], v[128:131], v[164:167], 0\n" \
"v_pk_add_f16 v162, v162, v166\n" \
"v_pk_add_f16 v163, v163, v167\n" \
"v_mfma_f32_16x16x32_f16 v[88:91], v[132:135], v[164:167], 0\n" \
"ds_read_b128 v[180:183], v240 offset:20992\n" \
"ds_write_b64 v246, v[114:115] offset:36864\n" \
"v_mfma_f32_16x16x32_f16 v[80:83], v[136:139], v[160:163], 0\n" \
"buffer_load_dwordx4 v232, s[36:39], s80 offen offset:3072 sc1 lds\n" \
"ds_write_b64 v246, v[116:117] offset:40960\n" \
"v_mfma_f32_16x16x32_f16 v[92:95], v[140:143], v[160:163], 0\n" \
"s_waitcnt lgkmcnt(9)\n" \
"v_mfma_f32_16x16x32_f16 v[96:99], v[120:123], v[168:171], 0\n" \
"v_pk_add_f16 v120, v144, v152\n" \
"v_pk_add_f16 v121, v145, v153\n" \
"ds_read_b128 v[160:163], v240 offset:22528\n" \
"v_mfma_f32_16x16x32_f16 v[108:111], v[124:127], v[168:171], 0\n" \
"v_pk_add_f16 v168, v168, v172\n" \
"v_pk_add_f16 v169, v169, v173\n" \
"ds_read_b128 v[164:167], v240 offset:23040\n" \
"v_mfma_f32_16x16x32_f16 v[100:103], v[128:131], v[172:175], 0\n" \
"v_pk_add_f16 v170, v170, v174\n" \
"v_pk_add_f16 v171, v171, v175\n" \
"v_mfma_f32_16x16x32_f16 v[112:115], v[132:135], v[172:175], 0\n" \
"s_waitcnt lgkmcnt(5)\n" \
"v_pk_add_f16 v122, v146, v154\n" \
"v_pk_add_f16 v123, v147, v155\n" \
"v_mfma_f32_16x16x32_f16 v[104:107], v[136:139], v[168:171], 0\n" \
"v_pk_add_f16 v124, v148, v156\n" \
"v_pk_add_f16 v125, v149, v157\n" \
"v_mfma_f32_16x16x32_f16 v[116:119], v[140:143], v[168:171], 0\n" \
"v_pk_add_f16 v126, v150, v158\n" \
"v_pk_add_f16 v127, v151, v159\n" \
"s_waitcnt vmcnt(8) lgkmcnt(0)\n" \
"s_barrier\n" \
"v_mfma_f32_16x16x32_f16 v[0:3], v[144:147], v[176:179], v[0:3]\n" \
"ds_read_b128 v[168:171], v240 offset:45056\n" \
"ds_read_b64_tr_b16 v[184:185], v236 offset:0\n" \
"v_mfma_f32_16x16x32_f16 v[12:15], v[148:151], v[176:179], v[12:15]\n" \
"v_pk_add_f16 v176, v176, v180\n" \
"v_pk_add_f16 v177, v177, v181\n" \
"ds_read_b64_tr_b16 v[188:189], v237 offset:0\n" \
"v_mfma_f32_16x16x32_f16 v[4:7], v[152:155], v[180:183], v[4:7]\n" \
"v_pk_add_f16 v178, v178, v182\n" \
"v_pk_add_f16 v179, v179, v183\n" \
"buffer_store_dwordx4 v[212:215], v235, s[36:39], s64 offen sc1\n" \
"v_mfma_f32_16x16x32_f16 v[16:19], v[156:159], v[180:183], v[16:19]\n" \
"ds_read_b128 v[172:175], v240 offset:45568\n" \
"s_add_i32 m0, s44, 0x4000\n" \
"s_add_i32 s80, s46, 0xc00000\n" \
"v_mfma_f32_16x16x32_f16 v[8:11], v[120:123], v[176:179], v[8:11]\n" \
"buffer_load_dwordx4 v232, s[36:39], s80 offen sc1 lds\n" \
"v_mfma_f32_16x16x32_f16 v[20:23], v[124:127], v[176:179], v[20:23]\n" \
"ds_read_b64_tr_b16 v[128:129], v241 offset:32768\n" \
"ds_read_b64_tr_b16 v[130:131], v241 offset:40960\n" \
"s_waitcnt lgkmcnt(4)\n" \
"v_mfma_f32_16x16x16_bf16 v[184:187], v[184:185], v[192:193], 0\n" \
"v_mfma_f32_16x16x32_f16 v[24:27], v[144:147], v[160:163], v[24:27]\n" \
"ds_read_b128 v[176:179], v240 offset:47104\n" \
"s_add_i32 s84, s52, 0x8004000\n" \
"s_waitcnt lgkmcnt(4)\n" \
"v_mfma_f32_16x16x16_bf16 v[188:191], v[188:189], v[192:193], 0\n" \
"v_mfma_f32_16x16x32_f16 v[36:39], v[148:151], v[160:163], v[36:39]\n" \
"v_pk_add_f16 v160, v160, v164\n" \
"v_pk_add_f16 v161, v161, v165\n" \
"buffer_store_dword v240, v243, s[36:39], s84 offen sc1\n" \
"v_mfma_f32_16x16x32_f16 v[28:31], v[152:155], v[164:167], v[28:31]\n" \
"v_pk_add_f16 v162, v162, v166\n" \
"v_pk_add_f16 v163, v163, v167\n" \
"ds_read_b64_tr_b16 v[132:133], v242 offset:32768\n" \
"v_mfma_f32_16x16x32_f16 v[40:43], v[156:159], v[164:167], v[40:43]\n" \
"v_cvt_pk_f16_f32 v184, v184, v185\n" \
"ds_read_b128 v[180:183], v240 offset:47616\n" \
"ds_read_b64_tr_b16 v[134:135], v242 offset:40960\n" \
"v_mfma_f32_16x16x32_f16 v[32:35], v[120:123], v[160:163], v[32:35]\n" \
"v_cvt_pk_f16_f32 v185, v186, v187\n" \
"v_cvt_pk_f16_f32 v186, v188, v189\n" \
"buffer_load_dwordx4 v232, s[36:39], s80 offen offset:1024 sc1 lds\n" \
"v_mfma_f32_16x16x32_f16 v[44:47], v[124:127], v[160:163], v[44:47]\n" \
"v_cvt_pk_f16_f32 v187, v190, v191\n" \
"v_add_u32 v184, s91, v184\n" \
"v_mfma_f32_16x16x32_f16 v[48:51], v[144:147], v[168:171], v[48:51]\n" \
"v_add_u32 v185, s91, v185\n" \
"v_add_u32 v186, s91, v186\n" \
"ds_read_b128 v[160:163], v240 offset:49152\n" \
"v_mfma_f32_16x16x32_f16 v[60:63], v[148:151], v[168:171], v[60:63]\n" \
"s_waitcnt lgkmcnt(4)\n" \
"v_pk_add_f16 v168, v168, v172\n" \
"v_pk_add_f16 v169, v169, v173\n" \
"v_add_u32 v187, s91, v187\n" \
"v_and_b32 v184, s92, v184\n" \
"s_add_i32 s82, s49, 0x2800000\n" \
"v_mfma_f32_16x16x32_f16 v[52:55], v[152:155], v[172:175], v[52:55]\n" \
"v_pk_add_f16 v170, v170, v174\n" \
"v_pk_add_f16 v171, v171, v175\n" \
"v_and_b32 v185, s92, v185\n" \
"v_and_b32 v186, s92, v186\n" \
"v_and_b32 v187, s92, v187\n" \
"v_mfma_f32_16x16x32_f16 v[64:67], v[156:159], v[172:175], v[64:67]\n" \
"ds_read_b128 v[164:167], v240 offset:49664\n" \
"buffer_store_dwordx4 v[184:187], v234, s[36:39], s82 offen sc1\n" \
"ds_read_b64_tr_b16 v[136:137], v241 offset:33280\n" \
"v_mfma_f32_16x16x32_f16 v[56:59], v[120:123], v[168:171], v[56:59]\n" \
"buffer_load_dwordx4 v232, s[36:39], s80 offen offset:2048 sc1 lds\n" \
"ds_read_b64_tr_b16 v[138:139], v241 offset:41472\n" \
"v_mfma_f32_16x16x32_f16 v[68:71], v[124:127], v[168:171], v[68:71]\n" \
"ds_read_b64_tr_b16 v[184:185], v238 offset:0\n" \
"ds_read_b64_tr_b16 v[188:189], v239 offset:0\n" \
"v_mfma_f32_16x16x32_f16 v[72:75], v[144:147], v[176:179], v[72:75]\n" \
"ds_read_b128 v[168:171], v240 offset:40960\n" \
"ds_read_b64_tr_b16 v[140:141], v242 offset:33280\n" \
"v_mfma_f32_16x16x32_f16 v[84:87], v[148:151], v[176:179], v[84:87]\n" \
"s_waitcnt lgkmcnt(7)\n" \
"v_pk_add_f16 v176, v176, v180\n" \
"v_pk_add_f16 v177, v177, v181\n" \
"v_mfma_f32_16x16x32_f16 v[76:79], v[152:155], v[180:183], v[76:79]\n" \
"v_pk_add_f16 v178, v178, v182\n" \
"v_pk_add_f16 v179, v179, v183\n" \
"s_waitcnt lgkmcnt(3)\n" \
"v_mfma_f32_16x16x16_bf16 v[184:187], v[184:185], v[192:193], 0\n" \
"v_mfma_f32_16x16x32_f16 v[88:91], v[156:159], v[180:183], v[88:91]\n" \
"ds_read_b128 v[172:175], v240 offset:41472\n" \
"ds_read_b64_tr_b16 v[142:143], v242 offset:41472\n" \
"s_waitcnt lgkmcnt(4)\n" \
"v_mfma_f32_16x16x16_bf16 v[188:191], v[188:189], v[192:193], 0\n" \
"v_mfma_f32_16x16x32_f16 v[80:83], v[120:123], v[176:179], v[80:83]\n" \
"buffer_load_dwordx4 v232, s[36:39], s80 offen offset:3072 sc1 lds\n" \
"v_mfma_f32_16x16x32_f16 v[92:95], v[124:127], v[176:179], v[92:95]\n" \
"v_mfma_f32_16x16x32_f16 v[96:99], v[144:147], v[160:163], v[96:99]\n" \
"v_pk_add_f16 v144, v128, v136\n" \
"v_pk_add_f16 v145, v129, v137\n" \
"v_cvt_pk_f16_f32 v184, v184, v185\n" \
"v_cvt_pk_f16_f32 v185, v186, v187\n" \
"ds_read_b128 v[176:179], v240 offset:43008\n" \
"v_mfma_f32_16x16x32_f16 v[108:111], v[148:151], v[160:163], v[108:111]\n" \
"v_pk_add_f16 v160, v160, v164\n" \
"v_pk_add_f16 v161, v161, v165\n" \
"v_cvt_pk_f16_f32 v186, v188, v189\n" \
"v_cvt_pk_f16_f32 v187, v190, v191\n" \
"ds_read_b128 v[180:183], v240 offset:43520\n" \
"v_mfma_f32_16x16x32_f16 v[100:103], v[152:155], v[164:167], v[100:103]\n" \
"v_pk_add_f16 v162, v162, v166\n" \
"v_pk_add_f16 v163, v163, v167\n" \
"v_add_u32 v184, s91, v184\n" \
"v_add_u32 v185, s91, v185\n" \
"v_add_u32 v186, s91, v186\n" \
"v_mfma_f32_16x16x32_f16 v[112:115], v[156:159], v[164:167], v[112:115]\n" \
"v_pk_add_f16 v146, v130, v138\n" \
"v_pk_add_f16 v147, v131, v139\n" \
"v_add_u32 v187, s91, v187\n" \
"v_and_b32 v184, s92, v184\n" \
"v_mfma_f32_16x16x32_f16 v[104:107], v[120:123], v[160:163], v[104:107]\n" \
"s_waitcnt lgkmcnt(2)\n" \
"v_pk_add_f16 v148, v132, v140\n" \
"v_pk_add_f16 v149, v133, v141\n" \
"v_and_b32 v185, s92, v185\n" \
"v_and_b32 v186, s92, v186\n" \
"v_and_b32 v187, s92, v187\n" \
"v_mfma_f32_16x16x32_f16 v[116:119], v[124:127], v[160:163], v[116:119]\n" \
"v_pk_add_f16 v150, v134, v142\n" \
"v_pk_add_f16 v151, v135, v143\n" \
"buffer_store_dwordx4 v[184:187], v234, s[36:39], s82 offen offset:1024 sc1\n" \
"s_waitcnt vmcnt(8) lgkmcnt(0)\n" \
"s_barrier\n" \
"v_mfma_f32_16x16x32_f16 v[0:3], v[128:131], v[168:171], v[0:3]\n" \
"ds_read_b128 v[160:163], v240 offset:4096\n" \
"ds_read_b64_tr_b16 v[184:185], v236 offset:4096\n" \
"ds_read_b64_tr_b16 v[188:189], v237 offset:4096\n" \
"v_mfma_f32_16x16x32_f16 v[12:15], v[132:135], v[168:171], v[12:15]\n" \
"v_pk_add_f16 v168, v168, v172\n" \
"v_pk_add_f16 v169, v169, v173\n" \
"v_mfma_f32_16x16x32_f16 v[4:7], v[136:139], v[172:175], v[4:7]\n" \
"v_pk_add_f16 v170, v170, v174\n" \
"v_pk_add_f16 v171, v171, v175\n" \
"v_mfma_f32_16x16x32_f16 v[16:19], v[140:143], v[172:175], v[16:19]\n" \
"ds_read_b128 v[164:167], v240 offset:4608\n" \
"s_add_i32 m0, s44, 0x8000\n" \
"s_add_i32 s80, s46, 0x1000000\n" \
"v_mfma_f32_16x16x32_f16 v[8:11], v[144:147], v[168:171], v[8:11]\n" \
"buffer_load_dwordx4 v232, s[36:39], s80 offen sc1 lds\n" \
"s_waitcnt lgkmcnt(2)\n" \
"v_mfma_f32_16x16x16_bf16 v[184:187], v[184:185], v[192:193], 0\n" \
"v_mfma_f32_16x16x32_f16 v[20:23], v[148:151], v[168:171], v[20:23]\n" \
"ds_read_b64_tr_b16 v[120:121], v241 offset:0\n" \
"ds_read_b64_tr_b16 v[122:123], v241 offset:8192\n" \
"s_waitcnt lgkmcnt(3)\n" \
"v_mfma_f32_16x16x16_bf16 v[188:191], v[188:189], v[192:193], 0\n" \
"v_mfma_f32_16x16x32_f16 v[24:27], v[128:131], v[176:179], v[24:27]\n" \
"ds_read_b128 v[168:171], v240 offset:6144\n" \
"v_mfma_f32_16x16x32_f16 v[36:39], v[132:135], v[176:179], v[36:39]\n" \
"v_pk_add_f16 v176, v176, v180\n" \
"v_pk_add_f16 v177, v177, v181\n" \
"ds_read_b64_tr_b16 v[124:125], v242 offset:0\n" \
"v_mfma_f32_16x16x32_f16 v[28:31], v[136:139], v[180:183], v[28:31]\n" \
"v_pk_add_f16 v178, v178, v182\n" \
"v_pk_add_f16 v179, v179, v183\n" \
"v_cvt_pk_f16_f32 v184, v184, v185\n" \
"v_mfma_f32_16x16x32_f16 v[40:43], v[140:143], v[180:183], v[40:43]\n" \
"v_cvt_pk_f16_f32 v185, v186, v187\n" \
"v_cvt_pk_f16_f32 v186, v188, v189\n" \
"ds_read_b128 v[172:175], v240 offset:6656\n" \
"v_mfma_f32_16x16x32_f16 v[32:35], v[144:147], v[176:179], v[32:35]\n" \
"v_cvt_pk_f16_f32 v187, v190, v191\n" \
"v_add_u32 v184, s91, v184\n" \
"buffer_load_dwordx4 v232, s[36:39], s80 offen offset:1024 sc1 lds\n" \
"v_mfma_f32_16x16x32_f16 v[44:47], v[148:151], v[176:179], v[44:47]\n" \
"v_add_u32 v185, s91, v185\n" \
"v_add_u32 v186, s91, v186\n" \
"ds_read_b64_tr_b16 v[126:127], v242 offset:8192\n" \
"v_mfma_f32_16x16x32_f16 v[48:51], v[128:131], v[160:163], v[48:51]\n" \
"v_add_u32 v187, s91, v187\n" \
"v_and_b32 v184, s92, v184\n" \
"ds_read_b128 v[176:179], v240 offset:8192\n" \
"v_mfma_f32_16x16x32_f16 v[60:63], v[132:135], v[160:163], v[60:63]\n" \
"s_waitcnt lgkmcnt(4)\n" \
"v_pk_add_f16 v160, v160, v164\n" \
"v_pk_add_f16 v161, v161, v165\n" \
"v_and_b32 v185, s92, v185\n" \
"s_add_i32 s82, s50, 0x3000000\n" \
"v_mfma_f32_16x16x32_f16 v[52:55], v[136:139], v[164:167], v[52:55]\n" \
"v_pk_add_f16 v162, v162, v166\n" \
"v_pk_add_f16 v163, v163, v167\n" \
"v_and_b32 v186, s92, v186\n" \
"v_and_b32 v187, s92, v187\n" \
"v_mfma_f32_16x16x32_f16 v[64:67], v[140:143], v[164:167], v[64:67]\n" \
"ds_read_b128 v[180:183], v240 offset:8704\n" \
"buffer_store_dwordx4 v[184:187], v234, s[36:39], s82 offen sc1\n" \
"ds_read_b64_tr_b16 v[152:153], v241 offset:512\n" \
"v_mfma_f32_16x16x32_f16 v[56:59], v[144:147], v[160:163], v[56:59]\n" \
"buffer_load_dwordx4 v232, s[36:39], s80 offen offset:2048 sc1 lds\n" \
"v_mfma_f32_16x16x32_f16 v[68:71], v[148:151], v[160:163], v[68:71]\n" \
"ds_read_b64_tr_b16 v[154:155], v241 offset:8704\n" \
"ds_read_b64_tr_b16 v[156:157], v242 offset:512\n" \
"v_mfma_f32_16x16x32_f16 v[72:75], v[128:131], v[168:171], v[72:75]\n" \
"ds_read_b128 v[160:163], v240 offset:0\n" \
"ds_read_b64_tr_b16 v[158:159], v242 offset:8704\n" \
"v_mfma_f32_16x16x32_f16 v[84:87], v[132:135], v[168:171], v[84:87]\n" \
"s_waitcnt lgkmcnt(6)\n" \
"v_pk_add_f16 v168, v168, v172\n" \
"v_pk_add_f16 v169, v169, v173\n" \
"v_mfma_f32_16x16x32_f16 v[76:79], v[136:139], v[172:175], v[76:79]\n" \
"v_pk_add_f16 v170, v170, v174\n" \
"v_pk_add_f16 v171, v171, v175\n" \
"v_mfma_f32_16x16x32_f16 v[88:91], v[140:143], v[172:175], v[88:91]\n" \
"ds_read_b128 v[164:167], v240 offset:512\n" \
"v_mfma_f32_16x16x32_f16 v[80:83], v[144:147], v[168:171], v[80:83]\n" \
"buffer_load_dwordx4 v232, s[36:39], s80 offen offset:3072 sc1 lds\n" \
"v_mfma_f32_16x16x32_f16 v[92:95], v[148:151], v[168:171], v[92:95]\n" \
"v_mfma_f32_16x16x32_f16 v[96:99], v[128:131], v[176:179], v[96:99]\n" \
"s_waitcnt lgkmcnt(5)\n" \
"v_pk_add_f16 v128, v120, v152\n" \
"v_pk_add_f16 v129, v121, v153\n" \
"ds_read_b128 v[168:171], v240 offset:2048\n" \
"v_mfma_f32_16x16x32_f16 v[108:111], v[132:135], v[176:179], v[108:111]\n" \
"v_pk_add_f16 v176, v176, v180\n" \
"v_pk_add_f16 v177, v177, v181\n" \
"ds_read_b128 v[172:175], v240 offset:2560\n" \
"v_mfma_f32_16x16x32_f16 v[100:103], v[136:139], v[180:183], v[100:103]\n" \
"v_pk_add_f16 v178, v178, v182\n" \
"v_pk_add_f16 v179, v179, v183\n" \
"v_mfma_f32_16x16x32_f16 v[112:115], v[140:143], v[180:183], v[112:115]\n" \
"s_waitcnt lgkmcnt(3)\n" \
"v_pk_add_f16 v130, v122, v154\n" \
"v_pk_add_f16 v131, v123, v155\n" \
"v_mfma_f32_16x16x32_f16 v[104:107], v[144:147], v[176:179], v[104:107]\n" \
"v_pk_add_f16 v132, v124, v156\n" \
"v_pk_add_f16 v133, v125, v157\n" \
"v_mfma_f32_16x16x32_f16 v[116:119], v[148:151], v[176:179], v[116:119]\n" \
"v_pk_add_f16 v134, v126, v158\n" \
"v_pk_add_f16 v135, v127, v159\n" \
"s_waitcnt vmcnt(5) lgkmcnt(0)\n" \
"s_barrier\n" \
"v_mfma_f32_16x16x32_f16 v[0:3], v[120:123], v[160:163], v[0:3]\n" \
"ds_read_b128 v[176:179], v240 offset:24576\n" \
"ds_read_b64_tr_b16 v[184:185], v238 offset:4096\n" \
"ds_read_b64_tr_b16 v[188:189], v239 offset:4096\n" \
"v_mfma_f32_16x16x32_f16 v[12:15], v[124:127], v[160:163], v[12:15]\n" \
"v_pk_add_f16 v160, v160, v164\n" \
"v_pk_add_f16 v161, v161, v165\n" \
"v_mfma_f32_16x16x32_f16 v[4:7], v[152:155], v[164:167], v[4:7]\n" \
"v_pk_add_f16 v162, v162, v166\n" \
"v_pk_add_f16 v163, v163, v167\n" \
"v_mfma_f32_16x16x32_f16 v[16:19], v[156:159], v[164:167], v[16:19]\n" \
"ds_read_b128 v[180:183], v240 offset:25088\n" \
"s_add_i32 m0, s44, 0x0\n" \
"s_add_i32 s80, s46, 0x1400000\n" \
"v_mfma_f32_16x16x32_f16 v[8:11], v[128:131], v[160:163], v[8:11]\n" \
"buffer_load_dwordx4 v232, s[36:39], s80 offen sc1 lds\n" \
"s_waitcnt lgkmcnt(2)\n" \
"v_mfma_f32_16x16x16_bf16 v[184:187], v[184:185], v[192:193], 0\n" \
"v_mfma_f32_16x16x32_f16 v[20:23], v[132:135], v[160:163], v[20:23]\n" \
"ds_read_b64_tr_b16 v[136:137], v241 offset:16384\n" \
"ds_read_b64_tr_b16 v[138:139], v241 offset:24576\n" \
"s_waitcnt lgkmcnt(3)\n" \
"v_mfma_f32_16x16x16_bf16 v[188:191], v[188:189], v[192:193], 0\n" \
"v_mfma_f32_16x16x32_f16 v[24:27], v[120:123], v[168:171], v[24:27]\n" \
"ds_read_b128 v[160:163], v240 offset:26624\n" \
"s_add_i32 s84, s53, 0x8006000\n" \
"v_mfma_f32_16x16x32_f16 v[36:39], v[124:127], v[168:171], v[36:39]\n" \
"v_pk_add_f16 v168, v168, v172\n" \
"v_pk_add_f16 v169, v169, v173\n" \
"buffer_store_dword v240, v243, s[36:39], s84 offen sc1\n" \
"v_mfma_f32_16x16x32_f16 v[28:31], v[152:155], v[172:175], v[28:31]\n" \
"v_pk_add_f16 v170, v170, v174\n" \
"v_pk_add_f16 v171, v171, v175\n" \
"v_cvt_pk_f16_f32 v184, v184, v185\n" \
"ds_read_b64_tr_b16 v[140:141], v242 offset:16384\n" \
"v_mfma_f32_16x16x32_f16 v[40:43], v[156:159], v[172:175], v[40:43]\n" \
"v_cvt_pk_f16_f32 v185, v186, v187\n" \
"v_cvt_pk_f16_f32 v186, v188, v189\n" \
"ds_read_b128 v[164:167], v240 offset:27136\n" \
"v_mfma_f32_16x16x32_f16 v[32:35], v[128:131], v[168:171], v[32:35]\n" \
"v_cvt_pk_f16_f32 v187, v190, v191\n" \
"v_add_u32 v184, s91, v184\n" \
"buffer_load_dwordx4 v232, s[36:39], s80 offen offset:1024 sc1 lds\n" \
"v_mfma_f32_16x16x32_f16 v[44:47], v[132:135], v[168:171], v[44:47]\n" \
"v_add_u32 v185, s91, v185\n" \
"v_add_u32 v186, s91, v186\n" \
"ds_read_b64_tr_b16 v[142:143], v242 offset:24576\n" \
"v_mfma_f32_16x16x32_f16 v[48:51], v[120:123], v[176:179], v[48:51]\n" \
"v_add_u32 v187, s91, v187\n" \
"v_and_b32 v184, s92, v184\n" \
"ds_read_b128 v[168:171], v240 offset:28672\n" \
"v_mfma_f32_16x16x32_f16 v[60:63], v[124:127], v[176:179], v[60:63]\n" \
"s_waitcnt lgkmcnt(4)\n" \
"v_pk_add_f16 v176, v176, v180\n" \
"v_pk_add_f16 v177, v177, v181\n" \
"v_and_b32 v185, s92, v185\n" \
"v_mfma_f32_16x16x32_f16 v[52:55], v[152:155], v[180:183], v[52:55]\n" \
"v_pk_add_f16 v178, v178, v182\n" \
"v_pk_add_f16 v179, v179, v183\n" \
"v_and_b32 v186, s92, v186\n" \
"v_and_b32 v187, s92, v187\n" \
"v_mfma_f32_16x16x32_f16 v[64:67], v[156:159], v[180:183], v[64:67]\n" \
"ds_read_b128 v[172:175], v240 offset:29184\n" \
"buffer_store_dwordx4 v[184:187], v234, s[36:39], s82 offen offset:1024 sc1\n" \
"ds_read_b64_tr_b16 v[144:145], v241 offset:16896\n" \
"v_mfma_f32_16x16x32_f16 v[56:59], v[128:131], v[176:179], v[56:59]\n" \
"buffer_load_dwordx4 v232, s[36:39], s80 offen offset:2048 sc1 lds\n" \
"v_mfma_f32_16x16x32_f16 v[68:71], v[132:135], v[176:179], v[68:71]\n" \
"ds_read_b64_tr_b16 v[146:147], v241 offset:25088\n" \
"ds_read_b64_tr_b16 v[148:149], v242 offset:16896\n" \
"v_mfma_f32_16x16x32_f16 v[72:75], v[120:123], v[160:163], v[72:75]\n" \
"ds_read_b128 v[176:179], v240 offset:20480\n" \
"ds_read_b64_tr_b16 v[150:151], v242 offset:25088\n" \
"v_mfma_f32_16x16x32_f16 v[84:87], v[124:127], v[160:163], v[84:87]\n" \
"s_waitcnt lgkmcnt(6)\n" \
"v_pk_add_f16 v160, v160, v164\n" \
"v_pk_add_f16 v161, v161, v165\n" \
"v_mfma_f32_16x16x32_f16 v[76:79], v[152:155], v[164:167], v[76:79]\n" \
"v_pk_add_f16 v162, v162, v166\n" \
"v_pk_add_f16 v163, v163, v167\n" \
"v_mfma_f32_16x16x32_f16 v[88:91], v[156:159], v[164:167], v[88:91]\n" \
"ds_read_b128 v[180:183], v240 offset:20992\n" \
"v_mfma_f32_16x16x32_f16 v[80:83], v[128:131], v[160:163], v[80:83]\n" \
"buffer_load_dwordx4 v232, s[36:39], s80 offen offset:3072 sc1 lds\n" \
"v_mfma_f32_16x16x32_f16 v[92:95], v[132:135], v[160:163], v[92:95]\n" \
"v_mfma_f32_16x16x32_f16 v[96:99], v[120:123], v[168:171], v[96:99]\n" \
"s_waitcnt lgkmcnt(5)\n" \
"v_pk_add_f16 v120, v136, v144\n" \
"v_pk_add_f16 v121, v137, v145\n" \
"ds_read_b128 v[160:163], v240 offset:22528\n" \
"v_mfma_f32_16x16x32_f16 v[108:111], v[124:127], v[168:171], v[108:111]\n" \
"v_pk_add_f16 v168, v168, v172\n" \
"v_pk_add_f16 v169, v169, v173\n" \
"ds_read_b128 v[164:167], v240 offset:23040\n" \
"v_mfma_f32_16x16x32_f16 v[100:103], v[152:155], v[172:175], v[100:103]\n" \
"v_pk_add_f16 v170, v170, v174\n" \
"v_pk_add_f16 v171, v171, v175\n" \
"v_mfma_f32_16x16x32_f16 v[112:115], v[156:159], v[172:175], v[112:115]\n" \
"s_waitcnt lgkmcnt(3)\n" \
"v_pk_add_f16 v122, v138, v146\n" \
"v_pk_add_f16 v123, v139, v147\n" \
"v_mfma_f32_16x16x32_f16 v[104:107], v[128:131], v[168:171], v[104:107]\n" \
"v_pk_add_f16 v124, v140, v148\n" \
"v_pk_add_f16 v125, v141, v149\n" \
"v_mfma_f32_16x16x32_f16 v[116:119], v[132:135], v[168:171], v[116:119]\n" \
"v_pk_add_f16 v126, v142, v150\n" \
"v_pk_add_f16 v127, v143, v151\n" \
"s_waitcnt vmcnt(6) lgkmcnt(0)\n" \
"s_barrier\n" \
"v_mfma_f32_16x16x32_f16 v[0:3], v[136:139], v[176:179], v[0:3]\n" \
"ds_read_b128 v[168:171], v240 offset:45056\n" \
"ds_read_b64_tr_b16 v[184:185], v236 offset:8192\n" \
"ds_read_b64_tr_b16 v[188:189], v237 offset:8192\n" \
"v_mfma_f32_16x16x32_f16 v[12:15], v[140:143], v[176:179], v[12:15]\n" \
"v_pk_add_f16 v176, v176, v180\n" \
"v_pk_add_f16 v177, v177, v181\n" \
"v_mfma_f32_16x16x32_f16 v[4:7], v[144:147], v[180:183], v[4:7]\n" \
"v_pk_add_f16 v178, v178, v182\n" \
"v_pk_add_f16 v179, v179, v183\n" \
"buffer_store_dwordx4 v[216:219], v235, s[36:39], s65 offen sc1\n" \
"v_mfma_f32_16x16x32_f16 v[16:19], v[148:151], v[180:183], v[16:19]\n" \
"ds_read_b128 v[172:175], v240 offset:45568\n" \
"s_add_i32 m0, s44, 0x4000\n" \
"s_add_i32 s80, s46, 0x1800000\n" \
"v_mfma_f32_16x16x32_f16 v[8:11], v[120:123], v[176:179], v[8:11]\n" \
"buffer_load_dwordx4 v232, s[36:39], s80 offen sc1 lds\n" \
"s_waitcnt lgkmcnt(2)\n" \
"v_mfma_f32_16x16x16_bf16 v[184:187], v[184:185], v[192:193], 0\n" \
"v_mfma_f32_16x16x32_f16 v[20:23], v[124:127], v[176:179], v[20:23]\n" \
"ds_read_b64_tr_b16 v[128:129], v241 offset:32768\n" \
"ds_read_b64_tr_b16 v[130:131], v241 offset:40960\n" \
"s_waitcnt lgkmcnt(3)\n" \
"v_mfma_f32_16x16x16_bf16 v[188:191], v[188:189], v[192:193], 0\n" \
"v_mfma_f32_16x16x32_f16 v[24:27], v[136:139], v[160:163], v[24:27]\n" \
"ds_read_b128 v[176:179], v240 offset:47104\n" \
"v_mfma_f32_16x16x32_f16 v[36:39], v[140:143], v[160:163], v[36:39]\n" \
"v_pk_add_f16 v160, v160, v164\n" \
"v_pk_add_f16 v161, v161, v165\n" \
"ds_read_b64_tr_b16 v[132:133], v242 offset:32768\n" \
"v_mfma_f32_16x16x32_f16 v[28:31], v[144:147], v[164:167], v[28:31]\n" \
"v_pk_add_f16 v162, v162, v166\n" \
"v_pk_add_f16 v163, v163, v167\n" \
"v_cvt_pk_f16_f32 v184, v184, v185\n" \
"v_mfma_f32_16x16x32_f16 v[40:43], v[148:151], v[164:167], v[40:43]\n" \
"v_cvt_pk_f16_f32 v185, v186, v187\n" \
"v_cvt_pk_f16_f32 v186, v188, v189\n" \
"ds_read_b128 v[180:183], v240 offset:47616\n" \
"v_mfma_f32_16x16x32_f16 v[32:35], v[120:123], v[160:163], v[32:35]\n" \
"v_cvt_pk_f16_f32 v187, v190, v191\n" \
"v_add_u32 v184, s91, v184\n" \
"buffer_load_dwordx4 v232, s[36:39], s80 offen offset:1024 sc1 lds\n" \
"v_mfma_f32_16x16x32_f16 v[44:47], v[124:127], v[160:163], v[44:47]\n" \
"v_add_u32 v185, s91, v185\n" \
"v_add_u32 v186, s91, v186\n" \
"ds_read_b64_tr_b16 v[134:135], v242 offset:40960\n" \
"v_mfma_f32_16x16x32_f16 v[48:51], v[136:139], v[168:171], v[48:51]\n" \
"v_add_u32 v187, s91, v187\n" \
"v_and_b32 v184, s92, v184\n" \
"ds_read_b128 v[160:163], v240 offset:49152\n" \
"v_mfma_f32_16x16x32_f16 v[60:63], v[140:143], v[168:171], v[60:63]\n" \
"s_waitcnt lgkmcnt(4)\n" \
"v_pk_add_f16 v168, v168, v172\n" \
"v_pk_add_f16 v169, v169, v173\n" \
"v_and_b32 v185, s92, v185\n" \
"s_add_i32 s82, s51, 0x3800000\n" \
"v_mfma_f32_16x16x32_f16 v[52:55], v[144:147], v[172:175], v[52:55]\n" \
"v_pk_add_f16 v170, v170, v174\n" \
"v_pk_add_f16 v171, v171, v175\n" \
"v_and_b32 v186, s92, v186\n" \
"v_and_b32 v187, s92, v187\n" \
"v_mfma_f32_16x16x32_f16 v[64:67], v[148:151], v[172:175], v[64:67]\n" \
"ds_read_b128 v[164:167], v240 offset:49664\n" \
"buffer_store_dwordx4 v[184:187], v234, s[36:39], s82 offen sc1\n" \
"ds_read_b64_tr_b16 v[152:153], v241 offset:33280\n" \
"v_mfma_f32_16x16x32_f16 v[56:59], v[120:123], v[168:171], v[56:59]\n" \
"buffer_load_dwordx4 v232, s[36:39], s80 offen offset:2048 sc1 lds\n" \
"v_mfma_f32_16x16x32_f16 v[68:71], v[124:127], v[168:171], v[68:71]\n" \
"ds_read_b64_tr_b16 v[154:155], v241 offset:41472\n" \
"ds_read_b64_tr_b16 v[156:157], v242 offset:33280\n" \
"v_mfma_f32_16x16x32_f16 v[72:75], v[136:139], v[176:179], v[72:75]\n" \
"ds_read_b128 v[168:171], v240 offset:40960\n" \
"ds_read_b64_tr_b16 v[158:159], v242 offset:41472\n" \
"v_mfma_f32_16x16x32_f16 v[84:87], v[140:143], v[176:179], v[84:87]\n" \
"s_waitcnt lgkmcnt(6)\n" \
"v_pk_add_f16 v176, v176, v180\n" \
"v_pk_add_f16 v177, v177, v181\n" \
"v_mfma_f32_16x16x32_f16 v[76:79], v[144:147], v[180:183], v[76:79]\n" \
"v_pk_add_f16 v178, v178, v182\n" \
"v_pk_add_f16 v179, v179, v183\n" \
"v_mfma_f32_16x16x32_f16 v[88:91], v[148:151], v[180:183], v[88:91]\n" \
"ds_read_b128 v[172:175], v240 offset:41472\n" \
"v_mfma_f32_16x16x32_f16 v[80:83], v[120:123], v[176:179], v[80:83]\n" \
"buffer_load_dwordx4 v232, s[36:39], s80 offen offset:3072 sc1 lds\n" \
"v_mfma_f32_16x16x32_f16 v[92:95], v[124:127], v[176:179], v[92:95]\n" \
"v_mfma_f32_16x16x32_f16 v[96:99], v[136:139], v[160:163], v[96:99]\n" \
"s_waitcnt lgkmcnt(5)\n" \
"v_pk_add_f16 v136, v128, v152\n" \
"v_pk_add_f16 v137, v129, v153\n" \
"ds_read_b128 v[176:179], v240 offset:43008\n" \
"v_mfma_f32_16x16x32_f16 v[108:111], v[140:143], v[160:163], v[108:111]\n" \
"v_pk_add_f16 v160, v160, v164\n" \
"v_pk_add_f16 v161, v161, v165\n" \
"ds_read_b128 v[180:183], v240 offset:43520\n" \
"v_mfma_f32_16x16x32_f16 v[100:103], v[144:147], v[164:167], v[100:103]\n" \
"v_pk_add_f16 v162, v162, v166\n" \
"v_pk_add_f16 v163, v163, v167\n" \
"v_mfma_f32_16x16x32_f16 v[112:115], v[148:151], v[164:167], v[112:115]\n" \
"s_waitcnt lgkmcnt(3)\n" \
"v_pk_add_f16 v138, v130, v154\n" \
"v_pk_add_f16 v139, v131, v155\n" \
"v_mfma_f32_16x16x32_f16 v[104:107], v[120:123], v[160:163], v[104:107]\n" \
"v_pk_add_f16 v140, v132, v156\n" \
"v_pk_add_f16 v141, v133, v157\n" \
"v_mfma_f32_16x16x32_f16 v[116:119], v[124:127], v[160:163], v[116:119]\n" \
"v_pk_add_f16 v142, v134, v158\n" \
"v_pk_add_f16 v143, v135, v159\n" \
"s_waitcnt vmcnt(6) lgkmcnt(0)\n" \
"s_barrier\n" \
"v_mfma_f32_16x16x32_f16 v[0:3], v[128:131], v[168:171], v[0:3]\n" \
"ds_read_b128 v[160:163], v240 offset:4096\n" \
"ds_read_b64_tr_b16 v[184:185], v238 offset:8192\n" \
"ds_read_b64_tr_b16 v[188:189], v239 offset:8192\n" \
"v_mfma_f32_16x16x32_f16 v[12:15], v[132:135], v[168:171], v[12:15]\n" \
"v_pk_add_f16 v168, v168, v172\n" \
"v_pk_add_f16 v169, v169, v173\n" \
"v_mfma_f32_16x16x32_f16 v[4:7], v[152:155], v[172:175], v[4:7]\n" \
"v_pk_add_f16 v170, v170, v174\n" \
"v_pk_add_f16 v171, v171, v175\n" \
"v_mfma_f32_16x16x32_f16 v[16:19], v[156:159], v[172:175], v[16:19]\n" \
"ds_read_b128 v[164:167], v240 offset:4608\n" \
"s_add_i32 m0, s44, 0x8000\n" \
"s_add_i32 s80, s46, 0x1c00000\n" \
"v_mfma_f32_16x16x32_f16 v[8:11], v[136:139], v[168:171], v[8:11]\n" \
"buffer_load_dwordx4 v232, s[36:39], s80 offen sc1 lds\n" \
"s_waitcnt lgkmcnt(2)\n" \
"v_mfma_f32_16x16x16_bf16 v[184:187], v[184:185], v[192:193], 0\n" \
"v_mfma_f32_16x16x32_f16 v[20:23], v[140:143], v[168:171], v[20:23]\n" \
"ds_read_b64_tr_b16 v[120:121], v241 offset:0\n" \
"ds_read_b64_tr_b16 v[122:123], v241 offset:8192\n" \
"s_waitcnt lgkmcnt(3)\n" \
"v_mfma_f32_16x16x16_bf16 v[188:191], v[188:189], v[192:193], 0\n" \
"v_mfma_f32_16x16x32_f16 v[24:27], v[128:131], v[176:179], v[24:27]\n" \
"ds_read_b128 v[168:171], v240 offset:6144\n" \
"s_add_i32 s84, s54, 0x8008000\n" \
"v_mfma_f32_16x16x32_f16 v[36:39], v[132:135], v[176:179], v[36:39]\n" \
"v_pk_add_f16 v176, v176, v180\n" \
"v_pk_add_f16 v177, v177, v181\n" \
"buffer_store_dword v240, v243, s[36:39], s84 offen sc1\n" \
"v_mfma_f32_16x16x32_f16 v[28:31], v[152:155], v[180:183], v[28:31]\n" \
"v_pk_add_f16 v178, v178, v182\n" \
"v_pk_add_f16 v179, v179, v183\n" \
"v_cvt_pk_f16_f32 v184, v184, v185\n" \
"ds_read_b64_tr_b16 v[124:125], v242 offset:0\n" \
"v_mfma_f32_16x16x32_f16 v[40:43], v[156:159], v[180:183], v[40:43]\n" \
"v_cvt_pk_f16_f32 v185, v186, v187\n" \
"v_cvt_pk_f16_f32 v186, v188, v189\n" \
"ds_read_b128 v[172:175], v240 offset:6656\n" \
"v_mfma_f32_16x16x32_f16 v[32:35], v[136:139], v[176:179], v[32:35]\n" \
"v_cvt_pk_f16_f32 v187, v190, v191\n" \
"v_add_u32 v184, s91, v184\n" \
"buffer_load_dwordx4 v232, s[36:39], s80 offen offset:1024 sc1 lds\n" \
"v_mfma_f32_16x16x32_f16 v[44:47], v[140:143], v[176:179], v[44:47]\n" \
"v_add_u32 v185, s91, v185\n" \
"v_add_u32 v186, s91, v186\n" \
"ds_read_b64_tr_b16 v[126:127], v242 offset:8192\n" \
"v_mfma_f32_16x16x32_f16 v[48:51], v[128:131], v[160:163], v[48:51]\n" \
"v_add_u32 v187, s91, v187\n" \
"v_and_b32 v184, s92, v184\n" \
"ds_read_b128 v[176:179], v240 offset:8192\n" \
"v_mfma_f32_16x16x32_f16 v[60:63], v[132:135], v[160:163], v[60:63]\n" \
"s_waitcnt lgkmcnt(4)\n" \
"v_pk_add_f16 v160, v160, v164\n" \
"v_pk_add_f16 v161, v161, v165\n" \
"v_and_b32 v185, s92, v185\n" \
"v_mfma_f32_16x16x32_f16 v[52:55], v[152:155], v[164:167], v[52:55]\n" \
"v_pk_add_f16 v162, v162, v166\n" \
"v_pk_add_f16 v163, v163, v167\n" \
"v_and_b32 v186, s92, v186\n" \
"v_and_b32 v187, s92, v187\n" \
"v_mfma_f32_16x16x32_f16 v[64:67], v[156:159], v[164:167], v[64:67]\n" \
"ds_read_b128 v[180:183], v240 offset:8704\n" \
"buffer_store_dwordx4 v[184:187], v234, s[36:39], s82 offen offset:1024 sc1\n" \
"ds_read_b64_tr_b16 v[144:145], v241 offset:512\n" \
"v_mfma_f32_16x16x32_f16 v[56:59], v[136:139], v[160:163], v[56:59]\n" \
"buffer_load_dwordx4 v232, s[36:39], s80 offen offset:2048 sc1 lds\n" \
"v_mfma_f32_16x16x32_f16 v[68:71], v[140:143], v[160:163], v[68:71]\n" \
"ds_read_b64_tr_b16 v[146:147], v241 offset:8704\n" \
"ds_read_b64_tr_b16 v[148:149], v242 offset:512\n" \
"v_mfma_f32_16x16x32_f16 v[72:75], v[128:131], v[168:171], v[72:75]\n" \
"ds_read_b128 v[160:163], v240 offset:0\n" \
"ds_read_b64_tr_b16 v[150:151], v242 offset:8704\n" \
"v_mfma_f32_16x16x32_f16 v[84:87], v[132:135], v[168:171], v[84:87]\n" \
"s_waitcnt lgkmcnt(6)\n" \
"v_pk_add_f16 v168, v168, v172\n" \
"v_pk_add_f16 v169, v169, v173\n" \
"v_mfma_f32_16x16x32_f16 v[76:79], v[152:155], v[172:175], v[76:79]\n" \
"v_pk_add_f16 v170, v170, v174\n" \
"v_pk_add_f16 v171, v171, v175\n" \
"v_mfma_f32_16x16x32_f16 v[88:91], v[156:159], v[172:175], v[88:91]\n" \
"ds_read_b128 v[164:167], v240 offset:512\n" \
"v_mfma_f32_16x16x32_f16 v[80:83], v[136:139], v[168:171], v[80:83]\n" \
"buffer_load_dwordx4 v232, s[36:39], s80 offen offset:3072 sc1 lds\n" \
"v_mfma_f32_16x16x32_f16 v[92:95], v[140:143], v[168:171], v[92:95]\n" \
"v_mfma_f32_16x16x32_f16 v[96:99], v[128:131], v[176:179], v[96:99]\n" \
"s_waitcnt lgkmcnt(5)\n" \
"v_pk_add_f16 v128, v120, v144\n" \
"v_pk_add_f16 v129, v121, v145\n" \
"ds_read_b128 v[168:171], v240 offset:2048\n" \
"v_mfma_f32_16x16x32_f16 v[108:111], v[132:135], v[176:179], v[108:111]\n" \
"v_pk_add_f16 v176, v176, v180\n" \
"v_pk_add_f16 v177, v177, v181\n" \
"ds_read_b128 v[172:175], v240 offset:2560\n" \
"v_mfma_f32_16x16x32_f16 v[100:103], v[152:155], v[180:183], v[100:103]\n" \
"v_pk_add_f16 v178, v178, v182\n" \
"v_pk_add_f16 v179, v179, v183\n" \
"v_mfma_f32_16x16x32_f16 v[112:115], v[156:159], v[180:183], v[112:115]\n" \
"s_waitcnt lgkmcnt(3)\n" \
"v_pk_add_f16 v130, v122, v146\n" \
"v_pk_add_f16 v131, v123, v147\n" \
"v_mfma_f32_16x16x32_f16 v[104:107], v[136:139], v[176:179], v[104:107]\n" \
"v_pk_add_f16 v132, v124, v148\n" \
"v_pk_add_f16 v133, v125, v149\n" \
"v_mfma_f32_16x16x32_f16 v[116:119], v[140:143], v[176:179], v[116:119]\n" \
"v_pk_add_f16 v134, v126, v150\n" \
"v_pk_add_f16 v135, v127, v151\n" \
"s_waitcnt vmcnt(6) lgkmcnt(0)\n" \
"s_barrier\n" \
"v_mfma_f32_16x16x32_f16 v[0:3], v[120:123], v[160:163], v[0:3]\n" \
"ds_read_b128 v[176:179], v240 offset:24576\n" \
"s_mov_b32 s81, 0x8006000\n" \
"ds_read_b64_tr_b16 v[184:185], v236 offset:12288\n" \
"v_mfma_f32_16x16x32_f16 v[12:15], v[124:127], v[160:163], v[12:15]\n" \
"v_pk_add_f16 v160, v160, v164\n" \
"v_pk_add_f16 v161, v161, v165\n" \
"buffer_load_dword v253, v252, s[36:39], s81 offen sc1\n" \
"v_mfma_f32_16x16x32_f16 v[4:7], v[144:147], v[164:167], v[4:7]\n" \
"v_pk_add_f16 v162, v162, v166\n" \
"v_pk_add_f16 v163, v163, v167\n" \
"ds_read_b64_tr_b16 v[188:189], v237 offset:12288\n" \
"v_mfma_f32_16x16x32_f16 v[16:19], v[148:151], v[164:167], v[16:19]\n" \
"ds_read_b128 v[180:183], v240 offset:25088\n" \
"s_add_i32 m0, s44, 0x0\n" \
"s_add_i32 s80, s46, 0x2000000\n" \
"v_mfma_f32_16x16x32_f16 v[8:11], v[128:131], v[160:163], v[8:11]\n" \
"buffer_load_dwordx4 v232, s[36:39], s80 offen sc1 lds\n" \
"s_waitcnt lgkmcnt(2)\n" \
"v_mfma_f32_16x16x16_bf16 v[184:187], v[184:185], v[192:193], 0\n" \
"v_mfma_f32_16x16x32_f16 v[20:23], v[132:135], v[160:163], v[20:23]\n" \
"ds_read_b64_tr_b16 v[136:137], v241 offset:16384\n" \
"ds_read_b64_tr_b16 v[138:139], v241 offset:24576\n" \
"s_waitcnt lgkmcnt(3)\n" \
"v_mfma_f32_16x16x16_bf16 v[188:191], v[188:189], v[192:193], 0\n" \
"v_mfma_f32_16x16x32_f16 v[24:27], v[120:123], v[168:171], v[24:27]\n" \
"ds_read_b128 v[160:163], v240 offset:26624\n" \
"v_mfma_f32_16x16x32_f16 v[36:39], v[124:127], v[168:171], v[36:39]\n" \
"v_pk_add_f16 v168, v168, v172\n" \
"v_pk_add_f16 v169, v169, v173\n" \
"ds_read_b64_tr_b16 v[140:141], v242 offset:16384\n" \
"v_mfma_f32_16x16x32_f16 v[28:31], v[144:147], v[172:175], v[28:31]\n" \
"v_pk_add_f16 v170, v170, v174\n" \
"v_pk_add_f16 v171, v171, v175\n" \
"v_cvt_pk_f16_f32 v184, v184, v185\n" \
"v_mfma_f32_16x16x32_f16 v[40:43], v[148:151], v[172:175], v[40:43]\n" \
"v_cvt_pk_f16_f32 v185, v186, v187\n" \
"v_cvt_pk_f16_f32 v186, v188, v189\n" \
"ds_read_b128 v[164:167], v240 offset:27136\n" \
"v_mfma_f32_16x16x32_f16 v[32:35], v[128:131], v[168:171], v[32:35]\n" \
"v_cvt_pk_f16_f32 v187, v190, v191\n" \
"v_add_u32 v184, s91, v184\n" \
"buffer_load_dwordx4 v232, s[36:39], s80 offen offset:1024 sc1 lds\n" \
"v_mfma_f32_16x16x32_f16 v[44:47], v[132:135], v[168:171], v[44:47]\n" \
"v_add_u32 v185, s91, v185\n" \
"v_add_u32 v186, s91, v186\n" \
"ds_read_b64_tr_b16 v[142:143], v242 offset:24576\n" \
"v_mfma_f32_16x16x32_f16 v[48:51], v[120:123], v[176:179], v[48:51]\n" \
"v_add_u32 v187, s91, v187\n" \
"v_and_b32 v184, s92, v184\n" \
"ds_read_b128 v[168:171], v240 offset:28672\n" \
"v_mfma_f32_16x16x32_f16 v[60:63], v[124:127], v[176:179], v[60:63]\n" \
"s_waitcnt lgkmcnt(4)\n" \
"v_pk_add_f16 v176, v176, v180\n" \
"v_pk_add_f16 v177, v177, v181\n" \
"v_and_b32 v185, s92, v185\n" \
"s_add_i32 s82, s48, 0x4000000\n" \
"v_mfma_f32_16x16x32_f16 v[52:55], v[144:147], v[180:183], v[52:55]\n" \
"v_pk_add_f16 v178, v178, v182\n" \
"v_pk_add_f16 v179, v179, v183\n" \
"v_and_b32 v186, s92, v186\n" \
"v_and_b32 v187, s92, v187\n" \
"v_mfma_f32_16x16x32_f16 v[64:67], v[148:151], v[180:183], v[64:67]\n" \
"ds_read_b128 v[172:175], v240 offset:29184\n" \
"buffer_store_dwordx4 v[184:187], v234, s[36:39], s82 offen sc1\n" \
"ds_read_b64_tr_b16 v[152:153], v241 offset:16896\n" \
"v_mfma_f32_16x16x32_f16 v[56:59], v[128:131], v[176:179], v[56:59]\n" \
"buffer_load_dwordx4 v232, s[36:39], s80 offen offset:2048 sc1 lds\n" \
"v_mfma_f32_16x16x32_f16 v[68:71], v[132:135], v[176:179], v[68:71]\n" \
"ds_read_b64_tr_b16 v[154:155], v241 offset:25088\n" \
"ds_read_b64_tr_b16 v[156:157], v242 offset:16896\n" \
"v_mfma_f32_16x16x32_f16 v[72:75], v[120:123], v[160:163], v[72:75]\n" \
"ds_read_b128 v[176:179], v240 offset:20480\n" \
"ds_read_b64_tr_b16 v[158:159], v242 offset:25088\n" \
"v_mfma_f32_16x16x32_f16 v[84:87], v[124:127], v[160:163], v[84:87]\n" \
"s_waitcnt lgkmcnt(6)\n" \
"v_pk_add_f16 v160, v160, v164\n" \
"v_pk_add_f16 v161, v161, v165\n" \
"v_mfma_f32_16x16x32_f16 v[76:79], v[144:147], v[164:167], v[76:79]\n" \
"v_pk_add_f16 v162, v162, v166\n" \
"v_pk_add_f16 v163, v163, v167\n" \
"v_mfma_f32_16x16x32_f16 v[88:91], v[148:151], v[164:167], v[88:91]\n" \
"ds_read_b128 v[180:183], v240 offset:20992\n" \
"v_mfma_f32_16x16x32_f16 v[80:83], v[128:131], v[160:163], v[80:83]\n" \
"buffer_load_dwordx4 v232, s[36:39], s80 offen offset:3072 sc1 lds\n" \
"v_mfma_f32_16x16x32_f16 v[92:95], v[132:135], v[160:163], v[92:95]\n" \
"v_mfma_f32_16x16x32_f16 v[96:99], v[120:123], v[168:171], v[96:99]\n" \
"s_waitcnt lgkmcnt(5)\n" \
"v_pk_add_f16 v120, v136, v152\n" \
"v_pk_add_f16 v121, v137, v153\n" \
"ds_read_b128 v[160:163], v240 offset:22528\n" \
"v_mfma_f32_16x16x32_f16 v[108:111], v[124:127], v[168:171], v[108:111]\n" \
"v_pk_add_f16 v168, v168, v172\n" \
"v_pk_add_f16 v169, v169, v173\n" \
"ds_read_b128 v[164:167], v240 offset:23040\n" \
"v_mfma_f32_16x16x32_f16 v[100:103], v[144:147], v[172:175], v[100:103]\n" \
"v_pk_add_f16 v170, v170, v174\n" \
"v_pk_add_f16 v171, v171, v175\n" \
"v_mfma_f32_16x16x32_f16 v[112:115], v[148:151], v[172:175], v[112:115]\n" \
"s_waitcnt lgkmcnt(3)\n" \
"v_pk_add_f16 v122, v138, v154\n" \
"v_pk_add_f16 v123, v139, v155\n" \
"v_mfma_f32_16x16x32_f16 v[104:107], v[128:131], v[168:171], v[104:107]\n" \
"v_pk_add_f16 v124, v140, v156\n" \
"v_pk_add_f16 v125, v141, v157\n" \
"v_mfma_f32_16x16x32_f16 v[116:119], v[132:135], v[168:171], v[116:119]\n" \
"v_pk_add_f16 v126, v142, v158\n" \
"v_pk_add_f16 v127, v143, v159\n" \
"s_waitcnt vmcnt(5) lgkmcnt(0)\n" \
"s_barrier\n" \
"v_mfma_f32_16x16x32_f16 v[0:3], v[136:139], v[176:179], v[0:3]\n" \
"ds_read_b128 v[168:171], v240 offset:45056\n" \
"ds_read_b64_tr_b16 v[184:185], v238 offset:12288\n" \
"ds_read_b64_tr_b16 v[188:189], v239 offset:12288\n" \
"v_mfma_f32_16x16x32_f16 v[12:15], v[140:143], v[176:179], v[12:15]\n" \
"v_pk_add_f16 v176, v176, v180\n" \
"v_pk_add_f16 v177, v177, v181\n" \
"v_mfma_f32_16x16x32_f16 v[4:7], v[152:155], v[180:183], v[4:7]\n" \
"v_pk_add_f16 v178, v178, v182\n" \
"v_pk_add_f16 v179, v179, v183\n" \
"v_cmp_eq_u32 vcc, 0, v253\n" \
"s_cbranch_vccnz .Ls03_%=\n" \
".Lr03_%=:\n" \
"buffer_store_dwordx4 v[220:223], v235, s[36:39], s66 offen sc1\n" \
"v_mfma_f32_16x16x32_f16 v[16:19], v[156:159], v[180:183], v[16:19]\n" \
"ds_read_b128 v[172:175], v240 offset:45568\n" \
"s_add_i32 m0, s44, 0x4000\n" \
"s_add_i32 s80, s46, 0x2400000\n" \
"v_mfma_f32_16x16x32_f16 v[8:11], v[120:123], v[176:179], v[8:11]\n" \
"buffer_load_dwordx4 v232, s[36:39], s80 offen sc1 lds\n" \
"s_waitcnt lgkmcnt(2)\n" \
"v_mfma_f32_16x16x16_bf16 v[184:187], v[184:185], v[192:193], 0\n" \
"v_mfma_f32_16x16x32_f16 v[20:23], v[124:127], v[176:179], v[20:23]\n" \
"ds_read_b64_tr_b16 v[128:129], v241 offset:32768\n" \
"ds_read_b64_tr_b16 v[130:131], v241 offset:40960\n" \
"s_waitcnt lgkmcnt(3)\n" \
"v_mfma_f32_16x16x16_bf16 v[188:191], v[188:189], v[192:193], 0\n" \
"v_mfma_f32_16x16x32_f16 v[24:27], v[136:139], v[160:163], v[24:27]\n" \
"ds_read_b128 v[176:179], v240 offset:47104\n" \
"s_add_i32 s84, s55, 0x800a000\n" \
"v_mfma_f32_16x16x32_f16 v[36:39], v[140:143], v[160:163], v[36:39]\n" \
"v_pk_add_f16 v160, v160, v164\n" \
"v_pk_add_f16 v161, v161, v165\n" \
"buffer_store_dword v240, v243, s[36:39], s84 offen sc1\n" \
"v_mfma_f32_16x16x32_f16 v[28:31], v[152:155], v[164:167], v[28:31]\n" \
"v_pk_add_f16 v162, v162, v166\n" \
"v_pk_add_f16 v163, v163, v167\n" \
"v_cvt_pk_f16_f32 v184, v184, v185\n" \
"ds_read_b64_tr_b16 v[132:133], v242 offset:32768\n" \
"v_mfma_f32_16x16x32_f16 v[40:43], v[156:159], v[164:167], v[40:43]\n" \
"v_cvt_pk_f16_f32 v185, v186, v187\n" \
"v_cvt_pk_f16_f32 v186, v188, v189\n" \
"ds_read_b128 v[180:183], v240 offset:47616\n" \
"v_mfma_f32_16x16x32_f16 v[32:35], v[120:123], v[160:163], v[32:35]\n" \
"v_cvt_pk_f16_f32 v187, v190, v191\n" \
"v_add_u32 v184, s91, v184\n" \
"buffer_load_dwordx4 v232, s[36:39], s80 offen offset:1024 sc1 lds\n" \
"v_mfma_f32_16x16x32_f16 v[44:47], v[124:127], v[160:163], v[44:47]\n" \
"v_add_u32 v185, s91, v185\n" \
"v_add_u32 v186, s91, v186\n" \
"ds_read_b64_tr_b16 v[134:135], v242 offset:40960\n" \
"v_mfma_f32_16x16x32_f16 v[48:51], v[136:139], v[168:171], v[48:51]\n" \
"v_add_u32 v187, s91, v187\n" \
"v_and_b32 v184, s92, v184\n" \
"ds_read_b128 v[160:163], v240 offset:49152\n" \
"v_mfma_f32_16x16x32_f16 v[60:63], v[140:143], v[168:171], v[60:63]\n" \
"s_waitcnt lgkmcnt(4)\n" \
"v_pk_add_f16 v168, v168, v172\n" \
"v_pk_add_f16 v169, v169, v173\n" \
"v_and_b32 v185, s92, v185\n" \
"v_mfma_f32_16x16x32_f16 v[52:55], v[152:155], v[172:175], v[52:55]\n" \
"v_pk_add_f16 v170, v170, v174\n" \
"v_pk_add_f16 v171, v171, v175\n" \
"v_and_b32 v186, s92, v186\n" \
"v_and_b32 v187, s92, v187\n" \
"v_mfma_f32_16x16x32_f16 v[64:67], v[156:159], v[172:175], v[64:67]\n" \
"ds_read_b128 v[164:167], v240 offset:49664\n" \
"buffer_store_dwordx4 v[184:187], v234, s[36:39], s82 offen offset:1024 sc1\n" \
"ds_read_b64_tr_b16 v[144:145], v241 offset:33280\n" \
"v_mfma_f32_16x16x32_f16 v[56:59], v[120:123], v[168:171], v[56:59]\n" \
"buffer_load_dwordx4 v232, s[36:39], s80 offen offset:2048 sc1 lds\n" \
"v_mfma_f32_16x16x32_f16 v[68:71], v[124:127], v[168:171], v[68:71]\n" \
"ds_read_b64_tr_b16 v[146:147], v241 offset:41472\n" \
"ds_read_b64_tr_b16 v[148:149], v242 offset:33280\n" \
"v_mfma_f32_16x16x32_f16 v[72:75], v[136:139], v[176:179], v[72:75]\n" \
"ds_read_b128 v[168:171], v240 offset:40960\n" \
"ds_read_b64_tr_b16 v[150:151], v242 offset:41472\n" \
"v_mfma_f32_16x16x32_f16 v[84:87], v[140:143], v[176:179], v[84:87]\n" \
"s_waitcnt lgkmcnt(6)\n" \
"v_pk_add_f16 v176, v176, v180\n" \
"v_pk_add_f16 v177, v177, v181\n" \
"v_mfma_f32_16x16x32_f16 v[76:79], v[152:155], v[180:183], v[76:79]\n" \
"v_pk_add_f16 v178, v178, v182\n" \
"v_pk_add_f16 v179, v179, v183\n" \
"v_mfma_f32_16x16x32_f16 v[88:91], v[156:159], v[180:183], v[88:91]\n" \
"ds_read_b128 v[172:175], v240 offset:41472\n" \
"v_mfma_f32_16x16x32_f16 v[80:83], v[120:123], v[176:179], v[80:83]\n" \
"buffer_load_dwordx4 v232, s[36:39], s80 offen offset:3072 sc1 lds\n" \
"v_mfma_f32_16x16x32_f16 v[92:95], v[124:127], v[176:179], v[92:95]\n" \
"v_mfma_f32_16x16x32_f16 v[96:99], v[136:139], v[160:163], v[96:99]\n" \
"s_waitcnt lgkmcnt(5)\n" \
"v_pk_add_f16 v136, v128, v144\n" \
"v_pk_add_f16 v137, v129, v145\n" \
"ds_read_b128 v[176:179], v240 offset:43008\n" \
"v_mfma_f32_16x16x32_f16 v[108:111], v[140:143], v[160:163], v[108:111]\n" \
"v_pk_add_f16 v160, v160, v164\n" \
"v_pk_add_f16 v161, v161, v165\n" \
"ds_read_b128 v[180:183], v240 offset:43520\n" \
"v_mfma_f32_16x16x32_f16 v[100:103], v[152:155], v[164:167], v[100:103]\n" \
"v_pk_add_f16 v162, v162, v166\n" \
"v_pk_add_f16 v163, v163, v167\n" \
"v_mfma_f32_16x16x32_f16 v[112:115], v[156:159], v[164:167], v[112:115]\n" \
"s_waitcnt lgkmcnt(3)\n" \
"v_pk_add_f16 v138, v130, v146\n" \
"v_pk_add_f16 v139, v131, v147\n" \
"v_mfma_f32_16x16x32_f16 v[104:107], v[120:123], v[160:163], v[104:107]\n" \
"v_pk_add_f16 v140, v132, v148\n" \
"v_pk_add_f16 v141, v133, v149\n" \
"v_mfma_f32_16x16x32_f16 v[116:119], v[124:127], v[160:163], v[116:119]\n" \
"v_pk_add_f16 v142, v134, v150\n" \
"v_pk_add_f16 v143, v135, v151\n" \
"s_waitcnt vmcnt(7) lgkmcnt(0)\n" \
"s_barrier\n" \
"v_mfma_f32_16x16x32_f16 v[0:3], v[128:131], v[168:171], v[0:3]\n" \
"ds_read_b128 v[160:163], v240 offset:4096\n" \
"s_mov_b32 s81, 0x8008000\n" \
"ds_read_b64_tr_b16 v[184:185], v236 offset:16384\n" \
"v_mfma_f32_16x16x32_f16 v[12:15], v[132:135], v[168:171], v[12:15]\n" \
"v_pk_add_f16 v168, v168, v172\n" \
"v_pk_add_f16 v169, v169, v173\n" \
"buffer_load_dword v253, v252, s[36:39], s81 offen sc1\n" \
"v_mfma_f32_16x16x32_f16 v[4:7], v[144:147], v[172:175], v[4:7]\n" \
"v_pk_add_f16 v170, v170, v174\n" \
"v_pk_add_f16 v171, v171, v175\n" \
"ds_read_b64_tr_b16 v[188:189], v237 offset:16384\n" \
"v_mfma_f32_16x16x32_f16 v[16:19], v[148:151], v[172:175], v[16:19]\n" \
"ds_read_b128 v[164:167], v240 offset:4608\n" \
"s_add_i32 m0, s44, 0x8000\n" \
"s_add_i32 s80, s46, 0x2800000\n" \
"v_mfma_f32_16x16x32_f16 v[8:11], v[136:139], v[168:171], v[8:11]\n" \
"buffer_load_dwordx4 v232, s[36:39], s80 offen sc1 lds\n" \
"s_waitcnt lgkmcnt(2)\n" \
"v_mfma_f32_16x16x16_bf16 v[184:187], v[184:185], v[192:193], 0\n" \
"v_mfma_f32_16x16x32_f16 v[20:23], v[140:143], v[168:171], v[20:23]\n" \
"ds_read_b64_tr_b16 v[120:121], v241 offset:0\n" \
"ds_read_b64_tr_b16 v[122:123], v241 offset:8192\n" \
"s_waitcnt lgkmcnt(3)\n" \
"v_mfma_f32_16x16x16_bf16 v[188:191], v[188:189], v[192:193], 0\n" \
"v_mfma_f32_16x16x32_f16 v[24:27], v[128:131], v[176:179], v[24:27]\n" \
"ds_read_b128 v[168:171], v240 offset:6144\n" \
"v_mfma_f32_16x16x32_f16 v[36:39], v[132:135], v[176:179], v[36:39]\n" \
"v_pk_add_f16 v176, v176, v180\n" \
"v_pk_add_f16 v177, v177, v181\n" \
"ds_read_b64_tr_b16 v[124:125], v242 offset:0\n" \
"v_mfma_f32_16x16x32_f16 v[28:31], v[144:147], v[180:183], v[28:31]\n" \
"v_pk_add_f16 v178, v178, v182\n" \
"v_pk_add_f16 v179, v179, v183\n" \
"v_cvt_pk_f16_f32 v184, v184, v185\n" \
"v_mfma_f32_16x16x32_f16 v[40:43], v[148:151], v[180:183], v[40:43]\n" \
"v_cvt_pk_f16_f32 v185, v186, v187\n" \
"v_cvt_pk_f16_f32 v186, v188, v189\n" \
"ds_read_b128 v[172:175], v240 offset:6656\n" \
"v_mfma_f32_16x16x32_f16 v[32:35], v[136:139], v[176:179], v[32:35]\n" \
"v_cvt_pk_f16_f32 v187, v190, v191\n" \
"v_add_u32 v184, s91, v184\n" \
"buffer_load_dwordx4 v232, s[36:39], s80 offen offset:1024 sc1 lds\n" \
"v_mfma_f32_16x16x32_f16 v[44:47], v[140:143], v[176:179], v[44:47]\n" \
"v_add_u32 v185, s91, v185\n" \
"v_add_u32 v186, s91, v186\n" \
"ds_read_b64_tr_b16 v[126:127], v242 offset:8192\n" \
"v_mfma_f32_16x16x32_f16 v[48:51], v[128:131], v[160:163], v[48:51]\n" \
"v_add_u32 v187, s91, v187\n" \
"v_and_b32 v184, s92, v184\n" \
"ds_read_b128 v[176:179], v240 offset:8192\n" \
"v_mfma_f32_16x16x32_f16 v[60:63], v[132:135], v[160:163], v[60:63]\n" \
"s_waitcnt lgkmcnt(4)\n" \
"v_pk_add_f16 v160, v160, v164\n" \
"v_pk_add_f16 v161, v161, v165\n" \
"v_and_b32 v185, s92, v185\n" \
"s_add_i32 s82, s49, 0x4800000\n" \
"v_mfma_f32_16x16x32_f16 v[52:55], v[144:147], v[164:167], v[52:55]\n" \
"v_pk_add_f16 v162, v162, v166\n" \
"v_pk_add_f16 v163, v163, v167\n" \
"v_and_b32 v186, s92, v186\n" \
"v_and_b32 v187, s92, v187\n" \
"v_mfma_f32_16x16x32_f16 v[64:67], v[148:151], v[164:167], v[64:67]\n" \
"ds_read_b128 v[180:183], v240 offset:8704\n" \
"buffer_store_dwordx4 v[184:187], v234, s[36:39], s82 offen sc1\n" \
"ds_read_b64_tr_b16 v[152:153], v241 offset:512\n" \
"v_mfma_f32_16x16x32_f16 v[56:59], v[136:139], v[160:163], v[56:59]\n" \
"buffer_load_dwordx4 v232, s[36:39], s80 offen offset:2048 sc1 lds\n" \
"v_mfma_f32_16x16x32_f16 v[68:71], v[140:143], v[160:163], v[68:71]\n" \
"ds_read_b64_tr_b16 v[154:155], v241 offset:8704\n" \
"ds_read_b64_tr_b16 v[156:157], v242 offset:512\n" \
"v_mfma_f32_16x16x32_f16 v[72:75], v[128:131], v[168:171], v[72:75]\n" \
"ds_read_b128 v[160:163], v240 offset:0\n" \
"ds_read_b64_tr_b16 v[158:159], v242 offset:8704\n" \
"v_mfma_f32_16x16x32_f16 v[84:87], v[132:135], v[168:171], v[84:87]\n" \
"s_waitcnt lgkmcnt(6)\n" \
"v_pk_add_f16 v168, v168, v172\n" \
"v_pk_add_f16 v169, v169, v173\n" \
"v_mfma_f32_16x16x32_f16 v[76:79], v[144:147], v[172:175], v[76:79]\n" \
"v_pk_add_f16 v170, v170, v174\n" \
"v_pk_add_f16 v171, v171, v175\n" \
"v_mfma_f32_16x16x32_f16 v[88:91], v[148:151], v[172:175], v[88:91]\n" \
"ds_read_b128 v[164:167], v240 offset:512\n" \
"v_mfma_f32_16x16x32_f16 v[80:83], v[136:139], v[168:171], v[80:83]\n" \
"buffer_load_dwordx4 v232, s[36:39], s80 offen offset:3072 sc1 lds\n" \
"v_mfma_f32_16x16x32_f16 v[92:95], v[140:143], v[168:171], v[92:95]\n" \
"v_mfma_f32_16x16x32_f16 v[96:99], v[128:131], v[176:179], v[96:99]\n" \
"s_waitcnt lgkmcnt(5)\n" \
"v_pk_add_f16 v128, v120, v152\n" \
"v_pk_add_f16 v129, v121, v153\n" \
"ds_read_b128 v[168:171], v240 offset:2048\n" \
"v_mfma_f32_16x16x32_f16 v[108:111], v[132:135], v[176:179], v[108:111]\n" \
"v_pk_add_f16 v176, v176, v180\n" \
"v_pk_add_f16 v177, v177, v181\n" \
"ds_read_b128 v[172:175], v240 offset:2560\n" \
"v_mfma_f32_16x16x32_f16 v[100:103], v[144:147], v[180:183], v[100:103]\n" \
"v_pk_add_f16 v178, v178, v182\n" \
"v_pk_add_f16 v179, v179, v183\n" \
"v_mfma_f32_16x16x32_f16 v[112:115], v[148:151], v[180:183], v[112:115]\n" \
"s_waitcnt lgkmcnt(3)\n" \
"v_pk_add_f16 v130, v122, v154\n" \
"v_pk_add_f16 v131, v123, v155\n" \
"v_mfma_f32_16x16x32_f16 v[104:107], v[136:139], v[176:179], v[104:107]\n" \
"v_pk_add_f16 v132, v124, v156\n" \
"v_pk_add_f16 v133, v125, v157\n" \
"v_mfma_f32_16x16x32_f16 v[116:119], v[140:143], v[176:179], v[116:119]\n" \
"v_pk_add_f16 v134, v126, v158\n" \
"v_pk_add_f16 v135, v127, v159\n" \
"s_waitcnt vmcnt(5) lgkmcnt(0)\n" \
"s_barrier\n" \
"v_mfma_f32_16x16x32_f16 v[0:3], v[120:123], v[160:163], v[0:3]\n" \
"ds_read_b128 v[176:179], v240 offset:24576\n" \
"ds_read_b64_tr_b16 v[184:185], v238 offset:16384\n" \
"ds_read_b64_tr_b16 v[188:189], v239 offset:16384\n" \
"v_mfma_f32_16x16x32_f16 v[12:15], v[124:127], v[160:163], v[12:15]\n" \
"v_pk_add_f16 v160, v160, v164\n" \
"v_pk_add_f16 v161, v161, v165\n" \
"v_mfma_f32_16x16x32_f16 v[4:7], v[152:155], v[164:167], v[4:7]\n" \
"v_pk_add_f16 v162, v162, v166\n" \
"v_pk_add_f16 v163, v163, v167\n" \
"v_cmp_eq_u32 vcc, 0, v253\n" \
"s_cbranch_vccnz .Ls04_%=\n" \
".Lr04_%=:\n" \
"v_mfma_f32_16x16x32_f16 v[16:19], v[156:159], v[164:167], v[16:19]\n" \
"ds_read_b128 v[180:183], v240 offset:25088\n" \
"s_add_i32 m0, s44, 0x0\n" \
"s_add_i32 s80, s46, 0x2c00000\n" \
"v_mfma_f32_16x16x32_f16 v[8:11], v[128:131], v[160:163], v[8:11]\n" \
"buffer_load_dwordx4 v232, s[36:39], s80 offen sc1 lds\n" \
"s_waitcnt lgkmcnt(2)\n" \
"v_mfma_f32_16x16x16_bf16 v[184:187], v[184:185], v[192:193], 0\n" \
"v_mfma_f32_16x16x32_f16 v[20:23], v[132:135], v[160:163], v[20:23]\n" \
"ds_read_b64_tr_b16 v[136:137], v241 offset:16384\n" \
"ds_read_b64_tr_b16 v[138:139], v241 offset:24576\n" \
"s_waitcnt lgkmcnt(3)\n" \
"v_mfma_f32_16x16x16_bf16 v[188:191], v[188:189], v[192:193], 0\n" \
"v_mfma_f32_16x16x32_f16 v[24:27], v[120:123], v[168:171], v[24:27]\n" \
"ds_read_b128 v[160:163], v240 offset:26624\n" \
"s_add_i32 s84, s52, 0x800c000\n" \
"v_mfma_f32_16x16x32_f16 v[36:39], v[124:127], v[168:171], v[36:39]\n" \
"v_pk_add_f16 v168, v168, v172\n" \
"v_pk_add_f16 v169, v169, v173\n" \
"buffer_store_dword v240, v243, s[36:39], s84 offen sc1\n" \
"v_mfma_f32_16x16x32_f16 v[28:31], v[152:155], v[172:175], v[28:31]\n" \
"v_pk_add_f16 v170, v170, v174\n" \
"v_pk_add_f16 v171, v171, v175\n" \
"v_cvt_pk_f16_f32 v184, v184, v185\n" \
"ds_read_b64_tr_b16 v[140:141], v242 offset:16384\n" \
"v_mfma_f32_16x16x32_f16 v[40:43], v[156:159], v[172:175], v[40:43]\n" \
"v_cvt_pk_f16_f32 v185, v186, v187\n" \
"v_cvt_pk_f16_f32 v186, v188, v189\n" \
"ds_read_b128 v[164:167], v240 offset:27136\n" \
"v_mfma_f32_16x16x32_f16 v[32:35], v[128:131], v[168:171], v[32:35]\n" \
"v_cvt_pk_f16_f32 v187, v190, v191\n" \
"v_add_u32 v184, s91, v184\n" \
"buffer_load_dwordx4 v232, s[36:39], s80 offen offset:1024 sc1 lds\n" \
"v_mfma_f32_16x16x32_f16 v[44:47], v[132:135], v[168:171], v[44:47]\n" \
"v_add_u32 v185, s91, v185\n" \
"v_add_u32 v186, s91, v186\n" \
"ds_read_b64_tr_b16 v[142:143], v242 offset:24576\n" \
"v_mfma_f32_16x16x32_f16 v[48:51], v[120:123], v[176:179], v[48:51]\n" \
"v_add_u32 v187, s91, v187\n" \
"v_and_b32 v184, s92, v184\n" \
"ds_read_b128 v[168:171], v240 offset:28672\n" \
"v_mfma_f32_16x16x32_f16 v[60:63], v[124:127], v[176:179], v[60:63]\n" \
"s_waitcnt lgkmcnt(4)\n" \
"v_pk_add_f16 v176, v176, v180\n" \
"v_pk_add_f16 v177, v177, v181\n" \
"v_and_b32 v185, s92, v185\n" \
"v_mfma_f32_16x16x32_f16 v[52:55], v[152:155], v[180:183], v[52:55]\n" \
"v_pk_add_f16 v178, v178, v182\n" \
"v_pk_add_f16 v179, v179, v183\n" \
"v_and_b32 v186, s92, v186\n" \
"v_and_b32 v187, s92, v187\n" \
"v_mfma_f32_16x16x32_f16 v[64:67], v[156:159], v[180:183], v[64:67]\n" \
"ds_read_b128 v[172:175], v240 offset:29184\n" \
"buffer_store_dwordx4 v[184:187], v234, s[36:39], s82 offen offset:1024 sc1\n" \
"ds_read_b64_tr_b16 v[144:145], v241 offset:16896\n" \
"v_mfma_f32_16x16x32_f16 v[56:59], v[128:131], v[176:179], v[56:59]\n" \
"buffer_load_dwordx4 v232, s[36:39], s80 offen offset:2048 sc1 lds\n" \
"v_mfma_f32_16x16x32_f16 v[68:71], v[132:135], v[176:179], v[68:71]\n" \
"ds_read_b64_tr_b16 v[146:147], v241 offset:25088\n" \
"ds_read_b64_tr_b16 v[148:149], v242 offset:16896\n" \
"v_mfma_f32_16x16x32_f16 v[72:75], v[120:123], v[160:163], v[72:75]\n" \
"ds_read_b128 v[176:179], v240 offset:20480\n" \
"ds_read_b64_tr_b16 v[150:151], v242 offset:25088\n" \
"v_mfma_f32_16x16x32_f16 v[84:87], v[124:127], v[160:163], v[84:87]\n" \
"s_waitcnt lgkmcnt(6)\n" \
"v_pk_add_f16 v160, v160, v164\n" \
"v_pk_add_f16 v161, v161, v165\n" \
"v_mfma_f32_16x16x32_f16 v[76:79], v[152:155], v[164:167], v[76:79]\n" \
"v_pk_add_f16 v162, v162, v166\n" \
"v_pk_add_f16 v163, v163, v167\n" \
"v_mfma_f32_16x16x32_f16 v[88:91], v[156:159], v[164:167], v[88:91]\n" \
"ds_read_b128 v[180:183], v240 offset:20992\n" \
"v_mfma_f32_16x16x32_f16 v[80:83], v[128:131], v[160:163], v[80:83]\n" \
"buffer_load_dwordx4 v232, s[36:39], s80 offen offset:3072 sc1 lds\n" \
"v_mfma_f32_16x16x32_f16 v[92:95], v[132:135], v[160:163], v[92:95]\n" \
"v_mfma_f32_16x16x32_f16 v[96:99], v[120:123], v[168:171], v[96:99]\n" \
"s_waitcnt lgkmcnt(5)\n" \
"v_pk_add_f16 v120, v136, v144\n" \
"v_pk_add_f16 v121, v137, v145\n" \
"ds_read_b128 v[160:163], v240 offset:22528\n" \
"v_mfma_f32_16x16x32_f16 v[108:111], v[124:127], v[168:171], v[108:111]\n" \
"v_pk_add_f16 v168, v168, v172\n" \
"v_pk_add_f16 v169, v169, v173\n" \
"ds_read_b128 v[164:167], v240 offset:23040\n" \
"v_mfma_f32_16x16x32_f16 v[100:103], v[152:155], v[172:175], v[100:103]\n" \
"v_pk_add_f16 v170, v170, v174\n" \
"v_pk_add_f16 v171, v171, v175\n" \
"v_mfma_f32_16x16x32_f16 v[112:115], v[156:159], v[172:175], v[112:115]\n" \
"s_waitcnt lgkmcnt(3)\n" \
"v_pk_add_f16 v122, v138, v146\n" \
"v_pk_add_f16 v123, v139, v147\n" \
"v_mfma_f32_16x16x32_f16 v[104:107], v[128:131], v[168:171], v[104:107]\n" \
"v_pk_add_f16 v124, v140, v148\n" \
"v_pk_add_f16 v125, v141, v149\n" \
"v_mfma_f32_16x16x32_f16 v[116:119], v[132:135], v[168:171], v[116:119]\n" \
"v_pk_add_f16 v126, v142, v150\n" \
"v_pk_add_f16 v127, v143, v151\n" \
"s_waitcnt vmcnt(6) lgkmcnt(0)\n" \
"s_barrier\n" \
"v_mfma_f32_16x16x32_f16 v[0:3], v[136:139], v[176:179], v[0:3]\n" \
"ds_read_b128 v[168:171], v240 offset:45056\n" \
"s_mov_b32 s81, 0x800a000\n" \
"ds_read_b64_tr_b16 v[184:185], v236 offset:20480\n" \
"ds_read_b64_tr_b16 v[188:189], v237 offset:20480\n" \
"v_mfma_f32_16x16x32_f16 v[12:15], v[140:143], v[176:179], v[12:15]\n" \
"v_pk_add_f16 v176, v176, v180\n" \
"v_pk_add_f16 v177, v177, v181\n" \
"buffer_load_dword v253, v252, s[36:39], s81 offen sc1\n" \
"v_mfma_f32_16x16x32_f16 v[4:7], v[144:147], v[180:183], v[4:7]\n" \
"v_pk_add_f16 v178, v178, v182\n" \
"v_pk_add_f16 v179, v179, v183\n" \
"buffer_store_dwordx4 v[224:227], v235, s[36:39], s67 offen sc1\n" \
"v_mfma_f32_16x16x32_f16 v[16:19], v[148:151], v[180:183], v[16:19]\n" \
"ds_read_b128 v[172:175], v240 offset:45568\n" \
"s_add_i32 m0, s44, 0x4000\n" \
"s_add_i32 s80, s46, 0x3000000\n" \
"v_mfma_f32_16x16x32_f16 v[8:11], v[120:123], v[176:179], v[8:11]\n" \
"buffer_load_dwordx4 v232, s[36:39], s80 offen sc1 lds\n" \
"s_waitcnt lgkmcnt(2)\n" \
"v_mfma_f32_16x16x16_bf16 v[184:187], v[184:185], v[192:193], 0\n" \
"v_mfma_f32_16x16x32_f16 v[20:23], v[124:127], v[176:179], v[20:23]\n" \
"ds_read_b64_tr_b16 v[128:129], v241 offset:32768\n" \
"ds_read_b64_tr_b16 v[130:131], v241 offset:40960\n" \
"s_waitcnt lgkmcnt(3)\n" \
"v_mfma_f32_16x16x16_bf16 v[188:191], v[188:189], v[192:193], 0\n" \
"v_mfma_f32_16x16x32_f16 v[24:27], v[136:139], v[160:163], v[24:27]\n" \
"ds_read_b128 v[176:179], v240 offset:47104\n" \
"v_mfma_f32_16x16x32_f16 v[36:39], v[140:143], v[160:163], v[36:39]\n" \
"v_pk_add_f16 v160, v160, v164\n" \
"v_pk_add_f16 v161, v161, v165\n" \
"ds_read_b64_tr_b16 v[132:133], v242 offset:32768\n" \
"v_mfma_f32_16x16x32_f16 v[28:31], v[144:147], v[164:167], v[28:31]\n" \
"v_pk_add_f16 v162, v162, v166\n" \
"v_pk_add_f16 v163, v163, v167\n" \
"v_cvt_pk_f16_f32 v184, v184, v185\n" \
"v_mfma_f32_16x16x32_f16 v[40:43], v[148:151], v[164:167], v[40:43]\n" \
"v_cvt_pk_f16_f32 v185, v186, v187\n" \
"v_cvt_pk_f16_f32 v186, v188, v189\n" \
"ds_read_b128 v[180:183], v240 offset:47616\n" \
"v_mfma_f32_16x16x32_f16 v[32:35], v[120:123], v[160:163], v[32:35]\n" \
"v_cvt_pk_f16_f32 v187, v190, v191\n" \
"v_add_u32 v184, s91, v184\n" \
"buffer_load_dwordx4 v232, s[36:39], s80 offen offset:1024 sc1 lds\n" \
"v_mfma_f32_16x16x32_f16 v[44:47], v[124:127], v[160:163], v[44:47]\n" \
"v_add_u32 v185, s91, v185\n" \
"v_add_u32 v186, s91, v186\n" \
"ds_read_b64_tr_b16 v[134:135], v242 offset:40960\n" \
"v_mfma_f32_16x16x32_f16 v[48:51], v[136:139], v[168:171], v[48:51]\n" \
"v_add_u32 v187, s91, v187\n" \
"v_and_b32 v184, s92, v184\n" \
"ds_read_b128 v[160:163], v240 offset:49152\n" \
"v_mfma_f32_16x16x32_f16 v[60:63], v[140:143], v[168:171], v[60:63]\n" \
"s_waitcnt lgkmcnt(4)\n" \
"v_pk_add_f16 v168, v168, v172\n" \
"v_pk_add_f16 v169, v169, v173\n" \
"v_and_b32 v185, s92, v185\n" \
"s_add_i32 s82, s50, 0x5000000\n" \
"v_mfma_f32_16x16x32_f16 v[52:55], v[144:147], v[172:175], v[52:55]\n" \
"v_pk_add_f16 v170, v170, v174\n" \
"v_pk_add_f16 v171, v171, v175\n" \
"v_and_b32 v186, s92, v186\n" \
"v_and_b32 v187, s92, v187\n" \
"v_mfma_f32_16x16x32_f16 v[64:67], v[148:151], v[172:175], v[64:67]\n" \
"ds_read_b128 v[164:167], v240 offset:49664\n" \
"buffer_store_dwordx4 v[184:187], v234, s[36:39], s82 offen sc1\n" \
"ds_read_b64_tr_b16 v[152:153], v241 offset:33280\n" \
"v_mfma_f32_16x16x32_f16 v[56:59], v[120:123], v[168:171], v[56:59]\n" \
"buffer_load_dwordx4 v232, s[36:39], s80 offen offset:2048 sc1 lds\n" \
"v_mfma_f32_16x16x32_f16 v[68:71], v[124:127], v[168:171], v[68:71]\n" \
"ds_read_b64_tr_b16 v[154:155], v241 offset:41472\n" \
"ds_read_b64_tr_b16 v[156:157], v242 offset:33280\n" \
"v_mfma_f32_16x16x32_f16 v[72:75], v[136:139], v[176:179], v[72:75]\n" \
"ds_read_b128 v[168:171], v240 offset:40960\n" \
"ds_read_b64_tr_b16 v[158:159], v242 offset:41472\n" \
"v_mfma_f32_16x16x32_f16 v[84:87], v[140:143], v[176:179], v[84:87]\n" \
"s_waitcnt lgkmcnt(6)\n" \
"v_pk_add_f16 v176, v176, v180\n" \
"v_pk_add_f16 v177, v177, v181\n" \
"v_mfma_f32_16x16x32_f16 v[76:79], v[144:147], v[180:183], v[76:79]\n" \
"v_pk_add_f16 v178, v178, v182\n" \
"v_pk_add_f16 v179, v179, v183\n" \
"v_mfma_f32_16x16x32_f16 v[88:91], v[148:151], v[180:183], v[88:91]\n" \
"ds_read_b128 v[172:175], v240 offset:41472\n" \
"v_mfma_f32_16x16x32_f16 v[80:83], v[120:123], v[176:179], v[80:83]\n" \
"buffer_load_dwordx4 v232, s[36:39], s80 offen offset:3072 sc1 lds\n" \
"v_mfma_f32_16x16x32_f16 v[92:95], v[124:127], v[176:179], v[92:95]\n" \
"v_mfma_f32_16x16x32_f16 v[96:99], v[136:139], v[160:163], v[96:99]\n" \
"s_waitcnt lgkmcnt(5)\n" \
"v_pk_add_f16 v136, v128, v152\n" \
"v_pk_add_f16 v137, v129, v153\n" \
"ds_read_b128 v[176:179], v240 offset:43008\n" \
"v_mfma_f32_16x16x32_f16 v[108:111], v[140:143], v[160:163], v[108:111]\n" \
"v_pk_add_f16 v160, v160, v164\n" \
"v_pk_add_f16 v161, v161, v165\n" \
"ds_read_b128 v[180:183], v240 offset:43520\n" \
"v_mfma_f32_16x16x32_f16 v[100:103], v[144:147], v[164:167], v[100:103]\n" \
"v_pk_add_f16 v162, v162, v166\n" \
"v_pk_add_f16 v163, v163, v167\n" \
"v_mfma_f32_16x16x32_f16 v[112:115], v[148:151], v[164:167], v[112:115]\n" \
"s_waitcnt lgkmcnt(3)\n" \
"v_pk_add_f16 v138, v130, v154\n" \
"v_pk_add_f16 v139, v131, v155\n" \
"v_mfma_f32_16x16x32_f16 v[104:107], v[120:123], v[160:163], v[104:107]\n" \
"v_pk_add_f16 v140, v132, v156\n" \
"v_pk_add_f16 v141, v133, v157\n" \
"v_mfma_f32_16x16x32_f16 v[116:119], v[124:127], v[160:163], v[116:119]\n" \
"v_pk_add_f16 v142, v134, v158\n" \
"v_pk_add_f16 v143, v135, v159\n" \
"s_waitcnt vmcnt(6) lgkmcnt(0)\n" \
"s_barrier\n" \
"v_mfma_f32_16x16x32_f16 v[0:3], v[128:131], v[168:171], v[0:3]\n" \
"ds_read_b128 v[160:163], v240 offset:4096\n" \
"ds_read_b64_tr_b16 v[184:185], v238 offset:20480\n" \
"ds_read_b64_tr_b16 v[188:189], v239 offset:20480\n" \
"v_mfma_f32_16x16x32_f16 v[12:15], v[132:135], v[168:171], v[12:15]\n" \
"v_pk_add_f16 v168, v168, v172\n" \
"v_pk_add_f16 v169, v169, v173\n" \
"v_mfma_f32_16x16x32_f16 v[4:7], v[152:155], v[172:175], v[4:7]\n" \
"v_pk_add_f16 v170, v170, v174\n" \
"v_pk_add_f16 v171, v171, v175\n" \
"v_cmp_eq_u32 vcc, 0, v253\n" \
"s_cbranch_vccnz .Ls05_%=\n" \
".Lr05_%=:\n" \
"v_mfma_f32_16x16x32_f16 v[16:19], v[156:159], v[172:175], v[16:19]\n" \
"ds_read_b128 v[164:167], v240 offset:4608\n" \
"s_add_i32 m0, s44, 0x8000\n" \
"s_add_i32 s80, s46, 0x3400000\n" \
"v_mfma_f32_16x16x32_f16 v[8:11], v[136:139], v[168:171], v[8:11]\n" \
"buffer_load_dwordx4 v232, s[36:39], s80 offen sc1 lds\n" \
"s_waitcnt lgkmcnt(2)\n" \
"v_mfma_f32_16x16x16_bf16 v[184:187], v[184:185], v[192:193], 0\n" \
"v_mfma_f32_16x16x32_f16 v[20:23], v[140:143], v[168:171], v[20:23]\n" \
"ds_read_b64_tr_b16 v[120:121], v241 offset:0\n" \
"ds_read_b64_tr_b16 v[122:123], v241 offset:8192\n" \
"s_waitcnt lgkmcnt(3)\n" \
"v_mfma_f32_16x16x16_bf16 v[188:191], v[188:189], v[192:193], 0\n" \
"v_mfma_f32_16x16x32_f16 v[24:27], v[128:131], v[176:179], v[24:27]\n" \
"ds_read_b128 v[168:171], v240 offset:6144\n" \
"s_add_i32 s84, s53, 0x800e000\n" \
"v_mfma_f32_16x16x32_f16 v[36:39], v[132:135], v[176:179], v[36:39]\n" \
"v_pk_add_f16 v176, v176, v180\n" \
"v_pk_add_f16 v177, v177, v181\n" \
"buffer_store_dword v240, v243, s[36:39], s84 offen sc1\n" \
"v_mfma_f32_16x16x32_f16 v[28:31], v[152:155], v[180:183], v[28:31]\n" \
"v_pk_add_f16 v178, v178, v182\n" \
"v_pk_add_f16 v179, v179, v183\n" \
"v_cvt_pk_f16_f32 v184, v184, v185\n" \
"ds_read_b64_tr_b16 v[124:125], v242 offset:0\n" \
"v_mfma_f32_16x16x32_f16 v[40:43], v[156:159], v[180:183], v[40:43]\n" \
"v_cvt_pk_f16_f32 v185, v186, v187\n" \
"v_cvt_pk_f16_f32 v186, v188, v189\n" \
"ds_read_b128 v[172:175], v240 offset:6656\n" \
"v_mfma_f32_16x16x32_f16 v[32:35], v[136:139], v[176:179], v[32:35]\n" \
"v_cvt_pk_f16_f32 v187, v190, v191\n" \
"v_add_u32 v184, s91, v184\n" \
"buffer_load_dwordx4 v232, s[36:39], s80 offen offset:1024 sc1 lds\n" \
"v_mfma_f32_16x16x32_f16 v[44:47], v[140:143], v[176:179], v[44:47]\n" \
"v_add_u32 v185, s91, v185\n" \
"v_add_u32 v186, s91, v186\n" \
"ds_read_b64_tr_b16 v[126:127], v242 offset:8192\n" \
"v_mfma_f32_16x16x32_f16 v[48:51], v[128:131], v[160:163], v[48:51]\n" \
"v_add_u32 v187, s91, v187\n" \
"v_and_b32 v184, s92, v184\n" \
"ds_read_b128 v[176:179], v240 offset:8192\n" \
"v_mfma_f32_16x16x32_f16 v[60:63], v[132:135], v[160:163], v[60:63]\n" \
"s_waitcnt lgkmcnt(4)\n" \
"v_pk_add_f16 v160, v160, v164\n" \
"v_pk_add_f16 v161, v161, v165\n" \
"v_and_b32 v185, s92, v185\n" \
"v_mfma_f32_16x16x32_f16 v[52:55], v[152:155], v[164:167], v[52:55]\n" \
"v_pk_add_f16 v162, v162, v166\n" \
"v_pk_add_f16 v163, v163, v167\n" \
"v_and_b32 v186, s92, v186\n" \
"v_and_b32 v187, s92, v187\n" \
"v_mfma_f32_16x16x32_f16 v[64:67], v[156:159], v[164:167], v[64:67]\n" \
"ds_read_b128 v[180:183], v240 offset:8704\n" \
"buffer_store_dwordx4 v[184:187], v234, s[36:39], s82 offen offset:1024 sc1\n" \
"ds_read_b64_tr_b16 v[144:145], v241 offset:512\n" \
"v_mfma_f32_16x16x32_f16 v[56:59], v[136:139], v[160:163], v[56:59]\n" \
"buffer_load_dwordx4 v232, s[36:39], s80 offen offset:2048 sc1 lds\n" \
"v_mfma_f32_16x16x32_f16 v[68:71], v[140:143], v[160:163], v[68:71]\n" \
"ds_read_b64_tr_b16 v[146:147], v241 offset:8704\n" \
"ds_read_b64_tr_b16 v[148:149], v242 offset:512\n" \
"v_mfma_f32_16x16x32_f16 v[72:75], v[128:131], v[168:171], v[72:75]\n" \
"ds_read_b128 v[160:163], v240 offset:0\n" \
"ds_read_b64_tr_b16 v[150:151], v242 offset:8704\n" \
"v_mfma_f32_16x16x32_f16 v[84:87], v[132:135], v[168:171], v[84:87]\n" \
"s_waitcnt lgkmcnt(6)\n" \
"v_pk_add_f16 v168, v168, v172\n" \
"v_pk_add_f16 v169, v169, v173\n" \
"v_mfma_f32_16x16x32_f16 v[76:79], v[152:155], v[172:175], v[76:79]\n" \
"v_pk_add_f16 v170, v170, v174\n" \
"v_pk_add_f16 v171, v171, v175\n" \
"v_mfma_f32_16x16x32_f16 v[88:91], v[156:159], v[172:175], v[88:91]\n" \
"ds_read_b128 v[164:167], v240 offset:512\n" \
"v_mfma_f32_16x16x32_f16 v[80:83], v[136:139], v[168:171], v[80:83]\n" \
"buffer_load_dwordx4 v232, s[36:39], s80 offen offset:3072 sc1 lds\n" \
"v_mfma_f32_16x16x32_f16 v[92:95], v[140:143], v[168:171], v[92:95]\n" \
"v_mfma_f32_16x16x32_f16 v[96:99], v[128:131], v[176:179], v[96:99]\n" \
"s_waitcnt lgkmcnt(5)\n" \
"v_pk_add_f16 v128, v120, v144\n" \
"v_pk_add_f16 v129, v121, v145\n" \
"ds_read_b128 v[168:171], v240 offset:2048\n" \
"v_mfma_f32_16x16x32_f16 v[108:111], v[132:135], v[176:179], v[108:111]\n" \
"v_pk_add_f16 v176, v176, v180\n" \
"v_pk_add_f16 v177, v177, v181\n" \
"ds_read_b128 v[172:175], v240 offset:2560\n" \
"v_mfma_f32_16x16x32_f16 v[100:103], v[152:155], v[180:183], v[100:103]\n" \
"v_pk_add_f16 v178, v178, v182\n" \
"v_pk_add_f16 v179, v179, v183\n" \
"v_mfma_f32_16x16x32_f16 v[112:115], v[156:159], v[180:183], v[112:115]\n" \
"s_waitcnt lgkmcnt(3)\n" \
"v_pk_add_f16 v130, v122, v146\n" \
"v_pk_add_f16 v131, v123, v147\n" \
"v_mfma_f32_16x16x32_f16 v[104:107], v[136:139], v[176:179], v[104:107]\n" \
"v_pk_add_f16 v132, v124, v148\n" \
"v_pk_add_f16 v133, v125, v149\n" \
"v_mfma_f32_16x16x32_f16 v[116:119], v[140:143], v[176:179], v[116:119]\n" \
"v_pk_add_f16 v134, v126, v150\n" \
"v_pk_add_f16 v135, v127, v151\n" \
"s_waitcnt vmcnt(6) lgkmcnt(0)\n" \
"s_barrier\n" \
"v_mfma_f32_16x16x32_f16 v[0:3], v[120:123], v[160:163], v[0:3]\n" \
"ds_read_b128 v[176:179], v240 offset:24576\n" \
"ds_read_b64_tr_b16 v[184:185], v236 offset:24576\n" \
"ds_read_b64_tr_b16 v[188:189], v237 offset:24576\n" \
"v_mfma_f32_16x16x32_f16 v[12:15], v[124:127], v[160:163], v[12:15]\n" \
"v_pk_add_f16 v160, v160, v164\n" \
"v_pk_add_f16 v161, v161, v165\n" \
"v_mfma_f32_16x16x32_f16 v[4:7], v[144:147], v[164:167], v[4:7]\n" \
"v_pk_add_f16 v162, v162, v166\n" \
"v_pk_add_f16 v163, v163, v167\n" \
"v_mfma_f32_16x16x32_f16 v[16:19], v[148:151], v[164:167], v[16:19]\n" \
"ds_read_b128 v[180:183], v240 offset:25088\n" \
"s_add_i32 m0, s44, 0x0\n" \
"s_add_i32 s80, s46, 0x3800000\n" \
"v_mfma_f32_16x16x32_f16 v[8:11], v[128:131], v[160:163], v[8:11]\n" \
"buffer_load_dwordx4 v232, s[36:39], s80 offen sc1 lds\n" \
"s_waitcnt lgkmcnt(2)\n" \
"v_mfma_f32_16x16x16_bf16 v[184:187], v[184:185], v[192:193], 0\n" \
"v_mfma_f32_16x16x32_f16 v[20:23], v[132:135], v[160:163], v[20:23]\n" \
"ds_read_b64_tr_b16 v[136:137], v241 offset:16384\n" \
"ds_read_b64_tr_b16 v[138:139], v241 offset:24576\n" \
"s_waitcnt lgkmcnt(3)\n" \
"v_mfma_f32_16x16x16_bf16 v[188:191], v[188:189], v[192:193], 0\n" \
"v_mfma_f32_16x16x32_f16 v[24:27], v[120:123], v[168:171], v[24:27]\n" \
"ds_read_b128 v[160:163], v240 offset:26624\n" \
"v_mfma_f32_16x16x32_f16 v[36:39], v[124:127], v[168:171], v[36:39]\n" \
"v_pk_add_f16 v168, v168, v172\n" \
"v_pk_add_f16 v169, v169, v173\n" \
"ds_read_b64_tr_b16 v[140:141], v242 offset:16384\n" \
"v_mfma_f32_16x16x32_f16 v[28:31], v[144:147], v[172:175], v[28:31]\n" \
"v_pk_add_f16 v170, v170, v174\n" \
"v_pk_add_f16 v171, v171, v175\n" \
"v_cvt_pk_f16_f32 v184, v184, v185\n" \
"v_mfma_f32_16x16x32_f16 v[40:43], v[148:151], v[172:175], v[40:43]\n" \
"v_cvt_pk_f16_f32 v185, v186, v187\n" \
"v_cvt_pk_f16_f32 v186, v188, v189\n" \
"ds_read_b128 v[164:167], v240 offset:27136\n" \
"v_mfma_f32_16x16x32_f16 v[32:35], v[128:131], v[168:171], v[32:35]\n" \
"v_cvt_pk_f16_f32 v187, v190, v191\n" \
"v_add_u32 v184, s91, v184\n" \
"buffer_load_dwordx4 v232, s[36:39], s80 offen offset:1024 sc1 lds\n" \
"v_mfma_f32_16x16x32_f16 v[44:47], v[132:135], v[168:171], v[44:47]\n" \
"v_add_u32 v185, s91, v185\n" \
"v_add_u32 v186, s91, v186\n" \
"ds_read_b64_tr_b16 v[142:143], v242 offset:24576\n" \
"v_mfma_f32_16x16x32_f16 v[48:51], v[120:123], v[176:179], v[48:51]\n" \
"v_add_u32 v187, s91, v187\n" \
"v_and_b32 v184, s92, v184\n" \
"ds_read_b128 v[168:171], v240 offset:28672\n" \
"v_mfma_f32_16x16x32_f16 v[60:63], v[124:127], v[176:179], v[60:63]\n" \
"s_waitcnt lgkmcnt(4)\n" \
"v_pk_add_f16 v176, v176, v180\n" \
"v_pk_add_f16 v177, v177, v181\n" \
"v_and_b32 v185, s92, v185\n" \
"s_add_i32 s82, s51, 0x5800000\n" \
"v_mfma_f32_16x16x32_f16 v[52:55], v[144:147], v[180:183], v[52:55]\n" \
"v_pk_add_f16 v178, v178, v182\n" \
"v_pk_add_f16 v179, v179, v183\n" \
"v_and_b32 v186, s92, v186\n" \
"v_and_b32 v187, s92, v187\n" \
"v_mfma_f32_16x16x32_f16 v[64:67], v[148:151], v[180:183], v[64:67]\n" \
"ds_read_b128 v[172:175], v240 offset:29184\n" \
"buffer_store_dwordx4 v[184:187], v234, s[36:39], s82 offen sc1\n" \
"ds_read_b64_tr_b16 v[152:153], v241 offset:16896\n" \
"v_mfma_f32_16x16x32_f16 v[56:59], v[128:131], v[176:179], v[56:59]\n" \
"buffer_load_dwordx4 v232, s[36:39], s80 offen offset:2048 sc1 lds\n" \
"v_mfma_f32_16x16x32_f16 v[68:71], v[132:135], v[176:179], v[68:71]\n" \
"ds_read_b64_tr_b16 v[154:155], v241 offset:25088\n" \
"ds_read_b64_tr_b16 v[156:157], v242 offset:16896\n" \
"v_mfma_f32_16x16x32_f16 v[72:75], v[120:123], v[160:163], v[72:75]\n" \
"ds_read_b128 v[176:179], v240 offset:20480\n" \
"ds_read_b64_tr_b16 v[158:159], v242 offset:25088\n" \
"v_mfma_f32_16x16x32_f16 v[84:87], v[124:127], v[160:163], v[84:87]\n" \
"s_waitcnt lgkmcnt(6)\n" \
"v_pk_add_f16 v160, v160, v164\n" \
"v_pk_add_f16 v161, v161, v165\n" \
"v_mfma_f32_16x16x32_f16 v[76:79], v[144:147], v[164:167], v[76:79]\n" \
"v_pk_add_f16 v162, v162, v166\n" \
"v_pk_add_f16 v163, v163, v167\n" \
"v_mfma_f32_16x16x32_f16 v[88:91], v[148:151], v[164:167], v[88:91]\n" \
"ds_read_b128 v[180:183], v240 offset:20992\n" \
"v_mfma_f32_16x16x32_f16 v[80:83], v[128:131], v[160:163], v[80:83]\n" \
"buffer_load_dwordx4 v232, s[36:39], s80 offen offset:3072 sc1 lds\n" \
"v_mfma_f32_16x16x32_f16 v[92:95], v[132:135], v[160:163], v[92:95]\n" \
"v_mfma_f32_16x16x32_f16 v[96:99], v[120:123], v[168:171], v[96:99]\n" \
"s_waitcnt lgkmcnt(5)\n" \
"v_pk_add_f16 v120, v136, v152\n" \
"v_pk_add_f16 v121, v137, v153\n" \
"ds_read_b128 v[160:163], v240 offset:22528\n" \
"v_mfma_f32_16x16x32_f16 v[108:111], v[124:127], v[168:171], v[108:111]\n" \
"v_pk_add_f16 v168, v168, v172\n" \
"v_pk_add_f16 v169, v169, v173\n" \
"ds_read_b128 v[164:167], v240 offset:23040\n" \
"v_mfma_f32_16x16x32_f16 v[100:103], v[144:147], v[172:175], v[100:103]\n" \
"v_pk_add_f16 v170, v170, v174\n" \
"v_pk_add_f16 v171, v171, v175\n" \
"v_mfma_f32_16x16x32_f16 v[112:115], v[148:151], v[172:175], v[112:115]\n" \
"s_waitcnt lgkmcnt(3)\n" \
"v_pk_add_f16 v122, v138, v154\n" \
"v_pk_add_f16 v123, v139, v155\n" \
"v_mfma_f32_16x16x32_f16 v[104:107], v[128:131], v[168:171], v[104:107]\n" \
"v_pk_add_f16 v124, v140, v156\n" \
"v_pk_add_f16 v125, v141, v157\n" \
"v_mfma_f32_16x16x32_f16 v[116:119], v[132:135], v[168:171], v[116:119]\n" \
"v_pk_add_f16 v126, v142, v158\n" \
"v_pk_add_f16 v127, v143, v159\n" \
"s_waitcnt vmcnt(5) lgkmcnt(0)\n" \
"s_barrier\n" \
"v_mfma_f32_16x16x32_f16 v[0:3], v[136:139], v[176:179], v[0:3]\n" \
"ds_read_b128 v[168:171], v240 offset:45056\n" \
"ds_read_b64_tr_b16 v[184:185], v238 offset:24576\n" \
"ds_read_b64_tr_b16 v[188:189], v239 offset:24576\n" \
"v_mfma_f32_16x16x32_f16 v[12:15], v[140:143], v[176:179], v[12:15]\n" \
"v_pk_add_f16 v176, v176, v180\n" \
"v_pk_add_f16 v177, v177, v181\n" \
"v_mfma_f32_16x16x32_f16 v[4:7], v[152:155], v[180:183], v[4:7]\n" \
"v_pk_add_f16 v178, v178, v182\n" \
"v_pk_add_f16 v179, v179, v183\n" \
"buffer_store_dwordx4 v[228:231], v235, s[36:39], s68 offen sc1\n" \
"v_mfma_f32_16x16x32_f16 v[16:19], v[156:159], v[180:183], v[16:19]\n" \
"ds_read_b128 v[172:175], v240 offset:45568\n" \
"s_add_i32 m0, s44, 0x4000\n" \
"s_add_i32 s80, s46, 0x3c00000\n" \
"v_mfma_f32_16x16x32_f16 v[8:11], v[120:123], v[176:179], v[8:11]\n" \
"buffer_load_dwordx4 v232, s[36:39], s80 offen sc1 lds\n" \
"s_waitcnt lgkmcnt(2)\n" \
"v_mfma_f32_16x16x16_bf16 v[184:187], v[184:185], v[192:193], 0\n" \
"v_mfma_f32_16x16x32_f16 v[20:23], v[124:127], v[176:179], v[20:23]\n" \
"ds_read_b64_tr_b16 v[128:129], v241 offset:32768\n" \
"ds_read_b64_tr_b16 v[130:131], v241 offset:40960\n" \
"s_waitcnt lgkmcnt(3)\n" \
"v_mfma_f32_16x16x16_bf16 v[188:191], v[188:189], v[192:193], 0\n" \
"v_mfma_f32_16x16x32_f16 v[24:27], v[136:139], v[160:163], v[24:27]\n" \
"ds_read_b128 v[176:179], v240 offset:47104\n" \
"s_add_i32 s84, s54, 0x8010000\n" \
"v_mfma_f32_16x16x32_f16 v[36:39], v[140:143], v[160:163], v[36:39]\n" \
"v_pk_add_f16 v160, v160, v164\n" \
"v_pk_add_f16 v161, v161, v165\n" \
"buffer_store_dword v240, v243, s[36:39], s84 offen sc1\n" \
"v_mfma_f32_16x16x32_f16 v[28:31], v[152:155], v[164:167], v[28:31]\n" \
"v_pk_add_f16 v162, v162, v166\n" \
"v_pk_add_f16 v163, v163, v167\n" \
"v_cvt_pk_f16_f32 v184, v184, v185\n" \
"ds_read_b64_tr_b16 v[132:133], v242 offset:32768\n" \
"v_mfma_f32_16x16x32_f16 v[40:43], v[156:159], v[164:167], v[40:43]\n" \
"v_cvt_pk_f16_f32 v185, v186, v187\n" \
"v_cvt_pk_f16_f32 v186, v188, v189\n" \
"ds_read_b128 v[180:183], v240 offset:47616\n" \
"v_mfma_f32_16x16x32_f16 v[32:35], v[120:123], v[160:163], v[32:35]\n" \
"v_cvt_pk_f16_f32 v187, v190, v191\n" \
"v_add_u32 v184, s91, v184\n" \
"buffer_load_dwordx4 v232, s[36:39], s80 offen offset:1024 sc1 lds\n" \
"v_mfma_f32_16x16x32_f16 v[44:47], v[124:127], v[160:163], v[44:47]\n" \
"v_add_u32 v185, s91, v185\n" \
"v_add_u32 v186, s91, v186\n" \
"ds_read_b64_tr_b16 v[134:135], v242 offset:40960\n" \
"v_mfma_f32_16x16x32_f16 v[48:51], v[136:139], v[168:171], v[48:51]\n" \
"v_add_u32 v187, s91, v187\n" \
"v_and_b32 v184, s92, v184\n" \
"ds_read_b128 v[160:163], v240 offset:49152\n" \
"v_mfma_f32_16x16x32_f16 v[60:63], v[140:143], v[168:171], v[60:63]\n" \
"s_waitcnt lgkmcnt(4)\n" \
"v_pk_add_f16 v168, v168, v172\n" \
"v_pk_add_f16 v169, v169, v173\n" \
"v_and_b32 v185, s92, v185\n" \
"v_mfma_f32_16x16x32_f16 v[52:55], v[152:155], v[172:175], v[52:55]\n" \
"v_pk_add_f16 v170, v170, v174\n" \
"v_pk_add_f16 v171, v171, v175\n" \
"v_and_b32 v186, s92, v186\n" \
"v_and_b32 v187, s92, v187\n" \
"v_mfma_f32_16x16x32_f16 v[64:67], v[156:159], v[172:175], v[64:67]\n" \
"ds_read_b128 v[164:167], v240 offset:49664\n" \
"buffer_store_dwordx4 v[184:187], v234, s[36:39], s82 offen offset:1024 sc1\n" \
"ds_read_b64_tr_b16 v[144:145], v241 offset:33280\n" \
"v_mfma_f32_16x16x32_f16 v[56:59], v[120:123], v[168:171], v[56:59]\n" \
"buffer_load_dwordx4 v232, s[36:39], s80 offen offset:2048 sc1 lds\n" \
"v_mfma_f32_16x16x32_f16 v[68:71], v[124:127], v[168:171], v[68:71]\n" \
"ds_read_b64_tr_b16 v[146:147], v241 offset:41472\n" \
"ds_read_b64_tr_b16 v[148:149], v242 offset:33280\n" \
"v_mfma_f32_16x16x32_f16 v[72:75], v[136:139], v[176:179], v[72:75]\n" \
"ds_read_b128 v[168:171], v240 offset:40960\n" \
"ds_read_b64_tr_b16 v[150:151], v242 offset:41472\n" \
"v_mfma_f32_16x16x32_f16 v[84:87], v[140:143], v[176:179], v[84:87]\n" \
"s_waitcnt lgkmcnt(6)\n" \
"v_pk_add_f16 v176, v176, v180\n" \
"v_pk_add_f16 v177, v177, v181\n" \
"v_mfma_f32_16x16x32_f16 v[76:79], v[152:155], v[180:183], v[76:79]\n" \
"v_pk_add_f16 v178, v178, v182\n" \
"v_pk_add_f16 v179, v179, v183\n" \
"v_mfma_f32_16x16x32_f16 v[88:91], v[156:159], v[180:183], v[88:91]\n" \
"ds_read_b128 v[172:175], v240 offset:41472\n" \
"v_mfma_f32_16x16x32_f16 v[80:83], v[120:123], v[176:179], v[80:83]\n" \
"buffer_load_dwordx4 v232, s[36:39], s80 offen offset:3072 sc1 lds\n" \
"v_mfma_f32_16x16x32_f16 v[92:95], v[124:127], v[176:179], v[92:95]\n" \
"v_mfma_f32_16x16x32_f16 v[96:99], v[136:139], v[160:163], v[96:99]\n" \
"s_waitcnt lgkmcnt(5)\n" \
"v_pk_add_f16 v136, v128, v144\n" \
"v_pk_add_f16 v137, v129, v145\n" \
"ds_read_b128 v[176:179], v240 offset:43008\n" \
"v_mfma_f32_16x16x32_f16 v[108:111], v[140:143], v[160:163], v[108:111]\n" \
"v_pk_add_f16 v160, v160, v164\n" \
"v_pk_add_f16 v161, v161, v165\n" \
"ds_read_b128 v[180:183], v240 offset:43520\n" \
"v_mfma_f32_16x16x32_f16 v[100:103], v[152:155], v[164:167], v[100:103]\n" \
"v_pk_add_f16 v162, v162, v166\n" \
"v_pk_add_f16 v163, v163, v167\n" \
"v_mfma_f32_16x16x32_f16 v[112:115], v[156:159], v[164:167], v[112:115]\n" \
"s_waitcnt lgkmcnt(3)\n" \
"v_pk_add_f16 v138, v130, v146\n" \
"v_pk_add_f16 v139, v131, v147\n" \
"v_mfma_f32_16x16x32_f16 v[104:107], v[120:123], v[160:163], v[104:107]\n" \
"v_pk_add_f16 v140, v132, v148\n" \
"v_pk_add_f16 v141, v133, v149\n" \
"v_mfma_f32_16x16x32_f16 v[116:119], v[124:127], v[160:163], v[116:119]\n" \
"v_pk_add_f16 v142, v134, v150\n" \
"v_pk_add_f16 v143, v135, v151\n" \
"s_waitcnt vmcnt(7) lgkmcnt(0)\n" \
"s_barrier\n" \
"v_mfma_f32_16x16x32_f16 v[0:3], v[128:131], v[168:171], v[0:3]\n" \
"ds_read_b128 v[160:163], v240 offset:4096\n" \
"s_mov_b32 s81, 0x800e000\n" \
"ds_read_b64_tr_b16 v[184:185], v236 offset:28672\n" \
"s_add_i32 m0, s45, 0x0\n" \
"s_mov_b32 s83, 0x0\n" \
"v_mfma_f32_16x16x32_f16 v[12:15], v[132:135], v[168:171], v[12:15]\n" \
"v_pk_add_f16 v168, v168, v172\n" \
"v_pk_add_f16 v169, v169, v173\n" \
"buffer_load_dword v253, v252, s[36:39], s81 offen sc1\n" \
"buffer_load_dwordx4 v233, s[36:39], s83 offen sc1 lds\n" \
"v_mfma_f32_16x16x32_f16 v[4:7], v[144:147], v[172:175], v[4:7]\n" \
"v_pk_add_f16 v170, v170, v174\n" \
"v_pk_add_f16 v171, v171, v175\n" \
"ds_read_b64_tr_b16 v[188:189], v237 offset:28672\n" \
"v_mfma_f32_16x16x32_f16 v[16:19], v[148:151], v[172:175], v[16:19]\n" \
"ds_read_b128 v[164:167], v240 offset:4608\n" \
"s_add_i32 m0, s44, 0x8000\n" \
"s_add_i32 s80, s46, 0x4000000\n" \
"v_mfma_f32_16x16x32_f16 v[8:11], v[136:139], v[168:171], v[8:11]\n" \
"buffer_load_dwordx4 v232, s[36:39], s80 offen sc1 lds\n" \
"s_waitcnt lgkmcnt(2)\n" \
"v_mfma_f32_16x16x16_bf16 v[184:187], v[184:185], v[192:193], 0\n" \
"v_mfma_f32_16x16x32_f16 v[20:23], v[140:143], v[168:171], v[20:23]\n" \
"ds_read_b64_tr_b16 v[120:121], v241 offset:0\n" \
"ds_read_b64_tr_b16 v[122:123], v241 offset:8192\n" \
"s_waitcnt lgkmcnt(3)\n" \
"v_mfma_f32_16x16x16_bf16 v[188:191], v[188:189], v[192:193], 0\n" \
"v_mfma_f32_16x16x32_f16 v[24:27], v[128:131], v[176:179], v[24:27]\n" \
"ds_read_b128 v[168:171], v240 offset:6144\n" \
"v_mfma_f32_16x16x32_f16 v[36:39], v[132:135], v[176:179], v[36:39]\n" \
"v_pk_add_f16 v176, v176, v180\n" \
"v_pk_add_f16 v177, v177, v181\n" \
"ds_read_b64_tr_b16 v[124:125], v242 offset:0\n" \
"v_mfma_f32_16x16x32_f16 v[28:31], v[144:147], v[180:183], v[28:31]\n" \
"v_pk_add_f16 v178, v178, v182\n" \
"v_pk_add_f16 v179, v179, v183\n" \
"v_cvt_pk_f16_f32 v184, v184, v185\n" \
"v_mfma_f32_16x16x32_f16 v[40:43], v[148:151], v[180:183], v[40:43]\n" \
"v_cvt_pk_f16_f32 v185, v186, v187\n" \
"v_cvt_pk_f16_f32 v186, v188, v189\n" \
"ds_read_b128 v[172:175], v240 offset:6656\n" \
"v_mfma_f32_16x16x32_f16 v[32:35], v[136:139], v[176:179], v[32:35]\n" \
"v_cvt_pk_f16_f32 v187, v190, v191\n" \
"v_add_u32 v184, s91, v184\n" \
"buffer_load_dwordx4 v232, s[36:39], s80 offen offset:1024 sc1 lds\n" \
"v_mfma_f32_16x16x32_f16 v[44:47], v[140:143], v[176:179], v[44:47]\n" \
"v_add_u32 v185, s91, v185\n" \
"v_add_u32 v186, s91, v186\n" \
"ds_read_b64_tr_b16 v[126:127], v242 offset:8192\n" \
"v_mfma_f32_16x16x32_f16 v[48:51], v[128:131], v[160:163], v[48:51]\n" \
"v_add_u32 v187, s91, v187\n" \
"v_and_b32 v184, s92, v184\n" \
"ds_read_b128 v[176:179], v240 offset:8192\n" \
"v_mfma_f32_16x16x32_f16 v[60:63], v[132:135], v[160:163], v[60:63]\n" \
"s_waitcnt lgkmcnt(4)\n" \
"v_pk_add_f16 v160, v160, v164\n" \
"v_pk_add_f16 v161, v161, v165\n" \
"v_and_b32 v185, s92, v185\n" \
"s_add_i32 s82, s48, 0x6000000\n" \
"v_mfma_f32_16x16x32_f16 v[52:55], v[144:147], v[164:167], v[52:55]\n" \
"v_pk_add_f16 v162, v162, v166\n" \
"v_pk_add_f16 v163, v163, v167\n" \
"v_and_b32 v186, s92, v186\n" \
"v_and_b32 v187, s92, v187\n" \
"v_mfma_f32_16x16x32_f16 v[64:67], v[148:151], v[164:167], v[64:67]\n" \
"ds_read_b128 v[180:183], v240 offset:8704\n" \
"buffer_store_dwordx4 v[184:187], v234, s[36:39], s82 offen sc1\n" \
"ds_read_b64_tr_b16 v[152:153], v241 offset:512\n" \
"v_mfma_f32_16x16x32_f16 v[56:59], v[136:139], v[160:163], v[56:59]\n" \
"buffer_load_dwordx4 v232, s[36:39], s80 offen offset:2048 sc1 lds\n" \
"v_mfma_f32_16x16x32_f16 v[68:71], v[140:143], v[160:163], v[68:71]\n" \
"ds_read_b64_tr_b16 v[154:155], v241 offset:8704\n" \
"ds_read_b64_tr_b16 v[156:157], v242 offset:512\n" \
"v_mfma_f32_16x16x32_f16 v[72:75], v[128:131], v[168:171], v[72:75]\n" \
"ds_read_b128 v[160:163], v240 offset:0\n" \
"ds_read_b64_tr_b16 v[158:159], v242 offset:8704\n" \
"v_mfma_f32_16x16x32_f16 v[84:87], v[132:135], v[168:171], v[84:87]\n" \
"s_waitcnt lgkmcnt(6)\n" \
"v_pk_add_f16 v168, v168, v172\n" \
"v_pk_add_f16 v169, v169, v173\n" \
"v_mfma_f32_16x16x32_f16 v[76:79], v[144:147], v[172:175], v[76:79]\n" \
"v_pk_add_f16 v170, v170, v174\n" \
"v_pk_add_f16 v171, v171, v175\n" \
"v_mfma_f32_16x16x32_f16 v[88:91], v[148:151], v[172:175], v[88:91]\n" \
"ds_read_b128 v[164:167], v240 offset:512\n" \
"v_mfma_f32_16x16x32_f16 v[80:83], v[136:139], v[168:171], v[80:83]\n" \
"buffer_load_dwordx4 v232, s[36:39], s80 offen offset:3072 sc1 lds\n" \
"v_mfma_f32_16x16x32_f16 v[92:95], v[140:143], v[168:171], v[92:95]\n" \
"v_mfma_f32_16x16x32_f16 v[96:99], v[128:131], v[176:179], v[96:99]\n" \
"s_waitcnt lgkmcnt(5)\n" \
"v_pk_add_f16 v128, v120, v152\n" \
"v_pk_add_f16 v129, v121, v153\n" \
"ds_read_b128 v[168:171], v240 offset:2048\n" \
"s_add_i32 m0, s45, 0x1000\n" \
"s_mov_b32 s83, 0x8000\n" \
"v_mfma_f32_16x16x32_f16 v[108:111], v[132:135], v[176:179], v[108:111]\n" \
"v_pk_add_f16 v176, v176, v180\n" \
"v_pk_add_f16 v177, v177, v181\n" \
"ds_read_b128 v[172:175], v240 offset:2560\n" \
"buffer_load_dwordx4 v233, s[36:39], s83 offen sc1 lds\n" \
"v_mfma_f32_16x16x32_f16 v[100:103], v[144:147], v[180:183], v[100:103]\n" \
"v_pk_add_f16 v178, v178, v182\n" \
"v_pk_add_f16 v179, v179, v183\n" \
"v_mfma_f32_16x16x32_f16 v[112:115], v[148:151], v[180:183], v[112:115]\n" \
"s_waitcnt lgkmcnt(3)\n" \
"v_pk_add_f16 v130, v122, v154\n" \
"v_pk_add_f16 v131, v123, v155\n" \
"v_mfma_f32_16x16x32_f16 v[104:107], v[136:139], v[176:179], v[104:107]\n" \
"v_pk_add_f16 v132, v124, v156\n" \
"v_pk_add_f16 v133, v125, v157\n" \
"v_mfma_f32_16x16x32_f16 v[116:119], v[140:143], v[176:179], v[116:119]\n" \
"v_pk_add_f16 v134, v126, v158\n" \
"v_pk_add_f16 v135, v127, v159\n" \
"s_waitcnt vmcnt(7) lgkmcnt(0)\n" \
"s_barrier\n" \
"v_mfma_f32_16x16x32_f16 v[0:3], v[120:123], v[160:163], v[0:3]\n" \
"ds_read_b128 v[176:179], v240 offset:24576\n" \
"ds_read_b64_tr_b16 v[184:185], v238 offset:28672\n" \
"ds_read_b64_tr_b16 v[188:189], v239 offset:28672\n" \
"s_add_i32 m0, s45, 0x2000\n" \
"s_mov_b32 s83, 0x10000\n" \
"v_mfma_f32_16x16x32_f16 v[12:15], v[124:127], v[160:163], v[12:15]\n" \
"v_pk_add_f16 v160, v160, v164\n" \
"v_pk_add_f16 v161, v161, v165\n" \
"buffer_load_dwordx4 v233, s[36:39], s83 offen sc1 lds\n" \
"v_mfma_f32_16x16x32_f16 v[4:7], v[152:155], v[164:167], v[4:7]\n" \
"v_pk_add_f16 v162, v162, v166\n" \
"v_pk_add_f16 v163, v163, v167\n" \
"v_cmp_eq_u32 vcc, 0, v253\n" \
"s_cbranch_vccnz .Ls07_%=\n" \
".Lr07_%=:\n" \
"v_mfma_f32_16x16x32_f16 v[16:19], v[156:159], v[164:167], v[16:19]\n" \
"ds_read_b128 v[180:183], v240 offset:25088\n" \
"s_add_i32 m0, s44, 0x0\n" \
"s_add_i32 s80, s46, 0x4400000\n" \
"v_mfma_f32_16x16x32_f16 v[8:11], v[128:131], v[160:163], v[8:11]\n" \
"buffer_load_dwordx4 v232, s[36:39], s80 offen sc1 lds\n" \
"s_waitcnt lgkmcnt(2)\n" \
"v_mfma_f32_16x16x16_bf16 v[184:187], v[184:185], v[192:193], 0\n" \
"v_mfma_f32_16x16x32_f16 v[20:23], v[132:135], v[160:163], v[20:23]\n" \
"ds_read_b64_tr_b16 v[136:137], v241 offset:16384\n" \
"ds_read_b64_tr_b16 v[138:139], v241 offset:24576\n" \
"s_waitcnt lgkmcnt(3)\n" \
"v_mfma_f32_16x16x16_bf16 v[188:191], v[188:189], v[192:193], 0\n" \
"v_mfma_f32_16x16x32_f16 v[24:27], v[120:123], v[168:171], v[24:27]\n" \
"ds_read_b128 v[160:163], v240 offset:26624\n" \
"s_add_i32 s84, s55, 0x8012000\n" \
"v_mfma_f32_16x16x32_f16 v[36:39], v[124:127], v[168:171], v[36:39]\n" \
"v_pk_add_f16 v168, v168, v172\n" \
"v_pk_add_f16 v169, v169, v173\n" \
"buffer_store_dword v240, v243, s[36:39], s84 offen sc1\n" \
"v_mfma_f32_16x16x32_f16 v[28:31], v[152:155], v[172:175], v[28:31]\n" \
"v_pk_add_f16 v170, v170, v174\n" \
"v_pk_add_f16 v171, v171, v175\n" \
"v_cvt_pk_f16_f32 v184, v184, v185\n" \
"ds_read_b64_tr_b16 v[140:141], v242 offset:16384\n" \
"v_mfma_f32_16x16x32_f16 v[40:43], v[156:159], v[172:175], v[40:43]\n" \
"v_cvt_pk_f16_f32 v185, v186, v187\n" \
"v_cvt_pk_f16_f32 v186, v188, v189\n" \
"ds_read_b128 v[164:167], v240 offset:27136\n" \
"v_mfma_f32_16x16x32_f16 v[32:35], v[128:131], v[168:171], v[32:35]\n" \
"v_cvt_pk_f16_f32 v187, v190, v191\n" \
"v_add_u32 v184, s91, v184\n" \
"buffer_load_dwordx4 v232, s[36:39], s80 offen offset:1024 sc1 lds\n" \
"v_mfma_f32_16x16x32_f16 v[44:47], v[132:135], v[168:171], v[44:47]\n" \
"v_add_u32 v185, s91, v185\n" \
"v_add_u32 v186, s91, v186\n" \
"ds_read_b64_tr_b16 v[142:143], v242 offset:24576\n" \
"v_mfma_f32_16x16x32_f16 v[48:51], v[120:123], v[176:179], v[48:51]\n" \
"v_add_u32 v187, s91, v187\n" \
"v_and_b32 v184, s92, v184\n" \
"ds_read_b128 v[168:171], v240 offset:28672\n" \
"v_mfma_f32_16x16x32_f16 v[60:63], v[124:127], v[176:179], v[60:63]\n" \
"s_waitcnt lgkmcnt(4)\n" \
"v_pk_add_f16 v176, v176, v180\n" \
"v_pk_add_f16 v177, v177, v181\n" \
"v_and_b32 v185, s92, v185\n" \
"v_mfma_f32_16x16x32_f16 v[52:55], v[152:155], v[180:183], v[52:55]\n" \
"v_pk_add_f16 v178, v178, v182\n" \
"v_pk_add_f16 v179, v179, v183\n" \
"v_and_b32 v186, s92, v186\n" \
"v_and_b32 v187, s92, v187\n" \
"v_mfma_f32_16x16x32_f16 v[64:67], v[156:159], v[180:183], v[64:67]\n" \
"ds_read_b128 v[172:175], v240 offset:29184\n" \
"buffer_store_dwordx4 v[184:187], v234, s[36:39], s82 offen offset:1024 sc1\n" \
"ds_read_b64_tr_b16 v[144:145], v241 offset:16896\n" \
"v_mfma_f32_16x16x32_f16 v[56:59], v[128:131], v[176:179], v[56:59]\n" \
"buffer_load_dwordx4 v232, s[36:39], s80 offen offset:2048 sc1 lds\n" \
"v_mfma_f32_16x16x32_f16 v[68:71], v[132:135], v[176:179], v[68:71]\n" \
"ds_read_b64_tr_b16 v[146:147], v241 offset:25088\n" \
"ds_read_b64_tr_b16 v[148:149], v242 offset:16896\n" \
"v_mfma_f32_16x16x32_f16 v[72:75], v[120:123], v[160:163], v[72:75]\n" \
"ds_read_b128 v[176:179], v240 offset:20480\n" \
"ds_read_b64_tr_b16 v[150:151], v242 offset:25088\n" \
"v_mfma_f32_16x16x32_f16 v[84:87], v[124:127], v[160:163], v[84:87]\n" \
"s_waitcnt lgkmcnt(6)\n" \
"v_pk_add_f16 v160, v160, v164\n" \
"v_pk_add_f16 v161, v161, v165\n" \
"v_mfma_f32_16x16x32_f16 v[76:79], v[152:155], v[164:167], v[76:79]\n" \
"v_pk_add_f16 v162, v162, v166\n" \
"v_pk_add_f16 v163, v163, v167\n" \
"v_mfma_f32_16x16x32_f16 v[88:91], v[156:159], v[164:167], v[88:91]\n" \
"ds_read_b128 v[180:183], v240 offset:20992\n" \
"v_mfma_f32_16x16x32_f16 v[80:83], v[128:131], v[160:163], v[80:83]\n" \
"buffer_load_dwordx4 v232, s[36:39], s80 offen offset:3072 sc1 lds\n" \
"v_mfma_f32_16x16x32_f16 v[92:95], v[132:135], v[160:163], v[92:95]\n" \
"v_mfma_f32_16x16x32_f16 v[96:99], v[120:123], v[168:171], v[96:99]\n" \
"s_waitcnt lgkmcnt(5)\n" \
"v_pk_add_f16 v120, v136, v144\n" \
"v_pk_add_f16 v121, v137, v145\n" \
"ds_read_b128 v[160:163], v240 offset:22528\n" \
"s_add_i32 m0, s45, 0x3000\n" \
"s_mov_b32 s83, 0x18000\n" \
"v_mfma_f32_16x16x32_f16 v[108:111], v[124:127], v[168:171], v[108:111]\n" \
"v_pk_add_f16 v168, v168, v172\n" \
"v_pk_add_f16 v169, v169, v173\n" \
"ds_read_b128 v[164:167], v240 offset:23040\n" \
"buffer_load_dwordx4 v233, s[36:39], s83 offen sc1 lds\n" \
"v_mfma_f32_16x16x32_f16 v[100:103], v[152:155], v[172:175], v[100:103]\n" \
"v_pk_add_f16 v170, v170, v174\n" \
"v_pk_add_f16 v171, v171, v175\n" \
"v_mfma_f32_16x16x32_f16 v[112:115], v[156:159], v[172:175], v[112:115]\n" \
"s_waitcnt lgkmcnt(3)\n" \
"v_pk_add_f16 v122, v138, v146\n" \
"v_pk_add_f16 v123, v139, v147\n" \
"v_mfma_f32_16x16x32_f16 v[104:107], v[128:131], v[168:171], v[104:107]\n" \
"v_pk_add_f16 v124, v140, v148\n" \
"v_pk_add_f16 v125, v141, v149\n" \
"v_mfma_f32_16x16x32_f16 v[116:119], v[132:135], v[168:171], v[116:119]\n" \
"v_pk_add_f16 v126, v142, v150\n" \
"v_pk_add_f16 v127, v143, v151\n" \
"s_waitcnt vmcnt(8) lgkmcnt(0)\n" \
"s_barrier\n" \
"v_mfma_f32_16x16x32_f16 v[0:3], v[136:139], v[176:179], v[0:3]\n" \
"ds_read_b128 v[168:171], v240 offset:45056\n" \
"s_mov_b32 s81, 0x8010000\n" \
"ds_read_b64_tr_b16 v[184:185], v236 offset:32768\n" \
"s_add_i32 m0, s45, 0x4000\n" \
"s_mov_b32 s83, 0x20000\n" \
"v_mfma_f32_16x16x32_f16 v[12:15], v[140:143], v[176:179], v[12:15]\n" \
"v_pk_add_f16 v176, v176, v180\n" \
"v_pk_add_f16 v177, v177, v181\n" \
"buffer_load_dword v253, v252, s[36:39], s81 offen sc1\n" \
"buffer_load_dwordx4 v233, s[36:39], s83 offen sc1 lds\n" \
"v_mfma_f32_16x16x32_f16 v[4:7], v[144:147], v[180:183], v[4:7]\n" \
"v_pk_add_f16 v178, v178, v182\n" \
"v_pk_add_f16 v179, v179, v183\n" \
"ds_read_b64_tr_b16 v[188:189], v237 offset:32768\n" \
"v_mfma_f32_16x16x32_f16 v[16:19], v[148:151], v[180:183], v[16:19]\n" \
"ds_read_b128 v[172:175], v240 offset:45568\n" \
"s_add_i32 m0, s44, 0x4000\n" \
"s_add_i32 s80, s46, 0x4800000\n" \
"v_mfma_f32_16x16x32_f16 v[8:11], v[120:123], v[176:179], v[8:11]\n" \
"buffer_load_dwordx4 v232, s[36:39], s80 offen sc1 lds\n" \
"s_waitcnt lgkmcnt(2)\n" \
"v_mfma_f32_16x16x16_bf16 v[184:187], v[184:185], v[192:193], 0\n" \
"v_mfma_f32_16x16x32_f16 v[20:23], v[124:127], v[176:179], v[20:23]\n" \
"ds_read_b128 v[196:199], v249 offset:0\n" \
"ds_read_b64_tr_b16 v[200:201], v247 offset:0\n" \
"s_waitcnt lgkmcnt(3)\n" \
"v_mfma_f32_16x16x16_bf16 v[188:191], v[188:189], v[192:193], 0\n" \
"v_mfma_f32_16x16x32_f16 v[24:27], v[136:139], v[160:163], v[24:27]\n" \
"ds_read_b128 v[176:179], v240 offset:47104\n" \
"ds_read_b64_tr_b16 v[128:129], v241 offset:32768\n" \
"ds_read_b64_tr_b16 v[130:131], v241 offset:40960\n" \
"v_mfma_f32_16x16x32_f16 v[36:39], v[140:143], v[160:163], v[36:39]\n" \
"v_pk_add_f16 v160, v160, v164\n" \
"v_pk_add_f16 v161, v161, v165\n" \
"ds_read_b64_tr_b16 v[202:203], v247 offset:512\n" \
"ds_read_b64_tr_b16 v[132:133], v242 offset:32768\n" \
"v_mfma_f32_16x16x32_f16 v[28:31], v[144:147], v[164:167], v[28:31]\n" \
"v_pk_add_f16 v162, v162, v166\n" \
"v_pk_add_f16 v163, v163, v167\n" \
"v_cvt_pk_f16_f32 v184, v184, v185\n" \
"v_mfma_f32_16x16x32_f16 v[40:43], v[148:151], v[164:167], v[40:43]\n" \
"v_cvt_pk_f16_f32 v185, v186, v187\n" \
"v_cvt_pk_f16_f32 v186, v188, v189\n" \
"ds_read_b128 v[180:183], v240 offset:47616\n" \
"v_mfma_f32_16x16x32_f16 v[32:35], v[120:123], v[160:163], v[32:35]\n" \
"v_cvt_pk_f16_f32 v187, v190, v191\n" \
"v_add_u32 v184, s91, v184\n" \
"buffer_load_dwordx4 v232, s[36:39], s80 offen offset:1024 sc1 lds\n" \
"v_mfma_f32_16x16x32_f16 v[44:47], v[124:127], v[160:163], v[44:47]\n" \
"v_add_u32 v185, s91, v185\n" \
"v_add_u32 v186, s91, v186\n" \
"ds_read_b64_tr_b16 v[134:135], v242 offset:40960\n" \
"v_mfma_f32_16x16x32_f16 v[48:51], v[136:139], v[168:171], v[48:51]\n" \
"v_add_u32 v187, s91, v187\n" \
"v_and_b32 v184, s92, v184\n" \
"ds_read_b128 v[160:163], v240 offset:49152\n" \
"v_mfma_f32_16x16x32_f16 v[60:63], v[140:143], v[168:171], v[60:63]\n" \
"s_waitcnt lgkmcnt(5)\n" \
"v_pk_add_f16 v168, v168, v172\n" \
"v_pk_add_f16 v169, v169, v173\n" \
"v_and_b32 v185, s92, v185\n" \
"s_add_i32 s82, s49, 0x6800000\n" \
"s_waitcnt lgkmcnt(3)\n" \
"v_mfma_f32_16x16x32_bf16 v[204:207], v[200:203], v[196:199], 0\n" \
"v_mfma_f32_16x16x32_f16 v[52:55], v[144:147], v[172:175], v[52:55]\n" \
"v_pk_add_f16 v170, v170, v174\n" \
"v_pk_add_f16 v171, v171, v175\n" \
"v_and_b32 v186, s92, v186\n" \
"v_and_b32 v187, s92, v187\n" \
"v_mfma_f32_16x16x32_f16 v[64:67], v[148:151], v[172:175], v[64:67]\n" \
"ds_read_b128 v[164:167], v240 offset:49664\n" \
"buffer_store_dwordx4 v[184:187], v234, s[36:39], s82 offen sc1\n" \
"ds_read_b64_tr_b16 v[152:153], v241 offset:33280\n" \
"v_mfma_f32_16x16x32_f16 v[56:59], v[120:123], v[168:171], v[56:59]\n" \
"buffer_load_dwordx4 v232, s[36:39], s80 offen offset:2048 sc1 lds\n" \
"ds_read_b64_tr_b16 v[154:155], v241 offset:41472\n" \
"v_mfma_f32_16x16x32_f16 v[68:71], v[124:127], v[168:171], v[68:71]\n" \
"ds_read_b128 v[196:199], v249 offset:1024\n" \
"ds_read_b64_tr_b16 v[200:201], v247 offset:4096\n" \
"ds_read_b64_tr_b16 v[156:157], v242 offset:33280\n" \
"v_mfma_f32_16x16x32_f16 v[72:75], v[136:139], v[176:179], v[72:75]\n" \
"ds_read_b128 v[168:171], v240 offset:40960\n" \
"ds_read_b64_tr_b16 v[202:203], v247 offset:4608\n" \
"v_mfma_f32_16x16x32_f16 v[84:87], v[140:143], v[176:179], v[84:87]\n" \
"s_waitcnt lgkmcnt(8)\n" \
"v_pk_add_f16 v176, v176, v180\n" \
"v_pk_add_f16 v177, v177, v181\n" \
"v_mfma_f32_16x16x32_f16 v[76:79], v[144:147], v[180:183], v[76:79]\n" \
"v_pk_add_f16 v178, v178, v182\n" \
"v_pk_add_f16 v179, v179, v183\n" \
"v_mfma_f32_16x16x32_f16 v[88:91], v[148:151], v[180:183], v[88:91]\n" \
"ds_read_b128 v[172:175], v240 offset:41472\n" \
"ds_read_b64_tr_b16 v[158:159], v242 offset:41472\n" \
"v_mfma_f32_16x16x32_f16 v[80:83], v[120:123], v[176:179], v[80:83]\n" \
"buffer_load_dwordx4 v232, s[36:39], s80 offen offset:3072 sc1 lds\n" \
"v_mfma_f32_16x16x32_f16 v[92:95], v[124:127], v[176:179], v[92:95]\n" \
"v_mfma_f32_16x16x32_f16 v[96:99], v[136:139], v[160:163], v[96:99]\n" \
"s_waitcnt lgkmcnt(8)\n" \
"v_pk_add_f16 v136, v128, v152\n" \
"v_pk_add_f16 v137, v129, v153\n" \
"ds_read_b128 v[176:179], v240 offset:43008\n" \
"s_add_i32 m0, s45, 0x5000\n" \
"s_mov_b32 s83, 0x28000\n" \
"s_waitcnt lgkmcnt(3)\n" \
"v_mfma_f32_16x16x32_bf16 v[204:207], v[200:203], v[196:199], v[204:207]\n" \
"v_mfma_f32_16x16x32_f16 v[108:111], v[140:143], v[160:163], v[108:111]\n" \
"v_pk_add_f16 v160, v160, v164\n" \
"v_pk_add_f16 v161, v161, v165\n" \
"ds_read_b128 v[180:183], v240 offset:43520\n" \
"buffer_load_dwordx4 v233, s[36:39], s83 offen sc1 lds\n" \
"v_mfma_f32_16x16x32_f16 v[100:103], v[144:147], v[164:167], v[100:103]\n" \
"v_pk_add_f16 v162, v162, v166\n" \
"v_pk_add_f16 v163, v163, v167\n" \
"v_mfma_f32_16x16x32_f16 v[112:115], v[148:151], v[164:167], v[112:115]\n" \
"v_pk_add_f16 v138, v130, v154\n" \
"v_pk_add_f16 v139, v131, v155\n" \
"v_mfma_f32_16x16x32_f16 v[104:107], v[120:123], v[160:163], v[104:107]\n" \
"v_pk_add_f16 v140, v132, v156\n" \
"v_pk_add_f16 v141, v133, v157\n" \
"v_mfma_f32_16x16x32_f16 v[116:119], v[124:127], v[160:163], v[116:119]\n" \
"s_waitcnt lgkmcnt(2)\n" \
"v_pk_add_f16 v142, v134, v158\n" \
"v_pk_add_f16 v143, v135, v159\n" \
"s_waitcnt vmcnt(7) lgkmcnt(0)\n" \
"s_barrier\n" \
"v_mfma_f32_16x16x32_f16 v[0:3], v[128:131], v[168:171], v[0:3]\n" \
"ds_read_b128 v[160:163], v240 offset:4096\n" \
"ds_read_b64_tr_b16 v[184:185], v238 offset:32768\n" \
"ds_read_b64_tr_b16 v[188:189], v239 offset:32768\n" \
"s_add_i32 m0, s45, 0x0\n" \
"s_mov_b32 s83, 0x30000\n" \
"v_mfma_f32_16x16x32_f16 v[12:15], v[132:135], v[168:171], v[12:15]\n" \
"v_pk_add_f16 v168, v168, v172\n" \
"v_pk_add_f16 v169, v169, v173\n" \
"buffer_load_dwordx4 v233, s[36:39], s83 offen sc1 lds\n" \
"v_mfma_f32_16x16x32_f16 v[4:7], v[152:155], v[172:175], v[4:7]\n" \
"v_pk_add_f16 v170, v170, v174\n" \
"v_pk_add_f16 v171, v171, v175\n" \
"v_cmp_eq_u32 vcc, 0, v253\n" \
"s_cbranch_vccnz .Ls08_%=\n" \
".Lr08_%=:\n" \
"v_mfma_f32_16x16x32_f16 v[16:19], v[156:159], v[172:175], v[16:19]\n" \
"ds_read_b128 v[164:167], v240 offset:4608\n" \
"s_add_i32 m0, s44, 0x8000\n" \
"s_add_i32 s80, s46, 0x4c00000\n" \
"v_mfma_f32_16x16x32_f16 v[8:11], v[136:139], v[168:171], v[8:11]\n" \
"buffer_load_dwordx4 v232, s[36:39], s80 offen sc1 lds\n" \
"ds_read_b64_tr_b16 v[120:121], v241 offset:0\n" \
"s_waitcnt lgkmcnt(3)\n" \
"v_mfma_f32_16x16x16_bf16 v[184:187], v[184:185], v[192:193], 0\n" \
"v_mfma_f32_16x16x32_f16 v[20:23], v[140:143], v[168:171], v[20:23]\n" \
"ds_read_b128 v[196:199], v249 offset:2048\n" \
"ds_read_b64_tr_b16 v[200:201], v247 offset:8192\n" \
"s_waitcnt lgkmcnt(4)\n" \
"v_mfma_f32_16x16x16_bf16 v[188:191], v[188:189], v[192:193], 0\n" \
"v_mfma_f32_16x16x32_f16 v[24:27], v[128:131], v[176:179], v[24:27]\n" \
"ds_read_b128 v[168:171], v240 offset:6144\n" \
"s_add_i32 s84, s52, 0x8014000\n" \
"ds_read_b64_tr_b16 v[122:123], v241 offset:8192\n" \
"v_mfma_f32_16x16x32_f16 v[36:39], v[132:135], v[176:179], v[36:39]\n" \
"v_pk_add_f16 v176, v176, v180\n" \
"v_pk_add_f16 v177, v177, v181\n" \
"buffer_store_dword v240, v243, s[36:39], s84 offen sc1\n" \
"v_mfma_f32_16x16x32_f16 v[28:31], v[152:155], v[180:183], v[28:31]\n" \
"v_pk_add_f16 v178, v178, v182\n" \
"v_pk_add_f16 v179, v179, v183\n" \
"v_cvt_pk_f16_f32 v184, v184, v185\n" \
"ds_read_b64_tr_b16 v[202:203], v247 offset:8704\n" \
"v_mfma_f32_16x16x32_f16 v[40:43], v[156:159], v[180:183], v[40:43]\n" \
"v_cvt_pk_f16_f32 v185, v186, v187\n" \
"v_cvt_pk_f16_f32 v186, v188, v189\n" \
"ds_read_b128 v[172:175], v240 offset:6656\n" \
"ds_read_b64_tr_b16 v[124:125], v242 offset:0\n" \
"v_mfma_f32_16x16x32_f16 v[32:35], v[136:139], v[176:179], v[32:35]\n" \
"v_cvt_pk_f16_f32 v187, v190, v191\n" \
"v_add_u32 v184, s91, v184\n" \
"buffer_load_dwordx4 v232, s[36:39], s80 offen offset:1024 sc1 lds\n" \
"v_mfma_f32_16x16x32_f16 v[44:47], v[140:143], v[176:179], v[44:47]\n" \
"v_add_u32 v185, s91, v185\n" \
"v_add_u32 v186, s91, v186\n" \
"ds_read_b64_tr_b16 v[126:127], v242 offset:8192\n" \
"v_mfma_f32_16x16x32_f16 v[48:51], v[128:131], v[160:163], v[48:51]\n" \
"v_add_u32 v187, s91, v187\n" \
"v_and_b32 v184, s92, v184\n" \
"ds_read_b128 v[176:179], v240 offset:8192\n" \
"v_mfma_f32_16x16x32_f16 v[60:63], v[132:135], v[160:163], v[60:63]\n" \
"s_waitcnt lgkmcnt(5)\n" \
"v_pk_add_f16 v160, v160, v164\n" \
"v_pk_add_f16 v161, v161, v165\n" \
"v_and_b32 v185, s92, v185\n" \
"s_waitcnt lgkmcnt(4)\n" \
"v_mfma_f32_16x16x32_bf16 v[204:207], v[200:203], v[196:199], v[204:207]\n" \
"v_mfma_f32_16x16x32_f16 v[52:55], v[152:155], v[164:167], v[52:55]\n" \
"v_pk_add_f16 v162, v162, v166\n" \
"v_pk_add_f16 v163, v163, v167\n" \
"v_and_b32 v186, s92, v186\n" \
"v_and_b32 v187, s92, v187\n" \
"v_mfma_f32_16x16x32_f16 v[64:67], v[156:159], v[164:167], v[64:67]\n" \
"ds_read_b128 v[180:183], v240 offset:8704\n" \
"buffer_store_dwordx4 v[184:187], v234, s[36:39], s82 offen offset:1024 sc1\n" \
"ds_read_b64_tr_b16 v[144:145], v241 offset:512\n" \
"v_mfma_f32_16x16x32_f16 v[56:59], v[136:139], v[160:163], v[56:59]\n" \
"buffer_load_dwordx4 v232, s[36:39], s80 offen offset:2048 sc1 lds\n" \
"ds_read_b64_tr_b16 v[146:147], v241 offset:8704\n" \
"v_mfma_f32_16x16x32_f16 v[68:71], v[140:143], v[160:163], v[68:71]\n" \
"ds_read_b128 v[196:199], v249 offset:3072\n" \
"ds_read_b64_tr_b16 v[200:201], v247 offset:12288\n" \
"ds_read_b64_tr_b16 v[148:149], v242 offset:512\n" \
"v_mfma_f32_16x16x32_f16 v[72:75], v[128:131], v[168:171], v[72:75]\n" \
"ds_read_b128 v[160:163], v240 offset:0\n" \
"ds_read_b64_tr_b16 v[202:203], v247 offset:12800\n" \
"v_mfma_f32_16x16x32_f16 v[84:87], v[132:135], v[168:171], v[84:87]\n" \
"s_waitcnt lgkmcnt(8)\n" \
"v_pk_add_f16 v168, v168, v172\n" \
"v_pk_add_f16 v169, v169, v173\n" \
"v_mfma_f32_16x16x32_f16 v[76:79], v[152:155], v[172:175], v[76:79]\n" \
"v_pk_add_f16 v170, v170, v174\n" \
"v_pk_add_f16 v171, v171, v175\n" \
"v_mfma_f32_16x16x32_f16 v[88:91], v[156:159], v[172:175], v[88:91]\n" \
"ds_read_b128 v[164:167], v240 offset:512\n" \
"ds_read_b64_tr_b16 v[150:151], v242 offset:8704\n" \
"v_mfma_f32_16x16x32_f16 v[80:83], v[136:139], v[168:171], v[80:83]\n" \
"buffer_load_dwordx4 v232, s[36:39], s80 offen offset:3072 sc1 lds\n" \
"v_mfma_f32_16x16x32_f16 v[92:95], v[140:143], v[168:171], v[92:95]\n" \
"v_mfma_f32_16x16x32_f16 v[96:99], v[128:131], v[176:179], v[96:99]\n" \
"s_waitcnt lgkmcnt(8)\n" \
"v_pk_add_f16 v128, v120, v144\n" \
"v_pk_add_f16 v129, v121, v145\n" \
"ds_read_b128 v[168:171], v240 offset:2048\n" \
"s_add_i32 m0, s45, 0x1000\n" \
"s_mov_b32 s83, 0x38000\n" \
"s_waitcnt lgkmcnt(3)\n" \
"v_mfma_f32_16x16x32_bf16 v[204:207], v[200:203], v[196:199], v[204:207]\n" \
"v_mfma_f32_16x16x32_f16 v[108:111], v[132:135], v[176:179], v[108:111]\n" \
"v_pk_add_f16 v176, v176, v180\n" \
"v_pk_add_f16 v177, v177, v181\n" \
"ds_read_b128 v[172:175], v240 offset:2560\n" \
"buffer_load_dwordx4 v233, s[36:39], s83 offen sc1 lds\n" \
"v_mfma_f32_16x16x32_f16 v[100:103], v[152:155], v[180:183], v[100:103]\n" \
"v_pk_add_f16 v178, v178, v182\n" \
"v_pk_add_f16 v179, v179, v183\n" \
"v_mfma_f32_16x16x32_f16 v[112:115], v[156:159], v[180:183], v[112:115]\n" \
"v_pk_add_f16 v130, v122, v146\n" \
"v_pk_add_f16 v131, v123, v147\n" \
"v_mfma_f32_16x16x32_f16 v[104:107], v[136:139], v[176:179], v[104:107]\n" \
"v_pk_add_f16 v132, v124, v148\n" \
"v_pk_add_f16 v133, v125, v149\n" \
"v_mfma_f32_16x16x32_f16 v[116:119], v[140:143], v[176:179], v[116:119]\n" \
"s_waitcnt lgkmcnt(2)\n" \
"v_pk_add_f16 v134, v126, v150\n" \
"v_pk_add_f16 v135, v127, v151\n" \
"s_waitcnt vmcnt(8) lgkmcnt(0)\n" \
"s_barrier\n" \
"v_mfma_f32_16x16x32_f16 v[0:3], v[120:123], v[160:163], v[0:3]\n" \
"ds_read_b128 v[176:179], v240 offset:24576\n" \
"s_mov_b32 s81, 0x8012000\n" \
"ds_read_b64_tr_b16 v[184:185], v236 offset:36864\n" \
"s_add_i32 m0, s45, 0x2000\n" \
"s_mov_b32 s83, 0x40000\n" \
"v_mfma_f32_16x16x32_f16 v[12:15], v[124:127], v[160:163], v[12:15]\n" \
"v_pk_add_f16 v160, v160, v164\n" \
"v_pk_add_f16 v161, v161, v165\n" \
"buffer_load_dword v253, v252, s[36:39], s81 offen sc1\n" \
"buffer_load_dwordx4 v233, s[36:39], s83 offen sc1 lds\n" \
"v_mfma_f32_16x16x32_f16 v[4:7], v[144:147], v[164:167], v[4:7]\n" \
"v_pk_add_f16 v162, v162, v166\n" \
"v_pk_add_f16 v163, v163, v167\n" \
"ds_read_b64_tr_b16 v[188:189], v237 offset:36864\n" \
"v_mfma_f32_16x16x32_f16 v[16:19], v[148:151], v[164:167], v[16:19]\n" \
"ds_read_b128 v[180:183], v240 offset:25088\n" \
"s_add_i32 m0, s44, 0x0\n" \
"s_add_i32 s80, s46, 0x5000000\n" \
"v_mfma_f32_16x16x32_f16 v[8:11], v[128:131], v[160:163], v[8:11]\n" \
"buffer_load_dwordx4 v232, s[36:39], s80 offen sc1 lds\n" \
"s_waitcnt lgkmcnt(2)\n" \
"v_mfma_f32_16x16x16_bf16 v[184:187], v[184:185], v[192:193], 0\n" \
"v_mfma_f32_16x16x32_f16 v[20:23], v[132:135], v[160:163], v[20:23]\n" \
"ds_read_b128 v[196:199], v249 offset:4096\n" \
"ds_read_b64_tr_b16 v[200:201], v247 offset:16384\n" \
"s_waitcnt lgkmcnt(3)\n" \
"v_mfma_f32_16x16x16_bf16 v[188:191], v[188:189], v[192:193], 0\n" \
"v_mfma_f32_16x16x32_f16 v[24:27], v[120:123], v[168:171], v[24:27]\n" \
"ds_read_b128 v[160:163], v240 offset:26624\n" \
"ds_read_b64_tr_b16 v[136:137], v241 offset:16384\n" \
"ds_read_b64_tr_b16 v[138:139], v241 offset:24576\n" \
"v_mfma_f32_16x16x32_f16 v[36:39], v[124:127], v[168:171], v[36:39]\n" \
"v_pk_add_f16 v168, v168, v172\n" \
"v_pk_add_f16 v169, v169, v173\n" \
"ds_read_b64_tr_b16 v[202:203], v247 offset:16896\n" \
"ds_read_b64_tr_b16 v[140:141], v242 offset:16384\n" \
"v_mfma_f32_16x16x32_f16 v[28:31], v[144:147], v[172:175], v[28:31]\n" \
"v_pk_add_f16 v170, v170, v174\n" \
"v_pk_add_f16 v171, v171, v175\n" \
"v_cvt_pk_f16_f32 v184, v184, v185\n" \
"v_mfma_f32_16x16x32_f16 v[40:43], v[148:151], v[172:175], v[40:43]\n" \
"v_cvt_pk_f16_f32 v185, v186, v187\n" \
"v_cvt_pk_f16_f32 v186, v188, v189\n" \
"ds_read_b128 v[164:167], v240 offset:27136\n" \
"v_mfma_f32_16x16x32_f16 v[32:35], v[128:131], v[168:171], v[32:35]\n" \
"v_cvt_pk_f16_f32 v187, v190, v191\n" \
"v_add_u32 v184, s91, v184\n" \
"buffer_load_dwordx4 v232, s[36:39], s80 offen offset:1024 sc1 lds\n" \
"v_mfma_f32_16x16x32_f16 v[44:47], v[132:135], v[168:171], v[44:47]\n" \
"v_add_u32 v185, s91, v185\n" \
"v_add_u32 v186, s91, v186\n" \
"ds_read_b64_tr_b16 v[142:143], v242 offset:24576\n" \
"v_mfma_f32_16x16x32_f16 v[48:51], v[120:123], v[176:179], v[48:51]\n" \
"v_add_u32 v187, s91, v187\n" \
"v_and_b32 v184, s92, v184\n" \
"ds_read_b128 v[168:171], v240 offset:28672\n" \
"v_mfma_f32_16x16x32_f16 v[60:63], v[124:127], v[176:179], v[60:63]\n" \
"s_waitcnt lgkmcnt(5)\n" \
"v_pk_add_f16 v176, v176, v180\n" \
"v_pk_add_f16 v177, v177, v181\n" \
"v_and_b32 v185, s92, v185\n" \
"s_add_i32 s82, s50, 0x7000000\n" \
"s_waitcnt lgkmcnt(3)\n" \
"v_mfma_f32_16x16x32_bf16 v[204:207], v[200:203], v[196:199], v[204:207]\n" \
"v_mfma_f32_16x16x32_f16 v[52:55], v[144:147], v[180:183], v[52:55]\n" \
"v_pk_add_f16 v178, v178, v182\n" \
"v_pk_add_f16 v179, v179, v183\n" \
"v_and_b32 v186, s92, v186\n" \
"v_and_b32 v187, s92, v187\n" \
"v_mfma_f32_16x16x32_f16 v[64:67], v[148:151], v[180:183], v[64:67]\n" \
"ds_read_b128 v[172:175], v240 offset:29184\n" \
"buffer_store_dwordx4 v[184:187], v234, s[36:39], s82 offen sc1\n" \
"ds_read_b64_tr_b16 v[152:153], v241 offset:16896\n" \
"v_mfma_f32_16x16x32_f16 v[56:59], v[128:131], v[176:179], v[56:59]\n" \
"buffer_load_dwordx4 v232, s[36:39], s80 offen offset:2048 sc1 lds\n" \
"ds_read_b64_tr_b16 v[154:155], v241 offset:25088\n" \
"v_mfma_f32_16x16x32_f16 v[68:71], v[132:135], v[176:179], v[68:71]\n" \
"ds_read_b128 v[196:199], v249 offset:5120\n" \
"ds_read_b64_tr_b16 v[200:201], v247 offset:20480\n" \
"ds_read_b64_tr_b16 v[156:157], v242 offset:16896\n" \
"v_mfma_f32_16x16x32_f16 v[72:75], v[120:123], v[160:163], v[72:75]\n" \
"ds_read_b128 v[176:179], v240 offset:20480\n" \
"ds_read_b64_tr_b16 v[202:203], v247 offset:20992\n" \
"v_mfma_f32_16x16x32_f16 v[84:87], v[124:127], v[160:163], v[84:87]\n" \
"s_waitcnt lgkmcnt(8)\n" \
"v_pk_add_f16 v160, v160, v164\n" \
"v_pk_add_f16 v161, v161, v165\n" \
"v_mfma_f32_16x16x32_f16 v[76:79], v[144:147], v[164:167], v[76:79]\n" \
"v_pk_add_f16 v162, v162, v166\n" \
"v_pk_add_f16 v163, v163, v167\n" \
"v_mfma_f32_16x16x32_f16 v[88:91], v[148:151], v[164:167], v[88:91]\n" \
"ds_read_b128 v[180:183], v240 offset:20992\n" \
"ds_read_b64_tr_b16 v[158:159], v242 offset:25088\n" \
"v_mfma_f32_16x16x32_f16 v[80:83], v[128:131], v[160:163], v[80:83]\n" \
"buffer_load_dwordx4 v232, s[36:39], s80 offen offset:3072 sc1 lds\n" \
"v_mfma_f32_16x16x32_f16 v[92:95], v[132:135], v[160:163], v[92:95]\n" \
"v_mfma_f32_16x16x32_f16 v[96:99], v[120:123], v[168:171], v[96:99]\n" \
"s_waitcnt lgkmcnt(8)\n" \
"v_pk_add_f16 v120, v136, v152\n" \
"v_pk_add_f16 v121, v137, v153\n" \
"ds_read_b128 v[160:163], v240 offset:22528\n" \
"s_add_i32 m0, s45, 0x3000\n" \
"s_mov_b32 s83, 0x48000\n" \
"s_waitcnt lgkmcnt(3)\n" \
"v_mfma_f32_16x16x32_bf16 v[204:207], v[200:203], v[196:199], v[204:207]\n" \
"v_mfma_f32_16x16x32_f16 v[108:111], v[124:127], v[168:171], v[108:111]\n" \
"v_pk_add_f16 v168, v168, v172\n" \
"v_pk_add_f16 v169, v169, v173\n" \
"ds_read_b128 v[164:167], v240 offset:23040\n" \
"buffer_load_dwordx4 v233, s[36:39], s83 offen sc1 lds\n" \
"v_mfma_f32_16x16x32_f16 v[100:103], v[144:147], v[172:175], v[100:103]\n" \
"v_pk_add_f16 v170, v170, v174\n" \
"v_pk_add_f16 v171, v171, v175\n" \
"v_mfma_f32_16x16x32_f16 v[112:115], v[148:151], v[172:175], v[112:115]\n" \
"v_pk_add_f16 v122, v138, v154\n" \
"v_pk_add_f16 v123, v139, v155\n" \
"v_mfma_f32_16x16x32_f16 v[104:107], v[128:131], v[168:171], v[104:107]\n" \
"v_pk_add_f16 v124, v140, v156\n" \
"v_pk_add_f16 v125, v141, v157\n" \
"v_mfma_f32_16x16x32_f16 v[116:119], v[132:135], v[168:171], v[116:119]\n" \
"s_waitcnt lgkmcnt(2)\n" \
"v_pk_add_f16 v126, v142, v158\n" \
"v_pk_add_f16 v127, v143, v159\n" \
"s_waitcnt vmcnt(7) lgkmcnt(0)\n" \
"s_barrier\n" \
"v_mfma_f32_16x16x32_f16 v[0:3], v[136:139], v[176:179], v[0:3]\n" \
"ds_read_b128 v[168:171], v240 offset:45056\n" \
"ds_read_b64_tr_b16 v[184:185], v238 offset:36864\n" \
"ds_read_b64_tr_b16 v[188:189], v239 offset:36864\n" \
"s_add_i32 m0, s45, 0x4000\n" \
"s_mov_b32 s83, 0x50000\n" \
"v_mfma_f32_16x16x32_f16 v[12:15], v[140:143], v[176:179], v[12:15]\n" \
"v_pk_add_f16 v176, v176, v180\n" \
"v_pk_add_f16 v177, v177, v181\n" \
"buffer_load_dwordx4 v233, s[36:39], s83 offen sc1 lds\n" \
"v_mfma_f32_16x16x32_f16 v[4:7], v[152:155], v[180:183], v[4:7]\n" \
"v_pk_add_f16 v178, v178, v182\n" \
"v_pk_add_f16 v179, v179, v183\n" \
"v_cmp_eq_u32 vcc, 0, v253\n" \
"s_cbranch_vccnz .Ls09_%=\n" \
".Lr09_%=:\n" \
"v_mfma_f32_16x16x32_f16 v[16:19], v[156:159], v[180:183], v[16:19]\n" \
"ds_read_b128 v[172:175], v240 offset:45568\n" \
"s_add_i32 m0, s44, 0x4000\n" \
"s_add_i32 s80, s46, 0x5400000\n" \
"v_mfma_f32_16x16x32_f16 v[8:11], v[120:123], v[176:179], v[8:11]\n" \
"buffer_load_dwordx4 v232, s[36:39], s80 offen sc1 lds\n" \
"ds_read_b64_tr_b16 v[128:129], v241 offset:32768\n" \
"s_waitcnt lgkmcnt(3)\n" \
"v_mfma_f32_16x16x16_bf16 v[184:187], v[184:185], v[192:193], 0\n" \
"v_mfma_f32_16x16x32_f16 v[20:23], v[124:127], v[176:179], v[20:23]\n" \
"ds_read_b128 v[196:199], v249 offset:6144\n" \
"ds_read_b64_tr_b16 v[200:201], v247 offset:0\n" \
"s_waitcnt lgkmcnt(4)\n" \
"v_mfma_f32_16x16x16_bf16 v[188:191], v[188:189], v[192:193], 0\n" \
"v_mfma_f32_16x16x32_f16 v[24:27], v[136:139], v[160:163], v[24:27]\n" \
"ds_read_b128 v[176:179], v240 offset:47104\n" \
"s_add_i32 s84, s53, 0x8016000\n" \
"ds_read_b64_tr_b16 v[130:131], v241 offset:40960\n" \
"v_mfma_f32_16x16x32_f16 v[36:39], v[140:143], v[160:163], v[36:39]\n" \
"v_pk_add_f16 v160, v160, v164\n" \
"v_pk_add_f16 v161, v161, v165\n" \
"buffer_store_dword v240, v243, s[36:39], s84 offen sc1\n" \
"v_mfma_f32_16x16x32_f16 v[28:31], v[152:155], v[164:167], v[28:31]\n" \
"v_pk_add_f16 v162, v162, v166\n" \
"v_pk_add_f16 v163, v163, v167\n" \
"v_cvt_pk_f16_f32 v184, v184, v185\n" \
"ds_read_b64_tr_b16 v[202:203], v247 offset:512\n" \
"v_mfma_f32_16x16x32_f16 v[40:43], v[156:159], v[164:167], v[40:43]\n" \
"v_cvt_pk_f16_f32 v185, v186, v187\n" \
"v_cvt_pk_f16_f32 v186, v188, v189\n" \
"ds_read_b128 v[180:183], v240 offset:47616\n" \
"ds_read_b64_tr_b16 v[132:133], v242 offset:32768\n" \
"v_mfma_f32_16x16x32_f16 v[32:35], v[120:123], v[160:163], v[32:35]\n" \
"v_cvt_pk_f16_f32 v187, v190, v191\n" \
"v_add_u32 v184, s91, v184\n" \
"buffer_load_dwordx4 v232, s[36:39], s80 offen offset:1024 sc1 lds\n" \
"v_mfma_f32_16x16x32_f16 v[44:47], v[124:127], v[160:163], v[44:47]\n" \
"v_add_u32 v185, s91, v185\n" \
"v_add_u32 v186, s91, v186\n" \
"ds_read_b64_tr_b16 v[134:135], v242 offset:40960\n" \
"v_mfma_f32_16x16x32_f16 v[48:51], v[136:139], v[168:171], v[48:51]\n" \
"v_add_u32 v187, s91, v187\n" \
"v_and_b32 v184, s92, v184\n" \
"ds_read_b128 v[160:163], v240 offset:49152\n" \
"v_mfma_f32_16x16x32_f16 v[60:63], v[140:143], v[168:171], v[60:63]\n" \
"s_waitcnt lgkmcnt(5)\n" \
"v_pk_add_f16 v168, v168, v172\n" \
"v_pk_add_f16 v169, v169, v173\n" \
"v_and_b32 v185, s92, v185\n" \
"s_waitcnt lgkmcnt(4)\n" \
"v_mfma_f32_16x16x32_bf16 v[204:207], v[200:203], v[196:199], v[204:207]\n" \
"v_mfma_f32_16x16x32_f16 v[52:55], v[152:155], v[172:175], v[52:55]\n" \
"v_pk_add_f16 v170, v170, v174\n" \
"v_pk_add_f16 v171, v171, v175\n" \
"v_and_b32 v186, s92, v186\n" \
"v_and_b32 v187, s92, v187\n" \
"v_mfma_f32_16x16x32_f16 v[64:67], v[156:159], v[172:175], v[64:67]\n" \
"ds_read_b128 v[164:167], v240 offset:49664\n" \
"buffer_store_dwordx4 v[184:187], v234, s[36:39], s82 offen offset:1024 sc1\n" \
"ds_read_b64_tr_b16 v[144:145], v241 offset:33280\n" \
"v_mfma_f32_16x16x32_f16 v[56:59], v[120:123], v[168:171], v[56:59]\n" \
"buffer_load_dwordx4 v232, s[36:39], s80 offen offset:2048 sc1 lds\n" \
"ds_read_b64_tr_b16 v[146:147], v241 offset:41472\n" \
"v_mfma_f32_16x16x32_f16 v[68:71], v[124:127], v[168:171], v[68:71]\n" \
"ds_read_b128 v[196:199], v249 offset:7168\n" \
"ds_read_b64_tr_b16 v[200:201], v247 offset:4096\n" \
"ds_read_b64_tr_b16 v[148:149], v242 offset:33280\n" \
"v_mfma_f32_16x16x32_f16 v[72:75], v[136:139], v[176:179], v[72:75]\n" \
"ds_read_b128 v[168:171], v240 offset:40960\n" \
"ds_read_b64_tr_b16 v[202:203], v247 offset:4608\n" \
"v_mfma_f32_16x16x32_f16 v[84:87], v[140:143], v[176:179], v[84:87]\n" \
"s_waitcnt lgkmcnt(8)\n" \
"v_pk_add_f16 v176, v176, v180\n" \
"v_pk_add_f16 v177, v177, v181\n" \
"v_mfma_f32_16x16x32_f16 v[76:79], v[152:155], v[180:183], v[76:79]\n" \
"v_pk_add_f16 v178, v178, v182\n" \
"v_pk_add_f16 v179, v179, v183\n" \
"v_mfma_f32_16x16x32_f16 v[88:91], v[156:159], v[180:183], v[88:91]\n" \
"ds_read_b128 v[172:175], v240 offset:41472\n" \
"ds_read_b64_tr_b16 v[150:151], v242 offset:41472\n" \
"v_mfma_f32_16x16x32_f16 v[80:83], v[120:123], v[176:179], v[80:83]\n" \
"buffer_load_dwordx4 v232, s[36:39], s80 offen offset:3072 sc1 lds\n" \
"v_mfma_f32_16x16x32_f16 v[92:95], v[124:127], v[176:179], v[92:95]\n" \
"v_mfma_f32_16x16x32_f16 v[96:99], v[136:139], v[160:163], v[96:99]\n" \
"s_waitcnt lgkmcnt(8)\n" \
"v_pk_add_f16 v136, v128, v144\n" \
"v_pk_add_f16 v137, v129, v145\n" \
"ds_read_b128 v[176:179], v240 offset:43008\n" \
"s_add_i32 m0, s45, 0x5000\n" \
"s_mov_b32 s83, 0x58000\n" \
"s_waitcnt lgkmcnt(3)\n" \
"v_mfma_f32_16x16x32_bf16 v[204:207], v[200:203], v[196:199], v[204:207]\n" \
"v_mfma_f32_16x16x32_f16 v[108:111], v[140:143], v[160:163], v[108:111]\n" \
"v_pk_add_f16 v160, v160, v164\n" \
"v_pk_add_f16 v161, v161, v165\n" \
"ds_read_b128 v[180:183], v240 offset:43520\n" \
"buffer_load_dwordx4 v233, s[36:39], s83 offen sc1 lds\n" \
"v_mfma_f32_16x16x32_f16 v[100:103], v[152:155], v[164:167], v[100:103]\n" \
"v_pk_add_f16 v162, v162, v166\n" \
"v_pk_add_f16 v163, v163, v167\n" \
"v_mfma_f32_16x16x32_f16 v[112:115], v[156:159], v[164:167], v[112:115]\n" \
"v_pk_add_f16 v138, v130, v146\n" \
"v_pk_add_f16 v139, v131, v147\n" \
"v_mfma_f32_16x16x32_f16 v[104:107], v[120:123], v[160:163], v[104:107]\n" \
"v_pk_add_f16 v140, v132, v148\n" \
"v_pk_add_f16 v141, v133, v149\n" \
"v_mfma_f32_16x16x32_f16 v[116:119], v[124:127], v[160:163], v[116:119]\n" \
"s_waitcnt lgkmcnt(2)\n" \
"v_pk_add_f16 v142, v134, v150\n" \
"v_pk_add_f16 v143, v135, v151\n" \
"s_waitcnt vmcnt(8) lgkmcnt(0)\n" \
"s_barrier\n" \
"v_mfma_f32_16x16x32_f16 v[0:3], v[128:131], v[168:171], v[0:3]\n" \
"ds_read_b128 v[160:163], v240 offset:4096\n" \
"ds_read_b64_tr_b16 v[184:185], v236 offset:40960\n" \
"s_add_i32 m0, s45, 0x0\n" \
"s_mov_b32 s83, 0x60000\n" \
"v_mfma_f32_16x16x32_f16 v[12:15], v[132:135], v[168:171], v[12:15]\n" \
"v_pk_add_f16 v168, v168, v172\n" \
"v_pk_add_f16 v169, v169, v173\n" \
"ds_read_b64_tr_b16 v[188:189], v237 offset:40960\n" \
"buffer_load_dwordx4 v233, s[36:39], s83 offen sc1 lds\n" \
"v_mfma_f32_16x16x32_f16 v[4:7], v[144:147], v[172:175], v[4:7]\n" \
"v_pk_add_f16 v170, v170, v174\n" \
"v_pk_add_f16 v171, v171, v175\n" \
"v_mfma_f32_16x16x32_f16 v[16:19], v[148:151], v[172:175], v[16:19]\n" \
"ds_read_b128 v[164:167], v240 offset:4608\n" \
"s_add_i32 m0, s44, 0x8000\n" \
"s_add_i32 s80, s46, 0x5800000\n" \
"v_mfma_f32_16x16x32_f16 v[8:11], v[136:139], v[168:171], v[8:11]\n" \
"buffer_load_dwordx4 v232, s[36:39], s80 offen sc1 lds\n" \
"v_mfma_f32_16x16x32_f16 v[20:23], v[140:143], v[168:171], v[20:23]\n" \
"ds_read_b128 v[196:199], v249 offset:0\n" \
"ds_read_b64_tr_b16 v[200:201], v247 offset:8192\n" \
"s_waitcnt lgkmcnt(4)\n" \
"v_mfma_f32_16x16x16_bf16 v[184:187], v[184:185], v[192:193], 0\n" \
"v_mfma_f32_16x16x32_f16 v[24:27], v[128:131], v[176:179], v[24:27]\n" \
"ds_read_b128 v[168:171], v240 offset:6144\n" \
"ds_read_b64_tr_b16 v[120:121], v241 offset:0\n" \
"ds_read_b64_tr_b16 v[122:123], v241 offset:8192\n" \
"s_waitcnt lgkmcnt(6)\n" \
"v_mfma_f32_16x16x16_bf16 v[188:191], v[188:189], v[192:193], 0\n" \
"v_mfma_f32_16x16x32_f16 v[36:39], v[132:135], v[176:179], v[36:39]\n" \
"v_pk_add_f16 v176, v176, v180\n" \
"v_pk_add_f16 v177, v177, v181\n" \
"ds_read_b64_tr_b16 v[202:203], v247 offset:8704\n" \
"v_mfma_f32_16x16x32_f16 v[28:31], v[144:147], v[180:183], v[28:31]\n" \
"v_pk_add_f16 v178, v178, v182\n" \
"v_pk_add_f16 v179, v179, v183\n" \
"ds_read_b64_tr_b16 v[124:125], v242 offset:0\n" \
"v_mfma_f32_16x16x32_f16 v[40:43], v[148:151], v[180:183], v[40:43]\n" \
"v_cvt_pk_f16_f32 v184, v184, v185\n" \
"ds_read_b128 v[172:175], v240 offset:6656\n" \
"ds_read_b64_tr_b16 v[126:127], v242 offset:8192\n" \
"v_mfma_f32_16x16x32_f16 v[32:35], v[136:139], v[176:179], v[32:35]\n" \
"v_cvt_pk_f16_f32 v185, v186, v187\n" \
"v_cvt_pk_f16_f32 v186, v188, v189\n" \
"buffer_load_dwordx4 v232, s[36:39], s80 offen offset:1024 sc1 lds\n" \
"v_mfma_f32_16x16x32_f16 v[44:47], v[140:143], v[176:179], v[44:47]\n" \
"v_cvt_pk_f16_f32 v187, v190, v191\n" \
"v_add_u32 v184, s91, v184\n" \
"v_mfma_f32_16x16x32_f16 v[48:51], v[128:131], v[160:163], v[48:51]\n" \
"v_add_u32 v185, s91, v185\n" \
"v_add_u32 v186, s91, v186\n" \
"ds_read_b128 v[176:179], v240 offset:8192\n" \
"v_mfma_f32_16x16x32_f16 v[60:63], v[132:135], v[160:163], v[60:63]\n" \
"s_waitcnt lgkmcnt(5)\n" \
"v_pk_add_f16 v160, v160, v164\n" \
"v_pk_add_f16 v161, v161, v165\n" \
"v_add_u32 v187, s91, v187\n" \
"v_and_b32 v184, s92, v184\n" \
"s_add_i32 s82, s51, 0x7800000\n" \
"s_waitcnt lgkmcnt(4)\n" \
"v_mfma_f32_16x16x32_bf16 v[204:207], v[200:203], v[196:199], v[204:207]\n" \
"v_mfma_f32_16x16x32_f16 v[52:55], v[144:147], v[164:167], v[52:55]\n" \
"v_pk_add_f16 v162, v162, v166\n" \
"v_pk_add_f16 v163, v163, v167\n" \
"v_and_b32 v185, s92, v185\n" \
"v_and_b32 v186, s92, v186\n" \
"v_and_b32 v187, s92, v187\n" \
"v_mfma_f32_16x16x32_f16 v[64:67], v[148:151], v[164:167], v[64:67]\n" \
"ds_read_b128 v[180:183], v240 offset:8704\n" \
"buffer_store_dwordx4 v[184:187], v234, s[36:39], s82 offen sc1\n" \
"ds_read_b64_tr_b16 v[152:153], v241 offset:512\n" \
"v_mfma_f32_16x16x32_f16 v[56:59], v[136:139], v[160:163], v[56:59]\n" \
"buffer_load_dwordx4 v232, s[36:39], s80 offen offset:2048 sc1 lds\n" \
"ds_read_b128 v[196:199], v249 offset:1024\n" \
"v_mfma_f32_16x16x32_f16 v[68:71], v[140:143], v[160:163], v[68:71]\n" \
"ds_read_b64_tr_b16 v[184:185], v238 offset:40960\n" \
"ds_read_b64_tr_b16 v[188:189], v239 offset:40960\n" \
"ds_read_b64_tr_b16 v[154:155], v241 offset:8704\n" \
"v_mfma_f32_16x16x32_f16 v[72:75], v[128:131], v[168:171], v[72:75]\n" \
"ds_read_b128 v[160:163], v240 offset:0\n" \
"ds_read_b64_tr_b16 v[200:201], v247 offset:12288\n" \
"ds_read_b64_tr_b16 v[202:203], v247 offset:12800\n" \
"v_mfma_f32_16x16x32_f16 v[84:87], v[132:135], v[168:171], v[84:87]\n" \
"s_waitcnt lgkmcnt(9)\n" \
"v_pk_add_f16 v168, v168, v172\n" \
"v_pk_add_f16 v169, v169, v173\n" \
"ds_read_b64_tr_b16 v[156:157], v242 offset:512\n" \
"v_mfma_f32_16x16x32_f16 v[76:79], v[144:147], v[172:175], v[76:79]\n" \
"v_pk_add_f16 v170, v170, v174\n" \
"v_pk_add_f16 v171, v171, v175\n" \
"s_waitcnt lgkmcnt(6)\n" \
"v_mfma_f32_16x16x16_bf16 v[184:187], v[184:185], v[192:193], 0\n" \
"v_mfma_f32_16x16x32_f16 v[88:91], v[148:151], v[172:175], v[88:91]\n" \
"ds_read_b128 v[164:167], v240 offset:512\n" \
"ds_read_b64_tr_b16 v[158:159], v242 offset:8704\n" \
"s_waitcnt lgkmcnt(7)\n" \
"v_mfma_f32_16x16x16_bf16 v[188:191], v[188:189], v[192:193], 0\n" \
"v_mfma_f32_16x16x32_f16 v[80:83], v[136:139], v[168:171], v[80:83]\n" \
"buffer_load_dwordx4 v232, s[36:39], s80 offen offset:3072 sc1 lds\n" \
"v_mfma_f32_16x16x32_f16 v[92:95], v[140:143], v[168:171], v[92:95]\n" \
"v_mfma_f32_16x16x32_f16 v[96:99], v[128:131], v[176:179], v[96:99]\n" \
"v_pk_add_f16 v128, v120, v152\n" \
"v_pk_add_f16 v129, v121, v153\n" \
"v_cvt_pk_f16_f32 v184, v184, v185\n" \
"v_cvt_pk_f16_f32 v185, v186, v187\n" \
"ds_read_b128 v[168:171], v240 offset:2048\n" \
"s_add_i32 m0, s45, 0x1000\n" \
"s_mov_b32 s83, 0x68000\n" \
"s_waitcnt lgkmcnt(4)\n" \
"v_mfma_f32_16x16x32_bf16 v[204:207], v[200:203], v[196:199], v[204:207]\n" \
"v_mfma_f32_16x16x32_f16 v[108:111], v[132:135], v[176:179], v[108:111]\n" \
"v_pk_add_f16 v176, v176, v180\n" \
"v_pk_add_f16 v177, v177, v181\n" \
"v_cvt_pk_f16_f32 v186, v188, v189\n" \
"v_cvt_pk_f16_f32 v187, v190, v191\n" \
"ds_read_b128 v[172:175], v240 offset:2560\n" \
"buffer_load_dwordx4 v233, s[36:39], s83 offen sc1 lds\n" \
"v_mfma_f32_16x16x32_f16 v[100:103], v[144:147], v[180:183], v[100:103]\n" \
"v_pk_add_f16 v178, v178, v182\n" \
"v_pk_add_f16 v179, v179, v183\n" \
"v_add_u32 v184, s91, v184\n" \
"v_add_u32 v185, s91, v185\n" \
"v_add_u32 v186, s91, v186\n" \
"v_mfma_f32_16x16x32_f16 v[112:115], v[148:151], v[180:183], v[112:115]\n" \
"v_pk_add_f16 v130, v122, v154\n" \
"v_pk_add_f16 v131, v123, v155\n" \
"v_add_u32 v187, s91, v187\n" \
"v_and_b32 v184, s92, v184\n" \
"v_mfma_f32_16x16x32_f16 v[104:107], v[136:139], v[176:179], v[104:107]\n" \
"s_waitcnt lgkmcnt(1)\n" \
"v_pk_add_f16 v132, v124, v156\n" \
"v_pk_add_f16 v133, v125, v157\n" \
"v_and_b32 v185, s92, v185\n" \
"v_and_b32 v186, s92, v186\n" \
"v_and_b32 v187, s92, v187\n" \
"v_mfma_f32_16x16x32_f16 v[116:119], v[140:143], v[176:179], v[116:119]\n" \
"v_pk_add_f16 v134, v126, v158\n" \
"v_pk_add_f16 v135, v127, v159\n" \
"buffer_store_dwordx4 v[184:187], v234, s[36:39], s82 offen offset:1024 sc1\n" \
"s_waitcnt vmcnt(8) lgkmcnt(0)\n" \
"s_barrier\n" \
"v_mfma_f32_16x16x32_f16 v[0:3], v[120:123], v[160:163], v[0:3]\n" \
"ds_read_b128 v[176:179], v240 offset:24576\n" \
"s_add_i32 m0, s45, 0x2000\n" \
"s_mov_b32 s83, 0x70000\n" \
"v_mfma_f32_16x16x32_f16 v[12:15], v[124:127], v[160:163], v[12:15]\n" \
"v_pk_add_f16 v160, v160, v164\n" \
"v_pk_add_f16 v161, v161, v165\n" \
"buffer_load_dwordx4 v233, s[36:39], s83 offen sc1 lds\n" \
"v_mfma_f32_16x16x32_f16 v[4:7], v[152:155], v[164:167], v[4:7]\n" \
"v_pk_add_f16 v162, v162, v166\n" \
"v_pk_add_f16 v163, v163, v167\n" \
"v_mfma_f32_16x16x32_f16 v[16:19], v[156:159], v[164:167], v[16:19]\n" \
"ds_read_b128 v[180:183], v240 offset:25088\n" \
"s_add_i32 m0, s44, 0x0\n" \
"s_add_i32 s80, s46, 0x5c00000\n" \
"v_mfma_f32_16x16x32_f16 v[8:11], v[128:131], v[160:163], v[8:11]\n" \
"buffer_load_dwordx4 v232, s[36:39], s80 offen sc1 lds\n" \
"ds_read_b64_tr_b16 v[136:137], v241 offset:16384\n" \
"v_mfma_f32_16x16x32_f16 v[20:23], v[132:135], v[160:163], v[20:23]\n" \
"ds_read_b128 v[196:199], v249 offset:2048\n" \
"ds_read_b64_tr_b16 v[200:201], v247 offset:16384\n" \
"v_mfma_f32_16x16x32_f16 v[24:27], v[120:123], v[168:171], v[24:27]\n" \
"ds_read_b128 v[160:163], v240 offset:26624\n" \
"s_add_i32 s84, s54, 0x8018000\n" \
"ds_read_b64_tr_b16 v[138:139], v241 offset:24576\n" \
"v_mfma_f32_16x16x32_f16 v[36:39], v[124:127], v[168:171], v[36:39]\n" \
"v_pk_add_f16 v168, v168, v172\n" \
"v_pk_add_f16 v169, v169, v173\n" \
"buffer_store_dword v240, v243, s[36:39], s84 offen sc1\n" \
"v_mfma_f32_16x16x32_f16 v[28:31], v[152:155], v[172:175], v[28:31]\n" \
"v_pk_add_f16 v170, v170, v174\n" \
"v_pk_add_f16 v171, v171, v175\n" \
"v_mfma_f32_16x16x32_f16 v[40:43], v[156:159], v[172:175], v[40:43]\n" \
"ds_read_b128 v[164:167], v240 offset:27136\n" \
"ds_read_b64_tr_b16 v[202:203], v247 offset:16896\n" \
"v_mfma_f32_16x16x32_f16 v[32:35], v[128:131], v[168:171], v[32:35]\n" \
"buffer_load_dwordx4 v232, s[36:39], s80 offen offset:1024 sc1 lds\n" \
"ds_read_b64_tr_b16 v[140:141], v242 offset:16384\n" \
"v_mfma_f32_16x16x32_f16 v[44:47], v[132:135], v[168:171], v[44:47]\n" \
"ds_read_b64_tr_b16 v[142:143], v242 offset:24576\n" \
"s_waitcnt lgkmcnt(9)\n" \
"v_mfma_f32_16x16x32_f16 v[48:51], v[120:123], v[176:179], v[48:51]\n" \
"ds_read_b128 v[168:171], v240 offset:28672\n" \
"ds_read_b64_tr_b16 v[144:145], v241 offset:16896\n" \
"v_mfma_f32_16x16x32_f16 v[60:63], v[124:127], v[176:179], v[60:63]\n" \
"v_pk_add_f16 v176, v176, v180\n" \
"v_pk_add_f16 v177, v177, v181\n" \
"s_waitcnt lgkmcnt(4)\n" \
"v_mfma_f32_16x16x32_bf16 v[204:207], v[200:203], v[196:199], v[204:207]\n" \
"v_mfma_f32_16x16x32_f16 v[52:55], v[152:155], v[180:183], v[52:55]\n" \
"v_pk_add_f16 v178, v178, v182\n" \
"v_pk_add_f16 v179, v179, v183\n" \
"v_mfma_f32_16x16x32_f16 v[64:67], v[156:159], v[180:183], v[64:67]\n" \
"ds_read_b128 v[172:175], v240 offset:29184\n" \
"ds_read_b64_tr_b16 v[146:147], v241 offset:25088\n" \
"v_mfma_f32_16x16x32_f16 v[56:59], v[128:131], v[176:179], v[56:59]\n" \
"buffer_load_dwordx4 v232, s[36:39], s80 offen offset:2048 sc1 lds\n" \
"ds_read_b64_tr_b16 v[148:149], v242 offset:16896\n" \
"v_mfma_f32_16x16x32_f16 v[68:71], v[132:135], v[176:179], v[68:71]\n" \
"ds_read_b128 v[196:199], v249 offset:3072\n" \
"ds_read_b64_tr_b16 v[200:201], v247 offset:20480\n" \
"v_mfma_f32_16x16x32_f16 v[72:75], v[120:123], v[160:163], v[72:75]\n" \
"ds_read_b128 v[176:179], v240 offset:20480\n" \
"ds_read_b64_tr_b16 v[202:203], v247 offset:20992\n" \
"v_mfma_f32_16x16x32_f16 v[84:87], v[124:127], v[160:163], v[84:87]\n" \
"v_pk_add_f16 v160, v160, v164\n" \
"v_pk_add_f16 v161, v161, v165\n" \
"v_mfma_f32_16x16x32_f16 v[76:79], v[152:155], v[164:167], v[76:79]\n" \
"v_pk_add_f16 v162, v162, v166\n" \
"v_pk_add_f16 v163, v163, v167\n" \
"v_mfma_f32_16x16x32_f16 v[88:91], v[156:159], v[164:167], v[88:91]\n" \
"ds_read_b128 v[180:183], v240 offset:20992\n" \
"ds_read_b64_tr_b16 v[150:151], v242 offset:25088\n" \
"v_mfma_f32_16x16x32_f16 v[80:83], v[128:131], v[160:163], v[80:83]\n" \
"buffer_load_dwordx4 v232, s[36:39], s80 offen offset:3072 sc1 lds\n" \
"v_mfma_f32_16x16x32_f16 v[92:95], v[132:135], v[160:163], v[92:95]\n" \
"s_waitcnt lgkmcnt(9)\n" \
"v_mfma_f32_16x16x32_f16 v[96:99], v[120:123], v[168:171], v[96:99]\n" \
"v_pk_add_f16 v120, v136, v144\n" \
"v_pk_add_f16 v121, v137, v145\n" \
"ds_read_b128 v[160:163], v240 offset:22528\n" \
"s_add_i32 m0, s45, 0x3000\n" \
"s_mov_b32 s83, 0x78000\n" \
"s_waitcnt lgkmcnt(3)\n" \
"v_mfma_f32_16x16x32_bf16 v[204:207], v[200:203], v[196:199], v[204:207]\n" \
"v_mfma_f32_16x16x32_f16 v[108:111], v[124:127], v[168:171], v[108:111]\n" \
"v_pk_add_f16 v168, v168, v172\n" \
"v_pk_add_f16 v169, v169, v173\n" \
"ds_read_b128 v[164:167], v240 offset:23040\n" \
"buffer_load_dwordx4 v233, s[36:39], s83 offen sc1 lds\n" \
"v_mfma_f32_16x16x32_f16 v[100:103], v[152:155], v[172:175], v[100:103]\n" \
"v_pk_add_f16 v170, v170, v174\n" \
"v_pk_add_f16 v171, v171, v175\n" \
"v_mfma_f32_16x16x32_f16 v[112:115], v[156:159], v[172:175], v[112:115]\n" \
"v_pk_add_f16 v122, v138, v146\n" \
"v_pk_add_f16 v123, v139, v147\n" \
"v_mfma_f32_16x16x32_f16 v[104:107], v[128:131], v[168:171], v[104:107]\n" \
"v_pk_add_f16 v124, v140, v148\n" \
"v_pk_add_f16 v125, v141, v149\n" \
"v_mfma_f32_16x16x32_f16 v[116:119], v[132:135], v[168:171], v[116:119]\n" \
"s_waitcnt lgkmcnt(2)\n" \
"v_pk_add_f16 v126, v142, v150\n" \
"v_pk_add_f16 v127, v143, v151\n" \
"s_waitcnt vmcnt(7) lgkmcnt(0)\n" \
"s_barrier\n" \
"v_mfma_f32_16x16x32_f16 v[0:3], v[136:139], v[176:179], v[0:3]\n" \
"ds_read_b128 v[168:171], v240 offset:45056\n" \
"s_mov_b32 s81, 0x8016000\n" \
"v_mfma_f32_16x16x32_f16 v[12:15], v[140:143], v[176:179], v[12:15]\n" \
"v_pk_add_f16 v176, v176, v180\n" \
"v_pk_add_f16 v177, v177, v181\n" \
"buffer_load_dword v253, v252, s[36:39], s81 offen sc1\n" \
"v_mfma_f32_16x16x32_f16 v[4:7], v[144:147], v[180:183], v[4:7]\n" \
"v_pk_add_f16 v178, v178, v182\n" \
"v_pk_add_f16 v179, v179, v183\n" \
"v_mfma_f32_16x16x32_f16 v[16:19], v[148:151], v[180:183], v[16:19]\n" \
"ds_read_b128 v[172:175], v240 offset:45568\n" \
"s_add_i32 m0, s44, 0x4000\n" \
"s_add_i32 s80, s46, 0x6000000\n" \
"v_mfma_f32_16x16x32_f16 v[8:11], v[120:123], v[176:179], v[8:11]\n" \
"buffer_load_dwordx4 v232, s[36:39], s80 offen sc1 lds\n" \
"ds_read_b64_tr_b16 v[128:129], v241 offset:32768\n" \
"v_mfma_f32_16x16x32_f16 v[20:23], v[124:127], v[176:179], v[20:23]\n" \
"ds_read_b128 v[196:199], v249 offset:4096\n" \
"ds_read_b64_tr_b16 v[200:201], v247 offset:0\n" \
"v_mfma_f32_16x16x32_f16 v[24:27], v[136:139], v[160:163], v[24:27]\n" \
"ds_read_b128 v[176:179], v240 offset:47104\n" \
"s_add_i32 s84, s55, 0x801a000\n" \
"ds_read_b64_tr_b16 v[130:131], v241 offset:40960\n" \
"v_mfma_f32_16x16x32_f16 v[36:39], v[140:143], v[160:163], v[36:39]\n" \
"v_pk_add_f16 v160, v160, v164\n" \
"v_pk_add_f16 v161, v161, v165\n" \
"buffer_store_dword v240, v243, s[36:39], s84 offen sc1\n" \
"v_mfma_f32_16x16x32_f16 v[28:31], v[144:147], v[164:167], v[28:31]\n" \
"v_pk_add_f16 v162, v162, v166\n" \
"v_pk_add_f16 v163, v163, v167\n" \
"v_mfma_f32_16x16x32_f16 v[40:43], v[148:151], v[164:167], v[40:43]\n" \
"ds_read_b128 v[180:183], v240 offset:47616\n" \
"ds_read_b64_tr_b16 v[202:203], v247 offset:512\n" \
"v_mfma_f32_16x16x32_f16 v[32:35], v[120:123], v[160:163], v[32:35]\n" \
"buffer_load_dwordx4 v232, s[36:39], s80 offen offset:1024 sc1 lds\n" \
"ds_read_b64_tr_b16 v[132:133], v242 offset:32768\n" \
"v_mfma_f32_16x16x32_f16 v[44:47], v[124:127], v[160:163], v[44:47]\n" \
"ds_read_b64_tr_b16 v[134:135], v242 offset:40960\n" \
"s_waitcnt lgkmcnt(9)\n" \
"v_mfma_f32_16x16x32_f16 v[48:51], v[136:139], v[168:171], v[48:51]\n" \
"ds_read_b128 v[160:163], v240 offset:49152\n" \
"ds_read_b64_tr_b16 v[152:153], v241 offset:33280\n" \
"v_mfma_f32_16x16x32_f16 v[60:63], v[140:143], v[168:171], v[60:63]\n" \
"v_pk_add_f16 v168, v168, v172\n" \
"v_pk_add_f16 v169, v169, v173\n" \
"s_waitcnt lgkmcnt(4)\n" \
"v_mfma_f32_16x16x32_bf16 v[204:207], v[200:203], v[196:199], v[204:207]\n" \
"v_mfma_f32_16x16x32_f16 v[52:55], v[144:147], v[172:175], v[52:55]\n" \
"v_pk_add_f16 v170, v170, v174\n" \
"v_pk_add_f16 v171, v171, v175\n" \
"v_mfma_f32_16x16x32_f16 v[64:67], v[148:151], v[172:175], v[64:67]\n" \
"ds_read_b128 v[164:167], v240 offset:49664\n" \
"ds_read_b64_tr_b16 v[154:155], v241 offset:41472\n" \
"v_mfma_f32_16x16x32_f16 v[56:59], v[120:123], v[168:171], v[56:59]\n" \
"buffer_load_dwordx4 v232, s[36:39], s80 offen offset:2048 sc1 lds\n" \
"ds_read_b64_tr_b16 v[156:157], v242 offset:33280\n" \
"v_mfma_f32_16x16x32_f16 v[68:71], v[124:127], v[168:171], v[68:71]\n" \
"ds_read_b128 v[196:199], v249 offset:5120\n" \
"ds_read_b64_tr_b16 v[200:201], v247 offset:4096\n" \
"v_mfma_f32_16x16x32_f16 v[72:75], v[136:139], v[176:179], v[72:75]\n" \
"ds_read_b128 v[168:171], v240 offset:40960\n" \
"ds_read_b64_tr_b16 v[202:203], v247 offset:4608\n" \
"v_mfma_f32_16x16x32_f16 v[84:87], v[140:143], v[176:179], v[84:87]\n" \
"v_pk_add_f16 v176, v176, v180\n" \
"v_pk_add_f16 v177, v177, v181\n" \
"v_mfma_f32_16x16x32_f16 v[76:79], v[144:147], v[180:183], v[76:79]\n" \
"v_pk_add_f16 v178, v178, v182\n" \
"v_pk_add_f16 v179, v179, v183\n" \
"v_mfma_f32_16x16x32_f16 v[88:91], v[148:151], v[180:183], v[88:91]\n" \
"ds_read_b128 v[172:175], v240 offset:41472\n" \
"ds_read_b64_tr_b16 v[158:159], v242 offset:41472\n" \
"v_mfma_f32_16x16x32_f16 v[80:83], v[120:123], v[176:179], v[80:83]\n" \
"buffer_load_dwordx4 v232, s[36:39], s80 offen offset:3072 sc1 lds\n" \
"v_mfma_f32_16x16x32_f16 v[92:95], v[124:127], v[176:179], v[92:95]\n" \
"s_waitcnt lgkmcnt(9)\n" \
"v_mfma_f32_16x16x32_f16 v[96:99], v[136:139], v[160:163], v[96:99]\n" \
"v_pk_add_f16 v136, v128, v152\n" \
"v_pk_add_f16 v137, v129, v153\n" \
"ds_read_b128 v[176:179], v240 offset:43008\n" \
"s_waitcnt lgkmcnt(3)\n" \
"v_mfma_f32_16x16x32_bf16 v[204:207], v[200:203], v[196:199], v[204:207]\n" \
"v_mfma_f32_16x16x32_f16 v[108:111], v[140:143], v[160:163], v[108:111]\n" \
"v_pk_add_f16 v160, v160, v164\n" \
"v_pk_add_f16 v161, v161, v165\n" \
"ds_read_b128 v[180:183], v240 offset:43520\n" \
"v_mfma_f32_16x16x32_f16 v[100:103], v[144:147], v[164:167], v[100:103]\n" \
"v_pk_add_f16 v162, v162, v166\n" \
"v_pk_add_f16 v163, v163, v167\n" \
"v_mfma_f32_16x16x32_f16 v[112:115], v[148:151], v[164:167], v[112:115]\n" \
"v_pk_add_f16 v138, v130, v154\n" \
"v_pk_add_f16 v139, v131, v155\n" \
"v_mfma_f32_16x16x32_f16 v[104:107], v[120:123], v[160:163], v[104:107]\n" \
"v_pk_add_f16 v140, v132, v156\n" \
"v_pk_add_f16 v141, v133, v157\n" \
"v_mfma_f32_16x16x32_f16 v[116:119], v[124:127], v[160:163], v[116:119]\n" \
"s_waitcnt lgkmcnt(2)\n" \
"v_pk_add_f16 v142, v134, v158\n" \
"v_pk_add_f16 v143, v135, v159\n" \
"s_waitcnt vmcnt(5) lgkmcnt(0)\n" \
"s_barrier\n" \
"v_mfma_f32_16x16x32_f16 v[0:3], v[128:131], v[168:171], v[0:3]\n" \
"ds_read_b128 v[160:163], v240 offset:4096\n" \
"v_mfma_f32_16x16x32_f16 v[12:15], v[132:135], v[168:171], v[12:15]\n" \
"v_pk_add_f16 v168, v168, v172\n" \
"v_pk_add_f16 v169, v169, v173\n" \
"v_mfma_f32_16x16x32_f16 v[4:7], v[152:155], v[172:175], v[4:7]\n" \
"v_pk_add_f16 v170, v170, v174\n" \
"v_pk_add_f16 v171, v171, v175\n" \
"v_cmp_eq_u32 vcc, 0, v253\n" \
"s_cbranch_vccnz .Ls011_%=\n" \
".Lr011_%=:\n" \
"v_mfma_f32_16x16x32_f16 v[16:19], v[156:159], v[172:175], v[16:19]\n" \
"ds_read_b128 v[164:167], v240 offset:4608\n" \
"s_add_i32 m0, s44, 0x8000\n" \
"s_add_i32 s80, s46, 0x6400000\n" \
"v_mfma_f32_16x16x32_f16 v[8:11], v[136:139], v[168:171], v[8:11]\n" \
"buffer_load_dwordx4 v232, s[36:39], s80 offen sc1 lds\n" \
"v_mfma_f32_16x16x32_f16 v[20:23], v[140:143], v[168:171], v[20:23]\n" \
"ds_read_b128 v[196:199], v249 offset:6144\n" \
"ds_read_b64_tr_b16 v[200:201], v247 offset:8192\n" \
"v_mfma_f32_16x16x32_f16 v[24:27], v[128:131], v[176:179], v[24:27]\n" \
"ds_read_b128 v[168:171], v240 offset:6144\n" \
"ds_read_b64_tr_b16 v[120:121], v241 offset:0\n" \
"ds_read_b64_tr_b16 v[122:123], v241 offset:8192\n" \
"v_mfma_f32_16x16x32_f16 v[36:39], v[132:135], v[176:179], v[36:39]\n" \
"v_pk_add_f16 v176, v176, v180\n" \
"v_pk_add_f16 v177, v177, v181\n" \
"v_mfma_f32_16x16x32_f16 v[28:31], v[152:155], v[180:183], v[28:31]\n" \
"v_pk_add_f16 v178, v178, v182\n" \
"v_pk_add_f16 v179, v179, v183\n" \
"v_mfma_f32_16x16x32_f16 v[40:43], v[156:159], v[180:183], v[40:43]\n" \
"ds_read_b128 v[172:175], v240 offset:6656\n" \
"ds_read_b64_tr_b16 v[202:203], v247 offset:8704\n" \
"v_mfma_f32_16x16x32_f16 v[32:35], v[136:139], v[176:179], v[32:35]\n" \
"buffer_load_dwordx4 v232, s[36:39], s80 offen offset:1024 sc1 lds\n" \
"ds_read_b64_tr_b16 v[124:125], v242 offset:0\n" \
"v_mfma_f32_16x16x32_f16 v[44:47], v[140:143], v[176:179], v[44:47]\n" \
"ds_read_b64_tr_b16 v[126:127], v242 offset:8192\n" \
"s_waitcnt lgkmcnt(9)\n" \
"v_mfma_f32_16x16x32_f16 v[48:51], v[128:131], v[160:163], v[48:51]\n" \
"ds_read_b128 v[176:179], v240 offset:8192\n" \
"ds_read_b64_tr_b16 v[144:145], v241 offset:512\n" \
"v_mfma_f32_16x16x32_f16 v[60:63], v[132:135], v[160:163], v[60:63]\n" \
"v_pk_add_f16 v160, v160, v164\n" \
"v_pk_add_f16 v161, v161, v165\n" \
"s_waitcnt lgkmcnt(4)\n" \
"v_mfma_f32_16x16x32_bf16 v[204:207], v[200:203], v[196:199], v[204:207]\n" \
"v_mfma_f32_16x16x32_f16 v[52:55], v[152:155], v[164:167], v[52:55]\n" \
"v_pk_add_f16 v162, v162, v166\n" \
"v_pk_add_f16 v163, v163, v167\n" \
"v_mfma_f32_16x16x32_f16 v[64:67], v[156:159], v[164:167], v[64:67]\n" \
"ds_read_b128 v[180:183], v240 offset:8704\n" \
"ds_read_b64_tr_b16 v[146:147], v241 offset:8704\n" \
"v_mfma_f32_16x16x32_f16 v[56:59], v[136:139], v[160:163], v[56:59]\n" \
"buffer_load_dwordx4 v232, s[36:39], s80 offen offset:2048 sc1 lds\n" \
"ds_read_b64_tr_b16 v[148:149], v242 offset:512\n" \
"v_mfma_f32_16x16x32_f16 v[68:71], v[140:143], v[160:163], v[68:71]\n" \
"ds_read_b128 v[196:199], v249 offset:7168\n" \
"ds_read_b64_tr_b16 v[200:201], v247 offset:12288\n" \
"v_mfma_f32_16x16x32_f16 v[72:75], v[128:131], v[168:171], v[72:75]\n" \
"ds_read_b128 v[160:163], v240 offset:0\n" \
"ds_read_b64_tr_b16 v[202:203], v247 offset:12800\n" \
"v_mfma_f32_16x16x32_f16 v[84:87], v[132:135], v[168:171], v[84:87]\n" \
"v_pk_add_f16 v168, v168, v172\n" \
"v_pk_add_f16 v169, v169, v173\n" \
"v_mfma_f32_16x16x32_f16 v[76:79], v[152:155], v[172:175], v[76:79]\n" \
"v_pk_add_f16 v170, v170, v174\n" \
"v_pk_add_f16 v171, v171, v175\n" \
"v_mfma_f32_16x16x32_f16 v[88:91], v[156:159], v[172:175], v[88:91]\n" \
"ds_read_b128 v[164:167], v240 offset:512\n" \
"ds_read_b64_tr_b16 v[150:151], v242 offset:8704\n" \
"v_mfma_f32_16x16x32_f16 v[80:83], v[136:139], v[168:171], v[80:83]\n" \
"buffer_load_dwordx4 v232, s[36:39], s80 offen offset:3072 sc1 lds\n" \
"v_mfma_f32_16x16x32_f16 v[92:95], v[140:143], v[168:171], v[92:95]\n" \
"s_waitcnt lgkmcnt(9)\n" \
"v_mfma_f32_16x16x32_f16 v[96:99], v[128:131], v[176:179], v[96:99]\n" \
"v_pk_add_f16 v128, v120, v144\n" \
"v_pk_add_f16 v129, v121, v145\n" \
"ds_read_b128 v[168:171], v240 offset:2048\n" \
"s_waitcnt lgkmcnt(3)\n" \
"v_mfma_f32_16x16x32_bf16 v[204:207], v[200:203], v[196:199], v[204:207]\n" \
"v_mfma_f32_16x16x32_f16 v[108:111], v[132:135], v[176:179], v[108:111]\n" \
"v_pk_add_f16 v176, v176, v180\n" \
"v_pk_add_f16 v177, v177, v181\n" \
"ds_read_b128 v[172:175], v240 offset:2560\n" \
"v_mfma_f32_16x16x32_f16 v[100:103], v[152:155], v[180:183], v[100:103]\n" \
"v_pk_add_f16 v178, v178, v182\n" \
"v_pk_add_f16 v179, v179, v183\n" \
"v_mfma_f32_16x16x32_f16 v[112:115], v[156:159], v[180:183], v[112:115]\n" \
"v_pk_add_f16 v130, v122, v146\n" \
"v_pk_add_f16 v131, v123, v147\n" \
"v_mfma_f32_16x16x32_f16 v[104:107], v[136:139], v[176:179], v[104:107]\n" \
"v_pk_add_f16 v132, v124, v148\n" \
"v_pk_add_f16 v133, v125, v149\n" \
"v_mfma_f32_16x16x32_f16 v[116:119], v[140:143], v[176:179], v[116:119]\n" \
"s_waitcnt lgkmcnt(2)\n" \
"v_pk_add_f16 v134, v126, v150\n" \
"v_pk_add_f16 v135, v127, v151\n" \
"s_waitcnt vmcnt(4) lgkmcnt(0)\n" \
"s_barrier\n" \
"v_mfma_f32_16x16x32_f16 v[0:3], v[120:123], v[160:163], v[0:3]\n" \
"ds_read_b128 v[176:179], v240 offset:24576\n" \
"s_mov_b32 s81, 0x8018000\n" \
"v_mfma_f32_16x16x32_f16 v[12:15], v[124:127], v[160:163], v[12:15]\n" \
"v_pk_add_f16 v160, v160, v164\n" \
"v_pk_add_f16 v161, v161, v165\n" \
"buffer_load_dword v253, v252, s[36:39], s81 offen sc1\n" \
"v_mfma_f32_16x16x32_f16 v[4:7], v[144:147], v[164:167], v[4:7]\n" \
"v_pk_add_f16 v162, v162, v166\n" \
"v_pk_add_f16 v163, v163, v167\n" \
"v_mfma_f32_16x16x32_f16 v[16:19], v[148:151], v[164:167], v[16:19]\n" \
"ds_read_b128 v[180:183], v240 offset:25088\n" \
"s_add_i32 m0, s44, 0x0\n" \
"s_add_i32 s80, s46, 0x6800000\n" \
"v_mfma_f32_16x16x32_f16 v[8:11], v[128:131], v[160:163], v[8:11]\n" \
"buffer_load_dwordx4 v232, s[36:39], s80 offen sc1 lds\n" \
"v_mfma_f32_16x16x32_f16 v[20:23], v[132:135], v[160:163], v[20:23]\n" \
"ds_write_b128 v248, v[204:207]\n" \
"ds_read_b64_tr_b16 v[136:137], v241 offset:16384\n" \
"v_mfma_f32_16x16x32_f16 v[24:27], v[120:123], v[168:171], v[24:27]\n" \
"ds_read_b128 v[160:163], v240 offset:26624\n" \
"ds_read_b64_tr_b16 v[138:139], v241 offset:24576\n" \
"v_mfma_f32_16x16x32_f16 v[36:39], v[124:127], v[168:171], v[36:39]\n" \
"v_pk_add_f16 v168, v168, v172\n" \
"v_pk_add_f16 v169, v169, v173\n" \
"v_mfma_f32_16x16x32_f16 v[28:31], v[144:147], v[172:175], v[28:31]\n" \
"v_pk_add_f16 v170, v170, v174\n" \
"v_pk_add_f16 v171, v171, v175\n" \
"v_mfma_f32_16x16x32_f16 v[40:43], v[148:151], v[172:175], v[40:43]\n" \
"ds_read_b128 v[164:167], v240 offset:27136\n" \
"ds_read_b64_tr_b16 v[140:141], v242 offset:16384\n" \
"v_mfma_f32_16x16x32_f16 v[32:35], v[128:131], v[168:171], v[32:35]\n" \
"buffer_load_dwordx4 v232, s[36:39], s80 offen offset:1024 sc1 lds\n" \
"v_mfma_f32_16x16x32_f16 v[44:47], v[132:135], v[168:171], v[44:47]\n" \
"ds_read_b64_tr_b16 v[142:143], v242 offset:24576\n" \
"s_waitcnt lgkmcnt(7)\n" \
"v_mfma_f32_16x16x32_f16 v[48:51], v[120:123], v[176:179], v[48:51]\n" \
"ds_read_b128 v[168:171], v240 offset:28672\n" \
"ds_read_b64_tr_b16 v[152:153], v241 offset:16896\n" \
"v_mfma_f32_16x16x32_f16 v[60:63], v[124:127], v[176:179], v[60:63]\n" \
"v_pk_add_f16 v176, v176, v180\n" \
"v_pk_add_f16 v177, v177, v181\n" \
"v_mfma_f32_16x16x32_f16 v[52:55], v[144:147], v[180:183], v[52:55]\n" \
"v_pk_add_f16 v178, v178, v182\n" \
"v_pk_add_f16 v179, v179, v183\n" \
"v_mfma_f32_16x16x32_f16 v[64:67], v[148:151], v[180:183], v[64:67]\n" \
"ds_read_b128 v[172:175], v240 offset:29184\n" \
"v_mfma_f32_16x16x32_f16 v[56:59], v[128:131], v[176:179], v[56:59]\n" \
"buffer_load_dwordx4 v232, s[36:39], s80 offen offset:2048 sc1 lds\n" \
"v_mfma_f32_16x16x32_f16 v[68:71], v[132:135], v[176:179], v[68:71]\n" \
"ds_read_b64_tr_b16 v[154:155], v241 offset:25088\n" \
"ds_read_b64_tr_b16 v[156:157], v242 offset:16896\n" \
"s_waitcnt lgkmcnt(8)\n" \
"v_mfma_f32_16x16x32_f16 v[72:75], v[120:123], v[160:163], v[72:75]\n" \
"ds_read_b128 v[176:179], v240 offset:20480\n" \
"ds_read_b64_tr_b16 v[158:159], v242 offset:25088\n" \
"v_mfma_f32_16x16x32_f16 v[84:87], v[124:127], v[160:163], v[84:87]\n" \
"s_waitcnt lgkmcnt(8)\n" \
"v_pk_add_f16 v160, v160, v164\n" \
"v_pk_add_f16 v161, v161, v165\n" \
"v_mfma_f32_16x16x32_f16 v[76:79], v[144:147], v[164:167], v[76:79]\n" \
"v_pk_add_f16 v162, v162, v166\n" \
"v_pk_add_f16 v163, v163, v167\n" \
"v_mfma_f32_16x16x32_f16 v[88:91], v[148:151], v[164:167], v[88:91]\n" \
"ds_read_b128 v[180:183], v240 offset:20992\n" \
"v_mfma_f32_16x16x32_f16 v[80:83], v[128:131], v[160:163], v[80:83]\n" \
"buffer_load_dwordx4 v232, s[36:39], s80 offen offset:3072 sc1 lds\n" \
"v_mfma_f32_16x16x32_f16 v[92:95], v[132:135], v[160:163], v[92:95]\n" \
"s_waitcnt lgkmcnt(6)\n" \
"v_mfma_f32_16x16x32_f16 v[96:99], v[120:123], v[168:171], v[96:99]\n" \
"v_pk_add_f16 v120, v136, v152\n" \
"v_pk_add_f16 v121, v137, v153\n" \
"ds_read_b128 v[160:163], v240 offset:22528\n" \
"v_mfma_f32_16x16x32_f16 v[108:111], v[124:127], v[168:171], v[108:111]\n" \
"s_waitcnt lgkmcnt(5)\n" \
"v_pk_add_f16 v168, v168, v172\n" \
"v_pk_add_f16 v169, v169, v173\n" \
"ds_read_b128 v[164:167], v240 offset:23040\n" \
"v_mfma_f32_16x16x32_f16 v[100:103], v[144:147], v[172:175], v[100:103]\n" \
"v_pk_add_f16 v170, v170, v174\n" \
"v_pk_add_f16 v171, v171, v175\n" \
"v_mfma_f32_16x16x32_f16 v[112:115], v[148:151], v[172:175], v[112:115]\n" \
"v_pk_add_f16 v122, v138, v154\n" \
"v_pk_add_f16 v123, v139, v155\n" \
"v_mfma_f32_16x16x32_f16 v[104:107], v[128:131], v[168:171], v[104:107]\n" \
"s_waitcnt lgkmcnt(3)\n" \
"v_pk_add_f16 v124, v140, v156\n" \
"v_pk_add_f16 v125, v141, v157\n" \
"v_mfma_f32_16x16x32_f16 v[116:119], v[132:135], v[168:171], v[116:119]\n" \
"v_pk_add_f16 v126, v142, v158\n" \
"v_pk_add_f16 v127, v143, v159\n" \
".Lcf_t:\n" \
"s_waitcnt vmcnt(4) lgkmcnt(0)\n" \
"s_barrier\n" \
"v_mfma_f32_16x16x32_f16 v[0:3], v[136:139], v[176:179], v[0:3]\n" \
"ds_read_b128 v[168:171], v240 offset:45056\n" \
"v_mfma_f32_16x16x32_f16 v[12:15], v[140:143], v[176:179], v[12:15]\n" \
"v_pk_add_f16 v176, v176, v180\n" \
"v_pk_add_f16 v177, v177, v181\n" \
"v_mfma_f32_16x16x32_f16 v[4:7], v[152:155], v[180:183], v[4:7]\n" \
"v_pk_add_f16 v178, v178, v182\n" \
"v_pk_add_f16 v179, v179, v183\n" \
"v_cmp_eq_u32 vcc, 0, v253\n" \
"s_cbranch_vccnz .Ls012_%=\n" \
".Lr012_%=:\n" \
"s_mov_b32 s81, 0x801a000\n" \
"v_mfma_f32_16x16x32_f16 v[16:19], v[156:159], v[180:183], v[16:19]\n" \
"ds_read_b128 v[172:175], v240 offset:45568\n" \
"buffer_load_dword v253, v252, s[36:39], s81 offen sc1\n" \
"s_add_i32 m0, s44, 0x4000\n" \
"s_add_i32 s80, s46, 0x6c00000\n" \
"v_mfma_f32_16x16x32_f16 v[8:11], v[120:123], v[176:179], v[8:11]\n" \
"buffer_load_dwordx4 v232, s[36:39], s80 offen sc1 lds\n" \
"v_mfma_f32_16x16x32_f16 v[20:23], v[124:127], v[176:179], v[20:23]\n" \
"ds_read_b64_tr_b16 v[128:129], v241 offset:32768\n" \
"ds_read_b64_tr_b16 v[130:131], v241 offset:40960\n" \
"v_mfma_f32_16x16x32_f16 v[24:27], v[136:139], v[160:163], v[24:27]\n" \
"ds_read_b128 v[176:179], v240 offset:47104\n" \
"v_mfma_f32_16x16x32_f16 v[36:39], v[140:143], v[160:163], v[36:39]\n" \
"v_pk_add_f16 v160, v160, v164\n" \
"v_pk_add_f16 v161, v161, v165\n" \
"v_mfma_f32_16x16x32_f16 v[28:31], v[152:155], v[164:167], v[28:31]\n" \
"v_pk_add_f16 v162, v162, v166\n" \
"v_pk_add_f16 v163, v163, v167\n" \
"v_mfma_f32_16x16x32_f16 v[40:43], v[156:159], v[164:167], v[40:43]\n" \
"ds_read_b128 v[180:183], v240 offset:47616\n" \
"ds_read_b64_tr_b16 v[132:133], v242 offset:32768\n" \
"v_mfma_f32_16x16x32_f16 v[32:35], v[120:123], v[160:163], v[32:35]\n" \
"buffer_load_dwordx4 v232, s[36:39], s80 offen offset:1024 sc1 lds\n" \
"v_mfma_f32_16x16x32_f16 v[44:47], v[124:127], v[160:163], v[44:47]\n" \
"ds_read_b64_tr_b16 v[134:135], v242 offset:40960\n" \
"s_waitcnt lgkmcnt(6)\n" \
"v_mfma_f32_16x16x32_f16 v[48:51], v[136:139], v[168:171], v[48:51]\n" \
"ds_read_b128 v[160:163], v240 offset:49152\n" \
"ds_read_b64_tr_b16 v[144:145], v241 offset:33280\n" \
"v_mfma_f32_16x16x32_f16 v[60:63], v[140:143], v[168:171], v[60:63]\n" \
"v_pk_add_f16 v168, v168, v172\n" \
"v_pk_add_f16 v169, v169, v173\n" \
"v_mfma_f32_16x16x32_f16 v[52:55], v[152:155], v[172:175], v[52:55]\n" \
"v_pk_add_f16 v170, v170, v174\n" \
"v_pk_add_f16 v171, v171, v175\n" \
"v_mfma_f32_16x16x32_f16 v[64:67], v[156:159], v[172:175], v[64:67]\n" \
"ds_read_b128 v[164:167], v240 offset:49664\n" \
"v_mfma_f32_16x16x32_f16 v[56:59], v[120:123], v[168:171], v[56:59]\n" \
"buffer_load_dwordx4 v232, s[36:39], s80 offen offset:2048 sc1 lds\n" \
"v_mfma_f32_16x16x32_f16 v[68:71], v[124:127], v[168:171], v[68:71]\n" \
"ds_read_b64_tr_b16 v[146:147], v241 offset:41472\n" \
"ds_read_b64_tr_b16 v[148:149], v242 offset:33280\n" \
"s_waitcnt lgkmcnt(8)\n" \
"v_mfma_f32_16x16x32_f16 v[72:75], v[136:139], v[176:179], v[72:75]\n" \
"ds_read_b128 v[168:171], v240 offset:40960\n" \
"ds_read_b64_tr_b16 v[150:151], v242 offset:41472\n" \
"v_mfma_f32_16x16x32_f16 v[84:87], v[140:143], v[176:179], v[84:87]\n" \
"s_waitcnt lgkmcnt(8)\n" \
"v_pk_add_f16 v176, v176, v180\n" \
"v_pk_add_f16 v177, v177, v181\n" \
"v_mfma_f32_16x16x32_f16 v[76:79], v[152:155], v[180:183], v[76:79]\n" \
"v_pk_add_f16 v178, v178, v182\n" \
"v_pk_add_f16 v179, v179, v183\n" \
"v_mfma_f32_16x16x32_f16 v[88:91], v[156:159], v[180:183], v[88:91]\n" \
"ds_read_b128 v[172:175], v240 offset:41472\n" \
"v_mfma_f32_16x16x32_f16 v[80:83], v[120:123], v[176:179], v[80:83]\n" \
"buffer_load_dwordx4 v232, s[36:39], s80 offen offset:3072 sc1 lds\n" \
"v_mfma_f32_16x16x32_f16 v[92:95], v[124:127], v[176:179], v[92:95]\n" \
"s_waitcnt lgkmcnt(6)\n" \
"v_mfma_f32_16x16x32_f16 v[96:99], v[136:139], v[160:163], v[96:99]\n" \
"v_pk_add_f16 v136, v128, v144\n" \
"v_pk_add_f16 v137, v129, v145\n" \
"ds_read_b128 v[176:179], v240 offset:43008\n" \
"v_mfma_f32_16x16x32_f16 v[108:111], v[140:143], v[160:163], v[108:111]\n" \
"s_waitcnt lgkmcnt(5)\n" \
"v_pk_add_f16 v160, v160, v164\n" \
"v_pk_add_f16 v161, v161, v165\n" \
"ds_read_b128 v[180:183], v240 offset:43520\n" \
"v_mfma_f32_16x16x32_f16 v[100:103], v[152:155], v[164:167], v[100:103]\n" \
"v_pk_add_f16 v162, v162, v166\n" \
"v_pk_add_f16 v163, v163, v167\n" \
"v_mfma_f32_16x16x32_f16 v[112:115], v[156:159], v[164:167], v[112:115]\n" \
"v_pk_add_f16 v138, v130, v146\n" \
"v_pk_add_f16 v139, v131, v147\n" \
"v_mfma_f32_16x16x32_f16 v[104:107], v[120:123], v[160:163], v[104:107]\n" \
"s_waitcnt lgkmcnt(3)\n" \
"v_pk_add_f16 v140, v132, v148\n" \
"v_pk_add_f16 v141, v133, v149\n" \
"v_mfma_f32_16x16x32_f16 v[116:119], v[124:127], v[160:163], v[116:119]\n" \
"v_pk_add_f16 v142, v134, v150\n" \
"v_pk_add_f16 v143, v135, v151\n" \
"s_waitcnt vmcnt(4) lgkmcnt(0)\n" \
"s_barrier\n" \
"v_mfma_f32_16x16x32_f16 v[0:3], v[128:131], v[168:171], v[0:3]\n" \
"ds_read_b128 v[160:163], v240 offset:4096\n" \
"v_mfma_f32_16x16x32_f16 v[12:15], v[132:135], v[168:171], v[12:15]\n" \
"v_pk_add_f16 v168, v168, v172\n" \
"v_pk_add_f16 v169, v169, v173\n" \
"s_add_i32 m0, s44, 0x8000\n" \
"s_add_i32 s80, s46, 0x7000000\n" \
"v_mfma_f32_16x16x32_f16 v[4:7], v[144:147], v[172:175], v[4:7]\n" \
"v_pk_add_f16 v170, v170, v174\n" \
"v_pk_add_f16 v171, v171, v175\n" \
"buffer_load_dwordx4 v232, s[36:39], s80 offen sc1 lds\n" \
"v_mfma_f32_16x16x32_f16 v[16:19], v[148:151], v[172:175], v[16:19]\n" \
"ds_read_b128 v[164:167], v240 offset:4608\n" \
"v_mfma_f32_16x16x32_f16 v[8:11], v[136:139], v[168:171], v[8:11]\n" \
"buffer_load_dwordx4 v232, s[36:39], s80 offen offset:1024 sc1 lds\n" \
"v_mfma_f32_16x16x32_f16 v[20:23], v[140:143], v[168:171], v[20:23]\n" \
"ds_read_b64_tr_b16 v[120:121], v241 offset:0\n" \
"ds_read_b64_tr_b16 v[122:123], v241 offset:8192\n" \
"v_mfma_f32_16x16x32_f16 v[24:27], v[128:131], v[176:179], v[24:27]\n" \
"ds_read_b128 v[168:171], v240 offset:6144\n" \
"buffer_load_dwordx4 v232, s[36:39], s80 offen offset:2048 sc1 lds\n" \
"v_mfma_f32_16x16x32_f16 v[36:39], v[132:135], v[176:179], v[36:39]\n" \
"v_pk_add_f16 v176, v176, v180\n" \
"v_pk_add_f16 v177, v177, v181\n" \
"ds_read_b64_tr_b16 v[124:125], v242 offset:0\n" \
"v_mfma_f32_16x16x32_f16 v[28:31], v[144:147], v[180:183], v[28:31]\n" \
"v_pk_add_f16 v178, v178, v182\n" \
"v_pk_add_f16 v179, v179, v183\n" \
"v_mfma_f32_16x16x32_f16 v[40:43], v[148:151], v[180:183], v[40:43]\n" \
"ds_read_b128 v[172:175], v240 offset:6656\n" \
"buffer_load_dwordx4 v232, s[36:39], s80 offen offset:3072 sc1 lds\n" \
"v_mfma_f32_16x16x32_f16 v[32:35], v[136:139], v[176:179], v[32:35]\n" \
"v_cmp_eq_u32 vcc, 0, v253\n" \
"s_cbranch_vccnz .Ls013_%=\n" \
".Lr013_%=:\n" \
"v_mfma_f32_16x16x32_f16 v[44:47], v[140:143], v[176:179], v[44:47]\n" \
"s_add_i32 m0, s44, 0x1b000\n" \
"s_add_i32 s80, s46, 0x7400000\n" \
"ds_read_b64_tr_b16 v[126:127], v242 offset:8192\n" \
"s_waitcnt lgkmcnt(6)\n" \
"v_mfma_f32_16x16x32_f16 v[48:51], v[128:131], v[160:163], v[48:51]\n" \
"ds_read_b128 v[176:179], v240 offset:8192\n" \
"buffer_load_dwordx4 v232, s[36:39], s80 offen sc1 lds\n" \
"v_mfma_f32_16x16x32_f16 v[60:63], v[132:135], v[160:163], v[60:63]\n" \
"v_pk_add_f16 v160, v160, v164\n" \
"v_pk_add_f16 v161, v161, v165\n" \
"v_mfma_f32_16x16x32_f16 v[52:55], v[144:147], v[164:167], v[52:55]\n" \
"v_pk_add_f16 v162, v162, v166\n" \
"v_pk_add_f16 v163, v163, v167\n" \
"buffer_load_dwordx4 v232, s[36:39], s80 offen offset:1024 sc1 lds\n" \
"v_mfma_f32_16x16x32_f16 v[64:67], v[148:151], v[164:167], v[64:67]\n" \
"ds_read_b128 v[180:183], v240 offset:8704\n" \
"ds_read_b64_tr_b16 v[152:153], v241 offset:512\n" \
"v_mfma_f32_16x16x32_f16 v[56:59], v[136:139], v[160:163], v[56:59]\n" \
"buffer_load_dwordx4 v232, s[36:39], s80 offen offset:2048 sc1 lds\n" \
"v_mfma_f32_16x16x32_f16 v[68:71], v[140:143], v[160:163], v[68:71]\n" \
"ds_read_b64_tr_b16 v[154:155], v241 offset:8704\n" \
"ds_read_b64_tr_b16 v[156:157], v242 offset:512\n" \
"s_waitcnt lgkmcnt(6)\n" \
"v_mfma_f32_16x16x32_f16 v[72:75], v[128:131], v[168:171], v[72:75]\n" \
"ds_read_b128 v[160:163], v240 offset:0\n" \
"buffer_load_dwordx4 v232, s[36:39], s80 offen offset:3072 sc1 lds\n" \
"v_mfma_f32_16x16x32_f16 v[84:87], v[132:135], v[168:171], v[84:87]\n" \
"v_pk_add_f16 v168, v168, v172\n" \
"v_pk_add_f16 v169, v169, v173\n" \
"ds_read_b64_tr_b16 v[158:159], v242 offset:8704\n" \
"v_mfma_f32_16x16x32_f16 v[76:79], v[144:147], v[172:175], v[76:79]\n" \
"v_pk_add_f16 v170, v170, v174\n" \
"v_pk_add_f16 v171, v171, v175\n" \
"s_add_i32 m0, s44, 0x1f000\n" \
"s_add_i32 s80, s46, 0x7800000\n" \
"v_mfma_f32_16x16x32_f16 v[88:91], v[148:151], v[172:175], v[88:91]\n" \
"ds_read_b128 v[164:167], v240 offset:512\n" \
"buffer_load_dwordx4 v232, s[36:39], s80 offen sc1 lds\n" \
"v_mfma_f32_16x16x32_f16 v[80:83], v[136:139], v[168:171], v[80:83]\n" \
"v_mfma_f32_16x16x32_f16 v[92:95], v[140:143], v[168:171], v[92:95]\n" \
"buffer_load_dwordx4 v232, s[36:39], s80 offen offset:1024 sc1 lds\n" \
"s_waitcnt lgkmcnt(5)\n" \
"v_mfma_f32_16x16x32_f16 v[96:99], v[128:131], v[176:179], v[96:99]\n" \
"v_pk_add_f16 v128, v120, v152\n" \
"v_pk_add_f16 v129, v121, v153\n" \
"ds_read_b128 v[168:171], v240 offset:2048\n" \
"v_mfma_f32_16x16x32_f16 v[108:111], v[132:135], v[176:179], v[108:111]\n" \
"v_pk_add_f16 v176, v176, v180\n" \
"v_pk_add_f16 v177, v177, v181\n" \
"ds_read_b128 v[172:175], v240 offset:2560\n" \
"buffer_load_dwordx4 v232, s[36:39], s80 offen offset:2048 sc1 lds\n" \
"v_mfma_f32_16x16x32_f16 v[100:103], v[144:147], v[180:183], v[100:103]\n" \
"v_pk_add_f16 v178, v178, v182\n" \
"v_pk_add_f16 v179, v179, v183\n" \
"v_mfma_f32_16x16x32_f16 v[112:115], v[148:151], v[180:183], v[112:115]\n" \
"s_waitcnt lgkmcnt(3)\n" \
"v_pk_add_f16 v130, v122, v154\n" \
"v_pk_add_f16 v131, v123, v155\n" \
"buffer_load_dwordx4 v232, s[36:39], s80 offen offset:3072 sc1 lds\n" \
"v_mfma_f32_16x16x32_f16 v[104:107], v[136:139], v[176:179], v[104:107]\n" \
"v_pk_add_f16 v132, v124, v156\n" \
"v_pk_add_f16 v133, v125, v157\n" \
"v_mfma_f32_16x16x32_f16 v[116:119], v[140:143], v[176:179], v[116:119]\n" \
"v_pk_add_f16 v134, v126, v158\n" \
"v_pk_add_f16 v135, v127, v159\n" \
"s_waitcnt vmcnt(12) lgkmcnt(0)\n" \
"s_barrier\n" \
"v_mfma_f32_16x16x32_f16 v[0:3], v[120:123], v[160:163], v[0:3]\n" \
"ds_read_b128 v[176:179], v240 offset:24576\n" \
"v_mfma_f32_16x16x32_f16 v[12:15], v[124:127], v[160:163], v[12:15]\n" \
"v_pk_add_f16 v160, v160, v164\n" \
"v_pk_add_f16 v161, v161, v165\n" \
"s_lshr_b32 m0, s44, 1\n" \
"v_mfma_f32_16x16x32_f16 v[4:7], v[152:155], v[164:167], v[4:7]\n" \
"v_pk_add_f16 v162, v162, v166\n" \
"v_pk_add_f16 v163, v163, v167\n" \
"v_add_u32 v252, m0, v244\n" \
"v_mfma_f32_16x16x32_f16 v[16:19], v[156:159], v[164:167], v[16:19]\n" \
"ds_read_b128 v[180:183], v240 offset:25088\n" \
"v_mfma_f32_16x16x32_f16 v[8:11], v[128:131], v[160:163], v[8:11]\n" \
"global_load_lds_dwordx4 v252, s[76:77]\n" \
"v_mfma_f32_16x16x32_f16 v[20:23], v[132:135], v[160:163], v[20:23]\n" \
"ds_read_b64_tr_b16 v[136:137], v241 offset:16384\n" \
"ds_read_b64_tr_b16 v[138:139], v241 offset:24576\n" \
"v_mfma_f32_16x16x32_f16 v[24:27], v[120:123], v[168:171], v[24:27]\n" \
"ds_read_b128 v[160:163], v240 offset:26624\n" \
"v_mfma_f32_16x16x32_f16 v[36:39], v[124:127], v[168:171], v[36:39]\n" \
"v_pk_add_f16 v168, v168, v172\n" \
"v_pk_add_f16 v169, v169, v173\n" \
"v_mfma_f32_16x16x32_f16 v[28:31], v[152:155], v[172:175], v[28:31]\n" \
"v_pk_add_f16 v170, v170, v174\n" \
"v_pk_add_f16 v171, v171, v175\n" \
"v_mfma_f32_16x16x32_f16 v[40:43], v[156:159], v[172:175], v[40:43]\n" \
"ds_read_b128 v[164:167], v240 offset:27136\n" \
"ds_read_b64_tr_b16 v[140:141], v242 offset:16384\n" \
"v_mfma_f32_16x16x32_f16 v[32:35], v[128:131], v[168:171], v[32:35]\n" \
"global_load_lds_dwordx4 v252, s[76:77] offset:1024\n" \
"v_mfma_f32_16x16x32_f16 v[44:47], v[132:135], v[168:171], v[44:47]\n" \
"ds_read_b64_tr_b16 v[142:143], v242 offset:24576\n" \
"s_waitcnt lgkmcnt(7)\n" \
"v_mfma_f32_16x16x32_f16 v[48:51], v[120:123], v[176:179], v[48:51]\n" \
"ds_read_b128 v[168:171], v240 offset:28672\n" \
"ds_read_b64_tr_b16 v[144:145], v241 offset:16896\n" \
"v_mfma_f32_16x16x32_f16 v[60:63], v[124:127], v[176:179], v[60:63]\n" \
"s_waitcnt lgkmcnt(8)\n" \
"v_pk_add_f16 v176, v176, v180\n" \
"v_pk_add_f16 v177, v177, v181\n" \
"v_mfma_f32_16x16x32_f16 v[52:55], v[152:155], v[180:183], v[52:55]\n" \
"v_pk_add_f16 v178, v178, v182\n" \
"v_pk_add_f16 v179, v179, v183\n" \
"v_mfma_f32_16x16x32_f16 v[64:67], v[156:159], v[180:183], v[64:67]\n" \
"ds_read_b128 v[172:175], v240 offset:29184\n" \
"v_mfma_f32_16x16x32_f16 v[56:59], v[128:131], v[176:179], v[56:59]\n" \
"ds_read_b64_tr_b16 v[146:147], v241 offset:25088\n" \
"v_mfma_f32_16x16x32_f16 v[68:71], v[132:135], v[176:179], v[68:71]\n" \
"ds_read_b64_tr_b16 v[148:149], v242 offset:16896\n" \
"s_waitcnt lgkmcnt(8)\n" \
"v_mfma_f32_16x16x32_f16 v[72:75], v[120:123], v[160:163], v[72:75]\n" \
"ds_read_b128 v[176:179], v240 offset:20480\n" \
"ds_read_b64_tr_b16 v[150:151], v242 offset:25088\n" \
"v_mfma_f32_16x16x32_f16 v[84:87], v[124:127], v[160:163], v[84:87]\n" \
"s_waitcnt lgkmcnt(8)\n" \
"v_pk_add_f16 v160, v160, v164\n" \
"v_pk_add_f16 v161, v161, v165\n" \
"v_mfma_f32_16x16x32_f16 v[76:79], v[152:155], v[164:167], v[76:79]\n" \
"v_pk_add_f16 v162, v162, v166\n" \
"v_pk_add_f16 v163, v163, v167\n" \
"v_add_u32 v243, 0x1b000, v241\n" \
"v_mfma_f32_16x16x32_f16 v[88:91], v[156:159], v[164:167], v[88:91]\n" \
"v_xor_b32 v244, 32, v243\n" \
"ds_read_b128 v[180:183], v240 offset:20992\n" \
"v_mfma_f32_16x16x32_f16 v[80:83], v[128:131], v[160:163], v[80:83]\n" \
"v_mfma_f32_16x16x32_f16 v[92:95], v[132:135], v[160:163], v[92:95]\n" \
"v_add_u32 v246, 0x17000, v240\n" \
"s_waitcnt lgkmcnt(6)\n" \
"v_mfma_f32_16x16x32_f16 v[96:99], v[120:123], v[168:171], v[96:99]\n" \
"v_pk_add_f16 v120, v136, v144\n" \
"v_pk_add_f16 v121, v137, v145\n" \
"ds_read_b128 v[160:163], v240 offset:22528\n" \
"v_mfma_f32_16x16x32_f16 v[108:111], v[124:127], v[168:171], v[108:111]\n" \
"s_waitcnt lgkmcnt(4)\n" \
"v_pk_add_f16 v168, v168, v172\n" \
"v_pk_add_f16 v169, v169, v173\n" \
"ds_read_b128 v[164:167], v240 offset:23040\n" \
"v_mfma_f32_16x16x32_f16 v[100:103], v[152:155], v[172:175], v[100:103]\n" \
"v_pk_add_f16 v170, v170, v174\n" \
"v_pk_add_f16 v171, v171, v175\n" \
"v_mfma_f32_16x16x32_f16 v[112:115], v[156:159], v[172:175], v[112:115]\n" \
"v_pk_add_f16 v122, v138, v146\n" \
"v_pk_add_f16 v123, v139, v147\n" \
"v_mfma_f32_16x16x32_f16 v[104:107], v[128:131], v[168:171], v[104:107]\n" \
"v_pk_add_f16 v124, v140, v148\n" \
"v_pk_add_f16 v125, v141, v149\n" \
"v_mfma_f32_16x16x32_f16 v[116:119], v[132:135], v[168:171], v[116:119]\n" \
"s_waitcnt lgkmcnt(2)\n" \
"v_pk_add_f16 v126, v142, v150\n" \
"v_pk_add_f16 v127, v143, v151\n" \
"s_waitcnt vmcnt(2) lgkmcnt(0)\n" \
"s_barrier\n" \
"v_mfma_f32_16x16x32_f16 v[0:3], v[136:139], v[176:179], v[0:3]\n" \
"ds_read_b128 v[168:171], v240 offset:40960\n" \
"ds_read_b64_tr_b16 v[128:129], v241 offset:32768\n" \
"ds_read_b64_tr_b16 v[130:131], v241 offset:40960\n" \
"v_mfma_f32_16x16x32_f16 v[12:15], v[140:143], v[176:179], v[12:15]\n" \
"v_pk_add_f16 v176, v176, v180\n" \
"v_pk_add_f16 v177, v177, v181\n" \
"ds_read_b64_tr_b16 v[132:133], v242 offset:32768\n" \
"v_mfma_f32_16x16x32_f16 v[4:7], v[144:147], v[180:183], v[4:7]\n" \
"v_pk_add_f16 v178, v178, v182\n" \
"v_pk_add_f16 v179, v179, v183\n" \
"v_mfma_f32_16x16x32_f16 v[16:19], v[148:151], v[180:183], v[16:19]\n" \
"ds_read_b128 v[172:175], v240 offset:41472\n" \
"ds_read_b64_tr_b16 v[134:135], v242 offset:40960\n" \
"ds_read_b64_tr_b16 v[152:153], v241 offset:33280\n" \
"v_mfma_f32_16x16x32_f16 v[8:11], v[120:123], v[176:179], v[8:11]\n" \
"ds_read_b64_tr_b16 v[154:155], v241 offset:41472\n" \
"v_mfma_f32_16x16x32_f16 v[20:23], v[124:127], v[176:179], v[20:23]\n" \
"ds_read_b64_tr_b16 v[156:157], v242 offset:33280\n" \
"ds_read_b64_tr_b16 v[158:159], v242 offset:41472\n" \
"v_mfma_f32_16x16x32_f16 v[24:27], v[136:139], v[160:163], v[24:27]\n" \
"ds_read_b128 v[176:179], v246 offset:0\n" \
"ds_read_b64_tr_b16 v[184:185], v243 offset:0\n" \
"ds_read_b64_tr_b16 v[186:187], v243 offset:8192\n" \
"v_mfma_f32_16x16x32_f16 v[36:39], v[140:143], v[160:163], v[36:39]\n" \
"v_pk_add_f16 v160, v160, v164\n" \
"v_pk_add_f16 v161, v161, v165\n" \
"ds_read_b64_tr_b16 v[188:189], v244 offset:0\n" \
"v_mfma_f32_16x16x32_f16 v[28:31], v[144:147], v[164:167], v[28:31]\n" \
"v_pk_add_f16 v162, v162, v166\n" \
"v_pk_add_f16 v163, v163, v167\n" \
"v_mfma_f32_16x16x32_f16 v[40:43], v[148:151], v[164:167], v[40:43]\n" \
"ds_read_b128 v[180:183], v246 offset:512\n" \
"ds_read_b64_tr_b16 v[190:191], v244 offset:8192\n" \
"ds_read_b64_tr_b16 v[192:193], v243 offset:512\n" \
"v_mfma_f32_16x16x32_f16 v[32:35], v[120:123], v[160:163], v[32:35]\n" \
"ds_read_b64_tr_b16 v[194:195], v243 offset:8704\n" \
"v_mfma_f32_16x16x32_f16 v[44:47], v[124:127], v[160:163], v[44:47]\n" \
"s_waitcnt lgkmcnt(11)\n" \
"v_pk_add_f16 v232, v128, v152\n" \
"v_pk_add_f16 v233, v129, v153\n" \
"ds_read_b64_tr_b16 v[196:197], v244 offset:512\n" \
"ds_read_b64_tr_b16 v[198:199], v244 offset:8704\n" \
"v_mfma_f32_16x16x32_f16 v[0:3], v[128:131], v[168:171], v[0:3]\n" \
"s_waitcnt lgkmcnt(10)\n" \
"v_pk_add_f16 v234, v130, v154\n" \
"ds_read_b128 v[160:163], v246 offset:8192\n" \
"ds_read_b64_tr_b16 v[208:209], v243 offset:16384\n" \
"ds_read_b64_tr_b16 v[210:211], v243 offset:24576\n" \
"v_mfma_f32_16x16x32_f16 v[12:15], v[132:135], v[168:171], v[12:15]\n" \
"v_pk_add_f16 v168, v168, v172\n" \
"v_pk_add_f16 v169, v169, v173\n" \
"v_pk_add_f16 v235, v131, v155\n" \
"v_pk_add_f16 v236, v132, v156\n" \
"ds_read_b64_tr_b16 v[212:213], v244 offset:16384\n" \
"v_mfma_f32_16x16x32_f16 v[4:7], v[152:155], v[172:175], v[4:7]\n" \
"v_pk_add_f16 v170, v170, v174\n" \
"v_pk_add_f16 v171, v171, v175\n" \
"v_pk_add_f16 v237, v133, v157\n" \
"v_pk_add_f16 v238, v134, v158\n" \
"v_pk_add_f16 v239, v135, v159\n" \
"v_mfma_f32_16x16x32_f16 v[16:19], v[156:159], v[172:175], v[16:19]\n" \
"s_waitcnt lgkmcnt(6)\n" \
"v_pk_add_f16 v200, v184, v192\n" \
"ds_read_b128 v[164:167], v246 offset:8704\n" \
"ds_read_b64_tr_b16 v[214:215], v244 offset:24576\n" \
"ds_read_b64_tr_b16 v[216:217], v243 offset:16896\n" \
"v_mfma_f32_16x16x32_f16 v[8:11], v[232:235], v[168:171], v[8:11]\n" \
"v_pk_add_f16 v201, v185, v193\n" \
"v_pk_add_f16 v202, v186, v194\n" \
"ds_read_b64_tr_b16 v[218:219], v243 offset:25088\n" \
"v_mfma_f32_16x16x32_f16 v[20:23], v[236:239], v[168:171], v[20:23]\n" \
"ds_read_b64_tr_b16 v[220:221], v244 offset:16896\n" \
"ds_read_b64_tr_b16 v[222:223], v244 offset:25088\n" \
"v_mfma_f32_16x16x32_f16 v[0:3], v[184:187], v[176:179], v[0:3]\n" \
"v_pk_add_f16 v203, v187, v195\n" \
"s_waitcnt lgkmcnt(7)\n" \
"v_pk_add_f16 v204, v188, v196\n" \
"ds_read_b128 v[168:171], v240 offset:43008\n" \
"v_mfma_f32_16x16x32_f16 v[12:15], v[188:191], v[176:179], v[12:15]\n" \
"v_pk_add_f16 v176, v176, v180\n" \
"v_pk_add_f16 v177, v177, v181\n" \
"v_pk_add_f16 v205, v189, v197\n" \
"v_mfma_f32_16x16x32_f16 v[4:7], v[192:195], v[180:183], v[4:7]\n" \
"v_pk_add_f16 v178, v178, v182\n" \
"v_pk_add_f16 v179, v179, v183\n" \
"v_pk_add_f16 v206, v190, v198\n" \
"v_pk_add_f16 v207, v191, v199\n" \
"v_mfma_f32_16x16x32_f16 v[16:19], v[196:199], v[180:183], v[16:19]\n" \
"s_waitcnt lgkmcnt(4)\n" \
"v_pk_add_f16 v224, v208, v216\n" \
"ds_read_b128 v[172:175], v240 offset:43520\n" \
"v_mfma_f32_16x16x32_f16 v[8:11], v[200:203], v[176:179], v[8:11]\n" \
"v_pk_add_f16 v225, v209, v217\n" \
"v_mfma_f32_16x16x32_f16 v[20:23], v[204:207], v[176:179], v[20:23]\n" \
"s_waitcnt lgkmcnt(4)\n" \
"v_pk_add_f16 v226, v210, v218\n" \
"v_pk_add_f16 v227, v211, v219\n" \
"v_mfma_f32_16x16x32_f16 v[0:3], v[208:211], v[160:163], v[0:3]\n" \
"s_waitcnt lgkmcnt(2)\n" \
"v_pk_add_f16 v228, v212, v220\n" \
"v_pk_add_f16 v229, v213, v221\n" \
"ds_read_b128 v[176:179], v246 offset:2048\n" \
"v_mfma_f32_16x16x32_f16 v[12:15], v[212:215], v[160:163], v[12:15]\n" \
"v_pk_add_f16 v160, v160, v164\n" \
"v_pk_add_f16 v161, v161, v165\n" \
"v_pk_add_f16 v230, v214, v222\n" \
"v_mfma_f32_16x16x32_f16 v[4:7], v[216:219], v[164:167], v[4:7]\n" \
"v_pk_add_f16 v162, v162, v166\n" \
"v_pk_add_f16 v163, v163, v167\n" \
"v_pk_add_f16 v231, v215, v223\n" \
"v_mfma_f32_16x16x32_f16 v[16:19], v[220:223], v[164:167], v[16:19]\n" \
"ds_read_b128 v[180:183], v246 offset:2560\n" \
"s_mov_b32 vcc_lo, 0xaaaaaaaa\n" \
"v_mfma_f32_16x16x32_f16 v[8:11], v[224:227], v[160:163], v[8:11]\n" \
"s_mov_b32 vcc_hi, 0xaaaaaaaa\n" \
"v_mfma_f32_16x16x32_f16 v[20:23], v[228:231], v[160:163], v[20:23]\n" \
"v_add_u32 v241, 0xffff7000, v240\n" \
"s_waitcnt lgkmcnt(2)\n" \
"v_mfma_f32_16x16x32_f16 v[24:27], v[128:131], v[168:171], v[24:27]\n" \
"ds_read_b128 v[160:163], v246 offset:10240\n" \
"v_mfma_f32_16x16x32_f16 v[36:39], v[132:135], v[168:171], v[36:39]\n" \
"v_pk_add_f16 v168, v168, v172\n" \
"v_pk_add_f16 v169, v169, v173\n" \
"v_mfma_f32_16x16x32_f16 v[28:31], v[152:155], v[172:175], v[28:31]\n" \
"v_pk_add_f16 v170, v170, v174\n" \
"v_pk_add_f16 v171, v171, v175\n" \
"v_add_f32 v252, v4, v8\n" \
"v_add_f32 v253, v5, v9\n" \
"v_add_f32 v254, v6, v10\n" \
"v_add_f32 v255, v7, v11\n" \
"v_mfma_f32_16x16x32_f16 v[40:43], v[156:159], v[172:175], v[40:43]\n" \
"ds_read_b128 v[164:167], v246 offset:10752\n" \
"v_fmac_f32 v4, s56, v8\n" \
"v_fmac_f32 v5, s56, v9\n" \
"v_fmac_f32 v6, s56, v10\n" \
"v_fmac_f32 v7, s56, v11\n" \
"v_mfma_f32_16x16x32_f16 v[32:35], v[232:235], v[168:171], v[32:35]\n" \
"v_fmac_f32 v0, s57, v252\n" \
"v_fmac_f32 v1, s57, v253\n" \
"v_fmac_f32 v2, s57, v254\n" \
"v_fmac_f32 v3, s57, v255\n" \
"v_add_f32 v252, v16, v20\n" \
"v_mfma_f32_16x16x32_f16 v[44:47], v[236:239], v[168:171], v[44:47]\n" \
"v_add_f32 v253, v17, v21\n" \
"v_add_f32 v254, v18, v22\n" \
"v_add_f32 v255, v19, v23\n" \
"v_fmac_f32 v16, s56, v20\n" \
"s_waitcnt lgkmcnt(1)\n" \
"v_mfma_f32_16x16x32_f16 v[24:27], v[184:187], v[176:179], v[24:27]\n" \
"ds_read_b128 v[168:171], v240 offset:45056\n" \
"v_fmac_f32 v17, s56, v21\n" \
"v_fmac_f32 v18, s56, v22\n" \
"v_fmac_f32 v19, s56, v23\n" \
"v_fmac_f32 v12, s57, v252\n" \
"v_fmac_f32 v13, s57, v253\n" \
"v_mfma_f32_16x16x32_f16 v[36:39], v[188:191], v[176:179], v[36:39]\n" \
"v_pk_add_f16 v176, v176, v180\n" \
"v_pk_add_f16 v177, v177, v181\n" \
"v_fmac_f32 v14, s57, v254\n" \
"v_fmac_f32 v15, s57, v255\n" \
"v_cvt_pk_bf16_f32 v8, v0, v1\n" \
"v_cvt_pk_bf16_f32 v20, v4, v5\n" \
"v_mfma_f32_16x16x32_f16 v[28:31], v[192:195], v[180:183], v[28:31]\n" \
"v_pk_add_f16 v178, v178, v182\n" \
"v_pk_add_f16 v179, v179, v183\n" \
"v_cvt_pk_bf16_f32 v9, v2, v3\n" \
"v_cvt_pk_bf16_f32 v21, v6, v7\n" \
"v_cvt_pk_bf16_f32 v10, v12, v13\n" \
"v_cvt_pk_bf16_f32 v22, v16, v17\n" \
"v_cvt_pk_bf16_f32 v11, v14, v15\n" \
"v_mfma_f32_16x16x32_f16 v[40:43], v[196:199], v[180:183], v[40:43]\n" \
"ds_read_b128 v[172:175], v240 offset:45568\n" \
"v_cvt_pk_bf16_f32 v23, v18, v19\n" \
"s_not_b64 vcc, vcc\n" \
"v_cndmask_b32_dpp v0, v20, v8, vcc quad_perm:[1,0,3,2] row_mask:0xf bank_mask:0xf\n" \
"v_cndmask_b32_dpp v1, v21, v9, vcc quad_perm:[1,0,3,2] row_mask:0xf bank_mask:0xf\n" \
"v_mfma_f32_16x16x32_f16 v[32:35], v[200:203], v[176:179], v[32:35]\n" \
"s_waitcnt vmcnt(0)\n" \
"s_barrier\n" \
"v_cndmask_b32_dpp v2, v22, v10, vcc quad_perm:[1,0,3,2] row_mask:0xf bank_mask:0xf\n" \
"v_cndmask_b32_dpp v3, v23, v11, vcc quad_perm:[1,0,3,2] row_mask:0xf bank_mask:0xf\n" \
"buffer_store_dwordx4 v[0:3], v245, s[40:43], s69 offen sc1\n" \
"s_not_b64 vcc, vcc\n" \
"s_add_i32 s85, s69, 0x10000\n" \
"v_mfma_f32_16x16x32_f16 v[44:47], v[204:207], v[176:179], v[44:47]\n" \
"v_cndmask_b32_dpp v12, v8, v20, vcc quad_perm:[1,0,3,2] row_mask:0xf bank_mask:0xf\n" \
"v_cndmask_b32_dpp v13, v9, v21, vcc quad_perm:[1,0,3,2] row_mask:0xf bank_mask:0xf\n" \
"v_cndmask_b32_dpp v14, v10, v22, vcc quad_perm:[1,0,3,2] row_mask:0xf bank_mask:0xf\n" \
"v_cndmask_b32_dpp v15, v11, v23, vcc quad_perm:[1,0,3,2] row_mask:0xf bank_mask:0xf\n" \
"buffer_store_dwordx4 v[12:15], v245, s[40:43], s85 offen sc1\n" \
"v_mfma_f32_16x16x32_f16 v[24:27], v[208:211], v[160:163], v[24:27]\n" \
"ds_read_b128 v[176:179], v240 offset:4096\n" \
"v_mfma_f32_16x16x32_f16 v[36:39], v[212:215], v[160:163], v[36:39]\n" \
"s_waitcnt lgkmcnt(2)\n" \
"v_pk_add_f16 v160, v160, v164\n" \
"v_pk_add_f16 v161, v161, v165\n" \
"v_mfma_f32_16x16x32_f16 v[28:31], v[216:219], v[164:167], v[28:31]\n" \
"v_pk_add_f16 v162, v162, v166\n" \
"v_pk_add_f16 v163, v163, v167\n" \
"v_mfma_f32_16x16x32_f16 v[40:43], v[220:223], v[164:167], v[40:43]\n" \
"ds_read_b128 v[180:183], v240 offset:4608\n" \
"v_mfma_f32_16x16x32_f16 v[32:35], v[224:227], v[160:163], v[32:35]\n" \
"v_mfma_f32_16x16x32_f16 v[44:47], v[228:231], v[160:163], v[44:47]\n" \
"v_mfma_f32_16x16x32_f16 v[48:51], v[136:139], v[168:171], v[48:51]\n" \
"ds_read_b128 v[160:163], v240 offset:24576\n" \
"v_mfma_f32_16x16x32_f16 v[60:63], v[140:143], v[168:171], v[60:63]\n" \
"s_waitcnt lgkmcnt(3)\n" \
"v_pk_add_f16 v168, v168, v172\n" \
"v_pk_add_f16 v169, v169, v173\n" \
"v_mfma_f32_16x16x32_f16 v[52:55], v[144:147], v[172:175], v[52:55]\n" \
"v_pk_add_f16 v170, v170, v174\n" \
"v_pk_add_f16 v171, v171, v175\n" \
"v_add_f32 v252, v28, v32\n" \
"v_add_f32 v253, v29, v33\n" \
"v_mfma_f32_16x16x32_f16 v[64:67], v[148:151], v[172:175], v[64:67]\n" \
"ds_read_b128 v[164:167], v240 offset:25088\n" \
"v_add_f32 v254, v30, v34\n" \
"v_add_f32 v255, v31, v35\n" \
"v_fmac_f32 v28, s56, v32\n" \
"v_mfma_f32_16x16x32_f16 v[56:59], v[120:123], v[168:171], v[56:59]\n" \
"v_fmac_f32 v29, s56, v33\n" \
"v_fmac_f32 v30, s56, v34\n" \
"v_fmac_f32 v31, s56, v35\n" \
"v_mfma_f32_16x16x32_f16 v[68:71], v[124:127], v[168:171], v[68:71]\n" \
"v_fmac_f32 v24, s57, v252\n" \
"v_fmac_f32 v25, s57, v253\n" \
"v_fmac_f32 v26, s57, v254\n" \
"s_waitcnt lgkmcnt(2)\n" \
"v_mfma_f32_16x16x32_f16 v[48:51], v[128:131], v[176:179], v[48:51]\n" \
"ds_read_b128 v[168:171], v241 offset:4096\n" \
"v_fmac_f32 v27, s57, v255\n" \
"v_add_f32 v252, v40, v44\n" \
"v_add_f32 v253, v41, v45\n" \
"v_mfma_f32_16x16x32_f16 v[60:63], v[132:135], v[176:179], v[60:63]\n" \
"v_pk_add_f16 v176, v176, v180\n" \
"v_pk_add_f16 v177, v177, v181\n" \
"v_add_f32 v254, v42, v46\n" \
"v_add_f32 v255, v43, v47\n" \
"v_mfma_f32_16x16x32_f16 v[52:55], v[152:155], v[180:183], v[52:55]\n" \
"v_pk_add_f16 v178, v178, v182\n" \
"v_pk_add_f16 v179, v179, v183\n" \
"v_fmac_f32 v40, s56, v44\n" \
"v_fmac_f32 v41, s56, v45\n" \
"v_fmac_f32 v42, s56, v46\n" \
"v_mfma_f32_16x16x32_f16 v[64:67], v[156:159], v[180:183], v[64:67]\n" \
"ds_read_b128 v[172:175], v241 offset:4608\n" \
"v_fmac_f32 v43, s56, v47\n" \
"v_fmac_f32 v36, s57, v252\n" \
"v_fmac_f32 v37, s57, v253\n" \
"v_mfma_f32_16x16x32_f16 v[56:59], v[232:235], v[176:179], v[56:59]\n" \
"v_fmac_f32 v38, s57, v254\n" \
"v_fmac_f32 v39, s57, v255\n" \
"v_cvt_pk_bf16_f32 v32, v24, v25\n" \
"v_mfma_f32_16x16x32_f16 v[68:71], v[236:239], v[176:179], v[68:71]\n" \
"v_cvt_pk_bf16_f32 v44, v28, v29\n" \
"v_cvt_pk_bf16_f32 v33, v26, v27\n" \
"v_cvt_pk_bf16_f32 v45, v30, v31\n" \
"s_waitcnt lgkmcnt(1)\n" \
"v_mfma_f32_16x16x32_f16 v[48:51], v[184:187], v[160:163], v[48:51]\n" \
"ds_read_b128 v[176:179], v240 offset:47104\n" \
"v_cvt_pk_bf16_f32 v34, v36, v37\n" \
"v_cvt_pk_bf16_f32 v46, v40, v41\n" \
"v_mfma_f32_16x16x32_f16 v[60:63], v[188:191], v[160:163], v[60:63]\n" \
"v_pk_add_f16 v160, v160, v164\n" \
"v_pk_add_f16 v161, v161, v165\n" \
"v_cvt_pk_bf16_f32 v35, v38, v39\n" \
"v_cvt_pk_bf16_f32 v47, v42, v43\n" \
"s_not_b64 vcc, vcc\n" \
"v_mfma_f32_16x16x32_f16 v[52:55], v[192:195], v[164:167], v[52:55]\n" \
"v_pk_add_f16 v162, v162, v166\n" \
"v_pk_add_f16 v163, v163, v167\n" \
"v_cndmask_b32_dpp v24, v44, v32, vcc quad_perm:[1,0,3,2] row_mask:0xf bank_mask:0xf\n" \
"v_cndmask_b32_dpp v25, v45, v33, vcc quad_perm:[1,0,3,2] row_mask:0xf bank_mask:0xf\n" \
"v_cndmask_b32_dpp v26, v46, v34, vcc quad_perm:[1,0,3,2] row_mask:0xf bank_mask:0xf\n" \
"v_mfma_f32_16x16x32_f16 v[64:67], v[196:199], v[164:167], v[64:67]\n" \
"ds_read_b128 v[180:183], v240 offset:47616\n" \
"v_cndmask_b32_dpp v27, v47, v35, vcc quad_perm:[1,0,3,2] row_mask:0xf bank_mask:0xf\n" \
"buffer_store_dwordx4 v[24:27], v245, s[40:43], s70 offen sc1\n" \
"s_not_b64 vcc, vcc\n" \
"v_mfma_f32_16x16x32_f16 v[56:59], v[200:203], v[160:163], v[56:59]\n" \
"s_add_i32 s85, s70, 0x10000\n" \
"v_cndmask_b32_dpp v36, v32, v44, vcc quad_perm:[1,0,3,2] row_mask:0xf bank_mask:0xf\n" \
"v_cndmask_b32_dpp v37, v33, v45, vcc quad_perm:[1,0,3,2] row_mask:0xf bank_mask:0xf\n" \
"v_mfma_f32_16x16x32_f16 v[68:71], v[204:207], v[160:163], v[68:71]\n" \
"v_cndmask_b32_dpp v38, v34, v46, vcc quad_perm:[1,0,3,2] row_mask:0xf bank_mask:0xf\n" \
"v_cndmask_b32_dpp v39, v35, v47, vcc quad_perm:[1,0,3,2] row_mask:0xf bank_mask:0xf\n" \
"buffer_store_dwordx4 v[36:39], v245, s[40:43], s85 offen sc1\n" \
"v_mfma_f32_16x16x32_f16 v[48:51], v[208:211], v[168:171], v[48:51]\n" \
"ds_read_b128 v[160:163], v240 offset:6144\n" \
"v_mfma_f32_16x16x32_f16 v[60:63], v[212:215], v[168:171], v[60:63]\n" \
"s_waitcnt lgkmcnt(2)\n" \
"v_pk_add_f16 v168, v168, v172\n" \
"v_pk_add_f16 v169, v169, v173\n" \
"v_mfma_f32_16x16x32_f16 v[52:55], v[216:219], v[172:175], v[52:55]\n" \
"v_pk_add_f16 v170, v170, v174\n" \
"v_pk_add_f16 v171, v171, v175\n" \
"v_mfma_f32_16x16x32_f16 v[64:67], v[220:223], v[172:175], v[64:67]\n" \
"ds_read_b128 v[164:167], v240 offset:6656\n" \
"v_mfma_f32_16x16x32_f16 v[56:59], v[224:227], v[168:171], v[56:59]\n" \
"v_mfma_f32_16x16x32_f16 v[68:71], v[228:231], v[168:171], v[68:71]\n" \
"v_mfma_f32_16x16x32_f16 v[72:75], v[136:139], v[176:179], v[72:75]\n" \
"ds_read_b128 v[168:171], v240 offset:26624\n" \
"v_mfma_f32_16x16x32_f16 v[84:87], v[140:143], v[176:179], v[84:87]\n" \
"s_waitcnt lgkmcnt(3)\n" \
"v_pk_add_f16 v176, v176, v180\n" \
"v_pk_add_f16 v177, v177, v181\n" \
"v_mfma_f32_16x16x32_f16 v[76:79], v[144:147], v[180:183], v[76:79]\n" \
"v_pk_add_f16 v178, v178, v182\n" \
"v_pk_add_f16 v179, v179, v183\n" \
"v_add_f32 v252, v52, v56\n" \
"v_add_f32 v253, v53, v57\n" \
"v_mfma_f32_16x16x32_f16 v[88:91], v[148:151], v[180:183], v[88:91]\n" \
"ds_read_b128 v[172:175], v240 offset:27136\n" \
"v_add_f32 v254, v54, v58\n" \
"v_add_f32 v255, v55, v59\n" \
"v_mfma_f32_16x16x32_f16 v[80:83], v[120:123], v[176:179], v[80:83]\n" \
"v_fmac_f32 v52, s56, v56\n" \
"v_fmac_f32 v53, s56, v57\n" \
"v_mfma_f32_16x16x32_f16 v[92:95], v[124:127], v[176:179], v[92:95]\n" \
"v_fmac_f32 v54, s56, v58\n" \
"v_fmac_f32 v55, s56, v59\n" \
"s_waitcnt lgkmcnt(2)\n" \
"v_mfma_f32_16x16x32_f16 v[72:75], v[128:131], v[160:163], v[72:75]\n" \
"ds_read_b128 v[176:179], v241 offset:6144\n" \
"v_fmac_f32 v48, s57, v252\n" \
"v_fmac_f32 v49, s57, v253\n" \
"v_mfma_f32_16x16x32_f16 v[84:87], v[132:135], v[160:163], v[84:87]\n" \
"v_pk_add_f16 v160, v160, v164\n" \
"v_pk_add_f16 v161, v161, v165\n" \
"v_fmac_f32 v50, s57, v254\n" \
"v_fmac_f32 v51, s57, v255\n" \
"v_mfma_f32_16x16x32_f16 v[76:79], v[152:155], v[164:167], v[76:79]\n" \
"v_pk_add_f16 v162, v162, v166\n" \
"v_pk_add_f16 v163, v163, v167\n" \
"v_add_f32 v252, v64, v68\n" \
"v_add_f32 v253, v65, v69\n" \
"v_mfma_f32_16x16x32_f16 v[88:91], v[156:159], v[164:167], v[88:91]\n" \
"ds_read_b128 v[180:183], v241 offset:6656\n" \
"v_add_f32 v254, v66, v70\n" \
"v_add_f32 v255, v67, v71\n" \
"v_mfma_f32_16x16x32_f16 v[80:83], v[232:235], v[160:163], v[80:83]\n" \
"v_fmac_f32 v64, s56, v68\n" \
"v_fmac_f32 v65, s56, v69\n" \
"v_mfma_f32_16x16x32_f16 v[92:95], v[236:239], v[160:163], v[92:95]\n" \
"v_fmac_f32 v66, s56, v70\n" \
"v_fmac_f32 v67, s56, v71\n" \
"s_waitcnt lgkmcnt(2)\n" \
"v_mfma_f32_16x16x32_f16 v[72:75], v[184:187], v[168:171], v[72:75]\n" \
"ds_read_b128 v[160:163], v240 offset:49152\n" \
"v_fmac_f32 v60, s57, v252\n" \
"v_fmac_f32 v61, s57, v253\n" \
"v_mfma_f32_16x16x32_f16 v[84:87], v[188:191], v[168:171], v[84:87]\n" \
"v_pk_add_f16 v168, v168, v172\n" \
"v_pk_add_f16 v169, v169, v173\n" \
"v_fmac_f32 v62, s57, v254\n" \
"v_fmac_f32 v63, s57, v255\n" \
"v_mfma_f32_16x16x32_f16 v[76:79], v[192:195], v[172:175], v[76:79]\n" \
"v_pk_add_f16 v170, v170, v174\n" \
"v_pk_add_f16 v171, v171, v175\n" \
"v_cvt_pk_bf16_f32 v56, v48, v49\n" \
"v_cvt_pk_bf16_f32 v68, v52, v53\n" \
"v_mfma_f32_16x16x32_f16 v[88:91], v[196:199], v[172:175], v[88:91]\n" \
"ds_read_b128 v[164:167], v240 offset:49664\n" \
"v_cvt_pk_bf16_f32 v57, v50, v51\n" \
"v_cvt_pk_bf16_f32 v69, v54, v55\n" \
"v_mfma_f32_16x16x32_f16 v[80:83], v[200:203], v[168:171], v[80:83]\n" \
"v_cvt_pk_bf16_f32 v58, v60, v61\n" \
"v_cvt_pk_bf16_f32 v70, v64, v65\n" \
"v_mfma_f32_16x16x32_f16 v[92:95], v[204:207], v[168:171], v[92:95]\n" \
"v_cvt_pk_bf16_f32 v59, v62, v63\n" \
"v_cvt_pk_bf16_f32 v71, v66, v67\n" \
"s_waitcnt lgkmcnt(2)\n" \
"v_mfma_f32_16x16x32_f16 v[72:75], v[208:211], v[176:179], v[72:75]\n" \
"ds_read_b128 v[168:171], v240 offset:8192\n" \
"s_not_b64 vcc, vcc\n" \
"v_cndmask_b32_dpp v48, v68, v56, vcc quad_perm:[1,0,3,2] row_mask:0xf bank_mask:0xf\n" \
"v_mfma_f32_16x16x32_f16 v[84:87], v[212:215], v[176:179], v[84:87]\n" \
"v_pk_add_f16 v176, v176, v180\n" \
"v_pk_add_f16 v177, v177, v181\n" \
"v_cndmask_b32_dpp v49, v69, v57, vcc quad_perm:[1,0,3,2] row_mask:0xf bank_mask:0xf\n" \
"v_cndmask_b32_dpp v50, v70, v58, vcc quad_perm:[1,0,3,2] row_mask:0xf bank_mask:0xf\n" \
"v_mfma_f32_16x16x32_f16 v[76:79], v[216:219], v[180:183], v[76:79]\n" \
"v_pk_add_f16 v178, v178, v182\n" \
"v_pk_add_f16 v179, v179, v183\n" \
"v_cndmask_b32_dpp v51, v71, v59, vcc quad_perm:[1,0,3,2] row_mask:0xf bank_mask:0xf\n" \
"buffer_store_dwordx4 v[48:51], v245, s[40:43], s71 offen sc1\n" \
"v_mfma_f32_16x16x32_f16 v[88:91], v[220:223], v[180:183], v[88:91]\n" \
"ds_read_b128 v[172:175], v240 offset:8704\n" \
"s_not_b64 vcc, vcc\n" \
"s_add_i32 s85, s71, 0x10000\n" \
"v_mfma_f32_16x16x32_f16 v[80:83], v[224:227], v[176:179], v[80:83]\n" \
"v_cndmask_b32_dpp v60, v56, v68, vcc quad_perm:[1,0,3,2] row_mask:0xf bank_mask:0xf\n" \
"v_cndmask_b32_dpp v61, v57, v69, vcc quad_perm:[1,0,3,2] row_mask:0xf bank_mask:0xf\n" \
"v_mfma_f32_16x16x32_f16 v[92:95], v[228:231], v[176:179], v[92:95]\n" \
"v_cndmask_b32_dpp v62, v58, v70, vcc quad_perm:[1,0,3,2] row_mask:0xf bank_mask:0xf\n" \
"v_cndmask_b32_dpp v63, v59, v71, vcc quad_perm:[1,0,3,2] row_mask:0xf bank_mask:0xf\n" \
"buffer_store_dwordx4 v[60:63], v245, s[40:43], s85 offen sc1\n" \
"s_waitcnt lgkmcnt(1)\n" \
"v_mfma_f32_16x16x32_f16 v[96:99], v[136:139], v[160:163], v[96:99]\n" \
"ds_read_b128 v[176:179], v240 offset:28672\n" \
"v_mfma_f32_16x16x32_f16 v[108:111], v[140:143], v[160:163], v[108:111]\n" \
"v_pk_add_f16 v160, v160, v164\n" \
"v_pk_add_f16 v161, v161, v165\n" \
"v_mfma_f32_16x16x32_f16 v[100:103], v[144:147], v[164:167], v[100:103]\n" \
"v_pk_add_f16 v162, v162, v166\n" \
"v_pk_add_f16 v163, v163, v167\n" \
"v_add_f32 v252, v76, v80\n" \
"v_add_f32 v253, v77, v81\n" \
"v_mfma_f32_16x16x32_f16 v[112:115], v[148:151], v[164:167], v[112:115]\n" \
"ds_read_b128 v[180:183], v240 offset:29184\n" \
"v_add_f32 v254, v78, v82\n" \
"v_add_f32 v255, v79, v83\n" \
"v_mfma_f32_16x16x32_f16 v[104:107], v[120:123], v[160:163], v[104:107]\n" \
"v_fmac_f32 v76, s56, v80\n" \
"v_fmac_f32 v77, s56, v81\n" \
"v_mfma_f32_16x16x32_f16 v[116:119], v[124:127], v[160:163], v[116:119]\n" \
"v_fmac_f32 v78, s56, v82\n" \
"v_fmac_f32 v79, s56, v83\n" \
"v_mfma_f32_16x16x32_f16 v[96:99], v[128:131], v[168:171], v[96:99]\n" \
"ds_read_b128 v[160:163], v241 offset:8192\n" \
"v_fmac_f32 v72, s57, v252\n" \
"v_fmac_f32 v73, s57, v253\n" \
"v_mfma_f32_16x16x32_f16 v[108:111], v[132:135], v[168:171], v[108:111]\n" \
"s_waitcnt lgkmcnt(2)\n" \
"v_pk_add_f16 v168, v168, v172\n" \
"v_pk_add_f16 v169, v169, v173\n" \
"v_fmac_f32 v74, s57, v254\n" \
"v_fmac_f32 v75, s57, v255\n" \
"v_mfma_f32_16x16x32_f16 v[100:103], v[152:155], v[172:175], v[100:103]\n" \
"v_pk_add_f16 v170, v170, v174\n" \
"v_pk_add_f16 v171, v171, v175\n" \
"v_add_f32 v252, v88, v92\n" \
"v_add_f32 v253, v89, v93\n" \
"v_mfma_f32_16x16x32_f16 v[112:115], v[156:159], v[172:175], v[112:115]\n" \
"ds_read_b128 v[164:167], v241 offset:8704\n" \
"v_add_f32 v254, v90, v94\n" \
"v_add_f32 v255, v91, v95\n" \
"v_mfma_f32_16x16x32_f16 v[104:107], v[232:235], v[168:171], v[104:107]\n" \
"v_fmac_f32 v88, s56, v92\n" \
"v_fmac_f32 v89, s56, v93\n" \
"v_mfma_f32_16x16x32_f16 v[116:119], v[236:239], v[168:171], v[116:119]\n" \
"v_fmac_f32 v90, s56, v94\n" \
"v_fmac_f32 v91, s56, v95\n" \
"v_mfma_f32_16x16x32_f16 v[96:99], v[184:187], v[176:179], v[96:99]\n" \
"v_fmac_f32 v84, s57, v252\n" \
"v_fmac_f32 v85, s57, v253\n" \
"v_fmac_f32 v86, s57, v254\n" \
"v_mfma_f32_16x16x32_f16 v[108:111], v[188:191], v[176:179], v[108:111]\n" \
"s_waitcnt lgkmcnt(1)\n" \
"v_pk_add_f16 v176, v176, v180\n" \
"v_pk_add_f16 v177, v177, v181\n" \
"v_fmac_f32 v87, s57, v255\n" \
"v_mfma_f32_16x16x32_f16 v[100:103], v[192:195], v[180:183], v[100:103]\n" \
"v_pk_add_f16 v178, v178, v182\n" \
"v_pk_add_f16 v179, v179, v183\n" \
"v_cvt_pk_bf16_f32 v80, v72, v73\n" \
"v_cvt_pk_bf16_f32 v92, v76, v77\n" \
"v_mfma_f32_16x16x32_f16 v[112:115], v[196:199], v[180:183], v[112:115]\n" \
"v_cvt_pk_bf16_f32 v81, v74, v75\n" \
"v_cvt_pk_bf16_f32 v93, v78, v79\n" \
"v_mfma_f32_16x16x32_f16 v[104:107], v[200:203], v[176:179], v[104:107]\n" \
"v_cvt_pk_bf16_f32 v82, v84, v85\n" \
"v_cvt_pk_bf16_f32 v94, v88, v89\n" \
"v_mfma_f32_16x16x32_f16 v[116:119], v[204:207], v[176:179], v[116:119]\n" \
"v_cvt_pk_bf16_f32 v83, v86, v87\n" \
"v_cvt_pk_bf16_f32 v95, v90, v91\n" \
"v_mfma_f32_16x16x32_f16 v[96:99], v[208:211], v[160:163], v[96:99]\n" \
"s_not_b64 vcc, vcc\n" \
"v_cndmask_b32_dpp v72, v92, v80, vcc quad_perm:[1,0,3,2] row_mask:0xf bank_mask:0xf\n" \
"v_cndmask_b32_dpp v73, v93, v81, vcc quad_perm:[1,0,3,2] row_mask:0xf bank_mask:0xf\n" \
"v_mfma_f32_16x16x32_f16 v[108:111], v[212:215], v[160:163], v[108:111]\n" \
"s_waitcnt lgkmcnt(0)\n" \
"v_pk_add_f16 v160, v160, v164\n" \
"v_pk_add_f16 v161, v161, v165\n" \
"v_cndmask_b32_dpp v74, v94, v82, vcc quad_perm:[1,0,3,2] row_mask:0xf bank_mask:0xf\n" \
"v_mfma_f32_16x16x32_f16 v[100:103], v[216:219], v[164:167], v[100:103]\n" \
"v_pk_add_f16 v162, v162, v166\n" \
"v_pk_add_f16 v163, v163, v167\n" \
"v_cndmask_b32_dpp v75, v95, v83, vcc quad_perm:[1,0,3,2] row_mask:0xf bank_mask:0xf\n" \
"buffer_store_dwordx4 v[72:75], v245, s[40:43], s72 offen sc1\n" \
"v_mfma_f32_16x16x32_f16 v[112:115], v[220:223], v[164:167], v[112:115]\n" \
"s_not_b64 vcc, vcc\n" \
"s_add_i32 s85, s72, 0x10000\n" \
"v_mfma_f32_16x16x32_f16 v[104:107], v[224:227], v[160:163], v[104:107]\n" \
"v_cndmask_b32_dpp v84, v80, v92, vcc quad_perm:[1,0,3,2] row_mask:0xf bank_mask:0xf\n" \
"v_cndmask_b32_dpp v85, v81, v93, vcc quad_perm:[1,0,3,2] row_mask:0xf bank_mask:0xf\n" \
"v_mfma_f32_16x16x32_f16 v[116:119], v[228:231], v[160:163], v[116:119]\n" \
"v_cndmask_b32_dpp v86, v82, v94, vcc quad_perm:[1,0,3,2] row_mask:0xf bank_mask:0xf\n" \
"v_cndmask_b32_dpp v87, v83, v95, vcc quad_perm:[1,0,3,2] row_mask:0xf bank_mask:0xf\n" \
"buffer_store_dwordx4 v[84:87], v245, s[40:43], s85 offen sc1\n" \
"s_nop 7\n" \
"v_add_f32 v252, v100, v104\n" \
"v_add_f32 v253, v101, v105\n" \
"v_add_f32 v254, v102, v106\n" \
"v_add_f32 v255, v103, v107\n" \
"v_fmac_f32 v100, s56, v104\n" \
"v_fmac_f32 v101, s56, v105\n" \
"v_fmac_f32 v102, s56, v106\n" \
"v_fmac_f32 v103, s56, v107\n" \
"v_fmac_f32 v96, s57, v252\n" \
"v_fmac_f32 v97, s57, v253\n" \
"v_fmac_f32 v98, s57, v254\n" \
"v_fmac_f32 v99, s57, v255\n" \
"v_add_f32 v252, v112, v116\n" \
"v_add_f32 v253, v113, v117\n" \
"v_add_f32 v254, v114, v118\n" \
"v_add_f32 v255, v115, v119\n" \
"v_fmac_f32 v112, s56, v116\n" \
"v_fmac_f32 v113, s56, v117\n" \
"v_fmac_f32 v114, s56, v118\n" \
"v_fmac_f32 v115, s56, v119\n" \
"v_fmac_f32 v108, s57, v252\n" \
"v_fmac_f32 v109, s57, v253\n" \
"v_fmac_f32 v110, s57, v254\n" \
"v_fmac_f32 v111, s57, v255\n" \
"v_cvt_pk_bf16_f32 v104, v96, v97\n" \
"v_cvt_pk_bf16_f32 v116, v100, v101\n" \
"v_cvt_pk_bf16_f32 v105, v98, v99\n" \
"v_cvt_pk_bf16_f32 v117, v102, v103\n" \
"v_cvt_pk_bf16_f32 v106, v108, v109\n" \
"v_cvt_pk_bf16_f32 v118, v112, v113\n" \
"v_cvt_pk_bf16_f32 v107, v110, v111\n" \
"v_cvt_pk_bf16_f32 v119, v114, v115\n" \
"s_not_b64 vcc, vcc\n" \
"v_cndmask_b32_dpp v96, v116, v104, vcc quad_perm:[1,0,3,2] row_mask:0xf bank_mask:0xf\n" \
"v_cndmask_b32_dpp v97, v117, v105, vcc quad_perm:[1,0,3,2] row_mask:0xf bank_mask:0xf\n" \
"v_cndmask_b32_dpp v98, v118, v106, vcc quad_perm:[1,0,3,2] row_mask:0xf bank_mask:0xf\n" \
"v_cndmask_b32_dpp v99, v119, v107, vcc quad_perm:[1,0,3,2] row_mask:0xf bank_mask:0xf\n" \
"buffer_store_dwordx4 v[96:99], v245, s[40:43], s73 offen sc1\n" \
"s_not_b64 vcc, vcc\n" \
"s_add_i32 s85, s73, 0x10000\n" \
"v_cndmask_b32_dpp v108, v104, v116, vcc quad_perm:[1,0,3,2] row_mask:0xf bank_mask:0xf\n" \
"v_cndmask_b32_dpp v109, v105, v117, vcc quad_perm:[1,0,3,2] row_mask:0xf bank_mask:0xf\n" \
"v_cndmask_b32_dpp v110, v106, v118, vcc quad_perm:[1,0,3,2] row_mask:0xf bank_mask:0xf\n" \
"v_cndmask_b32_dpp v111, v107, v119, vcc quad_perm:[1,0,3,2] row_mask:0xf bank_mask:0xf\n" \
"buffer_store_dwordx4 v[108:111], v245, s[40:43], s85 offen sc1\n" \
"s_waitcnt vmcnt(0)\n" \
"s_branch .Lcf_e\n" \
".Ls03_%=:\n" \
"s_mov_b32 s87, 0x200000\n" \
"s_mov_b32 s86, 0x8006000\n" \
".Lt03_%=:\n" \
"s_sleep 3\n" \
"buffer_load_dword v253, v252, s[36:39], s86 offen sc1\n" \
"s_waitcnt vmcnt(0)\n" \
"s_sub_u32 s87, s87, 1\n" \
"s_cbranch_scc1 .Lr03_%=\n" \
"v_cmp_eq_u32 vcc, 0, v253\n" \
"s_cbranch_vccnz .Lt03_%=\n" \
"s_branch .Lr03_%=\n" \
".Ls04_%=:\n" \
"s_mov_b32 s87, 0x200000\n" \
"s_mov_b32 s86, 0x8008000\n" \
".Lt04_%=:\n" \
"s_sleep 3\n" \
"buffer_load_dword v253, v252, s[36:39], s86 offen sc1\n" \
"s_waitcnt vmcnt(0)\n" \
"s_sub_u32 s87, s87, 1\n" \
"s_cbranch_scc1 .Lr04_%=\n" \
"v_cmp_eq_u32 vcc, 0, v253\n" \
"s_cbranch_vccnz .Lt04_%=\n" \
"s_branch .Lr04_%=\n" \
".Ls05_%=:\n" \
"s_mov_b32 s87, 0x200000\n" \
"s_mov_b32 s86, 0x800a000\n" \
".Lt05_%=:\n" \
"s_sleep 3\n" \
"buffer_load_dword v253, v252, s[36:39], s86 offen sc1\n" \
"s_waitcnt vmcnt(0)\n" \
"s_sub_u32 s87, s87, 1\n" \
"s_cbranch_scc1 .Lr05_%=\n" \
"v_cmp_eq_u32 vcc, 0, v253\n" \
"s_cbranch_vccnz .Lt05_%=\n" \
"s_branch .Lr05_%=\n" \
".Ls07_%=:\n" \
"s_mov_b32 s87, 0x200000\n" \
"s_mov_b32 s86, 0x800e000\n" \
".Lt07_%=:\n" \
"s_sleep 3\n" \
"buffer_load_dword v253, v252, s[36:39], s86 offen sc1\n" \
"s_waitcnt vmcnt(0)\n" \
"s_sub_u32 s87, s87, 1\n" \
"s_cbranch_scc1 .Lr07_%=\n" \
"v_cmp_eq_u32 vcc, 0, v253\n" \
"s_cbranch_vccnz .Lt07_%=\n" \
"s_branch .Lr07_%=\n" \
".Ls08_%=:\n" \
"s_mov_b32 s87, 0x200000\n" \
"s_mov_b32 s86, 0x8010000\n" \
".Lt08_%=:\n" \
"s_sleep 3\n" \
"buffer_load_dword v253, v252, s[36:39], s86 offen sc1\n" \
"s_waitcnt vmcnt(0)\n" \
"s_sub_u32 s87, s87, 1\n" \
"s_cbranch_scc1 .Lr08_%=\n" \
"v_cmp_eq_u32 vcc, 0, v253\n" \
"s_cbranch_vccnz .Lt08_%=\n" \
"s_branch .Lr08_%=\n" \
".Ls09_%=:\n" \
"s_mov_b32 s87, 0x200000\n" \
"s_mov_b32 s86, 0x8012000\n" \
".Lt09_%=:\n" \
"s_sleep 3\n" \
"buffer_load_dword v253, v252, s[36:39], s86 offen sc1\n" \
"s_waitcnt vmcnt(0)\n" \
"s_sub_u32 s87, s87, 1\n" \
"s_cbranch_scc1 .Lr09_%=\n" \
"v_cmp_eq_u32 vcc, 0, v253\n" \
"s_cbranch_vccnz .Lt09_%=\n" \
"s_branch .Lr09_%=\n" \
".Ls011_%=:\n" \
"s_mov_b32 s87, 0x200000\n" \
"s_mov_b32 s86, 0x8016000\n" \
".Lt011_%=:\n" \
"s_sleep 3\n" \
"buffer_load_dword v253, v252, s[36:39], s86 offen sc1\n" \
"s_waitcnt vmcnt(0)\n" \
"s_sub_u32 s87, s87, 1\n" \
"s_cbranch_scc1 .Lr011_%=\n" \
"v_cmp_eq_u32 vcc, 0, v253\n" \
"s_cbranch_vccnz .Lt011_%=\n" \
"s_branch .Lr011_%=\n" \
".Ls012_%=:\n" \
"s_mov_b32 s87, 0x200000\n" \
"s_mov_b32 s86, 0x8018000\n" \
".Lt012_%=:\n" \
"s_sleep 3\n" \
"buffer_load_dword v253, v252, s[36:39], s86 offen sc1\n" \
"s_waitcnt vmcnt(0)\n" \
"s_sub_u32 s87, s87, 1\n" \
"s_cbranch_scc1 .Lr012_%=\n" \
"v_cmp_eq_u32 vcc, 0, v253\n" \
"s_cbranch_vccnz .Lt012_%=\n" \
"s_branch .Lr012_%=\n" \
".Ls013_%=:\n" \
"s_mov_b32 s87, 0x200000\n" \
"s_mov_b32 s86, 0x801a000\n" \
".Lt013_%=:\n" \
"s_sleep 3\n" \
"buffer_load_dword v253, v252, s[36:39], s86 offen sc1\n" \
"s_waitcnt vmcnt(0)\n" \
"s_sub_u32 s87, s87, 1\n" \
"s_cbranch_scc1 .Lr013_%=\n" \
"v_cmp_eq_u32 vcc, 0, v253\n" \
"s_cbranch_vccnz .Lt013_%=\n" \
"s_branch .Lr013_%=\n" \
""
#define LOOP1 \
"s_mov_b32 s91, 0x40004\n" \
"s_mov_b32 s92, 0xfff8fff8\n" \
"ds_read_b64_tr_b16 v[120:121], v241 offset:0\n" \
"ds_read_b64_tr_b16 v[122:123], v241 offset:8192\n" \
"ds_read_b64_tr_b16 v[124:125], v242 offset:0\n" \
"ds_read_b64_tr_b16 v[126:127], v242 offset:8192\n" \
"ds_read_b64_tr_b16 v[128:129], v241 offset:512\n" \
"ds_read_b64_tr_b16 v[130:131], v241 offset:8704\n" \
"ds_read_b64_tr_b16 v[132:133], v242 offset:512\n" \
"ds_read_b64_tr_b16 v[134:135], v242 offset:8704\n" \
"ds_read_b128 v[160:163], v240 offset:2048\n" \
"ds_read_b128 v[164:167], v240 offset:2560\n" \
"ds_read_b128 v[168:171], v240 offset:4096\n" \
"ds_read_b128 v[172:175], v240 offset:4608\n" \
"s_add_i32 m0, s44, 0xa000\n" \
"s_add_i32 s80, s46, 0x400000\n" \
"buffer_load_dwordx4 v232, s[36:39], s80 offen sc1 lds\n" \
"buffer_load_dwordx4 v232, s[36:39], s80 offen offset:1024 sc1 lds\n" \
"buffer_load_dwordx4 v232, s[36:39], s80 offen offset:2048 sc1 lds\n" \
"buffer_load_dwordx4 v232, s[36:39], s80 offen offset:3072 sc1 lds\n" \
"s_add_i32 m0, s45, 0xa000\n" \
"s_add_i32 s81, s47, 0x400000\n" \
"buffer_load_dwordx4 v232, s[36:39], s81 offen sc1 lds\n" \
"s_mov_b32 s94, s53\n" \
"s_mov_b32 s95, s74\n" \
"s_setprio 1\n" \
"s_waitcnt vmcnt(9) lgkmcnt(0)\n" \
"s_cmp_eq_u32 s94, 0\n" \
"s_cbranch_scc1 .Lp0_%=\n" \
".Lq0_%=:\n" \
"s_memrealtime s[86:87]\n" \
"s_waitcnt lgkmcnt(0)\n" \
"s_and_b32 s87, s94, 0xff\n" \
"s_add_i32 s75, s86, s87\n" \
".Lp0_%=:\n" \
"s_barrier\n" \
"v_mfma_f32_16x16x32_f16 v[0:3], v[120:123], v[160:163], 0\n" \
"v_pk_add_f16 v136, v120, v128\n" \
"v_pk_add_f16 v137, v121, v129\n" \
"v_pk_add_f16 v138, v122, v130\n" \
"v_pk_add_f16 v139, v123, v131\n" \
"ds_read_b128 v[176:179], v240 offset:26624\n" \
"v_mfma_f32_16x16x32_f16 v[12:15], v[124:127], v[160:163], 0\n" \
"v_pk_add_f16 v160, v160, v164\n" \
"v_pk_add_f16 v161, v161, v165\n" \
"v_pk_add_f16 v140, v124, v132\n" \
"v_mfma_f32_16x16x32_f16 v[4:7], v[128:131], v[164:167], 0\n" \
"v_pk_add_f16 v162, v162, v166\n" \
"v_pk_add_f16 v163, v163, v167\n" \
"v_pk_add_f16 v141, v125, v133\n" \
"buffer_store_dwordx4 v[208:211], v235, s[36:39], s63 offen sc1\n" \
"v_mfma_f32_16x16x32_f16 v[16:19], v[132:135], v[164:167], 0\n" \
"v_pk_add_f16 v142, v126, v134\n" \
"v_pk_add_f16 v143, v127, v135\n" \
"ds_read_b128 v[180:183], v240 offset:27136\n" \
"s_add_i32 m0, s44, 0x0\n" \
"s_add_i32 s80, s46, 0x800000\n" \
"v_mfma_f32_16x16x32_f16 v[8:11], v[136:139], v[160:163], 0\n" \
"buffer_load_dwordx4 v232, s[36:39], s80 offen sc1 lds\n" \
"ds_write_b64 v246, v[96:97] offset:0\n" \
"v_mfma_f32_16x16x32_f16 v[20:23], v[140:143], v[160:163], 0\n" \
"buffer_store_dwordx4 v[72:75], v235, s[36:39], s60 offen sc1\n" \
"ds_read_b64_tr_b16 v[144:145], v241 offset:16384\n" \
"v_mfma_f32_16x16x32_f16 v[24:27], v[120:123], v[168:171], 0\n" \
"ds_read_b128 v[160:163], v240 offset:28672\n" \
"ds_write_b64 v246, v[98:99] offset:4096\n" \
"ds_write_b64 v246, v[100:101] offset:8192\n" \
"v_mfma_f32_16x16x32_f16 v[36:39], v[124:127], v[168:171], 0\n" \
"v_pk_add_f16 v168, v168, v172\n" \
"v_pk_add_f16 v169, v169, v173\n" \
"ds_read_b64_tr_b16 v[146:147], v241 offset:24576\n" \
"v_mfma_f32_16x16x32_f16 v[28:31], v[128:131], v[172:175], 0\n" \
"v_pk_add_f16 v170, v170, v174\n" \
"v_pk_add_f16 v171, v171, v175\n" \
"v_mfma_f32_16x16x32_f16 v[40:43], v[132:135], v[172:175], 0\n" \
"ds_read_b128 v[164:167], v240 offset:29184\n" \
"ds_write_b64 v246, v[102:103] offset:12288\n" \
"v_mfma_f32_16x16x32_f16 v[32:35], v[136:139], v[168:171], 0\n" \
"buffer_load_dwordx4 v232, s[36:39], s80 offen offset:1024 sc1 lds\n" \
"ds_read_b64_tr_b16 v[148:149], v242 offset:16384\n" \
"v_mfma_f32_16x16x32_f16 v[44:47], v[140:143], v[168:171], 0\n" \
"buffer_store_dwordx4 v[76:79], v235, s[36:39], s61 offen sc1\n" \
"ds_read_b64_tr_b16 v[150:151], v242 offset:24576\n" \
"s_waitcnt lgkmcnt(9)\n" \
"v_mfma_f32_16x16x32_f16 v[48:51], v[120:123], v[176:179], 0\n" \
"ds_read_b128 v[168:171], v240 offset:20480\n" \
"ds_write_b64 v246, v[104:105] offset:16384\n" \
"ds_write_b64 v246, v[106:107] offset:20480\n" \
"v_mfma_f32_16x16x32_f16 v[60:63], v[124:127], v[176:179], 0\n" \
"v_pk_add_f16 v176, v176, v180\n" \
"v_pk_add_f16 v177, v177, v181\n" \
"ds_read_b64_tr_b16 v[152:153], v241 offset:16896\n" \
"v_mfma_f32_16x16x32_f16 v[52:55], v[128:131], v[180:183], 0\n" \
"v_pk_add_f16 v178, v178, v182\n" \
"v_pk_add_f16 v179, v179, v183\n" \
"v_mfma_f32_16x16x32_f16 v[64:67], v[132:135], v[180:183], 0\n" \
"ds_read_b128 v[172:175], v240 offset:20992\n" \
"ds_write_b64 v246, v[108:109] offset:24576\n" \
"v_mfma_f32_16x16x32_f16 v[56:59], v[136:139], v[176:179], 0\n" \
"buffer_load_dwordx4 v232, s[36:39], s80 offen offset:2048 sc1 lds\n" \
"ds_write_b64 v246, v[110:111] offset:28672\n" \
"v_mfma_f32_16x16x32_f16 v[68:71], v[140:143], v[176:179], 0\n" \
"s_setprio 0\n" \
"buffer_store_dwordx4 v[80:83], v235, s[36:39], s62 offen sc1\n" \
"ds_read_b64_tr_b16 v[154:155], v241 offset:25088\n" \
"s_waitcnt lgkmcnt(9)\n" \
"v_mfma_f32_16x16x32_f16 v[72:75], v[120:123], v[160:163], 0\n" \
"ds_read_b128 v[176:179], v240 offset:22528\n" \
"ds_write_b64 v246, v[112:113] offset:32768\n" \
"ds_read_b64_tr_b16 v[156:157], v242 offset:16896\n" \
"v_mfma_f32_16x16x32_f16 v[84:87], v[124:127], v[160:163], 0\n" \
"v_pk_add_f16 v160, v160, v164\n" \
"v_pk_add_f16 v161, v161, v165\n" \
"ds_read_b64_tr_b16 v[158:159], v242 offset:25088\n" \
"v_mfma_f32_16x16x32_f16 v[76:79], v[128:131], v[164:167], 0\n" \
"v_pk_add_f16 v162, v162, v166\n" \
"v_pk_add_f16 v163, v163, v167\n" \
"v_mfma_f32_16x16x32_f16 v[88:91], v[132:135], v[164:167], 0\n" \
"ds_read_b128 v[180:183], v240 offset:23040\n" \
"ds_write_b64 v246, v[114:115] offset:36864\n" \
"v_mfma_f32_16x16x32_f16 v[80:83], v[136:139], v[160:163], 0\n" \
"buffer_load_dwordx4 v232, s[36:39], s80 offen offset:3072 sc1 lds\n" \
"ds_write_b64 v246, v[116:117] offset:40960\n" \
"v_mfma_f32_16x16x32_f16 v[92:95], v[140:143], v[160:163], 0\n" \
"s_waitcnt lgkmcnt(8)\n" \
"v_mfma_f32_16x16x32_f16 v[96:99], v[120:123], v[168:171], 0\n" \
"v_pk_add_f16 v120, v144, v152\n" \
"v_pk_add_f16 v121, v145, v153\n" \
"ds_read_b128 v[160:163], v240 offset:24576\n" \
"v_mfma_f32_16x16x32_f16 v[108:111], v[124:127], v[168:171], 0\n" \
"v_pk_add_f16 v168, v168, v172\n" \
"v_pk_add_f16 v169, v169, v173\n" \
"ds_read_b128 v[164:167], v240 offset:25088\n" \
"v_mfma_f32_16x16x32_f16 v[100:103], v[128:131], v[172:175], 0\n" \
"v_pk_add_f16 v170, v170, v174\n" \
"v_pk_add_f16 v171, v171, v175\n" \
"s_add_i32 m0, s45, 0x0\n" \
"s_add_i32 s81, s47, 0x800000\n" \
"v_mfma_f32_16x16x32_f16 v[112:115], v[132:135], v[172:175], 0\n" \
"s_waitcnt lgkmcnt(5)\n" \
"v_pk_add_f16 v122, v146, v154\n" \
"v_pk_add_f16 v123, v147, v155\n" \
"buffer_load_dwordx4 v232, s[36:39], s81 offen sc1 lds\n" \
"v_mfma_f32_16x16x32_f16 v[104:107], v[136:139], v[168:171], 0\n" \
"v_pk_add_f16 v124, v148, v156\n" \
"v_pk_add_f16 v125, v149, v157\n" \
"v_mfma_f32_16x16x32_f16 v[116:119], v[140:143], v[168:171], 0\n" \
"s_setprio 1\n" \
"v_pk_add_f16 v126, v150, v158\n" \
"v_pk_add_f16 v127, v151, v159\n" \
"s_waitcnt vmcnt(9) lgkmcnt(0)\n" \
"s_cmp_eq_u32 s94, 0\n" \
"s_cbranch_scc1 .Lp1_%=\n" \
".Lq1_%=:\n" \
"s_memrealtime s[86:87]\n" \
"s_waitcnt lgkmcnt(0)\n" \
"s_sub_i32 s87, s86, s75\n" \
"s_cmp_lt_i32 s87, 0\n" \
"s_cbranch_scc1 .Lq1_%=\n" \
"s_lshr_b64 s[94:95], s[94:95], 8\n" \
"s_and_b32 s87, s94, 0xff\n" \
"s_add_i32 s75, s86, s87\n" \
".Lp1_%=:\n" \
"s_barrier\n" \
"v_mfma_f32_16x16x32_f16 v[0:3], v[144:147], v[176:179], v[0:3]\n" \
"ds_read_b128 v[168:171], v240 offset:47104\n" \
"ds_read_b64_tr_b16 v[184:185], v236 offset:0\n" \
"v_mfma_f32_16x16x32_f16 v[12:15], v[148:151], v[176:179], v[12:15]\n" \
"v_pk_add_f16 v176, v176, v180\n" \
"v_pk_add_f16 v177, v177, v181\n" \
"ds_read_b64_tr_b16 v[188:189], v237 offset:0\n" \
"v_mfma_f32_16x16x32_f16 v[4:7], v[152:155], v[180:183], v[4:7]\n" \
"v_pk_add_f16 v178, v178, v182\n" \
"v_pk_add_f16 v179, v179, v183\n" \
"buffer_store_dwordx4 v[212:215], v235, s[36:39], s64 offen sc1\n" \
"v_mfma_f32_16x16x32_f16 v[16:19], v[156:159], v[180:183], v[16:19]\n" \
"ds_read_b128 v[172:175], v240 offset:47616\n" \
"s_add_i32 m0, s44, 0x5000\n" \
"s_add_i32 s80, s46, 0xc00000\n" \
"v_mfma_f32_16x16x32_f16 v[8:11], v[120:123], v[176:179], v[8:11]\n" \
"buffer_load_dwordx4 v232, s[36:39], s80 offen sc1 lds\n" \
"v_mfma_f32_16x16x32_f16 v[20:23], v[124:127], v[176:179], v[20:23]\n" \
"ds_read_b64_tr_b16 v[128:129], v241 offset:32768\n" \
"ds_read_b64_tr_b16 v[130:131], v241 offset:40960\n" \
"s_waitcnt lgkmcnt(4)\n" \
"v_mfma_f32_16x16x16_bf16 v[184:187], v[184:185], v[192:193], 0\n" \
"v_mfma_f32_16x16x32_f16 v[24:27], v[144:147], v[160:163], v[24:27]\n" \
"ds_read_b128 v[176:179], v240 offset:49152\n" \
"s_waitcnt lgkmcnt(4)\n" \
"v_mfma_f32_16x16x16_bf16 v[188:191], v[188:189], v[192:193], 0\n" \
"v_mfma_f32_16x16x32_f16 v[36:39], v[148:151], v[160:163], v[36:39]\n" \
"v_pk_add_f16 v160, v160, v164\n" \
"v_pk_add_f16 v161, v161, v165\n" \
"ds_read_b64_tr_b16 v[132:133], v242 offset:32768\n" \
"v_mfma_f32_16x16x32_f16 v[28:31], v[152:155], v[164:167], v[28:31]\n" \
"v_pk_add_f16 v162, v162, v166\n" \
"v_pk_add_f16 v163, v163, v167\n" \
"v_mfma_f32_16x16x32_f16 v[40:43], v[156:159], v[164:167], v[40:43]\n" \
"v_cvt_pk_f16_f32 v184, v184, v185\n" \
"ds_read_b128 v[180:183], v240 offset:49664\n" \
"ds_read_b64_tr_b16 v[134:135], v242 offset:40960\n" \
"v_mfma_f32_16x16x32_f16 v[32:35], v[120:123], v[160:163], v[32:35]\n" \
"v_cvt_pk_f16_f32 v185, v186, v187\n" \
"v_cvt_pk_f16_f32 v186, v188, v189\n" \
"buffer_load_dwordx4 v232, s[36:39], s80 offen offset:1024 sc1 lds\n" \
"v_mfma_f32_16x16x32_f16 v[44:47], v[124:127], v[160:163], v[44:47]\n" \
"v_cvt_pk_f16_f32 v187, v190, v191\n" \
"v_add_u32 v184, s91, v184\n" \
"v_mfma_f32_16x16x32_f16 v[48:51], v[144:147], v[168:171], v[48:51]\n" \
"v_add_u32 v185, s91, v185\n" \
"v_add_u32 v186, s91, v186\n" \
"ds_read_b128 v[160:163], v240 offset:40960\n" \
"v_mfma_f32_16x16x32_f16 v[60:63], v[148:151], v[168:171], v[60:63]\n" \
"s_waitcnt lgkmcnt(4)\n" \
"v_pk_add_f16 v168, v168, v172\n" \
"v_pk_add_f16 v169, v169, v173\n" \
"v_add_u32 v187, s91, v187\n" \
"v_and_b32 v184, s92, v184\n" \
"s_add_i32 s82, s49, 0x2800000\n" \
"v_mfma_f32_16x16x32_f16 v[52:55], v[152:155], v[172:175], v[52:55]\n" \
"v_pk_add_f16 v170, v170, v174\n" \
"v_pk_add_f16 v171, v171, v175\n" \
"v_and_b32 v185, s92, v185\n" \
"v_and_b32 v186, s92, v186\n" \
"v_and_b32 v187, s92, v187\n" \
"v_mfma_f32_16x16x32_f16 v[64:67], v[156:159], v[172:175], v[64:67]\n" \
"ds_read_b128 v[164:167], v240 offset:41472\n" \
"buffer_store_dwordx4 v[184:187], v234, s[36:39], s82 offen sc1\n" \
"ds_read_b64_tr_b16 v[136:137], v241 offset:33280\n" \
"v_mfma_f32_16x16x32_f16 v[56:59], v[120:123], v[168:171], v[56:59]\n" \
"buffer_load_dwordx4 v232, s[36:39], s80 offen offset:2048 sc1 lds\n" \
"ds_read_b64_tr_b16 v[138:139], v241 offset:41472\n" \
"v_mfma_f32_16x16x32_f16 v[68:71], v[124:127], v[168:171], v[68:71]\n" \
"s_setprio 0\n" \
"ds_read_b64_tr_b16 v[184:185], v238 offset:0\n" \
"ds_read_b64_tr_b16 v[188:189], v239 offset:0\n" \
"v_mfma_f32_16x16x32_f16 v[72:75], v[144:147], v[176:179], v[72:75]\n" \
"ds_read_b128 v[168:171], v240 offset:43008\n" \
"ds_read_b64_tr_b16 v[140:141], v242 offset:33280\n" \
"v_mfma_f32_16x16x32_f16 v[84:87], v[148:151], v[176:179], v[84:87]\n" \
"s_waitcnt lgkmcnt(7)\n" \
"v_pk_add_f16 v176, v176, v180\n" \
"v_pk_add_f16 v177, v177, v181\n" \
"v_mfma_f32_16x16x32_f16 v[76:79], v[152:155], v[180:183], v[76:79]\n" \
"v_pk_add_f16 v178, v178, v182\n" \
"v_pk_add_f16 v179, v179, v183\n" \
"s_waitcnt lgkmcnt(3)\n" \
"v_mfma_f32_16x16x16_bf16 v[184:187], v[184:185], v[192:193], 0\n" \
"v_mfma_f32_16x16x32_f16 v[88:91], v[156:159], v[180:183], v[88:91]\n" \
"ds_read_b128 v[172:175], v240 offset:43520\n" \
"ds_read_b64_tr_b16 v[142:143], v242 offset:41472\n" \
"s_waitcnt lgkmcnt(4)\n" \
"v_mfma_f32_16x16x16_bf16 v[188:191], v[188:189], v[192:193], 0\n" \
"v_mfma_f32_16x16x32_f16 v[80:83], v[120:123], v[176:179], v[80:83]\n" \
"buffer_load_dwordx4 v232, s[36:39], s80 offen offset:3072 sc1 lds\n" \
"v_mfma_f32_16x16x32_f16 v[92:95], v[124:127], v[176:179], v[92:95]\n" \
"v_mfma_f32_16x16x32_f16 v[96:99], v[144:147], v[160:163], v[96:99]\n" \
"v_pk_add_f16 v144, v128, v136\n" \
"v_pk_add_f16 v145, v129, v137\n" \
"v_cvt_pk_f16_f32 v184, v184, v185\n" \
"v_cvt_pk_f16_f32 v185, v186, v187\n" \
"ds_read_b128 v[176:179], v240 offset:45056\n" \
"v_mfma_f32_16x16x32_f16 v[108:111], v[148:151], v[160:163], v[108:111]\n" \
"v_pk_add_f16 v160, v160, v164\n" \
"v_pk_add_f16 v161, v161, v165\n" \
"v_cvt_pk_f16_f32 v186, v188, v189\n" \
"v_cvt_pk_f16_f32 v187, v190, v191\n" \
"ds_read_b128 v[180:183], v240 offset:45568\n" \
"v_mfma_f32_16x16x32_f16 v[100:103], v[152:155], v[164:167], v[100:103]\n" \
"v_pk_add_f16 v162, v162, v166\n" \
"v_pk_add_f16 v163, v163, v167\n" \
"v_add_u32 v184, s91, v184\n" \
"v_add_u32 v185, s91, v185\n" \
"v_add_u32 v186, s91, v186\n" \
"s_add_i32 m0, s45, 0x5000\n" \
"s_add_i32 s81, s47, 0xc00000\n" \
"v_mfma_f32_16x16x32_f16 v[112:115], v[156:159], v[164:167], v[112:115]\n" \
"v_pk_add_f16 v146, v130, v138\n" \
"v_pk_add_f16 v147, v131, v139\n" \
"v_add_u32 v187, s91, v187\n" \
"v_and_b32 v184, s92, v184\n" \
"buffer_load_dwordx4 v232, s[36:39], s81 offen sc1 lds\n" \
"v_mfma_f32_16x16x32_f16 v[104:107], v[120:123], v[160:163], v[104:107]\n" \
"s_waitcnt lgkmcnt(2)\n" \
"v_pk_add_f16 v148, v132, v140\n" \
"v_pk_add_f16 v149, v133, v141\n" \
"v_and_b32 v185, s92, v185\n" \
"v_and_b32 v186, s92, v186\n" \
"v_and_b32 v187, s92, v187\n" \
"v_mfma_f32_16x16x32_f16 v[116:119], v[124:127], v[160:163], v[116:119]\n" \
"s_setprio 1\n" \
"v_pk_add_f16 v150, v134, v142\n" \
"v_pk_add_f16 v151, v135, v143\n" \
"buffer_store_dwordx4 v[184:187], v234, s[36:39], s82 offen offset:1024 sc1\n" \
"s_waitcnt vmcnt(8) lgkmcnt(0)\n" \
"s_cmp_eq_u32 s94, 0\n" \
"s_cbranch_scc1 .Lp2_%=\n" \
".Lq2_%=:\n" \
"s_memrealtime s[86:87]\n" \
"s_waitcnt lgkmcnt(0)\n" \
"s_sub_i32 s87, s86, s75\n" \
"s_cmp_lt_i32 s87, 0\n" \
"s_cbranch_scc1 .Lq2_%=\n" \
"s_lshr_b64 s[94:95], s[94:95], 8\n" \
"s_and_b32 s87, s94, 0xff\n" \
"s_add_i32 s75, s86, s87\n" \
".Lp2_%=:\n" \
"s_barrier\n" \
"v_mfma_f32_16x16x32_f16 v[0:3], v[128:131], v[168:171], v[0:3]\n" \
"ds_read_b128 v[160:163], v240 offset:6144\n" \
"ds_read_b64_tr_b16 v[184:185], v236 offset:4096\n" \
"ds_read_b64_tr_b16 v[188:189], v237 offset:4096\n" \
"v_mfma_f32_16x16x32_f16 v[12:15], v[132:135], v[168:171], v[12:15]\n" \
"v_pk_add_f16 v168, v168, v172\n" \
"v_pk_add_f16 v169, v169, v173\n" \
"v_mfma_f32_16x16x32_f16 v[4:7], v[136:139], v[172:175], v[4:7]\n" \
"v_pk_add_f16 v170, v170, v174\n" \
"v_pk_add_f16 v171, v171, v175\n" \
"v_mfma_f32_16x16x32_f16 v[16:19], v[140:143], v[172:175], v[16:19]\n" \
"ds_read_b128 v[164:167], v240 offset:6656\n" \
"s_add_i32 m0, s44, 0xa000\n" \
"s_add_i32 s80, s46, 0x1000000\n" \
"v_mfma_f32_16x16x32_f16 v[8:11], v[144:147], v[168:171], v[8:11]\n" \
"buffer_load_dwordx4 v232, s[36:39], s80 offen sc1 lds\n" \
"s_waitcnt lgkmcnt(2)\n" \
"v_mfma_f32_16x16x16_bf16 v[184:187], v[184:185], v[192:193], 0\n" \
"v_mfma_f32_16x16x32_f16 v[20:23], v[148:151], v[168:171], v[20:23]\n" \
"ds_read_b64_tr_b16 v[120:121], v241 offset:0\n" \
"ds_read_b64_tr_b16 v[122:123], v241 offset:8192\n" \
"s_waitcnt lgkmcnt(3)\n" \
"v_mfma_f32_16x16x16_bf16 v[188:191], v[188:189], v[192:193], 0\n" \
"v_mfma_f32_16x16x32_f16 v[24:27], v[128:131], v[176:179], v[24:27]\n" \
"ds_read_b128 v[168:171], v240 offset:8192\n" \
"v_mfma_f32_16x16x32_f16 v[36:39], v[132:135], v[176:179], v[36:39]\n" \
"v_pk_add_f16 v176, v176, v180\n" \
"v_pk_add_f16 v177, v177, v181\n" \
"ds_read_b64_tr_b16 v[124:125], v242 offset:0\n" \
"v_mfma_f32_16x16x32_f16 v[28:31], v[136:139], v[180:183], v[28:31]\n" \
"v_pk_add_f16 v178, v178, v182\n" \
"v_pk_add_f16 v179, v179, v183\n" \
"v_cvt_pk_f16_f32 v184, v184, v185\n" \
"v_mfma_f32_16x16x32_f16 v[40:43], v[140:143], v[180:183], v[40:43]\n" \
"v_cvt_pk_f16_f32 v185, v186, v187\n" \
"v_cvt_pk_f16_f32 v186, v188, v189\n" \
"ds_read_b128 v[172:175], v240 offset:8704\n" \
"v_mfma_f32_16x16x32_f16 v[32:35], v[144:147], v[176:179], v[32:35]\n" \
"v_cvt_pk_f16_f32 v187, v190, v191\n" \
"v_add_u32 v184, s91, v184\n" \
"buffer_load_dwordx4 v232, s[36:39], s80 offen offset:1024 sc1 lds\n" \
"v_mfma_f32_16x16x32_f16 v[44:47], v[148:151], v[176:179], v[44:47]\n" \
"v_add_u32 v185, s91, v185\n" \
"v_add_u32 v186, s91, v186\n" \
"ds_read_b64_tr_b16 v[126:127], v242 offset:8192\n" \
"v_mfma_f32_16x16x32_f16 v[48:51], v[128:131], v[160:163], v[48:51]\n" \
"v_add_u32 v187, s91, v187\n" \
"v_and_b32 v184, s92, v184\n" \
"ds_read_b128 v[176:179], v240 offset:0\n" \
"v_mfma_f32_16x16x32_f16 v[60:63], v[132:135], v[160:163], v[60:63]\n" \
"s_waitcnt lgkmcnt(4)\n" \
"v_pk_add_f16 v160, v160, v164\n" \
"v_pk_add_f16 v161, v161, v165\n" \
"v_and_b32 v185, s92, v185\n" \
"s_add_i32 s82, s50, 0x3000000\n" \
"v_mfma_f32_16x16x32_f16 v[52:55], v[136:139], v[164:167], v[52:55]\n" \
"v_pk_add_f16 v162, v162, v166\n" \
"v_pk_add_f16 v163, v163, v167\n" \
"v_and_b32 v186, s92, v186\n" \
"v_and_b32 v187, s92, v187\n" \
"v_mfma_f32_16x16x32_f16 v[64:67], v[140:143], v[164:167], v[64:67]\n" \
"ds_read_b128 v[180:183], v240 offset:512\n" \
"buffer_store_dwordx4 v[184:187], v234, s[36:39], s82 offen sc1\n" \
"ds_read_b64_tr_b16 v[152:153], v241 offset:512\n" \
"v_mfma_f32_16x16x32_f16 v[56:59], v[144:147], v[160:163], v[56:59]\n" \
"buffer_load_dwordx4 v232, s[36:39], s80 offen offset:2048 sc1 lds\n" \
"v_mfma_f32_16x16x32_f16 v[68:71], v[148:151], v[160:163], v[68:71]\n" \
"s_setprio 0\n" \
"ds_read_b64_tr_b16 v[154:155], v241 offset:8704\n" \
"ds_read_b64_tr_b16 v[156:157], v242 offset:512\n" \
"v_mfma_f32_16x16x32_f16 v[72:75], v[128:131], v[168:171], v[72:75]\n" \
"ds_read_b128 v[160:163], v240 offset:2048\n" \
"ds_read_b64_tr_b16 v[158:159], v242 offset:8704\n" \
"v_mfma_f32_16x16x32_f16 v[84:87], v[132:135], v[168:171], v[84:87]\n" \
"s_waitcnt lgkmcnt(6)\n" \
"v_pk_add_f16 v168, v168, v172\n" \
"v_pk_add_f16 v169, v169, v173\n" \
"v_mfma_f32_16x16x32_f16 v[76:79], v[136:139], v[172:175], v[76:79]\n" \
"v_pk_add_f16 v170, v170, v174\n" \
"v_pk_add_f16 v171, v171, v175\n" \
"v_mfma_f32_16x16x32_f16 v[88:91], v[140:143], v[172:175], v[88:91]\n" \
"ds_read_b128 v[164:167], v240 offset:2560\n" \
"v_mfma_f32_16x16x32_f16 v[80:83], v[144:147], v[168:171], v[80:83]\n" \
"buffer_load_dwordx4 v232, s[36:39], s80 offen offset:3072 sc1 lds\n" \
"v_mfma_f32_16x16x32_f16 v[92:95], v[148:151], v[168:171], v[92:95]\n" \
"v_mfma_f32_16x16x32_f16 v[96:99], v[128:131], v[176:179], v[96:99]\n" \
"s_waitcnt lgkmcnt(5)\n" \
"v_pk_add_f16 v128, v120, v152\n" \
"v_pk_add_f16 v129, v121, v153\n" \
"ds_read_b128 v[168:171], v240 offset:4096\n" \
"v_mfma_f32_16x16x32_f16 v[108:111], v[132:135], v[176:179], v[108:111]\n" \
"v_pk_add_f16 v176, v176, v180\n" \
"v_pk_add_f16 v177, v177, v181\n" \
"ds_read_b128 v[172:175], v240 offset:4608\n" \
"v_mfma_f32_16x16x32_f16 v[100:103], v[136:139], v[180:183], v[100:103]\n" \
"v_pk_add_f16 v178, v178, v182\n" \
"v_pk_add_f16 v179, v179, v183\n" \
"s_add_i32 m0, s45, 0xa000\n" \
"s_add_i32 s81, s47, 0x1000000\n" \
"v_mfma_f32_16x16x32_f16 v[112:115], v[140:143], v[180:183], v[112:115]\n" \
"s_waitcnt lgkmcnt(3)\n" \
"v_pk_add_f16 v130, v122, v154\n" \
"v_pk_add_f16 v131, v123, v155\n" \
"buffer_load_dwordx4 v232, s[36:39], s81 offen sc1 lds\n" \
"v_mfma_f32_16x16x32_f16 v[104:107], v[144:147], v[176:179], v[104:107]\n" \
"v_pk_add_f16 v132, v124, v156\n" \
"v_pk_add_f16 v133, v125, v157\n" \
"v_mfma_f32_16x16x32_f16 v[116:119], v[148:151], v[176:179], v[116:119]\n" \
"s_setprio 1\n" \
"v_pk_add_f16 v134, v126, v158\n" \
"v_pk_add_f16 v135, v127, v159\n" \
"s_waitcnt vmcnt(6) lgkmcnt(0)\n" \
"s_cmp_eq_u32 s94, 0\n" \
"s_cbranch_scc1 .Lp3_%=\n" \
".Lq3_%=:\n" \
"s_memrealtime s[86:87]\n" \
"s_waitcnt lgkmcnt(0)\n" \
"s_sub_i32 s87, s86, s75\n" \
"s_cmp_lt_i32 s87, 0\n" \
"s_cbranch_scc1 .Lq3_%=\n" \
"s_lshr_b64 s[94:95], s[94:95], 8\n" \
"s_and_b32 s87, s94, 0xff\n" \
"s_add_i32 s75, s86, s87\n" \
".Lp3_%=:\n" \
"s_barrier\n" \
"v_mfma_f32_16x16x32_f16 v[0:3], v[120:123], v[160:163], v[0:3]\n" \
"ds_read_b128 v[176:179], v240 offset:26624\n" \
"ds_read_b64_tr_b16 v[184:185], v238 offset:4096\n" \
"ds_read_b64_tr_b16 v[188:189], v239 offset:4096\n" \
"v_mfma_f32_16x16x32_f16 v[12:15], v[124:127], v[160:163], v[12:15]\n" \
"v_pk_add_f16 v160, v160, v164\n" \
"v_pk_add_f16 v161, v161, v165\n" \
"v_mfma_f32_16x16x32_f16 v[4:7], v[152:155], v[164:167], v[4:7]\n" \
"v_pk_add_f16 v162, v162, v166\n" \
"v_pk_add_f16 v163, v163, v167\n" \
"v_mfma_f32_16x16x32_f16 v[16:19], v[156:159], v[164:167], v[16:19]\n" \
"ds_read_b128 v[180:183], v240 offset:27136\n" \
"s_add_i32 m0, s44, 0x0\n" \
"s_add_i32 s80, s46, 0x1400000\n" \
"v_mfma_f32_16x16x32_f16 v[8:11], v[128:131], v[160:163], v[8:11]\n" \
"buffer_load_dwordx4 v232, s[36:39], s80 offen sc1 lds\n" \
"s_waitcnt lgkmcnt(2)\n" \
"v_mfma_f32_16x16x16_bf16 v[184:187], v[184:185], v[192:193], 0\n" \
"v_mfma_f32_16x16x32_f16 v[20:23], v[132:135], v[160:163], v[20:23]\n" \
"ds_read_b64_tr_b16 v[136:137], v241 offset:16384\n" \
"ds_read_b64_tr_b16 v[138:139], v241 offset:24576\n" \
"s_waitcnt lgkmcnt(3)\n" \
"v_mfma_f32_16x16x16_bf16 v[188:191], v[188:189], v[192:193], 0\n" \
"v_mfma_f32_16x16x32_f16 v[24:27], v[120:123], v[168:171], v[24:27]\n" \
"ds_read_b128 v[160:163], v240 offset:28672\n" \
"v_mfma_f32_16x16x32_f16 v[36:39], v[124:127], v[168:171], v[36:39]\n" \
"v_pk_add_f16 v168, v168, v172\n" \
"v_pk_add_f16 v169, v169, v173\n" \
"ds_read_b64_tr_b16 v[140:141], v242 offset:16384\n" \
"v_mfma_f32_16x16x32_f16 v[28:31], v[152:155], v[172:175], v[28:31]\n" \
"v_pk_add_f16 v170, v170, v174\n" \
"v_pk_add_f16 v171, v171, v175\n" \
"v_cvt_pk_f16_f32 v184, v184, v185\n" \
"v_mfma_f32_16x16x32_f16 v[40:43], v[156:159], v[172:175], v[40:43]\n" \
"v_cvt_pk_f16_f32 v185, v186, v187\n" \
"v_cvt_pk_f16_f32 v186, v188, v189\n" \
"ds_read_b128 v[164:167], v240 offset:29184\n" \
"v_mfma_f32_16x16x32_f16 v[32:35], v[128:131], v[168:171], v[32:35]\n" \
"v_cvt_pk_f16_f32 v187, v190, v191\n" \
"v_add_u32 v184, s91, v184\n" \
"buffer_load_dwordx4 v232, s[36:39], s80 offen offset:1024 sc1 lds\n" \
"v_mfma_f32_16x16x32_f16 v[44:47], v[132:135], v[168:171], v[44:47]\n" \
"v_add_u32 v185, s91, v185\n" \
"v_add_u32 v186, s91, v186\n" \
"ds_read_b64_tr_b16 v[142:143], v242 offset:24576\n" \
"v_mfma_f32_16x16x32_f16 v[48:51], v[120:123], v[176:179], v[48:51]\n" \
"v_add_u32 v187, s91, v187\n" \
"v_and_b32 v184, s92, v184\n" \
"ds_read_b128 v[168:171], v240 offset:20480\n" \
"v_mfma_f32_16x16x32_f16 v[60:63], v[124:127], v[176:179], v[60:63]\n" \
"s_waitcnt lgkmcnt(4)\n" \
"v_pk_add_f16 v176, v176, v180\n" \
"v_pk_add_f16 v177, v177, v181\n" \
"v_and_b32 v185, s92, v185\n" \
"v_mfma_f32_16x16x32_f16 v[52:55], v[152:155], v[180:183], v[52:55]\n" \
"v_pk_add_f16 v178, v178, v182\n" \
"v_pk_add_f16 v179, v179, v183\n" \
"v_and_b32 v186, s92, v186\n" \
"v_and_b32 v187, s92, v187\n" \
"v_mfma_f32_16x16x32_f16 v[64:67], v[156:159], v[180:183], v[64:67]\n" \
"ds_read_b128 v[172:175], v240 offset:20992\n" \
"buffer_store_dwordx4 v[184:187], v234, s[36:39], s82 offen offset:1024 sc1\n" \
"ds_read_b64_tr_b16 v[144:145], v241 offset:16896\n" \
"v_mfma_f32_16x16x32_f16 v[56:59], v[128:131], v[176:179], v[56:59]\n" \
"buffer_load_dwordx4 v232, s[36:39], s80 offen offset:2048 sc1 lds\n" \
"v_mfma_f32_16x16x32_f16 v[68:71], v[132:135], v[176:179], v[68:71]\n" \
"s_setprio 0\n" \
"ds_read_b64_tr_b16 v[146:147], v241 offset:25088\n" \
"ds_read_b64_tr_b16 v[148:149], v242 offset:16896\n" \
"v_mfma_f32_16x16x32_f16 v[72:75], v[120:123], v[160:163], v[72:75]\n" \
"ds_read_b128 v[176:179], v240 offset:22528\n" \
"ds_read_b64_tr_b16 v[150:151], v242 offset:25088\n" \
"v_mfma_f32_16x16x32_f16 v[84:87], v[124:127], v[160:163], v[84:87]\n" \
"s_waitcnt lgkmcnt(6)\n" \
"v_pk_add_f16 v160, v160, v164\n" \
"v_pk_add_f16 v161, v161, v165\n" \
"v_mfma_f32_16x16x32_f16 v[76:79], v[152:155], v[164:167], v[76:79]\n" \
"v_pk_add_f16 v162, v162, v166\n" \
"v_pk_add_f16 v163, v163, v167\n" \
"v_mfma_f32_16x16x32_f16 v[88:91], v[156:159], v[164:167], v[88:91]\n" \
"ds_read_b128 v[180:183], v240 offset:23040\n" \
"v_mfma_f32_16x16x32_f16 v[80:83], v[128:131], v[160:163], v[80:83]\n" \
"buffer_load_dwordx4 v232, s[36:39], s80 offen offset:3072 sc1 lds\n" \
"v_mfma_f32_16x16x32_f16 v[92:95], v[132:135], v[160:163], v[92:95]\n" \
"v_mfma_f32_16x16x32_f16 v[96:99], v[120:123], v[168:171], v[96:99]\n" \
"s_waitcnt lgkmcnt(5)\n" \
"v_pk_add_f16 v120, v136, v144\n" \
"v_pk_add_f16 v121, v137, v145\n" \
"ds_read_b128 v[160:163], v240 offset:24576\n" \
"v_mfma_f32_16x16x32_f16 v[108:111], v[124:127], v[168:171], v[108:111]\n" \
"v_pk_add_f16 v168, v168, v172\n" \
"v_pk_add_f16 v169, v169, v173\n" \
"ds_read_b128 v[164:167], v240 offset:25088\n" \
"v_mfma_f32_16x16x32_f16 v[100:103], v[152:155], v[172:175], v[100:103]\n" \
"v_pk_add_f16 v170, v170, v174\n" \
"v_pk_add_f16 v171, v171, v175\n" \
"s_add_i32 m0, s45, 0x0\n" \
"s_add_i32 s81, s47, 0x1400000\n" \
"v_mfma_f32_16x16x32_f16 v[112:115], v[156:159], v[172:175], v[112:115]\n" \
"s_waitcnt lgkmcnt(3)\n" \
"v_pk_add_f16 v122, v138, v146\n" \
"v_pk_add_f16 v123, v139, v147\n" \
"buffer_load_dwordx4 v232, s[36:39], s81 offen sc1 lds\n" \
"v_mfma_f32_16x16x32_f16 v[104:107], v[128:131], v[168:171], v[104:107]\n" \
"v_pk_add_f16 v124, v140, v148\n" \
"v_pk_add_f16 v125, v141, v149\n" \
"v_mfma_f32_16x16x32_f16 v[116:119], v[132:135], v[168:171], v[116:119]\n" \
"s_setprio 1\n" \
"v_pk_add_f16 v126, v142, v150\n" \
"v_pk_add_f16 v127, v143, v151\n" \
"s_waitcnt vmcnt(6) lgkmcnt(0)\n" \
"s_cmp_eq_u32 s94, 0\n" \
"s_cbranch_scc1 .Lp4_%=\n" \
".Lq4_%=:\n" \
"s_memrealtime s[86:87]\n" \
"s_waitcnt lgkmcnt(0)\n" \
"s_sub_i32 s87, s86, s75\n" \
"s_cmp_lt_i32 s87, 0\n" \
"s_cbranch_scc1 .Lq4_%=\n" \
"s_lshr_b64 s[94:95], s[94:95], 8\n" \
"s_and_b32 s87, s94, 0xff\n" \
"s_add_i32 s75, s86, s87\n" \
".Lp4_%=:\n" \
"s_barrier\n" \
"v_mfma_f32_16x16x32_f16 v[0:3], v[136:139], v[176:179], v[0:3]\n" \
"ds_read_b128 v[168:171], v240 offset:47104\n" \
"ds_read_b64_tr_b16 v[184:185], v236 offset:8192\n" \
"ds_read_b64_tr_b16 v[188:189], v237 offset:8192\n" \
"v_mfma_f32_16x16x32_f16 v[12:15], v[140:143], v[176:179], v[12:15]\n" \
"v_pk_add_f16 v176, v176, v180\n" \
"v_pk_add_f16 v177, v177, v181\n" \
"s_mov_b32 s84, 0x8004000\n" \
"v_mfma_f32_16x16x32_f16 v[4:7], v[144:147], v[180:183], v[4:7]\n" \
"v_pk_add_f16 v178, v178, v182\n" \
"v_pk_add_f16 v179, v179, v183\n" \
"buffer_load_dword v253, v252, s[36:39], s84 offen sc1\n" \
"buffer_store_dwordx4 v[216:219], v235, s[36:39], s65 offen sc1\n" \
"v_mfma_f32_16x16x32_f16 v[16:19], v[148:151], v[180:183], v[16:19]\n" \
"ds_read_b128 v[172:175], v240 offset:47616\n" \
"s_add_i32 m0, s44, 0x5000\n" \
"s_add_i32 s80, s46, 0x1800000\n" \
"v_mfma_f32_16x16x32_f16 v[8:11], v[120:123], v[176:179], v[8:11]\n" \
"buffer_load_dwordx4 v232, s[36:39], s80 offen sc1 lds\n" \
"s_waitcnt lgkmcnt(2)\n" \
"v_mfma_f32_16x16x16_bf16 v[184:187], v[184:185], v[192:193], 0\n" \
"v_mfma_f32_16x16x32_f16 v[20:23], v[124:127], v[176:179], v[20:23]\n" \
"ds_read_b64_tr_b16 v[128:129], v241 offset:32768\n" \
"ds_read_b64_tr_b16 v[130:131], v241 offset:40960\n" \
"s_waitcnt lgkmcnt(3)\n" \
"v_mfma_f32_16x16x16_bf16 v[188:191], v[188:189], v[192:193], 0\n" \
"v_mfma_f32_16x16x32_f16 v[24:27], v[136:139], v[160:163], v[24:27]\n" \
"ds_read_b128 v[176:179], v240 offset:49152\n" \
"v_mfma_f32_16x16x32_f16 v[36:39], v[140:143], v[160:163], v[36:39]\n" \
"v_pk_add_f16 v160, v160, v164\n" \
"v_pk_add_f16 v161, v161, v165\n" \
"ds_read_b64_tr_b16 v[132:133], v242 offset:32768\n" \
"v_mfma_f32_16x16x32_f16 v[28:31], v[144:147], v[164:167], v[28:31]\n" \
"v_pk_add_f16 v162, v162, v166\n" \
"v_pk_add_f16 v163, v163, v167\n" \
"v_cvt_pk_f16_f32 v184, v184, v185\n" \
"v_mfma_f32_16x16x32_f16 v[40:43], v[148:151], v[164:167], v[40:43]\n" \
"v_cvt_pk_f16_f32 v185, v186, v187\n" \
"v_cvt_pk_f16_f32 v186, v188, v189\n" \
"ds_read_b128 v[180:183], v240 offset:49664\n" \
"v_mfma_f32_16x16x32_f16 v[32:35], v[120:123], v[160:163], v[32:35]\n" \
"v_cvt_pk_f16_f32 v187, v190, v191\n" \
"v_add_u32 v184, s91, v184\n" \
"buffer_load_dwordx4 v232, s[36:39], s80 offen offset:1024 sc1 lds\n" \
"v_mfma_f32_16x16x32_f16 v[44:47], v[124:127], v[160:163], v[44:47]\n" \
"v_add_u32 v185, s91, v185\n" \
"v_add_u32 v186, s91, v186\n" \
"ds_read_b64_tr_b16 v[134:135], v242 offset:40960\n" \
"v_mfma_f32_16x16x32_f16 v[48:51], v[136:139], v[168:171], v[48:51]\n" \
"v_add_u32 v187, s91, v187\n" \
"v_and_b32 v184, s92, v184\n" \
"ds_read_b128 v[160:163], v240 offset:40960\n" \
"v_mfma_f32_16x16x32_f16 v[60:63], v[140:143], v[168:171], v[60:63]\n" \
"s_waitcnt lgkmcnt(4)\n" \
"v_pk_add_f16 v168, v168, v172\n" \
"v_pk_add_f16 v169, v169, v173\n" \
"v_and_b32 v185, s92, v185\n" \
"s_add_i32 s82, s51, 0x3800000\n" \
"v_mfma_f32_16x16x32_f16 v[52:55], v[144:147], v[172:175], v[52:55]\n" \
"v_pk_add_f16 v170, v170, v174\n" \
"v_pk_add_f16 v171, v171, v175\n" \
"v_and_b32 v186, s92, v186\n" \
"v_and_b32 v187, s92, v187\n" \
"v_mfma_f32_16x16x32_f16 v[64:67], v[148:151], v[172:175], v[64:67]\n" \
"ds_read_b128 v[164:167], v240 offset:41472\n" \
"buffer_store_dwordx4 v[184:187], v234, s[36:39], s82 offen sc1\n" \
"ds_read_b64_tr_b16 v[152:153], v241 offset:33280\n" \
"v_mfma_f32_16x16x32_f16 v[56:59], v[120:123], v[168:171], v[56:59]\n" \
"buffer_load_dwordx4 v232, s[36:39], s80 offen offset:2048 sc1 lds\n" \
"v_mfma_f32_16x16x32_f16 v[68:71], v[124:127], v[168:171], v[68:71]\n" \
"s_setprio 0\n" \
"ds_read_b64_tr_b16 v[154:155], v241 offset:41472\n" \
"ds_read_b64_tr_b16 v[156:157], v242 offset:33280\n" \
"v_mfma_f32_16x16x32_f16 v[72:75], v[136:139], v[176:179], v[72:75]\n" \
"ds_read_b128 v[168:171], v240 offset:43008\n" \
"ds_read_b64_tr_b16 v[158:159], v242 offset:41472\n" \
"v_mfma_f32_16x16x32_f16 v[84:87], v[140:143], v[176:179], v[84:87]\n" \
"s_waitcnt lgkmcnt(6)\n" \
"v_pk_add_f16 v176, v176, v180\n" \
"v_pk_add_f16 v177, v177, v181\n" \
"v_mfma_f32_16x16x32_f16 v[76:79], v[144:147], v[180:183], v[76:79]\n" \
"v_pk_add_f16 v178, v178, v182\n" \
"v_pk_add_f16 v179, v179, v183\n" \
"v_mfma_f32_16x16x32_f16 v[88:91], v[148:151], v[180:183], v[88:91]\n" \
"ds_read_b128 v[172:175], v240 offset:43520\n" \
"v_mfma_f32_16x16x32_f16 v[80:83], v[120:123], v[176:179], v[80:83]\n" \
"buffer_load_dwordx4 v232, s[36:39], s80 offen offset:3072 sc1 lds\n" \
"v_mfma_f32_16x16x32_f16 v[92:95], v[124:127], v[176:179], v[92:95]\n" \
"v_mfma_f32_16x16x32_f16 v[96:99], v[136:139], v[160:163], v[96:99]\n" \
"s_waitcnt lgkmcnt(5)\n" \
"v_pk_add_f16 v136, v128, v152\n" \
"v_pk_add_f16 v137, v129, v153\n" \
"ds_read_b128 v[176:179], v240 offset:45056\n" \
"v_mfma_f32_16x16x32_f16 v[108:111], v[140:143], v[160:163], v[108:111]\n" \
"v_pk_add_f16 v160, v160, v164\n" \
"v_pk_add_f16 v161, v161, v165\n" \
"ds_read_b128 v[180:183], v240 offset:45568\n" \
"v_mfma_f32_16x16x32_f16 v[100:103], v[144:147], v[164:167], v[100:103]\n" \
"v_pk_add_f16 v162, v162, v166\n" \
"v_pk_add_f16 v163, v163, v167\n" \
"s_waitcnt vmcnt(6)\n" \
"v_cmp_eq_u32 vcc, 0, v253\n" \
"s_cbranch_vccnz .Ls12_%=\n" \
".Lr12_%=:\n" \
"s_add_i32 m0, s45, 0x5000\n" \
"s_add_i32 s81, s47, 0x1800000\n" \
"v_mfma_f32_16x16x32_f16 v[112:115], v[148:151], v[164:167], v[112:115]\n" \
"s_waitcnt lgkmcnt(3)\n" \
"v_pk_add_f16 v138, v130, v154\n" \
"v_pk_add_f16 v139, v131, v155\n" \
"buffer_load_dwordx4 v232, s[36:39], s81 offen sc1 lds\n" \
"v_mfma_f32_16x16x32_f16 v[104:107], v[120:123], v[160:163], v[104:107]\n" \
"v_pk_add_f16 v140, v132, v156\n" \
"v_pk_add_f16 v141, v133, v157\n" \
"v_mfma_f32_16x16x32_f16 v[116:119], v[124:127], v[160:163], v[116:119]\n" \
"s_setprio 1\n" \
"v_pk_add_f16 v142, v134, v158\n" \
"v_pk_add_f16 v143, v135, v159\n" \
"s_waitcnt vmcnt(7) lgkmcnt(0)\n" \
"s_cmp_eq_u32 s94, 0\n" \
"s_cbranch_scc1 .Lp5_%=\n" \
".Lq5_%=:\n" \
"s_memrealtime s[86:87]\n" \
"s_waitcnt lgkmcnt(0)\n" \
"s_sub_i32 s87, s86, s75\n" \
"s_cmp_lt_i32 s87, 0\n" \
"s_cbranch_scc1 .Lq5_%=\n" \
"s_lshr_b64 s[94:95], s[94:95], 8\n" \
"s_and_b32 s87, s94, 0xff\n" \
"s_add_i32 s75, s86, s87\n" \
".Lp5_%=:\n" \
"s_barrier\n" \
"v_mfma_f32_16x16x32_f16 v[0:3], v[128:131], v[168:171], v[0:3]\n" \
"ds_read_b128 v[160:163], v240 offset:6144\n" \
"ds_read_b64_tr_b16 v[184:185], v238 offset:8192\n" \
"ds_read_b64_tr_b16 v[188:189], v239 offset:8192\n" \
"v_mfma_f32_16x16x32_f16 v[12:15], v[132:135], v[168:171], v[12:15]\n" \
"v_pk_add_f16 v168, v168, v172\n" \
"v_pk_add_f16 v169, v169, v173\n" \
"v_mfma_f32_16x16x32_f16 v[4:7], v[152:155], v[172:175], v[4:7]\n" \
"v_pk_add_f16 v170, v170, v174\n" \
"v_pk_add_f16 v171, v171, v175\n" \
"v_mfma_f32_16x16x32_f16 v[16:19], v[156:159], v[172:175], v[16:19]\n" \
"ds_read_b128 v[164:167], v240 offset:6656\n" \
"s_add_i32 m0, s44, 0xa000\n" \
"s_add_i32 s80, s46, 0x1c00000\n" \
"v_mfma_f32_16x16x32_f16 v[8:11], v[136:139], v[168:171], v[8:11]\n" \
"buffer_load_dwordx4 v232, s[36:39], s80 offen sc1 lds\n" \
"s_waitcnt lgkmcnt(2)\n" \
"v_mfma_f32_16x16x16_bf16 v[184:187], v[184:185], v[192:193], 0\n" \
"v_mfma_f32_16x16x32_f16 v[20:23], v[140:143], v[168:171], v[20:23]\n" \
"ds_read_b64_tr_b16 v[120:121], v241 offset:0\n" \
"ds_read_b64_tr_b16 v[122:123], v241 offset:8192\n" \
"s_waitcnt lgkmcnt(3)\n" \
"v_mfma_f32_16x16x16_bf16 v[188:191], v[188:189], v[192:193], 0\n" \
"v_mfma_f32_16x16x32_f16 v[24:27], v[128:131], v[176:179], v[24:27]\n" \
"ds_read_b128 v[168:171], v240 offset:8192\n" \
"v_mfma_f32_16x16x32_f16 v[36:39], v[132:135], v[176:179], v[36:39]\n" \
"v_pk_add_f16 v176, v176, v180\n" \
"v_pk_add_f16 v177, v177, v181\n" \
"ds_read_b64_tr_b16 v[124:125], v242 offset:0\n" \
"v_mfma_f32_16x16x32_f16 v[28:31], v[152:155], v[180:183], v[28:31]\n" \
"v_pk_add_f16 v178, v178, v182\n" \
"v_pk_add_f16 v179, v179, v183\n" \
"v_cvt_pk_f16_f32 v184, v184, v185\n" \
"v_mfma_f32_16x16x32_f16 v[40:43], v[156:159], v[180:183], v[40:43]\n" \
"v_cvt_pk_f16_f32 v185, v186, v187\n" \
"v_cvt_pk_f16_f32 v186, v188, v189\n" \
"ds_read_b128 v[172:175], v240 offset:8704\n" \
"v_mfma_f32_16x16x32_f16 v[32:35], v[136:139], v[176:179], v[32:35]\n" \
"v_cvt_pk_f16_f32 v187, v190, v191\n" \
"v_add_u32 v184, s91, v184\n" \
"buffer_load_dwordx4 v232, s[36:39], s80 offen offset:1024 sc1 lds\n" \
"v_mfma_f32_16x16x32_f16 v[44:47], v[140:143], v[176:179], v[44:47]\n" \
"v_add_u32 v185, s91, v185\n" \
"v_add_u32 v186, s91, v186\n" \
"ds_read_b64_tr_b16 v[126:127], v242 offset:8192\n" \
"v_mfma_f32_16x16x32_f16 v[48:51], v[128:131], v[160:163], v[48:51]\n" \
"v_add_u32 v187, s91, v187\n" \
"v_and_b32 v184, s92, v184\n" \
"ds_read_b128 v[176:179], v240 offset:0\n" \
"v_mfma_f32_16x16x32_f16 v[60:63], v[132:135], v[160:163], v[60:63]\n" \
"s_waitcnt lgkmcnt(4)\n" \
"v_pk_add_f16 v160, v160, v164\n" \
"v_pk_add_f16 v161, v161, v165\n" \
"v_and_b32 v185, s92, v185\n" \
"v_mfma_f32_16x16x32_f16 v[52:55], v[152:155], v[164:167], v[52:55]\n" \
"v_pk_add_f16 v162, v162, v166\n" \
"v_pk_add_f16 v163, v163, v167\n" \
"v_and_b32 v186, s92, v186\n" \
"v_and_b32 v187, s92, v187\n" \
"v_mfma_f32_16x16x32_f16 v[64:67], v[156:159], v[164:167], v[64:67]\n" \
"ds_read_b128 v[180:183], v240 offset:512\n" \
"buffer_store_dwordx4 v[184:187], v234, s[36:39], s82 offen offset:1024 sc1\n" \
"ds_read_b64_tr_b16 v[144:145], v241 offset:512\n" \
"v_mfma_f32_16x16x32_f16 v[56:59], v[136:139], v[160:163], v[56:59]\n" \
"buffer_load_dwordx4 v232, s[36:39], s80 offen offset:2048 sc1 lds\n" \
"v_mfma_f32_16x16x32_f16 v[68:71], v[140:143], v[160:163], v[68:71]\n" \
"s_setprio 0\n" \
"ds_read_b64_tr_b16 v[146:147], v241 offset:8704\n" \
"ds_read_b64_tr_b16 v[148:149], v242 offset:512\n" \
"v_mfma_f32_16x16x32_f16 v[72:75], v[128:131], v[168:171], v[72:75]\n" \
"ds_read_b128 v[160:163], v240 offset:2048\n" \
"ds_read_b64_tr_b16 v[150:151], v242 offset:8704\n" \
"v_mfma_f32_16x16x32_f16 v[84:87], v[132:135], v[168:171], v[84:87]\n" \
"s_waitcnt lgkmcnt(6)\n" \
"v_pk_add_f16 v168, v168, v172\n" \
"v_pk_add_f16 v169, v169, v173\n" \
"v_mfma_f32_16x16x32_f16 v[76:79], v[152:155], v[172:175], v[76:79]\n" \
"v_pk_add_f16 v170, v170, v174\n" \
"v_pk_add_f16 v171, v171, v175\n" \
"v_mfma_f32_16x16x32_f16 v[88:91], v[156:159], v[172:175], v[88:91]\n" \
"ds_read_b128 v[164:167], v240 offset:2560\n" \
"v_mfma_f32_16x16x32_f16 v[80:83], v[136:139], v[168:171], v[80:83]\n" \
"buffer_load_dwordx4 v232, s[36:39], s80 offen offset:3072 sc1 lds\n" \
"v_mfma_f32_16x16x32_f16 v[92:95], v[140:143], v[168:171], v[92:95]\n" \
"v_mfma_f32_16x16x32_f16 v[96:99], v[128:131], v[176:179], v[96:99]\n" \
"s_waitcnt lgkmcnt(5)\n" \
"v_pk_add_f16 v128, v120, v144\n" \
"v_pk_add_f16 v129, v121, v145\n" \
"ds_read_b128 v[168:171], v240 offset:4096\n" \
"v_mfma_f32_16x16x32_f16 v[108:111], v[132:135], v[176:179], v[108:111]\n" \
"v_pk_add_f16 v176, v176, v180\n" \
"v_pk_add_f16 v177, v177, v181\n" \
"ds_read_b128 v[172:175], v240 offset:4608\n" \
"v_mfma_f32_16x16x32_f16 v[100:103], v[152:155], v[180:183], v[100:103]\n" \
"v_pk_add_f16 v178, v178, v182\n" \
"v_pk_add_f16 v179, v179, v183\n" \
"s_add_i32 m0, s45, 0xa000\n" \
"s_add_i32 s81, s47, 0x1c00000\n" \
"v_mfma_f32_16x16x32_f16 v[112:115], v[156:159], v[180:183], v[112:115]\n" \
"s_waitcnt lgkmcnt(3)\n" \
"v_pk_add_f16 v130, v122, v146\n" \
"v_pk_add_f16 v131, v123, v147\n" \
"buffer_load_dwordx4 v232, s[36:39], s81 offen sc1 lds\n" \
"v_mfma_f32_16x16x32_f16 v[104:107], v[136:139], v[176:179], v[104:107]\n" \
"v_pk_add_f16 v132, v124, v148\n" \
"v_pk_add_f16 v133, v125, v149\n" \
"v_mfma_f32_16x16x32_f16 v[116:119], v[140:143], v[176:179], v[116:119]\n" \
"s_setprio 1\n" \
"v_pk_add_f16 v134, v126, v150\n" \
"v_pk_add_f16 v135, v127, v151\n" \
"s_waitcnt vmcnt(6) lgkmcnt(0)\n" \
"s_cmp_eq_u32 s94, 0\n" \
"s_cbranch_scc1 .Lp6_%=\n" \
".Lq6_%=:\n" \
"s_memrealtime s[86:87]\n" \
"s_waitcnt lgkmcnt(0)\n" \
"s_sub_i32 s87, s86, s75\n" \
"s_cmp_lt_i32 s87, 0\n" \
"s_cbranch_scc1 .Lq6_%=\n" \
"s_lshr_b64 s[94:95], s[94:95], 8\n" \
"s_and_b32 s87, s94, 0xff\n" \
"s_add_i32 s75, s86, s87\n" \
".Lp6_%=:\n" \
"s_barrier\n" \
"v_mfma_f32_16x16x32_f16 v[0:3], v[120:123], v[160:163], v[0:3]\n" \
"ds_read_b128 v[176:179], v240 offset:26624\n" \
"ds_read_b64_tr_b16 v[184:185], v236 offset:12288\n" \
"ds_read_b64_tr_b16 v[188:189], v237 offset:12288\n" \
"v_mfma_f32_16x16x32_f16 v[12:15], v[124:127], v[160:163], v[12:15]\n" \
"v_pk_add_f16 v160, v160, v164\n" \
"v_pk_add_f16 v161, v161, v165\n" \
"v_mfma_f32_16x16x32_f16 v[4:7], v[144:147], v[164:167], v[4:7]\n" \
"v_pk_add_f16 v162, v162, v166\n" \
"v_pk_add_f16 v163, v163, v167\n" \
"v_mfma_f32_16x16x32_f16 v[16:19], v[148:151], v[164:167], v[16:19]\n" \
"ds_read_b128 v[180:183], v240 offset:27136\n" \
"s_add_i32 m0, s44, 0x0\n" \
"s_add_i32 s80, s46, 0x2000000\n" \
"v_mfma_f32_16x16x32_f16 v[8:11], v[128:131], v[160:163], v[8:11]\n" \
"buffer_load_dwordx4 v232, s[36:39], s80 offen sc1 lds\n" \
"s_waitcnt lgkmcnt(2)\n" \
"v_mfma_f32_16x16x16_bf16 v[184:187], v[184:185], v[192:193], 0\n" \
"v_mfma_f32_16x16x32_f16 v[20:23], v[132:135], v[160:163], v[20:23]\n" \
"ds_read_b64_tr_b16 v[136:137], v241 offset:16384\n" \
"ds_read_b64_tr_b16 v[138:139], v241 offset:24576\n" \
"s_waitcnt lgkmcnt(3)\n" \
"v_mfma_f32_16x16x16_bf16 v[188:191], v[188:189], v[192:193], 0\n" \
"v_mfma_f32_16x16x32_f16 v[24:27], v[120:123], v[168:171], v[24:27]\n" \
"ds_read_b128 v[160:163], v240 offset:28672\n" \
"v_mfma_f32_16x16x32_f16 v[36:39], v[124:127], v[168:171], v[36:39]\n" \
"v_pk_add_f16 v168, v168, v172\n" \
"v_pk_add_f16 v169, v169, v173\n" \
"ds_read_b64_tr_b16 v[140:141], v242 offset:16384\n" \
"v_mfma_f32_16x16x32_f16 v[28:31], v[144:147], v[172:175], v[28:31]\n" \
"v_pk_add_f16 v170, v170, v174\n" \
"v_pk_add_f16 v171, v171, v175\n" \
"v_cvt_pk_f16_f32 v184, v184, v185\n" \
"v_mfma_f32_16x16x32_f16 v[40:43], v[148:151], v[172:175], v[40:43]\n" \
"v_cvt_pk_f16_f32 v185, v186, v187\n" \
"v_cvt_pk_f16_f32 v186, v188, v189\n" \
"ds_read_b128 v[164:167], v240 offset:29184\n" \
"v_mfma_f32_16x16x32_f16 v[32:35], v[128:131], v[168:171], v[32:35]\n" \
"v_cvt_pk_f16_f32 v187, v190, v191\n" \
"v_add_u32 v184, s91, v184\n" \
"buffer_load_dwordx4 v232, s[36:39], s80 offen offset:1024 sc1 lds\n" \
"v_mfma_f32_16x16x32_f16 v[44:47], v[132:135], v[168:171], v[44:47]\n" \
"v_add_u32 v185, s91, v185\n" \
"v_add_u32 v186, s91, v186\n" \
"ds_read_b64_tr_b16 v[142:143], v242 offset:24576\n" \
"v_mfma_f32_16x16x32_f16 v[48:51], v[120:123], v[176:179], v[48:51]\n" \
"v_add_u32 v187, s91, v187\n" \
"v_and_b32 v184, s92, v184\n" \
"ds_read_b128 v[168:171], v240 offset:20480\n" \
"v_mfma_f32_16x16x32_f16 v[60:63], v[124:127], v[176:179], v[60:63]\n" \
"s_waitcnt lgkmcnt(4)\n" \
"v_pk_add_f16 v176, v176, v180\n" \
"v_pk_add_f16 v177, v177, v181\n" \
"v_and_b32 v185, s92, v185\n" \
"s_add_i32 s82, s48, 0x4000000\n" \
"v_mfma_f32_16x16x32_f16 v[52:55], v[144:147], v[180:183], v[52:55]\n" \
"v_pk_add_f16 v178, v178, v182\n" \
"v_pk_add_f16 v179, v179, v183\n" \
"v_and_b32 v186, s92, v186\n" \
"v_and_b32 v187, s92, v187\n" \
"v_mfma_f32_16x16x32_f16 v[64:67], v[148:151], v[180:183], v[64:67]\n" \
"ds_read_b128 v[172:175], v240 offset:20992\n" \
"buffer_store_dwordx4 v[184:187], v234, s[36:39], s82 offen sc1\n" \
"ds_read_b64_tr_b16 v[152:153], v241 offset:16896\n" \
"v_mfma_f32_16x16x32_f16 v[56:59], v[128:131], v[176:179], v[56:59]\n" \
"buffer_load_dwordx4 v232, s[36:39], s80 offen offset:2048 sc1 lds\n" \
"v_mfma_f32_16x16x32_f16 v[68:71], v[132:135], v[176:179], v[68:71]\n" \
"s_setprio 0\n" \
"ds_read_b64_tr_b16 v[154:155], v241 offset:25088\n" \
"ds_read_b64_tr_b16 v[156:157], v242 offset:16896\n" \
"v_mfma_f32_16x16x32_f16 v[72:75], v[120:123], v[160:163], v[72:75]\n" \
"ds_read_b128 v[176:179], v240 offset:22528\n" \
"ds_read_b64_tr_b16 v[158:159], v242 offset:25088\n" \
"v_mfma_f32_16x16x32_f16 v[84:87], v[124:127], v[160:163], v[84:87]\n" \
"s_waitcnt lgkmcnt(6)\n" \
"v_pk_add_f16 v160, v160, v164\n" \
"v_pk_add_f16 v161, v161, v165\n" \
"v_mfma_f32_16x16x32_f16 v[76:79], v[144:147], v[164:167], v[76:79]\n" \
"v_pk_add_f16 v162, v162, v166\n" \
"v_pk_add_f16 v163, v163, v167\n" \
"v_mfma_f32_16x16x32_f16 v[88:91], v[148:151], v[164:167], v[88:91]\n" \
"ds_read_b128 v[180:183], v240 offset:23040\n" \
"v_mfma_f32_16x16x32_f16 v[80:83], v[128:131], v[160:163], v[80:83]\n" \
"buffer_load_dwordx4 v232, s[36:39], s80 offen offset:3072 sc1 lds\n" \
"v_mfma_f32_16x16x32_f16 v[92:95], v[132:135], v[160:163], v[92:95]\n" \
"v_mfma_f32_16x16x32_f16 v[96:99], v[120:123], v[168:171], v[96:99]\n" \
"s_waitcnt lgkmcnt(5)\n" \
"v_pk_add_f16 v120, v136, v152\n" \
"v_pk_add_f16 v121, v137, v153\n" \
"ds_read_b128 v[160:163], v240 offset:24576\n" \
"v_mfma_f32_16x16x32_f16 v[108:111], v[124:127], v[168:171], v[108:111]\n" \
"v_pk_add_f16 v168, v168, v172\n" \
"v_pk_add_f16 v169, v169, v173\n" \
"ds_read_b128 v[164:167], v240 offset:25088\n" \
"v_mfma_f32_16x16x32_f16 v[100:103], v[144:147], v[172:175], v[100:103]\n" \
"v_pk_add_f16 v170, v170, v174\n" \
"v_pk_add_f16 v171, v171, v175\n" \
"s_add_i32 m0, s45, 0x0\n" \
"s_add_i32 s81, s47, 0x2000000\n" \
"v_mfma_f32_16x16x32_f16 v[112:115], v[148:151], v[172:175], v[112:115]\n" \
"s_waitcnt lgkmcnt(3)\n" \
"v_pk_add_f16 v122, v138, v154\n" \
"v_pk_add_f16 v123, v139, v155\n" \
"buffer_load_dwordx4 v232, s[36:39], s81 offen sc1 lds\n" \
"v_mfma_f32_16x16x32_f16 v[104:107], v[128:131], v[168:171], v[104:107]\n" \
"v_pk_add_f16 v124, v140, v156\n" \
"v_pk_add_f16 v125, v141, v157\n" \
"v_mfma_f32_16x16x32_f16 v[116:119], v[132:135], v[168:171], v[116:119]\n" \
"s_setprio 1\n" \
"v_pk_add_f16 v126, v142, v158\n" \
"v_pk_add_f16 v127, v143, v159\n" \
"s_waitcnt vmcnt(6) lgkmcnt(0)\n" \
"s_cmp_eq_u32 s94, 0\n" \
"s_cbranch_scc1 .Lp7_%=\n" \
".Lq7_%=:\n" \
"s_memrealtime s[86:87]\n" \
"s_waitcnt lgkmcnt(0)\n" \
"s_sub_i32 s87, s86, s75\n" \
"s_cmp_lt_i32 s87, 0\n" \
"s_cbranch_scc1 .Lq7_%=\n" \
"s_lshr_b64 s[94:95], s[94:95], 8\n" \
"s_and_b32 s87, s94, 0xff\n" \
"s_add_i32 s75, s86, s87\n" \
".Lp7_%=:\n" \
"s_barrier\n" \
"v_mfma_f32_16x16x32_f16 v[0:3], v[136:139], v[176:179], v[0:3]\n" \
"ds_read_b128 v[168:171], v240 offset:47104\n" \
"ds_read_b64_tr_b16 v[184:185], v238 offset:12288\n" \
"ds_read_b64_tr_b16 v[188:189], v239 offset:12288\n" \
"v_mfma_f32_16x16x32_f16 v[12:15], v[140:143], v[176:179], v[12:15]\n" \
"v_pk_add_f16 v176, v176, v180\n" \
"v_pk_add_f16 v177, v177, v181\n" \
"v_mfma_f32_16x16x32_f16 v[4:7], v[152:155], v[180:183], v[4:7]\n" \
"v_pk_add_f16 v178, v178, v182\n" \
"v_pk_add_f16 v179, v179, v183\n" \
"buffer_store_dwordx4 v[220:223], v235, s[36:39], s66 offen sc1\n" \
"v_mfma_f32_16x16x32_f16 v[16:19], v[156:159], v[180:183], v[16:19]\n" \
"ds_read_b128 v[172:175], v240 offset:47616\n" \
"s_add_i32 m0, s44, 0x5000\n" \
"s_add_i32 s80, s46, 0x2400000\n" \
"v_mfma_f32_16x16x32_f16 v[8:11], v[120:123], v[176:179], v[8:11]\n" \
"buffer_load_dwordx4 v232, s[36:39], s80 offen sc1 lds\n" \
"s_waitcnt lgkmcnt(2)\n" \
"v_mfma_f32_16x16x16_bf16 v[184:187], v[184:185], v[192:193], 0\n" \
"v_mfma_f32_16x16x32_f16 v[20:23], v[124:127], v[176:179], v[20:23]\n" \
"ds_read_b64_tr_b16 v[128:129], v241 offset:32768\n" \
"ds_read_b64_tr_b16 v[130:131], v241 offset:40960\n" \
"s_waitcnt lgkmcnt(3)\n" \
"v_mfma_f32_16x16x16_bf16 v[188:191], v[188:189], v[192:193], 0\n" \
"v_mfma_f32_16x16x32_f16 v[24:27], v[136:139], v[160:163], v[24:27]\n" \
"ds_read_b128 v[176:179], v240 offset:49152\n" \
"v_mfma_f32_16x16x32_f16 v[36:39], v[140:143], v[160:163], v[36:39]\n" \
"v_pk_add_f16 v160, v160, v164\n" \
"v_pk_add_f16 v161, v161, v165\n" \
"ds_read_b64_tr_b16 v[132:133], v242 offset:32768\n" \
"v_mfma_f32_16x16x32_f16 v[28:31], v[152:155], v[164:167], v[28:31]\n" \
"v_pk_add_f16 v162, v162, v166\n" \
"v_pk_add_f16 v163, v163, v167\n" \
"v_cvt_pk_f16_f32 v184, v184, v185\n" \
"v_mfma_f32_16x16x32_f16 v[40:43], v[156:159], v[164:167], v[40:43]\n" \
"v_cvt_pk_f16_f32 v185, v186, v187\n" \
"v_cvt_pk_f16_f32 v186, v188, v189\n" \
"ds_read_b128 v[180:183], v240 offset:49664\n" \
"v_mfma_f32_16x16x32_f16 v[32:35], v[120:123], v[160:163], v[32:35]\n" \
"v_cvt_pk_f16_f32 v187, v190, v191\n" \
"v_add_u32 v184, s91, v184\n" \
"buffer_load_dwordx4 v232, s[36:39], s80 offen offset:1024 sc1 lds\n" \
"v_mfma_f32_16x16x32_f16 v[44:47], v[124:127], v[160:163], v[44:47]\n" \
"v_add_u32 v185, s91, v185\n" \
"v_add_u32 v186, s91, v186\n" \
"ds_read_b64_tr_b16 v[134:135], v242 offset:40960\n" \
"v_mfma_f32_16x16x32_f16 v[48:51], v[136:139], v[168:171], v[48:51]\n" \
"v_add_u32 v187, s91, v187\n" \
"v_and_b32 v184, s92, v184\n" \
"ds_read_b128 v[160:163], v240 offset:40960\n" \
"v_mfma_f32_16x16x32_f16 v[60:63], v[140:143], v[168:171], v[60:63]\n" \
"s_waitcnt lgkmcnt(4)\n" \
"v_pk_add_f16 v168, v168, v172\n" \
"v_pk_add_f16 v169, v169, v173\n" \
"v_and_b32 v185, s92, v185\n" \
"v_mfma_f32_16x16x32_f16 v[52:55], v[152:155], v[172:175], v[52:55]\n" \
"v_pk_add_f16 v170, v170, v174\n" \
"v_pk_add_f16 v171, v171, v175\n" \
"v_and_b32 v186, s92, v186\n" \
"v_and_b32 v187, s92, v187\n" \
"v_mfma_f32_16x16x32_f16 v[64:67], v[156:159], v[172:175], v[64:67]\n" \
"ds_read_b128 v[164:167], v240 offset:41472\n" \
"buffer_store_dwordx4 v[184:187], v234, s[36:39], s82 offen offset:1024 sc1\n" \
"ds_read_b64_tr_b16 v[144:145], v241 offset:33280\n" \
"v_mfma_f32_16x16x32_f16 v[56:59], v[120:123], v[168:171], v[56:59]\n" \
"buffer_load_dwordx4 v232, s[36:39], s80 offen offset:2048 sc1 lds\n" \
"v_mfma_f32_16x16x32_f16 v[68:71], v[124:127], v[168:171], v[68:71]\n" \
"s_setprio 0\n" \
"ds_read_b64_tr_b16 v[146:147], v241 offset:41472\n" \
"ds_read_b64_tr_b16 v[148:149], v242 offset:33280\n" \
"v_mfma_f32_16x16x32_f16 v[72:75], v[136:139], v[176:179], v[72:75]\n" \
"ds_read_b128 v[168:171], v240 offset:43008\n" \
"ds_read_b64_tr_b16 v[150:151], v242 offset:41472\n" \
"v_mfma_f32_16x16x32_f16 v[84:87], v[140:143], v[176:179], v[84:87]\n" \
"s_waitcnt lgkmcnt(6)\n" \
"v_pk_add_f16 v176, v176, v180\n" \
"v_pk_add_f16 v177, v177, v181\n" \
"v_mfma_f32_16x16x32_f16 v[76:79], v[152:155], v[180:183], v[76:79]\n" \
"v_pk_add_f16 v178, v178, v182\n" \
"v_pk_add_f16 v179, v179, v183\n" \
"v_mfma_f32_16x16x32_f16 v[88:91], v[156:159], v[180:183], v[88:91]\n" \
"ds_read_b128 v[172:175], v240 offset:43520\n" \
"v_mfma_f32_16x16x32_f16 v[80:83], v[120:123], v[176:179], v[80:83]\n" \
"buffer_load_dwordx4 v232, s[36:39], s80 offen offset:3072 sc1 lds\n" \
"v_mfma_f32_16x16x32_f16 v[92:95], v[124:127], v[176:179], v[92:95]\n" \
"v_mfma_f32_16x16x32_f16 v[96:99], v[136:139], v[160:163], v[96:99]\n" \
"s_waitcnt lgkmcnt(5)\n" \
"v_pk_add_f16 v136, v128, v144\n" \
"v_pk_add_f16 v137, v129, v145\n" \
"ds_read_b128 v[176:179], v240 offset:45056\n" \
"v_mfma_f32_16x16x32_f16 v[108:111], v[140:143], v[160:163], v[108:111]\n" \
"v_pk_add_f16 v160, v160, v164\n" \
"v_pk_add_f16 v161, v161, v165\n" \
"ds_read_b128 v[180:183], v240 offset:45568\n" \
"v_mfma_f32_16x16x32_f16 v[100:103], v[152:155], v[164:167], v[100:103]\n" \
"v_pk_add_f16 v162, v162, v166\n" \
"v_pk_add_f16 v163, v163, v167\n" \
"s_add_i32 m0, s45, 0x5000\n" \
"s_add_i32 s81, s47, 0x2400000\n" \
"v_mfma_f32_16x16x32_f16 v[112:115], v[156:159], v[164:167], v[112:115]\n" \
"s_waitcnt lgkmcnt(3)\n" \
"v_pk_add_f16 v138, v130, v146\n" \
"v_pk_add_f16 v139, v131, v147\n" \
"buffer_load_dwordx4 v232, s[36:39], s81 offen sc1 lds\n" \
"v_mfma_f32_16x16x32_f16 v[104:107], v[120:123], v[160:163], v[104:107]\n" \
"v_pk_add_f16 v140, v132, v148\n" \
"v_pk_add_f16 v141, v133, v149\n" \
"v_mfma_f32_16x16x32_f16 v[116:119], v[124:127], v[160:163], v[116:119]\n" \
"s_setprio 1\n" \
"v_pk_add_f16 v142, v134, v150\n" \
"v_pk_add_f16 v143, v135, v151\n" \
"s_waitcnt vmcnt(7) lgkmcnt(0)\n" \
"s_barrier\n" \
"v_mfma_f32_16x16x32_f16 v[0:3], v[128:131], v[168:171], v[0:3]\n" \
"ds_read_b128 v[160:163], v240 offset:6144\n" \
"ds_read_b64_tr_b16 v[184:185], v236 offset:16384\n" \
"ds_read_b64_tr_b16 v[188:189], v237 offset:16384\n" \
"v_mfma_f32_16x16x32_f16 v[12:15], v[132:135], v[168:171], v[12:15]\n" \
"v_pk_add_f16 v168, v168, v172\n" \
"v_pk_add_f16 v169, v169, v173\n" \
"v_mfma_f32_16x16x32_f16 v[4:7], v[144:147], v[172:175], v[4:7]\n" \
"v_pk_add_f16 v170, v170, v174\n" \
"v_pk_add_f16 v171, v171, v175\n" \
"v_mfma_f32_16x16x32_f16 v[16:19], v[148:151], v[172:175], v[16:19]\n" \
"ds_read_b128 v[164:167], v240 offset:6656\n" \
"s_add_i32 m0, s44, 0xa000\n" \
"s_add_i32 s80, s46, 0x2800000\n" \
"v_mfma_f32_16x16x32_f16 v[8:11], v[136:139], v[168:171], v[8:11]\n" \
"buffer_load_dwordx4 v232, s[36:39], s80 offen sc1 lds\n" \
"s_waitcnt lgkmcnt(2)\n" \
"v_mfma_f32_16x16x16_bf16 v[184:187], v[184:185], v[192:193], 0\n" \
"v_mfma_f32_16x16x32_f16 v[20:23], v[140:143], v[168:171], v[20:23]\n" \
"ds_read_b64_tr_b16 v[120:121], v241 offset:0\n" \
"ds_read_b64_tr_b16 v[122:123], v241 offset:8192\n" \
"s_waitcnt lgkmcnt(3)\n" \
"v_mfma_f32_16x16x16_bf16 v[188:191], v[188:189], v[192:193], 0\n" \
"v_mfma_f32_16x16x32_f16 v[24:27], v[128:131], v[176:179], v[24:27]\n" \
"ds_read_b128 v[168:171], v240 offset:8192\n" \
"v_mfma_f32_16x16x32_f16 v[36:39], v[132:135], v[176:179], v[36:39]\n" \
"v_pk_add_f16 v176, v176, v180\n" \
"v_pk_add_f16 v177, v177, v181\n" \
"ds_read_b64_tr_b16 v[124:125], v242 offset:0\n" \
"v_mfma_f32_16x16x32_f16 v[28:31], v[144:147], v[180:183], v[28:31]\n" \
"v_pk_add_f16 v178, v178, v182\n" \
"v_pk_add_f16 v179, v179, v183\n" \
"v_cvt_pk_f16_f32 v184, v184, v185\n" \
"v_mfma_f32_16x16x32_f16 v[40:43], v[148:151], v[180:183], v[40:43]\n" \
"v_cvt_pk_f16_f32 v185, v186, v187\n" \
"v_cvt_pk_f16_f32 v186, v188, v189\n" \
"ds_read_b128 v[172:175], v240 offset:8704\n" \
"v_mfma_f32_16x16x32_f16 v[32:35], v[136:139], v[176:179], v[32:35]\n" \
"v_cvt_pk_f16_f32 v187, v190, v191\n" \
"v_add_u32 v184, s91, v184\n" \
"buffer_load_dwordx4 v232, s[36:39], s80 offen offset:1024 sc1 lds\n" \
"v_mfma_f32_16x16x32_f16 v[44:47], v[140:143], v[176:179], v[44:47]\n" \
"v_add_u32 v185, s91, v185\n" \
"v_add_u32 v186, s91, v186\n" \
"ds_read_b64_tr_b16 v[126:127], v242 offset:8192\n" \
"v_mfma_f32_16x16x32_f16 v[48:51], v[128:131], v[160:163], v[48:51]\n" \
"v_add_u32 v187, s91, v187\n" \
"v_and_b32 v184, s92, v184\n" \
"ds_read_b128 v[176:179], v240 offset:0\n" \
"v_mfma_f32_16x16x32_f16 v[60:63], v[132:135], v[160:163], v[60:63]\n" \
"s_waitcnt lgkmcnt(4)\n" \
"v_pk_add_f16 v160, v160, v164\n" \
"v_pk_add_f16 v161, v161, v165\n" \
"v_and_b32 v185, s92, v185\n" \
"s_add_i32 s82, s49, 0x4800000\n" \
"v_mfma_f32_16x16x32_f16 v[52:55], v[144:147], v[164:167], v[52:55]\n" \
"v_pk_add_f16 v162, v162, v166\n" \
"v_pk_add_f16 v163, v163, v167\n" \
"v_and_b32 v186, s92, v186\n" \
"v_and_b32 v187, s92, v187\n" \
"v_mfma_f32_16x16x32_f16 v[64:67], v[148:151], v[164:167], v[64:67]\n" \
"ds_read_b128 v[180:183], v240 offset:512\n" \
"buffer_store_dwordx4 v[184:187], v234, s[36:39], s82 offen sc1\n" \
"ds_read_b64_tr_b16 v[152:153], v241 offset:512\n" \
"v_mfma_f32_16x16x32_f16 v[56:59], v[136:139], v[160:163], v[56:59]\n" \
"buffer_load_dwordx4 v232, s[36:39], s80 offen offset:2048 sc1 lds\n" \
"v_mfma_f32_16x16x32_f16 v[68:71], v[140:143], v[160:163], v[68:71]\n" \
"s_setprio 0\n" \
"ds_read_b64_tr_b16 v[154:155], v241 offset:8704\n" \
"ds_read_b64_tr_b16 v[156:157], v242 offset:512\n" \
"v_mfma_f32_16x16x32_f16 v[72:75], v[128:131], v[168:171], v[72:75]\n" \
"ds_read_b128 v[160:163], v240 offset:2048\n" \
"ds_read_b64_tr_b16 v[158:159], v242 offset:8704\n" \
"v_mfma_f32_16x16x32_f16 v[84:87], v[132:135], v[168:171], v[84:87]\n" \
"s_waitcnt lgkmcnt(6)\n" \
"v_pk_add_f16 v168, v168, v172\n" \
"v_pk_add_f16 v169, v169, v173\n" \
"v_mfma_f32_16x16x32_f16 v[76:79], v[144:147], v[172:175], v[76:79]\n" \
"v_pk_add_f16 v170, v170, v174\n" \
"v_pk_add_f16 v171, v171, v175\n" \
"v_mfma_f32_16x16x32_f16 v[88:91], v[148:151], v[172:175], v[88:91]\n" \
"ds_read_b128 v[164:167], v240 offset:2560\n" \
"v_mfma_f32_16x16x32_f16 v[80:83], v[136:139], v[168:171], v[80:83]\n" \
"buffer_load_dwordx4 v232, s[36:39], s80 offen offset:3072 sc1 lds\n" \
"v_mfma_f32_16x16x32_f16 v[92:95], v[140:143], v[168:171], v[92:95]\n" \
"v_mfma_f32_16x16x32_f16 v[96:99], v[128:131], v[176:179], v[96:99]\n" \
"s_waitcnt lgkmcnt(5)\n" \
"v_pk_add_f16 v128, v120, v152\n" \
"v_pk_add_f16 v129, v121, v153\n" \
"ds_read_b128 v[168:171], v240 offset:4096\n" \
"v_mfma_f32_16x16x32_f16 v[108:111], v[132:135], v[176:179], v[108:111]\n" \
"v_pk_add_f16 v176, v176, v180\n" \
"v_pk_add_f16 v177, v177, v181\n" \
"ds_read_b128 v[172:175], v240 offset:4608\n" \
"v_mfma_f32_16x16x32_f16 v[100:103], v[144:147], v[180:183], v[100:103]\n" \
"v_pk_add_f16 v178, v178, v182\n" \
"v_pk_add_f16 v179, v179, v183\n" \
"s_add_i32 m0, s45, 0xa000\n" \
"s_add_i32 s81, s47, 0x2800000\n" \
"v_mfma_f32_16x16x32_f16 v[112:115], v[148:151], v[180:183], v[112:115]\n" \
"s_waitcnt lgkmcnt(3)\n" \
"v_pk_add_f16 v130, v122, v154\n" \
"v_pk_add_f16 v131, v123, v155\n" \
"buffer_load_dwordx4 v232, s[36:39], s81 offen sc1 lds\n" \
"v_mfma_f32_16x16x32_f16 v[104:107], v[136:139], v[176:179], v[104:107]\n" \
"v_pk_add_f16 v132, v124, v156\n" \
"v_pk_add_f16 v133, v125, v157\n" \
"v_mfma_f32_16x16x32_f16 v[116:119], v[140:143], v[176:179], v[116:119]\n" \
"s_setprio 1\n" \
"v_pk_add_f16 v134, v126, v158\n" \
"v_pk_add_f16 v135, v127, v159\n" \
"s_waitcnt vmcnt(6) lgkmcnt(0)\n" \
"s_barrier\n" \
"v_mfma_f32_16x16x32_f16 v[0:3], v[120:123], v[160:163], v[0:3]\n" \
"ds_read_b128 v[176:179], v240 offset:26624\n" \
"ds_read_b64_tr_b16 v[184:185], v238 offset:16384\n" \
"ds_read_b64_tr_b16 v[188:189], v239 offset:16384\n" \
"v_mfma_f32_16x16x32_f16 v[12:15], v[124:127], v[160:163], v[12:15]\n" \
"v_pk_add_f16 v160, v160, v164\n" \
"v_pk_add_f16 v161, v161, v165\n" \
"v_mfma_f32_16x16x32_f16 v[4:7], v[152:155], v[164:167], v[4:7]\n" \
"v_pk_add_f16 v162, v162, v166\n" \
"v_pk_add_f16 v163, v163, v167\n" \
"v_mfma_f32_16x16x32_f16 v[16:19], v[156:159], v[164:167], v[16:19]\n" \
"ds_read_b128 v[180:183], v240 offset:27136\n" \
"s_add_i32 m0, s44, 0x0\n" \
"s_add_i32 s80, s46, 0x2c00000\n" \
"v_mfma_f32_16x16x32_f16 v[8:11], v[128:131], v[160:163], v[8:11]\n" \
"buffer_load_dwordx4 v232, s[36:39], s80 offen sc1 lds\n" \
"s_waitcnt lgkmcnt(2)\n" \
"v_mfma_f32_16x16x16_bf16 v[184:187], v[184:185], v[192:193], 0\n" \
"v_mfma_f32_16x16x32_f16 v[20:23], v[132:135], v[160:163], v[20:23]\n" \
"ds_read_b64_tr_b16 v[136:137], v241 offset:16384\n" \
"ds_read_b64_tr_b16 v[138:139], v241 offset:24576\n" \
"s_waitcnt lgkmcnt(3)\n" \
"v_mfma_f32_16x16x16_bf16 v[188:191], v[188:189], v[192:193], 0\n" \
"v_mfma_f32_16x16x32_f16 v[24:27], v[120:123], v[168:171], v[24:27]\n" \
"ds_read_b128 v[160:163], v240 offset:28672\n" \
"v_mfma_f32_16x16x32_f16 v[36:39], v[124:127], v[168:171], v[36:39]\n" \
"v_pk_add_f16 v168, v168, v172\n" \
"v_pk_add_f16 v169, v169, v173\n" \
"ds_read_b64_tr_b16 v[140:141], v242 offset:16384\n" \
"v_mfma_f32_16x16x32_f16 v[28:31], v[152:155], v[172:175], v[28:31]\n" \
"v_pk_add_f16 v170, v170, v174\n" \
"v_pk_add_f16 v171, v171, v175\n" \
"v_cvt_pk_f16_f32 v184, v184, v185\n" \
"v_mfma_f32_16x16x32_f16 v[40:43], v[156:159], v[172:175], v[40:43]\n" \
"v_cvt_pk_f16_f32 v185, v186, v187\n" \
"v_cvt_pk_f16_f32 v186, v188, v189\n" \
"ds_read_b128 v[164:167], v240 offset:29184\n" \
"v_mfma_f32_16x16x32_f16 v[32:35], v[128:131], v[168:171], v[32:35]\n" \
"v_cvt_pk_f16_f32 v187, v190, v191\n" \
"v_add_u32 v184, s91, v184\n" \
"buffer_load_dwordx4 v232, s[36:39], s80 offen offset:1024 sc1 lds\n" \
"v_mfma_f32_16x16x32_f16 v[44:47], v[132:135], v[168:171], v[44:47]\n" \
"v_add_u32 v185, s91, v185\n" \
"v_add_u32 v186, s91, v186\n" \
"ds_read_b64_tr_b16 v[142:143], v242 offset:24576\n" \
"v_mfma_f32_16x16x32_f16 v[48:51], v[120:123], v[176:179], v[48:51]\n" \
"v_add_u32 v187, s91, v187\n" \
"v_and_b32 v184, s92, v184\n" \
"ds_read_b128 v[168:171], v240 offset:20480\n" \
"v_mfma_f32_16x16x32_f16 v[60:63], v[124:127], v[176:179], v[60:63]\n" \
"s_waitcnt lgkmcnt(4)\n" \
"v_pk_add_f16 v176, v176, v180\n" \
"v_pk_add_f16 v177, v177, v181\n" \
"v_and_b32 v185, s92, v185\n" \
"v_mfma_f32_16x16x32_f16 v[52:55], v[152:155], v[180:183], v[52:55]\n" \
"v_pk_add_f16 v178, v178, v182\n" \
"v_pk_add_f16 v179, v179, v183\n" \
"v_and_b32 v186, s92, v186\n" \
"v_and_b32 v187, s92, v187\n" \
"v_mfma_f32_16x16x32_f16 v[64:67], v[156:159], v[180:183], v[64:67]\n" \
"ds_read_b128 v[172:175], v240 offset:20992\n" \
"buffer_store_dwordx4 v[184:187], v234, s[36:39], s82 offen offset:1024 sc1\n" \
"ds_read_b64_tr_b16 v[144:145], v241 offset:16896\n" \
"v_mfma_f32_16x16x32_f16 v[56:59], v[128:131], v[176:179], v[56:59]\n" \
"buffer_load_dwordx4 v232, s[36:39], s80 offen offset:2048 sc1 lds\n" \
"v_mfma_f32_16x16x32_f16 v[68:71], v[132:135], v[176:179], v[68:71]\n" \
"s_setprio 0\n" \
"ds_read_b64_tr_b16 v[146:147], v241 offset:25088\n" \
"ds_read_b64_tr_b16 v[148:149], v242 offset:16896\n" \
"v_mfma_f32_16x16x32_f16 v[72:75], v[120:123], v[160:163], v[72:75]\n" \
"ds_read_b128 v[176:179], v240 offset:22528\n" \
"ds_read_b64_tr_b16 v[150:151], v242 offset:25088\n" \
"v_mfma_f32_16x16x32_f16 v[84:87], v[124:127], v[160:163], v[84:87]\n" \
"s_waitcnt lgkmcnt(6)\n" \
"v_pk_add_f16 v160, v160, v164\n" \
"v_pk_add_f16 v161, v161, v165\n" \
"v_mfma_f32_16x16x32_f16 v[76:79], v[152:155], v[164:167], v[76:79]\n" \
"v_pk_add_f16 v162, v162, v166\n" \
"v_pk_add_f16 v163, v163, v167\n" \
"v_mfma_f32_16x16x32_f16 v[88:91], v[156:159], v[164:167], v[88:91]\n" \
"ds_read_b128 v[180:183], v240 offset:23040\n" \
"v_mfma_f32_16x16x32_f16 v[80:83], v[128:131], v[160:163], v[80:83]\n" \
"buffer_load_dwordx4 v232, s[36:39], s80 offen offset:3072 sc1 lds\n" \
"v_mfma_f32_16x16x32_f16 v[92:95], v[132:135], v[160:163], v[92:95]\n" \
"v_mfma_f32_16x16x32_f16 v[96:99], v[120:123], v[168:171], v[96:99]\n" \
"s_waitcnt lgkmcnt(5)\n" \
"v_pk_add_f16 v120, v136, v144\n" \
"v_pk_add_f16 v121, v137, v145\n" \
"ds_read_b128 v[160:163], v240 offset:24576\n" \
"v_mfma_f32_16x16x32_f16 v[108:111], v[124:127], v[168:171], v[108:111]\n" \
"v_pk_add_f16 v168, v168, v172\n" \
"v_pk_add_f16 v169, v169, v173\n" \
"ds_read_b128 v[164:167], v240 offset:25088\n" \
"v_mfma_f32_16x16x32_f16 v[100:103], v[152:155], v[172:175], v[100:103]\n" \
"v_pk_add_f16 v170, v170, v174\n" \
"v_pk_add_f16 v171, v171, v175\n" \
"s_add_i32 m0, s45, 0x0\n" \
"s_add_i32 s81, s47, 0x2c00000\n" \
"v_mfma_f32_16x16x32_f16 v[112:115], v[156:159], v[172:175], v[112:115]\n" \
"s_waitcnt lgkmcnt(3)\n" \
"v_pk_add_f16 v122, v138, v146\n" \
"v_pk_add_f16 v123, v139, v147\n" \
"buffer_load_dwordx4 v232, s[36:39], s81 offen sc1 lds\n" \
"v_mfma_f32_16x16x32_f16 v[104:107], v[128:131], v[168:171], v[104:107]\n" \
"v_pk_add_f16 v124, v140, v148\n" \
"v_pk_add_f16 v125, v141, v149\n" \
"v_mfma_f32_16x16x32_f16 v[116:119], v[132:135], v[168:171], v[116:119]\n" \
"s_setprio 1\n" \
"v_pk_add_f16 v126, v142, v150\n" \
"v_pk_add_f16 v127, v143, v151\n" \
"s_waitcnt vmcnt(6) lgkmcnt(0)\n" \
"s_barrier\n" \
"v_mfma_f32_16x16x32_f16 v[0:3], v[136:139], v[176:179], v[0:3]\n" \
"ds_read_b128 v[168:171], v240 offset:47104\n" \
"ds_read_b64_tr_b16 v[184:185], v236 offset:20480\n" \
"ds_read_b64_tr_b16 v[188:189], v237 offset:20480\n" \
"v_mfma_f32_16x16x32_f16 v[12:15], v[140:143], v[176:179], v[12:15]\n" \
"v_pk_add_f16 v176, v176, v180\n" \
"v_pk_add_f16 v177, v177, v181\n" \
"v_mfma_f32_16x16x32_f16 v[4:7], v[144:147], v[180:183], v[4:7]\n" \
"v_pk_add_f16 v178, v178, v182\n" \
"v_pk_add_f16 v179, v179, v183\n" \
"buffer_store_dwordx4 v[224:227], v235, s[36:39], s67 offen sc1\n" \
"v_mfma_f32_16x16x32_f16 v[16:19], v[148:151], v[180:183], v[16:19]\n" \
"ds_read_b128 v[172:175], v240 offset:47616\n" \
"s_add_i32 m0, s44, 0x5000\n" \
"s_add_i32 s80, s46, 0x3000000\n" \
"v_mfma_f32_16x16x32_f16 v[8:11], v[120:123], v[176:179], v[8:11]\n" \
"buffer_load_dwordx4 v232, s[36:39], s80 offen sc1 lds\n" \
"s_waitcnt lgkmcnt(2)\n" \
"v_mfma_f32_16x16x16_bf16 v[184:187], v[184:185], v[192:193], 0\n" \
"v_mfma_f32_16x16x32_f16 v[20:23], v[124:127], v[176:179], v[20:23]\n" \
"ds_read_b64_tr_b16 v[128:129], v241 offset:32768\n" \
"ds_read_b64_tr_b16 v[130:131], v241 offset:40960\n" \
"s_waitcnt lgkmcnt(3)\n" \
"v_mfma_f32_16x16x16_bf16 v[188:191], v[188:189], v[192:193], 0\n" \
"v_mfma_f32_16x16x32_f16 v[24:27], v[136:139], v[160:163], v[24:27]\n" \
"ds_read_b128 v[176:179], v240 offset:49152\n" \
"v_mfma_f32_16x16x32_f16 v[36:39], v[140:143], v[160:163], v[36:39]\n" \
"v_pk_add_f16 v160, v160, v164\n" \
"v_pk_add_f16 v161, v161, v165\n" \
"ds_read_b64_tr_b16 v[132:133], v242 offset:32768\n" \
"v_mfma_f32_16x16x32_f16 v[28:31], v[144:147], v[164:167], v[28:31]\n" \
"v_pk_add_f16 v162, v162, v166\n" \
"v_pk_add_f16 v163, v163, v167\n" \
"v_cvt_pk_f16_f32 v184, v184, v185\n" \
"v_mfma_f32_16x16x32_f16 v[40:43], v[148:151], v[164:167], v[40:43]\n" \
"v_cvt_pk_f16_f32 v185, v186, v187\n" \
"v_cvt_pk_f16_f32 v186, v188, v189\n" \
"ds_read_b128 v[180:183], v240 offset:49664\n" \
"v_mfma_f32_16x16x32_f16 v[32:35], v[120:123], v[160:163], v[32:35]\n" \
"v_cvt_pk_f16_f32 v187, v190, v191\n" \
"v_add_u32 v184, s91, v184\n" \
"buffer_load_dwordx4 v232, s[36:39], s80 offen offset:1024 sc1 lds\n" \
"v_mfma_f32_16x16x32_f16 v[44:47], v[124:127], v[160:163], v[44:47]\n" \
"v_add_u32 v185, s91, v185\n" \
"v_add_u32 v186, s91, v186\n" \
"ds_read_b64_tr_b16 v[134:135], v242 offset:40960\n" \
"v_mfma_f32_16x16x32_f16 v[48:51], v[136:139], v[168:171], v[48:51]\n" \
"v_add_u32 v187, s91, v187\n" \
"v_and_b32 v184, s92, v184\n" \
"ds_read_b128 v[160:163], v240 offset:40960\n" \
"v_mfma_f32_16x16x32_f16 v[60:63], v[140:143], v[168:171], v[60:63]\n" \
"s_waitcnt lgkmcnt(4)\n" \
"v_pk_add_f16 v168, v168, v172\n" \
"v_pk_add_f16 v169, v169, v173\n" \
"v_and_b32 v185, s92, v185\n" \
"s_add_i32 s82, s50, 0x5000000\n" \
"v_mfma_f32_16x16x32_f16 v[52:55], v[144:147], v[172:175], v[52:55]\n" \
"v_pk_add_f16 v170, v170, v174\n" \
"v_pk_add_f16 v171, v171, v175\n" \
"v_and_b32 v186, s92, v186\n" \
"v_and_b32 v187, s92, v187\n" \
"v_mfma_f32_16x16x32_f16 v[64:67], v[148:151], v[172:175], v[64:67]\n" \
"ds_read_b128 v[164:167], v240 offset:41472\n" \
"buffer_store_dwordx4 v[184:187], v234, s[36:39], s82 offen sc1\n" \
"ds_read_b64_tr_b16 v[152:153], v241 offset:33280\n" \
"v_mfma_f32_16x16x32_f16 v[56:59], v[120:123], v[168:171], v[56:59]\n" \
"buffer_load_dwordx4 v232, s[36:39], s80 offen offset:2048 sc1 lds\n" \
"v_mfma_f32_16x16x32_f16 v[68:71], v[124:127], v[168:171], v[68:71]\n" \
"s_setprio 0\n" \
"ds_read_b64_tr_b16 v[154:155], v241 offset:41472\n" \
"ds_read_b64_tr_b16 v[156:157], v242 offset:33280\n" \
"v_mfma_f32_16x16x32_f16 v[72:75], v[136:139], v[176:179], v[72:75]\n" \
"ds_read_b128 v[168:171], v240 offset:43008\n" \
"ds_read_b64_tr_b16 v[158:159], v242 offset:41472\n" \
"v_mfma_f32_16x16x32_f16 v[84:87], v[140:143], v[176:179], v[84:87]\n" \
"s_waitcnt lgkmcnt(6)\n" \
"v_pk_add_f16 v176, v176, v180\n" \
"v_pk_add_f16 v177, v177, v181\n" \
"v_mfma_f32_16x16x32_f16 v[76:79], v[144:147], v[180:183], v[76:79]\n" \
"v_pk_add_f16 v178, v178, v182\n" \
"v_pk_add_f16 v179, v179, v183\n" \
"v_mfma_f32_16x16x32_f16 v[88:91], v[148:151], v[180:183], v[88:91]\n" \
"ds_read_b128 v[172:175], v240 offset:43520\n" \
"v_mfma_f32_16x16x32_f16 v[80:83], v[120:123], v[176:179], v[80:83]\n" \
"buffer_load_dwordx4 v232, s[36:39], s80 offen offset:3072 sc1 lds\n" \
"v_mfma_f32_16x16x32_f16 v[92:95], v[124:127], v[176:179], v[92:95]\n" \
"v_mfma_f32_16x16x32_f16 v[96:99], v[136:139], v[160:163], v[96:99]\n" \
"s_waitcnt lgkmcnt(5)\n" \
"v_pk_add_f16 v136, v128, v152\n" \
"v_pk_add_f16 v137, v129, v153\n" \
"ds_read_b128 v[176:179], v240 offset:45056\n" \
"v_mfma_f32_16x16x32_f16 v[108:111], v[140:143], v[160:163], v[108:111]\n" \
"v_pk_add_f16 v160, v160, v164\n" \
"v_pk_add_f16 v161, v161, v165\n" \
"ds_read_b128 v[180:183], v240 offset:45568\n" \
"v_mfma_f32_16x16x32_f16 v[100:103], v[144:147], v[164:167], v[100:103]\n" \
"v_pk_add_f16 v162, v162, v166\n" \
"v_pk_add_f16 v163, v163, v167\n" \
"s_add_i32 m0, s45, 0x5000\n" \
"s_add_i32 s81, s47, 0x3000000\n" \
"v_mfma_f32_16x16x32_f16 v[112:115], v[148:151], v[164:167], v[112:115]\n" \
"s_waitcnt lgkmcnt(3)\n" \
"v_pk_add_f16 v138, v130, v154\n" \
"v_pk_add_f16 v139, v131, v155\n" \
"buffer_load_dwordx4 v232, s[36:39], s81 offen sc1 lds\n" \
"v_mfma_f32_16x16x32_f16 v[104:107], v[120:123], v[160:163], v[104:107]\n" \
"v_pk_add_f16 v140, v132, v156\n" \
"v_pk_add_f16 v141, v133, v157\n" \
"v_mfma_f32_16x16x32_f16 v[116:119], v[124:127], v[160:163], v[116:119]\n" \
"s_setprio 1\n" \
"v_pk_add_f16 v142, v134, v158\n" \
"v_pk_add_f16 v143, v135, v159\n" \
"s_waitcnt vmcnt(7) lgkmcnt(0)\n" \
"s_barrier\n" \
"v_mfma_f32_16x16x32_f16 v[0:3], v[128:131], v[168:171], v[0:3]\n" \
"ds_read_b128 v[160:163], v240 offset:6144\n" \
"ds_read_b64_tr_b16 v[184:185], v238 offset:20480\n" \
"ds_read_b64_tr_b16 v[188:189], v239 offset:20480\n" \
"v_mfma_f32_16x16x32_f16 v[12:15], v[132:135], v[168:171], v[12:15]\n" \
"v_pk_add_f16 v168, v168, v172\n" \
"v_pk_add_f16 v169, v169, v173\n" \
"v_mfma_f32_16x16x32_f16 v[4:7], v[152:155], v[172:175], v[4:7]\n" \
"v_pk_add_f16 v170, v170, v174\n" \
"v_pk_add_f16 v171, v171, v175\n" \
"v_mfma_f32_16x16x32_f16 v[16:19], v[156:159], v[172:175], v[16:19]\n" \
"ds_read_b128 v[164:167], v240 offset:6656\n" \
"s_add_i32 m0, s44, 0xa000\n" \
"s_add_i32 s80, s46, 0x3400000\n" \
"v_mfma_f32_16x16x32_f16 v[8:11], v[136:139], v[168:171], v[8:11]\n" \
"buffer_load_dwordx4 v232, s[36:39], s80 offen sc1 lds\n" \
"s_waitcnt lgkmcnt(2)\n" \
"v_mfma_f32_16x16x16_bf16 v[184:187], v[184:185], v[192:193], 0\n" \
"v_mfma_f32_16x16x32_f16 v[20:23], v[140:143], v[168:171], v[20:23]\n" \
"ds_read_b64_tr_b16 v[120:121], v241 offset:0\n" \
"ds_read_b64_tr_b16 v[122:123], v241 offset:8192\n" \
"s_waitcnt lgkmcnt(3)\n" \
"v_mfma_f32_16x16x16_bf16 v[188:191], v[188:189], v[192:193], 0\n" \
"v_mfma_f32_16x16x32_f16 v[24:27], v[128:131], v[176:179], v[24:27]\n" \
"ds_read_b128 v[168:171], v240 offset:8192\n" \
"v_mfma_f32_16x16x32_f16 v[36:39], v[132:135], v[176:179], v[36:39]\n" \
"v_pk_add_f16 v176, v176, v180\n" \
"v_pk_add_f16 v177, v177, v181\n" \
"ds_read_b64_tr_b16 v[124:125], v242 offset:0\n" \
"v_mfma_f32_16x16x32_f16 v[28:31], v[152:155], v[180:183], v[28:31]\n" \
"v_pk_add_f16 v178, v178, v182\n" \
"v_pk_add_f16 v179, v179, v183\n" \
"v_cvt_pk_f16_f32 v184, v184, v185\n" \
"v_mfma_f32_16x16x32_f16 v[40:43], v[156:159], v[180:183], v[40:43]\n" \
"v_cvt_pk_f16_f32 v185, v186, v187\n" \
"v_cvt_pk_f16_f32 v186, v188, v189\n" \
"ds_read_b128 v[172:175], v240 offset:8704\n" \
"v_mfma_f32_16x16x32_f16 v[32:35], v[136:139], v[176:179], v[32:35]\n" \
"v_cvt_pk_f16_f32 v187, v190, v191\n" \
"v_add_u32 v184, s91, v184\n" \
"buffer_load_dwordx4 v232, s[36:39], s80 offen offset:1024 sc1 lds\n" \
"v_mfma_f32_16x16x32_f16 v[44:47], v[140:143], v[176:179], v[44:47]\n" \
"v_add_u32 v185, s91, v185\n" \
"v_add_u32 v186, s91, v186\n" \
"ds_read_b64_tr_b16 v[126:127], v242 offset:8192\n" \
"v_mfma_f32_16x16x32_f16 v[48:51], v[128:131], v[160:163], v[48:51]\n" \
"v_add_u32 v187, s91, v187\n" \
"v_and_b32 v184, s92, v184\n" \
"ds_read_b128 v[176:179], v240 offset:0\n" \
"v_mfma_f32_16x16x32_f16 v[60:63], v[132:135], v[160:163], v[60:63]\n" \
"s_waitcnt lgkmcnt(4)\n" \
"v_pk_add_f16 v160, v160, v164\n" \
"v_pk_add_f16 v161, v161, v165\n" \
"v_and_b32 v185, s92, v185\n" \
"v_mfma_f32_16x16x32_f16 v[52:55], v[152:155], v[164:167], v[52:55]\n" \
"v_pk_add_f16 v162, v162, v166\n" \
"v_pk_add_f16 v163, v163, v167\n" \
"v_and_b32 v186, s92, v186\n" \
"v_and_b32 v187, s92, v187\n" \
"v_mfma_f32_16x16x32_f16 v[64:67], v[156:159], v[164:167], v[64:67]\n" \
"ds_read_b128 v[180:183], v240 offset:512\n" \
"buffer_store_dwordx4 v[184:187], v234, s[36:39], s82 offen offset:1024 sc1\n" \
"ds_read_b64_tr_b16 v[144:145], v241 offset:512\n" \
"v_mfma_f32_16x16x32_f16 v[56:59], v[136:139], v[160:163], v[56:59]\n" \
"buffer_load_dwordx4 v232, s[36:39], s80 offen offset:2048 sc1 lds\n" \
"v_mfma_f32_16x16x32_f16 v[68:71], v[140:143], v[160:163], v[68:71]\n" \
"s_setprio 0\n" \
"ds_read_b64_tr_b16 v[146:147], v241 offset:8704\n" \
"ds_read_b64_tr_b16 v[148:149], v242 offset:512\n" \
"v_mfma_f32_16x16x32_f16 v[72:75], v[128:131], v[168:171], v[72:75]\n" \
"ds_read_b128 v[160:163], v240 offset:2048\n" \
"ds_read_b64_tr_b16 v[150:151], v242 offset:8704\n" \
"v_mfma_f32_16x16x32_f16 v[84:87], v[132:135], v[168:171], v[84:87]\n" \
"s_waitcnt lgkmcnt(6)\n" \
"v_pk_add_f16 v168, v168, v172\n" \
"v_pk_add_f16 v169, v169, v173\n" \
"v_mfma_f32_16x16x32_f16 v[76:79], v[152:155], v[172:175], v[76:79]\n" \
"v_pk_add_f16 v170, v170, v174\n" \
"v_pk_add_f16 v171, v171, v175\n" \
"v_mfma_f32_16x16x32_f16 v[88:91], v[156:159], v[172:175], v[88:91]\n" \
"ds_read_b128 v[164:167], v240 offset:2560\n" \
"v_mfma_f32_16x16x32_f16 v[80:83], v[136:139], v[168:171], v[80:83]\n" \
"buffer_load_dwordx4 v232, s[36:39], s80 offen offset:3072 sc1 lds\n" \
"v_mfma_f32_16x16x32_f16 v[92:95], v[140:143], v[168:171], v[92:95]\n" \
"v_mfma_f32_16x16x32_f16 v[96:99], v[128:131], v[176:179], v[96:99]\n" \
"s_waitcnt lgkmcnt(5)\n" \
"v_pk_add_f16 v128, v120, v144\n" \
"v_pk_add_f16 v129, v121, v145\n" \
"ds_read_b128 v[168:171], v240 offset:4096\n" \
"v_mfma_f32_16x16x32_f16 v[108:111], v[132:135], v[176:179], v[108:111]\n" \
"v_pk_add_f16 v176, v176, v180\n" \
"v_pk_add_f16 v177, v177, v181\n" \
"ds_read_b128 v[172:175], v240 offset:4608\n" \
"v_mfma_f32_16x16x32_f16 v[100:103], v[152:155], v[180:183], v[100:103]\n" \
"v_pk_add_f16 v178, v178, v182\n" \
"v_pk_add_f16 v179, v179, v183\n" \
"s_add_i32 m0, s45, 0xa000\n" \
"s_add_i32 s81, s47, 0x3400000\n" \
"v_mfma_f32_16x16x32_f16 v[112:115], v[156:159], v[180:183], v[112:115]\n" \
"s_waitcnt lgkmcnt(3)\n" \
"v_pk_add_f16 v130, v122, v146\n" \
"v_pk_add_f16 v131, v123, v147\n" \
"buffer_load_dwordx4 v232, s[36:39], s81 offen sc1 lds\n" \
"v_mfma_f32_16x16x32_f16 v[104:107], v[136:139], v[176:179], v[104:107]\n" \
"v_pk_add_f16 v132, v124, v148\n" \
"v_pk_add_f16 v133, v125, v149\n" \
"v_mfma_f32_16x16x32_f16 v[116:119], v[140:143], v[176:179], v[116:119]\n" \
"s_setprio 1\n" \
"v_pk_add_f16 v134, v126, v150\n" \
"v_pk_add_f16 v135, v127, v151\n" \
"s_waitcnt vmcnt(6) lgkmcnt(0)\n" \
"s_barrier\n" \
"v_mfma_f32_16x16x32_f16 v[0:3], v[120:123], v[160:163], v[0:3]\n" \
"ds_read_b128 v[176:179], v240 offset:26624\n" \
"ds_read_b64_tr_b16 v[184:185], v236 offset:24576\n" \
"ds_read_b64_tr_b16 v[188:189], v237 offset:24576\n" \
"v_mfma_f32_16x16x32_f16 v[12:15], v[124:127], v[160:163], v[12:15]\n" \
"v_pk_add_f16 v160, v160, v164\n" \
"v_pk_add_f16 v161, v161, v165\n" \
"s_mov_b32 s84, 0x800c000\n" \
"v_mfma_f32_16x16x32_f16 v[4:7], v[144:147], v[164:167], v[4:7]\n" \
"v_pk_add_f16 v162, v162, v166\n" \
"v_pk_add_f16 v163, v163, v167\n" \
"buffer_load_dword v253, v252, s[36:39], s84 offen sc1\n" \
"v_mfma_f32_16x16x32_f16 v[16:19], v[148:151], v[164:167], v[16:19]\n" \
"ds_read_b128 v[180:183], v240 offset:27136\n" \
"s_add_i32 m0, s44, 0x0\n" \
"s_add_i32 s80, s46, 0x3800000\n" \
"v_mfma_f32_16x16x32_f16 v[8:11], v[128:131], v[160:163], v[8:11]\n" \
"buffer_load_dwordx4 v232, s[36:39], s80 offen sc1 lds\n" \
"s_waitcnt lgkmcnt(2)\n" \
"v_mfma_f32_16x16x16_bf16 v[184:187], v[184:185], v[192:193], 0\n" \
"v_mfma_f32_16x16x32_f16 v[20:23], v[132:135], v[160:163], v[20:23]\n" \
"ds_read_b64_tr_b16 v[136:137], v241 offset:16384\n" \
"ds_read_b64_tr_b16 v[138:139], v241 offset:24576\n" \
"s_waitcnt lgkmcnt(3)\n" \
"v_mfma_f32_16x16x16_bf16 v[188:191], v[188:189], v[192:193], 0\n" \
"v_mfma_f32_16x16x32_f16 v[24:27], v[120:123], v[168:171], v[24:27]\n" \
"ds_read_b128 v[160:163], v240 offset:28672\n" \
"v_mfma_f32_16x16x32_f16 v[36:39], v[124:127], v[168:171], v[36:39]\n" \
"v_pk_add_f16 v168, v168, v172\n" \
"v_pk_add_f16 v169, v169, v173\n" \
"ds_read_b64_tr_b16 v[140:141], v242 offset:16384\n" \
"v_mfma_f32_16x16x32_f16 v[28:31], v[144:147], v[172:175], v[28:31]\n" \
"v_pk_add_f16 v170, v170, v174\n" \
"v_pk_add_f16 v171, v171, v175\n" \
"v_cvt_pk_f16_f32 v184, v184, v185\n" \
"v_mfma_f32_16x16x32_f16 v[40:43], v[148:151], v[172:175], v[40:43]\n" \
"v_cvt_pk_f16_f32 v185, v186, v187\n" \
"v_cvt_pk_f16_f32 v186, v188, v189\n" \
"ds_read_b128 v[164:167], v240 offset:29184\n" \
"v_mfma_f32_16x16x32_f16 v[32:35], v[128:131], v[168:171], v[32:35]\n" \
"v_cvt_pk_f16_f32 v187, v190, v191\n" \
"v_add_u32 v184, s91, v184\n" \
"buffer_load_dwordx4 v232, s[36:39], s80 offen offset:1024 sc1 lds\n" \
"v_mfma_f32_16x16x32_f16 v[44:47], v[132:135], v[168:171], v[44:47]\n" \
"v_add_u32 v185, s91, v185\n" \
"v_add_u32 v186, s91, v186\n" \
"ds_read_b64_tr_b16 v[142:143], v242 offset:24576\n" \
"v_mfma_f32_16x16x32_f16 v[48:51], v[120:123], v[176:179], v[48:51]\n" \
"v_add_u32 v187, s91, v187\n" \
"v_and_b32 v184, s92, v184\n" \
"ds_read_b128 v[168:171], v240 offset:20480\n" \
"v_mfma_f32_16x16x32_f16 v[60:63], v[124:127], v[176:179], v[60:63]\n" \
"s_waitcnt lgkmcnt(4)\n" \
"v_pk_add_f16 v176, v176, v180\n" \
"v_pk_add_f16 v177, v177, v181\n" \
"v_and_b32 v185, s92, v185\n" \
"s_add_i32 s82, s51, 0x5800000\n" \
"v_mfma_f32_16x16x32_f16 v[52:55], v[144:147], v[180:183], v[52:55]\n" \
"v_pk_add_f16 v178, v178, v182\n" \
"v_pk_add_f16 v179, v179, v183\n" \
"v_and_b32 v186, s92, v186\n" \
"v_and_b32 v187, s92, v187\n" \
"v_mfma_f32_16x16x32_f16 v[64:67], v[148:151], v[180:183], v[64:67]\n" \
"ds_read_b128 v[172:175], v240 offset:20992\n" \
"buffer_store_dwordx4 v[184:187], v234, s[36:39], s82 offen sc1\n" \
"ds_read_b64_tr_b16 v[152:153], v241 offset:16896\n" \
"v_mfma_f32_16x16x32_f16 v[56:59], v[128:131], v[176:179], v[56:59]\n" \
"buffer_load_dwordx4 v232, s[36:39], s80 offen offset:2048 sc1 lds\n" \
"v_mfma_f32_16x16x32_f16 v[68:71], v[132:135], v[176:179], v[68:71]\n" \
"s_setprio 0\n" \
"ds_read_b64_tr_b16 v[154:155], v241 offset:25088\n" \
"ds_read_b64_tr_b16 v[156:157], v242 offset:16896\n" \
"v_mfma_f32_16x16x32_f16 v[72:75], v[120:123], v[160:163], v[72:75]\n" \
"ds_read_b128 v[176:179], v240 offset:22528\n" \
"ds_read_b64_tr_b16 v[158:159], v242 offset:25088\n" \
"v_mfma_f32_16x16x32_f16 v[84:87], v[124:127], v[160:163], v[84:87]\n" \
"s_waitcnt lgkmcnt(6)\n" \
"v_pk_add_f16 v160, v160, v164\n" \
"v_pk_add_f16 v161, v161, v165\n" \
"v_mfma_f32_16x16x32_f16 v[76:79], v[144:147], v[164:167], v[76:79]\n" \
"v_pk_add_f16 v162, v162, v166\n" \
"v_pk_add_f16 v163, v163, v167\n" \
"v_mfma_f32_16x16x32_f16 v[88:91], v[148:151], v[164:167], v[88:91]\n" \
"ds_read_b128 v[180:183], v240 offset:23040\n" \
"v_mfma_f32_16x16x32_f16 v[80:83], v[128:131], v[160:163], v[80:83]\n" \
"buffer_load_dwordx4 v232, s[36:39], s80 offen offset:3072 sc1 lds\n" \
"v_mfma_f32_16x16x32_f16 v[92:95], v[132:135], v[160:163], v[92:95]\n" \
"v_mfma_f32_16x16x32_f16 v[96:99], v[120:123], v[168:171], v[96:99]\n" \
"s_waitcnt lgkmcnt(5)\n" \
"v_pk_add_f16 v120, v136, v152\n" \
"v_pk_add_f16 v121, v137, v153\n" \
"ds_read_b128 v[160:163], v240 offset:24576\n" \
"v_mfma_f32_16x16x32_f16 v[108:111], v[124:127], v[168:171], v[108:111]\n" \
"v_pk_add_f16 v168, v168, v172\n" \
"v_pk_add_f16 v169, v169, v173\n" \
"ds_read_b128 v[164:167], v240 offset:25088\n" \
"v_mfma_f32_16x16x32_f16 v[100:103], v[144:147], v[172:175], v[100:103]\n" \
"v_pk_add_f16 v170, v170, v174\n" \
"v_pk_add_f16 v171, v171, v175\n" \
"s_waitcnt vmcnt(5)\n" \
"v_cmp_eq_u32 vcc, 0, v253\n" \
"s_cbranch_vccnz .Ls16_%=\n" \
".Lr16_%=:\n" \
"s_add_i32 m0, s45, 0x0\n" \
"s_add_i32 s81, s47, 0x3800000\n" \
"v_mfma_f32_16x16x32_f16 v[112:115], v[148:151], v[172:175], v[112:115]\n" \
"s_waitcnt lgkmcnt(3)\n" \
"v_pk_add_f16 v122, v138, v154\n" \
"v_pk_add_f16 v123, v139, v155\n" \
"buffer_load_dwordx4 v232, s[36:39], s81 offen sc1 lds\n" \
"v_mfma_f32_16x16x32_f16 v[104:107], v[128:131], v[168:171], v[104:107]\n" \
"v_pk_add_f16 v124, v140, v156\n" \
"v_pk_add_f16 v125, v141, v157\n" \
"v_mfma_f32_16x16x32_f16 v[116:119], v[132:135], v[168:171], v[116:119]\n" \
"s_setprio 1\n" \
"v_pk_add_f16 v126, v142, v158\n" \
"v_pk_add_f16 v127, v143, v159\n" \
"s_waitcnt vmcnt(6) lgkmcnt(0)\n" \
"s_barrier\n" \
"v_mfma_f32_16x16x32_f16 v[0:3], v[136:139], v[176:179], v[0:3]\n" \
"ds_read_b128 v[168:171], v240 offset:47104\n" \
"ds_read_b64_tr_b16 v[184:185], v238 offset:24576\n" \
"ds_read_b64_tr_b16 v[188:189], v239 offset:24576\n" \
"v_mfma_f32_16x16x32_f16 v[12:15], v[140:143], v[176:179], v[12:15]\n" \
"v_pk_add_f16 v176, v176, v180\n" \
"v_pk_add_f16 v177, v177, v181\n" \
"v_mfma_f32_16x16x32_f16 v[4:7], v[152:155], v[180:183], v[4:7]\n" \
"v_pk_add_f16 v178, v178, v182\n" \
"v_pk_add_f16 v179, v179, v183\n" \
"buffer_store_dwordx4 v[228:231], v235, s[36:39], s68 offen sc1\n" \
"v_mfma_f32_16x16x32_f16 v[16:19], v[156:159], v[180:183], v[16:19]\n" \
"ds_read_b128 v[172:175], v240 offset:47616\n" \
"s_add_i32 m0, s44, 0x5000\n" \
"s_add_i32 s80, s46, 0x3c00000\n" \
"v_mfma_f32_16x16x32_f16 v[8:11], v[120:123], v[176:179], v[8:11]\n" \
"buffer_load_dwordx4 v232, s[36:39], s80 offen sc1 lds\n" \
"s_waitcnt lgkmcnt(2)\n" \
"v_mfma_f32_16x16x16_bf16 v[184:187], v[184:185], v[192:193], 0\n" \
"v_mfma_f32_16x16x32_f16 v[20:23], v[124:127], v[176:179], v[20:23]\n" \
"ds_read_b64_tr_b16 v[128:129], v241 offset:32768\n" \
"ds_read_b64_tr_b16 v[130:131], v241 offset:40960\n" \
"s_waitcnt lgkmcnt(3)\n" \
"v_mfma_f32_16x16x16_bf16 v[188:191], v[188:189], v[192:193], 0\n" \
"v_mfma_f32_16x16x32_f16 v[24:27], v[136:139], v[160:163], v[24:27]\n" \
"ds_read_b128 v[176:179], v240 offset:49152\n" \
"v_mfma_f32_16x16x32_f16 v[36:39], v[140:143], v[160:163], v[36:39]\n" \
"v_pk_add_f16 v160, v160, v164\n" \
"v_pk_add_f16 v161, v161, v165\n" \
"ds_read_b64_tr_b16 v[132:133], v242 offset:32768\n" \
"v_mfma_f32_16x16x32_f16 v[28:31], v[152:155], v[164:167], v[28:31]\n" \
"v_pk_add_f16 v162, v162, v166\n" \
"v_pk_add_f16 v163, v163, v167\n" \
"v_cvt_pk_f16_f32 v184, v184, v185\n" \
"v_mfma_f32_16x16x32_f16 v[40:43], v[156:159], v[164:167], v[40:43]\n" \
"v_cvt_pk_f16_f32 v185, v186, v187\n" \
"v_cvt_pk_f16_f32 v186, v188, v189\n" \
"ds_read_b128 v[180:183], v240 offset:49664\n" \
"v_mfma_f32_16x16x32_f16 v[32:35], v[120:123], v[160:163], v[32:35]\n" \
"v_cvt_pk_f16_f32 v187, v190, v191\n" \
"v_add_u32 v184, s91, v184\n" \
"buffer_load_dwordx4 v232, s[36:39], s80 offen offset:1024 sc1 lds\n" \
"v_mfma_f32_16x16x32_f16 v[44:47], v[124:127], v[160:163], v[44:47]\n" \
"v_add_u32 v185, s91, v185\n" \
"v_add_u32 v186, s91, v186\n" \
"ds_read_b64_tr_b16 v[134:135], v242 offset:40960\n" \
"v_mfma_f32_16x16x32_f16 v[48:51], v[136:139], v[168:171], v[48:51]\n" \
"v_add_u32 v187, s91, v187\n" \
"v_and_b32 v184, s92, v184\n" \
"ds_read_b128 v[160:163], v240 offset:40960\n" \
"v_mfma_f32_16x16x32_f16 v[60:63], v[140:143], v[168:171], v[60:63]\n" \
"s_waitcnt lgkmcnt(4)\n" \
"v_pk_add_f16 v168, v168, v172\n" \
"v_pk_add_f16 v169, v169, v173\n" \
"v_and_b32 v185, s92, v185\n" \
"v_mfma_f32_16x16x32_f16 v[52:55], v[152:155], v[172:175], v[52:55]\n" \
"v_pk_add_f16 v170, v170, v174\n" \
"v_pk_add_f16 v171, v171, v175\n" \
"v_and_b32 v186, s92, v186\n" \
"v_and_b32 v187, s92, v187\n" \
"v_mfma_f32_16x16x32_f16 v[64:67], v[156:159], v[172:175], v[64:67]\n" \
"ds_read_b128 v[164:167], v240 offset:41472\n" \
"buffer_store_dwordx4 v[184:187], v234, s[36:39], s82 offen offset:1024 sc1\n" \
"ds_read_b64_tr_b16 v[144:145], v241 offset:33280\n" \
"v_mfma_f32_16x16x32_f16 v[56:59], v[120:123], v[168:171], v[56:59]\n" \
"buffer_load_dwordx4 v232, s[36:39], s80 offen offset:2048 sc1 lds\n" \
"v_mfma_f32_16x16x32_f16 v[68:71], v[124:127], v[168:171], v[68:71]\n" \
"s_setprio 0\n" \
"ds_read_b64_tr_b16 v[146:147], v241 offset:41472\n" \
"ds_read_b64_tr_b16 v[148:149], v242 offset:33280\n" \
"v_mfma_f32_16x16x32_f16 v[72:75], v[136:139], v[176:179], v[72:75]\n" \
"ds_read_b128 v[168:171], v240 offset:43008\n" \
"ds_read_b64_tr_b16 v[150:151], v242 offset:41472\n" \
"v_mfma_f32_16x16x32_f16 v[84:87], v[140:143], v[176:179], v[84:87]\n" \
"s_waitcnt lgkmcnt(6)\n" \
"v_pk_add_f16 v176, v176, v180\n" \
"v_pk_add_f16 v177, v177, v181\n" \
"v_mfma_f32_16x16x32_f16 v[76:79], v[152:155], v[180:183], v[76:79]\n" \
"v_pk_add_f16 v178, v178, v182\n" \
"v_pk_add_f16 v179, v179, v183\n" \
"v_mfma_f32_16x16x32_f16 v[88:91], v[156:159], v[180:183], v[88:91]\n" \
"ds_read_b128 v[172:175], v240 offset:43520\n" \
"v_mfma_f32_16x16x32_f16 v[80:83], v[120:123], v[176:179], v[80:83]\n" \
"buffer_load_dwordx4 v232, s[36:39], s80 offen offset:3072 sc1 lds\n" \
"v_mfma_f32_16x16x32_f16 v[92:95], v[124:127], v[176:179], v[92:95]\n" \
"v_mfma_f32_16x16x32_f16 v[96:99], v[136:139], v[160:163], v[96:99]\n" \
"s_waitcnt lgkmcnt(5)\n" \
"v_pk_add_f16 v136, v128, v144\n" \
"v_pk_add_f16 v137, v129, v145\n" \
"ds_read_b128 v[176:179], v240 offset:45056\n" \
"v_mfma_f32_16x16x32_f16 v[108:111], v[140:143], v[160:163], v[108:111]\n" \
"v_pk_add_f16 v160, v160, v164\n" \
"v_pk_add_f16 v161, v161, v165\n" \
"ds_read_b128 v[180:183], v240 offset:45568\n" \
"v_mfma_f32_16x16x32_f16 v[100:103], v[152:155], v[164:167], v[100:103]\n" \
"v_pk_add_f16 v162, v162, v166\n" \
"v_pk_add_f16 v163, v163, v167\n" \
"s_add_i32 m0, s45, 0x5000\n" \
"s_add_i32 s81, s47, 0x3c00000\n" \
"v_mfma_f32_16x16x32_f16 v[112:115], v[156:159], v[164:167], v[112:115]\n" \
"s_waitcnt lgkmcnt(3)\n" \
"v_pk_add_f16 v138, v130, v146\n" \
"v_pk_add_f16 v139, v131, v147\n" \
"buffer_load_dwordx4 v232, s[36:39], s81 offen sc1 lds\n" \
"v_mfma_f32_16x16x32_f16 v[104:107], v[120:123], v[160:163], v[104:107]\n" \
"v_pk_add_f16 v140, v132, v148\n" \
"v_pk_add_f16 v141, v133, v149\n" \
"v_mfma_f32_16x16x32_f16 v[116:119], v[124:127], v[160:163], v[116:119]\n" \
"s_setprio 1\n" \
"v_pk_add_f16 v142, v134, v150\n" \
"v_pk_add_f16 v143, v135, v151\n" \
"s_waitcnt vmcnt(7) lgkmcnt(0)\n" \
"s_barrier\n" \
"v_mfma_f32_16x16x32_f16 v[0:3], v[128:131], v[168:171], v[0:3]\n" \
"ds_read_b128 v[160:163], v240 offset:6144\n" \
"ds_read_b64_tr_b16 v[184:185], v236 offset:28672\n" \
"ds_read_b64_tr_b16 v[188:189], v237 offset:28672\n" \
"s_add_i32 m0, s54, 0x0\n" \
"s_mov_b32 s83, 0x0\n" \
"v_mfma_f32_16x16x32_f16 v[12:15], v[132:135], v[168:171], v[12:15]\n" \
"v_pk_add_f16 v168, v168, v172\n" \
"v_pk_add_f16 v169, v169, v173\n" \
"buffer_load_dwordx4 v233, s[36:39], s83 offen sc1 lds\n" \
"v_mfma_f32_16x16x32_f16 v[4:7], v[144:147], v[172:175], v[4:7]\n" \
"v_pk_add_f16 v170, v170, v174\n" \
"v_pk_add_f16 v171, v171, v175\n" \
"v_mfma_f32_16x16x32_f16 v[16:19], v[148:151], v[172:175], v[16:19]\n" \
"ds_read_b128 v[164:167], v240 offset:6656\n" \
"s_add_i32 m0, s44, 0xa000\n" \
"s_add_i32 s80, s46, 0x4000000\n" \
"v_mfma_f32_16x16x32_f16 v[8:11], v[136:139], v[168:171], v[8:11]\n" \
"buffer_load_dwordx4 v232, s[36:39], s80 offen sc1 lds\n" \
"s_waitcnt lgkmcnt(2)\n" \
"v_mfma_f32_16x16x16_bf16 v[184:187], v[184:185], v[192:193], 0\n" \
"v_mfma_f32_16x16x32_f16 v[20:23], v[140:143], v[168:171], v[20:23]\n" \
"ds_read_b64_tr_b16 v[120:121], v241 offset:0\n" \
"ds_read_b64_tr_b16 v[122:123], v241 offset:8192\n" \
"s_waitcnt lgkmcnt(3)\n" \
"v_mfma_f32_16x16x16_bf16 v[188:191], v[188:189], v[192:193], 0\n" \
"v_mfma_f32_16x16x32_f16 v[24:27], v[128:131], v[176:179], v[24:27]\n" \
"ds_read_b128 v[168:171], v240 offset:8192\n" \
"v_mfma_f32_16x16x32_f16 v[36:39], v[132:135], v[176:179], v[36:39]\n" \
"v_pk_add_f16 v176, v176, v180\n" \
"v_pk_add_f16 v177, v177, v181\n" \
"ds_read_b64_tr_b16 v[124:125], v242 offset:0\n" \
"v_mfma_f32_16x16x32_f16 v[28:31], v[144:147], v[180:183], v[28:31]\n" \
"v_pk_add_f16 v178, v178, v182\n" \
"v_pk_add_f16 v179, v179, v183\n" \
"v_cvt_pk_f16_f32 v184, v184, v185\n" \
"v_mfma_f32_16x16x32_f16 v[40:43], v[148:151], v[180:183], v[40:43]\n" \
"v_cvt_pk_f16_f32 v185, v186, v187\n" \
"v_cvt_pk_f16_f32 v186, v188, v189\n" \
"ds_read_b128 v[172:175], v240 offset:8704\n" \
"v_mfma_f32_16x16x32_f16 v[32:35], v[136:139], v[176:179], v[32:35]\n" \
"v_cvt_pk_f16_f32 v187, v190, v191\n" \
"v_add_u32 v184, s91, v184\n" \
"buffer_load_dwordx4 v232, s[36:39], s80 offen offset:1024 sc1 lds\n" \
"v_mfma_f32_16x16x32_f16 v[44:47], v[140:143], v[176:179], v[44:47]\n" \
"v_add_u32 v185, s91, v185\n" \
"v_add_u32 v186, s91, v186\n" \
"ds_read_b64_tr_b16 v[126:127], v242 offset:8192\n" \
"v_mfma_f32_16x16x32_f16 v[48:51], v[128:131], v[160:163], v[48:51]\n" \
"v_add_u32 v187, s91, v187\n" \
"v_and_b32 v184, s92, v184\n" \
"ds_read_b128 v[176:179], v240 offset:0\n" \
"v_mfma_f32_16x16x32_f16 v[60:63], v[132:135], v[160:163], v[60:63]\n" \
"s_waitcnt lgkmcnt(4)\n" \
"v_pk_add_f16 v160, v160, v164\n" \
"v_pk_add_f16 v161, v161, v165\n" \
"v_and_b32 v185, s92, v185\n" \
"s_add_i32 s82, s48, 0x6000000\n" \
"v_mfma_f32_16x16x32_f16 v[52:55], v[144:147], v[164:167], v[52:55]\n" \
"v_pk_add_f16 v162, v162, v166\n" \
"v_pk_add_f16 v163, v163, v167\n" \
"v_and_b32 v186, s92, v186\n" \
"v_and_b32 v187, s92, v187\n" \
"v_mfma_f32_16x16x32_f16 v[64:67], v[148:151], v[164:167], v[64:67]\n" \
"ds_read_b128 v[180:183], v240 offset:512\n" \
"buffer_store_dwordx4 v[184:187], v234, s[36:39], s82 offen sc1\n" \
"ds_read_b64_tr_b16 v[152:153], v241 offset:512\n" \
"v_mfma_f32_16x16x32_f16 v[56:59], v[136:139], v[160:163], v[56:59]\n" \
"buffer_load_dwordx4 v232, s[36:39], s80 offen offset:2048 sc1 lds\n" \
"v_mfma_f32_16x16x32_f16 v[68:71], v[140:143], v[160:163], v[68:71]\n" \
"s_setprio 0\n" \
"ds_read_b64_tr_b16 v[154:155], v241 offset:8704\n" \
"ds_read_b64_tr_b16 v[156:157], v242 offset:512\n" \
"v_mfma_f32_16x16x32_f16 v[72:75], v[128:131], v[168:171], v[72:75]\n" \
"ds_read_b128 v[160:163], v240 offset:2048\n" \
"ds_read_b64_tr_b16 v[158:159], v242 offset:8704\n" \
"v_mfma_f32_16x16x32_f16 v[84:87], v[132:135], v[168:171], v[84:87]\n" \
"s_waitcnt lgkmcnt(6)\n" \
"v_pk_add_f16 v168, v168, v172\n" \
"v_pk_add_f16 v169, v169, v173\n" \
"v_mfma_f32_16x16x32_f16 v[76:79], v[144:147], v[172:175], v[76:79]\n" \
"v_pk_add_f16 v170, v170, v174\n" \
"v_pk_add_f16 v171, v171, v175\n" \
"v_mfma_f32_16x16x32_f16 v[88:91], v[148:151], v[172:175], v[88:91]\n" \
"ds_read_b128 v[164:167], v240 offset:2560\n" \
"v_mfma_f32_16x16x32_f16 v[80:83], v[136:139], v[168:171], v[80:83]\n" \
"buffer_load_dwordx4 v232, s[36:39], s80 offen offset:3072 sc1 lds\n" \
"v_mfma_f32_16x16x32_f16 v[92:95], v[140:143], v[168:171], v[92:95]\n" \
"v_mfma_f32_16x16x32_f16 v[96:99], v[128:131], v[176:179], v[96:99]\n" \
"s_waitcnt lgkmcnt(5)\n" \
"v_pk_add_f16 v128, v120, v152\n" \
"v_pk_add_f16 v129, v121, v153\n" \
"ds_read_b128 v[168:171], v240 offset:4096\n" \
"v_mfma_f32_16x16x32_f16 v[108:111], v[132:135], v[176:179], v[108:111]\n" \
"v_pk_add_f16 v176, v176, v180\n" \
"v_pk_add_f16 v177, v177, v181\n" \
"ds_read_b128 v[172:175], v240 offset:4608\n" \
"v_mfma_f32_16x16x32_f16 v[100:103], v[144:147], v[180:183], v[100:103]\n" \
"v_pk_add_f16 v178, v178, v182\n" \
"v_pk_add_f16 v179, v179, v183\n" \
"s_add_i32 m0, s45, 0xa000\n" \
"s_add_i32 s81, s47, 0x4000000\n" \
"v_mfma_f32_16x16x32_f16 v[112:115], v[148:151], v[180:183], v[112:115]\n" \
"s_waitcnt lgkmcnt(3)\n" \
"v_pk_add_f16 v130, v122, v154\n" \
"v_pk_add_f16 v131, v123, v155\n" \
"buffer_load_dwordx4 v232, s[36:39], s81 offen sc1 lds\n" \
"v_mfma_f32_16x16x32_f16 v[104:107], v[136:139], v[176:179], v[104:107]\n" \
"v_pk_add_f16 v132, v124, v156\n" \
"v_pk_add_f16 v133, v125, v157\n" \
"v_mfma_f32_16x16x32_f16 v[116:119], v[140:143], v[176:179], v[116:119]\n" \
"s_setprio 1\n" \
"v_pk_add_f16 v134, v126, v158\n" \
"v_pk_add_f16 v135, v127, v159\n" \
"s_waitcnt vmcnt(7) lgkmcnt(0)\n" \
"s_barrier\n" \
"v_mfma_f32_16x16x32_f16 v[0:3], v[120:123], v[160:163], v[0:3]\n" \
"ds_read_b128 v[176:179], v240 offset:26624\n" \
"ds_read_b64_tr_b16 v[184:185], v238 offset:28672\n" \
"ds_read_b64_tr_b16 v[188:189], v239 offset:28672\n" \
"v_mfma_f32_16x16x32_f16 v[12:15], v[124:127], v[160:163], v[12:15]\n" \
"v_pk_add_f16 v160, v160, v164\n" \
"v_pk_add_f16 v161, v161, v165\n" \
"v_mfma_f32_16x16x32_f16 v[4:7], v[152:155], v[164:167], v[4:7]\n" \
"v_pk_add_f16 v162, v162, v166\n" \
"v_pk_add_f16 v163, v163, v167\n" \
"v_mfma_f32_16x16x32_f16 v[16:19], v[156:159], v[164:167], v[16:19]\n" \
"ds_read_b128 v[180:183], v240 offset:27136\n" \
"s_add_i32 m0, s44, 0x0\n" \
"s_add_i32 s80, s46, 0x4400000\n" \
"v_mfma_f32_16x16x32_f16 v[8:11], v[128:131], v[160:163], v[8:11]\n" \
"buffer_load_dwordx4 v232, s[36:39], s80 offen sc1 lds\n" \
"s_waitcnt lgkmcnt(2)\n" \
"v_mfma_f32_16x16x16_bf16 v[184:187], v[184:185], v[192:193], 0\n" \
"v_mfma_f32_16x16x32_f16 v[20:23], v[132:135], v[160:163], v[20:23]\n" \
"ds_read_b64_tr_b16 v[136:137], v241 offset:16384\n" \
"ds_read_b64_tr_b16 v[138:139], v241 offset:24576\n" \
"s_waitcnt lgkmcnt(3)\n" \
"v_mfma_f32_16x16x16_bf16 v[188:191], v[188:189], v[192:193], 0\n" \
"v_mfma_f32_16x16x32_f16 v[24:27], v[120:123], v[168:171], v[24:27]\n" \
"ds_read_b128 v[160:163], v240 offset:28672\n" \
"v_mfma_f32_16x16x32_f16 v[36:39], v[124:127], v[168:171], v[36:39]\n" \
"v_pk_add_f16 v168, v168, v172\n" \
"v_pk_add_f16 v169, v169, v173\n" \
"ds_read_b64_tr_b16 v[140:141], v242 offset:16384\n" \
"v_mfma_f32_16x16x32_f16 v[28:31], v[152:155], v[172:175], v[28:31]\n" \
"v_pk_add_f16 v170, v170, v174\n" \
"v_pk_add_f16 v171, v171, v175\n" \
"v_cvt_pk_f16_f32 v184, v184, v185\n" \
"v_mfma_f32_16x16x32_f16 v[40:43], v[156:159], v[172:175], v[40:43]\n" \
"v_cvt_pk_f16_f32 v185, v186, v187\n" \
"v_cvt_pk_f16_f32 v186, v188, v189\n" \
"ds_read_b128 v[164:167], v240 offset:29184\n" \
"v_mfma_f32_16x16x32_f16 v[32:35], v[128:131], v[168:171], v[32:35]\n" \
"v_cvt_pk_f16_f32 v187, v190, v191\n" \
"v_add_u32 v184, s91, v184\n" \
"buffer_load_dwordx4 v232, s[36:39], s80 offen offset:1024 sc1 lds\n" \
"v_mfma_f32_16x16x32_f16 v[44:47], v[132:135], v[168:171], v[44:47]\n" \
"v_add_u32 v185, s91, v185\n" \
"v_add_u32 v186, s91, v186\n" \
"ds_read_b64_tr_b16 v[142:143], v242 offset:24576\n" \
"v_mfma_f32_16x16x32_f16 v[48:51], v[120:123], v[176:179], v[48:51]\n" \
"v_add_u32 v187, s91, v187\n" \
"v_and_b32 v184, s92, v184\n" \
"ds_read_b128 v[168:171], v240 offset:20480\n" \
"v_mfma_f32_16x16x32_f16 v[60:63], v[124:127], v[176:179], v[60:63]\n" \
"s_waitcnt lgkmcnt(4)\n" \
"v_pk_add_f16 v176, v176, v180\n" \
"v_pk_add_f16 v177, v177, v181\n" \
"v_and_b32 v185, s92, v185\n" \
"v_mfma_f32_16x16x32_f16 v[52:55], v[152:155], v[180:183], v[52:55]\n" \
"v_pk_add_f16 v178, v178, v182\n" \
"v_pk_add_f16 v179, v179, v183\n" \
"v_and_b32 v186, s92, v186\n" \
"v_and_b32 v187, s92, v187\n" \
"v_mfma_f32_16x16x32_f16 v[64:67], v[156:159], v[180:183], v[64:67]\n" \
"ds_read_b128 v[172:175], v240 offset:20992\n" \
"buffer_store_dwordx4 v[184:187], v234, s[36:39], s82 offen offset:1024 sc1\n" \
"ds_read_b64_tr_b16 v[144:145], v241 offset:16896\n" \
"v_mfma_f32_16x16x32_f16 v[56:59], v[128:131], v[176:179], v[56:59]\n" \
"buffer_load_dwordx4 v232, s[36:39], s80 offen offset:2048 sc1 lds\n" \
"v_mfma_f32_16x16x32_f16 v[68:71], v[132:135], v[176:179], v[68:71]\n" \
"s_setprio 0\n" \
"ds_read_b64_tr_b16 v[146:147], v241 offset:25088\n" \
"ds_read_b64_tr_b16 v[148:149], v242 offset:16896\n" \
"v_mfma_f32_16x16x32_f16 v[72:75], v[120:123], v[160:163], v[72:75]\n" \
"ds_read_b128 v[176:179], v240 offset:22528\n" \
"ds_read_b64_tr_b16 v[150:151], v242 offset:25088\n" \
"v_mfma_f32_16x16x32_f16 v[84:87], v[124:127], v[160:163], v[84:87]\n" \
"s_waitcnt lgkmcnt(6)\n" \
"v_pk_add_f16 v160, v160, v164\n" \
"v_pk_add_f16 v161, v161, v165\n" \
"v_mfma_f32_16x16x32_f16 v[76:79], v[152:155], v[164:167], v[76:79]\n" \
"v_pk_add_f16 v162, v162, v166\n" \
"v_pk_add_f16 v163, v163, v167\n" \
"v_mfma_f32_16x16x32_f16 v[88:91], v[156:159], v[164:167], v[88:91]\n" \
"ds_read_b128 v[180:183], v240 offset:23040\n" \
"v_mfma_f32_16x16x32_f16 v[80:83], v[128:131], v[160:163], v[80:83]\n" \
"buffer_load_dwordx4 v232, s[36:39], s80 offen offset:3072 sc1 lds\n" \
"v_mfma_f32_16x16x32_f16 v[92:95], v[132:135], v[160:163], v[92:95]\n" \
"v_mfma_f32_16x16x32_f16 v[96:99], v[120:123], v[168:171], v[96:99]\n" \
"s_waitcnt lgkmcnt(5)\n" \
"v_pk_add_f16 v120, v136, v144\n" \
"v_pk_add_f16 v121, v137, v145\n" \
"ds_read_b128 v[160:163], v240 offset:24576\n" \
"v_mfma_f32_16x16x32_f16 v[108:111], v[124:127], v[168:171], v[108:111]\n" \
"v_pk_add_f16 v168, v168, v172\n" \
"v_pk_add_f16 v169, v169, v173\n" \
"ds_read_b128 v[164:167], v240 offset:25088\n" \
"v_mfma_f32_16x16x32_f16 v[100:103], v[152:155], v[172:175], v[100:103]\n" \
"v_pk_add_f16 v170, v170, v174\n" \
"v_pk_add_f16 v171, v171, v175\n" \
"s_add_i32 m0, s45, 0x0\n" \
"s_add_i32 s81, s47, 0x4400000\n" \
"v_mfma_f32_16x16x32_f16 v[112:115], v[156:159], v[172:175], v[112:115]\n" \
"s_waitcnt lgkmcnt(3)\n" \
"v_pk_add_f16 v122, v138, v146\n" \
"v_pk_add_f16 v123, v139, v147\n" \
"buffer_load_dwordx4 v232, s[36:39], s81 offen sc1 lds\n" \
"v_mfma_f32_16x16x32_f16 v[104:107], v[128:131], v[168:171], v[104:107]\n" \
"v_pk_add_f16 v124, v140, v148\n" \
"v_pk_add_f16 v125, v141, v149\n" \
"v_mfma_f32_16x16x32_f16 v[116:119], v[132:135], v[168:171], v[116:119]\n" \
"s_setprio 1\n" \
"v_pk_add_f16 v126, v142, v150\n" \
"v_pk_add_f16 v127, v143, v151\n" \
"s_waitcnt vmcnt(6) lgkmcnt(0)\n" \
"s_barrier\n" \
"v_mfma_f32_16x16x32_f16 v[0:3], v[136:139], v[176:179], v[0:3]\n" \
"ds_read_b128 v[168:171], v240 offset:47104\n" \
"ds_read_b64_tr_b16 v[184:185], v236 offset:32768\n" \
"ds_read_b64_tr_b16 v[188:189], v237 offset:32768\n" \
"s_add_i32 m0, s54, 0x1000\n" \
"s_mov_b32 s83, 0x100\n" \
"v_mfma_f32_16x16x32_f16 v[12:15], v[140:143], v[176:179], v[12:15]\n" \
"v_pk_add_f16 v176, v176, v180\n" \
"v_pk_add_f16 v177, v177, v181\n" \
"buffer_load_dwordx4 v233, s[36:39], s83 offen sc1 lds\n" \
"v_mfma_f32_16x16x32_f16 v[4:7], v[144:147], v[180:183], v[4:7]\n" \
"v_pk_add_f16 v178, v178, v182\n" \
"v_pk_add_f16 v179, v179, v183\n" \
"v_mfma_f32_16x16x32_f16 v[16:19], v[148:151], v[180:183], v[16:19]\n" \
"ds_read_b128 v[172:175], v240 offset:47616\n" \
"s_add_i32 m0, s44, 0x5000\n" \
"s_add_i32 s80, s46, 0x4800000\n" \
"v_mfma_f32_16x16x32_f16 v[8:11], v[120:123], v[176:179], v[8:11]\n" \
"buffer_load_dwordx4 v232, s[36:39], s80 offen sc1 lds\n" \
"s_waitcnt lgkmcnt(2)\n" \
"v_mfma_f32_16x16x16_bf16 v[184:187], v[184:185], v[192:193], 0\n" \
"v_mfma_f32_16x16x32_f16 v[20:23], v[124:127], v[176:179], v[20:23]\n" \
"ds_read_b64_tr_b16 v[128:129], v241 offset:32768\n" \
"ds_read_b64_tr_b16 v[130:131], v241 offset:40960\n" \
"s_waitcnt lgkmcnt(3)\n" \
"v_mfma_f32_16x16x16_bf16 v[188:191], v[188:189], v[192:193], 0\n" \
"v_mfma_f32_16x16x32_f16 v[24:27], v[136:139], v[160:163], v[24:27]\n" \
"ds_read_b128 v[176:179], v240 offset:49152\n" \
"v_mfma_f32_16x16x32_f16 v[36:39], v[140:143], v[160:163], v[36:39]\n" \
"v_pk_add_f16 v160, v160, v164\n" \
"v_pk_add_f16 v161, v161, v165\n" \
"ds_read_b64_tr_b16 v[132:133], v242 offset:32768\n" \
"v_mfma_f32_16x16x32_f16 v[28:31], v[144:147], v[164:167], v[28:31]\n" \
"v_pk_add_f16 v162, v162, v166\n" \
"v_pk_add_f16 v163, v163, v167\n" \
"v_cvt_pk_f16_f32 v184, v184, v185\n" \
"v_mfma_f32_16x16x32_f16 v[40:43], v[148:151], v[164:167], v[40:43]\n" \
"v_cvt_pk_f16_f32 v185, v186, v187\n" \
"v_cvt_pk_f16_f32 v186, v188, v189\n" \
"ds_read_b128 v[180:183], v240 offset:49664\n" \
"v_mfma_f32_16x16x32_f16 v[32:35], v[120:123], v[160:163], v[32:35]\n" \
"v_cvt_pk_f16_f32 v187, v190, v191\n" \
"v_add_u32 v184, s91, v184\n" \
"buffer_load_dwordx4 v232, s[36:39], s80 offen offset:1024 sc1 lds\n" \
"v_mfma_f32_16x16x32_f16 v[44:47], v[124:127], v[160:163], v[44:47]\n" \
"v_add_u32 v185, s91, v185\n" \
"v_add_u32 v186, s91, v186\n" \
"ds_read_b64_tr_b16 v[134:135], v242 offset:40960\n" \
"v_mfma_f32_16x16x32_f16 v[48:51], v[136:139], v[168:171], v[48:51]\n" \
"v_add_u32 v187, s91, v187\n" \
"v_and_b32 v184, s92, v184\n" \
"ds_read_b128 v[160:163], v240 offset:40960\n" \
"v_mfma_f32_16x16x32_f16 v[60:63], v[140:143], v[168:171], v[60:63]\n" \
"s_waitcnt lgkmcnt(4)\n" \
"v_pk_add_f16 v168, v168, v172\n" \
"v_pk_add_f16 v169, v169, v173\n" \
"v_and_b32 v185, s92, v185\n" \
"s_add_i32 s82, s49, 0x6800000\n" \
"v_mfma_f32_16x16x32_f16 v[52:55], v[144:147], v[172:175], v[52:55]\n" \
"v_pk_add_f16 v170, v170, v174\n" \
"v_pk_add_f16 v171, v171, v175\n" \
"v_and_b32 v186, s92, v186\n" \
"v_and_b32 v187, s92, v187\n" \
"v_mfma_f32_16x16x32_f16 v[64:67], v[148:151], v[172:175], v[64:67]\n" \
"ds_read_b128 v[164:167], v240 offset:41472\n" \
"buffer_store_dwordx4 v[184:187], v234, s[36:39], s82 offen sc1\n" \
"ds_read_b64_tr_b16 v[152:153], v241 offset:33280\n" \
"v_mfma_f32_16x16x32_f16 v[56:59], v[120:123], v[168:171], v[56:59]\n" \
"buffer_load_dwordx4 v232, s[36:39], s80 offen offset:2048 sc1 lds\n" \
"v_mfma_f32_16x16x32_f16 v[68:71], v[124:127], v[168:171], v[68:71]\n" \
"s_setprio 0\n" \
"ds_read_b64_tr_b16 v[154:155], v241 offset:41472\n" \
"ds_read_b64_tr_b16 v[156:157], v242 offset:33280\n" \
"v_mfma_f32_16x16x32_f16 v[72:75], v[136:139], v[176:179], v[72:75]\n" \
"ds_read_b128 v[168:171], v240 offset:43008\n" \
"ds_read_b64_tr_b16 v[158:159], v242 offset:41472\n" \
"v_mfma_f32_16x16x32_f16 v[84:87], v[140:143], v[176:179], v[84:87]\n" \
"s_waitcnt lgkmcnt(6)\n" \
"v_pk_add_f16 v176, v176, v180\n" \
"v_pk_add_f16 v177, v177, v181\n" \
"v_mfma_f32_16x16x32_f16 v[76:79], v[144:147], v[180:183], v[76:79]\n" \
"v_pk_add_f16 v178, v178, v182\n" \
"v_pk_add_f16 v179, v179, v183\n" \
"v_mfma_f32_16x16x32_f16 v[88:91], v[148:151], v[180:183], v[88:91]\n" \
"ds_read_b128 v[172:175], v240 offset:43520\n" \
"v_mfma_f32_16x16x32_f16 v[80:83], v[120:123], v[176:179], v[80:83]\n" \
"buffer_load_dwordx4 v232, s[36:39], s80 offen offset:3072 sc1 lds\n" \
"v_mfma_f32_16x16x32_f16 v[92:95], v[124:127], v[176:179], v[92:95]\n" \
"v_mfma_f32_16x16x32_f16 v[96:99], v[136:139], v[160:163], v[96:99]\n" \
"s_waitcnt lgkmcnt(5)\n" \
"v_pk_add_f16 v136, v128, v152\n" \
"v_pk_add_f16 v137, v129, v153\n" \
"ds_read_b128 v[176:179], v240 offset:45056\n" \
"v_mfma_f32_16x16x32_f16 v[108:111], v[140:143], v[160:163], v[108:111]\n" \
"v_pk_add_f16 v160, v160, v164\n" \
"v_pk_add_f16 v161, v161, v165\n" \
"ds_read_b128 v[180:183], v240 offset:45568\n" \
"v_mfma_f32_16x16x32_f16 v[100:103], v[144:147], v[164:167], v[100:103]\n" \
"v_pk_add_f16 v162, v162, v166\n" \
"v_pk_add_f16 v163, v163, v167\n" \
"s_add_i32 m0, s45, 0x5000\n" \
"s_add_i32 s81, s47, 0x4800000\n" \
"v_mfma_f32_16x16x32_f16 v[112:115], v[148:151], v[164:167], v[112:115]\n" \
"s_waitcnt lgkmcnt(3)\n" \
"v_pk_add_f16 v138, v130, v154\n" \
"v_pk_add_f16 v139, v131, v155\n" \
"buffer_load_dwordx4 v232, s[36:39], s81 offen sc1 lds\n" \
"v_mfma_f32_16x16x32_f16 v[104:107], v[120:123], v[160:163], v[104:107]\n" \
"v_pk_add_f16 v140, v132, v156\n" \
"v_pk_add_f16 v141, v133, v157\n" \
"v_mfma_f32_16x16x32_f16 v[116:119], v[124:127], v[160:163], v[116:119]\n" \
"s_setprio 1\n" \
"v_pk_add_f16 v142, v134, v158\n" \
"v_pk_add_f16 v143, v135, v159\n" \
"s_waitcnt vmcnt(7) lgkmcnt(0)\n" \
"s_barrier\n" \
"v_mfma_f32_16x16x32_f16 v[0:3], v[128:131], v[168:171], v[0:3]\n" \
"ds_read_b128 v[160:163], v240 offset:6144\n" \
"ds_read_b64_tr_b16 v[184:185], v238 offset:32768\n" \
"ds_read_b64_tr_b16 v[188:189], v239 offset:32768\n" \
"v_mfma_f32_16x16x32_f16 v[12:15], v[132:135], v[168:171], v[12:15]\n" \
"v_pk_add_f16 v168, v168, v172\n" \
"v_pk_add_f16 v169, v169, v173\n" \
"v_mfma_f32_16x16x32_f16 v[4:7], v[152:155], v[172:175], v[4:7]\n" \
"v_pk_add_f16 v170, v170, v174\n" \
"v_pk_add_f16 v171, v171, v175\n" \
"v_mfma_f32_16x16x32_f16 v[16:19], v[156:159], v[172:175], v[16:19]\n" \
"ds_read_b128 v[164:167], v240 offset:6656\n" \
"s_add_i32 m0, s44, 0xa000\n" \
"s_add_i32 s80, s46, 0x4c00000\n" \
"v_mfma_f32_16x16x32_f16 v[8:11], v[136:139], v[168:171], v[8:11]\n" \
"buffer_load_dwordx4 v232, s[36:39], s80 offen sc1 lds\n" \
"s_waitcnt lgkmcnt(2)\n" \
"v_mfma_f32_16x16x16_bf16 v[184:187], v[184:185], v[192:193], 0\n" \
"v_mfma_f32_16x16x32_f16 v[20:23], v[140:143], v[168:171], v[20:23]\n" \
"ds_read_b64_tr_b16 v[120:121], v241 offset:0\n" \
"ds_read_b64_tr_b16 v[122:123], v241 offset:8192\n" \
"s_waitcnt lgkmcnt(3)\n" \
"v_mfma_f32_16x16x16_bf16 v[188:191], v[188:189], v[192:193], 0\n" \
"v_mfma_f32_16x16x32_f16 v[24:27], v[128:131], v[176:179], v[24:27]\n" \
"ds_read_b128 v[168:171], v240 offset:8192\n" \
"v_mfma_f32_16x16x32_f16 v[36:39], v[132:135], v[176:179], v[36:39]\n" \
"v_pk_add_f16 v176, v176, v180\n" \
"v_pk_add_f16 v177, v177, v181\n" \
"ds_read_b64_tr_b16 v[124:125], v242 offset:0\n" \
"v_mfma_f32_16x16x32_f16 v[28:31], v[152:155], v[180:183], v[28:31]\n" \
"v_pk_add_f16 v178, v178, v182\n" \
"v_pk_add_f16 v179, v179, v183\n" \
"v_cvt_pk_f16_f32 v184, v184, v185\n" \
"v_mfma_f32_16x16x32_f16 v[40:43], v[156:159], v[180:183], v[40:43]\n" \
"v_cvt_pk_f16_f32 v185, v186, v187\n" \
"v_cvt_pk_f16_f32 v186, v188, v189\n" \
"ds_read_b128 v[172:175], v240 offset:8704\n" \
"v_mfma_f32_16x16x32_f16 v[32:35], v[136:139], v[176:179], v[32:35]\n" \
"v_cvt_pk_f16_f32 v187, v190, v191\n" \
"v_add_u32 v184, s91, v184\n" \
"buffer_load_dwordx4 v232, s[36:39], s80 offen offset:1024 sc1 lds\n" \
"v_mfma_f32_16x16x32_f16 v[44:47], v[140:143], v[176:179], v[44:47]\n" \
"v_add_u32 v185, s91, v185\n" \
"v_add_u32 v186, s91, v186\n" \
"ds_read_b64_tr_b16 v[126:127], v242 offset:8192\n" \
"v_mfma_f32_16x16x32_f16 v[48:51], v[128:131], v[160:163], v[48:51]\n" \
"v_add_u32 v187, s91, v187\n" \
"v_and_b32 v184, s92, v184\n" \
"ds_read_b128 v[176:179], v240 offset:0\n" \
"v_mfma_f32_16x16x32_f16 v[60:63], v[132:135], v[160:163], v[60:63]\n" \
"s_waitcnt lgkmcnt(4)\n" \
"v_pk_add_f16 v160, v160, v164\n" \
"v_pk_add_f16 v161, v161, v165\n" \
"v_and_b32 v185, s92, v185\n" \
"v_mfma_f32_16x16x32_f16 v[52:55], v[152:155], v[164:167], v[52:55]\n" \
"v_pk_add_f16 v162, v162, v166\n" \
"v_pk_add_f16 v163, v163, v167\n" \
"v_and_b32 v186, s92, v186\n" \
"v_and_b32 v187, s92, v187\n" \
"v_mfma_f32_16x16x32_f16 v[64:67], v[156:159], v[164:167], v[64:67]\n" \
"ds_read_b128 v[180:183], v240 offset:512\n" \
"buffer_store_dwordx4 v[184:187], v234, s[36:39], s82 offen offset:1024 sc1\n" \
"ds_read_b64_tr_b16 v[144:145], v241 offset:512\n" \
"v_mfma_f32_16x16x32_f16 v[56:59], v[136:139], v[160:163], v[56:59]\n" \
"buffer_load_dwordx4 v232, s[36:39], s80 offen offset:2048 sc1 lds\n" \
"v_mfma_f32_16x16x32_f16 v[68:71], v[140:143], v[160:163], v[68:71]\n" \
"s_setprio 0\n" \
"ds_read_b64_tr_b16 v[146:147], v241 offset:8704\n" \
"ds_read_b64_tr_b16 v[148:149], v242 offset:512\n" \
"v_mfma_f32_16x16x32_f16 v[72:75], v[128:131], v[168:171], v[72:75]\n" \
"ds_read_b128 v[160:163], v240 offset:2048\n" \
"ds_read_b64_tr_b16 v[150:151], v242 offset:8704\n" \
"v_mfma_f32_16x16x32_f16 v[84:87], v[132:135], v[168:171], v[84:87]\n" \
"s_waitcnt lgkmcnt(6)\n" \
"v_pk_add_f16 v168, v168, v172\n" \
"v_pk_add_f16 v169, v169, v173\n" \
"v_mfma_f32_16x16x32_f16 v[76:79], v[152:155], v[172:175], v[76:79]\n" \
"v_pk_add_f16 v170, v170, v174\n" \
"v_pk_add_f16 v171, v171, v175\n" \
"v_mfma_f32_16x16x32_f16 v[88:91], v[156:159], v[172:175], v[88:91]\n" \
"ds_read_b128 v[164:167], v240 offset:2560\n" \
"v_mfma_f32_16x16x32_f16 v[80:83], v[136:139], v[168:171], v[80:83]\n" \
"buffer_load_dwordx4 v232, s[36:39], s80 offen offset:3072 sc1 lds\n" \
"v_mfma_f32_16x16x32_f16 v[92:95], v[140:143], v[168:171], v[92:95]\n" \
"v_mfma_f32_16x16x32_f16 v[96:99], v[128:131], v[176:179], v[96:99]\n" \
"s_waitcnt lgkmcnt(5)\n" \
"v_pk_add_f16 v128, v120, v144\n" \
"v_pk_add_f16 v129, v121, v145\n" \
"ds_read_b128 v[168:171], v240 offset:4096\n" \
"v_mfma_f32_16x16x32_f16 v[108:111], v[132:135], v[176:179], v[108:111]\n" \
"v_pk_add_f16 v176, v176, v180\n" \
"v_pk_add_f16 v177, v177, v181\n" \
"ds_read_b128 v[172:175], v240 offset:4608\n" \
"v_mfma_f32_16x16x32_f16 v[100:103], v[152:155], v[180:183], v[100:103]\n" \
"v_pk_add_f16 v178, v178, v182\n" \
"v_pk_add_f16 v179, v179, v183\n" \
"s_add_i32 m0, s45, 0xa000\n" \
"s_add_i32 s81, s47, 0x4c00000\n" \
"v_mfma_f32_16x16x32_f16 v[112:115], v[156:159], v[180:183], v[112:115]\n" \
"s_waitcnt lgkmcnt(3)\n" \
"v_pk_add_f16 v130, v122, v146\n" \
"v_pk_add_f16 v131, v123, v147\n" \
"buffer_load_dwordx4 v232, s[36:39], s81 offen sc1 lds\n" \
"v_mfma_f32_16x16x32_f16 v[104:107], v[136:139], v[176:179], v[104:107]\n" \
"v_pk_add_f16 v132, v124, v148\n" \
"v_pk_add_f16 v133, v125, v149\n" \
"v_mfma_f32_16x16x32_f16 v[116:119], v[140:143], v[176:179], v[116:119]\n" \
"s_setprio 1\n" \
"v_pk_add_f16 v134, v126, v150\n" \
"v_pk_add_f16 v135, v127, v151\n" \
"s_waitcnt vmcnt(6) lgkmcnt(0)\n" \
"s_barrier\n" \
"v_mfma_f32_16x16x32_f16 v[0:3], v[120:123], v[160:163], v[0:3]\n" \
"ds_read_b128 v[176:179], v240 offset:26624\n" \
"ds_read_b64_tr_b16 v[184:185], v236 offset:36864\n" \
"ds_read_b64_tr_b16 v[188:189], v237 offset:36864\n" \
"s_add_i32 m0, s54, 0x0\n" \
"s_mov_b32 s83, 0x200\n" \
"v_mfma_f32_16x16x32_f16 v[12:15], v[124:127], v[160:163], v[12:15]\n" \
"v_pk_add_f16 v160, v160, v164\n" \
"v_pk_add_f16 v161, v161, v165\n" \
"buffer_load_dwordx4 v233, s[36:39], s83 offen sc1 lds\n" \
"v_mfma_f32_16x16x32_f16 v[4:7], v[144:147], v[164:167], v[4:7]\n" \
"v_pk_add_f16 v162, v162, v166\n" \
"v_pk_add_f16 v163, v163, v167\n" \
"v_mfma_f32_16x16x32_f16 v[16:19], v[148:151], v[164:167], v[16:19]\n" \
"ds_read_b128 v[180:183], v240 offset:27136\n" \
"s_add_i32 m0, s44, 0x0\n" \
"s_add_i32 s80, s46, 0x5000000\n" \
"v_mfma_f32_16x16x32_f16 v[8:11], v[128:131], v[160:163], v[8:11]\n" \
"buffer_load_dwordx4 v232, s[36:39], s80 offen sc1 lds\n" \
"s_waitcnt lgkmcnt(2)\n" \
"v_mfma_f32_16x16x16_bf16 v[184:187], v[184:185], v[192:193], 0\n" \
"v_mfma_f32_16x16x32_f16 v[20:23], v[132:135], v[160:163], v[20:23]\n" \
"ds_read_b64_tr_b16 v[136:137], v241 offset:16384\n" \
"ds_read_b64_tr_b16 v[138:139], v241 offset:24576\n" \
"s_waitcnt lgkmcnt(3)\n" \
"v_mfma_f32_16x16x16_bf16 v[188:191], v[188:189], v[192:193], 0\n" \
"v_mfma_f32_16x16x32_f16 v[24:27], v[120:123], v[168:171], v[24:27]\n" \
"ds_read_b128 v[160:163], v240 offset:28672\n" \
"v_mfma_f32_16x16x32_f16 v[36:39], v[124:127], v[168:171], v[36:39]\n" \
"v_pk_add_f16 v168, v168, v172\n" \
"v_pk_add_f16 v169, v169, v173\n" \
"ds_read_b64_tr_b16 v[140:141], v242 offset:16384\n" \
"v_mfma_f32_16x16x32_f16 v[28:31], v[144:147], v[172:175], v[28:31]\n" \
"v_pk_add_f16 v170, v170, v174\n" \
"v_pk_add_f16 v171, v171, v175\n" \
"v_cvt_pk_f16_f32 v184, v184, v185\n" \
"v_mfma_f32_16x16x32_f16 v[40:43], v[148:151], v[172:175], v[40:43]\n" \
"v_cvt_pk_f16_f32 v185, v186, v187\n" \
"v_cvt_pk_f16_f32 v186, v188, v189\n" \
"ds_read_b128 v[164:167], v240 offset:29184\n" \
"v_mfma_f32_16x16x32_f16 v[32:35], v[128:131], v[168:171], v[32:35]\n" \
"v_cvt_pk_f16_f32 v187, v190, v191\n" \
"v_add_u32 v184, s91, v184\n" \
"buffer_load_dwordx4 v232, s[36:39], s80 offen offset:1024 sc1 lds\n" \
"v_mfma_f32_16x16x32_f16 v[44:47], v[132:135], v[168:171], v[44:47]\n" \
"v_add_u32 v185, s91, v185\n" \
"v_add_u32 v186, s91, v186\n" \
"ds_read_b64_tr_b16 v[142:143], v242 offset:24576\n" \
"v_mfma_f32_16x16x32_f16 v[48:51], v[120:123], v[176:179], v[48:51]\n" \
"v_add_u32 v187, s91, v187\n" \
"v_and_b32 v184, s92, v184\n" \
"ds_read_b128 v[168:171], v240 offset:20480\n" \
"v_mfma_f32_16x16x32_f16 v[60:63], v[124:127], v[176:179], v[60:63]\n" \
"s_waitcnt lgkmcnt(4)\n" \
"v_pk_add_f16 v176, v176, v180\n" \
"v_pk_add_f16 v177, v177, v181\n" \
"v_and_b32 v185, s92, v185\n" \
"s_add_i32 s82, s50, 0x7000000\n" \
"v_mfma_f32_16x16x32_f16 v[52:55], v[144:147], v[180:183], v[52:55]\n" \
"v_pk_add_f16 v178, v178, v182\n" \
"v_pk_add_f16 v179, v179, v183\n" \
"v_and_b32 v186, s92, v186\n" \
"v_and_b32 v187, s92, v187\n" \
"v_mfma_f32_16x16x32_f16 v[64:67], v[148:151], v[180:183], v[64:67]\n" \
"ds_read_b128 v[172:175], v240 offset:20992\n" \
"buffer_store_dwordx4 v[184:187], v234, s[36:39], s82 offen sc1\n" \
"ds_read_b64_tr_b16 v[152:153], v241 offset:16896\n" \
"v_mfma_f32_16x16x32_f16 v[56:59], v[128:131], v[176:179], v[56:59]\n" \
"buffer_load_dwordx4 v232, s[36:39], s80 offen offset:2048 sc1 lds\n" \
"v_mfma_f32_16x16x32_f16 v[68:71], v[132:135], v[176:179], v[68:71]\n" \
"s_setprio 0\n" \
"ds_read_b64_tr_b16 v[154:155], v241 offset:25088\n" \
"ds_read_b64_tr_b16 v[156:157], v242 offset:16896\n" \
"v_mfma_f32_16x16x32_f16 v[72:75], v[120:123], v[160:163], v[72:75]\n" \
"ds_read_b128 v[176:179], v240 offset:22528\n" \
"ds_read_b64_tr_b16 v[158:159], v242 offset:25088\n" \
"v_mfma_f32_16x16x32_f16 v[84:87], v[124:127], v[160:163], v[84:87]\n" \
"s_waitcnt lgkmcnt(6)\n" \
"v_pk_add_f16 v160, v160, v164\n" \
"v_pk_add_f16 v161, v161, v165\n" \
"v_mfma_f32_16x16x32_f16 v[76:79], v[144:147], v[164:167], v[76:79]\n" \
"v_pk_add_f16 v162, v162, v166\n" \
"v_pk_add_f16 v163, v163, v167\n" \
"v_mfma_f32_16x16x32_f16 v[88:91], v[148:151], v[164:167], v[88:91]\n" \
"ds_read_b128 v[180:183], v240 offset:23040\n" \
"v_mfma_f32_16x16x32_f16 v[80:83], v[128:131], v[160:163], v[80:83]\n" \
"buffer_load_dwordx4 v232, s[36:39], s80 offen offset:3072 sc1 lds\n" \
"v_mfma_f32_16x16x32_f16 v[92:95], v[132:135], v[160:163], v[92:95]\n" \
"v_mfma_f32_16x16x32_f16 v[96:99], v[120:123], v[168:171], v[96:99]\n" \
"s_waitcnt lgkmcnt(5)\n" \
"v_pk_add_f16 v120, v136, v152\n" \
"v_pk_add_f16 v121, v137, v153\n" \
"ds_read_b128 v[160:163], v240 offset:24576\n" \
"v_mfma_f32_16x16x32_f16 v[108:111], v[124:127], v[168:171], v[108:111]\n" \
"v_pk_add_f16 v168, v168, v172\n" \
"v_pk_add_f16 v169, v169, v173\n" \
"ds_read_b128 v[164:167], v240 offset:25088\n" \
"v_mfma_f32_16x16x32_f16 v[100:103], v[144:147], v[172:175], v[100:103]\n" \
"v_pk_add_f16 v170, v170, v174\n" \
"v_pk_add_f16 v171, v171, v175\n" \
"s_add_i32 m0, s45, 0x0\n" \
"s_add_i32 s81, s47, 0x5000000\n" \
"v_mfma_f32_16x16x32_f16 v[112:115], v[148:151], v[172:175], v[112:115]\n" \
"s_waitcnt lgkmcnt(3)\n" \
"v_pk_add_f16 v122, v138, v154\n" \
"v_pk_add_f16 v123, v139, v155\n" \
"buffer_load_dwordx4 v232, s[36:39], s81 offen sc1 lds\n" \
"v_mfma_f32_16x16x32_f16 v[104:107], v[128:131], v[168:171], v[104:107]\n" \
"v_pk_add_f16 v124, v140, v156\n" \
"v_pk_add_f16 v125, v141, v157\n" \
"v_mfma_f32_16x16x32_f16 v[116:119], v[132:135], v[168:171], v[116:119]\n" \
"s_setprio 1\n" \
"v_pk_add_f16 v126, v142, v158\n" \
"v_pk_add_f16 v127, v143, v159\n" \
"s_waitcnt vmcnt(7) lgkmcnt(0)\n" \
"s_barrier\n" \
"v_mfma_f32_16x16x32_f16 v[0:3], v[136:139], v[176:179], v[0:3]\n" \
"ds_read_b128 v[168:171], v240 offset:47104\n" \
"ds_read_b64_tr_b16 v[184:185], v238 offset:36864\n" \
"ds_read_b64_tr_b16 v[188:189], v239 offset:36864\n" \
"v_mfma_f32_16x16x32_f16 v[12:15], v[140:143], v[176:179], v[12:15]\n" \
"v_pk_add_f16 v176, v176, v180\n" \
"v_pk_add_f16 v177, v177, v181\n" \
"v_mfma_f32_16x16x32_f16 v[4:7], v[152:155], v[180:183], v[4:7]\n" \
"v_pk_add_f16 v178, v178, v182\n" \
"v_pk_add_f16 v179, v179, v183\n" \
"v_mfma_f32_16x16x32_f16 v[16:19], v[156:159], v[180:183], v[16:19]\n" \
"ds_read_b128 v[172:175], v240 offset:47616\n" \
"s_add_i32 m0, s44, 0x5000\n" \
"s_add_i32 s80, s46, 0x5400000\n" \
"v_mfma_f32_16x16x32_f16 v[8:11], v[120:123], v[176:179], v[8:11]\n" \
"buffer_load_dwordx4 v232, s[36:39], s80 offen sc1 lds\n" \
"s_waitcnt lgkmcnt(2)\n" \
"v_mfma_f32_16x16x16_bf16 v[184:187], v[184:185], v[192:193], 0\n" \
"v_mfma_f32_16x16x32_f16 v[20:23], v[124:127], v[176:179], v[20:23]\n" \
"ds_read_b64_tr_b16 v[128:129], v241 offset:32768\n" \
"ds_read_b64_tr_b16 v[130:131], v241 offset:40960\n" \
"s_waitcnt lgkmcnt(3)\n" \
"v_mfma_f32_16x16x16_bf16 v[188:191], v[188:189], v[192:193], 0\n" \
"v_mfma_f32_16x16x32_f16 v[24:27], v[136:139], v[160:163], v[24:27]\n" \
"ds_read_b128 v[176:179], v240 offset:49152\n" \
"v_mfma_f32_16x16x32_f16 v[36:39], v[140:143], v[160:163], v[36:39]\n" \
"v_pk_add_f16 v160, v160, v164\n" \
"v_pk_add_f16 v161, v161, v165\n" \
"ds_read_b64_tr_b16 v[132:133], v242 offset:32768\n" \
"v_mfma_f32_16x16x32_f16 v[28:31], v[152:155], v[164:167], v[28:31]\n" \
"v_pk_add_f16 v162, v162, v166\n" \
"v_pk_add_f16 v163, v163, v167\n" \
"v_cvt_pk_f16_f32 v184, v184, v185\n" \
"v_mfma_f32_16x16x32_f16 v[40:43], v[156:159], v[164:167], v[40:43]\n" \
"v_cvt_pk_f16_f32 v185, v186, v187\n" \
"v_cvt_pk_f16_f32 v186, v188, v189\n" \
"ds_read_b128 v[180:183], v240 offset:49664\n" \
"v_mfma_f32_16x16x32_f16 v[32:35], v[120:123], v[160:163], v[32:35]\n" \
"v_cvt_pk_f16_f32 v187, v190, v191\n" \
"v_add_u32 v184, s91, v184\n" \
"buffer_load_dwordx4 v232, s[36:39], s80 offen offset:1024 sc1 lds\n" \
"v_mfma_f32_16x16x32_f16 v[44:47], v[124:127], v[160:163], v[44:47]\n" \
"v_add_u32 v185, s91, v185\n" \
"v_add_u32 v186, s91, v186\n" \
"ds_read_b64_tr_b16 v[134:135], v242 offset:40960\n" \
"v_mfma_f32_16x16x32_f16 v[48:51], v[136:139], v[168:171], v[48:51]\n" \
"v_add_u32 v187, s91, v187\n" \
"v_and_b32 v184, s92, v184\n" \
"ds_read_b128 v[160:163], v240 offset:40960\n" \
"v_mfma_f32_16x16x32_f16 v[60:63], v[140:143], v[168:171], v[60:63]\n" \
"s_waitcnt lgkmcnt(4)\n" \
"v_pk_add_f16 v168, v168, v172\n" \
"v_pk_add_f16 v169, v169, v173\n" \
"v_and_b32 v185, s92, v185\n" \
"v_mfma_f32_16x16x32_f16 v[52:55], v[152:155], v[172:175], v[52:55]\n" \
"v_pk_add_f16 v170, v170, v174\n" \
"v_pk_add_f16 v171, v171, v175\n" \
"v_and_b32 v186, s92, v186\n" \
"v_and_b32 v187, s92, v187\n" \
"v_mfma_f32_16x16x32_f16 v[64:67], v[156:159], v[172:175], v[64:67]\n" \
"ds_read_b128 v[164:167], v240 offset:41472\n" \
"buffer_store_dwordx4 v[184:187], v234, s[36:39], s82 offen offset:1024 sc1\n" \
"ds_read_b64_tr_b16 v[144:145], v241 offset:33280\n" \
"v_mfma_f32_16x16x32_f16 v[56:59], v[120:123], v[168:171], v[56:59]\n" \
"buffer_load_dwordx4 v232, s[36:39], s80 offen offset:2048 sc1 lds\n" \
"v_mfma_f32_16x16x32_f16 v[68:71], v[124:127], v[168:171], v[68:71]\n" \
"s_setprio 0\n" \
"ds_read_b64_tr_b16 v[146:147], v241 offset:41472\n" \
"ds_read_b64_tr_b16 v[148:149], v242 offset:33280\n" \
"v_mfma_f32_16x16x32_f16 v[72:75], v[136:139], v[176:179], v[72:75]\n" \
"ds_read_b128 v[168:171], v240 offset:43008\n" \
"ds_read_b64_tr_b16 v[150:151], v242 offset:41472\n" \
"v_mfma_f32_16x16x32_f16 v[84:87], v[140:143], v[176:179], v[84:87]\n" \
"s_waitcnt lgkmcnt(6)\n" \
"v_pk_add_f16 v176, v176, v180\n" \
"v_pk_add_f16 v177, v177, v181\n" \
"v_mfma_f32_16x16x32_f16 v[76:79], v[152:155], v[180:183], v[76:79]\n" \
"v_pk_add_f16 v178, v178, v182\n" \
"v_pk_add_f16 v179, v179, v183\n" \
"v_mfma_f32_16x16x32_f16 v[88:91], v[156:159], v[180:183], v[88:91]\n" \
"ds_read_b128 v[172:175], v240 offset:43520\n" \
"v_mfma_f32_16x16x32_f16 v[80:83], v[120:123], v[176:179], v[80:83]\n" \
"buffer_load_dwordx4 v232, s[36:39], s80 offen offset:3072 sc1 lds\n" \
"v_mfma_f32_16x16x32_f16 v[92:95], v[124:127], v[176:179], v[92:95]\n" \
"v_mfma_f32_16x16x32_f16 v[96:99], v[136:139], v[160:163], v[96:99]\n" \
"s_waitcnt lgkmcnt(5)\n" \
"v_pk_add_f16 v136, v128, v144\n" \
"v_pk_add_f16 v137, v129, v145\n" \
"ds_read_b128 v[176:179], v240 offset:45056\n" \
"v_mfma_f32_16x16x32_f16 v[108:111], v[140:143], v[160:163], v[108:111]\n" \
"v_pk_add_f16 v160, v160, v164\n" \
"v_pk_add_f16 v161, v161, v165\n" \
"ds_read_b128 v[180:183], v240 offset:45568\n" \
"v_mfma_f32_16x16x32_f16 v[100:103], v[152:155], v[164:167], v[100:103]\n" \
"v_pk_add_f16 v162, v162, v166\n" \
"v_pk_add_f16 v163, v163, v167\n" \
"s_add_i32 m0, s45, 0x5000\n" \
"s_add_i32 s81, s47, 0x5400000\n" \
"v_mfma_f32_16x16x32_f16 v[112:115], v[156:159], v[164:167], v[112:115]\n" \
"s_waitcnt lgkmcnt(3)\n" \
"v_pk_add_f16 v138, v130, v146\n" \
"v_pk_add_f16 v139, v131, v147\n" \
"buffer_load_dwordx4 v232, s[36:39], s81 offen sc1 lds\n" \
"v_mfma_f32_16x16x32_f16 v[104:107], v[120:123], v[160:163], v[104:107]\n" \
"v_pk_add_f16 v140, v132, v148\n" \
"v_pk_add_f16 v141, v133, v149\n" \
"v_mfma_f32_16x16x32_f16 v[116:119], v[124:127], v[160:163], v[116:119]\n" \
"s_setprio 1\n" \
"v_pk_add_f16 v142, v134, v150\n" \
"v_pk_add_f16 v143, v135, v151\n" \
"s_waitcnt vmcnt(6) lgkmcnt(0)\n" \
"s_barrier\n" \
"v_mfma_f32_16x16x32_f16 v[0:3], v[128:131], v[168:171], v[0:3]\n" \
"ds_read_b128 v[160:163], v240 offset:6144\n" \
"ds_read_b64_tr_b16 v[184:185], v236 offset:40960\n" \
"s_add_i32 m0, s54, 0x1000\n" \
"s_mov_b32 s83, 0x300\n" \
"v_mfma_f32_16x16x32_f16 v[12:15], v[132:135], v[168:171], v[12:15]\n" \
"v_pk_add_f16 v168, v168, v172\n" \
"v_pk_add_f16 v169, v169, v173\n" \
"s_mov_b32 s84, 0x8014000\n" \
"buffer_load_dwordx4 v233, s[36:39], s83 offen sc1 lds\n" \
"v_mfma_f32_16x16x32_f16 v[4:7], v[144:147], v[172:175], v[4:7]\n" \
"v_pk_add_f16 v170, v170, v174\n" \
"v_pk_add_f16 v171, v171, v175\n" \
"buffer_load_dword v253, v252, s[36:39], s84 offen sc1\n" \
"v_mfma_f32_16x16x32_f16 v[16:19], v[148:151], v[172:175], v[16:19]\n" \
"ds_read_b128 v[164:167], v240 offset:6656\n" \
"s_add_i32 m0, s44, 0xa000\n" \
"s_add_i32 s80, s46, 0x5800000\n" \
"ds_read_b64_tr_b16 v[188:189], v237 offset:40960\n" \
"v_mfma_f32_16x16x32_f16 v[8:11], v[136:139], v[168:171], v[8:11]\n" \
"buffer_load_dwordx4 v232, s[36:39], s80 offen sc1 lds\n" \
"v_mfma_f32_16x16x32_f16 v[20:23], v[140:143], v[168:171], v[20:23]\n" \
"ds_read_b64_tr_b16 v[120:121], v241 offset:0\n" \
"ds_read_b64_tr_b16 v[122:123], v241 offset:8192\n" \
"s_waitcnt lgkmcnt(4)\n" \
"v_mfma_f32_16x16x16_bf16 v[184:187], v[184:185], v[192:193], 0\n" \
"v_mfma_f32_16x16x32_f16 v[24:27], v[128:131], v[176:179], v[24:27]\n" \
"ds_read_b128 v[168:171], v240 offset:8192\n" \
"s_waitcnt lgkmcnt(3)\n" \
"v_mfma_f32_16x16x16_bf16 v[188:191], v[188:189], v[192:193], 0\n" \
"v_mfma_f32_16x16x32_f16 v[36:39], v[132:135], v[176:179], v[36:39]\n" \
"v_pk_add_f16 v176, v176, v180\n" \
"v_pk_add_f16 v177, v177, v181\n" \
"ds_read_b64_tr_b16 v[124:125], v242 offset:0\n" \
"v_mfma_f32_16x16x32_f16 v[28:31], v[144:147], v[180:183], v[28:31]\n" \
"v_pk_add_f16 v178, v178, v182\n" \
"v_pk_add_f16 v179, v179, v183\n" \
"v_mfma_f32_16x16x32_f16 v[40:43], v[148:151], v[180:183], v[40:43]\n" \
"v_cvt_pk_f16_f32 v184, v184, v185\n" \
"ds_read_b128 v[172:175], v240 offset:8704\n" \
"ds_read_b64_tr_b16 v[126:127], v242 offset:8192\n" \
"v_mfma_f32_16x16x32_f16 v[32:35], v[136:139], v[176:179], v[32:35]\n" \
"v_cvt_pk_f16_f32 v185, v186, v187\n" \
"v_cvt_pk_f16_f32 v186, v188, v189\n" \
"buffer_load_dwordx4 v232, s[36:39], s80 offen offset:1024 sc1 lds\n" \
"v_mfma_f32_16x16x32_f16 v[44:47], v[140:143], v[176:179], v[44:47]\n" \
"v_cvt_pk_f16_f32 v187, v190, v191\n" \
"v_add_u32 v184, s91, v184\n" \
"v_mfma_f32_16x16x32_f16 v[48:51], v[128:131], v[160:163], v[48:51]\n" \
"v_add_u32 v185, s91, v185\n" \
"v_add_u32 v186, s91, v186\n" \
"ds_read_b128 v[176:179], v240 offset:0\n" \
"v_mfma_f32_16x16x32_f16 v[60:63], v[132:135], v[160:163], v[60:63]\n" \
"v_pk_add_f16 v160, v160, v164\n" \
"v_pk_add_f16 v161, v161, v165\n" \
"v_add_u32 v187, s91, v187\n" \
"v_and_b32 v184, s92, v184\n" \
"s_add_i32 s82, s51, 0x7800000\n" \
"v_mfma_f32_16x16x32_f16 v[52:55], v[144:147], v[164:167], v[52:55]\n" \
"v_pk_add_f16 v162, v162, v166\n" \
"v_pk_add_f16 v163, v163, v167\n" \
"v_and_b32 v185, s92, v185\n" \
"v_and_b32 v186, s92, v186\n" \
"v_and_b32 v187, s92, v187\n" \
"v_mfma_f32_16x16x32_f16 v[64:67], v[148:151], v[164:167], v[64:67]\n" \
"ds_read_b128 v[180:183], v240 offset:512\n" \
"buffer_store_dwordx4 v[184:187], v234, s[36:39], s82 offen sc1\n" \
"ds_read_b64_tr_b16 v[152:153], v241 offset:512\n" \
"v_mfma_f32_16x16x32_f16 v[56:59], v[136:139], v[160:163], v[56:59]\n" \
"buffer_load_dwordx4 v232, s[36:39], s80 offen offset:2048 sc1 lds\n" \
"ds_read_b64_tr_b16 v[154:155], v241 offset:8704\n" \
"v_mfma_f32_16x16x32_f16 v[68:71], v[140:143], v[160:163], v[68:71]\n" \
"s_setprio 0\n" \
"ds_read_b64_tr_b16 v[184:185], v238 offset:40960\n" \
"ds_read_b64_tr_b16 v[188:189], v239 offset:40960\n" \
"s_waitcnt lgkmcnt(5)\n" \
"v_mfma_f32_16x16x32_f16 v[72:75], v[128:131], v[168:171], v[72:75]\n" \
"ds_read_b128 v[160:163], v240 offset:2048\n" \
"ds_read_b64_tr_b16 v[156:157], v242 offset:512\n" \
"v_mfma_f32_16x16x32_f16 v[84:87], v[132:135], v[168:171], v[84:87]\n" \
"v_pk_add_f16 v168, v168, v172\n" \
"v_pk_add_f16 v169, v169, v173\n" \
"v_mfma_f32_16x16x32_f16 v[76:79], v[144:147], v[172:175], v[76:79]\n" \
"v_pk_add_f16 v170, v170, v174\n" \
"v_pk_add_f16 v171, v171, v175\n" \
"s_waitcnt lgkmcnt(3)\n" \
"v_mfma_f32_16x16x16_bf16 v[184:187], v[184:185], v[192:193], 0\n" \
"v_mfma_f32_16x16x32_f16 v[88:91], v[148:151], v[172:175], v[88:91]\n" \
"ds_read_b128 v[164:167], v240 offset:2560\n" \
"ds_read_b64_tr_b16 v[158:159], v242 offset:8704\n" \
"s_waitcnt lgkmcnt(4)\n" \
"v_mfma_f32_16x16x16_bf16 v[188:191], v[188:189], v[192:193], 0\n" \
"v_mfma_f32_16x16x32_f16 v[80:83], v[136:139], v[168:171], v[80:83]\n" \
"buffer_load_dwordx4 v232, s[36:39], s80 offen offset:3072 sc1 lds\n" \
"v_mfma_f32_16x16x32_f16 v[92:95], v[140:143], v[168:171], v[92:95]\n" \
"v_mfma_f32_16x16x32_f16 v[96:99], v[128:131], v[176:179], v[96:99]\n" \
"v_pk_add_f16 v128, v120, v152\n" \
"v_pk_add_f16 v129, v121, v153\n" \
"v_cvt_pk_f16_f32 v184, v184, v185\n" \
"v_cvt_pk_f16_f32 v185, v186, v187\n" \
"ds_read_b128 v[168:171], v240 offset:4096\n" \
"v_mfma_f32_16x16x32_f16 v[108:111], v[132:135], v[176:179], v[108:111]\n" \
"v_pk_add_f16 v176, v176, v180\n" \
"v_pk_add_f16 v177, v177, v181\n" \
"v_cvt_pk_f16_f32 v186, v188, v189\n" \
"v_cvt_pk_f16_f32 v187, v190, v191\n" \
"v_add_u32 v184, s91, v184\n" \
"v_add_u32 v185, s91, v185\n" \
"v_add_u32 v186, s91, v186\n" \
"ds_read_b128 v[172:175], v240 offset:4608\n" \
"v_mfma_f32_16x16x32_f16 v[100:103], v[144:147], v[180:183], v[100:103]\n" \
"v_pk_add_f16 v178, v178, v182\n" \
"v_pk_add_f16 v179, v179, v183\n" \
"s_waitcnt vmcnt(5)\n" \
"v_cmp_eq_u32 vcc, 0, v253\n" \
"s_cbranch_vccnz .Ls110_%=\n" \
".Lr110_%=:\n" \
"s_add_i32 m0, s45, 0xa000\n" \
"s_add_i32 s81, s47, 0x5800000\n" \
"v_mfma_f32_16x16x32_f16 v[112:115], v[148:151], v[180:183], v[112:115]\n" \
"v_pk_add_f16 v130, v122, v154\n" \
"v_pk_add_f16 v131, v123, v155\n" \
"v_add_u32 v187, s91, v187\n" \
"v_and_b32 v184, s92, v184\n" \
"buffer_load_dwordx4 v232, s[36:39], s81 offen sc1 lds\n" \
"v_mfma_f32_16x16x32_f16 v[104:107], v[136:139], v[176:179], v[104:107]\n" \
"s_waitcnt lgkmcnt(1)\n" \
"v_pk_add_f16 v132, v124, v156\n" \
"v_pk_add_f16 v133, v125, v157\n" \
"v_and_b32 v185, s92, v185\n" \
"v_and_b32 v186, s92, v186\n" \
"v_and_b32 v187, s92, v187\n" \
"v_mfma_f32_16x16x32_f16 v[116:119], v[140:143], v[176:179], v[116:119]\n" \
"s_setprio 1\n" \
"v_pk_add_f16 v134, v126, v158\n" \
"v_pk_add_f16 v135, v127, v159\n" \
"buffer_store_dwordx4 v[184:187], v234, s[36:39], s82 offen offset:1024 sc1\n" \
"s_waitcnt vmcnt(7) lgkmcnt(0)\n" \
"s_barrier\n" \
"v_mfma_f32_16x16x32_f16 v[0:3], v[120:123], v[160:163], v[0:3]\n" \
"ds_read_b128 v[176:179], v240 offset:26624\n" \
"v_mfma_f32_16x16x32_f16 v[12:15], v[124:127], v[160:163], v[12:15]\n" \
"v_pk_add_f16 v160, v160, v164\n" \
"v_pk_add_f16 v161, v161, v165\n" \
"v_mfma_f32_16x16x32_f16 v[4:7], v[152:155], v[164:167], v[4:7]\n" \
"v_pk_add_f16 v162, v162, v166\n" \
"v_pk_add_f16 v163, v163, v167\n" \
"v_mfma_f32_16x16x32_f16 v[16:19], v[156:159], v[164:167], v[16:19]\n" \
"ds_read_b128 v[180:183], v240 offset:27136\n" \
"s_add_i32 m0, s44, 0x0\n" \
"s_add_i32 s80, s46, 0x5c00000\n" \
"v_mfma_f32_16x16x32_f16 v[8:11], v[128:131], v[160:163], v[8:11]\n" \
"buffer_load_dwordx4 v232, s[36:39], s80 offen sc1 lds\n" \
"v_mfma_f32_16x16x32_f16 v[20:23], v[132:135], v[160:163], v[20:23]\n" \
"ds_read_b64_tr_b16 v[136:137], v241 offset:16384\n" \
"ds_read_b64_tr_b16 v[138:139], v241 offset:24576\n" \
"v_mfma_f32_16x16x32_f16 v[24:27], v[120:123], v[168:171], v[24:27]\n" \
"ds_read_b128 v[160:163], v240 offset:28672\n" \
"v_mfma_f32_16x16x32_f16 v[36:39], v[124:127], v[168:171], v[36:39]\n" \
"v_pk_add_f16 v168, v168, v172\n" \
"v_pk_add_f16 v169, v169, v173\n" \
"v_mfma_f32_16x16x32_f16 v[28:31], v[152:155], v[172:175], v[28:31]\n" \
"v_pk_add_f16 v170, v170, v174\n" \
"v_pk_add_f16 v171, v171, v175\n" \
"v_mfma_f32_16x16x32_f16 v[40:43], v[156:159], v[172:175], v[40:43]\n" \
"ds_read_b128 v[164:167], v240 offset:29184\n" \
"ds_read_b64_tr_b16 v[140:141], v242 offset:16384\n" \
"v_mfma_f32_16x16x32_f16 v[32:35], v[128:131], v[168:171], v[32:35]\n" \
"buffer_load_dwordx4 v232, s[36:39], s80 offen offset:1024 sc1 lds\n" \
"v_mfma_f32_16x16x32_f16 v[44:47], v[132:135], v[168:171], v[44:47]\n" \
"ds_read_b64_tr_b16 v[142:143], v242 offset:24576\n" \
"s_waitcnt lgkmcnt(7)\n" \
"v_mfma_f32_16x16x32_f16 v[48:51], v[120:123], v[176:179], v[48:51]\n" \
"ds_read_b128 v[168:171], v240 offset:20480\n" \
"ds_read_b64_tr_b16 v[144:145], v241 offset:16896\n" \
"v_mfma_f32_16x16x32_f16 v[60:63], v[124:127], v[176:179], v[60:63]\n" \
"s_waitcnt lgkmcnt(8)\n" \
"v_pk_add_f16 v176, v176, v180\n" \
"v_pk_add_f16 v177, v177, v181\n" \
"v_mfma_f32_16x16x32_f16 v[52:55], v[152:155], v[180:183], v[52:55]\n" \
"v_pk_add_f16 v178, v178, v182\n" \
"v_pk_add_f16 v179, v179, v183\n" \
"v_mfma_f32_16x16x32_f16 v[64:67], v[156:159], v[180:183], v[64:67]\n" \
"ds_read_b128 v[172:175], v240 offset:20992\n" \
"v_mfma_f32_16x16x32_f16 v[56:59], v[128:131], v[176:179], v[56:59]\n" \
"buffer_load_dwordx4 v232, s[36:39], s80 offen offset:2048 sc1 lds\n" \
"v_mfma_f32_16x16x32_f16 v[68:71], v[132:135], v[176:179], v[68:71]\n" \
"s_setprio 0\n" \
"ds_read_b64_tr_b16 v[146:147], v241 offset:25088\n" \
"ds_read_b64_tr_b16 v[148:149], v242 offset:16896\n" \
"s_waitcnt lgkmcnt(6)\n" \
"v_mfma_f32_16x16x32_f16 v[72:75], v[120:123], v[160:163], v[72:75]\n" \
"ds_read_b128 v[176:179], v240 offset:22528\n" \
"ds_read_b64_tr_b16 v[150:151], v242 offset:25088\n" \
"v_mfma_f32_16x16x32_f16 v[84:87], v[124:127], v[160:163], v[84:87]\n" \
"v_pk_add_f16 v160, v160, v164\n" \
"v_pk_add_f16 v161, v161, v165\n" \
"v_mfma_f32_16x16x32_f16 v[76:79], v[152:155], v[164:167], v[76:79]\n" \
"v_pk_add_f16 v162, v162, v166\n" \
"v_pk_add_f16 v163, v163, v167\n" \
"v_mfma_f32_16x16x32_f16 v[88:91], v[156:159], v[164:167], v[88:91]\n" \
"ds_read_b128 v[180:183], v240 offset:23040\n" \
"v_mfma_f32_16x16x32_f16 v[80:83], v[128:131], v[160:163], v[80:83]\n" \
"buffer_load_dwordx4 v232, s[36:39], s80 offen offset:3072 sc1 lds\n" \
"v_mfma_f32_16x16x32_f16 v[92:95], v[132:135], v[160:163], v[92:95]\n" \
"s_waitcnt lgkmcnt(6)\n" \
"v_mfma_f32_16x16x32_f16 v[96:99], v[120:123], v[168:171], v[96:99]\n" \
"v_pk_add_f16 v120, v136, v144\n" \
"v_pk_add_f16 v121, v137, v145\n" \
"ds_read_b128 v[160:163], v240 offset:24576\n" \
"v_mfma_f32_16x16x32_f16 v[108:111], v[124:127], v[168:171], v[108:111]\n" \
"s_waitcnt lgkmcnt(6)\n" \
"v_pk_add_f16 v168, v168, v172\n" \
"v_pk_add_f16 v169, v169, v173\n" \
"ds_read_b128 v[164:167], v240 offset:25088\n" \
"v_mfma_f32_16x16x32_f16 v[100:103], v[152:155], v[172:175], v[100:103]\n" \
"v_pk_add_f16 v170, v170, v174\n" \
"v_pk_add_f16 v171, v171, v175\n" \
"s_add_i32 m0, s45, 0x0\n" \
"s_add_i32 s81, s47, 0x5c00000\n" \
"v_mfma_f32_16x16x32_f16 v[112:115], v[156:159], v[172:175], v[112:115]\n" \
"s_waitcnt lgkmcnt(3)\n" \
"v_pk_add_f16 v122, v138, v146\n" \
"v_pk_add_f16 v123, v139, v147\n" \
"buffer_load_dwordx4 v232, s[36:39], s81 offen sc1 lds\n" \
"v_mfma_f32_16x16x32_f16 v[104:107], v[128:131], v[168:171], v[104:107]\n" \
"v_pk_add_f16 v124, v140, v148\n" \
"v_pk_add_f16 v125, v141, v149\n" \
"v_mfma_f32_16x16x32_f16 v[116:119], v[132:135], v[168:171], v[116:119]\n" \
"s_setprio 1\n" \
"v_pk_add_f16 v126, v142, v150\n" \
"v_pk_add_f16 v127, v143, v151\n" \
"s_waitcnt vmcnt(5) lgkmcnt(0)\n" \
"s_barrier\n" \
"v_mfma_f32_16x16x32_f16 v[0:3], v[136:139], v[176:179], v[0:3]\n" \
"ds_read_b128 v[168:171], v240 offset:47104\n" \
"v_mfma_f32_16x16x32_f16 v[12:15], v[140:143], v[176:179], v[12:15]\n" \
"v_pk_add_f16 v176, v176, v180\n" \
"v_pk_add_f16 v177, v177, v181\n" \
"v_mfma_f32_16x16x32_f16 v[4:7], v[144:147], v[180:183], v[4:7]\n" \
"v_pk_add_f16 v178, v178, v182\n" \
"v_pk_add_f16 v179, v179, v183\n" \
"v_mfma_f32_16x16x32_f16 v[16:19], v[148:151], v[180:183], v[16:19]\n" \
"ds_read_b128 v[172:175], v240 offset:47616\n" \
"s_add_i32 m0, s44, 0x5000\n" \
"s_add_i32 s80, s46, 0x6000000\n" \
"v_mfma_f32_16x16x32_f16 v[8:11], v[120:123], v[176:179], v[8:11]\n" \
"buffer_load_dwordx4 v232, s[36:39], s80 offen sc1 lds\n" \
"v_mfma_f32_16x16x32_f16 v[20:23], v[124:127], v[176:179], v[20:23]\n" \
"ds_read_b64_tr_b16 v[128:129], v241 offset:32768\n" \
"ds_read_b64_tr_b16 v[130:131], v241 offset:40960\n" \
"v_mfma_f32_16x16x32_f16 v[24:27], v[136:139], v[160:163], v[24:27]\n" \
"ds_read_b128 v[176:179], v240 offset:49152\n" \
"v_mfma_f32_16x16x32_f16 v[36:39], v[140:143], v[160:163], v[36:39]\n" \
"v_pk_add_f16 v160, v160, v164\n" \
"v_pk_add_f16 v161, v161, v165\n" \
"v_mfma_f32_16x16x32_f16 v[28:31], v[144:147], v[164:167], v[28:31]\n" \
"v_pk_add_f16 v162, v162, v166\n" \
"v_pk_add_f16 v163, v163, v167\n" \
"v_mfma_f32_16x16x32_f16 v[40:43], v[148:151], v[164:167], v[40:43]\n" \
"ds_read_b128 v[180:183], v240 offset:49664\n" \
"ds_read_b64_tr_b16 v[132:133], v242 offset:32768\n" \
"v_mfma_f32_16x16x32_f16 v[32:35], v[120:123], v[160:163], v[32:35]\n" \
"buffer_load_dwordx4 v232, s[36:39], s80 offen offset:1024 sc1 lds\n" \
"v_mfma_f32_16x16x32_f16 v[44:47], v[124:127], v[160:163], v[44:47]\n" \
"ds_read_b64_tr_b16 v[134:135], v242 offset:40960\n" \
"s_waitcnt lgkmcnt(7)\n" \
"v_mfma_f32_16x16x32_f16 v[48:51], v[136:139], v[168:171], v[48:51]\n" \
"ds_read_b128 v[160:163], v240 offset:40960\n" \
"ds_read_b64_tr_b16 v[152:153], v241 offset:33280\n" \
"v_mfma_f32_16x16x32_f16 v[60:63], v[140:143], v[168:171], v[60:63]\n" \
"s_waitcnt lgkmcnt(8)\n" \
"v_pk_add_f16 v168, v168, v172\n" \
"v_pk_add_f16 v169, v169, v173\n" \
"v_mfma_f32_16x16x32_f16 v[52:55], v[144:147], v[172:175], v[52:55]\n" \
"v_pk_add_f16 v170, v170, v174\n" \
"v_pk_add_f16 v171, v171, v175\n" \
"v_mfma_f32_16x16x32_f16 v[64:67], v[148:151], v[172:175], v[64:67]\n" \
"ds_read_b128 v[164:167], v240 offset:41472\n" \
"v_mfma_f32_16x16x32_f16 v[56:59], v[120:123], v[168:171], v[56:59]\n" \
"buffer_load_dwordx4 v232, s[36:39], s80 offen offset:2048 sc1 lds\n" \
"v_mfma_f32_16x16x32_f16 v[68:71], v[124:127], v[168:171], v[68:71]\n" \
"s_setprio 0\n" \
"ds_read_b64_tr_b16 v[154:155], v241 offset:41472\n" \
"ds_read_b64_tr_b16 v[156:157], v242 offset:33280\n" \
"s_waitcnt lgkmcnt(6)\n" \
"v_mfma_f32_16x16x32_f16 v[72:75], v[136:139], v[176:179], v[72:75]\n" \
"ds_read_b128 v[168:171], v240 offset:43008\n" \
"ds_read_b64_tr_b16 v[158:159], v242 offset:41472\n" \
"v_mfma_f32_16x16x32_f16 v[84:87], v[140:143], v[176:179], v[84:87]\n" \
"v_pk_add_f16 v176, v176, v180\n" \
"v_pk_add_f16 v177, v177, v181\n" \
"v_mfma_f32_16x16x32_f16 v[76:79], v[144:147], v[180:183], v[76:79]\n" \
"v_pk_add_f16 v178, v178, v182\n" \
"v_pk_add_f16 v179, v179, v183\n" \
"v_mfma_f32_16x16x32_f16 v[88:91], v[148:151], v[180:183], v[88:91]\n" \
"ds_read_b128 v[172:175], v240 offset:43520\n" \
"v_mfma_f32_16x16x32_f16 v[80:83], v[120:123], v[176:179], v[80:83]\n" \
"buffer_load_dwordx4 v232, s[36:39], s80 offen offset:3072 sc1 lds\n" \
"v_mfma_f32_16x16x32_f16 v[92:95], v[124:127], v[176:179], v[92:95]\n" \
"s_waitcnt lgkmcnt(6)\n" \
"v_mfma_f32_16x16x32_f16 v[96:99], v[136:139], v[160:163], v[96:99]\n" \
"v_pk_add_f16 v136, v128, v152\n" \
"v_pk_add_f16 v137, v129, v153\n" \
"ds_read_b128 v[176:179], v240 offset:45056\n" \
"v_mfma_f32_16x16x32_f16 v[108:111], v[140:143], v[160:163], v[108:111]\n" \
"s_waitcnt lgkmcnt(6)\n" \
"v_pk_add_f16 v160, v160, v164\n" \
"v_pk_add_f16 v161, v161, v165\n" \
"ds_read_b128 v[180:183], v240 offset:45568\n" \
"v_mfma_f32_16x16x32_f16 v[100:103], v[144:147], v[164:167], v[100:103]\n" \
"v_pk_add_f16 v162, v162, v166\n" \
"v_pk_add_f16 v163, v163, v167\n" \
"s_add_i32 m0, s45, 0x5000\n" \
"s_add_i32 s81, s47, 0x6000000\n" \
"v_mfma_f32_16x16x32_f16 v[112:115], v[148:151], v[164:167], v[112:115]\n" \
"s_waitcnt lgkmcnt(3)\n" \
"v_pk_add_f16 v138, v130, v154\n" \
"v_pk_add_f16 v139, v131, v155\n" \
"buffer_load_dwordx4 v232, s[36:39], s81 offen sc1 lds\n" \
"v_mfma_f32_16x16x32_f16 v[104:107], v[120:123], v[160:163], v[104:107]\n" \
"v_pk_add_f16 v140, v132, v156\n" \
"v_pk_add_f16 v141, v133, v157\n" \
"v_mfma_f32_16x16x32_f16 v[116:119], v[124:127], v[160:163], v[116:119]\n" \
"s_setprio 1\n" \
"v_pk_add_f16 v142, v134, v158\n" \
"v_pk_add_f16 v143, v135, v159\n" \
"s_waitcnt vmcnt(5) lgkmcnt(0)\n" \
"s_barrier\n" \
"v_mfma_f32_16x16x32_f16 v[0:3], v[128:131], v[168:171], v[0:3]\n" \
"ds_read_b128 v[160:163], v240 offset:6144\n" \
"v_mfma_f32_16x16x32_f16 v[12:15], v[132:135], v[168:171], v[12:15]\n" \
"v_pk_add_f16 v168, v168, v172\n" \
"v_pk_add_f16 v169, v169, v173\n" \
"v_mfma_f32_16x16x32_f16 v[4:7], v[152:155], v[172:175], v[4:7]\n" \
"v_pk_add_f16 v170, v170, v174\n" \
"v_pk_add_f16 v171, v171, v175\n" \
"v_mfma_f32_16x16x32_f16 v[16:19], v[156:159], v[172:175], v[16:19]\n" \
"ds_read_b128 v[164:167], v240 offset:6656\n" \
"s_add_i32 m0, s44, 0xa000\n" \
"s_add_i32 s80, s46, 0x6400000\n" \
"v_mfma_f32_16x16x32_f16 v[8:11], v[136:139], v[168:171], v[8:11]\n" \
"buffer_load_dwordx4 v232, s[36:39], s80 offen sc1 lds\n" \
"v_mfma_f32_16x16x32_f16 v[20:23], v[140:143], v[168:171], v[20:23]\n" \
"v_lshlrev_b32 v254, 3, v244\n" \
"ds_read_b64_tr_b16 v[120:121], v241 offset:0\n" \
"ds_read_b64_tr_b16 v[122:123], v241 offset:8192\n" \
"v_mfma_f32_16x16x32_f16 v[24:27], v[128:131], v[176:179], v[24:27]\n" \
"ds_read_b128 v[168:171], v240 offset:8192\n" \
"v_mfma_f32_16x16x32_f16 v[36:39], v[132:135], v[176:179], v[36:39]\n" \
"v_pk_add_f16 v176, v176, v180\n" \
"v_pk_add_f16 v177, v177, v181\n" \
"v_mfma_f32_16x16x32_f16 v[28:31], v[152:155], v[180:183], v[28:31]\n" \
"v_pk_add_f16 v178, v178, v182\n" \
"v_pk_add_f16 v179, v179, v183\n" \
"v_mfma_f32_16x16x32_f16 v[40:43], v[156:159], v[180:183], v[40:43]\n" \
"ds_read_b128 v[172:175], v240 offset:8704\n" \
"ds_read_b64_tr_b16 v[124:125], v242 offset:0\n" \
"v_mfma_f32_16x16x32_f16 v[32:35], v[136:139], v[176:179], v[32:35]\n" \
"buffer_load_dwordx4 v232, s[36:39], s80 offen offset:1024 sc1 lds\n" \
"v_mfma_f32_16x16x32_f16 v[44:47], v[140:143], v[176:179], v[44:47]\n" \
"ds_read_b64_tr_b16 v[126:127], v242 offset:8192\n" \
"s_mov_b64 exec, s[58:59]\n" \
"global_load_dword v255, v254, s[78:79]\n" \
"s_mov_b64 exec, -1\n" \
"s_waitcnt lgkmcnt(6)\n" \
"v_mfma_f32_16x16x32_f16 v[48:51], v[128:131], v[160:163], v[48:51]\n" \
"ds_read_b128 v[176:179], v240 offset:0\n" \
"ds_read_b64_tr_b16 v[144:145], v241 offset:512\n" \
"v_mfma_f32_16x16x32_f16 v[60:63], v[132:135], v[160:163], v[60:63]\n" \
"v_pk_add_f16 v160, v160, v164\n" \
"v_pk_add_f16 v161, v161, v165\n" \
"v_mfma_f32_16x16x32_f16 v[52:55], v[152:155], v[164:167], v[52:55]\n" \
"v_pk_add_f16 v162, v162, v166\n" \
"v_pk_add_f16 v163, v163, v167\n" \
"v_mfma_f32_16x16x32_f16 v[64:67], v[156:159], v[164:167], v[64:67]\n" \
"ds_read_b128 v[180:183], v240 offset:512\n" \
"v_mfma_f32_16x16x32_f16 v[56:59], v[136:139], v[160:163], v[56:59]\n" \
"buffer_load_dwordx4 v232, s[36:39], s80 offen offset:2048 sc1 lds\n" \
"v_mfma_f32_16x16x32_f16 v[68:71], v[140:143], v[160:163], v[68:71]\n" \
"s_setprio 0\n" \
"ds_read_b64_tr_b16 v[146:147], v241 offset:8704\n" \
"ds_read_b64_tr_b16 v[148:149], v242 offset:512\n" \
"s_waitcnt lgkmcnt(6)\n" \
"v_mfma_f32_16x16x32_f16 v[72:75], v[128:131], v[168:171], v[72:75]\n" \
"ds_read_b128 v[160:163], v240 offset:2048\n" \
"ds_read_b64_tr_b16 v[150:151], v242 offset:8704\n" \
"v_mfma_f32_16x16x32_f16 v[84:87], v[132:135], v[168:171], v[84:87]\n" \
"v_pk_add_f16 v168, v168, v172\n" \
"v_pk_add_f16 v169, v169, v173\n" \
"v_mfma_f32_16x16x32_f16 v[76:79], v[152:155], v[172:175], v[76:79]\n" \
"v_pk_add_f16 v170, v170, v174\n" \
"v_pk_add_f16 v171, v171, v175\n" \
"v_mfma_f32_16x16x32_f16 v[88:91], v[156:159], v[172:175], v[88:91]\n" \
"ds_read_b128 v[164:167], v240 offset:2560\n" \
"v_mfma_f32_16x16x32_f16 v[80:83], v[136:139], v[168:171], v[80:83]\n" \
"buffer_load_dwordx4 v232, s[36:39], s80 offen offset:3072 sc1 lds\n" \
"v_mfma_f32_16x16x32_f16 v[92:95], v[140:143], v[168:171], v[92:95]\n" \
"s_waitcnt lgkmcnt(6)\n" \
"v_mfma_f32_16x16x32_f16 v[96:99], v[128:131], v[176:179], v[96:99]\n" \
"v_pk_add_f16 v128, v120, v144\n" \
"v_pk_add_f16 v129, v121, v145\n" \
"ds_read_b128 v[168:171], v240 offset:4096\n" \
"v_mfma_f32_16x16x32_f16 v[108:111], v[132:135], v[176:179], v[108:111]\n" \
"s_waitcnt lgkmcnt(6)\n" \
"v_pk_add_f16 v176, v176, v180\n" \
"v_pk_add_f16 v177, v177, v181\n" \
"ds_read_b128 v[172:175], v240 offset:4608\n" \
"v_mfma_f32_16x16x32_f16 v[100:103], v[152:155], v[180:183], v[100:103]\n" \
"v_pk_add_f16 v178, v178, v182\n" \
"v_pk_add_f16 v179, v179, v183\n" \
"s_add_i32 m0, s45, 0xa000\n" \
"s_add_i32 s81, s47, 0x6400000\n" \
"v_mfma_f32_16x16x32_f16 v[112:115], v[156:159], v[180:183], v[112:115]\n" \
"s_waitcnt lgkmcnt(3)\n" \
"v_pk_add_f16 v130, v122, v146\n" \
"v_pk_add_f16 v131, v123, v147\n" \
"buffer_load_dwordx4 v232, s[36:39], s81 offen sc1 lds\n" \
"v_mfma_f32_16x16x32_f16 v[104:107], v[136:139], v[176:179], v[104:107]\n" \
"v_pk_add_f16 v132, v124, v148\n" \
"v_pk_add_f16 v133, v125, v149\n" \
"v_mfma_f32_16x16x32_f16 v[116:119], v[140:143], v[176:179], v[116:119]\n" \
"s_setprio 1\n" \
"v_pk_add_f16 v134, v126, v150\n" \
"v_pk_add_f16 v135, v127, v151\n" \
"s_waitcnt vmcnt(6) lgkmcnt(0)\n" \
"s_barrier\n" \
"v_mfma_f32_16x16x32_f16 v[0:3], v[120:123], v[160:163], v[0:3]\n" \
"ds_read_b128 v[176:179], v240 offset:26624\n" \
"v_mfma_f32_16x16x32_f16 v[12:15], v[124:127], v[160:163], v[12:15]\n" \
"v_pk_add_f16 v160, v160, v164\n" \
"v_pk_add_f16 v161, v161, v165\n" \
"v_mfma_f32_16x16x32_f16 v[4:7], v[144:147], v[164:167], v[4:7]\n" \
"v_pk_add_f16 v162, v162, v166\n" \
"v_pk_add_f16 v163, v163, v167\n" \
"v_mfma_f32_16x16x32_f16 v[16:19], v[148:151], v[164:167], v[16:19]\n" \
"ds_read_b128 v[180:183], v240 offset:27136\n" \
"s_add_i32 m0, s44, 0x0\n" \
"s_add_i32 s80, s46, 0x6800000\n" \
"v_mfma_f32_16x16x32_f16 v[8:11], v[128:131], v[160:163], v[8:11]\n" \
"buffer_load_dwordx4 v232, s[36:39], s80 offen sc1 lds\n" \
"v_mfma_f32_16x16x32_f16 v[20:23], v[132:135], v[160:163], v[20:23]\n" \
"ds_read_b64_tr_b16 v[136:137], v241 offset:16384\n" \
"ds_read_b64_tr_b16 v[138:139], v241 offset:24576\n" \
"v_mfma_f32_16x16x32_f16 v[24:27], v[120:123], v[168:171], v[24:27]\n" \
"ds_read_b128 v[160:163], v240 offset:28672\n" \
"v_mfma_f32_16x16x32_f16 v[36:39], v[124:127], v[168:171], v[36:39]\n" \
"v_pk_add_f16 v168, v168, v172\n" \
"v_pk_add_f16 v169, v169, v173\n" \
"v_mfma_f32_16x16x32_f16 v[28:31], v[144:147], v[172:175], v[28:31]\n" \
"v_pk_add_f16 v170, v170, v174\n" \
"v_pk_add_f16 v171, v171, v175\n" \
"v_mfma_f32_16x16x32_f16 v[40:43], v[148:151], v[172:175], v[40:43]\n" \
"ds_read_b128 v[164:167], v240 offset:29184\n" \
"ds_read_b64_tr_b16 v[140:141], v242 offset:16384\n" \
"v_mfma_f32_16x16x32_f16 v[32:35], v[128:131], v[168:171], v[32:35]\n" \
"buffer_load_dwordx4 v232, s[36:39], s80 offen offset:1024 sc1 lds\n" \
"v_mfma_f32_16x16x32_f16 v[44:47], v[132:135], v[168:171], v[44:47]\n" \
"ds_read_b64_tr_b16 v[142:143], v242 offset:24576\n" \
"s_waitcnt lgkmcnt(7)\n" \
"v_mfma_f32_16x16x32_f16 v[48:51], v[120:123], v[176:179], v[48:51]\n" \
"ds_read_b128 v[168:171], v240 offset:20480\n" \
"ds_read_b64_tr_b16 v[152:153], v241 offset:16896\n" \
"v_mfma_f32_16x16x32_f16 v[60:63], v[124:127], v[176:179], v[60:63]\n" \
"s_waitcnt lgkmcnt(8)\n" \
"v_pk_add_f16 v176, v176, v180\n" \
"v_pk_add_f16 v177, v177, v181\n" \
"v_mfma_f32_16x16x32_f16 v[52:55], v[144:147], v[180:183], v[52:55]\n" \
"v_pk_add_f16 v178, v178, v182\n" \
"v_pk_add_f16 v179, v179, v183\n" \
"v_mfma_f32_16x16x32_f16 v[64:67], v[148:151], v[180:183], v[64:67]\n" \
"ds_read_b128 v[172:175], v240 offset:20992\n" \
"v_mfma_f32_16x16x32_f16 v[56:59], v[128:131], v[176:179], v[56:59]\n" \
"buffer_load_dwordx4 v232, s[36:39], s80 offen offset:2048 sc1 lds\n" \
"v_mfma_f32_16x16x32_f16 v[68:71], v[132:135], v[176:179], v[68:71]\n" \
"s_setprio 0\n" \
"ds_read_b64_tr_b16 v[154:155], v241 offset:25088\n" \
"ds_read_b64_tr_b16 v[156:157], v242 offset:16896\n" \
"s_waitcnt lgkmcnt(6)\n" \
"v_mfma_f32_16x16x32_f16 v[72:75], v[120:123], v[160:163], v[72:75]\n" \
"ds_read_b128 v[176:179], v240 offset:22528\n" \
"ds_read_b64_tr_b16 v[158:159], v242 offset:25088\n" \
"v_mfma_f32_16x16x32_f16 v[84:87], v[124:127], v[160:163], v[84:87]\n" \
"v_pk_add_f16 v160, v160, v164\n" \
"v_pk_add_f16 v161, v161, v165\n" \
"v_mfma_f32_16x16x32_f16 v[76:79], v[144:147], v[164:167], v[76:79]\n" \
"v_pk_add_f16 v162, v162, v166\n" \
"v_pk_add_f16 v163, v163, v167\n" \
"v_mfma_f32_16x16x32_f16 v[88:91], v[148:151], v[164:167], v[88:91]\n" \
"ds_read_b128 v[180:183], v240 offset:23040\n" \
"v_mfma_f32_16x16x32_f16 v[80:83], v[128:131], v[160:163], v[80:83]\n" \
"buffer_load_dwordx4 v232, s[36:39], s80 offen offset:3072 sc1 lds\n" \
"v_mfma_f32_16x16x32_f16 v[92:95], v[132:135], v[160:163], v[92:95]\n" \
"s_waitcnt lgkmcnt(6)\n" \
"v_mfma_f32_16x16x32_f16 v[96:99], v[120:123], v[168:171], v[96:99]\n" \
"v_pk_add_f16 v120, v136, v152\n" \
"v_pk_add_f16 v121, v137, v153\n" \
"ds_read_b128 v[160:163], v240 offset:24576\n" \
"v_mfma_f32_16x16x32_f16 v[108:111], v[124:127], v[168:171], v[108:111]\n" \
"s_waitcnt lgkmcnt(6)\n" \
"v_pk_add_f16 v168, v168, v172\n" \
"v_pk_add_f16 v169, v169, v173\n" \
"ds_read_b128 v[164:167], v240 offset:25088\n" \
"v_mfma_f32_16x16x32_f16 v[100:103], v[144:147], v[172:175], v[100:103]\n" \
"v_pk_add_f16 v170, v170, v174\n" \
"v_pk_add_f16 v171, v171, v175\n" \
"s_add_i32 m0, s45, 0x0\n" \
"s_add_i32 s81, s47, 0x6800000\n" \
"v_mfma_f32_16x16x32_f16 v[112:115], v[148:151], v[172:175], v[112:115]\n" \
"s_waitcnt lgkmcnt(3)\n" \
"v_pk_add_f16 v122, v138, v154\n" \
"v_pk_add_f16 v123, v139, v155\n" \
"buffer_load_dwordx4 v232, s[36:39], s81 offen sc1 lds\n" \
"v_mfma_f32_16x16x32_f16 v[104:107], v[128:131], v[168:171], v[104:107]\n" \
"v_pk_add_f16 v124, v140, v156\n" \
"v_pk_add_f16 v125, v141, v157\n" \
"v_mfma_f32_16x16x32_f16 v[116:119], v[132:135], v[168:171], v[116:119]\n" \
"s_setprio 1\n" \
"v_pk_add_f16 v126, v142, v158\n" \
"v_pk_add_f16 v127, v143, v159\n" \
"s_waitcnt vmcnt(5) lgkmcnt(0)\n" \
"s_barrier\n" \
"v_mfma_f32_16x16x32_f16 v[0:3], v[136:139], v[176:179], v[0:3]\n" \
"ds_read_b128 v[168:171], v240 offset:47104\n" \
"v_mfma_f32_16x16x32_f16 v[12:15], v[140:143], v[176:179], v[12:15]\n" \
"v_pk_add_f16 v176, v176, v180\n" \
"v_pk_add_f16 v177, v177, v181\n" \
"v_mfma_f32_16x16x32_f16 v[4:7], v[152:155], v[180:183], v[4:7]\n" \
"v_pk_add_f16 v178, v178, v182\n" \
"v_pk_add_f16 v179, v179, v183\n" \
"s_add_i32 m0, s44, 0x5000\n" \
"s_add_i32 s80, s46, 0x6c00000\n" \
"v_mfma_f32_16x16x32_f16 v[16:19], v[156:159], v[180:183], v[16:19]\n" \
"ds_read_b128 v[172:175], v240 offset:47616\n" \
"buffer_load_dwordx4 v232, s[36:39], s80 offen sc1 lds\n" \
"v_mfma_f32_16x16x32_f16 v[8:11], v[120:123], v[176:179], v[8:11]\n" \
"ds_read_b64_tr_b16 v[128:129], v241 offset:32768\n" \
"v_mfma_f32_16x16x32_f16 v[20:23], v[124:127], v[176:179], v[20:23]\n" \
"ds_read_b64_tr_b16 v[130:131], v241 offset:40960\n" \
"v_mfma_f32_16x16x32_f16 v[24:27], v[136:139], v[160:163], v[24:27]\n" \
"ds_read_b128 v[176:179], v240 offset:49152\n" \
"buffer_load_dwordx4 v232, s[36:39], s80 offen offset:1024 sc1 lds\n" \
"v_mfma_f32_16x16x32_f16 v[36:39], v[140:143], v[160:163], v[36:39]\n" \
"v_pk_add_f16 v160, v160, v164\n" \
"v_pk_add_f16 v161, v161, v165\n" \
"v_mfma_f32_16x16x32_f16 v[28:31], v[152:155], v[164:167], v[28:31]\n" \
"v_pk_add_f16 v162, v162, v166\n" \
"v_pk_add_f16 v163, v163, v167\n" \
"v_mfma_f32_16x16x32_f16 v[40:43], v[156:159], v[164:167], v[40:43]\n" \
"ds_read_b128 v[180:183], v240 offset:49664\n" \
"buffer_load_dwordx4 v232, s[36:39], s80 offen offset:2048 sc1 lds\n" \
"v_mfma_f32_16x16x32_f16 v[32:35], v[120:123], v[160:163], v[32:35]\n" \
"ds_read_b64_tr_b16 v[132:133], v242 offset:32768\n" \
"v_mfma_f32_16x16x32_f16 v[44:47], v[124:127], v[160:163], v[44:47]\n" \
"ds_read_b64_tr_b16 v[134:135], v242 offset:40960\n" \
"s_waitcnt lgkmcnt(7)\n" \
"v_mfma_f32_16x16x32_f16 v[48:51], v[136:139], v[168:171], v[48:51]\n" \
"ds_read_b128 v[160:163], v240 offset:40960\n" \
"buffer_load_dwordx4 v232, s[36:39], s80 offen offset:3072 sc1 lds\n" \
"v_mfma_f32_16x16x32_f16 v[60:63], v[140:143], v[168:171], v[60:63]\n" \
"s_waitcnt lgkmcnt(6)\n" \
"v_pk_add_f16 v168, v168, v172\n" \
"v_pk_add_f16 v169, v169, v173\n" \
"ds_read_b64_tr_b16 v[144:145], v241 offset:33280\n" \
"v_mfma_f32_16x16x32_f16 v[52:55], v[152:155], v[172:175], v[52:55]\n" \
"v_pk_add_f16 v170, v170, v174\n" \
"v_pk_add_f16 v171, v171, v175\n" \
"s_add_i32 m0, s45, 0x5000\n" \
"s_add_i32 s81, s47, 0x6c00000\n" \
"v_mfma_f32_16x16x32_f16 v[64:67], v[156:159], v[172:175], v[64:67]\n" \
"ds_read_b128 v[164:167], v240 offset:41472\n" \
"buffer_load_dwordx4 v232, s[36:39], s81 offen sc1 lds\n" \
"v_mfma_f32_16x16x32_f16 v[56:59], v[120:123], v[168:171], v[56:59]\n" \
"ds_read_b64_tr_b16 v[146:147], v241 offset:41472\n" \
"ds_read_b64_tr_b16 v[148:149], v242 offset:33280\n" \
"v_mfma_f32_16x16x32_f16 v[68:71], v[124:127], v[168:171], v[68:71]\n" \
"s_setprio 0\n" \
"s_add_i32 m0, s52, 0x0\n" \
"s_add_i32 s80, s55, 0x0\n" \
"s_waitcnt lgkmcnt(5)\n" \
"v_mfma_f32_16x16x32_f16 v[72:75], v[136:139], v[176:179], v[72:75]\n" \
"ds_read_b128 v[168:171], v240 offset:43008\n" \
"buffer_load_dwordx4 v232, s[36:39], s80 offen sc1 lds\n" \
"v_mfma_f32_16x16x32_f16 v[84:87], v[140:143], v[176:179], v[84:87]\n" \
"v_pk_add_f16 v176, v176, v180\n" \
"v_pk_add_f16 v177, v177, v181\n" \
"ds_read_b64_tr_b16 v[150:151], v242 offset:41472\n" \
"v_mfma_f32_16x16x32_f16 v[76:79], v[152:155], v[180:183], v[76:79]\n" \
"v_pk_add_f16 v178, v178, v182\n" \
"v_pk_add_f16 v179, v179, v183\n" \
"v_mfma_f32_16x16x32_f16 v[88:91], v[156:159], v[180:183], v[88:91]\n" \
"ds_read_b128 v[172:175], v240 offset:43520\n" \
"buffer_load_dwordx4 v232, s[36:39], s80 offen offset:1024 sc1 lds\n" \
"v_mfma_f32_16x16x32_f16 v[80:83], v[120:123], v[176:179], v[80:83]\n" \
"v_mfma_f32_16x16x32_f16 v[92:95], v[124:127], v[176:179], v[92:95]\n" \
"s_waitcnt lgkmcnt(5)\n" \
"v_mfma_f32_16x16x32_f16 v[96:99], v[136:139], v[160:163], v[96:99]\n" \
"v_pk_add_f16 v136, v128, v144\n" \
"v_pk_add_f16 v137, v129, v145\n" \
"ds_read_b128 v[176:179], v240 offset:45056\n" \
"buffer_load_dwordx4 v232, s[36:39], s80 offen offset:2048 sc1 lds\n" \
"v_mfma_f32_16x16x32_f16 v[108:111], v[140:143], v[160:163], v[108:111]\n" \
"v_pk_add_f16 v160, v160, v164\n" \
"v_pk_add_f16 v161, v161, v165\n" \
"ds_read_b128 v[180:183], v240 offset:45568\n" \
"v_mfma_f32_16x16x32_f16 v[100:103], v[152:155], v[164:167], v[100:103]\n" \
"v_pk_add_f16 v162, v162, v166\n" \
"v_pk_add_f16 v163, v163, v167\n" \
"v_mfma_f32_16x16x32_f16 v[112:115], v[156:159], v[164:167], v[112:115]\n" \
"s_waitcnt lgkmcnt(4)\n" \
"v_pk_add_f16 v138, v130, v146\n" \
"v_pk_add_f16 v139, v131, v147\n" \
"buffer_load_dwordx4 v232, s[36:39], s80 offen offset:3072 sc1 lds\n" \
"v_mfma_f32_16x16x32_f16 v[104:107], v[120:123], v[160:163], v[104:107]\n" \
"v_pk_add_f16 v140, v132, v148\n" \
"v_pk_add_f16 v141, v133, v149\n" \
"v_mfma_f32_16x16x32_f16 v[116:119], v[124:127], v[160:163], v[116:119]\n" \
"s_setprio 1\n" \
"s_waitcnt lgkmcnt(2)\n" \
"v_pk_add_f16 v142, v134, v150\n" \
"v_pk_add_f16 v143, v135, v151\n" \
"s_waitcnt vmcnt(9) lgkmcnt(0)\n" \
"s_barrier\n" \
"v_mfma_f32_16x16x32_f16 v[0:3], v[128:131], v[168:171], v[0:3]\n" \
"ds_read_b128 v[160:163], v240 offset:6144\n" \
"v_mfma_f32_16x16x32_f16 v[12:15], v[132:135], v[168:171], v[12:15]\n" \
"v_pk_add_f16 v168, v168, v172\n" \
"v_pk_add_f16 v169, v169, v173\n" \
"v_mfma_f32_16x16x32_f16 v[4:7], v[144:147], v[172:175], v[4:7]\n" \
"v_pk_add_f16 v170, v170, v174\n" \
"v_pk_add_f16 v171, v171, v175\n" \
"v_mfma_f32_16x16x32_f16 v[16:19], v[148:151], v[172:175], v[16:19]\n" \
"ds_read_b128 v[164:167], v240 offset:6656\n" \
"s_add_i32 m0, s44, 0xa000\n" \
"s_add_i32 s80, s46, 0x7000000\n" \
"v_mfma_f32_16x16x32_f16 v[8:11], v[136:139], v[168:171], v[8:11]\n" \
"buffer_load_dwordx4 v232, s[36:39], s80 offen sc1 lds\n" \
"v_mfma_f32_16x16x32_f16 v[20:23], v[140:143], v[168:171], v[20:23]\n" \
"ds_read_b64_tr_b16 v[120:121], v241 offset:0\n" \
"ds_read_b64_tr_b16 v[122:123], v241 offset:8192\n" \
"v_mfma_f32_16x16x32_f16 v[24:27], v[128:131], v[176:179], v[24:27]\n" \
"ds_read_b128 v[168:171], v240 offset:8192\n" \
"v_mfma_f32_16x16x32_f16 v[36:39], v[132:135], v[176:179], v[36:39]\n" \
"v_pk_add_f16 v176, v176, v180\n" \
"v_pk_add_f16 v177, v177, v181\n" \
"v_mfma_f32_16x16x32_f16 v[28:31], v[144:147], v[180:183], v[28:31]\n" \
"v_pk_add_f16 v178, v178, v182\n" \
"v_pk_add_f16 v179, v179, v183\n" \
"v_mfma_f32_16x16x32_f16 v[40:43], v[148:151], v[180:183], v[40:43]\n" \
"ds_read_b128 v[172:175], v240 offset:8704\n" \
"ds_read_b64_tr_b16 v[124:125], v242 offset:0\n" \
"v_mfma_f32_16x16x32_f16 v[32:35], v[136:139], v[176:179], v[32:35]\n" \
"buffer_load_dwordx4 v232, s[36:39], s80 offen offset:1024 sc1 lds\n" \
"v_mfma_f32_16x16x32_f16 v[44:47], v[140:143], v[176:179], v[44:47]\n" \
"ds_read_b64_tr_b16 v[126:127], v242 offset:8192\n" \
"s_waitcnt lgkmcnt(7)\n" \
"v_mfma_f32_16x16x32_f16 v[48:51], v[128:131], v[160:163], v[48:51]\n" \
"ds_read_b128 v[176:179], v240 offset:0\n" \
"ds_read_b64_tr_b16 v[152:153], v241 offset:512\n" \
"v_mfma_f32_16x16x32_f16 v[60:63], v[132:135], v[160:163], v[60:63]\n" \
"s_waitcnt lgkmcnt(8)\n" \
"v_pk_add_f16 v160, v160, v164\n" \
"v_pk_add_f16 v161, v161, v165\n" \
"v_mfma_f32_16x16x32_f16 v[52:55], v[144:147], v[164:167], v[52:55]\n" \
"v_pk_add_f16 v162, v162, v166\n" \
"v_pk_add_f16 v163, v163, v167\n" \
"v_mfma_f32_16x16x32_f16 v[64:67], v[148:151], v[164:167], v[64:67]\n" \
"ds_read_b128 v[180:183], v240 offset:512\n" \
"v_mfma_f32_16x16x32_f16 v[56:59], v[136:139], v[160:163], v[56:59]\n" \
"buffer_load_dwordx4 v232, s[36:39], s80 offen offset:2048 sc1 lds\n" \
"v_mfma_f32_16x16x32_f16 v[68:71], v[140:143], v[160:163], v[68:71]\n" \
"s_setprio 0\n" \
"ds_read_b64_tr_b16 v[154:155], v241 offset:8704\n" \
"ds_read_b64_tr_b16 v[156:157], v242 offset:512\n" \
"s_waitcnt lgkmcnt(6)\n" \
"v_mfma_f32_16x16x32_f16 v[72:75], v[128:131], v[168:171], v[72:75]\n" \
"ds_read_b128 v[160:163], v240 offset:2048\n" \
"ds_read_b64_tr_b16 v[158:159], v242 offset:8704\n" \
"v_mfma_f32_16x16x32_f16 v[84:87], v[132:135], v[168:171], v[84:87]\n" \
"v_pk_add_f16 v168, v168, v172\n" \
"v_pk_add_f16 v169, v169, v173\n" \
"v_mfma_f32_16x16x32_f16 v[76:79], v[144:147], v[172:175], v[76:79]\n" \
"v_pk_add_f16 v170, v170, v174\n" \
"v_pk_add_f16 v171, v171, v175\n" \
"v_mfma_f32_16x16x32_f16 v[88:91], v[148:151], v[172:175], v[88:91]\n" \
"ds_read_b128 v[164:167], v240 offset:2560\n" \
"v_mfma_f32_16x16x32_f16 v[80:83], v[136:139], v[168:171], v[80:83]\n" \
"buffer_load_dwordx4 v232, s[36:39], s80 offen offset:3072 sc1 lds\n" \
"v_mfma_f32_16x16x32_f16 v[92:95], v[140:143], v[168:171], v[92:95]\n" \
"s_waitcnt lgkmcnt(6)\n" \
"v_mfma_f32_16x16x32_f16 v[96:99], v[128:131], v[176:179], v[96:99]\n" \
"v_pk_add_f16 v128, v120, v152\n" \
"v_pk_add_f16 v129, v121, v153\n" \
"ds_read_b128 v[168:171], v240 offset:4096\n" \
"v_mfma_f32_16x16x32_f16 v[108:111], v[132:135], v[176:179], v[108:111]\n" \
"s_waitcnt lgkmcnt(6)\n" \
"v_pk_add_f16 v176, v176, v180\n" \
"v_pk_add_f16 v177, v177, v181\n" \
"ds_read_b128 v[172:175], v240 offset:4608\n" \
"v_mfma_f32_16x16x32_f16 v[100:103], v[144:147], v[180:183], v[100:103]\n" \
"v_pk_add_f16 v178, v178, v182\n" \
"v_pk_add_f16 v179, v179, v183\n" \
"s_add_i32 m0, s45, 0xa000\n" \
"s_add_i32 s81, s47, 0x7000000\n" \
"v_mfma_f32_16x16x32_f16 v[112:115], v[148:151], v[180:183], v[112:115]\n" \
"s_waitcnt lgkmcnt(3)\n" \
"v_pk_add_f16 v130, v122, v154\n" \
"v_pk_add_f16 v131, v123, v155\n" \
"buffer_load_dwordx4 v232, s[36:39], s81 offen sc1 lds\n" \
"v_mfma_f32_16x16x32_f16 v[104:107], v[136:139], v[176:179], v[104:107]\n" \
"v_pk_add_f16 v132, v124, v156\n" \
"v_pk_add_f16 v133, v125, v157\n" \
"v_mfma_f32_16x16x32_f16 v[116:119], v[140:143], v[176:179], v[116:119]\n" \
"s_setprio 1\n" \
"v_pk_add_f16 v134, v126, v158\n" \
"v_pk_add_f16 v135, v127, v159\n" \
"s_waitcnt vmcnt(9) lgkmcnt(0)\n" \
"s_barrier\n" \
"v_mfma_f32_16x16x32_f16 v[0:3], v[120:123], v[160:163], v[0:3]\n" \
"ds_read_b128 v[176:179], v240 offset:26624\n" \
"v_mfma_f32_16x16x32_f16 v[12:15], v[124:127], v[160:163], v[12:15]\n" \
"v_pk_add_f16 v160, v160, v164\n" \
"v_pk_add_f16 v161, v161, v165\n" \
"v_mfma_f32_16x16x32_f16 v[4:7], v[152:155], v[164:167], v[4:7]\n" \
"v_pk_add_f16 v162, v162, v166\n" \
"v_pk_add_f16 v163, v163, v167\n" \
"v_mfma_f32_16x16x32_f16 v[16:19], v[156:159], v[164:167], v[16:19]\n" \
"ds_read_b128 v[180:183], v240 offset:27136\n" \
"s_add_i32 m0, s44, 0x0\n" \
"s_add_i32 s80, s46, 0x7400000\n" \
"s_bitcmp1_b32 s46, 22\n" \
"s_cselect_b32 s80, 0x80000000, s80\n" \
"v_mfma_f32_16x16x32_f16 v[8:11], v[128:131], v[160:163], v[8:11]\n" \
"buffer_load_dwordx4 v232, s[36:39], s80 offen sc1 lds\n" \
"v_mfma_f32_16x16x32_f16 v[20:23], v[132:135], v[160:163], v[20:23]\n" \
"ds_read_b64_tr_b16 v[136:137], v241 offset:16384\n" \
"ds_read_b64_tr_b16 v[138:139], v241 offset:24576\n" \
"v_mfma_f32_16x16x32_f16 v[24:27], v[120:123], v[168:171], v[24:27]\n" \
"ds_read_b128 v[160:163], v240 offset:28672\n" \
"v_mfma_f32_16x16x32_f16 v[36:39], v[124:127], v[168:171], v[36:39]\n" \
"v_pk_add_f16 v168, v168, v172\n" \
"v_pk_add_f16 v169, v169, v173\n" \
"v_mfma_f32_16x16x32_f16 v[28:31], v[152:155], v[172:175], v[28:31]\n" \
"v_pk_add_f16 v170, v170, v174\n" \
"v_pk_add_f16 v171, v171, v175\n" \
"v_mfma_f32_16x16x32_f16 v[40:43], v[156:159], v[172:175], v[40:43]\n" \
"ds_read_b128 v[164:167], v240 offset:29184\n" \
"ds_read_b64_tr_b16 v[140:141], v242 offset:16384\n" \
"v_mfma_f32_16x16x32_f16 v[32:35], v[128:131], v[168:171], v[32:35]\n" \
"buffer_load_dwordx4 v232, s[36:39], s80 offen offset:1024 sc1 lds\n" \
"v_mfma_f32_16x16x32_f16 v[44:47], v[132:135], v[168:171], v[44:47]\n" \
"ds_read_b64_tr_b16 v[142:143], v242 offset:24576\n" \
"s_waitcnt lgkmcnt(6)\n" \
"v_mfma_f32_16x16x32_f16 v[48:51], v[120:123], v[176:179], v[48:51]\n" \
"ds_read_b128 v[168:171], v240 offset:20480\n" \
"ds_read_b64_tr_b16 v[144:145], v241 offset:16896\n" \
"v_mfma_f32_16x16x32_f16 v[60:63], v[124:127], v[176:179], v[60:63]\n" \
"v_pk_add_f16 v176, v176, v180\n" \
"v_pk_add_f16 v177, v177, v181\n" \
"v_mfma_f32_16x16x32_f16 v[52:55], v[152:155], v[180:183], v[52:55]\n" \
"v_pk_add_f16 v178, v178, v182\n" \
"v_pk_add_f16 v179, v179, v183\n" \
"v_mfma_f32_16x16x32_f16 v[64:67], v[156:159], v[180:183], v[64:67]\n" \
"ds_read_b128 v[172:175], v240 offset:20992\n" \
"v_mfma_f32_16x16x32_f16 v[56:59], v[128:131], v[176:179], v[56:59]\n" \
"buffer_load_dwordx4 v232, s[36:39], s80 offen offset:2048 sc1 lds\n" \
"v_mfma_f32_16x16x32_f16 v[68:71], v[132:135], v[176:179], v[68:71]\n" \
"s_setprio 0\n" \
"ds_read_b64_tr_b16 v[146:147], v241 offset:25088\n" \
"ds_read_b64_tr_b16 v[148:149], v242 offset:16896\n" \
"s_waitcnt lgkmcnt(7)\n" \
"v_mfma_f32_16x16x32_f16 v[72:75], v[120:123], v[160:163], v[72:75]\n" \
"ds_read_b128 v[176:179], v240 offset:22528\n" \
"ds_read_b64_tr_b16 v[150:151], v242 offset:25088\n" \
"v_mfma_f32_16x16x32_f16 v[84:87], v[124:127], v[160:163], v[84:87]\n" \
"v_pk_add_f16 v160, v160, v164\n" \
"v_pk_add_f16 v161, v161, v165\n" \
"v_mfma_f32_16x16x32_f16 v[76:79], v[152:155], v[164:167], v[76:79]\n" \
"v_pk_add_f16 v162, v162, v166\n" \
"v_pk_add_f16 v163, v163, v167\n" \
"v_add_u32 v243, 0x1b000, v241\n" \
"v_mfma_f32_16x16x32_f16 v[88:91], v[156:159], v[164:167], v[88:91]\n" \
"v_xor_b32 v244, 32, v243\n" \
"ds_read_b128 v[180:183], v240 offset:23040\n" \
"v_mfma_f32_16x16x32_f16 v[80:83], v[128:131], v[160:163], v[80:83]\n" \
"buffer_load_dwordx4 v232, s[36:39], s80 offen offset:3072 sc1 lds\n" \
"v_mfma_f32_16x16x32_f16 v[92:95], v[132:135], v[160:163], v[92:95]\n" \
"v_add_u32 v246, 0x15000, v240\n" \
"s_waitcnt lgkmcnt(5)\n" \
"v_mfma_f32_16x16x32_f16 v[96:99], v[120:123], v[168:171], v[96:99]\n" \
"v_pk_add_f16 v120, v136, v144\n" \
"v_pk_add_f16 v121, v137, v145\n" \
"ds_read_b128 v[160:163], v240 offset:24576\n" \
"v_mfma_f32_16x16x32_f16 v[108:111], v[124:127], v[168:171], v[108:111]\n" \
"v_pk_add_f16 v168, v168, v172\n" \
"v_pk_add_f16 v169, v169, v173\n" \
"ds_read_b128 v[164:167], v240 offset:25088\n" \
"v_mfma_f32_16x16x32_f16 v[100:103], v[152:155], v[172:175], v[100:103]\n" \
"v_pk_add_f16 v170, v170, v174\n" \
"v_pk_add_f16 v171, v171, v175\n" \
"s_add_i32 m0, s45, 0x0\n" \
"s_add_i32 s81, s47, 0x7400000\n" \
"v_mfma_f32_16x16x32_f16 v[112:115], v[156:159], v[172:175], v[112:115]\n" \
"s_waitcnt lgkmcnt(3)\n" \
"v_pk_add_f16 v122, v138, v146\n" \
"v_pk_add_f16 v123, v139, v147\n" \
"buffer_load_dwordx4 v232, s[36:39], s81 offen sc1 lds\n" \
"v_mfma_f32_16x16x32_f16 v[104:107], v[128:131], v[168:171], v[104:107]\n" \
"v_pk_add_f16 v124, v140, v148\n" \
"v_pk_add_f16 v125, v141, v149\n" \
"s_mov_b32 m0, 0x27c00\n" \
"v_mfma_f32_16x16x32_f16 v[116:119], v[132:135], v[168:171], v[116:119]\n" \
"v_pk_add_f16 v126, v142, v150\n" \
"v_pk_add_f16 v127, v143, v151\n" \
"global_load_lds_dword v[248:249], off\n" \
"s_waitcnt vmcnt(6) lgkmcnt(0)\n" \
"s_barrier\n" \
"v_mfma_f32_16x16x32_f16 v[0:3], v[136:139], v[176:179], v[0:3]\n" \
"ds_read_b128 v[168:171], v240 offset:43008\n" \
"s_add_i32 m0, s44, 0x5000\n" \
"s_add_i32 s80, s46, 0x7800000\n" \
"s_bitcmp1_b32 s46, 22\n" \
"s_cselect_b32 s80, 0x80000000, s80\n" \
"ds_read_b64_tr_b16 v[128:129], v241 offset:32768\n" \
"v_mfma_f32_16x16x32_f16 v[12:15], v[140:143], v[176:179], v[12:15]\n" \
"v_pk_add_f16 v176, v176, v180\n" \
"v_pk_add_f16 v177, v177, v181\n" \
"buffer_load_dwordx4 v232, s[36:39], s80 offen sc1 lds\n" \
"ds_read_b64_tr_b16 v[130:131], v241 offset:40960\n" \
"v_mfma_f32_16x16x32_f16 v[4:7], v[144:147], v[180:183], v[4:7]\n" \
"v_pk_add_f16 v178, v178, v182\n" \
"v_pk_add_f16 v179, v179, v183\n" \
"buffer_load_dwordx4 v232, s[36:39], s80 offen offset:1024 sc1 lds\n" \
"ds_read_b64_tr_b16 v[132:133], v242 offset:32768\n" \
"v_mfma_f32_16x16x32_f16 v[16:19], v[148:151], v[180:183], v[16:19]\n" \
"ds_read_b128 v[172:175], v240 offset:43520\n" \
"buffer_load_dwordx4 v232, s[36:39], s80 offen offset:2048 sc1 lds\n" \
"ds_read_b64_tr_b16 v[134:135], v242 offset:40960\n" \
"ds_read_b64_tr_b16 v[152:153], v241 offset:33280\n" \
"v_mfma_f32_16x16x32_f16 v[8:11], v[120:123], v[176:179], v[8:11]\n" \
"buffer_load_dwordx4 v232, s[36:39], s80 offen offset:3072 sc1 lds\n" \
"ds_read_b64_tr_b16 v[154:155], v241 offset:41472\n" \
"v_mfma_f32_16x16x32_f16 v[20:23], v[124:127], v[176:179], v[20:23]\n" \
"s_add_i32 m0, s45, 0x5000\n" \
"s_add_i32 s81, s47, 0x7800000\n" \
"ds_read_b64_tr_b16 v[156:157], v242 offset:33280\n" \
"ds_read_b64_tr_b16 v[158:159], v242 offset:41472\n" \
"v_mfma_f32_16x16x32_f16 v[24:27], v[136:139], v[160:163], v[24:27]\n" \
"ds_read_b128 v[176:179], v246 offset:2048\n" \
"buffer_load_dwordx4 v232, s[36:39], s81 offen sc1 lds\n" \
"ds_read_b64_tr_b16 v[184:185], v243 offset:0\n" \
"v_mfma_f32_16x16x32_f16 v[36:39], v[140:143], v[160:163], v[36:39]\n" \
"v_pk_add_f16 v160, v160, v164\n" \
"v_pk_add_f16 v161, v161, v165\n" \
"ds_read_b64_tr_b16 v[186:187], v243 offset:8192\n" \
"ds_read_b64_tr_b16 v[188:189], v244 offset:0\n" \
"v_mfma_f32_16x16x32_f16 v[28:31], v[144:147], v[164:167], v[28:31]\n" \
"v_pk_add_f16 v162, v162, v166\n" \
"v_pk_add_f16 v163, v163, v167\n" \
"s_add_i32 m0, s44, 0xffff7000\n" \
"s_add_i32 s80, s46, 0x7c00000\n" \
"s_bitcmp0_b32 s46, 22\n" \
"s_cselect_b64 exec, -1, 0\n" \
"buffer_load_dwordx4 v232, s[36:39], s80 offen sc1 lds\n" \
"buffer_load_dwordx4 v232, s[36:39], s80 offen offset:1024 sc1 lds\n" \
"buffer_load_dwordx4 v232, s[36:39], s80 offen offset:2048 sc1 lds\n" \
"buffer_load_dwordx4 v232, s[36:39], s80 offen offset:3072 sc1 lds\n" \
"s_mov_b64 exec, -1\n" \
"v_mfma_f32_16x16x32_f16 v[40:43], v[148:151], v[164:167], v[40:43]\n" \
"ds_read_b128 v[180:183], v246 offset:2560\n" \
"s_add_i32 m0, s45, 0xffff7000\n" \
"s_add_i32 s81, s47, 0x7c00000\n" \
"ds_read_b64_tr_b16 v[190:191], v244 offset:8192\n" \
"ds_read_b64_tr_b16 v[192:193], v243 offset:512\n" \
"v_mfma_f32_16x16x32_f16 v[32:35], v[120:123], v[160:163], v[32:35]\n" \
"buffer_load_dwordx4 v232, s[36:39], s81 offen sc1 lds\n" \
"ds_read_b64_tr_b16 v[194:195], v243 offset:8704\n" \
"v_mfma_f32_16x16x32_f16 v[44:47], v[124:127], v[160:163], v[44:47]\n" \
"s_waitcnt lgkmcnt(5)\n" \
"v_pk_add_f16 v232, v128, v152\n" \
"v_pk_add_f16 v233, v129, v153\n" \
"ds_read_b64_tr_b16 v[196:197], v244 offset:512\n" \
"ds_read_b64_tr_b16 v[198:199], v244 offset:8704\n" \
"v_mfma_f32_16x16x32_f16 v[0:3], v[128:131], v[168:171], v[0:3]\n" \
"v_pk_add_f16 v234, v130, v154\n" \
"ds_read_b128 v[160:163], v246 offset:10240\n" \
"ds_read_b64_tr_b16 v[208:209], v243 offset:16384\n" \
"ds_read_b64_tr_b16 v[210:211], v243 offset:24576\n" \
"v_mfma_f32_16x16x32_f16 v[12:15], v[132:135], v[168:171], v[12:15]\n" \
"v_pk_add_f16 v168, v168, v172\n" \
"v_pk_add_f16 v169, v169, v173\n" \
"v_pk_add_f16 v235, v131, v155\n" \
"v_pk_add_f16 v236, v132, v156\n" \
"ds_read_b64_tr_b16 v[212:213], v244 offset:16384\n" \
"v_mfma_f32_16x16x32_f16 v[4:7], v[152:155], v[172:175], v[4:7]\n" \
"v_pk_add_f16 v170, v170, v174\n" \
"v_pk_add_f16 v171, v171, v175\n" \
"v_pk_add_f16 v237, v133, v157\n" \
"v_pk_add_f16 v238, v134, v158\n" \
"v_pk_add_f16 v239, v135, v159\n" \
"v_mfma_f32_16x16x32_f16 v[16:19], v[156:159], v[172:175], v[16:19]\n" \
"s_waitcnt lgkmcnt(6)\n" \
"v_pk_add_f16 v200, v184, v192\n" \
"ds_read_b128 v[164:167], v246 offset:10752\n" \
"ds_read_b64_tr_b16 v[214:215], v244 offset:24576\n" \
"ds_read_b64_tr_b16 v[216:217], v243 offset:16896\n" \
"v_mfma_f32_16x16x32_f16 v[8:11], v[232:235], v[168:171], v[8:11]\n" \
"v_pk_add_f16 v201, v185, v193\n" \
"v_pk_add_f16 v202, v186, v194\n" \
"ds_read_b64_tr_b16 v[218:219], v243 offset:25088\n" \
"v_mfma_f32_16x16x32_f16 v[20:23], v[236:239], v[168:171], v[20:23]\n" \
"ds_read_b64_tr_b16 v[220:221], v244 offset:16896\n" \
"ds_read_b64_tr_b16 v[222:223], v244 offset:25088\n" \
"v_mfma_f32_16x16x32_f16 v[0:3], v[184:187], v[176:179], v[0:3]\n" \
"v_pk_add_f16 v203, v187, v195\n" \
"s_waitcnt lgkmcnt(7)\n" \
"v_pk_add_f16 v204, v188, v196\n" \
"ds_read_b128 v[168:171], v240 offset:45056\n" \
"v_mfma_f32_16x16x32_f16 v[12:15], v[188:191], v[176:179], v[12:15]\n" \
"v_pk_add_f16 v176, v176, v180\n" \
"v_pk_add_f16 v177, v177, v181\n" \
"v_pk_add_f16 v205, v189, v197\n" \
"v_mfma_f32_16x16x32_f16 v[4:7], v[192:195], v[180:183], v[4:7]\n" \
"v_pk_add_f16 v178, v178, v182\n" \
"v_pk_add_f16 v179, v179, v183\n" \
"v_pk_add_f16 v206, v190, v198\n" \
"v_pk_add_f16 v207, v191, v199\n" \
"v_mfma_f32_16x16x32_f16 v[16:19], v[196:199], v[180:183], v[16:19]\n" \
"s_waitcnt lgkmcnt(4)\n" \
"v_pk_add_f16 v224, v208, v216\n" \
"ds_read_b128 v[172:175], v240 offset:45568\n" \
"v_mfma_f32_16x16x32_f16 v[8:11], v[200:203], v[176:179], v[8:11]\n" \
"v_pk_add_f16 v225, v209, v217\n" \
"v_mfma_f32_16x16x32_f16 v[20:23], v[204:207], v[176:179], v[20:23]\n" \
"s_waitcnt lgkmcnt(4)\n" \
"v_pk_add_f16 v226, v210, v218\n" \
"v_pk_add_f16 v227, v211, v219\n" \
"v_mfma_f32_16x16x32_f16 v[0:3], v[208:211], v[160:163], v[0:3]\n" \
"s_waitcnt lgkmcnt(2)\n" \
"v_pk_add_f16 v228, v212, v220\n" \
"v_pk_add_f16 v229, v213, v221\n" \
"ds_read_b128 v[176:179], v246 offset:4096\n" \
"v_mfma_f32_16x16x32_f16 v[12:15], v[212:215], v[160:163], v[12:15]\n" \
"v_pk_add_f16 v160, v160, v164\n" \
"v_pk_add_f16 v161, v161, v165\n" \
"v_pk_add_f16 v230, v214, v222\n" \
"v_mfma_f32_16x16x32_f16 v[4:7], v[216:219], v[164:167], v[4:7]\n" \
"v_pk_add_f16 v162, v162, v166\n" \
"v_pk_add_f16 v163, v163, v167\n" \
"v_pk_add_f16 v231, v215, v223\n" \
"v_mfma_f32_16x16x32_f16 v[16:19], v[220:223], v[164:167], v[16:19]\n" \
"ds_read_b128 v[180:183], v246 offset:4608\n" \
"s_mov_b32 vcc_lo, 0xaaaaaaaa\n" \
"v_mfma_f32_16x16x32_f16 v[8:11], v[224:227], v[160:163], v[8:11]\n" \
"s_mov_b32 vcc_hi, 0xaaaaaaaa\n" \
"v_mfma_f32_16x16x32_f16 v[20:23], v[228:231], v[160:163], v[20:23]\n" \
"v_add_u32 v241, 0xffff7000, v240\n" \
"s_waitcnt lgkmcnt(2)\n" \
"v_mfma_f32_16x16x32_f16 v[24:27], v[128:131], v[168:171], v[24:27]\n" \
"ds_read_b128 v[160:163], v246 offset:12288\n" \
"v_mfma_f32_16x16x32_f16 v[36:39], v[132:135], v[168:171], v[36:39]\n" \
"v_pk_add_f16 v168, v168, v172\n" \
"v_pk_add_f16 v169, v169, v173\n" \
"v_mfma_f32_16x16x32_f16 v[28:31], v[152:155], v[172:175], v[28:31]\n" \
"v_pk_add_f16 v170, v170, v174\n" \
"v_pk_add_f16 v171, v171, v175\n" \
"v_add_f32 v252, v4, v8\n" \
"v_add_f32 v253, v5, v9\n" \
"v_add_f32 v254, v6, v10\n" \
"v_add_f32 v255, v7, v11\n" \
"v_mfma_f32_16x16x32_f16 v[40:43], v[156:159], v[172:175], v[40:43]\n" \
"ds_read_b128 v[164:167], v246 offset:12800\n" \
"v_fmac_f32 v4, s56, v8\n" \
"v_fmac_f32 v5, s56, v9\n" \
"v_fmac_f32 v6, s56, v10\n" \
"v_fmac_f32 v7, s56, v11\n" \
"v_mfma_f32_16x16x32_f16 v[32:35], v[232:235], v[168:171], v[32:35]\n" \
"v_fmac_f32 v0, s57, v252\n" \
"v_fmac_f32 v1, s57, v253\n" \
"v_fmac_f32 v2, s57, v254\n" \
"v_fmac_f32 v3, s57, v255\n" \
"v_add_f32 v252, v16, v20\n" \
"v_mfma_f32_16x16x32_f16 v[44:47], v[236:239], v[168:171], v[44:47]\n" \
"v_add_f32 v253, v17, v21\n" \
"v_add_f32 v254, v18, v22\n" \
"v_add_f32 v255, v19, v23\n" \
"v_fmac_f32 v16, s56, v20\n" \
"s_waitcnt lgkmcnt(1)\n" \
"v_mfma_f32_16x16x32_f16 v[24:27], v[184:187], v[176:179], v[24:27]\n" \
"ds_read_b128 v[168:171], v240 offset:47104\n" \
"v_fmac_f32 v17, s56, v21\n" \
"v_fmac_f32 v18, s56, v22\n" \
"v_fmac_f32 v19, s56, v23\n" \
"v_fmac_f32 v12, s57, v252\n" \
"v_fmac_f32 v13, s57, v253\n" \
"v_mfma_f32_16x16x32_f16 v[36:39], v[188:191], v[176:179], v[36:39]\n" \
"v_pk_add_f16 v176, v176, v180\n" \
"v_pk_add_f16 v177, v177, v181\n" \
"v_fmac_f32 v14, s57, v254\n" \
"v_fmac_f32 v15, s57, v255\n" \
"v_cvt_pk_bf16_f32 v8, v0, v1\n" \
"v_cvt_pk_bf16_f32 v20, v4, v5\n" \
"v_mfma_f32_16x16x32_f16 v[28:31], v[192:195], v[180:183], v[28:31]\n" \
"v_pk_add_f16 v178, v178, v182\n" \
"v_pk_add_f16 v179, v179, v183\n" \
"v_cvt_pk_bf16_f32 v9, v2, v3\n" \
"v_cvt_pk_bf16_f32 v21, v6, v7\n" \
"v_cvt_pk_bf16_f32 v10, v12, v13\n" \
"v_cvt_pk_bf16_f32 v22, v16, v17\n" \
"v_cvt_pk_bf16_f32 v11, v14, v15\n" \
"v_mfma_f32_16x16x32_f16 v[40:43], v[196:199], v[180:183], v[40:43]\n" \
"ds_read_b128 v[172:175], v240 offset:47616\n" \
"v_cvt_pk_bf16_f32 v23, v18, v19\n" \
"s_not_b64 vcc, vcc\n" \
"v_cndmask_b32_dpp v0, v20, v8, vcc quad_perm:[1,0,3,2] row_mask:0xf bank_mask:0xf\n" \
"v_cndmask_b32_dpp v1, v21, v9, vcc quad_perm:[1,0,3,2] row_mask:0xf bank_mask:0xf\n" \
"v_mfma_f32_16x16x32_f16 v[32:35], v[200:203], v[176:179], v[32:35]\n" \
"s_waitcnt vmcnt(0)\n" \
"s_barrier\n" \
"v_cndmask_b32_dpp v2, v22, v10, vcc quad_perm:[1,0,3,2] row_mask:0xf bank_mask:0xf\n" \
"v_cndmask_b32_dpp v3, v23, v11, vcc quad_perm:[1,0,3,2] row_mask:0xf bank_mask:0xf\n" \
"buffer_store_dwordx4 v[0:3], v245, s[40:43], s69 offen sc1\n" \
"s_not_b64 vcc, vcc\n" \
"s_add_i32 s85, s69, 0x10000\n" \
"v_mfma_f32_16x16x32_f16 v[44:47], v[204:207], v[176:179], v[44:47]\n" \
"v_cndmask_b32_dpp v12, v8, v20, vcc quad_perm:[1,0,3,2] row_mask:0xf bank_mask:0xf\n" \
"v_cndmask_b32_dpp v13, v9, v21, vcc quad_perm:[1,0,3,2] row_mask:0xf bank_mask:0xf\n" \
"v_cndmask_b32_dpp v14, v10, v22, vcc quad_perm:[1,0,3,2] row_mask:0xf bank_mask:0xf\n" \
"v_cndmask_b32_dpp v15, v11, v23, vcc quad_perm:[1,0,3,2] row_mask:0xf bank_mask:0xf\n" \
"buffer_store_dwordx4 v[12:15], v245, s[40:43], s85 offen sc1\n" \
"v_mfma_f32_16x16x32_f16 v[24:27], v[208:211], v[160:163], v[24:27]\n" \
"ds_read_b128 v[176:179], v240 offset:6144\n" \
"v_mfma_f32_16x16x32_f16 v[36:39], v[212:215], v[160:163], v[36:39]\n" \
"s_waitcnt lgkmcnt(2)\n" \
"v_pk_add_f16 v160, v160, v164\n" \
"v_pk_add_f16 v161, v161, v165\n" \
"v_mfma_f32_16x16x32_f16 v[28:31], v[216:219], v[164:167], v[28:31]\n" \
"v_pk_add_f16 v162, v162, v166\n" \
"v_pk_add_f16 v163, v163, v167\n" \
"v_mfma_f32_16x16x32_f16 v[40:43], v[220:223], v[164:167], v[40:43]\n" \
"ds_read_b128 v[180:183], v240 offset:6656\n" \
"v_mfma_f32_16x16x32_f16 v[32:35], v[224:227], v[160:163], v[32:35]\n" \
"v_mfma_f32_16x16x32_f16 v[44:47], v[228:231], v[160:163], v[44:47]\n" \
"v_mfma_f32_16x16x32_f16 v[48:51], v[136:139], v[168:171], v[48:51]\n" \
"ds_read_b128 v[160:163], v240 offset:26624\n" \
"v_mfma_f32_16x16x32_f16 v[60:63], v[140:143], v[168:171], v[60:63]\n" \
"s_waitcnt lgkmcnt(3)\n" \
"v_pk_add_f16 v168, v168, v172\n" \
"v_pk_add_f16 v169, v169, v173\n" \
"v_mfma_f32_16x16x32_f16 v[52:55], v[144:147], v[172:175], v[52:55]\n" \
"v_pk_add_f16 v170, v170, v174\n" \
"v_pk_add_f16 v171, v171, v175\n" \
"v_add_f32 v252, v28, v32\n" \
"v_add_f32 v253, v29, v33\n" \
"v_mfma_f32_16x16x32_f16 v[64:67], v[148:151], v[172:175], v[64:67]\n" \
"ds_read_b128 v[164:167], v240 offset:27136\n" \
"v_add_f32 v254, v30, v34\n" \
"v_add_f32 v255, v31, v35\n" \
"v_fmac_f32 v28, s56, v32\n" \
"v_mfma_f32_16x16x32_f16 v[56:59], v[120:123], v[168:171], v[56:59]\n" \
"v_fmac_f32 v29, s56, v33\n" \
"v_fmac_f32 v30, s56, v34\n" \
"v_fmac_f32 v31, s56, v35\n" \
"v_mfma_f32_16x16x32_f16 v[68:71], v[124:127], v[168:171], v[68:71]\n" \
"v_fmac_f32 v24, s57, v252\n" \
"v_fmac_f32 v25, s57, v253\n" \
"v_fmac_f32 v26, s57, v254\n" \
"s_waitcnt lgkmcnt(2)\n" \
"v_mfma_f32_16x16x32_f16 v[48:51], v[128:131], v[176:179], v[48:51]\n" \
"ds_read_b128 v[168:171], v241 offset:6144\n" \
"v_fmac_f32 v27, s57, v255\n" \
"v_add_f32 v252, v40, v44\n" \
"v_add_f32 v253, v41, v45\n" \
"v_mfma_f32_16x16x32_f16 v[60:63], v[132:135], v[176:179], v[60:63]\n" \
"v_pk_add_f16 v176, v176, v180\n" \
"v_pk_add_f16 v177, v177, v181\n" \
"v_add_f32 v254, v42, v46\n" \
"v_add_f32 v255, v43, v47\n" \
"v_mfma_f32_16x16x32_f16 v[52:55], v[152:155], v[180:183], v[52:55]\n" \
"v_pk_add_f16 v178, v178, v182\n" \
"v_pk_add_f16 v179, v179, v183\n" \
"v_fmac_f32 v40, s56, v44\n" \
"v_fmac_f32 v41, s56, v45\n" \
"v_fmac_f32 v42, s56, v46\n" \
"v_mfma_f32_16x16x32_f16 v[64:67], v[156:159], v[180:183], v[64:67]\n" \
"ds_read_b128 v[172:175], v241 offset:6656\n" \
"v_fmac_f32 v43, s56, v47\n" \
"v_fmac_f32 v36, s57, v252\n" \
"v_fmac_f32 v37, s57, v253\n" \
"v_mfma_f32_16x16x32_f16 v[56:59], v[232:235], v[176:179], v[56:59]\n" \
"v_fmac_f32 v38, s57, v254\n" \
"v_fmac_f32 v39, s57, v255\n" \
"v_cvt_pk_bf16_f32 v32, v24, v25\n" \
"v_mfma_f32_16x16x32_f16 v[68:71], v[236:239], v[176:179], v[68:71]\n" \
"v_cvt_pk_bf16_f32 v44, v28, v29\n" \
"v_cvt_pk_bf16_f32 v33, v26, v27\n" \
"v_cvt_pk_bf16_f32 v45, v30, v31\n" \
"s_waitcnt lgkmcnt(1)\n" \
"v_mfma_f32_16x16x32_f16 v[48:51], v[184:187], v[160:163], v[48:51]\n" \
"ds_read_b128 v[176:179], v240 offset:49152\n" \
"v_cvt_pk_bf16_f32 v34, v36, v37\n" \
"v_cvt_pk_bf16_f32 v46, v40, v41\n" \
"v_mfma_f32_16x16x32_f16 v[60:63], v[188:191], v[160:163], v[60:63]\n" \
"v_pk_add_f16 v160, v160, v164\n" \
"v_pk_add_f16 v161, v161, v165\n" \
"v_cvt_pk_bf16_f32 v35, v38, v39\n" \
"v_cvt_pk_bf16_f32 v47, v42, v43\n" \
"s_not_b64 vcc, vcc\n" \
"v_mfma_f32_16x16x32_f16 v[52:55], v[192:195], v[164:167], v[52:55]\n" \
"v_pk_add_f16 v162, v162, v166\n" \
"v_pk_add_f16 v163, v163, v167\n" \
"v_cndmask_b32_dpp v24, v44, v32, vcc quad_perm:[1,0,3,2] row_mask:0xf bank_mask:0xf\n" \
"v_cndmask_b32_dpp v25, v45, v33, vcc quad_perm:[1,0,3,2] row_mask:0xf bank_mask:0xf\n" \
"v_cndmask_b32_dpp v26, v46, v34, vcc quad_perm:[1,0,3,2] row_mask:0xf bank_mask:0xf\n" \
"v_mfma_f32_16x16x32_f16 v[64:67], v[196:199], v[164:167], v[64:67]\n" \
"ds_read_b128 v[180:183], v240 offset:49664\n" \
"v_cndmask_b32_dpp v27, v47, v35, vcc quad_perm:[1,0,3,2] row_mask:0xf bank_mask:0xf\n" \
"buffer_store_dwordx4 v[24:27], v245, s[40:43], s70 offen sc1\n" \
"s_not_b64 vcc, vcc\n" \
"v_mfma_f32_16x16x32_f16 v[56:59], v[200:203], v[160:163], v[56:59]\n" \
"s_add_i32 s85, s70, 0x10000\n" \
"v_cndmask_b32_dpp v36, v32, v44, vcc quad_perm:[1,0,3,2] row_mask:0xf bank_mask:0xf\n" \
"v_cndmask_b32_dpp v37, v33, v45, vcc quad_perm:[1,0,3,2] row_mask:0xf bank_mask:0xf\n" \
"v_mfma_f32_16x16x32_f16 v[68:71], v[204:207], v[160:163], v[68:71]\n" \
"v_cndmask_b32_dpp v38, v34, v46, vcc quad_perm:[1,0,3,2] row_mask:0xf bank_mask:0xf\n" \
"v_cndmask_b32_dpp v39, v35, v47, vcc quad_perm:[1,0,3,2] row_mask:0xf bank_mask:0xf\n" \
"buffer_store_dwordx4 v[36:39], v245, s[40:43], s85 offen sc1\n" \
"v_mfma_f32_16x16x32_f16 v[48:51], v[208:211], v[168:171], v[48:51]\n" \
"ds_read_b128 v[160:163], v240 offset:8192\n" \
"v_mfma_f32_16x16x32_f16 v[60:63], v[212:215], v[168:171], v[60:63]\n" \
"s_waitcnt lgkmcnt(2)\n" \
"v_pk_add_f16 v168, v168, v172\n" \
"v_pk_add_f16 v169, v169, v173\n" \
"v_mfma_f32_16x16x32_f16 v[52:55], v[216:219], v[172:175], v[52:55]\n" \
"v_pk_add_f16 v170, v170, v174\n" \
"v_pk_add_f16 v171, v171, v175\n" \
"v_mfma_f32_16x16x32_f16 v[64:67], v[220:223], v[172:175], v[64:67]\n" \
"ds_read_b128 v[164:167], v240 offset:8704\n" \
"v_mfma_f32_16x16x32_f16 v[56:59], v[224:227], v[168:171], v[56:59]\n" \
"v_mfma_f32_16x16x32_f16 v[68:71], v[228:231], v[168:171], v[68:71]\n" \
"v_mfma_f32_16x16x32_f16 v[72:75], v[136:139], v[176:179], v[72:75]\n" \
"ds_read_b128 v[168:171], v240 offset:28672\n" \
"v_mfma_f32_16x16x32_f16 v[84:87], v[140:143], v[176:179], v[84:87]\n" \
"s_waitcnt lgkmcnt(3)\n" \
"v_pk_add_f16 v176, v176, v180\n" \
"v_pk_add_f16 v177, v177, v181\n" \
"v_mfma_f32_16x16x32_f16 v[76:79], v[144:147], v[180:183], v[76:79]\n" \
"v_pk_add_f16 v178, v178, v182\n" \
"v_pk_add_f16 v179, v179, v183\n" \
"v_add_f32 v252, v52, v56\n" \
"v_add_f32 v253, v53, v57\n" \
"v_mfma_f32_16x16x32_f16 v[88:91], v[148:151], v[180:183], v[88:91]\n" \
"ds_read_b128 v[172:175], v240 offset:29184\n" \
"v_add_f32 v254, v54, v58\n" \
"v_add_f32 v255, v55, v59\n" \
"v_mfma_f32_16x16x32_f16 v[80:83], v[120:123], v[176:179], v[80:83]\n" \
"v_fmac_f32 v52, s56, v56\n" \
"v_fmac_f32 v53, s56, v57\n" \
"v_mfma_f32_16x16x32_f16 v[92:95], v[124:127], v[176:179], v[92:95]\n" \
"v_fmac_f32 v54, s56, v58\n" \
"v_fmac_f32 v55, s56, v59\n" \
"s_waitcnt lgkmcnt(2)\n" \
"v_mfma_f32_16x16x32_f16 v[72:75], v[128:131], v[160:163], v[72:75]\n" \
"ds_read_b128 v[176:179], v241 offset:8192\n" \
"v_fmac_f32 v48, s57, v252\n" \
"v_fmac_f32 v49, s57, v253\n" \
"v_mfma_f32_16x16x32_f16 v[84:87], v[132:135], v[160:163], v[84:87]\n" \
"v_pk_add_f16 v160, v160, v164\n" \
"v_pk_add_f16 v161, v161, v165\n" \
"v_fmac_f32 v50, s57, v254\n" \
"v_fmac_f32 v51, s57, v255\n" \
"v_mfma_f32_16x16x32_f16 v[76:79], v[152:155], v[164:167], v[76:79]\n" \
"v_pk_add_f16 v162, v162, v166\n" \
"v_pk_add_f16 v163, v163, v167\n" \
"v_add_f32 v252, v64, v68\n" \
"v_add_f32 v253, v65, v69\n" \
"v_mfma_f32_16x16x32_f16 v[88:91], v[156:159], v[164:167], v[88:91]\n" \
"ds_read_b128 v[180:183], v241 offset:8704\n" \
"v_add_f32 v254, v66, v70\n" \
"v_add_f32 v255, v67, v71\n" \
"v_mfma_f32_16x16x32_f16 v[80:83], v[232:235], v[160:163], v[80:83]\n" \
"v_fmac_f32 v64, s56, v68\n" \
"v_fmac_f32 v65, s56, v69\n" \
"v_mfma_f32_16x16x32_f16 v[92:95], v[236:239], v[160:163], v[92:95]\n" \
"v_fmac_f32 v66, s56, v70\n" \
"v_fmac_f32 v67, s56, v71\n" \
"s_waitcnt lgkmcnt(2)\n" \
"v_mfma_f32_16x16x32_f16 v[72:75], v[184:187], v[168:171], v[72:75]\n" \
"ds_read_b128 v[160:163], v240 offset:40960\n" \
"v_fmac_f32 v60, s57, v252\n" \
"v_fmac_f32 v61, s57, v253\n" \
"v_mfma_f32_16x16x32_f16 v[84:87], v[188:191], v[168:171], v[84:87]\n" \
"v_pk_add_f16 v168, v168, v172\n" \
"v_pk_add_f16 v169, v169, v173\n" \
"v_fmac_f32 v62, s57, v254\n" \
"v_fmac_f32 v63, s57, v255\n" \
"v_mfma_f32_16x16x32_f16 v[76:79], v[192:195], v[172:175], v[76:79]\n" \
"v_pk_add_f16 v170, v170, v174\n" \
"v_pk_add_f16 v171, v171, v175\n" \
"v_cvt_pk_bf16_f32 v56, v48, v49\n" \
"v_cvt_pk_bf16_f32 v68, v52, v53\n" \
"v_mfma_f32_16x16x32_f16 v[88:91], v[196:199], v[172:175], v[88:91]\n" \
"ds_read_b128 v[164:167], v240 offset:41472\n" \
"v_cvt_pk_bf16_f32 v57, v50, v51\n" \
"v_cvt_pk_bf16_f32 v69, v54, v55\n" \
"v_mfma_f32_16x16x32_f16 v[80:83], v[200:203], v[168:171], v[80:83]\n" \
"v_cvt_pk_bf16_f32 v58, v60, v61\n" \
"v_cvt_pk_bf16_f32 v70, v64, v65\n" \
"v_mfma_f32_16x16x32_f16 v[92:95], v[204:207], v[168:171], v[92:95]\n" \
"v_cvt_pk_bf16_f32 v59, v62, v63\n" \
"v_cvt_pk_bf16_f32 v71, v66, v67\n" \
"s_waitcnt lgkmcnt(2)\n" \
"v_mfma_f32_16x16x32_f16 v[72:75], v[208:211], v[176:179], v[72:75]\n" \
"ds_read_b128 v[168:171], v240 offset:0\n" \
"s_not_b64 vcc, vcc\n" \
"v_cndmask_b32_dpp v48, v68, v56, vcc quad_perm:[1,0,3,2] row_mask:0xf bank_mask:0xf\n" \
"v_mfma_f32_16x16x32_f16 v[84:87], v[212:215], v[176:179], v[84:87]\n" \
"v_pk_add_f16 v176, v176, v180\n" \
"v_pk_add_f16 v177, v177, v181\n" \
"v_cndmask_b32_dpp v49, v69, v57, vcc quad_perm:[1,0,3,2] row_mask:0xf bank_mask:0xf\n" \
"v_cndmask_b32_dpp v50, v70, v58, vcc quad_perm:[1,0,3,2] row_mask:0xf bank_mask:0xf\n" \
"v_mfma_f32_16x16x32_f16 v[76:79], v[216:219], v[180:183], v[76:79]\n" \
"v_pk_add_f16 v178, v178, v182\n" \
"v_pk_add_f16 v179, v179, v183\n" \
"v_cndmask_b32_dpp v51, v71, v59, vcc quad_perm:[1,0,3,2] row_mask:0xf bank_mask:0xf\n" \
"buffer_store_dwordx4 v[48:51], v245, s[40:43], s71 offen sc1\n" \
"v_mfma_f32_16x16x32_f16 v[88:91], v[220:223], v[180:183], v[88:91]\n" \
"ds_read_b128 v[172:175], v240 offset:512\n" \
"s_not_b64 vcc, vcc\n" \
"s_add_i32 s85, s71, 0x10000\n" \
"v_mfma_f32_16x16x32_f16 v[80:83], v[224:227], v[176:179], v[80:83]\n" \
"v_cndmask_b32_dpp v60, v56, v68, vcc quad_perm:[1,0,3,2] row_mask:0xf bank_mask:0xf\n" \
"v_cndmask_b32_dpp v61, v57, v69, vcc quad_perm:[1,0,3,2] row_mask:0xf bank_mask:0xf\n" \
"v_mfma_f32_16x16x32_f16 v[92:95], v[228:231], v[176:179], v[92:95]\n" \
"v_cndmask_b32_dpp v62, v58, v70, vcc quad_perm:[1,0,3,2] row_mask:0xf bank_mask:0xf\n" \
"v_cndmask_b32_dpp v63, v59, v71, vcc quad_perm:[1,0,3,2] row_mask:0xf bank_mask:0xf\n" \
"buffer_store_dwordx4 v[60:63], v245, s[40:43], s85 offen sc1\n" \
"s_waitcnt lgkmcnt(1)\n" \
"v_mfma_f32_16x16x32_f16 v[96:99], v[136:139], v[160:163], v[96:99]\n" \
"ds_read_b128 v[176:179], v240 offset:20480\n" \
"v_mfma_f32_16x16x32_f16 v[108:111], v[140:143], v[160:163], v[108:111]\n" \
"v_pk_add_f16 v160, v160, v164\n" \
"v_pk_add_f16 v161, v161, v165\n" \
"v_mfma_f32_16x16x32_f16 v[100:103], v[144:147], v[164:167], v[100:103]\n" \
"v_pk_add_f16 v162, v162, v166\n" \
"v_pk_add_f16 v163, v163, v167\n" \
"v_add_f32 v252, v76, v80\n" \
"v_add_f32 v253, v77, v81\n" \
"v_mfma_f32_16x16x32_f16 v[112:115], v[148:151], v[164:167], v[112:115]\n" \
"ds_read_b128 v[180:183], v240 offset:20992\n" \
"v_add_f32 v254, v78, v82\n" \
"v_add_f32 v255, v79, v83\n" \
"v_mfma_f32_16x16x32_f16 v[104:107], v[120:123], v[160:163], v[104:107]\n" \
"v_fmac_f32 v76, s56, v80\n" \
"v_fmac_f32 v77, s56, v81\n" \
"v_mfma_f32_16x16x32_f16 v[116:119], v[124:127], v[160:163], v[116:119]\n" \
"v_fmac_f32 v78, s56, v82\n" \
"v_fmac_f32 v79, s56, v83\n" \
"v_mfma_f32_16x16x32_f16 v[96:99], v[128:131], v[168:171], v[96:99]\n" \
"ds_read_b128 v[160:163], v241 offset:0\n" \
"v_fmac_f32 v72, s57, v252\n" \
"v_fmac_f32 v73, s57, v253\n" \
"v_mfma_f32_16x16x32_f16 v[108:111], v[132:135], v[168:171], v[108:111]\n" \
"s_waitcnt lgkmcnt(2)\n" \
"v_pk_add_f16 v168, v168, v172\n" \
"v_pk_add_f16 v169, v169, v173\n" \
"v_fmac_f32 v74, s57, v254\n" \
"v_fmac_f32 v75, s57, v255\n" \
"v_mfma_f32_16x16x32_f16 v[100:103], v[152:155], v[172:175], v[100:103]\n" \
"v_pk_add_f16 v170, v170, v174\n" \
"v_pk_add_f16 v171, v171, v175\n" \
"v_add_f32 v252, v88, v92\n" \
"v_add_f32 v253, v89, v93\n" \
"v_mfma_f32_16x16x32_f16 v[112:115], v[156:159], v[172:175], v[112:115]\n" \
"ds_read_b128 v[164:167], v241 offset:512\n" \
"v_add_f32 v254, v90, v94\n" \
"v_add_f32 v255, v91, v95\n" \
"v_mfma_f32_16x16x32_f16 v[104:107], v[232:235], v[168:171], v[104:107]\n" \
"v_fmac_f32 v88, s56, v92\n" \
"v_fmac_f32 v89, s56, v93\n" \
"v_mfma_f32_16x16x32_f16 v[116:119], v[236:239], v[168:171], v[116:119]\n" \
"v_fmac_f32 v90, s56, v94\n" \
"v_fmac_f32 v91, s56, v95\n" \
"v_mfma_f32_16x16x32_f16 v[96:99], v[184:187], v[176:179], v[96:99]\n" \
"v_fmac_f32 v84, s57, v252\n" \
"v_fmac_f32 v85, s57, v253\n" \
"v_fmac_f32 v86, s57, v254\n" \
"v_mfma_f32_16x16x32_f16 v[108:111], v[188:191], v[176:179], v[108:111]\n" \
"s_waitcnt lgkmcnt(1)\n" \
"v_pk_add_f16 v176, v176, v180\n" \
"v_pk_add_f16 v177, v177, v181\n" \
"v_fmac_f32 v87, s57, v255\n" \
"v_mfma_f32_16x16x32_f16 v[100:103], v[192:195], v[180:183], v[100:103]\n" \
"v_pk_add_f16 v178, v178, v182\n" \
"v_pk_add_f16 v179, v179, v183\n" \
"v_cvt_pk_bf16_f32 v80, v72, v73\n" \
"v_cvt_pk_bf16_f32 v92, v76, v77\n" \
"v_mfma_f32_16x16x32_f16 v[112:115], v[196:199], v[180:183], v[112:115]\n" \
"v_cvt_pk_bf16_f32 v81, v74, v75\n" \
"v_cvt_pk_bf16_f32 v93, v78, v79\n" \
"v_mfma_f32_16x16x32_f16 v[104:107], v[200:203], v[176:179], v[104:107]\n" \
"v_cvt_pk_bf16_f32 v82, v84, v85\n" \
"v_cvt_pk_bf16_f32 v94, v88, v89\n" \
"v_mfma_f32_16x16x32_f16 v[116:119], v[204:207], v[176:179], v[116:119]\n" \
"v_cvt_pk_bf16_f32 v83, v86, v87\n" \
"v_cvt_pk_bf16_f32 v95, v90, v91\n" \
"v_mfma_f32_16x16x32_f16 v[96:99], v[208:211], v[160:163], v[96:99]\n" \
"s_not_b64 vcc, vcc\n" \
"v_cndmask_b32_dpp v72, v92, v80, vcc quad_perm:[1,0,3,2] row_mask:0xf bank_mask:0xf\n" \
"v_cndmask_b32_dpp v73, v93, v81, vcc quad_perm:[1,0,3,2] row_mask:0xf bank_mask:0xf\n" \
"v_mfma_f32_16x16x32_f16 v[108:111], v[212:215], v[160:163], v[108:111]\n" \
"s_waitcnt lgkmcnt(0)\n" \
"v_pk_add_f16 v160, v160, v164\n" \
"v_pk_add_f16 v161, v161, v165\n" \
"v_cndmask_b32_dpp v74, v94, v82, vcc quad_perm:[1,0,3,2] row_mask:0xf bank_mask:0xf\n" \
"v_mfma_f32_16x16x32_f16 v[100:103], v[216:219], v[164:167], v[100:103]\n" \
"v_pk_add_f16 v162, v162, v166\n" \
"v_pk_add_f16 v163, v163, v167\n" \
"v_cndmask_b32_dpp v75, v95, v83, vcc quad_perm:[1,0,3,2] row_mask:0xf bank_mask:0xf\n" \
"buffer_store_dwordx4 v[72:75], v245, s[40:43], s72 offen sc1\n" \
"v_mfma_f32_16x16x32_f16 v[112:115], v[220:223], v[164:167], v[112:115]\n" \
"s_not_b64 vcc, vcc\n" \
"s_add_i32 s85, s72, 0x10000\n" \
"v_mfma_f32_16x16x32_f16 v[104:107], v[224:227], v[160:163], v[104:107]\n" \
"v_cndmask_b32_dpp v84, v80, v92, vcc quad_perm:[1,0,3,2] row_mask:0xf bank_mask:0xf\n" \
"v_cndmask_b32_dpp v85, v81, v93, vcc quad_perm:[1,0,3,2] row_mask:0xf bank_mask:0xf\n" \
"v_mfma_f32_16x16x32_f16 v[116:119], v[228:231], v[160:163], v[116:119]\n" \
"v_cndmask_b32_dpp v86, v82, v94, vcc quad_perm:[1,0,3,2] row_mask:0xf bank_mask:0xf\n" \
"v_cndmask_b32_dpp v87, v83, v95, vcc quad_perm:[1,0,3,2] row_mask:0xf bank_mask:0xf\n" \
"buffer_store_dwordx4 v[84:87], v245, s[40:43], s85 offen sc1\n" \
"s_nop 7\n" \
"v_add_f32 v252, v100, v104\n" \
"v_add_f32 v253, v101, v105\n" \
"v_add_f32 v254, v102, v106\n" \
"v_add_f32 v255, v103, v107\n" \
"v_fmac_f32 v100, s56, v104\n" \
"v_fmac_f32 v101, s56, v105\n" \
"v_fmac_f32 v102, s56, v106\n" \
"v_fmac_f32 v103, s56, v107\n" \
"v_fmac_f32 v96, s57, v252\n" \
"v_fmac_f32 v97, s57, v253\n" \
"v_fmac_f32 v98, s57, v254\n" \
"v_fmac_f32 v99, s57, v255\n" \
"v_add_f32 v252, v112, v116\n" \
"v_add_f32 v253, v113, v117\n" \
"v_add_f32 v254, v114, v118\n" \
"v_add_f32 v255, v115, v119\n" \
"v_fmac_f32 v112, s56, v116\n" \
"v_fmac_f32 v113, s56, v117\n" \
"v_fmac_f32 v114, s56, v118\n" \
"v_fmac_f32 v115, s56, v119\n" \
"v_fmac_f32 v108, s57, v252\n" \
"v_fmac_f32 v109, s57, v253\n" \
"v_fmac_f32 v110, s57, v254\n" \
"v_fmac_f32 v111, s57, v255\n" \
"v_cvt_pk_bf16_f32 v104, v96, v97\n" \
"v_cvt_pk_bf16_f32 v116, v100, v101\n" \
"v_cvt_pk_bf16_f32 v105, v98, v99\n" \
"v_cvt_pk_bf16_f32 v117, v102, v103\n" \
"v_cvt_pk_bf16_f32 v106, v108, v109\n" \
"v_cvt_pk_bf16_f32 v118, v112, v113\n" \
"v_cvt_pk_bf16_f32 v107, v110, v111\n" \
"v_cvt_pk_bf16_f32 v119, v114, v115\n" \
"s_not_b64 vcc, vcc\n" \
"v_cndmask_b32_dpp v96, v116, v104, vcc quad_perm:[1,0,3,2] row_mask:0xf bank_mask:0xf\n" \
"v_cndmask_b32_dpp v97, v117, v105, vcc quad_perm:[1,0,3,2] row_mask:0xf bank_mask:0xf\n" \
"v_cndmask_b32_dpp v98, v118, v106, vcc quad_perm:[1,0,3,2] row_mask:0xf bank_mask:0xf\n" \
"v_cndmask_b32_dpp v99, v119, v107, vcc quad_perm:[1,0,3,2] row_mask:0xf bank_mask:0xf\n" \
"buffer_store_dwordx4 v[96:99], v245, s[40:43], s73 offen sc1\n" \
"s_not_b64 vcc, vcc\n" \
"s_add_i32 s85, s73, 0x10000\n" \
"v_cndmask_b32_dpp v108, v104, v116, vcc quad_perm:[1,0,3,2] row_mask:0xf bank_mask:0xf\n" \
"v_cndmask_b32_dpp v109, v105, v117, vcc quad_perm:[1,0,3,2] row_mask:0xf bank_mask:0xf\n" \
"v_cndmask_b32_dpp v110, v106, v118, vcc quad_perm:[1,0,3,2] row_mask:0xf bank_mask:0xf\n" \
"v_cndmask_b32_dpp v111, v107, v119, vcc quad_perm:[1,0,3,2] row_mask:0xf bank_mask:0xf\n" \
"buffer_store_dwordx4 v[108:111], v245, s[40:43], s85 offen sc1\n" \
"s_waitcnt vmcnt(0)\n" \
"s_branch .Lcf_e\n" \
".Ls12_%=:\n" \
"s_mov_b32 s87, 0x200000\n" \
"s_mov_b32 s86, 0x8004000\n" \
".Lt12_%=:\n" \
"s_sleep 3\n" \
"buffer_load_dword v253, v252, s[36:39], s86 offen sc1\n" \
"s_waitcnt vmcnt(0)\n" \
"s_sub_u32 s87, s87, 1\n" \
"s_cbranch_scc1 .Lr12_%=\n" \
"v_cmp_eq_u32 vcc, 0, v253\n" \
"s_cbranch_vccnz .Lt12_%=\n" \
"s_branch .Lr12_%=\n" \
".Ls16_%=:\n" \
"s_mov_b32 s87, 0x200000\n" \
"s_mov_b32 s86, 0x800c000\n" \
".Lt16_%=:\n" \
"s_sleep 3\n" \
"buffer_load_dword v253, v252, s[36:39], s86 offen sc1\n" \
"s_waitcnt vmcnt(0)\n" \
"s_sub_u32 s87, s87, 1\n" \
"s_cbranch_scc1 .Lr16_%=\n" \
"v_cmp_eq_u32 vcc, 0, v253\n" \
"s_cbranch_vccnz .Lt16_%=\n" \
"s_branch .Lr16_%=\n" \
".Ls110_%=:\n" \
"s_mov_b32 s87, 0x200000\n" \
"s_mov_b32 s86, 0x8014000\n" \
".Lt110_%=:\n" \
"s_sleep 3\n" \
"buffer_load_dword v253, v252, s[36:39], s86 offen sc1\n" \
"s_waitcnt vmcnt(0)\n" \
"s_sub_u32 s87, s87, 1\n" \
"s_cbranch_scc1 .Lr110_%=\n" \
"v_cmp_eq_u32 vcc, 0, v253\n" \
"s_cbranch_vccnz .Lt110_%=\n" \
"s_branch .Lr110_%=\n" \
""
#define CLOBBERS "v0", "v1", "v2", "v3", "v4", "v5", "v6", "v7", "v8", "v9", "v10", "v11", "v12", "v13", "v14", "v15", "v16", "v17", "v18", "v19", "v20", "v21", "v22", "v23", "v24", "v25", "v26", "v27", "v28", "v29", "v30", "v31", "v32", "v33", "v34", "v35", "v36", "v37", "v38", "v39", "v40", "v41", "v42", "v43", "v44", "v45", "v46", "v47", "v48", "v49", "v50", "v51", "v52", "v53", "v54", "v55", "v56", "v57", "v58", "v59", "v60", "v61", "v62", "v63", "v64", "v65", "v66", "v67", "v68", "v69", "v70", "v71", "v84", "v85", "v86", "v87", "v88", "v89", "v90", "v91", "v92", "v93", "v94", "v95", "v120", "v121", "v122", "v123", "v124", "v125", "v126", "v127", "v128", "v129", "v130", "v131", "v132", "v133", "v134", "v135", "v136", "v137", "v138", "v139", "v140", "v141", "v142", "v143", "v144", "v145", "v146", "v147", "v148", "v149", "v150", "v151", "v152", "v153", "v154", "v155", "v156", "v157", "v158", "v159", "v160", "v161", "v162", "v163", "v164", "v165", "v166", "v167", "v168", "v169", "v170", "v171", "v172", "v173", "v174", "v175", "v176", "v177", "v178", "v179", "v180", "v181", "v182", "v183", "v184", "v185", "v186", "v187", "v188", "v189", "v190", "v191", "v194", "v195", "v196", "v197", "v198", "v199", "v200", "v201", "v202", "v203", "v204", "v205", "v206", "v207", "v253", "v254", "v255", "s80", "s81", "s82", "s83", "s84", "s85", "s86", "s87", "s88", "s89", "s90", "s91", "s92", "s75", "s94", "s95"
template <int NM>
__device__ __forceinline__ void run1(Ctx& cx, const unsigned short* __restrict__ x, const unsigned short* __restrict__ w, RQ& rq) {
  unsigned xs;
  phase1<NM>(cx, x, w, rq, xs, rq.vb, [&]() __attribute__((always_inline)) {
  static_assert(NS - st_first(PG) == 4 && NH == 6 && NST == 3 && NQW == 11 && RAWL == 110592, "constants of the generated code");
  const unsigned bid = cx.vb, idx = bid >> 3, slot = (bid & 7) * 4 + (idx >> 3), lane = cx.lane4 >> 2, h = (idx >> 2) & 1, wv = cx.wv;
  const unsigned lds0 = (unsigned)(uintptr_t)(lds_void*)cx.smem;
  unsigned own = ((wv ^ idx) & 3) - 1u;
  asm volatile("s_ashr_i32 %0, %0, 31" : "+s"(own) :: "scc");
  unsigned* sa = rq.s, *sb = rq.s + 16;
  sa[0] = cx.ldg; sa[1] = cx.ldf; sa[2] = cx.sg; sa[3] = cx.sf;
#pragma unroll
  for (int j = 0; j < 4; ++j) {
    sa[4 + j] = ((bid + j) & 3) * 16384;
    sa[8 + j] = ((bid + j) & 3) * 256;
  }
  if (NM == 5) {
    sa[8] = lds0 + RAWL + 32768 + (wv & 1) * 8192 + (wv & 2) * 2048;
    sa[11] = (30 + (wv & 1)) * SBLK + (wv & 2) * 10240;
    const unsigned long long pace = wv == 7 ? (bid & 1 ? bytes(125, 119, 90, 88, 86, 84, 82) : bytes(137, 131, 109, 90, 88, 86, 84)) : 0ull;
    sa[9] = (unsigned)pace;
    sb[14] = (unsigned)(pace >> 32);
    sa[10] = lds0 + CORD + (wv & 3) * 1024;
  }
  sa[12] = min(slot, 1u) * 0xbf800000u;
  sa[13] = min(slot, 1u) * 0xbf000000u;
  sa[14] = sa[15] = own;
#pragma unroll
  for (int e = 0; e < NST + NH; ++e) {
    const unsigned n = 10 - NH - NST + e, blk = (xs & 255u) + n % 5;
    sb[e] = (xs & ~255u) + (n / 5) * (8 * SBLK) + (blk + (blk & 192u)) * 128u;
  }
#pragma unroll
  for (int m = 0; m < 5; ++m) {
    const unsigned t = NM == 5 ? (m + 1) % 5 : m;
    sb[9 + m] = t * (16 * 65536) + ((h * 10 + (wv >> 2) * 5 + (wv & 2) + t) & 2) * 4096;
  }
#pragma unroll
  for (int i = 0; i < 31; ++i) rq.s[i] = __builtin_amdgcn_readfirstlane(rq.s[i]);
  const unsigned prd = cx.prd + (RAWL - RAWB);
  const unsigned yo = (h * NBLK + (wv >> 2) * 80 + (lane & 14)) * 65536 + ((idx & 3) * 4 + (wv & 1)) * 4096 + (lane >> 4) * 16 + (2 * slot + (lane & 1)) * 64;
  unsigned* v = rq.v;
  v[0] = cx.voff; v[1] = cx.voff5; v[2] = cx.poff; v[3] = cx.xo; v[4] = prd; v[5] = prd ^ 32u; v[6] = prd ^ 64u; v[7] = prd ^ 96u;
  v[8] = cx.xbase; v[9] = cx.wbase; v[10] = cx.wbase ^ 32u; v[11] = cx.fvo; v[12] = lane * 16; v[13] = yo; v[14] = cx.pwr + (RAWL - RAWB);
  if (NM == 6) {
    v[15] = cx.crd;
    v[16] = CBUF + min(lane & 15, 10u) * 272 + wv * 64 + (cx.lane4 >> 6) * 16;
    v[17] = CORD + lane * 16;
    v[18] = (bid & 7) * 1024 + (idx & 3) * 256 + lane * 4;
  } else {
    const unsigned co = (((bid >> 3) & 3) * 2 + ((bid >> 2) & 1)) * 128u + (cx.lane4 & 127u);
    const unsigned long long p = (unsigned long long)((const char*)(cx.lane4 < 128 ? cx.gamma : cx.beta) + co);
    v[15] = 0u;
    v[16] = (unsigned)p;
    v[17] = (unsigned)(p >> 32);
    v[18] = cx.fvo;
  }
  rq.s[31] = __builtin_amdgcn_readfirstlane(wv & 2 ? cx.pft + (wv & 1) * 8192 : cx.pfo - 8192 - (wv & 1) * 8192);
#pragma unroll
  for (int i = 0; i < 32; ++i) asm volatile("" : "+s"(rq.s[i]));
#pragma unroll
  for (int i = 0; i < 19; ++i) asm volatile("" : "+v"(rq.v[i]));
  SB0;
  });
}

template <int CTRL>
__device__ __forceinline__ float bfly(float a, float b, bool hi) {
  const float give = hi ? a : b, keep = hi ? b : a;
  return keep + __builtin_bit_cast(float, __builtin_amdgcn_update_dpp(0, __builtin_bit_cast(int, give), CTRL, 0xf, 0xf, false));
}
template <int CTRL>
__device__ __forceinline__ float dpp_add(float v) {
  asm("" : "+v"(v));
  return v + __builtin_bit_cast(float, __builtin_amdgcn_update_dpp(0, __builtin_bit_cast(int, v), CTRL, 0xf, 0xf, true));
}
__device__ __forceinline__ void row16_sum(float (&v)[8]) {
#pragma unroll
  for (int r = 0; r < 8; ++r) v[r] = dpp_add<0x128>(v[r]);
  SB0;
#pragma unroll
  for (int r = 0; r < 8; ++r) v[r] = dpp_add<0x124>(v[r]);
  SB0;
#pragma unroll
  for (int r = 0; r < 8; ++r) v[r] = dpp_add<0x122>(v[r]);
  SB0;
#pragma unroll
  for (int r = 0; r < 8; ++r) v[r] = dpp_add<0x121>(v[r]);
#pragma unroll
  for (int r = 0; r < 8; ++r) asm("" : "+v"(v[r]));
  SB0;
}

extern "C" __global__ __launch_bounds__(512, 2) void conv_fft(char* ws, const unsigned short* __restrict__ x,
                                                   const unsigned short* __restrict__ w,
                                                   const unsigned short* __restrict__ gamma,
                                                   const unsigned short* __restrict__ beta,
                                                   unsigned short* __restrict__ out) {
  __shared__ __attribute__((aligned(16))) char smem[160 * 1024];
  const unsigned lds0 = (unsigned)(uintptr_t)(lds_void*)smem;
  const int wv = __builtin_amdgcn_readfirstlane(threadIdx.x >> 6);
  Ctx cx;
  {
    unsigned len;
    asm volatile("s_getpc_b64 %0\n.Lcf_a:\n\ts_mov_b32 %1, .Lcf_b-.Lcf_a\n\ts_mov_b32 %2, .Lcf_t-.Lcf_a" : "=s"(cx.pc), "=s"(len), "=s"(cx.pft));
    cx.wv = wv;
    cx.pfo = len;
    unsigned xcc;
    asm volatile("s_getreg_b32 %0, hwreg(HW_REG_XCC_ID, 0, 3)" : "=s"(xcc));
    unsigned id = blockIdx.x;
    asm volatile("" : "+s"(id));
    cx.id = id;
    cx.vb = (int)((id & 248u) | (id & 256u ? (id >> 10) & 7u : xcc));
    const char* pb = ws + WS_PUB;
    u32x4 sx, sw;
    if ((id | wv) == 0) {
      const unsigned l = threadIdx.x;
      const u32x4 z = {0u, 0u, 0u, 0u};
      {
#pragma unroll
        for (int k = 0; k < 2; ++k)
          asm volatile("global_store_dwordx4 %0, %1, %2 sc1" :: "v"(32768 + (l & 7) * 16 + (l & 8) * 512 + (l & 16) * 1024 + (l & 32) * 32 + k * 8192), "v"(z), "s"(pb) : "memory");
      }
      {
        const unsigned so = l * (31u * 1024u + 16u) + (4u << 20);
        asm volatile("global_store_dwordx4 %2, %3, %4 sc1\n\tglobal_load_dwordx4 %0, %5, %6\n\tglobal_load_dwordx4 %1, %7, %8"
                     : "=&v"(sx), "=&v"(sw) : "v"(l * 16 + 1024), "v"(z), "s"(pb), "v"(so + (l & 3u) * (8u << 20)), "s"(x), "v"(so + (l & 1u) * (6u << 20)), "s"(w) : "memory");
      }
    }
    if (id) code_touch(cx);
    SB0;
    issue_x(0, wv, x_addr(cx.vb, 0, wv), x_rsrc(x, cx.vb), lds0);
    SB0;
    if ((id | wv) == 0) {
      wait_vm<9>();
      asm volatile("" : "+v"(sx), "+v"(sw));
      unsigned m = expo(amax(amax(amax(amax(0, sx[0]), sx[1]), sx[2]), sx[3])) | expo(amax(amax(amax(amax(0, sw[0]), sw[1]), sw[2]), sw[3])) << 16;
      m = amax(m, (unsigned)__builtin_amdgcn_update_dpp(0, (int)m, 0x128, 0xf, 0xf, false));
      m = amax(m, (unsigned)__builtin_amdgcn_update_dpp(0, (int)m, 0x124, 0xf, 0xf, false));
      m = amax(m, (unsigned)__builtin_amdgcn_update_dpp(0, (int)m, 0x122, 0xf, 0xf, false));
      m = amax(m, (unsigned)__builtin_amdgcn_update_dpp(0, (int)m, 0x121, 0xf, 0xf, false));
      unsigned ex = 28, ew = 28;
#pragma unroll
      for (int l = 0; l < 64; l += 16) {
        const unsigned v = (unsigned)__builtin_amdgcn_readlane((int)m, l);
        ex = max(ex + 2, v & 255u) - 2;
        ew = max(ew + 3, v >> 16) - 3;
      }
      const unsigned long long tg = __builtin_amdgcn_s_memrealtime();
      const unsigned lo = (unsigned)tg << 4 | xcc << 1, hi = ((unsigned)(tg >> 28) & 0xffffu) | min(ex, 220u) << 16 | min(ew, 220u) << 24;
      asm volatile("global_store_dwordx4 %0, %1, %2 sc1\n\ts_nop 1" :: "v"(threadIdx.x * 16), "v"(u32x4{lo, hi, ~lo, ~hi}), "s"(pb) : "memory");
      cx.tag[0] = lo;
      cx.tag[1] = hi;
    }
  }
  cx.smem = smem;
  cx.ws = ws;
  cx.gamma = gamma;
  cx.beta = beta;
  bf16x8 gb[8];
  {
    RQ rq;
    if (wv < 4) run1<6>(cx, x, w, rq); else run1<5>(cx, x, w, rq);
    const unsigned long long tpc = cx.pc + rq.s[31];
    __builtin_amdgcn_s_barrier();
    asm volatile("s_cmp_gt_u32 %[wv], 3\n\ts_cbranch_scc1 .Lcf_1\n" LOOP0 ".Lcf_1:\n" LOOP1 ".Lcf_e:"
      :
        "+{v232}"(rq.v[0]), "+{v233}"(rq.v[1]), "+{v234}"(rq.v[2]),
        "+{v235}"(rq.v[3]), "+{v236}"(rq.v[4]), "+{v237}"(rq.v[5]), "+{v238}"(rq.v[6]), "+{v239}"(rq.v[7]), "+{v[72:75]}"(rq.h[0]),
        "+{v[76:79]}"(rq.h[1]), "+{v[80:83]}"(rq.h[2]), "+{v[96:97]}"(rq.raw[0]), "+{v[98:99]}"(rq.raw[1]), "+{v[100:101]}"(rq.raw[2]),
        "+{v[102:103]}"(rq.raw[3]), "+{v[104:105]}"(rq.raw[4]), "+{v[106:107]}"(rq.raw[5]), "+{v[108:109]}"(rq.raw[6]), "+{v[110:111]}"(rq.raw[7]),
        "+{v[112:113]}"(rq.raw[8]), "+{v[114:115]}"(rq.raw[9]), "+{v[116:117]}"(rq.raw[10]), "+{v241}"(rq.v[9]), "+{v243}"(rq.v[11]), "+{v244}"(rq.v[12]), "+{v252}"(rq.v[18]), "+{v[192:193]}"(rq.vb),
        "+{v[208:211]}"(rq.h[3]), "+{v[212:215]}"(rq.h[4]), "+{v[216:219]}"(rq.h[5]), "+{v[220:223]}"(rq.h[6]), "+{v[224:227]}"(rq.h[7]), "+{v[228:231]}"(rq.h[8]),
        "+{v246}"(rq.v[14]), "+{v247}"(rq.v[15])
      :
        "{v240}"(rq.v[8]), "{v242}"(rq.v[10]), "{v245}"(rq.v[13]),
        "{v248}"(rq.v[16]), "{v249}"(rq.v[17]),
            "{s44}"(rq.s[0]), "{s45}"(rq.s[1]),
        "{s46}"(rq.s[2]), "{s47}"(rq.s[3]), "{s48}"(rq.s[4]), "{s49}"(rq.s[5]), "{s50}"(rq.s[6]), "{s51}"(rq.s[7]), "{s52}"(rq.s[8]), "{s53}"(rq.s[9]),
        "{s54}"(rq.s[10]), "{s55}"(rq.s[11]), "{s56}"(rq.s[12]), "{s57}"(rq.s[13]), "{s58}"(rq.s[14]), "{s59}"(rq.s[15]), "{s60}"(rq.s[16]),
        "{s61}"(rq.s[17]), "{s62}"(rq.s[18]), "{s63}"(rq.s[19]), "{s64}"(rq.s[20]), "{s65}"(rq.s[21]), "{s66}"(rq.s[22]), "{s67}"(rq.s[23]),
        "{s68}"(rq.s[24]), "{s69}"(rq.s[25]), "{s70}"(rq.s[26]), "{s71}"(rq.s[27]), "{s72}"(rq.s[28]), "{s73}"(rq.s[29]), "{s74}"(rq.s[30]),
        "{s[36:39]}"(cx.rs), "{s[40:43]}"(make_rsrc(cx.ws + WS_Y, ROWS * 65536)), "{s[76:77]}"(&TAB.G[0][0][0][0]), "{s[78:79]}"(tpc), [wv] "s"(wv)
      : "memory", "vcc", "scc", "v118", "v119", CLOBBERS);
  }
  ws = cx.ws;
  const int bid = cx.vb, idx = bid >> 3;
  const int slot = (bid & 7) * 4 + (idx >> 3), b = (idx >> 2) & 1, nt = idx & 3;
  const int lane = (int)__builtin_amdgcn_mbcnt_hi(~0u, __builtin_amdgcn_mbcnt_lo(~0u, 0u));
  __builtin_amdgcn_s_barrier();
  gu32* fl = (gu32*)(ws + WS_GRP + nt * 256);
  if (wv == 1 && lane < 8) __hip_atomic_store(fl + (lane + (lane & 4)) * 256 + b * 32 + slot, 1u, RLX_AGENT);
  const int c64i = nt * 2 + ((bid >> 2) & 1), rg = (bid & 1) * 8 + (slot & 1) * 4 + (bid & 2) + (idx >> 4);
  const int hw = wv >> 2, wp = hw ? 7 - wv : wv, cg = c64i * 2 + hw;
  const int bs = 10 * rg;
  const int ntk = wp < 2 ? 3 : 2;
  const int g = lane >> 4, qq = (lane >> 2) & 3, pp = lane & 3, fr = lane & 15;
  const int ch0 = cg * 32 + 8 * g;
  const int fi = b * 32 + (lane & 31);
  const unsigned cnt = lds0 + 8 * 12288 + 3072 + hw * 8;
  if (wv == 2) asm volatile("ds_write_b32 %0, %1\n\ts_waitcnt lgkmcnt(0)" :: "v"(cnt + (lane & 3) * 4), "v"(0u) : "memory");
#pragma unroll
  for (int i = 0; i < 8; ++i) gb[i] = *reinterpret_cast<const bf16x8*>(smem + i * 1024 + lane * 16);
  const int l0 = HOP * (bs + wp) + fr, le = (fr >> 1) < ntk ? HOP * (bs + wp + 4 * (fr >> 1)) + 48 + (fr & 1) : 1 << 20;
  const int lend = min(HOP * (bs + 10), L_);
  f32x4 nk, rk;
  float rn = 1.0f / (float)(lend - HOP * bs);
#pragma unroll
  for (int k = 0; k < 4; ++k) {
    nk[k] = (float)(2 * HOP + min(max(lend - HOP * (bs + k + 8), 0), HOP));
    rk[k] = 1.0f / nk[k];
  }
  asm volatile("" : "+v"(nk), "+v"(rk), "+v"(rn));
  f32x4 cin[2];
#pragma unroll
    for (int s = 0; s < 2; ++s) {
      cin[s] = *reinterpret_cast<const f32x4*>(smem + CBUF + min(wp + 4 * (fr >> 1), 10) * 272 + hw * 128 + g * 32 + s * 16);
#pragma unroll
      for (int r = 0; r < 4; ++r) cin[s][r] = fr & 1 ? __builtin_amdgcn_ldexpf(cin[s][r], cx.es) : 0.f;
    }
    asm volatile("" : "+v"(cin[0]), "+v"(cin[1]));
#pragma unroll
  for (int i = 0; i < 8; i += 4) asm volatile("" : "+v"(gb[i]), "+v"(gb[i + 1]), "+v"(gb[i + 2]), "+v"(gb[i + 3]));
  SB0;
  if (wv == 0) spin_flags((const char*)(fl + ((bid & 7) + (bid & 4)) * 256 + (fi ^ (fi == b * 32 + slot))));
  __builtin_amdgcn_s_barrier();
  char* my = smem + wv * 12288;
  {
    const u32x4 rs = make_rsrc(ws + WS_Y, ROWS * 65536);
    const unsigned vo = (unsigned)((lane >> 2) * 64 + (((lane & 3) ^ ((lane >> 3) & 2)) << 4));
#pragma unroll
    for (int i = 0; i < 3; ++i) {
      const unsigned yr = (unsigned)(b * NBLK + bs + wp + 4 * i);
      const unsigned so = i < ntk ? (yr * 16 + (cg ^ ((yr >> 4) & 2))) * 4096 : 0xf0000000u;
#pragma unroll
      for (int j = 0; j < 4; ++j) DMA16("sc1", rs, lds0 + wv * 12288 + i * 4096 + j * 1024, vo, so + j * 1024);
    }
  }
  f32x4 y[24], npv[2];
  float s1[8], s2[8];
  {
    bf16x8 a[12];
    const char* src = my + (4 * g + qq) * 64 + ((g & 1) << 5) + pp * 8;
    auto rd = [&](int q) __attribute__((always_inline)) {
      const char* p = reinterpret_cast<const char*>(smem + (((unsigned)(src - smem)) ^ (unsigned)((q & 2) << 4)) + (q >> 2) * 4096 + (q & 1) * 2048);
      a[q] = cat(tr_read(p), tr_read(p + 1024));
    };
    auto sums = [&](int cp, int q) __attribute__((always_inline)) {
      const int e = (cp & 4) + q;
      if ((cp & 3) == 3) {
        if (cp >= 8) y[cp & 7][q] = (fr >> 1) == (cp >> 3) ? y[cp][q] : y[cp & 7][q];
        return;
      }
      float d = y[cp][q];
      if (cp >= 16 && l0 + 8 * HOP + 16 * (cp & 3) >= lend) d = 0.f;
      if (cp == 0 || cp == 4) { s1[e] = d; s2[e] = d * d; }
      else { s1[e] += d; s2[e] = __builtin_fmaf(d, d, s2[e]); }
      asm volatile("" : "+v"(s1[e]), "+v"(s2[e]));
    };
    wait_vm<8>();
    SB0;
    u32x2 pv[2];
#pragma unroll
    for (int s = 0; s < 2; ++s) pv[s] = *reinterpret_cast<const u32x2*>(my + s * 32 + g * 8);
    SB0;
#pragma unroll
    for (int q = 0; q < 4; ++q) { rd(q); SB0; }
#pragma unroll
    for (int s = 0; s < 2; ++s) {
      npv[s] = f32x4{bf_lo(pv[s][0]), bf_hi(pv[s][0]), bf_lo(pv[s][1]), bf_hi(pv[s][1])} * (-1.0f / 64.0f);
      cin[s] += npv[s];
    }
    SB0;
    auto chain = [&](auto cc) __attribute__((always_inline)) {
      constexpr int c = cc.v, is = c >> 2, tt = c & 3;
      if (c == 7) wait_vm<4>();
      if (c == 15) wait_vm<0>();
#pragma unroll
      for (int q = 0; q < 4; ++q) {
        if (c < 24 && !(q & 1)) y[c % 24] = MFMA(a[(2 * is + (q >> 1)) % 12], gb[2 * tt + (q >> 1)], q ? y[c % 24] : tt == 3 ? cin[is & 1] : npv[is & 1]);
        SB0;
        if (c == 7 || c == 15) rd((c == 7 ? 4 : 8) + q);
        if (c >= 2) sums(c - 2, q);
        SB0;
      }
    };
    sfor<0, 16>(chain);
    if (ntk == 3) {
      sfor<16, 26>(chain);
    } else {
#pragma unroll
      for (int q = 0; q < 8; ++q) sums(14 + (q >> 2), q & 3);
    }
#pragma unroll
    for (int e = 0; e < 8; ++e) {
      const float d = le < lend ? y[3 + (e & 4)][e & 3] : 0.f;
      s1[e] += d;
      s2[e] = __builtin_fmaf(d, d, s2[e]);
    }
  }
  float* red = reinterpret_cast<float*>(smem + 8 * 12288);
  {
    float v[8];
#pragma unroll
    for (int j = 0; j < 8; ++j) v[j] = bfly<0x140>(s1[j], s2[j], fr & 8);
#pragma unroll
    for (int j = 0; j < 4; ++j) v[j] = bfly<0x141>(v[j], v[j + 4], fr & 4);
#pragma unroll
    for (int j = 0; j < 2; ++j) v[j] = bfly<0x4E>(v[j], v[j + 2], fr & 2);
    red[((fr >> 3) * 8 + 2 * wp + hw) * 32 + 8 * g + (fr & 7)] = bfly<0xB1>(v[0], v[1], fr & 1);
    if (fr < 2) *reinterpret_cast<f32x4*>(red + (16 + 2 * wp + hw) * 32 + 8 * g + 4 * fr) = fr ? npv[1] : npv[0];
  }
  unsigned arr = 1u;
  if (lane == 0) asm volatile("ds_add_rtn_u32 %0, %1, %0\n\ts_waitcnt lgkmcnt(0)" : "+v"(arr) : "v"(cnt) : "memory");
  unsigned long long* stats = reinterpret_cast<unsigned long long*>(ws + WS_STATS);
  if (__builtin_amdgcn_readfirstlane(arr) == 3u && lane < 32) {
    const int tid = hw * 32 + lane;
    float mk[4], sm = 0.f, m2 = 0.f;
#pragma unroll
    for (int k = 0; k < 4; ++k) {
      const float* e = red + 64 * k + tid;
      const float dm = e[0] * rk[k];
      mk[k] = dm - e[512];
      m2 += fmaxf(e[256] - e[0] * dm, 0.f);
      sm += nk[k] * mk[k];
    }
    const float mean = sm * rn;
#pragma unroll
    for (int k = 0; k < 4; ++k) m2 += nk[k] * (mk[k] - mean) * (mk[k] - mean);
    unsigned long long pk = (unsigned long long)__float_as_uint(mean) | ((unsigned long long)__float_as_uint(m2) << 32);
    if (pk == ~0ull) pk = ~1ull;
    __hip_atomic_store((gu64*)(stats + (b * 16 + rg + (rg & 12) + (c64i & 1) * 4 + b * 16) * 256 + nt * 64 + tid), pk, RLX_AGENT);
  }
  const u32x4 gam = *reinterpret_cast<const u32x4*>(smem + GAM + hw * 64 + g * 16);
  const u32x4 bet = *reinterpret_cast<const u32x4*>(smem + GAM + 128 + hw * 64 + g * 16);
  f32x4 ga[2], be[2];
#pragma unroll
  for (int r = 0; r < 8; ++r) {
    ga[r >> 2][r & 3] = (r & 1) ? bf_hi(gam[r >> 1]) : bf_lo(gam[r >> 1]);
    be[r >> 2][r & 3] = (r & 1) ? bf_hi(bet[r >> 1]) : bf_lo(bet[r >> 1]);
  }
  const float eps = fmaxf(__builtin_amdgcn_ldexpf(1e-5f, 2 * cx.es), 1e-30f);
  float na = fr < 15 ? 500.f : 485.f;
  asm volatile("" : "+v"(ga[0]), "+v"(ga[1]), "+v"(be[0]), "+v"(be[1]), "+v"(na));
  unsigned long long* gl = reinterpret_cast<unsigned long long*>(smem + 8 * 12288 + 4096);
  {
    unsigned long long pk[2];
    const int row = 4 * wp + (lane >> 5), col = hw * 32 + (lane & 31);
    for (unsigned spins = 0;; ++spins) {
#pragma unroll
      for (int r = 0; r < 2; ++r)
        pk[r] = __hip_atomic_load((gu64*)(stats + (b * 32 + row + 2 * r + 4 * wp + (c64i & 1) * 4) * 256 + nt * 64 + col), RLX_AGENT);
      if (__all(pk[0] != ~0ull && pk[1] != ~0ull) || spins > (1u << 20)) break;
      __builtin_amdgcn_s_sleep(1);
    }
    gl[row * 66 + col] = pk[0];
    gl[(row + 2) * 66 + col] = pk[1];
    if (lane == 0) asm volatile("ds_add_u32 %0, %1" :: "v"(cnt + 4), "v"(1u) : "memory");
    unsigned c;
    do asm volatile("ds_read_b32 %0, %1\n\ts_waitcnt lgkmcnt(0)" : "=v"(c) : "v"(cnt + 4) : "memory");
    while (__builtin_amdgcn_readfirstlane(c) < 4u);
  }
  {
    u32x4 pk[4];
#pragma unroll
    for (int r = 0; r < 4; ++r)
      pk[r] = *reinterpret_cast<const u32x4*>(gl + fr * 66 + hw * 32 + 8 * g + 2 * r);
    float mu[8], vs[8];
#pragma unroll
    for (int r = 0; r < 8; ++r) mu[r] = na * __uint_as_float(pk[r >> 1][(r & 1) * 2]);
    row16_sum(mu);
#pragma unroll
    for (int r = 0; r < 8; ++r) {
      mu[r] *= 1.0f / (float)L_;
      const float da = __uint_as_float(pk[r >> 1][(r & 1) * 2]) - mu[r];
      vs[r] = __uint_as_float(pk[r >> 1][(r & 1) * 2 + 1]) + na * da * da;
    }
    row16_sum(vs);
#pragma unroll
    for (int r = 0; r < 8; ++r) {
      const float mean = mu[r], var = vs[r] * (1.0f / (float)L_);
      const float rstd = __builtin_amdgcn_rsqf(var + eps);
      const float gg = ga[r >> 2][r & 3] * rstd;
      ga[r >> 2][r & 3] = gg;
      be[r >> 2][r & 3] -= (mean + npv[r >> 2][r & 3]) * gg;
    }
  }
  {
    const float k1 = -2.0f * 0.7978845608028654f * 1.4426950408889634f, k2 = k1 * 0.044715f;
    f32x4 kk1 = {k1, k1, k1, k1}, kk2 = {k2, k2, k2, k2};
    asm volatile("" : "+v"(kk1), "+v"(kk2));
    const u32x4 ro = make_rsrc(out + (size_t)b * L_ * D_, lend * (D_ * 2));
    const unsigned oo = (unsigned)((l0 * D_ + ch0) * 2);
#pragma unroll
    for (int i = 0; i < 3; ++i)
      if (i < 2 || ntk == 3)
#pragma unroll
      for (int tt = 0; tt < 4; ++tt) {
        if (tt == 3 && i) break;
        f32x4 o[2];
#pragma unroll
        for (int s = 0; s < 2; ++s) {
          const f32x4 z = y[8 * i + 4 * s + tt] * ga[s] + be[s];
          const f32x4 a = z * (z * z * kk2 + kk1);
          f32x4 e;
#pragma unroll
          for (int r = 0; r < 4; ++r) e[r] = __builtin_amdgcn_exp2f(a[r]);
          e += 1.0f;
#pragma unroll
          for (int r = 0; r < 4; ++r) e[r] = __builtin_amdgcn_rcpf(e[r]);
          o[s] = z * e;
        }
        st16(pack8(o[0], o[1]), ro, tt < 3 ? oo : (unsigned)((le * D_ + ch0) * 2), tt < 3 ? (4 * HOP * i + 16 * tt) * (D_ * 2) : 0);
      }
  }
  asm volatile(".Lcf_b:\n\ts_nop 0" ::: "memory");
}

extern "C" void kernel_launch(void* const* d_in, const int* in_sizes, int n_in,
                              void* d_out, int out_size, void* d_ws, size_t ws_size,
                              hipStream_t stream) {
  const unsigned short* x = (const unsigned short*)d_in[0];
  const unsigned short* w = (const unsigned short*)d_in[1];
  const unsigned short* gamma = (const unsigned short*)d_in[2];
  const unsigned short* beta = (const unsigned short*)d_in[3];
  char* ws = (char*)(((uintptr_t)d_ws + (2u << 20) - 1) & ~(uintptr_t)((2u << 20) - 1));
  conv_fft<<<256, 512, 0, stream>>>(ws, x, w, gamma, beta, (unsigned short*)d_out);
}
```

```cpp
#include <hip/hip_runtime.h>
#include <stdint.h>

typedef __attribute__((ext_vector_type(8))) short bf16x8;
typedef __attribute__((ext_vector_type(4))) short s16x4;
typedef __attribute__((ext_vector_type(4))) float f32x4;
typedef __attribute__((ext_vector_type(2))) float f32x2;
typedef __attribute__((ext_vector_type(2))) __bf16 bf16x2v;
typedef __attribute__((ext_vector_type(4))) unsigned int u32x4;
typedef __attribute__((ext_vector_type(2))) unsigned int u32x2;
constexpr int NH = 10;
typedef __attribute__((ext_vector_type(8))) unsigned int u32x8;
typedef __attribute__((ext_vector_type(16))) unsigned int u32x16;
typedef __attribute__((ext_vector_type(8))) _Float16 f16x8;
typedef __attribute__((ext_vector_type(2))) _Float16 f16x2;
typedef __attribute__((ext_vector_type(2))) unsigned short u16x2;
typedef __attribute__((address_space(1))) unsigned long long gu64;
typedef __attribute__((address_space(1))) unsigned gu32;
typedef __attribute__((address_space(3))) void lds_void;
typedef __attribute__((address_space(3))) s16x4 lds_s16x4;

constexpr unsigned long long bytes(unsigned a, unsigned b = 0, unsigned c = 0, unsigned d = 0, unsigned e = 0, unsigned f = 0, unsigned g = 0, unsigned h = 0) {
  return a | b << 8 | c << 16 | d << 24 | (unsigned long long)(e | f << 8 | g << 16 | h << 24) << 32;
}
constexpr int T_ = 16000, D_ = 512, L_ = 7985;
constexpr int HOP = 50, NBLK = 160, ROWS = 2 * NBLK;

constexpr unsigned WS_S = 0;
constexpr unsigned SBLK = 4u << 20;
constexpr unsigned sbase(unsigned sl) { return sl * 131072 + (sl >> 4) * 8192; }
constexpr unsigned FLG = 32u * SBLK;
constexpr unsigned WS_GRP = WS_S + FLG + 16 * 8192;
constexpr unsigned WS_D = WS_S + FLG + 20 * 8192;
constexpr unsigned WS_L = WS_D + ROWS * 1024;
constexpr unsigned WS_Y = 40u << 20;
constexpr unsigned WS_STATS = 129u << 20;
constexpr unsigned WS_FA = WS_STATS + (1u << 17);
constexpr unsigned WS_PUB = WS_STATS + (1u << 19);
static_assert(WS_L + D_ * D_ * 2 <= WS_STATS && FLG % (16 * 8192) == 0, "workspace layout");

#define RLX_AGENT __ATOMIC_RELAXED, __HIP_MEMORY_SCOPE_AGENT

constexpr double COS64[17] = {1.0, 0.99518472667219688624, 0.98078528040323044913, 0.95694033573220886494,
    0.92387953251128675613, 0.88192126434835502971, 0.83146961230254523708, 0.77301045336273696081,
    0.70710678118654752440, 0.63439328416364549822, 0.55557023301960222474, 0.47139673682599764856,
    0.38268343236508977173, 0.29028467725446236764, 0.19509032201612826785, 0.09801714032956060199, 0.0};
constexpr double c64(int k) { k &= 63; if (k > 32) k = 64 - k; return k <= 16 ? COS64[k] : -COS64[32 - k]; }
constexpr double s64(int k) { return c64(k + 48); }
constexpr unsigned short bf_rne(double v) {
  const unsigned u = __builtin_bit_cast(unsigned, (float)v);
  return (unsigned short)((u + 0x7fffu + ((u >> 16) & 1u)) >> 16);
}
constexpr double R3 = 0.86602540378443864676;
constexpr double dft(int c, int t, double sgn) {
  return (c >> 1) == 0 ? ((c & t) & 1 ? -1.0 : 1.0)
                       : ((c & 1) ? -0.5 * c64((c >> 1) * t) - R3 * sgn * s64((c >> 1) * t) : c64((c >> 1) * t));
}
constexpr double idft(int c, int t) {
  return (c >> 1) == 0 ? ((c & t) & 1 ? -1.0 : 1.0) / 64.0
                       : ((c & 1) ? -s64((c >> 1) * t) / R3 : c64((c >> 1) * t) * (4.0 / 3.0)) / 32.0;
}
constexpr int kidx(int g, int j) { return j < 4 ? 4 * g + j : 12 + 4 * g + j; }
struct Tables {
  unsigned short G[4][2][64][8];
  unsigned short F[4][2][64][8];
  unsigned short V[4][64][4];
};
constexpr Tables make_tables() {
  Tables tb{};
  for (int l = 0; l < 64; ++l)
    for (int j = 0; j < 8; ++j)
      for (int t = 0; t < 4; ++t) {
        const int g = l >> 4, n = l & 15;
        for (int ks = 0; ks < 2; ++ks) {
          tb.F[t][ks][l][j] = bf_rne(dft(16 * t + n, 32 * ks + kidx(g, j), -1.0));
          tb.G[t][ks][l][j] = bf_rne(idft(32 * ks + kidx(g, j), 16 * t + (t < 3 ? n : n & 1)));
        }
        tb.V[t][l][j & 3] = bf_rne(dft(16 * t + n, 4 * g + (j & 3), 1.0));
      }
  return tb;
}
__device__ const __attribute__((aligned(16))) Tables TAB = make_tables();

__device__ __forceinline__ unsigned pk_bf16(float lo, float hi) {
  return __builtin_bit_cast(unsigned, __builtin_convertvector(f32x2{lo, hi}, bf16x2v));
}
__device__ __forceinline__ float bf_lo(unsigned v) { return __uint_as_float(v << 16); }
__device__ __forceinline__ float bf_hi(unsigned v) { return __uint_as_float(v & 0xffff0000u); }
__device__ __forceinline__ u32x4 pack8(f32x4 a, f32x4 b) {
  return u32x4{pk_bf16(a[0], a[1]), pk_bf16(a[2], a[3]), pk_bf16(b[0], b[1]), pk_bf16(b[2], b[3])};
}
__device__ __forceinline__ unsigned pk_f16(float lo, float hi) {
  return (__builtin_bit_cast(unsigned, __builtin_convertvector(f32x2{lo, hi}, f16x2)) + 0x40004u) & 0xfff8fff8u;
}
__device__ __forceinline__ u32x4 pack8h(f32x4 a, f32x4 b) {
  return u32x4{pk_f16(a[0], a[1]), pk_f16(a[2], a[3]), pk_f16(b[0], b[1]), pk_f16(b[2], b[3])};
}
__device__ __forceinline__ unsigned add_h(const bf16x8& a, const bf16x8& b, int d) {
  const unsigned x = __builtin_bit_cast(u32x4, a)[d], y = __builtin_bit_cast(u32x4, b)[d];
  unsigned r = __builtin_bit_cast(unsigned, __builtin_bit_cast(f16x2, x) + __builtin_bit_cast(f16x2, y));
  return r;
}
__device__ __forceinline__ unsigned amax(unsigned m, unsigned v) {
  return __builtin_bit_cast(unsigned, __builtin_elementwise_max(__builtin_bit_cast(u16x2, m), __builtin_bit_cast(u16x2, v & 0x7fff7fffu)));
}
struct Rng { f16x2 hi = {0, 0}, lo = {0, 0}; };
__device__ __forceinline__ void widen(Rng& r, unsigned a, unsigned b) {
  const f16x2 x = __builtin_bit_cast(f16x2, a), y = __builtin_bit_cast(f16x2, b);
  r.hi = __builtin_elementwise_maximum(__builtin_elementwise_maximum(r.hi, x), y);
  r.lo = __builtin_elementwise_minimum(__builtin_elementwise_minimum(r.lo, x), y);
}
__device__ __forceinline__ unsigned amax(const Rng& r) { return amax(amax(0, __builtin_bit_cast(unsigned, r.hi)), __builtin_bit_cast(unsigned, r.lo)); }
__device__ __forceinline__ unsigned expo(unsigned m) { return max(m & 0xffffu, m >> 16) >> 7; }
__device__ __forceinline__ unsigned wave_max(unsigned v) {
  unsigned m = 0;
#pragma unroll 1
  for (int l = 0; l < 64; ++l) m = max(m, (unsigned)__builtin_amdgcn_readlane((int)v, l));
  return m;
}
__device__ __forceinline__ unsigned scale2(unsigned u, int e) {
  return __builtin_amdgcn_perm(__float_as_uint(__builtin_amdgcn_ldexpf(bf_hi(u), e)), __float_as_uint(__builtin_amdgcn_ldexpf(bf_lo(u), e)), 0x07060302u);
}
__device__ __forceinline__ void restart(unsigned id) {
  asm volatile("s_waitcnt vmcnt(0)\n\ts_barrier\n\ts_branch conv_fft" :: "{s[0:1]}"(__builtin_amdgcn_kernarg_segment_ptr()), "{s2}"(id), "{v0}"(threadIdx.x) : "memory");
}
__device__ __forceinline__ s16x4 tr_read(const char* p) {
  return __builtin_amdgcn_ds_read_tr16_b64_v4i16((lds_s16x4*)p);
}
__device__ __forceinline__ bf16x8 cat(s16x4 a, s16x4 b) {
  const u32x2 x = __builtin_bit_cast(u32x2, a), y = __builtin_bit_cast(u32x2, b);
  return __builtin_bit_cast(bf16x8, u32x4{x[0], x[1], y[0], y[1]});
}
#define MFMA(a, b, c) __builtin_amdgcn_mfma_f32_16x16x32_bf16(a, b, c, 0, 0, 0)
#define MFMS(a, b) __builtin_amdgcn_mfma_f32_16x16x16bf16_1k(a, __builtin_bit_cast(s16x4, b), ZERO4, 0, 0, 0)
#define SB0 __builtin_amdgcn_sched_barrier(0)
#define ZERO4 (f32x4{0.f, 0.f, 0.f, 0.f})
#define DMA16(POL, rs, lds, voff, soff) \
  asm volatile("s_mov_b32 m0, %0\n\ts_nop 0\n\tbuffer_load_dwordx4 %1, %2, %3 offen " POL " lds" \
               :: "s"(lds), "v"(voff), "s"(rs), "s"(soff) : "memory")
__device__ __forceinline__ void st16(const u32x4& v, const u32x4& rs, unsigned voff, unsigned soff) {
  asm volatile("buffer_store_dwordx4 %0, %1, %2, %3 offen sc1\n\ts_nop 1" :: "v"(v), "v"(voff), "s"(rs), "s"(soff) : "memory");
}
template <int N> __device__ __forceinline__ void wait_vm() { asm volatile("s_waitcnt vmcnt(%0)" :: "i"(N) : "memory"); }
__device__ __forceinline__ u32x4 make_rsrc(const void* p, unsigned bytes) {
  const unsigned long long a = (unsigned long long)p;
  return u32x4{(unsigned)a, (unsigned)(a >> 32) & 0xffffu, bytes, 0x00020000u};
}
__device__ __forceinline__ void spin_flags(const char* p) {
  for (unsigned spins = 0; !__all(__hip_atomic_load((const gu32*)p, RLX_AGENT) != 0u) && spins < (1u << 21); ++spins)
    __builtin_amdgcn_s_sleep(3);
}

constexpr int GW = 10;
constexpr int NQ1 = GW / 2, NQW = 16 - NQ1;
constexpr int WSL = 16384, XSL = 20480, XRING = 3 * WSL;
constexpr int RAWB = 67 * 1024, XT1 = RAWB + NQ1 * 4096, SINK = XT1 + RAWB;
constexpr int RAWL = XRING + 3 * XSL;
constexpr int CORR = RAWL + 4096, CORD = RAWL + NQW * 4096;
constexpr int GAM = RAWL + 52224, CBUF = RAWL + 49152;

struct Ctx {
  char* smem;
  char* ws;
  const unsigned short *gamma, *beta;
  u32x4 rs;
  unsigned ldg, ldf, sg, sf;
  unsigned voff, voff5, crd;
  unsigned xbase, wbase;
  unsigned pwr, prd, poff;
  unsigned xo;
  unsigned lane4, fvo;
  unsigned id;
  unsigned tag[2];
  int es;
  int wv, vb;
  unsigned long long pc;
  unsigned pfo, pft;
};
__device__ __forceinline__ void code_touch(const Ctx& cx) {
  if ((cx.wv ^ (cx.vb >> 3)) & 3) return;
#pragma unroll
  for (int h = 0; h < 2; ++h)
    asm volatile("s_mov_b32 m0, %0\n\ts_nop 0\n\tglobal_load_lds_dword %1, %2" :: "s"(SINK + cx.wv * 256), "v"(min(__builtin_amdgcn_mbcnt_hi(~0u, __builtin_amdgcn_mbcnt_lo(~0u, cx.wv * 64u + h * 512u)) * 128u, cx.pfo)), "s"(cx.pc) : "memory");
}
__device__ __forceinline__ void x_store(const Ctx& cx, const u32x4& v, unsigned kb, unsigned blk) {
  st16(v, cx.rs, cx.xo, kb + (blk + (blk & 192u)) * 128u);
}
template <int NM>
__device__ __forceinline__ void stage_issue(const Ctx& cx, int kc) {
  const unsigned slot = (unsigned)(kc + 1) * (NM == 6 ? WSL : XSL);
  if (kc >= 0 || NM == 6 || !(cx.wv & 1))
    asm volatile("s_mov_b32 m0, %0\n\ts_nop 0\n\tbuffer_load_dwordx4 %1, %2, %3 offen sc1 lds\n\tbuffer_load_dwordx4 %1, %2, %3 offen offset:1024 sc1 lds\n\t"
                 "buffer_load_dwordx4 %1, %2, %3 offen offset:2048 sc1 lds\n\tbuffer_load_dwordx4 %1, %2, %3 offen offset:3072 sc1 lds"
                 :: "s"(cx.ldg + slot), "v"(cx.voff), "s"(cx.rs), "s"(cx.sg + kc * SBLK) : "memory");
  if (kc >= 0 && NM == 5) DMA16("sc1", cx.rs, cx.ldf + slot, cx.voff, cx.sf);
}
template <int V> struct IC { static constexpr int v = V; constexpr operator int() const { return V; } };
template <int A, int B, class F> __device__ __forceinline__ void sfor(F f) {
  if constexpr (A < B) { f(IC<A>{}); sfor<A + 1, B>(f); }
}
struct RQ { u32x4 h[NH]; u32x2 raw[NQW]; u32x2 vb; unsigned s[33], v[21]; };

__device__ __forceinline__ int run_bs(int vb, int t) { return 5 * ((vb >> 4) + 16 * ((vb ^ t) & 1)); }
__device__ __forceinline__ int run_q(int vb, int t) { return (((vb >> 1) & 1) + ((vb >> 2) & 2) + 2 * t) & 3; }
__device__ __forceinline__ unsigned x_addr(int vb, int t, int wv) {
  const int lane = threadIdx.x & 63, q = run_q(vb, t);
  const unsigned c = (unsigned)((lane & 15) ^ (((4 * wv + (lane >> 4)) & 7) << 1));
  return (unsigned)(2 * (HOP * run_bs(vb, t) + 4 * wv + (lane >> 4)) + ((t ^ q ^ (q >> 1)) & 1)) * 1024u + c * 16 + q * 256;
}
__device__ __forceinline__ u32x4 x_rsrc(const unsigned short* x, int vb) { return make_rsrc(x + (size_t)((vb >> 2) & 1) * T_ * D_, T_ * D_ * 2); }
__device__ __forceinline__ void issue_x(int t, int wv, unsigned a, const u32x4& rx, unsigned lds0, int j0 = 0, int j1 = 9) {
#pragma unroll
  for (int jj = 0; jj < 9; ++jj)
    if (jj >= j0 && jj < j1 && (jj < 8 || wv < 3)) DMA16("nt", rx, lds0 + t * XT1 + wv * 1024 + jj * 8192, a, jj * 65536u);
}
struct XR { s16x4 t[8]; };
constexpr int P0 = 3, P1 = 23;
constexpr int PG = 20, NFA = 1;
constexpr int TE = 4;
constexpr int NIT = 23, NS = 30 - NH, NA = 7, NB = 13;
constexpr int ord(int p) { return p < 0 || p >= NIT ? -1 : p == 0 ? 0 : p < 6 ? p + 2 : p < 8 ? p - 5 : p; }
constexpr int st_w(int j) { return j < 2 ? j : j < 7 ? -1 : j < 15 ? j - 5 : -1; }
constexpr int st_x(int j) { return j < 7 ? j - 2 : j - 10; }
constexpr int st_pos(int j) { return st_w(j) >= 0 ? (j < 9 ? 0 : j < 13 ? 6 : 7) : j < 7 ? j - 1 : j - 7; }
constexpr int st_first(int p) {
  int j = p <= P0 ? 0 : p >= P1 ? NS : (p - P0) * NS / (P1 - P0);
  while (j > 0 && p <= NIT && st_pos(j - 1) + 2 > p - 1) --j;
  return j;
}
template <int NM, class F>
__device__ __forceinline__ void phase1(Ctx& cx, const unsigned short* __restrict__ x, const unsigned short* __restrict__ w,
                                       RQ& rqv, unsigned& xs, u32x2& vt, F setup) {
  u32x2 rq[NQW];
  static_assert(NQ1 == 5, "phase-1 wait counts assume five quanta");
  char* smem = cx.smem;
  const unsigned lds0 = (unsigned)(uintptr_t)(lds_void*)smem;
  const int idx = cx.vb >> 3, wv = cx.wv, lane = threadIdx.x & 63;
  const u32x4 rw = make_rsrc(w, 31 * D_ * D_ * 2);
  const int tap = threadIdx.x >> 5, pc = threadIdx.x & 31;
  const unsigned roff = (unsigned)(tap * (2 * D_ * D_ * 2) + pc * 8);
  const int ng = wv & 3, mg = wv >> 2;
  const int g = lane >> 4, qq = (lane >> 2) & 3, pp = lane & 3;
  u32x2 r1[NQ1];
  u32x4 fb[2];
  unsigned long long tag;
  const char* rec = cx.ws + WS_PUB + ((cx.id & 7) * 8 + wv) * 16;
  u32x4 pr, wl;
  {
    const int vb = cx.vb;
    const int kr = (vb >> 7) * 32 + (vb & 4) * 4 + (idx & 15);
    auto raw_load = [&](u32x2& r, int q) {
      const unsigned so = (unsigned)(kr + 64 * (2 * (q & 3) + ((q >> 2) & 1)) + 512 * (((q >> 3) ^ q ^ (q >> 1)) & 1)) * 1024u + (unsigned)((vb + q) & 3) * 256u;
      asm volatile("buffer_load_dwordx2 %0, %1, %2, %3 offen nt" : "=v"(r) : "v"(roff), "s"(rw), "s"(so) : "memory");
    };
#pragma unroll
    for (int k = 0; k < NQ1; ++k) raw_load(r1[k], k);
    const unsigned xa1 = x_addr(vb, 1, wv);
    issue_x(1, wv, xa1, x_rsrc(x, vb), lds0, 0, TE);
    if (NM == 6 && (cx.id | wv) == 0) {
      unsigned g[2] = {~0u, ~0u};
#pragma unroll 1
      for (int k = -1; k < 16 && ((k & 1) || min(g[0], g[1]) > 440u); ++k) {
        unsigned long long t0, t1;
        unsigned d;
        asm volatile("s_memtime %0\n\ts_waitcnt lgkmcnt(0)\n\ts_load_dword %2, %3, 0x0\n\ts_waitcnt lgkmcnt(0)\n\ts_memtime %1\n\ts_waitcnt lgkmcnt(0)"
                     : "=&s"(t0), "=&s"(t1), "=&s"(d) : "s"(rec + 32768 + (k < 0 ? 128 : (k & 1) * 8192 + (k & 2) * 2048 + (k & 4) * 4096 + (k & 8) * 128)) : "memory");
        if (k >= 0) { if (k & 1) g[1] = min(g[1], (unsigned)(t1 - t0)); else g[0] = min(g[0], (unsigned)(t1 - t0)); }
      }
      const unsigned lo = cx.tag[0] | (((g[0] < g[1] ? 0u : 1u) ^ (cx.vb >> 2)) & 1u), hi = cx.tag[1];
      asm volatile("global_store_dwordx4 %0, %1, %2 sc1\n\ts_nop 1" :: "v"(lane * 16 + 2048), "v"(u32x4{lo, hi, lo, hi}), "s"(rec) : "memory");
    }
    {
#pragma unroll
      for (int i = 0; i < 2; ++i) asm volatile("global_load_dwordx4 %0, %1, off" : "=v"(fb[i]) : "v"(TAB.F[ng][i][lane]) : "memory");
      asm volatile("global_load_dwordx2 %0, %1, off" : "=v"(vt) : "v"(TAB.V[ng][lane]) : "memory");
      asm volatile("global_load_dwordx4 %0, %1, %2 sc1" : "=v"(pr) : "v"(0u), "s"(rec) : "memory");
    }
    if (NM == 5 && wv >= 6)
      asm volatile("buffer_load_dwordx4 %0, %1, %2, %3 offen nt" : "=v"(wl) : "v"((threadIdx.x & 127) * 16), "s"(rw), "s"(30 * D_ * D_ * 2 + vb * 2048) : "memory");
    if (cx.id == 0 && wv >= TE - 1)
      DMA16("nt", x_rsrc(x, vb), lds0 + XT1 + (wv + 1) * 8192, x_addr(vb, 1, 0), (wv + 1) * 65536u);
    if (NM == 5 || (cx.id | wv)) issue_x(1, wv, xa1, x_rsrc(x, vb), lds0, TE, 9);
#pragma unroll
    for (int k = 0; k < NQW; ++k) raw_load(rq[k], NQ1 + k);
    {
      const int bid = vb, xb = (bid >> 2) & 1, krow = kr;
      const int slot = (bid & 7) * 4 + (idx >> 3), b = (idx >> 2) & 1, nt = idx & 3;
      const int rt = 4 * g + qq;
      cx.lane4 = lane * 4;
      cx.fvo = NM == 5 ? (unsigned)((bid & 7) * 1024 + ng * 256 + lane * 4) : wv == 0 && lane < 8 ? (unsigned)(lane * 1024 + (bid >> 2) * 4) : 0x80000000u;
      cx.pwr = (unsigned)(RAWB + tap * 256 + (((pc >> 2) ^ (tap & 7)) << 5) + (pc & 3) * 8);
      cx.prd = (unsigned)(RAWB + rt * 256 + (((4 * mg) ^ (rt & 7)) << 5) + pp * 8);
      cx.poff = (unsigned)(sbase(8 * ng + ((lane >> 1) & 7)) + (krow >> 5) * SBLK + (krow & 24) * 512 + (krow & 7) * 128 +
                           mg * 2048 + (lane & 1) * 64 + g * 16);
      cx.xo = (unsigned)(sbase(8 * ng + ((lane >> 1) & 7)) + mg * SBLK + 65536 + xb * 8192 + (lane & 1) * 64 + g * 16);
      cx.xbase = (unsigned)(XRING + mg * 5 * 2048 + (lane & 8) * 128 + (lane & 7) * 64 + ((g ^ ((lane >> 1) & 2)) << 4));
      cx.wbase = (unsigned)((rt >> 3) * 4096 + ng * 1024 + (rt & 7) * 64 + (rt & 4) * 8 + pp * 8);
      const int pat = ((lane >> 2) & 7) * 128 + (lane >> 5) * 64 + (((lane & 3) ^ ((lane >> 3) & 2)) << 4);
      if (NM == 6) {
        cx.voff = (unsigned)(sbase(slot) + nt * 16384 + ng * 4096 + pat);
        cx.ldg = lds0 + ng * 4096;
        cx.ldf = lds0 + CORR + ng * 1024;
        cx.sg = SBLK;
        cx.sf = 0;
        const int kr8 = lane >> 3;
        cx.voff5 = (unsigned)(WS_L - WS_S + (8 * ng + kr8) * 1024 + (nt * 2 + xb) * 128 + (((lane & 7) ^ ((kr8 & 2) | (ng & 1) << 2)) << 4));
        cx.crd = (unsigned)(CORR + (8 * g + qq) * 128 + ((ng ^ ((qq >> 1) | (g & 1) << 1)) << 5) + pp * 8);
      } else {
        const int rg = (bid & 1) * 8 + (slot & 1) * 4 + (bid & 2) + (idx >> 4);
        cx.voff = (unsigned)(sbase(slot) + 65536 + b * 8192 + pat);
        cx.ldg = lds0 + XRING + (ng + (ng >> 1)) * 4096;
        cx.ldf = lds0 + XRING + 8192 + ng * 1024;
        cx.sg = (~ng & 1) * SBLK + (ng + (ng >> 1) * 3 + ((ng + 1) >> 2) * 2) * 4096;
        cx.sf = 16384 + ng * 1024;
        cx.voff5 = (unsigned)(WS_D - WS_S + (b * NBLK + 10 * rg + min(lane & 15, 9)) * 1024 + ng * 64 + g * 16);
      }
    }
    asm volatile("" : "+v"(cx.fvo), "+v"(cx.poff), "+v"(cx.xo), "+v"(cx.voff), "+v"(cx.voff5), "+v"(cx.lane4));
  }
  Rng gx, gw;
  auto scan = [&](int t) __attribute__((always_inline)) {
    u32x4 v[9];
#pragma unroll
    for (int j = 0; j < 9; ++j)
      if (j < 8 || NM == 6) v[j] = *reinterpret_cast<const u32x4*>(smem + t * XT1 + (j < 8 || wv < 3 ? j : 7) * 8192 + threadIdx.x * 16);
    SB0;
#pragma unroll
    for (int j = 0; j < 9; ++j)
      if (j < 8 || NM == 6) {
        widen(gx, v[j][0], v[j][1]);
        widen(gx, v[j][2], v[j][3]);
      }
    asm volatile("" : "+v"(gx.hi), "+v"(gx.lo));
    SB0;
  };
  if (NM == 6 && (cx.id | wv) == 0) wait_vm<NQW>();
  wait_vm<8 + 4 + NQW>();
  unsigned near;
  scan(0);
#pragma unroll
  for (int q = 0; q < NQ1; ++q) {
    asm volatile("" : "+v"(r1[q]));
    widen(gw, r1[q][0], r1[q][1]);
  }
  {
    wait_vm<NQW + 8 - TE>();
    unsigned d0, d1;
    for (unsigned spins = 0;; ++spins) {
      asm volatile("" : "+v"(pr));
      d0 = __builtin_amdgcn_readfirstlane(pr[0]);
      d1 = __builtin_amdgcn_readfirstlane(pr[1]);
      const unsigned d2 = __builtin_amdgcn_readfirstlane(pr[2]), d3 = __builtin_amdgcn_readfirstlane(pr[3]);
      if (__builtin_expect((d0 == ~d2 && d1 == ~d3) || spins > (1u << 21), 1)) break;
      __builtin_amdgcn_s_sleep(2);
      asm volatile("global_load_dwordx4 %0, %1, %2 sc1\n\ts_waitcnt vmcnt(0)" : "=v"(pr) : "v"(0u), "s"(rec) : "memory");
    }
    tag = d0 | (unsigned long long)d1 << 32;
    const unsigned vn = (cx.id + (d0 >> 1)) & 7u;
    if (__builtin_expect(vn != (cx.vb & 7u), 0)) restart(cx.id | 256u | vn << 10);
  }
  const unsigned ee = (cx.id & 512u ? cx.id : (unsigned)(tag >> 32)) >> 16, ex = ee & 255u, ew = ee >> 8;
  cx.es = 256 - (int)(ex + ew);
  char* ws;
  const int bid = cx.vb;
  u32x4 f0, f1;
  char* fab;
  const u32x4* fa;
  u32x2 ov;
  unsigned pollv;
  const int rb[2] = {run_bs(bid, 0), run_bs(bid, 1)};
  unsigned xb0[4];
#pragma unroll
  for (int k = 0; k < 4; ++k) xb0[k] = (unsigned)((4 * g + qq) * 256 + (((2 * mg) ^ ((4 * g + qq + 2 * k) & 7)) << 5) + pp * 8);
#pragma unroll
  for (int q = 0; q < NQ1; ++q) *reinterpret_cast<u32x2*>(smem + cx.pwr + q * 4096) = r1[q];
  asm volatile("" : "+v"(fb[0]), "+v"(fb[1]), "+v"(vt));
#pragma unroll
  for (int i = 0; i < 2; ++i) vt[i] = scale2(vt[i], 128 - (int)ew);
#pragma unroll
  for (int i = 0; i < 8; ++i) fb[i >> 2][i & 3] = scale2(fb[i >> 2][i & 3], 128 - (int)ex);
  asm volatile("" : "+v"(fb[0]), "+v"(fb[1]), "+v"(vt));
  asm volatile("s_waitcnt lgkmcnt(0)" : "+v"(xb0[0]), "+v"(xb0[1]), "+v"(xb0[2]), "+v"(xb0[3]) :: "memory");
  wait_vm<NQW>();
  asm volatile("global_load_dwordx2 %0, %1, %2 sc1" : "=v"(ov) : "v"(2048u), "s"(rec) : "memory");
  __builtin_amdgcn_s_barrier();
  SB0;
  u32x4 hw[6][2], hx[20];
  f32x4 pend[2];
  auto rd = [&](int it, int i) __attribute__((always_inline)) {
    if (it < 3) return tr_read(smem + ((cx.prd ^ (unsigned)((i & 3) << 5)) + (2 * it + (i >> 2)) * 4096));
    const int n = it - 3;
    const unsigned base = (xb0[n % 5 & 3] ^ (unsigned)((4 * ((n / 5) & 1) + (i & 1)) << 5)) + n / 10 * XT1;
    return tr_read(smem + (base + n % 5 * (HOP * 256) + (i >> 1) * (16 * 256)));
  };
  auto dst = [&](int it) -> u32x4& { return it < 3 ? hw[it == 2 ? 4 : 2 * it + 1][1] : hx[it - 3]; };
  auto item = [&](int it, int nx, int pv, XR& xr, auto side) __attribute__((always_inline)) {
    const XR c = xr;
    const int nm = it < 2 ? 8 : 4, nr = nx == 2 ? 4 : 8;
    f32x4 r[8];
#pragma unroll
    for (int i = 0; i < 8; ++i) {
      if (i >= nm) break;
      const int s2 = i & 1, ks = (i >> 1) & 1;
      if (it < 3) r[i] = MFMS(c.t[i], vt);
      else r[i] = MFMA(cat(c.t[4 * ks + s2], c.t[4 * ks + 2 + s2]), __builtin_bit_cast(bf16x8, fb[ks]), i < 2 ? ZERO4 : r[i - 2]);
      SB0;
      if (nx >= 0) {
#pragma unroll
        for (int j = 0; j < 8; ++j) if (j < nr && j * nm / nr == i) xr.t[j] = rd(nx, j);
      }
      if (pv >= 0 && (nm == 4 ? i >= 2 : i >= 1 && i <= 4)) {
        u32x4& d = dst(pv);
#pragma unroll
        for (int e = 0; e < 4; ++e)
          if ((nm == 4 ? 2 + (e >> 1) : 1 + e) == i) d[e] = pk_f16(pend[e >> 1][(e & 1) * 2], pend[e >> 1][(e & 1) * 2 + 1]);
        if (i == (nm == 4 ? 3 : 4)) asm volatile("" : "+v"(d));
      }
      if (it < 3 && i >= 3 && (i & 1)) {
        u32x4& d = hw[2 * it + ((i - 3) >> 2)][((i - 3) >> 1) & 1];
        d = pack8h(r[i - 3], r[i - 2]);
        asm volatile("" : "+v"(d));
      }
      side(i * (8 / nm));
      if (nm == 4) side(2 * i + 1);
      SB0;
    }
    pend[0] = r[nm - 2];
    pend[1] = r[nm - 1];
  };
  const unsigned so0 = (unsigned)(2 * run_q(bid, 0)) * SBLK, so1 = (unsigned)(16 + 2 * run_q(bid, 1)) * SBLK;
  auto store = [&](int j) __attribute__((always_inline)) {
    if (j == 0) {
      wait_vm<0>();
      for (unsigned spins = 0;; ++spins) {
        asm volatile("" : "+v"(ov));
        near = __builtin_amdgcn_readfirstlane(ov[0]) ^ (unsigned)tag;
        if (__builtin_expect((near < 2u && __builtin_amdgcn_readfirstlane(ov[1]) == (unsigned)(tag >> 32)) || spins > (1u << 21), 1)) break;
        __builtin_amdgcn_s_sleep(2);
        asm volatile("global_load_dwordx2 %0, %1, %2 sc1\n\ts_waitcnt vmcnt(0)" : "=v"(ov) : "v"(2048u), "s"(rec) : "memory");
      }
      ws = cx.ws += (near & 1u) * 8192;
      cx.rs = make_rsrc(ws + WS_S, WS_L - WS_S + D_ * D_ * 2);
      fab = ws + WS_FA + (cx.id & 512u) * 32;
      fa = reinterpret_cast<const u32x4*>(fab + (bid & 7) * 2048) + lane;
      if (NM == 6 && wv == 1) {
        const unsigned o = (lane < 32 ? WS_STATS : WS_S + FLG + 16384 - 512) + bid * 512 + lane * 16;
        const unsigned v = lane < 32 ? ~0u : 0u;
        asm volatile("global_store_dwordx4 %0, %1, %2 sc1\n\ts_nop 1" :: "v"(o), "v"(u32x4{v, v, v, v}), "s"(ws) : "memory");
        if (lane < 8) __hip_atomic_store((gu32*)(ws + WS_S + FLG + lane * 1024 + ((bid + 2) & 3) * 256 + (bid >> 2) * 4), 0u, RLX_AGENT);
      }
    }
    if (j == NB + 1 && NM == 6) {
      if (((bid >> 1) ^ (bid >> 3) ^ 1) & 1 && threadIdx.x < 160) {
        const int t = threadIdx.x >= 80, blk = (threadIdx.x >> 4) - 5 * t;
        const unsigned o = (unsigned)(t * XT1 + blk * (HOP * 256) + (((lane & 15) ^ ((blk & 3) << 2)) << 4));
        const u32x4 lo = *reinterpret_cast<const u32x4*>(smem + o), hi = *reinterpret_cast<const u32x4*>(smem + o + 64 * 256);
        u32x4 d;
#pragma unroll
        for (int e = 0; e < 4; ++e) d[e] = pk_bf16(bf_lo(hi[e]) - bf_lo(lo[e]), bf_hi(hi[e]) - bf_hi(lo[e]));
        st16(d, cx.rs, (unsigned)((((bid >> 2) & 1) * NBLK + (t ? rb[1] : rb[0]) + blk) * 1024 + run_q(bid, t) * 256 + (lane & 15) * 16), WS_D - WS_S);
      }
    }
    if (j == NB + 1 && NM == 5) {
      if (wv >= 6) {
        asm volatile("" : "+v"(wl));
        st16(wl, cx.rs, (threadIdx.x & 127) * 16, WS_L - WS_S + bid * 2048);
      }
    }
    const int wq = st_w(j), xn = st_x(j);
    if (wq >= 0) st16(hw[wq >> 1][wq & 1], cx.rs, cx.poff, (unsigned)(wq >> 1) * (2 * SBLK) + (unsigned)((bid + (wq >> 1)) & 3) * 16384u + (wq & 1) * 1024u);
    else x_store(cx, hx[xn], (xn < 10 ? so0 : so1) + ((xn / 5) & 1) * (8 * SBLK), (unsigned)(rb[xn / 10] + xn % 5));
    if (j == NA + NFA - 1) {
      scan(1);
#pragma unroll
      for (int k = 0; k < NQW; ++k) {
        asm volatile("" : "+v"(rq[k]));
        widen(gw, rq[k][0], rq[k][1]);
      }
      const unsigned mx = amax(gx), mw = amax(gw);
      if (__builtin_expect(__any(expo(mx) > ex + 7 || expo(mw) > ew + 9) && !(cx.id & 512u), 0)) {
        const u32x2 m = {wave_max(expo(mx)), wave_max(expo(mw))};
        asm volatile("global_atomic_umax %0, %1, %3 sc1\n\tglobal_atomic_umax %0, %2, %3 offset:4 sc1\n\ts_waitcnt vmcnt(0)"
                     :: "v"(lane * 16 + 1024), "v"(m[0]), "v"(m[1]), "s"(rec - ((cx.id & 7) * 8 + wv) * 16) : "memory");
      }
    }
    if (j == NA + NFA - 1 || j == NB + NFA - 1) {
      wait_vm<NFA>();
      __builtin_amdgcn_s_barrier();
      if (wv == 0 && lane < 8) {
        if (j < NB) __hip_atomic_store((gu64*)(fab + lane * 2048) + bid, tag, RLX_AGENT);
        else __hip_atomic_store((gu32*)(ws + WS_S + FLG + lane * 1024 + ((bid + 2) & 3) * 256 + (bid >> 2) * 4), 1u, RLX_AGENT);
      }
    }
    SB0;
  };
  XR xr;
#pragma unroll
  for (int j = 0; j < 8; ++j) xr.t[j] = rd(0, j);
  SB0;
  static_assert(st_first(NIT) == NS && PG >= 14 && st_first(PG - 2) >= NB + 2 && NA + NFA <= NB && NFA >= 1, "order of the hand-off steps");
  sfor<0, NIT>([&](auto p) __attribute__((always_inline)) {
    constexpr int j0 = st_first(p.v), j1 = st_first(p.v + 1);
    if (p >= 12 && p < PG) { if (!(bid & 1) && cx.id) __builtin_amdgcn_s_sleep(3); }
    if (p == PG - 2) {
      code_touch(cx);
      if (NM == 6 && wv == 0)
        asm volatile("global_load_dwordx4 %0, %2, off sc1\n\tglobal_load_dwordx4 %1, %2, off offset:1024 sc1" : "=&v"(f0), "=&v"(f1) : "v"(fa) : "memory");
    }
    if (p == PG) {
      if (NM == 6 && wv == 0) {
        wait_vm<st_first(PG) - st_first(PG - 2)>();
        for (unsigned spins = 0; spins < (1u << 21); ++spins) {
          asm volatile("" : "+v"(f0), "+v"(f1));
          const unsigned t0 = (unsigned)tag, t1 = (unsigned)(tag >> 32);
          if (__all(f0[0] == t0 && f0[1] == t1 && f0[2] == t0 && f0[3] == t1 && f1[0] == t0 && f1[1] == t1 && f1[2] == t0 && f1[3] == t1)) break;
          __builtin_amdgcn_s_sleep(2);
          asm volatile("global_load_dwordx4 %0, %2, off sc1\n\tglobal_load_dwordx4 %1, %2, off offset:1024 sc1\n\ts_waitcnt vmcnt(0)"
                       : "=&v"(f0), "=&v"(f1) : "v"(fa) : "memory");
        }
      }
      __builtin_amdgcn_s_barrier();
      stage_issue<NM>(cx, -1);
      asm volatile("global_load_dwordx2 %0, %1, %2 sc1" : "=v"(ov) : "v"(1024u), "s"(rec) : "memory");
      stage_issue<NM>(cx, 0);
      SB0;
    }
    if (p == NIT - 1 && NM == 5) {
      asm volatile("buffer_load_dword %0, %1, %2, %3 offen sc1" : "=v"(pollv) : "v"(cx.fvo), "s"(cx.rs), "s"(FLG) : "memory");
    }
    item(ord(p), ord(p + 1), ord(p - 1), xr, [&](int i) __attribute__((always_inline)) {
      if ((i * (j1 - j0) + 7) / 8 < ((i + 1) * (j1 - j0) + 7) / 8) store(j0 + (i * (j1 - j0) + 7) / 8);
    });
  });
  hx[19] = pack8h(pend[0], pend[1]);
  xs = so1 + (unsigned)rb[1];
#pragma unroll
  for (int e = 0; e < NH; ++e) rqv.h[e] = hx[20 - NH + e];
  asm volatile("" : "+v"(rqv.h[NH - 1]));
  setup();
  wait_vm<NM == 5 ? NS - st_first(NIT - 1) : 10 - NM + NS - st_first(PG)>();
  {
    asm volatile("" : "+v"(ov));
    const unsigned ox = __builtin_amdgcn_readfirstlane(ov[0]), ow = __builtin_amdgcn_readfirstlane(ov[1]);
    if (__builtin_expect((ox | ow) && !(cx.id & 512u), 0))
      restart((cx.id & 255u) | 768u | (bid & 7u) << 10 | min(max(ex + 7, ox) - 7, 220u) << 16 | min(max(ew + 9, ow) - 9, 220u) << 24);
    if (NM == 6 && (cx.id & 255u) == 0 && wv == 0)
      asm volatile("global_store_dwordx4 %0, %1, %2 sc1\n\ts_nop 1" :: "v"(lane * 16), "v"(u32x4{0u, 0u, 0u, 0u}), "s"(rec) : "memory");
  }
  if (NM == 5) {
    asm volatile("" : "+v"(pollv));
    if (!__all(pollv != 0u)) spin_flags(ws + WS_S + FLG + cx.fvo);
  }
#pragma unroll
  for (int k = 0; k < NQW; ++k) rqv.raw[k] = rq[k];
}

#define LOOP0 \
"s_mov_b32 s91, 0x40004\n" \
"s_mov_b32 s92, 0xfff8fff8\n" \
"ds_read_b64_tr_b16 v[120:121], v241 offset:0\n" \
"ds_read_b64_tr_b16 v[122:123], v241 offset:8192\n" \
"ds_read_b64_tr_b16 v[124:125], v242 offset:0\n" \
"ds_read_b64_tr_b16 v[126:127], v242 offset:8192\n" \
"ds_read_b64_tr_b16 v[128:129], v241 offset:512\n" \
"ds_read_b64_tr_b16 v[130:131], v241 offset:8704\n" \
"ds_read_b64_tr_b16 v[132:133], v242 offset:512\n" \
"ds_read_b64_tr_b16 v[134:135], v242 offset:8704\n" \
"ds_read_b128 v[160:163], v240 offset:0\n" \
"ds_read_b128 v[164:167], v240 offset:512\n" \
"ds_read_b128 v[168:171], v240 offset:2048\n" \
"ds_read_b128 v[172:175], v240 offset:2560\n" \
"s_waitcnt lgkmcnt(0)\n" \
"v_mfma_f32_16x16x32_f16 v[0:3], v[120:123], v[160:163], 0\n" \
"v_pk_add_f16 v136, v120, v128\n" \
"v_pk_add_f16 v137, v121, v129\n" \
"v_pk_add_f16 v138, v122, v130\n" \
"v_pk_add_f16 v139, v123, v131\n" \
"s_add_i32 m0, s44, 0x8000\n" \
"s_add_i32 s80, s46, 0x400000\n" \
"v_mfma_f32_16x16x32_f16 v[12:15], v[124:127], v[160:163], 0\n" \
"v_pk_add_f16 v160, v160, v164\n" \
"v_pk_add_f16 v161, v161, v165\n" \
"v_pk_add_f16 v140, v124, v132\n" \
"buffer_load_dwordx4 v232, s[36:39], s80 offen sc1 lds\n" \
"v_mfma_f32_16x16x32_f16 v[4:7], v[128:131], v[164:167], 0\n" \
"v_pk_add_f16 v162, v162, v166\n" \
"v_pk_add_f16 v163, v163, v167\n" \
"v_pk_add_f16 v141, v125, v133\n" \
"v_mfma_f32_16x16x32_f16 v[16:19], v[132:135], v[164:167], 0\n" \
"v_pk_add_f16 v142, v126, v134\n" \
"v_pk_add_f16 v143, v127, v135\n" \
"buffer_load_dwordx4 v232, s[36:39], s80 offen offset:1024 sc1 lds\n" \
"v_mfma_f32_16x16x32_f16 v[8:11], v[136:139], v[160:163], 0\n" \
"ds_write_b64 v246, v[96:97] offset:0\n" \
"ds_write_b64 v246, v[98:99] offset:4096\n" \
"v_mfma_f32_16x16x32_f16 v[20:23], v[140:143], v[160:163], 0\n" \
"s_waitcnt vmcnt(5)\n" \
"s_barrier\n" \
"v_mfma_f32_16x16x32_f16 v[24:27], v[120:123], v[168:171], 0\n" \
"ds_read_b128 v[176:179], v240 offset:24576\n" \
"ds_write_b64 v246, v[100:101] offset:8192\n" \
"buffer_store_dwordx4 v[72:75], v235, s[36:39], s93 offen sc1\n" \
"v_mfma_f32_16x16x32_f16 v[36:39], v[124:127], v[168:171], 0\n" \
"v_pk_add_f16 v168, v168, v172\n" \
"v_pk_add_f16 v169, v169, v173\n" \
"ds_read_b128 v[180:183], v240 offset:25088\n" \
"buffer_load_dwordx4 v232, s[36:39], s80 offen offset:2048 sc1 lds\n" \
"v_mfma_f32_16x16x32_f16 v[28:31], v[128:131], v[172:175], 0\n" \
"v_pk_add_f16 v170, v170, v174\n" \
"v_pk_add_f16 v171, v171, v175\n" \
"ds_read_b128 v[160:163], v240 offset:26624\n" \
"v_mfma_f32_16x16x32_f16 v[40:43], v[132:135], v[172:175], 0\n" \
"ds_read_b128 v[164:167], v240 offset:27136\n" \
"buffer_load_dwordx4 v232, s[36:39], s80 offen offset:3072 sc1 lds\n" \
"v_mfma_f32_16x16x32_f16 v[32:35], v[136:139], v[168:171], 0\n" \
"ds_write_b64 v246, v[102:103] offset:12288\n" \
"ds_read_b64_tr_b16 v[144:145], v241 offset:16384\n" \
"ds_read_b64_tr_b16 v[146:147], v241 offset:24576\n" \
"v_mfma_f32_16x16x32_f16 v[44:47], v[140:143], v[168:171], 0\n" \
"ds_write_b64 v246, v[104:105] offset:16384\n" \
"ds_write_b64 v246, v[106:107] offset:20480\n" \
"s_waitcnt lgkmcnt(9)\n" \
"v_mfma_f32_16x16x32_f16 v[48:51], v[120:123], v[176:179], 0\n" \
"ds_read_b128 v[168:171], v240 offset:28672\n" \
"ds_write_b64 v246, v[108:109] offset:24576\n" \
"buffer_store_dwordx4 v[196:199], v235, s[36:39], s60 offen sc1\n" \
"ds_read_b64_tr_b16 v[148:149], v242 offset:16384\n" \
"v_mfma_f32_16x16x32_f16 v[60:63], v[124:127], v[176:179], 0\n" \
"s_waitcnt lgkmcnt(10)\n" \
"v_pk_add_f16 v176, v176, v180\n" \
"v_pk_add_f16 v177, v177, v181\n" \
"s_add_i32 m0, s44, 0x0\n" \
"s_add_i32 s80, s46, 0x800000\n" \
"v_mfma_f32_16x16x32_f16 v[52:55], v[128:131], v[180:183], 0\n" \
"v_pk_add_f16 v178, v178, v182\n" \
"v_pk_add_f16 v179, v179, v183\n" \
"buffer_load_dwordx4 v232, s[36:39], s80 offen sc1 lds\n" \
"v_mfma_f32_16x16x32_f16 v[64:67], v[132:135], v[180:183], 0\n" \
"ds_read_b128 v[172:175], v240 offset:29184\n" \
"ds_read_b64_tr_b16 v[150:151], v242 offset:24576\n" \
"v_mfma_f32_16x16x32_f16 v[56:59], v[136:139], v[176:179], 0\n" \
"ds_write_b64 v246, v[110:111] offset:28672\n" \
"ds_write_b64 v246, v[112:113] offset:32768\n" \
"v_mfma_f32_16x16x32_f16 v[68:71], v[140:143], v[176:179], 0\n" \
"buffer_load_dwordx4 v232, s[36:39], s80 offen offset:1024 sc1 lds\n" \
"ds_read_b64_tr_b16 v[152:153], v241 offset:16896\n" \
"s_waitcnt lgkmcnt(8)\n" \
"v_mfma_f32_16x16x32_f16 v[72:75], v[120:123], v[160:163], 0\n" \
"ds_read_b128 v[176:179], v240 offset:20480\n" \
"ds_write_b64 v246, v[114:115] offset:36864\n" \
"buffer_store_dwordx4 v[200:203], v235, s[36:39], s61 offen sc1\n" \
"v_mfma_f32_16x16x32_f16 v[84:87], v[124:127], v[160:163], 0\n" \
"v_pk_add_f16 v160, v160, v164\n" \
"v_pk_add_f16 v161, v161, v165\n" \
"ds_read_b64_tr_b16 v[154:155], v241 offset:25088\n" \
"ds_read_b64_tr_b16 v[156:157], v242 offset:16896\n" \
"v_mfma_f32_16x16x32_f16 v[76:79], v[128:131], v[164:167], 0\n" \
"v_pk_add_f16 v162, v162, v166\n" \
"v_pk_add_f16 v163, v163, v167\n" \
"buffer_load_dwordx4 v232, s[36:39], s80 offen offset:2048 sc1 lds\n" \
"v_mfma_f32_16x16x32_f16 v[88:91], v[132:135], v[164:167], 0\n" \
"ds_read_b128 v[180:183], v240 offset:20992\n" \
"ds_read_b64_tr_b16 v[158:159], v242 offset:25088\n" \
"v_mfma_f32_16x16x32_f16 v[80:83], v[136:139], v[160:163], 0\n" \
"ds_write_b64 v246, v[116:117] offset:40960\n" \
"v_mfma_f32_16x16x32_f16 v[92:95], v[140:143], v[160:163], 0\n" \
"buffer_load_dwordx4 v232, s[36:39], s80 offen offset:3072 sc1 lds\n" \
"s_waitcnt lgkmcnt(8)\n" \
"v_mfma_f32_16x16x32_f16 v[96:99], v[120:123], v[168:171], 0\n" \
"s_waitcnt lgkmcnt(7)\n" \
"v_pk_add_f16 v120, v144, v152\n" \
"v_pk_add_f16 v121, v145, v153\n" \
"ds_read_b128 v[160:163], v240 offset:22528\n" \
"v_mfma_f32_16x16x32_f16 v[108:111], v[124:127], v[168:171], 0\n" \
"v_pk_add_f16 v168, v168, v172\n" \
"v_pk_add_f16 v169, v169, v173\n" \
"ds_read_b128 v[164:167], v240 offset:23040\n" \
"v_mfma_f32_16x16x32_f16 v[100:103], v[128:131], v[172:175], 0\n" \
"v_pk_add_f16 v170, v170, v174\n" \
"v_pk_add_f16 v171, v171, v175\n" \
"v_mfma_f32_16x16x32_f16 v[112:115], v[132:135], v[172:175], 0\n" \
"s_waitcnt lgkmcnt(5)\n" \
"v_pk_add_f16 v122, v146, v154\n" \
"v_pk_add_f16 v123, v147, v155\n" \
"v_mfma_f32_16x16x32_f16 v[104:107], v[136:139], v[168:171], 0\n" \
"v_pk_add_f16 v124, v148, v156\n" \
"v_pk_add_f16 v125, v149, v157\n" \
"v_mfma_f32_16x16x32_f16 v[116:119], v[140:143], v[168:171], 0\n" \
"s_waitcnt lgkmcnt(2)\n" \
"v_pk_add_f16 v126, v150, v158\n" \
"v_pk_add_f16 v127, v151, v159\n" \
"s_waitcnt vmcnt(6) lgkmcnt(0)\n" \
"s_barrier\n" \
"v_mfma_f32_16x16x32_f16 v[0:3], v[144:147], v[176:179], v[0:3]\n" \
"ds_read_b128 v[168:171], v240 offset:45056\n" \
"ds_read_b64_tr_b16 v[184:185], v236 offset:0\n" \
"v_mfma_f32_16x16x32_f16 v[12:15], v[148:151], v[176:179], v[12:15]\n" \
"v_pk_add_f16 v176, v176, v180\n" \
"v_pk_add_f16 v177, v177, v181\n" \
"ds_read_b64_tr_b16 v[188:189], v237 offset:0\n" \
"v_mfma_f32_16x16x32_f16 v[4:7], v[152:155], v[180:183], v[4:7]\n" \
"v_pk_add_f16 v178, v178, v182\n" \
"v_pk_add_f16 v179, v179, v183\n" \
"v_mfma_f32_16x16x32_f16 v[16:19], v[156:159], v[180:183], v[16:19]\n" \
"ds_read_b128 v[172:175], v240 offset:45568\n" \
"s_add_i32 m0, s44, 0x4000\n" \
"s_add_i32 s80, s46, 0xc00000\n" \
"v_mfma_f32_16x16x32_f16 v[8:11], v[120:123], v[176:179], v[8:11]\n" \
"buffer_load_dwordx4 v232, s[36:39], s80 offen sc1 lds\n" \
"v_mfma_f32_16x16x32_f16 v[20:23], v[124:127], v[176:179], v[20:23]\n" \
"ds_read_b64_tr_b16 v[128:129], v241 offset:32768\n" \
"ds_read_b64_tr_b16 v[130:131], v241 offset:40960\n" \
"s_waitcnt lgkmcnt(4)\n" \
"v_mfma_f32_16x16x16_bf16 v[184:187], v[184:185], v[192:193], 0\n" \
"v_mfma_f32_16x16x32_f16 v[24:27], v[144:147], v[160:163], v[24:27]\n" \
"ds_read_b128 v[176:179], v240 offset:47104\n" \
"s_add_i32 s84, s52, 0x8004000\n" \
"s_waitcnt lgkmcnt(4)\n" \
"v_mfma_f32_16x16x16_bf16 v[188:191], v[188:189], v[192:193], 0\n" \
"v_mfma_f32_16x16x32_f16 v[36:39], v[148:151], v[160:163], v[36:39]\n" \
"v_pk_add_f16 v160, v160, v164\n" \
"v_pk_add_f16 v161, v161, v165\n" \
"buffer_store_dword v240, v243, s[36:39], s84 offen sc1\n" \
"v_mfma_f32_16x16x32_f16 v[28:31], v[152:155], v[164:167], v[28:31]\n" \
"v_pk_add_f16 v162, v162, v166\n" \
"v_pk_add_f16 v163, v163, v167\n" \
"ds_read_b64_tr_b16 v[132:133], v242 offset:32768\n" \
"v_mfma_f32_16x16x32_f16 v[40:43], v[156:159], v[164:167], v[40:43]\n" \
"v_cvt_pk_f16_f32 v184, v184, v185\n" \
"ds_read_b128 v[180:183], v240 offset:47616\n" \
"ds_read_b64_tr_b16 v[134:135], v242 offset:40960\n" \
"v_mfma_f32_16x16x32_f16 v[32:35], v[120:123], v[160:163], v[32:35]\n" \
"v_cvt_pk_f16_f32 v185, v186, v187\n" \
"v_cvt_pk_f16_f32 v186, v188, v189\n" \
"buffer_load_dwordx4 v232, s[36:39], s80 offen offset:1024 sc1 lds\n" \
"v_mfma_f32_16x16x32_f16 v[44:47], v[124:127], v[160:163], v[44:47]\n" \
"v_cvt_pk_f16_f32 v187, v190, v191\n" \
"v_add_u32 v184, s91, v184\n" \
"v_mfma_f32_16x16x32_f16 v[48:51], v[144:147], v[168:171], v[48:51]\n" \
"v_add_u32 v185, s91, v185\n" \
"v_add_u32 v186, s91, v186\n" \
"ds_read_b128 v[160:163], v240 offset:49152\n" \
"v_mfma_f32_16x16x32_f16 v[60:63], v[148:151], v[168:171], v[60:63]\n" \
"s_waitcnt lgkmcnt(4)\n" \
"v_pk_add_f16 v168, v168, v172\n" \
"v_pk_add_f16 v169, v169, v173\n" \
"v_add_u32 v187, s91, v187\n" \
"v_and_b32 v184, s92, v184\n" \
"s_add_i32 s82, s49, 0x2800000\n" \
"v_mfma_f32_16x16x32_f16 v[52:55], v[152:155], v[172:175], v[52:55]\n" \
"v_pk_add_f16 v170, v170, v174\n" \
"v_pk_add_f16 v171, v171, v175\n" \
"v_and_b32 v185, s92, v185\n" \
"v_and_b32 v186, s92, v186\n" \
"v_and_b32 v187, s92, v187\n" \
"v_mfma_f32_16x16x32_f16 v[64:67], v[156:159], v[172:175], v[64:67]\n" \
"ds_read_b128 v[164:167], v240 offset:49664\n" \
"buffer_store_dwordx4 v[184:187], v234, s[36:39], s82 offen sc1\n" \
"ds_read_b64_tr_b16 v[136:137], v241 offset:33280\n" \
"v_mfma_f32_16x16x32_f16 v[56:59], v[120:123], v[168:171], v[56:59]\n" \
"buffer_load_dwordx4 v232, s[36:39], s80 offen offset:2048 sc1 lds\n" \
"ds_read_b64_tr_b16 v[138:139], v241 offset:41472\n" \
"v_mfma_f32_16x16x32_f16 v[68:71], v[124:127], v[168:171], v[68:71]\n" \
"ds_read_b64_tr_b16 v[184:185], v238 offset:0\n" \
"ds_read_b64_tr_b16 v[188:189], v239 offset:0\n" \
"v_mfma_f32_16x16x32_f16 v[72:75], v[144:147], v[176:179], v[72:75]\n" \
"ds_read_b128 v[168:171], v240 offset:40960\n" \
"ds_read_b64_tr_b16 v[140:141], v242 offset:33280\n" \
"v_mfma_f32_16x16x32_f16 v[84:87], v[148:151], v[176:179], v[84:87]\n" \
"s_waitcnt lgkmcnt(7)\n" \
"v_pk_add_f16 v176, v176, v180\n" \
"v_pk_add_f16 v177, v177, v181\n" \
"v_mfma_f32_16x16x32_f16 v[76:79], v[152:155], v[180:183], v[76:79]\n" \
"v_pk_add_f16 v178, v178, v182\n" \
"v_pk_add_f16 v179, v179, v183\n" \
"s_waitcnt lgkmcnt(3)\n" \
"v_mfma_f32_16x16x16_bf16 v[184:187], v[184:185], v[192:193], 0\n" \
"v_mfma_f32_16x16x32_f16 v[88:91], v[156:159], v[180:183], v[88:91]\n" \
"ds_read_b128 v[172:175], v240 offset:41472\n" \
"ds_read_b64_tr_b16 v[142:143], v242 offset:41472\n" \
"s_waitcnt lgkmcnt(4)\n" \
"v_mfma_f32_16x16x16_bf16 v[188:191], v[188:189], v[192:193], 0\n" \
"v_mfma_f32_16x16x32_f16 v[80:83], v[120:123], v[176:179], v[80:83]\n" \
"buffer_load_dwordx4 v232, s[36:39], s80 offen offset:3072 sc1 lds\n" \
"v_mfma_f32_16x16x32_f16 v[92:95], v[124:127], v[176:179], v[92:95]\n" \
"v_mfma_f32_16x16x32_f16 v[96:99], v[144:147], v[160:163], v[96:99]\n" \
"v_pk_add_f16 v144, v128, v136\n" \
"v_pk_add_f16 v145, v129, v137\n" \
"v_cvt_pk_f16_f32 v184, v184, v185\n" \
"v_cvt_pk_f16_f32 v185, v186, v187\n" \
"ds_read_b128 v[176:179], v240 offset:43008\n" \
"v_mfma_f32_16x16x32_f16 v[108:111], v[148:151], v[160:163], v[108:111]\n" \
"v_pk_add_f16 v160, v160, v164\n" \
"v_pk_add_f16 v161, v161, v165\n" \
"v_cvt_pk_f16_f32 v186, v188, v189\n" \
"v_cvt_pk_f16_f32 v187, v190, v191\n" \
"ds_read_b128 v[180:183], v240 offset:43520\n" \
"v_mfma_f32_16x16x32_f16 v[100:103], v[152:155], v[164:167], v[100:103]\n" \
"v_pk_add_f16 v162, v162, v166\n" \
"v_pk_add_f16 v163, v163, v167\n" \
"v_add_u32 v184, s91, v184\n" \
"v_add_u32 v185, s91, v185\n" \
"v_add_u32 v186, s91, v186\n" \
"v_mfma_f32_16x16x32_f16 v[112:115], v[156:159], v[164:167], v[112:115]\n" \
"v_pk_add_f16 v146, v130, v138\n" \
"v_pk_add_f16 v147, v131, v139\n" \
"v_add_u32 v187, s91, v187\n" \
"v_and_b32 v184, s92, v184\n" \
"v_mfma_f32_16x16x32_f16 v[104:107], v[120:123], v[160:163], v[104:107]\n" \
"s_waitcnt lgkmcnt(2)\n" \
"v_pk_add_f16 v148, v132, v140\n" \
"v_pk_add_f16 v149, v133, v141\n" \
"v_and_b32 v185, s92, v185\n" \
"v_and_b32 v186, s92, v186\n" \
"v_and_b32 v187, s92, v187\n" \
"v_mfma_f32_16x16x32_f16 v[116:119], v[124:127], v[160:163], v[116:119]\n" \
"v_pk_add_f16 v150, v134, v142\n" \
"v_pk_add_f16 v151, v135, v143\n" \
"buffer_store_dwordx4 v[184:187], v234, s[36:39], s82 offen offset:1024 sc1\n" \
"s_waitcnt vmcnt(7) lgkmcnt(0)\n" \
"s_barrier\n" \
"v_mfma_f32_16x16x32_f16 v[0:3], v[128:131], v[168:171], v[0:3]\n" \
"ds_read_b128 v[160:163], v240 offset:4096\n" \
"ds_read_b64_tr_b16 v[184:185], v236 offset:4096\n" \
"ds_read_b64_tr_b16 v[188:189], v237 offset:4096\n" \
"v_mfma_f32_16x16x32_f16 v[12:15], v[132:135], v[168:171], v[12:15]\n" \
"v_pk_add_f16 v168, v168, v172\n" \
"v_pk_add_f16 v169, v169, v173\n" \
"v_mfma_f32_16x16x32_f16 v[4:7], v[136:139], v[172:175], v[4:7]\n" \
"v_pk_add_f16 v170, v170, v174\n" \
"v_pk_add_f16 v171, v171, v175\n" \
"buffer_store_dwordx4 v[204:207], v235, s[36:39], s62 offen sc1\n" \
"v_mfma_f32_16x16x32_f16 v[16:19], v[140:143], v[172:175], v[16:19]\n" \
"ds_read_b128 v[164:167], v240 offset:4608\n" \
"s_add_i32 m0, s44, 0x8000\n" \
"s_add_i32 s80, s46, 0x1000000\n" \
"v_mfma_f32_16x16x32_f16 v[8:11], v[144:147], v[168:171], v[8:11]\n" \
"buffer_load_dwordx4 v232, s[36:39], s80 offen sc1 lds\n" \
"s_waitcnt lgkmcnt(2)\n" \
"v_mfma_f32_16x16x16_bf16 v[184:187], v[184:185], v[192:193], 0\n" \
"v_mfma_f32_16x16x32_f16 v[20:23], v[148:151], v[168:171], v[20:23]\n" \
"ds_read_b64_tr_b16 v[120:121], v241 offset:0\n" \
"ds_read_b64_tr_b16 v[122:123], v241 offset:8192\n" \
"s_waitcnt lgkmcnt(3)\n" \
"v_mfma_f32_16x16x16_bf16 v[188:191], v[188:189], v[192:193], 0\n" \
"v_mfma_f32_16x16x32_f16 v[24:27], v[128:131], v[176:179], v[24:27]\n" \
"ds_read_b128 v[168:171], v240 offset:6144\n" \
"v_mfma_f32_16x16x32_f16 v[36:39], v[132:135], v[176:179], v[36:39]\n" \
"v_pk_add_f16 v176, v176, v180\n" \
"v_pk_add_f16 v177, v177, v181\n" \
"ds_read_b64_tr_b16 v[124:125], v242 offset:0\n" \
"v_mfma_f32_16x16x32_f16 v[28:31], v[136:139], v[180:183], v[28:31]\n" \
"v_pk_add_f16 v178, v178, v182\n" \
"v_pk_add_f16 v179, v179, v183\n" \
"v_cvt_pk_f16_f32 v184, v184, v185\n" \
"v_mfma_f32_16x16x32_f16 v[40:43], v[140:143], v[180:183], v[40:43]\n" \
"v_cvt_pk_f16_f32 v185, v186, v187\n" \
"v_cvt_pk_f16_f32 v186, v188, v189\n" \
"ds_read_b128 v[172:175], v240 offset:6656\n" \
"v_mfma_f32_16x16x32_f16 v[32:35], v[144:147], v[176:179], v[32:35]\n" \
"v_cvt_pk_f16_f32 v187, v190, v191\n" \
"v_add_u32 v184, s91, v184\n" \
"buffer_load_dwordx4 v232, s[36:39], s80 offen offset:1024 sc1 lds\n" \
"v_mfma_f32_16x16x32_f16 v[44:47], v[148:151], v[176:179], v[44:47]\n" \
"v_add_u32 v185, s91, v185\n" \
"v_add_u32 v186, s91, v186\n" \
"ds_read_b64_tr_b16 v[126:127], v242 offset:8192\n" \
"v_mfma_f32_16x16x32_f16 v[48:51], v[128:131], v[160:163], v[48:51]\n" \
"v_add_u32 v187, s91, v187\n" \
"v_and_b32 v184, s92, v184\n" \
"ds_read_b128 v[176:179], v240 offset:8192\n" \
"v_mfma_f32_16x16x32_f16 v[60:63], v[132:135], v[160:163], v[60:63]\n" \
"s_waitcnt lgkmcnt(4)\n" \
"v_pk_add_f16 v160, v160, v164\n" \
"v_pk_add_f16 v161, v161, v165\n" \
"v_and_b32 v185, s92, v185\n" \
"s_add_i32 s82, s50, 0x3000000\n" \
"v_mfma_f32_16x16x32_f16 v[52:55], v[136:139], v[164:167], v[52:55]\n" \
"v_pk_add_f16 v162, v162, v166\n" \
"v_pk_add_f16 v163, v163, v167\n" \
"v_and_b32 v186, s92, v186\n" \
"v_and_b32 v187, s92, v187\n" \
"v_mfma_f32_16x16x32_f16 v[64:67], v[140:143], v[164:167], v[64:67]\n" \
"ds_read_b128 v[180:183], v240 offset:8704\n" \
"buffer_store_dwordx4 v[184:187], v234, s[36:39], s82 offen sc1\n" \
"ds_read_b64_tr_b16 v[152:153], v241 offset:512\n" \
"v_mfma_f32_16x16x32_f16 v[56:59], v[144:147], v[160:163], v[56:59]\n" \
"buffer_load_dwordx4 v232, s[36:39], s80 offen offset:2048 sc1 lds\n" \
"v_mfma_f32_16x16x32_f16 v[68:71], v[148:151], v[160:163], v[68:71]\n" \
"ds_read_b64_tr_b16 v[154:155], v241 offset:8704\n" \
"ds_read_b64_tr_b16 v[156:157], v242 offset:512\n" \
"v_mfma_f32_16x16x32_f16 v[72:75], v[128:131], v[168:171], v[72:75]\n" \
"ds_read_b128 v[160:163], v240 offset:0\n" \
"ds_read_b64_tr_b16 v[158:159], v242 offset:8704\n" \
"v_mfma_f32_16x16x32_f16 v[84:87], v[132:135], v[168:171], v[84:87]\n" \
"s_waitcnt lgkmcnt(6)\n" \
"v_pk_add_f16 v168, v168, v172\n" \
"v_pk_add_f16 v169, v169, v173\n" \
"v_mfma_f32_16x16x32_f16 v[76:79], v[136:139], v[172:175], v[76:79]\n" \
"v_pk_add_f16 v170, v170, v174\n" \
"v_pk_add_f16 v171, v171, v175\n" \
"v_mfma_f32_16x16x32_f16 v[88:91], v[140:143], v[172:175], v[88:91]\n" \
"ds_read_b128 v[164:167], v240 offset:512\n" \
"v_mfma_f32_16x16x32_f16 v[80:83], v[144:147], v[168:171], v[80:83]\n" \
"buffer_load_dwordx4 v232, s[36:39], s80 offen offset:3072 sc1 lds\n" \
"v_mfma_f32_16x16x32_f16 v[92:95], v[148:151], v[168:171], v[92:95]\n" \
"v_mfma_f32_16x16x32_f16 v[96:99], v[128:131], v[176:179], v[96:99]\n" \
"s_waitcnt lgkmcnt(5)\n" \
"v_pk_add_f16 v128, v120, v152\n" \
"v_pk_add_f16 v129, v121, v153\n" \
"ds_read_b128 v[168:171], v240 offset:2048\n" \
"v_mfma_f32_16x16x32_f16 v[108:111], v[132:135], v[176:179], v[108:111]\n" \
"v_pk_add_f16 v176, v176, v180\n" \
"v_pk_add_f16 v177, v177, v181\n" \
"ds_read_b128 v[172:175], v240 offset:2560\n" \
"v_mfma_f32_16x16x32_f16 v[100:103], v[136:139], v[180:183], v[100:103]\n" \
"v_pk_add_f16 v178, v178, v182\n" \
"v_pk_add_f16 v179, v179, v183\n" \
"v_mfma_f32_16x16x32_f16 v[112:115], v[140:143], v[180:183], v[112:115]\n" \
"s_waitcnt lgkmcnt(3)\n" \
"v_pk_add_f16 v130, v122, v154\n" \
"v_pk_add_f16 v131, v123, v155\n" \
"v_mfma_f32_16x16x32_f16 v[104:107], v[144:147], v[176:179], v[104:107]\n" \
"v_pk_add_f16 v132, v124, v156\n" \
"v_pk_add_f16 v133, v125, v157\n" \
"v_mfma_f32_16x16x32_f16 v[116:119], v[148:151], v[176:179], v[116:119]\n" \
"v_pk_add_f16 v134, v126, v158\n" \
"v_pk_add_f16 v135, v127, v159\n" \
"s_waitcnt vmcnt(6) lgkmcnt(0)\n" \
"s_barrier\n" \
"v_mfma_f32_16x16x32_f16 v[0:3], v[120:123], v[160:163], v[0:3]\n" \
"ds_read_b128 v[176:179], v240 offset:24576\n" \
"ds_read_b64_tr_b16 v[184:185], v238 offset:4096\n" \
"ds_read_b64_tr_b16 v[188:189], v239 offset:4096\n" \
"v_mfma_f32_16x16x32_f16 v[12:15], v[124:127], v[160:163], v[12:15]\n" \
"v_pk_add_f16 v160, v160, v164\n" \
"v_pk_add_f16 v161, v161, v165\n" \
"v_mfma_f32_16x16x32_f16 v[4:7], v[152:155], v[164:167], v[4:7]\n" \
"v_pk_add_f16 v162, v162, v166\n" \
"v_pk_add_f16 v163, v163, v167\n" \
"v_mfma_f32_16x16x32_f16 v[16:19], v[156:159], v[164:167], v[16:19]\n" \
"ds_read_b128 v[180:183], v240 offset:25088\n" \
"s_add_i32 m0, s44, 0x0\n" \
"s_add_i32 s80, s46, 0x1400000\n" \
"v_mfma_f32_16x16x32_f16 v[8:11], v[128:131], v[160:163], v[8:11]\n" \
"buffer_load_dwordx4 v232, s[36:39], s80 offen sc1 lds\n" \
"s_waitcnt lgkmcnt(2)\n" \
"v_mfma_f32_16x16x16_bf16 v[184:187], v[184:185], v[192:193], 0\n" \
"v_mfma_f32_16x16x32_f16 v[20:23], v[132:135], v[160:163], v[20:23]\n" \
"ds_read_b64_tr_b16 v[136:137], v241 offset:16384\n" \
"ds_read_b64_tr_b16 v[138:139], v241 offset:24576\n" \
"s_waitcnt lgkmcnt(3)\n" \
"v_mfma_f32_16x16x16_bf16 v[188:191], v[188:189], v[192:193], 0\n" \
"v_mfma_f32_16x16x32_f16 v[24:27], v[120:123], v[168:171], v[24:27]\n" \
"ds_read_b128 v[160:163], v240 offset:26624\n" \
"s_add_i32 s84, s53, 0x8006000\n" \
"v_mfma_f32_16x16x32_f16 v[36:39], v[124:127], v[168:171], v[36:39]\n" \
"v_pk_add_f16 v168, v168, v172\n" \
"v_pk_add_f16 v169, v169, v173\n" \
"buffer_store_dword v240, v243, s[36:39], s84 offen sc1\n" \
"v_mfma_f32_16x16x32_f16 v[28:31], v[152:155], v[172:175], v[28:31]\n" \
"v_pk_add_f16 v170, v170, v174\n" \
"v_pk_add_f16 v171, v171, v175\n" \
"v_cvt_pk_f16_f32 v184, v184, v185\n" \
"ds_read_b64_tr_b16 v[140:141], v242 offset:16384\n" \
"v_mfma_f32_16x16x32_f16 v[40:43], v[156:159], v[172:175], v[40:43]\n" \
"v_cvt_pk_f16_f32 v185, v186, v187\n" \
"v_cvt_pk_f16_f32 v186, v188, v189\n" \
"ds_read_b128 v[164:167], v240 offset:27136\n" \
"v_mfma_f32_16x16x32_f16 v[32:35], v[128:131], v[168:171], v[32:35]\n" \
"v_cvt_pk_f16_f32 v187, v190, v191\n" \
"v_add_u32 v184, s91, v184\n" \
"buffer_load_dwordx4 v232, s[36:39], s80 offen offset:1024 sc1 lds\n" \
"v_mfma_f32_16x16x32_f16 v[44:47], v[132:135], v[168:171], v[44:47]\n" \
"v_add_u32 v185, s91, v185\n" \
"v_add_u32 v186, s91, v186\n" \
"ds_read_b64_tr_b16 v[142:143], v242 offset:24576\n" \
"v_mfma_f32_16x16x32_f16 v[48:51], v[120:123], v[176:179], v[48:51]\n" \
"v_add_u32 v187, s91, v187\n" \
"v_and_b32 v184, s92, v184\n" \
"ds_read_b128 v[168:171], v240 offset:28672\n" \
"v_mfma_f32_16x16x32_f16 v[60:63], v[124:127], v[176:179], v[60:63]\n" \
"s_waitcnt lgkmcnt(4)\n" \
"v_pk_add_f16 v176, v176, v180\n" \
"v_pk_add_f16 v177, v177, v181\n" \
"v_and_b32 v185, s92, v185\n" \
"v_mfma_f32_16x16x32_f16 v[52:55], v[152:155], v[180:183], v[52:55]\n" \
"v_pk_add_f16 v178, v178, v182\n" \
"v_pk_add_f16 v179, v179, v183\n" \
"v_and_b32 v186, s92, v186\n" \
"v_and_b32 v187, s92, v187\n" \
"v_mfma_f32_16x16x32_f16 v[64:67], v[156:159], v[180:183], v[64:67]\n" \
"ds_read_b128 v[172:175], v240 offset:29184\n" \
"buffer_store_dwordx4 v[184:187], v234, s[36:39], s82 offen offset:1024 sc1\n" \
"ds_read_b64_tr_b16 v[144:145], v241 offset:16896\n" \
"v_mfma_f32_16x16x32_f16 v[56:59], v[128:131], v[176:179], v[56:59]\n" \
"buffer_load_dwordx4 v232, s[36:39], s80 offen offset:2048 sc1 lds\n" \
"v_mfma_f32_16x16x32_f16 v[68:71], v[132:135], v[176:179], v[68:71]\n" \
"ds_read_b64_tr_b16 v[146:147], v241 offset:25088\n" \
"ds_read_b64_tr_b16 v[148:149], v242 offset:16896\n" \
"v_mfma_f32_16x16x32_f16 v[72:75], v[120:123], v[160:163], v[72:75]\n" \
"ds_read_b128 v[176:179], v240 offset:20480\n" \
"ds_read_b64_tr_b16 v[150:151], v242 offset:25088\n" \
"v_mfma_f32_16x16x32_f16 v[84:87], v[124:127], v[160:163], v[84:87]\n" \
"s_waitcnt lgkmcnt(6)\n" \
"v_pk_add_f16 v160, v160, v164\n" \
"v_pk_add_f16 v161, v161, v165\n" \
"v_mfma_f32_16x16x32_f16 v[76:79], v[152:155], v[164:167], v[76:79]\n" \
"v_pk_add_f16 v162, v162, v166\n" \
"v_pk_add_f16 v163, v163, v167\n" \
"v_mfma_f32_16x16x32_f16 v[88:91], v[156:159], v[164:167], v[88:91]\n" \
"ds_read_b128 v[180:183], v240 offset:20992\n" \
"v_mfma_f32_16x16x32_f16 v[80:83], v[128:131], v[160:163], v[80:83]\n" \
"buffer_load_dwordx4 v232, s[36:39], s80 offen offset:3072 sc1 lds\n" \
"v_mfma_f32_16x16x32_f16 v[92:95], v[132:135], v[160:163], v[92:95]\n" \
"v_mfma_f32_16x16x32_f16 v[96:99], v[120:123], v[168:171], v[96:99]\n" \
"s_waitcnt lgkmcnt(5)\n" \
"v_pk_add_f16 v120, v136, v144\n" \
"v_pk_add_f16 v121, v137, v145\n" \
"ds_read_b128 v[160:163], v240 offset:22528\n" \
"v_mfma_f32_16x16x32_f16 v[108:111], v[124:127], v[168:171], v[108:111]\n" \
"v_pk_add_f16 v168, v168, v172\n" \
"v_pk_add_f16 v169, v169, v173\n" \
"ds_read_b128 v[164:167], v240 offset:23040\n" \
"v_mfma_f32_16x16x32_f16 v[100:103], v[152:155], v[172:175], v[100:103]\n" \
"v_pk_add_f16 v170, v170, v174\n" \
"v_pk_add_f16 v171, v171, v175\n" \
"v_mfma_f32_16x16x32_f16 v[112:115], v[156:159], v[172:175], v[112:115]\n" \
"s_waitcnt lgkmcnt(3)\n" \
"v_pk_add_f16 v122, v138, v146\n" \
"v_pk_add_f16 v123, v139, v147\n" \
"v_mfma_f32_16x16x32_f16 v[104:107], v[128:131], v[168:171], v[104:107]\n" \
"v_pk_add_f16 v124, v140, v148\n" \
"v_pk_add_f16 v125, v141, v149\n" \
"v_mfma_f32_16x16x32_f16 v[116:119], v[132:135], v[168:171], v[116:119]\n" \
"v_pk_add_f16 v126, v142, v150\n" \
"v_pk_add_f16 v127, v143, v151\n" \
"s_waitcnt vmcnt(6) lgkmcnt(0)\n" \
"s_barrier\n" \
"v_mfma_f32_16x16x32_f16 v[0:3], v[136:139], v[176:179], v[0:3]\n" \
"ds_read_b128 v[168:171], v240 offset:45056\n" \
"ds_read_b64_tr_b16 v[184:185], v236 offset:8192\n" \
"ds_read_b64_tr_b16 v[188:189], v237 offset:8192\n" \
"v_mfma_f32_16x16x32_f16 v[12:15], v[140:143], v[176:179], v[12:15]\n" \
"v_pk_add_f16 v176, v176, v180\n" \
"v_pk_add_f16 v177, v177, v181\n" \
"v_mfma_f32_16x16x32_f16 v[4:7], v[144:147], v[180:183], v[4:7]\n" \
"v_pk_add_f16 v178, v178, v182\n" \
"v_pk_add_f16 v179, v179, v183\n" \
"buffer_store_dwordx4 v[208:211], v235, s[36:39], s63 offen sc1\n" \
"v_mfma_f32_16x16x32_f16 v[16:19], v[148:151], v[180:183], v[16:19]\n" \
"ds_read_b128 v[172:175], v240 offset:45568\n" \
"s_add_i32 m0, s44, 0x4000\n" \
"s_add_i32 s80, s46, 0x1800000\n" \
"v_mfma_f32_16x16x32_f16 v[8:11], v[120:123], v[176:179], v[8:11]\n" \
"buffer_load_dwordx4 v232, s[36:39], s80 offen sc1 lds\n" \
"s_waitcnt lgkmcnt(2)\n" \
"v_mfma_f32_16x16x16_bf16 v[184:187], v[184:185], v[192:193], 0\n" \
"v_mfma_f32_16x16x32_f16 v[20:23], v[124:127], v[176:179], v[20:23]\n" \
"ds_read_b64_tr_b16 v[128:129], v241 offset:32768\n" \
"ds_read_b64_tr_b16 v[130:131], v241 offset:40960\n" \
"s_waitcnt lgkmcnt(3)\n" \
"v_mfma_f32_16x16x16_bf16 v[188:191], v[188:189], v[192:193], 0\n" \
"v_mfma_f32_16x16x32_f16 v[24:27], v[136:139], v[160:163], v[24:27]\n" \
"ds_read_b128 v[176:179], v240 offset:47104\n" \
"v_mfma_f32_16x16x32_f16 v[36:39], v[140:143], v[160:163], v[36:39]\n" \
"v_pk_add_f16 v160, v160, v164\n" \
"v_pk_add_f16 v161, v161, v165\n" \
"ds_read_b64_tr_b16 v[132:133], v242 offset:32768\n" \
"v_mfma_f32_16x16x32_f16 v[28:31], v[144:147], v[164:167], v[28:31]\n" \
"v_pk_add_f16 v162, v162, v166\n" \
"v_pk_add_f16 v163, v163, v167\n" \
"v_cvt_pk_f16_f32 v184, v184, v185\n" \
"v_mfma_f32_16x16x32_f16 v[40:43], v[148:151], v[164:167], v[40:43]\n" \
"v_cvt_pk_f16_f32 v185, v186, v187\n" \
"v_cvt_pk_f16_f32 v186, v188, v189\n" \
"ds_read_b128 v[180:183], v240 offset:47616\n" \
"v_mfma_f32_16x16x32_f16 v[32:35], v[120:123], v[160:163], v[32:35]\n" \
"v_cvt_pk_f16_f32 v187, v190, v191\n" \
"v_add_u32 v184, s91, v184\n" \
"buffer_load_dwordx4 v232, s[36:39], s80 offen offset:1024 sc1 lds\n" \
"v_mfma_f32_16x16x32_f16 v[44:47], v[124:127], v[160:163], v[44:47]\n" \
"v_add_u32 v185, s91, v185\n" \
"v_add_u32 v186, s91, v186\n" \
"ds_read_b64_tr_b16 v[134:135], v242 offset:40960\n" \
"v_mfma_f32_16x16x32_f16 v[48:51], v[136:139], v[168:171], v[48:51]\n" \
"v_add_u32 v187, s91, v187\n" \
"v_and_b32 v184, s92, v184\n" \
"ds_read_b128 v[160:163], v240 offset:49152\n" \
"v_mfma_f32_16x16x32_f16 v[60:63], v[140:143], v[168:171], v[60:63]\n" \
"s_waitcnt lgkmcnt(4)\n" \
"v_pk_add_f16 v168, v168, v172\n" \
"v_pk_add_f16 v169, v169, v173\n" \
"v_and_b32 v185, s92, v185\n" \
"s_add_i32 s82, s51, 0x3800000\n" \
"v_mfma_f32_16x16x32_f16 v[52:55], v[144:147], v[172:175], v[52:55]\n" \
"v_pk_add_f16 v170, v170, v174\n" \
"v_pk_add_f16 v171, v171, v175\n" \
"v_and_b32 v186, s92, v186\n" \
"v_and_b32 v187, s92, v187\n" \
"v_mfma_f32_16x16x32_f16 v[64:67], v[148:151], v[172:175], v[64:67]\n" \
"ds_read_b128 v[164:167], v240 offset:49664\n" \
"buffer_store_dwordx4 v[184:187], v234, s[36:39], s82 offen sc1\n" \
"ds_read_b64_tr_b16 v[152:153], v241 offset:33280\n" \
"v_mfma_f32_16x16x32_f16 v[56:59], v[120:123], v[168:171], v[56:59]\n" \
"buffer_load_dwordx4 v232, s[36:39], s80 offen offset:2048 sc1 lds\n" \
"v_mfma_f32_16x16x32_f16 v[68:71], v[124:127], v[168:171], v[68:71]\n" \
"ds_read_b64_tr_b16 v[154:155], v241 offset:41472\n" \
"ds_read_b64_tr_b16 v[156:157], v242 offset:33280\n" \
"v_mfma_f32_16x16x32_f16 v[72:75], v[136:139], v[176:179], v[72:75]\n" \
"ds_read_b128 v[168:171], v240 offset:40960\n" \
"ds_read_b64_tr_b16 v[158:159], v242 offset:41472\n" \
"v_mfma_f32_16x16x32_f16 v[84:87], v[140:143], v[176:179], v[84:87]\n" \
"s_waitcnt lgkmcnt(6)\n" \
"v_pk_add_f16 v176, v176, v180\n" \
"v_pk_add_f16 v177, v177, v181\n" \
"v_mfma_f32_16x16x32_f16 v[76:79], v[144:147], v[180:183], v[76:79]\n" \
"v_pk_add_f16 v178, v178, v182\n" \
"v_pk_add_f16 v179, v179, v183\n" \
"v_mfma_f32_16x16x32_f16 v[88:91], v[148:151], v[180:183], v[88:91]\n" \
"ds_read_b128 v[172:175], v240 offset:41472\n" \
"v_mfma_f32_16x16x32_f16 v[80:83], v[120:123], v[176:179], v[80:83]\n" \
"buffer_load_dwordx4 v232, s[36:39], s80 offen offset:3072 sc1 lds\n" \
"v_mfma_f32_16x16x32_f16 v[92:95], v[124:127], v[176:179], v[92:95]\n" \
"v_mfma_f32_16x16x32_f16 v[96:99], v[136:139], v[160:163], v[96:99]\n" \
"s_waitcnt lgkmcnt(5)\n" \
"v_pk_add_f16 v136, v128, v152\n" \
"v_pk_add_f16 v137, v129, v153\n" \
"ds_read_b128 v[176:179], v240 offset:43008\n" \
"v_mfma_f32_16x16x32_f16 v[108:111], v[140:143], v[160:163], v[108:111]\n" \
"v_pk_add_f16 v160, v160, v164\n" \
"v_pk_add_f16 v161, v161, v165\n" \
"ds_read_b128 v[180:183], v240 offset:43520\n" \
"v_mfma_f32_16x16x32_f16 v[100:103], v[144:147], v[164:167], v[100:103]\n" \
"v_pk_add_f16 v162, v162, v166\n" \
"v_pk_add_f16 v163, v163, v167\n" \
"v_mfma_f32_16x16x32_f16 v[112:115], v[148:151], v[164:167], v[112:115]\n" \
"s_waitcnt lgkmcnt(3)\n" \
"v_pk_add_f16 v138, v130, v154\n" \
"v_pk_add_f16 v139, v131, v155\n" \
"v_mfma_f32_16x16x32_f16 v[104:107], v[120:123], v[160:163], v[104:107]\n" \
"v_pk_add_f16 v140, v132, v156\n" \
"v_pk_add_f16 v141, v133, v157\n" \
"v_mfma_f32_16x16x32_f16 v[116:119], v[124:127], v[160:163], v[116:119]\n" \
"v_pk_add_f16 v142, v134, v158\n" \
"v_pk_add_f16 v143, v135, v159\n" \
"s_waitcnt vmcnt(6) lgkmcnt(0)\n" \
"s_barrier\n" \
"v_mfma_f32_16x16x32_f16 v[0:3], v[128:131], v[168:171], v[0:3]\n" \
"ds_read_b128 v[160:163], v240 offset:4096\n" \
"ds_read_b64_tr_b16 v[184:185], v238 offset:8192\n" \
"ds_read_b64_tr_b16 v[188:189], v239 offset:8192\n" \
"v_mfma_f32_16x16x32_f16 v[12:15], v[132:135], v[168:171], v[12:15]\n" \
"v_pk_add_f16 v168, v168, v172\n" \
"v_pk_add_f16 v169, v169, v173\n" \
"v_mfma_f32_16x16x32_f16 v[4:7], v[152:155], v[172:175], v[4:7]\n" \
"v_pk_add_f16 v170, v170, v174\n" \
"v_pk_add_f16 v171, v171, v175\n" \
"v_mfma_f32_16x16x32_f16 v[16:19], v[156:159], v[172:175], v[16:19]\n" \
"ds_read_b128 v[164:167], v240 offset:4608\n" \
"s_add_i32 m0, s44, 0x8000\n" \
"s_add_i32 s80, s46, 0x1c00000\n" \
"v_mfma_f32_16x16x32_f16 v[8:11], v[136:139], v[168:171], v[8:11]\n" \
"buffer_load_dwordx4 v232, s[36:39], s80 offen sc1 lds\n" \
"s_waitcnt lgkmcnt(2)\n" \
"v_mfma_f32_16x16x16_bf16 v[184:187], v[184:185], v[192:193], 0\n" \
"v_mfma_f32_16x16x32_f16 v[20:23], v[140:143], v[168:171], v[20:23]\n" \
"ds_read_b64_tr_b16 v[120:121], v241 offset:0\n" \
"ds_read_b64_tr_b16 v[122:123], v241 offset:8192\n" \
"s_waitcnt lgkmcnt(3)\n" \
"v_mfma_f32_16x16x16_bf16 v[188:191], v[188:189], v[192:193], 0\n" \
"v_mfma_f32_16x16x32_f16 v[24:27], v[128:131], v[176:179], v[24:27]\n" \
"ds_read_b128 v[168:171], v240 offset:6144\n" \
"s_add_i32 s84, s54, 0x8008000\n" \
"v_mfma_f32_16x16x32_f16 v[36:39], v[132:135], v[176:179], v[36:39]\n" \
"v_pk_add_f16 v176, v176, v180\n" \
"v_pk_add_f16 v177, v177, v181\n" \
"buffer_store_dword v240, v243, s[36:39], s84 offen sc1\n" \
"v_mfma_f32_16x16x32_f16 v[28:31], v[152:155], v[180:183], v[28:31]\n" \
"v_pk_add_f16 v178, v178, v182\n" \
"v_pk_add_f16 v179, v179, v183\n" \
"v_cvt_pk_f16_f32 v184, v184, v185\n" \
"ds_read_b64_tr_b16 v[124:125], v242 offset:0\n" \
"v_mfma_f32_16x16x32_f16 v[40:43], v[156:159], v[180:183], v[40:43]\n" \
"v_cvt_pk_f16_f32 v185, v186, v187\n" \
"v_cvt_pk_f16_f32 v186, v188, v189\n" \
"ds_read_b128 v[172:175], v240 offset:6656\n" \
"v_mfma_f32_16x16x32_f16 v[32:35], v[136:139], v[176:179], v[32:35]\n" \
"v_cvt_pk_f16_f32 v187, v190, v191\n" \
"v_add_u32 v184, s91, v184\n" \
"buffer_load_dwordx4 v232, s[36:39], s80 offen offset:1024 sc1 lds\n" \
"v_mfma_f32_16x16x32_f16 v[44:47], v[140:143], v[176:179], v[44:47]\n" \
"v_add_u32 v185, s91, v185\n" \
"v_add_u32 v186, s91, v186\n" \
"ds_read_b64_tr_b16 v[126:127], v242 offset:8192\n" \
"v_mfma_f32_16x16x32_f16 v[48:51], v[128:131], v[160:163], v[48:51]\n" \
"v_add_u32 v187, s91, v187\n" \
"v_and_b32 v184, s92, v184\n" \
"ds_read_b128 v[176:179], v240 offset:8192\n" \
"v_mfma_f32_16x16x32_f16 v[60:63], v[132:135], v[160:163], v[60:63]\n" \
"s_waitcnt lgkmcnt(4)\n" \
"v_pk_add_f16 v160, v160, v164\n" \
"v_pk_add_f16 v161, v161, v165\n" \
"v_and_b32 v185, s92, v185\n" \
"v_mfma_f32_16x16x32_f16 v[52:55], v[152:155], v[164:167], v[52:55]\n" \
"v_pk_add_f16 v162, v162, v166\n" \
"v_pk_add_f16 v163, v163, v167\n" \
"v_and_b32 v186, s92, v186\n" \
"v_and_b32 v187, s92, v187\n" \
"v_mfma_f32_16x16x32_f16 v[64:67], v[156:159], v[164:167], v[64:67]\n" \
"ds_read_b128 v[180:183], v240 offset:8704\n" \
"buffer_store_dwordx4 v[184:187], v234, s[36:39], s82 offen offset:1024 sc1\n" \
"ds_read_b64_tr_b16 v[144:145], v241 offset:512\n" \
"v_mfma_f32_16x16x32_f16 v[56:59], v[136:139], v[160:163], v[56:59]\n" \
"buffer_load_dwordx4 v232, s[36:39], s80 offen offset:2048 sc1 lds\n" \
"v_mfma_f32_16x16x32_f16 v[68:71], v[140:143], v[160:163], v[68:71]\n" \
"ds_read_b64_tr_b16 v[146:147], v241 offset:8704\n" \
"ds_read_b64_tr_b16 v[148:149], v242 offset:512\n" \
"v_mfma_f32_16x16x32_f16 v[72:75], v[128:131], v[168:171], v[72:75]\n" \
"ds_read_b128 v[160:163], v240 offset:0\n" \
"ds_read_b64_tr_b16 v[150:151], v242 offset:8704\n" \
"v_mfma_f32_16x16x32_f16 v[84:87], v[132:135], v[168:171], v[84:87]\n" \
"s_waitcnt lgkmcnt(6)\n" \
"v_pk_add_f16 v168, v168, v172\n" \
"v_pk_add_f16 v169, v169, v173\n" \
"v_mfma_f32_16x16x32_f16 v[76:79], v[152:155], v[172:175], v[76:79]\n" \
"v_pk_add_f16 v170, v170, v174\n" \
"v_pk_add_f16 v171, v171, v175\n" \
"v_mfma_f32_16x16x32_f16 v[88:91], v[156:159], v[172:175], v[88:91]\n" \
"ds_read_b128 v[164:167], v240 offset:512\n" \
"v_mfma_f32_16x16x32_f16 v[80:83], v[136:139], v[168:171], v[80:83]\n" \
"buffer_load_dwordx4 v232, s[36:39], s80 offen offset:3072 sc1 lds\n" \
"v_mfma_f32_16x16x32_f16 v[92:95], v[140:143], v[168:171], v[92:95]\n" \
"v_mfma_f32_16x16x32_f16 v[96:99], v[128:131], v[176:179], v[96:99]\n" \
"s_waitcnt lgkmcnt(5)\n" \
"v_pk_add_f16 v128, v120, v144\n" \
"v_pk_add_f16 v129, v121, v145\n" \
"ds_read_b128 v[168:171], v240 offset:2048\n" \
"v_mfma_f32_16x16x32_f16 v[108:111], v[132:135], v[176:179], v[108:111]\n" \
"v_pk_add_f16 v176, v176, v180\n" \
"v_pk_add_f16 v177, v177, v181\n" \
"ds_read_b128 v[172:175], v240 offset:2560\n" \
"v_mfma_f32_16x16x32_f16 v[100:103], v[152:155], v[180:183], v[100:103]\n" \
"v_pk_add_f16 v178, v178, v182\n" \
"v_pk_add_f16 v179, v179, v183\n" \
"v_mfma_f32_16x16x32_f16 v[112:115], v[156:159], v[180:183], v[112:115]\n" \
"s_waitcnt lgkmcnt(3)\n" \
"v_pk_add_f16 v130, v122, v146\n" \
"v_pk_add_f16 v131, v123, v147\n" \
"v_mfma_f32_16x16x32_f16 v[104:107], v[136:139], v[176:179], v[104:107]\n" \
"v_pk_add_f16 v132, v124, v148\n" \
"v_pk_add_f16 v133, v125, v149\n" \
"v_mfma_f32_16x16x32_f16 v[116:119], v[140:143], v[176:179], v[116:119]\n" \
"v_pk_add_f16 v134, v126, v150\n" \
"v_pk_add_f16 v135, v127, v151\n" \
"s_waitcnt vmcnt(6) lgkmcnt(0)\n" \
"s_barrier\n" \
"v_mfma_f32_16x16x32_f16 v[0:3], v[120:123], v[160:163], v[0:3]\n" \
"ds_read_b128 v[176:179], v240 offset:24576\n" \
"s_mov_b32 s81, 0x8006000\n" \
"ds_read_b64_tr_b16 v[184:185], v236 offset:12288\n" \
"ds_read_b64_tr_b16 v[188:189], v237 offset:12288\n" \
"v_mfma_f32_16x16x32_f16 v[12:15], v[124:127], v[160:163], v[12:15]\n" \
"v_pk_add_f16 v160, v160, v164\n" \
"v_pk_add_f16 v161, v161, v165\n" \
"buffer_load_dword v253, v252, s[36:39], s81 offen sc1\n" \
"v_mfma_f32_16x16x32_f16 v[4:7], v[144:147], v[164:167], v[4:7]\n" \
"v_pk_add_f16 v162, v162, v166\n" \
"v_pk_add_f16 v163, v163, v167\n" \
"buffer_store_dwordx4 v[212:215], v235, s[36:39], s64 offen sc1\n" \
"v_mfma_f32_16x16x32_f16 v[16:19], v[148:151], v[164:167], v[16:19]\n" \
"ds_read_b128 v[180:183], v240 offset:25088\n" \
"s_add_i32 m0, s44, 0x0\n" \
"s_add_i32 s80, s46, 0x2000000\n" \
"v_mfma_f32_16x16x32_f16 v[8:11], v[128:131], v[160:163], v[8:11]\n" \
"buffer_load_dwordx4 v232, s[36:39], s80 offen sc1 lds\n" \
"s_waitcnt lgkmcnt(2)\n" \
"v_mfma_f32_16x16x16_bf16 v[184:187], v[184:185], v[192:193], 0\n" \
"v_mfma_f32_16x16x32_f16 v[20:23], v[132:135], v[160:163], v[20:23]\n" \
"ds_read_b64_tr_b16 v[136:137], v241 offset:16384\n" \
"ds_read_b64_tr_b16 v[138:139], v241 offset:24576\n" \
"s_waitcnt lgkmcnt(3)\n" \
"v_mfma_f32_16x16x16_bf16 v[188:191], v[188:189], v[192:193], 0\n" \
"v_mfma_f32_16x16x32_f16 v[24:27], v[120:123], v[168:171], v[24:27]\n" \
"ds_read_b128 v[160:163], v240 offset:26624\n" \
"v_mfma_f32_16x16x32_f16 v[36:39], v[124:127], v[168:171], v[36:39]\n" \
"v_pk_add_f16 v168, v168, v172\n" \
"v_pk_add_f16 v169, v169, v173\n" \
"ds_read_b64_tr_b16 v[140:141], v242 offset:16384\n" \
"v_mfma_f32_16x16x32_f16 v[28:31], v[144:147], v[172:175], v[28:31]\n" \
"v_pk_add_f16 v170, v170, v174\n" \
"v_pk_add_f16 v171, v171, v175\n" \
"v_cvt_pk_f16_f32 v184, v184, v185\n" \
"v_mfma_f32_16x16x32_f16 v[40:43], v[148:151], v[172:175], v[40:43]\n" \
"v_cvt_pk_f16_f32 v185, v186, v187\n" \
"v_cvt_pk_f16_f32 v186, v188, v189\n" \
"ds_read_b128 v[164:167], v240 offset:27136\n" \
"v_mfma_f32_16x16x32_f16 v[32:35], v[128:131], v[168:171], v[32:35]\n" \
"v_cvt_pk_f16_f32 v187, v190, v191\n" \
"v_add_u32 v184, s91, v184\n" \
"buffer_load_dwordx4 v232, s[36:39], s80 offen offset:1024 sc1 lds\n" \
"v_mfma_f32_16x16x32_f16 v[44:47], v[132:135], v[168:171], v[44:47]\n" \
"v_add_u32 v185, s91, v185\n" \
"v_add_u32 v186, s91, v186\n" \
"ds_read_b64_tr_b16 v[142:143], v242 offset:24576\n" \
"v_mfma_f32_16x16x32_f16 v[48:51], v[120:123], v[176:179], v[48:51]\n" \
"v_add_u32 v187, s91, v187\n" \
"v_and_b32 v184, s92, v184\n" \
"ds_read_b128 v[168:171], v240 offset:28672\n" \
"v_mfma_f32_16x16x32_f16 v[60:63], v[124:127], v[176:179], v[60:63]\n" \
"s_waitcnt lgkmcnt(4)\n" \
"v_pk_add_f16 v176, v176, v180\n" \
"v_pk_add_f16 v177, v177, v181\n" \
"v_and_b32 v185, s92, v185\n" \
"s_add_i32 s82, s48, 0x4000000\n" \
"v_mfma_f32_16x16x32_f16 v[52:55], v[144:147], v[180:183], v[52:55]\n" \
"v_pk_add_f16 v178, v178, v182\n" \
"v_pk_add_f16 v179, v179, v183\n" \
"v_and_b32 v186, s92, v186\n" \
"v_and_b32 v187, s92, v187\n" \
"v_mfma_f32_16x16x32_f16 v[64:67], v[148:151], v[180:183], v[64:67]\n" \
"ds_read_b128 v[172:175], v240 offset:29184\n" \
"buffer_store_dwordx4 v[184:187], v234, s[36:39], s82 offen sc1\n" \
"ds_read_b64_tr_b16 v[152:153], v241 offset:16896\n" \
"v_mfma_f32_16x16x32_f16 v[56:59], v[128:131], v[176:179], v[56:59]\n" \
"buffer_load_dwordx4 v232, s[36:39], s80 offen offset:2048 sc1 lds\n" \
"v_mfma_f32_16x16x32_f16 v[68:71], v[132:135], v[176:179], v[68:71]\n" \
"ds_read_b64_tr_b16 v[154:155], v241 offset:25088\n" \
"ds_read_b64_tr_b16 v[156:157], v242 offset:16896\n" \
"v_mfma_f32_16x16x32_f16 v[72:75], v[120:123], v[160:163], v[72:75]\n" \
"ds_read_b128 v[176:179], v240 offset:20480\n" \
"ds_read_b64_tr_b16 v[158:159], v242 offset:25088\n" \
"v_mfma_f32_16x16x32_f16 v[84:87], v[124:127], v[160:163], v[84:87]\n" \
"s_waitcnt lgkmcnt(6)\n" \
"v_pk_add_f16 v160, v160, v164\n" \
"v_pk_add_f16 v161, v161, v165\n" \
"v_mfma_f32_16x16x32_f16 v[76:79], v[144:147], v[164:167], v[76:79]\n" \
"v_pk_add_f16 v162, v162, v166\n" \
"v_pk_add_f16 v163, v163, v167\n" \
"v_mfma_f32_16x16x32_f16 v[88:91], v[148:151], v[164:167], v[88:91]\n" \
"ds_read_b128 v[180:183], v240 offset:20992\n" \
"v_mfma_f32_16x16x32_f16 v[80:83], v[128:131], v[160:163], v[80:83]\n" \
"buffer_load_dwordx4 v232, s[36:39], s80 offen offset:3072 sc1 lds\n" \
"v_mfma_f32_16x16x32_f16 v[92:95], v[132:135], v[160:163], v[92:95]\n" \
"v_mfma_f32_16x16x32_f16 v[96:99], v[120:123], v[168:171], v[96:99]\n" \
"s_waitcnt lgkmcnt(5)\n" \
"v_pk_add_f16 v120, v136, v152\n" \
"v_pk_add_f16 v121, v137, v153\n" \
"ds_read_b128 v[160:163], v240 offset:22528\n" \
"v_mfma_f32_16x16x32_f16 v[108:111], v[124:127], v[168:171], v[108:111]\n" \
"v_pk_add_f16 v168, v168, v172\n" \
"v_pk_add_f16 v169, v169, v173\n" \
"ds_read_b128 v[164:167], v240 offset:23040\n" \
"v_mfma_f32_16x16x32_f16 v[100:103], v[144:147], v[172:175], v[100:103]\n" \
"v_pk_add_f16 v170, v170, v174\n" \
"v_pk_add_f16 v171, v171, v175\n" \
"v_mfma_f32_16x16x32_f16 v[112:115], v[148:151], v[172:175], v[112:115]\n" \
"s_waitcnt lgkmcnt(3)\n" \
"v_pk_add_f16 v122, v138, v154\n" \
"v_pk_add_f16 v123, v139, v155\n" \
"v_mfma_f32_16x16x32_f16 v[104:107], v[128:131], v[168:171], v[104:107]\n" \
"v_pk_add_f16 v124, v140, v156\n" \
"v_pk_add_f16 v125, v141, v157\n" \
"v_mfma_f32_16x16x32_f16 v[116:119], v[132:135], v[168:171], v[116:119]\n" \
"v_pk_add_f16 v126, v142, v158\n" \
"v_pk_add_f16 v127, v143, v159\n" \
"s_waitcnt vmcnt(6) lgkmcnt(0)\n" \
"s_barrier\n" \
"v_mfma_f32_16x16x32_f16 v[0:3], v[136:139], v[176:179], v[0:3]\n" \
"ds_read_b128 v[168:171], v240 offset:45056\n" \
"ds_read_b64_tr_b16 v[184:185], v238 offset:12288\n" \
"ds_read_b64_tr_b16 v[188:189], v239 offset:12288\n" \
"v_mfma_f32_16x16x32_f16 v[12:15], v[140:143], v[176:179], v[12:15]\n" \
"v_pk_add_f16 v176, v176, v180\n" \
"v_pk_add_f16 v177, v177, v181\n" \
"v_mfma_f32_16x16x32_f16 v[4:7], v[152:155], v[180:183], v[4:7]\n" \
"v_pk_add_f16 v178, v178, v182\n" \
"v_pk_add_f16 v179, v179, v183\n" \
"v_cmp_eq_u32 vcc, 0, v253\n" \
"s_cbranch_vccnz .Ls03_%=\n" \
".Lr03_%=:\n" \
"v_mfma_f32_16x16x32_f16 v[16:19], v[156:159], v[180:183], v[16:19]\n" \
"ds_read_b128 v[172:175], v240 offset:45568\n" \
"s_add_i32 m0, s44, 0x4000\n" \
"s_add_i32 s80, s46, 0x2400000\n" \
"v_mfma_f32_16x16x32_f16 v[8:11], v[120:123], v[176:179], v[8:11]\n" \
"buffer_load_dwordx4 v232, s[36:39], s80 offen sc1 lds\n" \
"s_waitcnt lgkmcnt(2)\n" \
"v_mfma_f32_16x16x16_bf16 v[184:187], v[184:185], v[192:193], 0\n" \
"v_mfma_f32_16x16x32_f16 v[20:23], v[124:127], v[176:179], v[20:23]\n" \
"ds_read_b64_tr_b16 v[128:129], v241 offset:32768\n" \
"ds_read_b64_tr_b16 v[130:131], v241 offset:40960\n" \
"s_waitcnt lgkmcnt(3)\n" \
"v_mfma_f32_16x16x16_bf16 v[188:191], v[188:189], v[192:193], 0\n" \
"v_mfma_f32_16x16x32_f16 v[24:27], v[136:139], v[160:163], v[24:27]\n" \
"ds_read_b128 v[176:179], v240 offset:47104\n" \
"s_add_i32 s84, s55, 0x800a000\n" \
"v_mfma_f32_16x16x32_f16 v[36:39], v[140:143], v[160:163], v[36:39]\n" \
"v_pk_add_f16 v160, v160, v164\n" \
"v_pk_add_f16 v161, v161, v165\n" \
"buffer_store_dword v240, v243, s[36:39], s84 offen sc1\n" \
"v_mfma_f32_16x16x32_f16 v[28:31], v[152:155], v[164:167], v[28:31]\n" \
"v_pk_add_f16 v162, v162, v166\n" \
"v_pk_add_f16 v163, v163, v167\n" \
"v_cvt_pk_f16_f32 v184, v184, v185\n" \
"ds_read_b64_tr_b16 v[132:133], v242 offset:32768\n" \
"v_mfma_f32_16x16x32_f16 v[40:43], v[156:159], v[164:167], v[40:43]\n" \
"v_cvt_pk_f16_f32 v185, v186, v187\n" \
"v_cvt_pk_f16_f32 v186, v188, v189\n" \
"ds_read_b128 v[180:183], v240 offset:47616\n" \
"v_mfma_f32_16x16x32_f16 v[32:35], v[120:123], v[160:163], v[32:35]\n" \
"v_cvt_pk_f16_f32 v187, v190, v191\n" \
"v_add_u32 v184, s91, v184\n" \
"buffer_load_dwordx4 v232, s[36:39], s80 offen offset:1024 sc1 lds\n" \
"v_mfma_f32_16x16x32_f16 v[44:47], v[124:127], v[160:163], v[44:47]\n" \
"v_add_u32 v185, s91, v185\n" \
"v_add_u32 v186, s91, v186\n" \
"ds_read_b64_tr_b16 v[134:135], v242 offset:40960\n" \
"v_mfma_f32_16x16x32_f16 v[48:51], v[136:139], v[168:171], v[48:51]\n" \
"v_add_u32 v187, s91, v187\n" \
"v_and_b32 v184, s92, v184\n" \
"ds_read_b128 v[160:163], v240 offset:49152\n" \
"v_mfma_f32_16x16x32_f16 v[60:63], v[140:143], v[168:171], v[60:63]\n" \
"s_waitcnt lgkmcnt(4)\n" \
"v_pk_add_f16 v168, v168, v172\n" \
"v_pk_add_f16 v169, v169, v173\n" \
"v_and_b32 v185, s92, v185\n" \
"v_mfma_f32_16x16x32_f16 v[52:55], v[152:155], v[172:175], v[52:55]\n" \
"v_pk_add_f16 v170, v170, v174\n" \
"v_pk_add_f16 v171, v171, v175\n" \
"v_and_b32 v186, s92, v186\n" \
"v_and_b32 v187, s92, v187\n" \
"v_mfma_f32_16x16x32_f16 v[64:67], v[156:159], v[172:175], v[64:67]\n" \
"ds_read_b128 v[164:167], v240 offset:49664\n" \
"buffer_store_dwordx4 v[184:187], v234, s[36:39], s82 offen offset:1024 sc1\n" \
"ds_read_b64_tr_b16 v[144:145], v241 offset:33280\n" \
"v_mfma_f32_16x16x32_f16 v[56:59], v[120:123], v[168:171], v[56:59]\n" \
"buffer_load_dwordx4 v232, s[36:39], s80 offen offset:2048 sc1 lds\n" \
"v_mfma_f32_16x16x32_f16 v[68:71], v[124:127], v[168:171], v[68:71]\n" \
"ds_read_b64_tr_b16 v[146:147], v241 offset:41472\n" \
"ds_read_b64_tr_b16 v[148:149], v242 offset:33280\n" \
"v_mfma_f32_16x16x32_f16 v[72:75], v[136:139], v[176:179], v[72:75]\n" \
"ds_read_b128 v[168:171], v240 offset:40960\n" \
"ds_read_b64_tr_b16 v[150:151], v242 offset:41472\n" \
"v_mfma_f32_16x16x32_f16 v[84:87], v[140:143], v[176:179], v[84:87]\n" \
"s_waitcnt lgkmcnt(6)\n" \
"v_pk_add_f16 v176, v176, v180\n" \
"v_pk_add_f16 v177, v177, v181\n" \
"v_mfma_f32_16x16x32_f16 v[76:79], v[152:155], v[180:183], v[76:79]\n" \
"v_pk_add_f16 v178, v178, v182\n" \
"v_pk_add_f16 v179, v179, v183\n" \
"v_mfma_f32_16x16x32_f16 v[88:91], v[156:159], v[180:183], v[88:91]\n" \
"ds_read_b128 v[172:175], v240 offset:41472\n" \
"v_mfma_f32_16x16x32_f16 v[80:83], v[120:123], v[176:179], v[80:83]\n" \
"buffer_load_dwordx4 v232, s[36:39], s80 offen offset:3072 sc1 lds\n" \
"v_mfma_f32_16x16x32_f16 v[92:95], v[124:127], v[176:179], v[92:95]\n" \
"v_mfma_f32_16x16x32_f16 v[96:99], v[136:139], v[160:163], v[96:99]\n" \
"s_waitcnt lgkmcnt(5)\n" \
"v_pk_add_f16 v136, v128, v144\n" \
"v_pk_add_f16 v137, v129, v145\n" \
"ds_read_b128 v[176:179], v240 offset:43008\n" \
"v_mfma_f32_16x16x32_f16 v[108:111], v[140:143], v[160:163], v[108:111]\n" \
"v_pk_add_f16 v160, v160, v164\n" \
"v_pk_add_f16 v161, v161, v165\n" \
"ds_read_b128 v[180:183], v240 offset:43520\n" \
"v_mfma_f32_16x16x32_f16 v[100:103], v[152:155], v[164:167], v[100:103]\n" \
"v_pk_add_f16 v162, v162, v166\n" \
"v_pk_add_f16 v163, v163, v167\n" \
"v_mfma_f32_16x16x32_f16 v[112:115], v[156:159], v[164:167], v[112:115]\n" \
"s_waitcnt lgkmcnt(3)\n" \
"v_pk_add_f16 v138, v130, v146\n" \
"v_pk_add_f16 v139, v131, v147\n" \
"v_mfma_f32_16x16x32_f16 v[104:107], v[120:123], v[160:163], v[104:107]\n" \
"v_pk_add_f16 v140, v132, v148\n" \
"v_pk_add_f16 v141, v133, v149\n" \
"v_mfma_f32_16x16x32_f16 v[116:119], v[124:127], v[160:163], v[116:119]\n" \
"v_pk_add_f16 v142, v134, v150\n" \
"v_pk_add_f16 v143, v135, v151\n" \
"s_waitcnt vmcnt(6) lgkmcnt(0)\n" \
"s_barrier\n" \
"v_mfma_f32_16x16x32_f16 v[0:3], v[128:131], v[168:171], v[0:3]\n" \
"ds_read_b128 v[160:163], v240 offset:4096\n" \
"s_mov_b32 s81, 0x8008000\n" \
"ds_read_b64_tr_b16 v[184:185], v236 offset:16384\n" \
"ds_read_b64_tr_b16 v[188:189], v237 offset:16384\n" \
"v_mfma_f32_16x16x32_f16 v[12:15], v[132:135], v[168:171], v[12:15]\n" \
"v_pk_add_f16 v168, v168, v172\n" \
"v_pk_add_f16 v169, v169, v173\n" \
"buffer_load_dword v253, v252, s[36:39], s81 offen sc1\n" \
"v_mfma_f32_16x16x32_f16 v[4:7], v[144:147], v[172:175], v[4:7]\n" \
"v_pk_add_f16 v170, v170, v174\n" \
"v_pk_add_f16 v171, v171, v175\n" \
"buffer_store_dwordx4 v[216:219], v235, s[36:39], s65 offen sc1\n" \
"v_mfma_f32_16x16x32_f16 v[16:19], v[148:151], v[172:175], v[16:19]\n" \
"ds_read_b128 v[164:167], v240 offset:4608\n" \
"s_add_i32 m0, s44, 0x8000\n" \
"s_add_i32 s80, s46, 0x2800000\n" \
"v_mfma_f32_16x16x32_f16 v[8:11], v[136:139], v[168:171], v[8:11]\n" \
"buffer_load_dwordx4 v232, s[36:39], s80 offen sc1 lds\n" \
"s_waitcnt lgkmcnt(2)\n" \
"v_mfma_f32_16x16x16_bf16 v[184:187], v[184:185], v[192:193], 0\n" \
"v_mfma_f32_16x16x32_f16 v[20:23], v[140:143], v[168:171], v[20:23]\n" \
"ds_read_b64_tr_b16 v[120:121], v241 offset:0\n" \
"ds_read_b64_tr_b16 v[122:123], v241 offset:8192\n" \
"s_waitcnt lgkmcnt(3)\n" \
"v_mfma_f32_16x16x16_bf16 v[188:191], v[188:189], v[192:193], 0\n" \
"v_mfma_f32_16x16x32_f16 v[24:27], v[128:131], v[176:179], v[24:27]\n" \
"ds_read_b128 v[168:171], v240 offset:6144\n" \
"v_mfma_f32_16x16x32_f16 v[36:39], v[132:135], v[176:179], v[36:39]\n" \
"v_pk_add_f16 v176, v176, v180\n" \
"v_pk_add_f16 v177, v177, v181\n" \
"ds_read_b64_tr_b16 v[124:125], v242 offset:0\n" \
"v_mfma_f32_16x16x32_f16 v[28:31], v[144:147], v[180:183], v[28:31]\n" \
"v_pk_add_f16 v178, v178, v182\n" \
"v_pk_add_f16 v179, v179, v183\n" \
"v_cvt_pk_f16_f32 v184, v184, v185\n" \
"v_mfma_f32_16x16x32_f16 v[40:43], v[148:151], v[180:183], v[40:43]\n" \
"v_cvt_pk_f16_f32 v185, v186, v187\n" \
"v_cvt_pk_f16_f32 v186, v188, v189\n" \
"ds_read_b128 v[172:175], v240 offset:6656\n" \
"v_mfma_f32_16x16x32_f16 v[32:35], v[136:139], v[176:179], v[32:35]\n" \
"v_cvt_pk_f16_f32 v187, v190, v191\n" \
"v_add_u32 v184, s91, v184\n" \
"buffer_load_dwordx4 v232, s[36:39], s80 offen offset:1024 sc1 lds\n" \
"v_mfma_f32_16x16x32_f16 v[44:47], v[140:143], v[176:179], v[44:47]\n" \
"v_add_u32 v185, s91, v185\n" \
"v_add_u32 v186, s91, v186\n" \
"ds_read_b64_tr_b16 v[126:127], v242 offset:8192\n" \
"v_mfma_f32_16x16x32_f16 v[48:51], v[128:131], v[160:163], v[48:51]\n" \
"v_add_u32 v187, s91, v187\n" \
"v_and_b32 v184, s92, v184\n" \
"ds_read_b128 v[176:179], v240 offset:8192\n" \
"v_mfma_f32_16x16x32_f16 v[60:63], v[132:135], v[160:163], v[60:63]\n" \
"s_waitcnt lgkmcnt(4)\n" \
"v_pk_add_f16 v160, v160, v164\n" \
"v_pk_add_f16 v161, v161, v165\n" \
"v_and_b32 v185, s92, v185\n" \
"s_add_i32 s82, s49, 0x4800000\n" \
"v_mfma_f32_16x16x32_f16 v[52:55], v[144:147], v[164:167], v[52:55]\n" \
"v_pk_add_f16 v162, v162, v166\n" \
"v_pk_add_f16 v163, v163, v167\n" \
"v_and_b32 v186, s92, v186\n" \
"v_and_b32 v187, s92, v187\n" \
"v_mfma_f32_16x16x32_f16 v[64:67], v[148:151], v[164:167], v[64:67]\n" \
"ds_read_b128 v[180:183], v240 offset:8704\n" \
"buffer_store_dwordx4 v[184:187], v234, s[36:39], s82 offen sc1\n" \
"ds_read_b64_tr_b16 v[152:153], v241 offset:512\n" \
"v_mfma_f32_16x16x32_f16 v[56:59], v[136:139], v[160:163], v[56:59]\n" \
"buffer_load_dwordx4 v232, s[36:39], s80 offen offset:2048 sc1 lds\n" \
"v_mfma_f32_16x16x32_f16 v[68:71], v[140:143], v[160:163], v[68:71]\n" \
"ds_read_b64_tr_b16 v[154:155], v241 offset:8704\n" \
"ds_read_b64_tr_b16 v[156:157], v242 offset:512\n" \
"v_mfma_f32_16x16x32_f16 v[72:75], v[128:131], v[168:171], v[72:75]\n" \
"ds_read_b128 v[160:163], v240 offset:0\n" \
"ds_read_b64_tr_b16 v[158:159], v242 offset:8704\n" \
"v_mfma_f32_16x16x32_f16 v[84:87], v[132:135], v[168:171], v[84:87]\n" \
"s_waitcnt lgkmcnt(6)\n" \
"v_pk_add_f16 v168, v168, v172\n" \
"v_pk_add_f16 v169, v169, v173\n" \
"v_mfma_f32_16x16x32_f16 v[76:79], v[144:147], v[172:175], v[76:79]\n" \
"v_pk_add_f16 v170, v170, v174\n" \
"v_pk_add_f16 v171, v171, v175\n" \
"v_mfma_f32_16x16x32_f16 v[88:91], v[148:151], v[172:175], v[88:91]\n" \
"ds_read_b128 v[164:167], v240 offset:512\n" \
"v_mfma_f32_16x16x32_f16 v[80:83], v[136:139], v[168:171], v[80:83]\n" \
"buffer_load_dwordx4 v232, s[36:39], s80 offen offset:3072 sc1 lds\n" \
"v_mfma_f32_16x16x32_f16 v[92:95], v[140:143], v[168:171], v[92:95]\n" \
"v_mfma_f32_16x16x32_f16 v[96:99], v[128:131], v[176:179], v[96:99]\n" \
"s_waitcnt lgkmcnt(5)\n" \
"v_pk_add_f16 v128, v120, v152\n" \
"v_pk_add_f16 v129, v121, v153\n" \
"ds_read_b128 v[168:171], v240 offset:2048\n" \
"v_mfma_f32_16x16x32_f16 v[108:111], v[132:135], v[176:179], v[108:111]\n" \
"v_pk_add_f16 v176, v176, v180\n" \
"v_pk_add_f16 v177, v177, v181\n" \
"ds_read_b128 v[172:175], v240 offset:2560\n" \
"v_mfma_f32_16x16x32_f16 v[100:103], v[144:147], v[180:183], v[100:103]\n" \
"v_pk_add_f16 v178, v178, v182\n" \
"v_pk_add_f16 v179, v179, v183\n" \
"v_mfma_f32_16x16x32_f16 v[112:115], v[148:151], v[180:183], v[112:115]\n" \
"s_waitcnt lgkmcnt(3)\n" \
"v_pk_add_f16 v130, v122, v154\n" \
"v_pk_add_f16 v131, v123, v155\n" \
"v_mfma_f32_16x16x32_f16 v[104:107], v[136:139], v[176:179], v[104:107]\n" \
"v_pk_add_f16 v132, v124, v156\n" \
"v_pk_add_f16 v133, v125, v157\n" \
"v_mfma_f32_16x16x32_f16 v[116:119], v[140:143], v[176:179], v[116:119]\n" \
"v_pk_add_f16 v134, v126, v158\n" \
"v_pk_add_f16 v135, v127, v159\n" \
"s_waitcnt vmcnt(6) lgkmcnt(0)\n" \
"s_barrier\n" \
"v_mfma_f32_16x16x32_f16 v[0:3], v[120:123], v[160:163], v[0:3]\n" \
"ds_read_b128 v[176:179], v240 offset:24576\n" \
"ds_read_b64_tr_b16 v[184:185], v238 offset:16384\n" \
"ds_read_b64_tr_b16 v[188:189], v239 offset:16384\n" \
"v_mfma_f32_16x16x32_f16 v[12:15], v[124:127], v[160:163], v[12:15]\n" \
"v_pk_add_f16 v160, v160, v164\n" \
"v_pk_add_f16 v161, v161, v165\n" \
"v_mfma_f32_16x16x32_f16 v[4:7], v[152:155], v[164:167], v[4:7]\n" \
"v_pk_add_f16 v162, v162, v166\n" \
"v_pk_add_f16 v163, v163, v167\n" \
"v_cmp_eq_u32 vcc, 0, v253\n" \
"s_cbranch_vccnz .Ls04_%=\n" \
".Lr04_%=:\n" \
"v_mfma_f32_16x16x32_f16 v[16:19], v[156:159], v[164:167], v[16:19]\n" \
"ds_read_b128 v[180:183], v240 offset:25088\n" \
"s_add_i32 m0, s44, 0x0\n" \
"s_add_i32 s80, s46, 0x2c00000\n" \
"v_mfma_f32_16x16x32_f16 v[8:11], v[128:131], v[160:163], v[8:11]\n" \
"buffer_load_dwordx4 v232, s[36:39], s80 offen sc1 lds\n" \
"s_waitcnt lgkmcnt(2)\n" \
"v_mfma_f32_16x16x16_bf16 v[184:187], v[184:185], v[192:193], 0\n" \
"v_mfma_f32_16x16x32_f16 v[20:23], v[132:135], v[160:163], v[20:23]\n" \
"ds_read_b64_tr_b16 v[136:137], v241 offset:16384\n" \
"ds_read_b64_tr_b16 v[138:139], v241 offset:24576\n" \
"s_waitcnt lgkmcnt(3)\n" \
"v_mfma_f32_16x16x16_bf16 v[188:191], v[188:189], v[192:193], 0\n" \
"v_mfma_f32_16x16x32_f16 v[24:27], v[120:123], v[168:171], v[24:27]\n" \
"ds_read_b128 v[160:163], v240 offset:26624\n" \
"s_add_i32 s84, s52, 0x800c000\n" \
"v_mfma_f32_16x16x32_f16 v[36:39], v[124:127], v[168:171], v[36:39]\n" \
"v_pk_add_f16 v168, v168, v172\n" \
"v_pk_add_f16 v169, v169, v173\n" \
"buffer_store_dword v240, v243, s[36:39], s84 offen sc1\n" \
"v_mfma_f32_16x16x32_f16 v[28:31], v[152:155], v[172:175], v[28:31]\n" \
"v_pk_add_f16 v170, v170, v174\n" \
"v_pk_add_f16 v171, v171, v175\n" \
"v_cvt_pk_f16_f32 v184, v184, v185\n" \
"ds_read_b64_tr_b16 v[140:141], v242 offset:16384\n" \
"v_mfma_f32_16x16x32_f16 v[40:43], v[156:159], v[172:175], v[40:43]\n" \
"v_cvt_pk_f16_f32 v185, v186, v187\n" \
"v_cvt_pk_f16_f32 v186, v188, v189\n" \
"ds_read_b128 v[164:167], v240 offset:27136\n" \
"v_mfma_f32_16x16x32_f16 v[32:35], v[128:131], v[168:171], v[32:35]\n" \
"v_cvt_pk_f16_f32 v187, v190, v191\n" \
"v_add_u32 v184, s91, v184\n" \
"buffer_load_dwordx4 v232, s[36:39], s80 offen offset:1024 sc1 lds\n" \
"v_mfma_f32_16x16x32_f16 v[44:47], v[132:135], v[168:171], v[44:47]\n" \
"v_add_u32 v185, s91, v185\n" \
"v_add_u32 v186, s91, v186\n" \
"ds_read_b64_tr_b16 v[142:143], v242 offset:24576\n" \
"v_mfma_f32_16x16x32_f16 v[48:51], v[120:123], v[176:179], v[48:51]\n" \
"v_add_u32 v187, s91, v187\n" \
"v_and_b32 v184, s92, v184\n" \
"ds_read_b128 v[168:171], v240 offset:28672\n" \
"v_mfma_f32_16x16x32_f16 v[60:63], v[124:127], v[176:179], v[60:63]\n" \
"s_waitcnt lgkmcnt(4)\n" \
"v_pk_add_f16 v176, v176, v180\n" \
"v_pk_add_f16 v177, v177, v181\n" \
"v_and_b32 v185, s92, v185\n" \
"v_mfma_f32_16x16x32_f16 v[52:55], v[152:155], v[180:183], v[52:55]\n" \
"v_pk_add_f16 v178, v178, v182\n" \
"v_pk_add_f16 v179, v179, v183\n" \
"v_and_b32 v186, s92, v186\n" \
"v_and_b32 v187, s92, v187\n" \
"v_mfma_f32_16x16x32_f16 v[64:67], v[156:159], v[180:183], v[64:67]\n" \
"ds_read_b128 v[172:175], v240 offset:29184\n" \
"buffer_store_dwordx4 v[184:187], v234, s[36:39], s82 offen offset:1024 sc1\n" \
"ds_read_b64_tr_b16 v[144:145], v241 offset:16896\n" \
"v_mfma_f32_16x16x32_f16 v[56:59], v[128:131], v[176:179], v[56:59]\n" \
"buffer_load_dwordx4 v232, s[36:39], s80 offen offset:2048 sc1 lds\n" \
"v_mfma_f32_16x16x32_f16 v[68:71], v[132:135], v[176:179], v[68:71]\n" \
"ds_read_b64_tr_b16 v[146:147], v241 offset:25088\n" \
"ds_read_b64_tr_b16 v[148:149], v242 offset:16896\n" \
"v_mfma_f32_16x16x32_f16 v[72:75], v[120:123], v[160:163], v[72:75]\n" \
"ds_read_b128 v[176:179], v240 offset:20480\n" \
"ds_read_b64_tr_b16 v[150:151], v242 offset:25088\n" \
"v_mfma_f32_16x16x32_f16 v[84:87], v[124:127], v[160:163], v[84:87]\n" \
"s_waitcnt lgkmcnt(6)\n" \
"v_pk_add_f16 v160, v160, v164\n" \
"v_pk_add_f16 v161, v161, v165\n" \
"v_mfma_f32_16x16x32_f16 v[76:79], v[152:155], v[164:167], v[76:79]\n" \
"v_pk_add_f16 v162, v162, v166\n" \
"v_pk_add_f16 v163, v163, v167\n" \
"v_mfma_f32_16x16x32_f16 v[88:91], v[156:159], v[164:167], v[88:91]\n" \
"ds_read_b128 v[180:183], v240 offset:20992\n" \
"v_mfma_f32_16x16x32_f16 v[80:83], v[128:131], v[160:163], v[80:83]\n" \
"buffer_load_dwordx4 v232, s[36:39], s80 offen offset:3072 sc1 lds\n" \
"v_mfma_f32_16x16x32_f16 v[92:95], v[132:135], v[160:163], v[92:95]\n" \
"v_mfma_f32_16x16x32_f16 v[96:99], v[120:123], v[168:171], v[96:99]\n" \
"s_waitcnt lgkmcnt(5)\n" \
"v_pk_add_f16 v120, v136, v144\n" \
"v_pk_add_f16 v121, v137, v145\n" \
"ds_read_b128 v[160:163], v240 offset:22528\n" \
"v_mfma_f32_16x16x32_f16 v[108:111], v[124:127], v[168:171], v[108:111]\n" \
"v_pk_add_f16 v168, v168, v172\n" \
"v_pk_add_f16 v169, v169, v173\n" \
"ds_read_b128 v[164:167], v240 offset:23040\n" \
"v_mfma_f32_16x16x32_f16 v[100:103], v[152:155], v[172:175], v[100:103]\n" \
"v_pk_add_f16 v170, v170, v174\n" \
"v_pk_add_f16 v171, v171, v175\n" \
"v_mfma_f32_16x16x32_f16 v[112:115], v[156:159], v[172:175], v[112:115]\n" \
"s_waitcnt lgkmcnt(3)\n" \
"v_pk_add_f16 v122, v138, v146\n" \
"v_pk_add_f16 v123, v139, v147\n" \
"v_mfma_f32_16x16x32_f16 v[104:107], v[128:131], v[168:171], v[104:107]\n" \
"v_pk_add_f16 v124, v140, v148\n" \
"v_pk_add_f16 v125, v141, v149\n" \
"v_mfma_f32_16x16x32_f16 v[116:119], v[132:135], v[168:171], v[116:119]\n" \
"v_pk_add_f16 v126, v142, v150\n" \
"v_pk_add_f16 v127, v143, v151\n" \
"s_waitcnt vmcnt(6) lgkmcnt(0)\n" \
"s_barrier\n" \
"v_mfma_f32_16x16x32_f16 v[0:3], v[136:139], v[176:179], v[0:3]\n" \
"ds_read_b128 v[168:171], v240 offset:45056\n" \
"s_mov_b32 s81, 0x800a000\n" \
"ds_read_b64_tr_b16 v[184:185], v236 offset:20480\n" \
"ds_read_b64_tr_b16 v[188:189], v237 offset:20480\n" \
"v_mfma_f32_16x16x32_f16 v[12:15], v[140:143], v[176:179], v[12:15]\n" \
"v_pk_add_f16 v176, v176, v180\n" \
"v_pk_add_f16 v177, v177, v181\n" \
"buffer_load_dword v253, v252, s[36:39], s81 offen sc1\n" \
"v_mfma_f32_16x16x32_f16 v[4:7], v[144:147], v[180:183], v[4:7]\n" \
"v_pk_add_f16 v178, v178, v182\n" \
"v_pk_add_f16 v179, v179, v183\n" \
"buffer_store_dwordx4 v[220:223], v235, s[36:39], s66 offen sc1\n" \
"v_mfma_f32_16x16x32_f16 v[16:19], v[148:151], v[180:183], v[16:19]\n" \
"ds_read_b128 v[172:175], v240 offset:45568\n" \
"s_add_i32 m0, s44, 0x4000\n" \
"s_add_i32 s80, s46, 0x3000000\n" \
"v_mfma_f32_16x16x32_f16 v[8:11], v[120:123], v[176:179], v[8:11]\n" \
"buffer_load_dwordx4 v232, s[36:39], s80 offen sc1 lds\n" \
"s_waitcnt lgkmcnt(2)\n" \
"v_mfma_f32_16x16x16_bf16 v[184:187], v[184:185], v[192:193], 0\n" \
"v_mfma_f32_16x16x32_f16 v[20:23], v[124:127], v[176:179], v[20:23]\n" \
"ds_read_b64_tr_b16 v[128:129], v241 offset:32768\n" \
"ds_read_b64_tr_b16 v[130:131], v241 offset:40960\n" \
"s_waitcnt lgkmcnt(3)\n" \
"v_mfma_f32_16x16x16_bf16 v[188:191], v[188:189], v[192:193], 0\n" \
"v_mfma_f32_16x16x32_f16 v[24:27], v[136:139], v[160:163], v[24:27]\n" \
"ds_read_b128 v[176:179], v240 offset:47104\n" \
"v_mfma_f32_16x16x32_f16 v[36:39], v[140:143], v[160:163], v[36:39]\n" \
"v_pk_add_f16 v160, v160, v164\n" \
"v_pk_add_f16 v161, v161, v165\n" \
"ds_read_b64_tr_b16 v[132:133], v242 offset:32768\n" \
"v_mfma_f32_16x16x32_f16 v[28:31], v[144:147], v[164:167], v[28:31]\n" \
"v_pk_add_f16 v162, v162, v166\n" \
"v_pk_add_f16 v163, v163, v167\n" \
"v_cvt_pk_f16_f32 v184, v184, v185\n" \
"v_mfma_f32_16x16x32_f16 v[40:43], v[148:151], v[164:167], v[40:43]\n" \
"v_cvt_pk_f16_f32 v185, v186, v187\n" \
"v_cvt_pk_f16_f32 v186, v188, v189\n" \
"ds_read_b128 v[180:183], v240 offset:47616\n" \
"v_mfma_f32_16x16x32_f16 v[32:35], v[120:123], v[160:163], v[32:35]\n" \
"v_cvt_pk_f16_f32 v187, v190, v191\n" \
"v_add_u32 v184, s91, v184\n" \
"buffer_load_dwordx4 v232, s[36:39], s80 offen offset:1024 sc1 lds\n" \
"v_mfma_f32_16x16x32_f16 v[44:47], v[124:127], v[160:163], v[44:47]\n" \
"v_add_u32 v185, s91, v185\n" \
"v_add_u32 v186, s91, v186\n" \
"ds_read_b64_tr_b16 v[134:135], v242 offset:40960\n" \
"v_mfma_f32_16x16x32_f16 v[48:51], v[136:139], v[168:171], v[48:51]\n" \
"v_add_u32 v187, s91, v187\n" \
"v_and_b32 v184, s92, v184\n" \
"ds_read_b128 v[160:163], v240 offset:49152\n" \
"v_mfma_f32_16x16x32_f16 v[60:63], v[140:143], v[168:171], v[60:63]\n" \
"s_waitcnt lgkmcnt(4)\n" \
"v_pk_add_f16 v168, v168, v172\n" \
"v_pk_add_f16 v169, v169, v173\n" \
"v_and_b32 v185, s92, v185\n" \
"s_add_i32 s82, s50, 0x5000000\n" \
"v_mfma_f32_16x16x32_f16 v[52:55], v[144:147], v[172:175], v[52:55]\n" \
"v_pk_add_f16 v170, v170, v174\n" \
"v_pk_add_f16 v171, v171, v175\n" \
"v_and_b32 v186, s92, v186\n" \
"v_and_b32 v187, s92, v187\n" \
"v_mfma_f32_16x16x32_f16 v[64:67], v[148:151], v[172:175], v[64:67]\n" \
"ds_read_b128 v[164:167], v240 offset:49664\n" \
"buffer_store_dwordx4 v[184:187], v234, s[36:39], s82 offen sc1\n" \
"ds_read_b64_tr_b16 v[152:153], v241 offset:33280\n" \
"v_mfma_f32_16x16x32_f16 v[56:59], v[120:123], v[168:171], v[56:59]\n" \
"buffer_load_dwordx4 v232, s[36:39], s80 offen offset:2048 sc1 lds\n" \
"v_mfma_f32_16x16x32_f16 v[68:71], v[124:127], v[168:171], v[68:71]\n" \
"ds_read_b64_tr_b16 v[154:155], v241 offset:41472\n" \
"ds_read_b64_tr_b16 v[156:157], v242 offset:33280\n" \
"v_mfma_f32_16x16x32_f16 v[72:75], v[136:139], v[176:179], v[72:75]\n" \
"ds_read_b128 v[168:171], v240 offset:40960\n" \
"ds_read_b64_tr_b16 v[158:159], v242 offset:41472\n" \
"v_mfma_f32_16x16x32_f16 v[84:87], v[140:143], v[176:179], v[84:87]\n" \
"s_waitcnt lgkmcnt(6)\n" \
"v_pk_add_f16 v176, v176, v180\n" \
"v_pk_add_f16 v177, v177, v181\n" \
"v_mfma_f32_16x16x32_f16 v[76:79], v[144:147], v[180:183], v[76:79]\n" \
"v_pk_add_f16 v178, v178, v182\n" \
"v_pk_add_f16 v179, v179, v183\n" \
"v_mfma_f32_16x16x32_f16 v[88:91], v[148:151], v[180:183], v[88:91]\n" \
"ds_read_b128 v[172:175], v240 offset:41472\n" \
"v_mfma_f32_16x16x32_f16 v[80:83], v[120:123], v[176:179], v[80:83]\n" \
"buffer_load_dwordx4 v232, s[36:39], s80 offen offset:3072 sc1 lds\n" \
"v_mfma_f32_16x16x32_f16 v[92:95], v[124:127], v[176:179], v[92:95]\n" \
"v_mfma_f32_16x16x32_f16 v[96:99], v[136:139], v[160:163], v[96:99]\n" \
"s_waitcnt lgkmcnt(5)\n" \
"v_pk_add_f16 v136, v128, v152\n" \
"v_pk_add_f16 v137, v129, v153\n" \
"ds_read_b128 v[176:179], v240 offset:43008\n" \
"v_mfma_f32_16x16x32_f16 v[108:111], v[140:143], v[160:163], v[108:111]\n" \
"v_pk_add_f16 v160, v160, v164\n" \
"v_pk_add_f16 v161, v161, v165\n" \
"ds_read_b128 v[180:183], v240 offset:43520\n" \
"v_mfma_f32_16x16x32_f16 v[100:103], v[144:147], v[164:167], v[100:103]\n" \
"v_pk_add_f16 v162, v162, v166\n" \
"v_pk_add_f16 v163, v163, v167\n" \
"v_mfma_f32_16x16x32_f16 v[112:115], v[148:151], v[164:167], v[112:115]\n" \
"s_waitcnt lgkmcnt(3)\n" \
"v_pk_add_f16 v138, v130, v154\n" \
"v_pk_add_f16 v139, v131, v155\n" \
"v_mfma_f32_16x16x32_f16 v[104:107], v[120:123], v[160:163], v[104:107]\n" \
"v_pk_add_f16 v140, v132, v156\n" \
"v_pk_add_f16 v141, v133, v157\n" \
"v_mfma_f32_16x16x32_f16 v[116:119], v[124:127], v[160:163], v[116:119]\n" \
"v_pk_add_f16 v142, v134, v158\n" \
"v_pk_add_f16 v143, v135, v159\n" \
"s_waitcnt vmcnt(6) lgkmcnt(0)\n" \
"s_barrier\n" \
"v_mfma_f32_16x16x32_f16 v[0:3], v[128:131], v[168:171], v[0:3]\n" \
"ds_read_b128 v[160:163], v240 offset:4096\n" \
"ds_read_b64_tr_b16 v[184:185], v238 offset:20480\n" \
"ds_read_b64_tr_b16 v[188:189], v239 offset:20480\n" \
"v_mfma_f32_16x16x32_f16 v[12:15], v[132:135], v[168:171], v[12:15]\n" \
"v_pk_add_f16 v168, v168, v172\n" \
"v_pk_add_f16 v169, v169, v173\n" \
"v_mfma_f32_16x16x32_f16 v[4:7], v[152:155], v[172:175], v[4:7]\n" \
"v_pk_add_f16 v170, v170, v174\n" \
"v_pk_add_f16 v171, v171, v175\n" \
"v_cmp_eq_u32 vcc, 0, v253\n" \
"s_cbranch_vccnz .Ls05_%=\n" \
".Lr05_%=:\n" \
"v_mfma_f32_16x16x32_f16 v[16:19], v[156:159], v[172:175], v[16:19]\n" \
"ds_read_b128 v[164:167], v240 offset:4608\n" \
"s_add_i32 m0, s44, 0x8000\n" \
"s_add_i32 s80, s46, 0x3400000\n" \
"v_mfma_f32_16x16x32_f16 v[8:11], v[136:139], v[168:171], v[8:11]\n" \
"buffer_load_dwordx4 v232, s[36:39], s80 offen sc1 lds\n" \
"s_waitcnt lgkmcnt(2)\n" \
"v_mfma_f32_16x16x16_bf16 v[184:187], v[184:185], v[192:193], 0\n" \
"v_mfma_f32_16x16x32_f16 v[20:23], v[140:143], v[168:171], v[20:23]\n" \
"ds_read_b64_tr_b16 v[120:121], v241 offset:0\n" \
"ds_read_b64_tr_b16 v[122:123], v241 offset:8192\n" \
"s_waitcnt lgkmcnt(3)\n" \
"v_mfma_f32_16x16x16_bf16 v[188:191], v[188:189], v[192:193], 0\n" \
"v_mfma_f32_16x16x32_f16 v[24:27], v[128:131], v[176:179], v[24:27]\n" \
"ds_read_b128 v[168:171], v240 offset:6144\n" \
"s_add_i32 s84, s53, 0x800e000\n" \
"v_mfma_f32_16x16x32_f16 v[36:39], v[132:135], v[176:179], v[36:39]\n" \
"v_pk_add_f16 v176, v176, v180\n" \
"v_pk_add_f16 v177, v177, v181\n" \
"buffer_store_dword v240, v243, s[36:39], s84 offen sc1\n" \
"v_mfma_f32_16x16x32_f16 v[28:31], v[152:155], v[180:183], v[28:31]\n" \
"v_pk_add_f16 v178, v178, v182\n" \
"v_pk_add_f16 v179, v179, v183\n" \
"v_cvt_pk_f16_f32 v184, v184, v185\n" \
"ds_read_b64_tr_b16 v[124:125], v242 offset:0\n" \
"v_mfma_f32_16x16x32_f16 v[40:43], v[156:159], v[180:183], v[40:43]\n" \
"v_cvt_pk_f16_f32 v185, v186, v187\n" \
"v_cvt_pk_f16_f32 v186, v188, v189\n" \
"ds_read_b128 v[172:175], v240 offset:6656\n" \
"v_mfma_f32_16x16x32_f16 v[32:35], v[136:139], v[176:179], v[32:35]\n" \
"v_cvt_pk_f16_f32 v187, v190, v191\n" \
"v_add_u32 v184, s91, v184\n" \
"buffer_load_dwordx4 v232, s[36:39], s80 offen offset:1024 sc1 lds\n" \
"v_mfma_f32_16x16x32_f16 v[44:47], v[140:143], v[176:179], v[44:47]\n" \
"v_add_u32 v185, s91, v185\n" \
"v_add_u32 v186, s91, v186\n" \
"ds_read_b64_tr_b16 v[126:127], v242 offset:8192\n" \
"v_mfma_f32_16x16x32_f16 v[48:51], v[128:131], v[160:163], v[48:51]\n" \
"v_add_u32 v187, s91, v187\n" \
"v_and_b32 v184, s92, v184\n" \
"ds_read_b128 v[176:179], v240 offset:8192\n" \
"v_mfma_f32_16x16x32_f16 v[60:63], v[132:135], v[160:163], v[60:63]\n" \
"s_waitcnt lgkmcnt(4)\n" \
"v_pk_add_f16 v160, v160, v164\n" \
"v_pk_add_f16 v161, v161, v165\n" \
"v_and_b32 v185, s92, v185\n" \
"v_mfma_f32_16x16x32_f16 v[52:55], v[152:155], v[164:167], v[52:55]\n" \
"v_pk_add_f16 v162, v162, v166\n" \
"v_pk_add_f16 v163, v163, v167\n" \
"v_and_b32 v186, s92, v186\n" \
"v_and_b32 v187, s92, v187\n" \
"v_mfma_f32_16x16x32_f16 v[64:67], v[156:159], v[164:167], v[64:67]\n" \
"ds_read_b128 v[180:183], v240 offset:8704\n" \
"buffer_store_dwordx4 v[184:187], v234, s[36:39], s82 offen offset:1024 sc1\n" \
"ds_read_b64_tr_b16 v[144:145], v241 offset:512\n" \
"v_mfma_f32_16x16x32_f16 v[56:59], v[136:139], v[160:163], v[56:59]\n" \
"buffer_load_dwordx4 v232, s[36:39], s80 offen offset:2048 sc1 lds\n" \
"v_mfma_f32_16x16x32_f16 v[68:71], v[140:143], v[160:163], v[68:71]\n" \
"ds_read_b64_tr_b16 v[146:147], v241 offset:8704\n" \
"ds_read_b64_tr_b16 v[148:149], v242 offset:512\n" \
"v_mfma_f32_16x16x32_f16 v[72:75], v[128:131], v[168:171], v[72:75]\n" \
"ds_read_b128 v[160:163], v240 offset:0\n" \
"ds_read_b64_tr_b16 v[150:151], v242 offset:8704\n" \
"v_mfma_f32_16x16x32_f16 v[84:87], v[132:135], v[168:171], v[84:87]\n" \
"s_waitcnt lgkmcnt(6)\n" \
"v_pk_add_f16 v168, v168, v172\n" \
"v_pk_add_f16 v169, v169, v173\n" \
"v_mfma_f32_16x16x32_f16 v[76:79], v[152:155], v[172:175], v[76:79]\n" \
"v_pk_add_f16 v170, v170, v174\n" \
"v_pk_add_f16 v171, v171, v175\n" \
"v_mfma_f32_16x16x32_f16 v[88:91], v[156:159], v[172:175], v[88:91]\n" \
"ds_read_b128 v[164:167], v240 offset:512\n" \
"v_mfma_f32_16x16x32_f16 v[80:83], v[136:139], v[168:171], v[80:83]\n" \
"buffer_load_dwordx4 v232, s[36:39], s80 offen offset:3072 sc1 lds\n" \
"v_mfma_f32_16x16x32_f16 v[92:95], v[140:143], v[168:171], v[92:95]\n" \
"v_mfma_f32_16x16x32_f16 v[96:99], v[128:131], v[176:179], v[96:99]\n" \
"s_waitcnt lgkmcnt(5)\n" \
"v_pk_add_f16 v128, v120, v144\n" \
"v_pk_add_f16 v129, v121, v145\n" \
"ds_read_b128 v[168:171], v240 offset:2048\n" \
"v_mfma_f32_16x16x32_f16 v[108:111], v[132:135], v[176:179], v[108:111]\n" \
"v_pk_add_f16 v176, v176, v180\n" \
"v_pk_add_f16 v177, v177, v181\n" \
"ds_read_b128 v[172:175], v240 offset:2560\n" \
"v_mfma_f32_16x16x32_f16 v[100:103], v[152:155], v[180:183], v[100:103]\n" \
"v_pk_add_f16 v178, v178, v182\n" \
"v_pk_add_f16 v179, v179, v183\n" \
"v_mfma_f32_16x16x32_f16 v[112:115], v[156:159], v[180:183], v[112:115]\n" \
"s_waitcnt lgkmcnt(3)\n" \
"v_pk_add_f16 v130, v122, v146\n" \
"v_pk_add_f16 v131, v123, v147\n" \
"v_mfma_f32_16x16x32_f16 v[104:107], v[136:139], v[176:179], v[104:107]\n" \
"v_pk_add_f16 v132, v124, v148\n" \
"v_pk_add_f16 v133, v125, v149\n" \
"v_mfma_f32_16x16x32_f16 v[116:119], v[140:143], v[176:179], v[116:119]\n" \
"v_pk_add_f16 v134, v126, v150\n" \
"v_pk_add_f16 v135, v127, v151\n" \
"s_waitcnt vmcnt(6) lgkmcnt(0)\n" \
"s_barrier\n" \
"v_mfma_f32_16x16x32_f16 v[0:3], v[120:123], v[160:163], v[0:3]\n" \
"ds_read_b128 v[176:179], v240 offset:24576\n" \
"ds_read_b64_tr_b16 v[184:185], v236 offset:24576\n" \
"ds_read_b64_tr_b16 v[188:189], v237 offset:24576\n" \
"v_mfma_f32_16x16x32_f16 v[12:15], v[124:127], v[160:163], v[12:15]\n" \
"v_pk_add_f16 v160, v160, v164\n" \
"v_pk_add_f16 v161, v161, v165\n" \
"v_mfma_f32_16x16x32_f16 v[4:7], v[144:147], v[164:167], v[4:7]\n" \
"v_pk_add_f16 v162, v162, v166\n" \
"v_pk_add_f16 v163, v163, v167\n" \
"buffer_store_dwordx4 v[224:227], v235, s[36:39], s67 offen sc1\n" \
"v_mfma_f32_16x16x32_f16 v[16:19], v[148:151], v[164:167], v[16:19]\n" \
"ds_read_b128 v[180:183], v240 offset:25088\n" \
"s_add_i32 m0, s44, 0x0\n" \
"s_add_i32 s80, s46, 0x3800000\n" \
"v_mfma_f32_16x16x32_f16 v[8:11], v[128:131], v[160:163], v[8:11]\n" \
"buffer_load_dwordx4 v232, s[36:39], s80 offen sc1 lds\n" \
"s_waitcnt lgkmcnt(2)\n" \
"v_mfma_f32_16x16x16_bf16 v[184:187], v[184:185], v[192:193], 0\n" \
"v_mfma_f32_16x16x32_f16 v[20:23], v[132:135], v[160:163], v[20:23]\n" \
"ds_read_b64_tr_b16 v[136:137], v241 offset:16384\n" \
"ds_read_b64_tr_b16 v[138:139], v241 offset:24576\n" \
"s_waitcnt lgkmcnt(3)\n" \
"v_mfma_f32_16x16x16_bf16 v[188:191], v[188:189], v[192:193], 0\n" \
"v_mfma_f32_16x16x32_f16 v[24:27], v[120:123], v[168:171], v[24:27]\n" \
"ds_read_b128 v[160:163], v240 offset:26624\n" \
"v_mfma_f32_16x16x32_f16 v[36:39], v[124:127], v[168:171], v[36:39]\n" \
"v_pk_add_f16 v168, v168, v172\n" \
"v_pk_add_f16 v169, v169, v173\n" \
"ds_read_b64_tr_b16 v[140:141], v242 offset:16384\n" \
"v_mfma_f32_16x16x32_f16 v[28:31], v[144:147], v[172:175], v[28:31]\n" \
"v_pk_add_f16 v170, v170, v174\n" \
"v_pk_add_f16 v171, v171, v175\n" \
"v_cvt_pk_f16_f32 v184, v184, v185\n" \
"v_mfma_f32_16x16x32_f16 v[40:43], v[148:151], v[172:175], v[40:43]\n" \
"v_cvt_pk_f16_f32 v185, v186, v187\n" \
"v_cvt_pk_f16_f32 v186, v188, v189\n" \
"ds_read_b128 v[164:167], v240 offset:27136\n" \
"v_mfma_f32_16x16x32_f16 v[32:35], v[128:131], v[168:171], v[32:35]\n" \
"v_cvt_pk_f16_f32 v187, v190, v191\n" \
"v_add_u32 v184, s91, v184\n" \
"buffer_load_dwordx4 v232, s[36:39], s80 offen offset:1024 sc1 lds\n" \
"v_mfma_f32_16x16x32_f16 v[44:47], v[132:135], v[168:171], v[44:47]\n" \
"v_add_u32 v185, s91, v185\n" \
"v_add_u32 v186, s91, v186\n" \
"ds_read_b64_tr_b16 v[142:143], v242 offset:24576\n" \
"v_mfma_f32_16x16x32_f16 v[48:51], v[120:123], v[176:179], v[48:51]\n" \
"v_add_u32 v187, s91, v187\n" \
"v_and_b32 v184, s92, v184\n" \
"ds_read_b128 v[168:171], v240 offset:28672\n" \
"v_mfma_f32_16x16x32_f16 v[60:63], v[124:127], v[176:179], v[60:63]\n" \
"s_waitcnt lgkmcnt(4)\n" \
"v_pk_add_f16 v176, v176, v180\n" \
"v_pk_add_f16 v177, v177, v181\n" \
"v_and_b32 v185, s92, v185\n" \
"s_add_i32 s82, s51, 0x5800000\n" \
"v_mfma_f32_16x16x32_f16 v[52:55], v[144:147], v[180:183], v[52:55]\n" \
"v_pk_add_f16 v178, v178, v182\n" \
"v_pk_add_f16 v179, v179, v183\n" \
"v_and_b32 v186, s92, v186\n" \
"v_and_b32 v187, s92, v187\n" \
"v_mfma_f32_16x16x32_f16 v[64:67], v[148:151], v[180:183], v[64:67]\n" \
"ds_read_b128 v[172:175], v240 offset:29184\n" \
"buffer_store_dwordx4 v[184:187], v234, s[36:39], s82 offen sc1\n" \
"ds_read_b64_tr_b16 v[152:153], v241 offset:16896\n" \
"v_mfma_f32_16x16x32_f16 v[56:59], v[128:131], v[176:179], v[56:59]\n" \
"buffer_load_dwordx4 v232, s[36:39], s80 offen offset:2048 sc1 lds\n" \
"v_mfma_f32_16x16x32_f16 v[68:71], v[132:135], v[176:179], v[68:71]\n" \
"ds_read_b64_tr_b16 v[154:155], v241 offset:25088\n" \
"ds_read_b64_tr_b16 v[156:157], v242 offset:16896\n" \
"v_mfma_f32_16x16x32_f16 v[72:75], v[120:123], v[160:163], v[72:75]\n" \
"ds_read_b128 v[176:179], v240 offset:20480\n" \
"ds_read_b64_tr_b16 v[158:159], v242 offset:25088\n" \
"v_mfma_f32_16x16x32_f16 v[84:87], v[124:127], v[160:163], v[84:87]\n" \
"s_waitcnt lgkmcnt(6)\n" \
"v_pk_add_f16 v160, v160, v164\n" \
"v_pk_add_f16 v161, v161, v165\n" \
"v_mfma_f32_16x16x32_f16 v[76:79], v[144:147], v[164:167], v[76:79]\n" \
"v_pk_add_f16 v162, v162, v166\n" \
"v_pk_add_f16 v163, v163, v167\n" \
"v_mfma_f32_16x16x32_f16 v[88:91], v[148:151], v[164:167], v[88:91]\n" \
"ds_read_b128 v[180:183], v240 offset:20992\n" \
"v_mfma_f32_16x16x32_f16 v[80:83], v[128:131], v[160:163], v[80:83]\n" \
"buffer_load_dwordx4 v232, s[36:39], s80 offen offset:3072 sc1 lds\n" \
"v_mfma_f32_16x16x32_f16 v[92:95], v[132:135], v[160:163], v[92:95]\n" \
"v_mfma_f32_16x16x32_f16 v[96:99], v[120:123], v[168:171], v[96:99]\n" \
"s_waitcnt lgkmcnt(5)\n" \
"v_pk_add_f16 v120, v136, v152\n" \
"v_pk_add_f16 v121, v137, v153\n" \
"ds_read_b128 v[160:163], v240 offset:22528\n" \
"v_mfma_f32_16x16x32_f16 v[108:111], v[124:127], v[168:171], v[108:111]\n" \
"v_pk_add_f16 v168, v168, v172\n" \
"v_pk_add_f16 v169, v169, v173\n" \
"ds_read_b128 v[164:167], v240 offset:23040\n" \
"v_mfma_f32_16x16x32_f16 v[100:103], v[144:147], v[172:175], v[100:103]\n" \
"v_pk_add_f16 v170, v170, v174\n" \
"v_pk_add_f16 v171, v171, v175\n" \
"v_mfma_f32_16x16x32_f16 v[112:115], v[148:151], v[172:175], v[112:115]\n" \
"s_waitcnt lgkmcnt(3)\n" \
"v_pk_add_f16 v122, v138, v154\n" \
"v_pk_add_f16 v123, v139, v155\n" \
"v_mfma_f32_16x16x32_f16 v[104:107], v[128:131], v[168:171], v[104:107]\n" \
"v_pk_add_f16 v124, v140, v156\n" \
"v_pk_add_f16 v125, v141, v157\n" \
"v_mfma_f32_16x16x32_f16 v[116:119], v[132:135], v[168:171], v[116:119]\n" \
"v_pk_add_f16 v126, v142, v158\n" \
"v_pk_add_f16 v127, v143, v159\n" \
"s_waitcnt vmcnt(6) lgkmcnt(0)\n" \
"s_barrier\n" \
"v_mfma_f32_16x16x32_f16 v[0:3], v[136:139], v[176:179], v[0:3]\n" \
"ds_read_b128 v[168:171], v240 offset:45056\n" \
"ds_read_b64_tr_b16 v[184:185], v238 offset:24576\n" \
"ds_read_b64_tr_b16 v[188:189], v239 offset:24576\n" \
"v_mfma_f32_16x16x32_f16 v[12:15], v[140:143], v[176:179], v[12:15]\n" \
"v_pk_add_f16 v176, v176, v180\n" \
"v_pk_add_f16 v177, v177, v181\n" \
"v_mfma_f32_16x16x32_f16 v[4:7], v[152:155], v[180:183], v[4:7]\n" \
"v_pk_add_f16 v178, v178, v182\n" \
"v_pk_add_f16 v179, v179, v183\n" \
"v_mfma_f32_16x16x32_f16 v[16:19], v[156:159], v[180:183], v[16:19]\n" \
"ds_read_b128 v[172:175], v240 offset:45568\n" \
"s_add_i32 m0, s44, 0x4000\n" \
"s_add_i32 s80, s46, 0x3c00000\n" \
"v_mfma_f32_16x16x32_f16 v[8:11], v[120:123], v[176:179], v[8:11]\n" \
"buffer_load_dwordx4 v232, s[36:39], s80 offen sc1 lds\n" \
"s_waitcnt lgkmcnt(2)\n" \
"v_mfma_f32_16x16x16_bf16 v[184:187], v[184:185], v[192:193], 0\n" \
"v_mfma_f32_16x16x32_f16 v[20:23], v[124:127], v[176:179], v[20:23]\n" \
"ds_read_b64_tr_b16 v[128:129], v241 offset:32768\n" \
"ds_read_b64_tr_b16 v[130:131], v241 offset:40960\n" \
"s_waitcnt lgkmcnt(3)\n" \
"v_mfma_f32_16x16x16_bf16 v[188:191], v[188:189], v[192:193], 0\n" \
"v_mfma_f32_16x16x32_f16 v[24:27], v[136:139], v[160:163], v[24:27]\n" \
"ds_read_b128 v[176:179], v240 offset:47104\n" \
"s_add_i32 s84, s54, 0x8010000\n" \
"v_mfma_f32_16x16x32_f16 v[36:39], v[140:143], v[160:163], v[36:39]\n" \
"v_pk_add_f16 v160, v160, v164\n" \
"v_pk_add_f16 v161, v161, v165\n" \
"buffer_store_dword v240, v243, s[36:39], s84 offen sc1\n" \
"v_mfma_f32_16x16x32_f16 v[28:31], v[152:155], v[164:167], v[28:31]\n" \
"v_pk_add_f16 v162, v162, v166\n" \
"v_pk_add_f16 v163, v163, v167\n" \
"v_cvt_pk_f16_f32 v184, v184, v185\n" \
"ds_read_b64_tr_b16 v[132:133], v242 offset:32768\n" \
"v_mfma_f32_16x16x32_f16 v[40:43], v[156:159], v[164:167], v[40:43]\n" \
"v_cvt_pk_f16_f32 v185, v186, v187\n" \
"v_cvt_pk_f16_f32 v186, v188, v189\n" \
"ds_read_b128 v[180:183], v240 offset:47616\n" \
"v_mfma_f32_16x16x32_f16 v[32:35], v[120:123], v[160:163], v[32:35]\n" \
"v_cvt_pk_f16_f32 v187, v190, v191\n" \
"v_add_u32 v184, s91, v184\n" \
"buffer_load_dwordx4 v232, s[36:39], s80 offen offset:1024 sc1 lds\n" \
"v_mfma_f32_16x16x32_f16 v[44:47], v[124:127], v[160:163], v[44:47]\n" \
"v_add_u32 v185, s91, v185\n" \
"v_add_u32 v186, s91, v186\n" \
"ds_read_b64_tr_b16 v[134:135], v242 offset:40960\n" \
"v_mfma_f32_16x16x32_f16 v[48:51], v[136:139], v[168:171], v[48:51]\n" \
"v_add_u32 v187, s91, v187\n" \
"v_and_b32 v184, s92, v184\n" \
"ds_read_b128 v[160:163], v240 offset:49152\n" \
"v_mfma_f32_16x16x32_f16 v[60:63], v[140:143], v[168:171], v[60:63]\n" \
"s_waitcnt lgkmcnt(4)\n" \
"v_pk_add_f16 v168, v168, v172\n" \
"v_pk_add_f16 v169, v169, v173\n" \
"v_and_b32 v185, s92, v185\n" \
"v_mfma_f32_16x16x32_f16 v[52:55], v[152:155], v[172:175], v[52:55]\n" \
"v_pk_add_f16 v170, v170, v174\n" \
"v_pk_add_f16 v171, v171, v175\n" \
"v_and_b32 v186, s92, v186\n" \
"v_and_b32 v187, s92, v187\n" \
"v_mfma_f32_16x16x32_f16 v[64:67], v[156:159], v[172:175], v[64:67]\n" \
"ds_read_b128 v[164:167], v240 offset:49664\n" \
"buffer_store_dwordx4 v[184:187], v234, s[36:39], s82 offen offset:1024 sc1\n" \
"ds_read_b64_tr_b16 v[144:145], v241 offset:33280\n" \
"v_mfma_f32_16x16x32_f16 v[56:59], v[120:123], v[168:171], v[56:59]\n" \
"buffer_load_dwordx4 v232, s[36:39], s80 offen offset:2048 sc1 lds\n" \
"v_mfma_f32_16x16x32_f16 v[68:71], v[124:127], v[168:171], v[68:71]\n" \
"ds_read_b64_tr_b16 v[146:147], v241 offset:41472\n" \
"ds_read_b64_tr_b16 v[148:149], v242 offset:33280\n" \
"v_mfma_f32_16x16x32_f16 v[72:75], v[136:139], v[176:179], v[72:75]\n" \
"ds_read_b128 v[168:171], v240 offset:40960\n" \
"ds_read_b64_tr_b16 v[150:151], v242 offset:41472\n" \
"v_mfma_f32_16x16x32_f16 v[84:87], v[140:143], v[176:179], v[84:87]\n" \
"s_waitcnt lgkmcnt(6)\n" \
"v_pk_add_f16 v176, v176, v180\n" \
"v_pk_add_f16 v177, v177, v181\n" \
"v_mfma_f32_16x16x32_f16 v[76:79], v[152:155], v[180:183], v[76:79]\n" \
"v_pk_add_f16 v178, v178, v182\n" \
"v_pk_add_f16 v179, v179, v183\n" \
"v_mfma_f32_16x16x32_f16 v[88:91], v[156:159], v[180:183], v[88:91]\n" \
"ds_read_b128 v[172:175], v240 offset:41472\n" \
"v_mfma_f32_16x16x32_f16 v[80:83], v[120:123], v[176:179], v[80:83]\n" \
"buffer_load_dwordx4 v232, s[36:39], s80 offen offset:3072 sc1 lds\n" \
"v_mfma_f32_16x16x32_f16 v[92:95], v[124:127], v[176:179], v[92:95]\n" \
"v_mfma_f32_16x16x32_f16 v[96:99], v[136:139], v[160:163], v[96:99]\n" \
"s_waitcnt lgkmcnt(5)\n" \
"v_pk_add_f16 v136, v128, v144\n" \
"v_pk_add_f16 v137, v129, v145\n" \
"ds_read_b128 v[176:179], v240 offset:43008\n" \
"v_mfma_f32_16x16x32_f16 v[108:111], v[140:143], v[160:163], v[108:111]\n" \
"v_pk_add_f16 v160, v160, v164\n" \
"v_pk_add_f16 v161, v161, v165\n" \
"ds_read_b128 v[180:183], v240 offset:43520\n" \
"v_mfma_f32_16x16x32_f16 v[100:103], v[152:155], v[164:167], v[100:103]\n" \
"v_pk_add_f16 v162, v162, v166\n" \
"v_pk_add_f16 v163, v163, v167\n" \
"v_mfma_f32_16x16x32_f16 v[112:115], v[156:159], v[164:167], v[112:115]\n" \
"s_waitcnt lgkmcnt(3)\n" \
"v_pk_add_f16 v138, v130, v146\n" \
"v_pk_add_f16 v139, v131, v147\n" \
"v_mfma_f32_16x16x32_f16 v[104:107], v[120:123], v[160:163], v[104:107]\n" \
"v_pk_add_f16 v140, v132, v148\n" \
"v_pk_add_f16 v141, v133, v149\n" \
"v_mfma_f32_16x16x32_f16 v[116:119], v[124:127], v[160:163], v[116:119]\n" \
"v_pk_add_f16 v142, v134, v150\n" \
"v_pk_add_f16 v143, v135, v151\n" \
"s_waitcnt vmcnt(6) lgkmcnt(0)\n" \
"s_barrier\n" \
"v_mfma_f32_16x16x32_f16 v[0:3], v[128:131], v[168:171], v[0:3]\n" \
"ds_read_b128 v[160:163], v240 offset:4096\n" \
"s_mov_b32 s81, 0x800e000\n" \
"ds_read_b64_tr_b16 v[184:185], v236 offset:28672\n" \
"ds_read_b64_tr_b16 v[188:189], v237 offset:28672\n" \
"s_add_i32 m0, s45, 0x0\n" \
"s_mov_b32 s83, 0x0\n" \
"v_mfma_f32_16x16x32_f16 v[12:15], v[132:135], v[168:171], v[12:15]\n" \
"v_pk_add_f16 v168, v168, v172\n" \
"v_pk_add_f16 v169, v169, v173\n" \
"buffer_load_dword v253, v252, s[36:39], s81 offen sc1\n" \
"buffer_load_dwordx4 v233, s[36:39], s83 offen sc1 lds\n" \
"v_mfma_f32_16x16x32_f16 v[4:7], v[144:147], v[172:175], v[4:7]\n" \
"v_pk_add_f16 v170, v170, v174\n" \
"v_pk_add_f16 v171, v171, v175\n" \
"buffer_store_dwordx4 v[228:231], v235, s[36:39], s68 offen sc1\n" \
"v_mfma_f32_16x16x32_f16 v[16:19], v[148:151], v[172:175], v[16:19]\n" \
"ds_read_b128 v[164:167], v240 offset:4608\n" \
"s_add_i32 m0, s44, 0x8000\n" \
"s_add_i32 s80, s46, 0x4000000\n" \
"v_mfma_f32_16x16x32_f16 v[8:11], v[136:139], v[168:171], v[8:11]\n" \
"buffer_load_dwordx4 v232, s[36:39], s80 offen sc1 lds\n" \
"s_waitcnt lgkmcnt(2)\n" \
"v_mfma_f32_16x16x16_bf16 v[184:187], v[184:185], v[192:193], 0\n" \
"v_mfma_f32_16x16x32_f16 v[20:23], v[140:143], v[168:171], v[20:23]\n" \
"ds_read_b64_tr_b16 v[120:121], v241 offset:0\n" \
"ds_read_b64_tr_b16 v[122:123], v241 offset:8192\n" \
"s_waitcnt lgkmcnt(3)\n" \
"v_mfma_f32_16x16x16_bf16 v[188:191], v[188:189], v[192:193], 0\n" \
"v_mfma_f32_16x16x32_f16 v[24:27], v[128:131], v[176:179], v[24:27]\n" \
"ds_read_b128 v[168:171], v240 offset:6144\n" \
"v_mfma_f32_16x16x32_f16 v[36:39], v[132:135], v[176:179], v[36:39]\n" \
"v_pk_add_f16 v176, v176, v180\n" \
"v_pk_add_f16 v177, v177, v181\n" \
"ds_read_b64_tr_b16 v[124:125], v242 offset:0\n" \
"v_mfma_f32_16x16x32_f16 v[28:31], v[144:147], v[180:183], v[28:31]\n" \
"v_pk_add_f16 v178, v178, v182\n" \
"v_pk_add_f16 v179, v179, v183\n" \
"v_cvt_pk_f16_f32 v184, v184, v185\n" \
"v_mfma_f32_16x16x32_f16 v[40:43], v[148:151], v[180:183], v[40:43]\n" \
"v_cvt_pk_f16_f32 v185, v186, v187\n" \
"v_cvt_pk_f16_f32 v186, v188, v189\n" \
"ds_read_b128 v[172:175], v240 offset:6656\n" \
"v_mfma_f32_16x16x32_f16 v[32:35], v[136:139], v[176:179], v[32:35]\n" \
"v_cvt_pk_f16_f32 v187, v190, v191\n" \
"v_add_u32 v184, s91, v184\n" \
"buffer_load_dwordx4 v232, s[36:39], s80 offen offset:1024 sc1 lds\n" \
"v_mfma_f32_16x16x32_f16 v[44:47], v[140:143], v[176:179], v[44:47]\n" \
"v_add_u32 v185, s91, v185\n" \
"v_add_u32 v186, s91, v186\n" \
"ds_read_b64_tr_b16 v[126:127], v242 offset:8192\n" \
"v_mfma_f32_16x16x32_f16 v[48:51], v[128:131], v[160:163], v[48:51]\n" \
"v_add_u32 v187, s91, v187\n" \
"v_and_b32 v184, s92, v184\n" \
"ds_read_b128 v[176:179], v240 offset:8192\n" \
"v_mfma_f32_16x16x32_f16 v[60:63], v[132:135], v[160:163], v[60:63]\n" \
"s_waitcnt lgkmcnt(4)\n" \
"v_pk_add_f16 v160, v160, v164\n" \
"v_pk_add_f16 v161, v161, v165\n" \
"v_and_b32 v185, s92, v185\n" \
"s_add_i32 s82, s48, 0x6000000\n" \
"v_mfma_f32_16x16x32_f16 v[52:55], v[144:147], v[164:167], v[52:55]\n" \
"v_pk_add_f16 v162, v162, v166\n" \
"v_pk_add_f16 v163, v163, v167\n" \
"v_and_b32 v186, s92, v186\n" \
"v_and_b32 v187, s92, v187\n" \
"v_mfma_f32_16x16x32_f16 v[64:67], v[148:151], v[164:167], v[64:67]\n" \
"ds_read_b128 v[180:183], v240 offset:8704\n" \
"buffer_store_dwordx4 v[184:187], v234, s[36:39], s82 offen sc1\n" \
"ds_read_b64_tr_b16 v[152:153], v241 offset:512\n" \
"v_mfma_f32_16x16x32_f16 v[56:59], v[136:139], v[160:163], v[56:59]\n" \
"buffer_load_dwordx4 v232, s[36:39], s80 offen offset:2048 sc1 lds\n" \
"v_mfma_f32_16x16x32_f16 v[68:71], v[140:143], v[160:163], v[68:71]\n" \
"ds_read_b64_tr_b16 v[154:155], v241 offset:8704\n" \
"ds_read_b64_tr_b16 v[156:157], v242 offset:512\n" \
"v_mfma_f32_16x16x32_f16 v[72:75], v[128:131], v[168:171], v[72:75]\n" \
"ds_read_b128 v[160:163], v240 offset:0\n" \
"ds_read_b64_tr_b16 v[158:159], v242 offset:8704\n" \
"v_mfma_f32_16x16x32_f16 v[84:87], v[132:135], v[168:171], v[84:87]\n" \
"s_waitcnt lgkmcnt(6)\n" \
"v_pk_add_f16 v168, v168, v172\n" \
"v_pk_add_f16 v169, v169, v173\n" \
"v_mfma_f32_16x16x32_f16 v[76:79], v[144:147], v[172:175], v[76:79]\n" \
"v_pk_add_f16 v170, v170, v174\n" \
"v_pk_add_f16 v171, v171, v175\n" \
"v_mfma_f32_16x16x32_f16 v[88:91], v[148:151], v[172:175], v[88:91]\n" \
"ds_read_b128 v[164:167], v240 offset:512\n" \
"v_mfma_f32_16x16x32_f16 v[80:83], v[136:139], v[168:171], v[80:83]\n" \
"buffer_load_dwordx4 v232, s[36:39], s80 offen offset:3072 sc1 lds\n" \
"v_mfma_f32_16x16x32_f16 v[92:95], v[140:143], v[168:171], v[92:95]\n" \
"v_mfma_f32_16x16x32_f16 v[96:99], v[128:131], v[176:179], v[96:99]\n" \
"s_waitcnt lgkmcnt(5)\n" \
"v_pk_add_f16 v128, v120, v152\n" \
"v_pk_add_f16 v129, v121, v153\n" \
"ds_read_b128 v[168:171], v240 offset:2048\n" \
"s_add_i32 m0, s45, 0x1000\n" \
"s_mov_b32 s83, 0x8000\n" \
"v_mfma_f32_16x16x32_f16 v[108:111], v[132:135], v[176:179], v[108:111]\n" \
"v_pk_add_f16 v176, v176, v180\n" \
"v_pk_add_f16 v177, v177, v181\n" \
"ds_read_b128 v[172:175], v240 offset:2560\n" \
"buffer_load_dwordx4 v233, s[36:39], s83 offen sc1 lds\n" \
"v_mfma_f32_16x16x32_f16 v[100:103], v[144:147], v[180:183], v[100:103]\n" \
"v_pk_add_f16 v178, v178, v182\n" \
"v_pk_add_f16 v179, v179, v183\n" \
"v_mfma_f32_16x16x32_f16 v[112:115], v[148:151], v[180:183], v[112:115]\n" \
"s_waitcnt lgkmcnt(3)\n" \
"v_pk_add_f16 v130, v122, v154\n" \
"v_pk_add_f16 v131, v123, v155\n" \
"v_mfma_f32_16x16x32_f16 v[104:107], v[136:139], v[176:179], v[104:107]\n" \
"v_pk_add_f16 v132, v124, v156\n" \
"v_pk_add_f16 v133, v125, v157\n" \
"v_mfma_f32_16x16x32_f16 v[116:119], v[140:143], v[176:179], v[116:119]\n" \
"v_pk_add_f16 v134, v126, v158\n" \
"v_pk_add_f16 v135, v127, v159\n" \
"s_waitcnt vmcnt(8) lgkmcnt(0)\n" \
"s_barrier\n" \
"v_mfma_f32_16x16x32_f16 v[0:3], v[120:123], v[160:163], v[0:3]\n" \
"ds_read_b128 v[176:179], v240 offset:24576\n" \
"ds_read_b64_tr_b16 v[184:185], v238 offset:28672\n" \
"ds_read_b64_tr_b16 v[188:189], v239 offset:28672\n" \
"s_add_i32 m0, s45, 0x2000\n" \
"s_mov_b32 s83, 0x10000\n" \
"v_mfma_f32_16x16x32_f16 v[12:15], v[124:127], v[160:163], v[12:15]\n" \
"v_pk_add_f16 v160, v160, v164\n" \
"v_pk_add_f16 v161, v161, v165\n" \
"buffer_load_dwordx4 v233, s[36:39], s83 offen sc1 lds\n" \
"v_mfma_f32_16x16x32_f16 v[4:7], v[152:155], v[164:167], v[4:7]\n" \
"v_pk_add_f16 v162, v162, v166\n" \
"v_pk_add_f16 v163, v163, v167\n" \
"v_cmp_eq_u32 vcc, 0, v253\n" \
"s_cbranch_vccnz .Ls07_%=\n" \
".Lr07_%=:\n" \
"v_mfma_f32_16x16x32_f16 v[16:19], v[156:159], v[164:167], v[16:19]\n" \
"ds_read_b128 v[180:183], v240 offset:25088\n" \
"s_add_i32 m0, s44, 0x0\n" \
"s_add_i32 s80, s46, 0x4400000\n" \
"v_mfma_f32_16x16x32_f16 v[8:11], v[128:131], v[160:163], v[8:11]\n" \
"buffer_load_dwordx4 v232, s[36:39], s80 offen sc1 lds\n" \
"s_waitcnt lgkmcnt(2)\n" \
"v_mfma_f32_16x16x16_bf16 v[184:187], v[184:185], v[192:193], 0\n" \
"v_mfma_f32_16x16x32_f16 v[20:23], v[132:135], v[160:163], v[20:23]\n" \
"ds_read_b64_tr_b16 v[136:137], v241 offset:16384\n" \
"ds_read_b64_tr_b16 v[138:139], v241 offset:24576\n" \
"s_waitcnt lgkmcnt(3)\n" \
"v_mfma_f32_16x16x16_bf16 v[188:191], v[188:189], v[192:193], 0\n" \
"v_mfma_f32_16x16x32_f16 v[24:27], v[120:123], v[168:171], v[24:27]\n" \
"ds_read_b128 v[160:163], v240 offset:26624\n" \
"s_add_i32 s84, s55, 0x8012000\n" \
"v_mfma_f32_16x16x32_f16 v[36:39], v[124:127], v[168:171], v[36:39]\n" \
"v_pk_add_f16 v168, v168, v172\n" \
"v_pk_add_f16 v169, v169, v173\n" \
"buffer_store_dword v240, v243, s[36:39], s84 offen sc1\n" \
"v_mfma_f32_16x16x32_f16 v[28:31], v[152:155], v[172:175], v[28:31]\n" \
"v_pk_add_f16 v170, v170, v174\n" \
"v_pk_add_f16 v171, v171, v175\n" \
"v_cvt_pk_f16_f32 v184, v184, v185\n" \
"ds_read_b64_tr_b16 v[140:141], v242 offset:16384\n" \
"v_mfma_f32_16x16x32_f16 v[40:43], v[156:159], v[172:175], v[40:43]\n" \
"v_cvt_pk_f16_f32 v185, v186, v187\n" \
"v_cvt_pk_f16_f32 v186, v188, v189\n" \
"ds_read_b128 v[164:167], v240 offset:27136\n" \
"v_mfma_f32_16x16x32_f16 v[32:35], v[128:131], v[168:171], v[32:35]\n" \
"v_cvt_pk_f16_f32 v187, v190, v191\n" \
"v_add_u32 v184, s91, v184\n" \
"buffer_load_dwordx4 v232, s[36:39], s80 offen offset:1024 sc1 lds\n" \
"v_mfma_f32_16x16x32_f16 v[44:47], v[132:135], v[168:171], v[44:47]\n" \
"v_add_u32 v185, s91, v185\n" \
"v_add_u32 v186, s91, v186\n" \
"ds_read_b64_tr_b16 v[142:143], v242 offset:24576\n" \
"v_mfma_f32_16x16x32_f16 v[48:51], v[120:123], v[176:179], v[48:51]\n" \
"v_add_u32 v187, s91, v187\n" \
"v_and_b32 v184, s92, v184\n" \
"ds_read_b128 v[168:171], v240 offset:28672\n" \
"v_mfma_f32_16x16x32_f16 v[60:63], v[124:127], v[176:179], v[60:63]\n" \
"s_waitcnt lgkmcnt(4)\n" \
"v_pk_add_f16 v176, v176, v180\n" \
"v_pk_add_f16 v177, v177, v181\n" \
"v_and_b32 v185, s92, v185\n" \
"v_mfma_f32_16x16x32_f16 v[52:55], v[152:155], v[180:183], v[52:55]\n" \
"v_pk_add_f16 v178, v178, v182\n" \
"v_pk_add_f16 v179, v179, v183\n" \
"v_and_b32 v186, s92, v186\n" \
"v_and_b32 v187, s92, v187\n" \
"v_mfma_f32_16x16x32_f16 v[64:67], v[156:159], v[180:183], v[64:67]\n" \
"ds_read_b128 v[172:175], v240 offset:29184\n" \
"buffer_store_dwordx4 v[184:187], v234, s[36:39], s82 offen offset:1024 sc1\n" \
"ds_read_b64_tr_b16 v[144:145], v241 offset:16896\n" \
"v_mfma_f32_16x16x32_f16 v[56:59], v[128:131], v[176:179], v[56:59]\n" \
"buffer_load_dwordx4 v232, s[36:39], s80 offen offset:2048 sc1 lds\n" \
"v_mfma_f32_16x16x32_f16 v[68:71], v[132:135], v[176:179], v[68:71]\n" \
"ds_read_b64_tr_b16 v[146:147], v241 offset:25088\n" \
"ds_read_b64_tr_b16 v[148:149], v242 offset:16896\n" \
"v_mfma_f32_16x16x32_f16 v[72:75], v[120:123], v[160:163], v[72:75]\n" \
"ds_read_b128 v[176:179], v240 offset:20480\n" \
"ds_read_b64_tr_b16 v[150:151], v242 offset:25088\n" \
"v_mfma_f32_16x16x32_f16 v[84:87], v[124:127], v[160:163], v[84:87]\n" \
"s_waitcnt lgkmcnt(6)\n" \
"v_pk_add_f16 v160, v160, v164\n" \
"v_pk_add_f16 v161, v161, v165\n" \
"v_mfma_f32_16x16x32_f16 v[76:79], v[152:155], v[164:167], v[76:79]\n" \
"v_pk_add_f16 v162, v162, v166\n" \
"v_pk_add_f16 v163, v163, v167\n" \
"v_mfma_f32_16x16x32_f16 v[88:91], v[156:159], v[164:167], v[88:91]\n" \
"ds_read_b128 v[180:183], v240 offset:20992\n" \
"v_mfma_f32_16x16x32_f16 v[80:83], v[128:131], v[160:163], v[80:83]\n" \
"buffer_load_dwordx4 v232, s[36:39], s80 offen offset:3072 sc1 lds\n" \
"v_mfma_f32_16x16x32_f16 v[92:95], v[132:135], v[160:163], v[92:95]\n" \
"v_mfma_f32_16x16x32_f16 v[96:99], v[120:123], v[168:171], v[96:99]\n" \
"s_waitcnt lgkmcnt(5)\n" \
"v_pk_add_f16 v120, v136, v144\n" \
"v_pk_add_f16 v121, v137, v145\n" \
"ds_read_b128 v[160:163], v240 offset:22528\n" \
"s_add_i32 m0, s45, 0x3000\n" \
"s_mov_b32 s83, 0x18000\n" \
"v_mfma_f32_16x16x32_f16 v[108:111], v[124:127], v[168:171], v[108:111]\n" \
"v_pk_add_f16 v168, v168, v172\n" \
"v_pk_add_f16 v169, v169, v173\n" \
"ds_read_b128 v[164:167], v240 offset:23040\n" \
"buffer_load_dwordx4 v233, s[36:39], s83 offen sc1 lds\n" \
"v_mfma_f32_16x16x32_f16 v[100:103], v[152:155], v[172:175], v[100:103]\n" \
"v_pk_add_f16 v170, v170, v174\n" \
"v_pk_add_f16 v171, v171, v175\n" \
"v_mfma_f32_16x16x32_f16 v[112:115], v[156:159], v[172:175], v[112:115]\n" \
"s_waitcnt lgkmcnt(3)\n" \
"v_pk_add_f16 v122, v138, v146\n" \
"v_pk_add_f16 v123, v139, v147\n" \
"v_mfma_f32_16x16x32_f16 v[104:107], v[128:131], v[168:171], v[104:107]\n" \
"v_pk_add_f16 v124, v140, v148\n" \
"v_pk_add_f16 v125, v141, v149\n" \
"v_mfma_f32_16x16x32_f16 v[116:119], v[132:135], v[168:171], v[116:119]\n" \
"v_pk_add_f16 v126, v142, v150\n" \
"v_pk_add_f16 v127, v143, v151\n" \
"s_waitcnt vmcnt(8) lgkmcnt(0)\n" \
"s_barrier\n" \
"v_mfma_f32_16x16x32_f16 v[0:3], v[136:139], v[176:179], v[0:3]\n" \
"ds_read_b128 v[168:171], v240 offset:45056\n" \
"s_mov_b32 s81, 0x8010000\n" \
"ds_read_b64_tr_b16 v[184:185], v236 offset:32768\n" \
"s_add_i32 m0, s45, 0x4000\n" \
"s_mov_b32 s83, 0x20000\n" \
"v_mfma_f32_16x16x32_f16 v[12:15], v[140:143], v[176:179], v[12:15]\n" \
"v_pk_add_f16 v176, v176, v180\n" \
"v_pk_add_f16 v177, v177, v181\n" \
"buffer_load_dword v253, v252, s[36:39], s81 offen sc1\n" \
"buffer_load_dwordx4 v233, s[36:39], s83 offen sc1 lds\n" \
"v_mfma_f32_16x16x32_f16 v[4:7], v[144:147], v[180:183], v[4:7]\n" \
"v_pk_add_f16 v178, v178, v182\n" \
"v_pk_add_f16 v179, v179, v183\n" \
"ds_read_b64_tr_b16 v[188:189], v237 offset:32768\n" \
"v_mfma_f32_16x16x32_f16 v[16:19], v[148:151], v[180:183], v[16:19]\n" \
"ds_read_b128 v[172:175], v240 offset:45568\n" \
"s_add_i32 m0, s44, 0x4000\n" \
"s_add_i32 s80, s46, 0x4800000\n" \
"v_mfma_f32_16x16x32_f16 v[8:11], v[120:123], v[176:179], v[8:11]\n" \
"buffer_load_dwordx4 v232, s[36:39], s80 offen sc1 lds\n" \
"s_waitcnt lgkmcnt(2)\n" \
"v_mfma_f32_16x16x16_bf16 v[184:187], v[184:185], v[192:193], 0\n" \
"v_mfma_f32_16x16x32_f16 v[20:23], v[124:127], v[176:179], v[20:23]\n" \
"ds_read_b128 v[196:199], v249 offset:0\n" \
"ds_read_b64_tr_b16 v[200:201], v247 offset:0\n" \
"s_waitcnt lgkmcnt(3)\n" \
"v_mfma_f32_16x16x16_bf16 v[188:191], v[188:189], v[192:193], 0\n" \
"v_mfma_f32_16x16x32_f16 v[24:27], v[136:139], v[160:163], v[24:27]\n" \
"ds_read_b128 v[176:179], v240 offset:47104\n" \
"ds_read_b64_tr_b16 v[128:129], v241 offset:32768\n" \
"ds_read_b64_tr_b16 v[130:131], v241 offset:40960\n" \
"v_mfma_f32_16x16x32_f16 v[36:39], v[140:143], v[160:163], v[36:39]\n" \
"v_pk_add_f16 v160, v160, v164\n" \
"v_pk_add_f16 v161, v161, v165\n" \
"ds_read_b64_tr_b16 v[202:203], v247 offset:512\n" \
"ds_read_b64_tr_b16 v[132:133], v242 offset:32768\n" \
"v_mfma_f32_16x16x32_f16 v[28:31], v[144:147], v[164:167], v[28:31]\n" \
"v_pk_add_f16 v162, v162, v166\n" \
"v_pk_add_f16 v163, v163, v167\n" \
"v_cvt_pk_f16_f32 v184, v184, v185\n" \
"v_mfma_f32_16x16x32_f16 v[40:43], v[148:151], v[164:167], v[40:43]\n" \
"v_cvt_pk_f16_f32 v185, v186, v187\n" \
"v_cvt_pk_f16_f32 v186, v188, v189\n" \
"ds_read_b128 v[180:183], v240 offset:47616\n" \
"v_mfma_f32_16x16x32_f16 v[32:35], v[120:123], v[160:163], v[32:35]\n" \
"v_cvt_pk_f16_f32 v187, v190, v191\n" \
"v_add_u32 v184, s91, v184\n" \
"buffer_load_dwordx4 v232, s[36:39], s80 offen offset:1024 sc1 lds\n" \
"v_mfma_f32_16x16x32_f16 v[44:47], v[124:127], v[160:163], v[44:47]\n" \
"v_add_u32 v185, s91, v185\n" \
"v_add_u32 v186, s91, v186\n" \
"ds_read_b64_tr_b16 v[134:135], v242 offset:40960\n" \
"v_mfma_f32_16x16x32_f16 v[48:51], v[136:139], v[168:171], v[48:51]\n" \
"v_add_u32 v187, s91, v187\n" \
"v_and_b32 v184, s92, v184\n" \
"ds_read_b128 v[160:163], v240 offset:49152\n" \
"v_mfma_f32_16x16x32_f16 v[60:63], v[140:143], v[168:171], v[60:63]\n" \
"s_waitcnt lgkmcnt(5)\n" \
"v_pk_add_f16 v168, v168, v172\n" \
"v_pk_add_f16 v169, v169, v173\n" \
"v_and_b32 v185, s92, v185\n" \
"s_add_i32 s82, s49, 0x6800000\n" \
"s_waitcnt lgkmcnt(3)\n" \
"v_mfma_f32_16x16x32_bf16 v[204:207], v[200:203], v[196:199], 0\n" \
"v_mfma_f32_16x16x32_f16 v[52:55], v[144:147], v[172:175], v[52:55]\n" \
"v_pk_add_f16 v170, v170, v174\n" \
"v_pk_add_f16 v171, v171, v175\n" \
"v_and_b32 v186, s92, v186\n" \
"v_and_b32 v187, s92, v187\n" \
"v_mfma_f32_16x16x32_f16 v[64:67], v[148:151], v[172:175], v[64:67]\n" \
"ds_read_b128 v[164:167], v240 offset:49664\n" \
"buffer_store_dwordx4 v[184:187], v234, s[36:39], s82 offen sc1\n" \
"ds_read_b64_tr_b16 v[152:153], v241 offset:33280\n" \
"v_mfma_f32_16x16x32_f16 v[56:59], v[120:123], v[168:171], v[56:59]\n" \
"buffer_load_dwordx4 v232, s[36:39], s80 offen offset:2048 sc1 lds\n" \
"ds_read_b64_tr_b16 v[154:155], v241 offset:41472\n" \
"v_mfma_f32_16x16x32_f16 v[68:71], v[124:127], v[168:171], v[68:71]\n" \
"ds_read_b128 v[196:199], v249 offset:1024\n" \
"ds_read_b64_tr_b16 v[200:201], v247 offset:4096\n" \
"ds_read_b64_tr_b16 v[156:157], v242 offset:33280\n" \
"v_mfma_f32_16x16x32_f16 v[72:75], v[136:139], v[176:179], v[72:75]\n" \
"ds_read_b128 v[168:171], v240 offset:40960\n" \
"ds_read_b64_tr_b16 v[202:203], v247 offset:4608\n" \
"v_mfma_f32_16x16x32_f16 v[84:87], v[140:143], v[176:179], v[84:87]\n" \
"s_waitcnt lgkmcnt(8)\n" \
"v_pk_add_f16 v176, v176, v180\n" \
"v_pk_add_f16 v177, v177, v181\n" \
"v_mfma_f32_16x16x32_f16 v[76:79], v[144:147], v[180:183], v[76:79]\n" \
"v_pk_add_f16 v178, v178, v182\n" \
"v_pk_add_f16 v179, v179, v183\n" \
"v_mfma_f32_16x16x32_f16 v[88:91], v[148:151], v[180:183], v[88:91]\n" \
"ds_read_b128 v[172:175], v240 offset:41472\n" \
"ds_read_b64_tr_b16 v[158:159], v242 offset:41472\n" \
"v_mfma_f32_16x16x32_f16 v[80:83], v[120:123], v[176:179], v[80:83]\n" \
"buffer_load_dwordx4 v232, s[36:39], s80 offen offset:3072 sc1 lds\n" \
"v_mfma_f32_16x16x32_f16 v[92:95], v[124:127], v[176:179], v[92:95]\n" \
"v_mfma_f32_16x16x32_f16 v[96:99], v[136:139], v[160:163], v[96:99]\n" \
"s_waitcnt lgkmcnt(8)\n" \
"v_pk_add_f16 v136, v128, v152\n" \
"v_pk_add_f16 v137, v129, v153\n" \
"ds_read_b128 v[176:179], v240 offset:43008\n" \
"s_add_i32 m0, s45, 0x5000\n" \
"s_mov_b32 s83, 0x28000\n" \
"s_waitcnt lgkmcnt(3)\n" \
"v_mfma_f32_16x16x32_bf16 v[204:207], v[200:203], v[196:199], v[204:207]\n" \
"v_mfma_f32_16x16x32_f16 v[108:111], v[140:143], v[160:163], v[108:111]\n" \
"v_pk_add_f16 v160, v160, v164\n" \
"v_pk_add_f16 v161, v161, v165\n" \
"ds_read_b128 v[180:183], v240 offset:43520\n" \
"buffer_load_dwordx4 v233, s[36:39], s83 offen sc1 lds\n" \
"v_mfma_f32_16x16x32_f16 v[100:103], v[144:147], v[164:167], v[100:103]\n" \
"v_pk_add_f16 v162, v162, v166\n" \
"v_pk_add_f16 v163, v163, v167\n" \
"v_mfma_f32_16x16x32_f16 v[112:115], v[148:151], v[164:167], v[112:115]\n" \
"v_pk_add_f16 v138, v130, v154\n" \
"v_pk_add_f16 v139, v131, v155\n" \
"v_mfma_f32_16x16x32_f16 v[104:107], v[120:123], v[160:163], v[104:107]\n" \
"v_pk_add_f16 v140, v132, v156\n" \
"v_pk_add_f16 v141, v133, v157\n" \
"v_mfma_f32_16x16x32_f16 v[116:119], v[124:127], v[160:163], v[116:119]\n" \
"s_waitcnt lgkmcnt(2)\n" \
"v_pk_add_f16 v142, v134, v158\n" \
"v_pk_add_f16 v143, v135, v159\n" \
"s_waitcnt vmcnt(7) lgkmcnt(0)\n" \
"s_barrier\n" \
"v_mfma_f32_16x16x32_f16 v[0:3], v[128:131], v[168:171], v[0:3]\n" \
"ds_read_b128 v[160:163], v240 offset:4096\n" \
"ds_read_b64_tr_b16 v[184:185], v238 offset:32768\n" \
"ds_read_b64_tr_b16 v[188:189], v239 offset:32768\n" \
"s_add_i32 m0, s45, 0x0\n" \
"s_mov_b32 s83, 0x30000\n" \
"v_mfma_f32_16x16x32_f16 v[12:15], v[132:135], v[168:171], v[12:15]\n" \
"v_pk_add_f16 v168, v168, v172\n" \
"v_pk_add_f16 v169, v169, v173\n" \
"buffer_load_dwordx4 v233, s[36:39], s83 offen sc1 lds\n" \
"v_mfma_f32_16x16x32_f16 v[4:7], v[152:155], v[172:175], v[4:7]\n" \
"v_pk_add_f16 v170, v170, v174\n" \
"v_pk_add_f16 v171, v171, v175\n" \
"v_cmp_eq_u32 vcc, 0, v253\n" \
"s_cbranch_vccnz .Ls08_%=\n" \
".Lr08_%=:\n" \
"v_mfma_f32_16x16x32_f16 v[16:19], v[156:159], v[172:175], v[16:19]\n" \
"ds_read_b128 v[164:167], v240 offset:4608\n" \
"s_add_i32 m0, s44, 0x8000\n" \
"s_add_i32 s80, s46, 0x4c00000\n" \
"v_mfma_f32_16x16x32_f16 v[8:11], v[136:139], v[168:171], v[8:11]\n" \
"buffer_load_dwordx4 v232, s[36:39], s80 offen sc1 lds\n" \
"ds_read_b64_tr_b16 v[120:121], v241 offset:0\n" \
"s_waitcnt lgkmcnt(3)\n" \
"v_mfma_f32_16x16x16_bf16 v[184:187], v[184:185], v[192:193], 0\n" \
"v_mfma_f32_16x16x32_f16 v[20:23], v[140:143], v[168:171], v[20:23]\n" \
"ds_read_b128 v[196:199], v249 offset:2048\n" \
"ds_read_b64_tr_b16 v[200:201], v247 offset:8192\n" \
"s_waitcnt lgkmcnt(4)\n" \
"v_mfma_f32_16x16x16_bf16 v[188:191], v[188:189], v[192:193], 0\n" \
"v_mfma_f32_16x16x32_f16 v[24:27], v[128:131], v[176:179], v[24:27]\n" \
"ds_read_b128 v[168:171], v240 offset:6144\n" \
"s_add_i32 s84, s52, 0x8014000\n" \
"ds_read_b64_tr_b16 v[122:123], v241 offset:8192\n" \
"v_mfma_f32_16x16x32_f16 v[36:39], v[132:135], v[176:179], v[36:39]\n" \
"v_pk_add_f16 v176, v176, v180\n" \
"v_pk_add_f16 v177, v177, v181\n" \
"buffer_store_dword v240, v243, s[36:39], s84 offen sc1\n" \
"v_mfma_f32_16x16x32_f16 v[28:31], v[152:155], v[180:183], v[28:31]\n" \
"v_pk_add_f16 v178, v178, v182\n" \
"v_pk_add_f16 v179, v179, v183\n" \
"v_cvt_pk_f16_f32 v184, v184, v185\n" \
"ds_read_b64_tr_b16 v[202:203], v247 offset:8704\n" \
"v_mfma_f32_16x16x32_f16 v[40:43], v[156:159], v[180:183], v[40:43]\n" \
"v_cvt_pk_f16_f32 v185, v186, v187\n" \
"v_cvt_pk_f16_f32 v186, v188, v189\n" \
"ds_read_b128 v[172:175], v240 offset:6656\n" \
"ds_read_b64_tr_b16 v[124:125], v242 offset:0\n" \
"v_mfma_f32_16x16x32_f16 v[32:35], v[136:139], v[176:179], v[32:35]\n" \
"v_cvt_pk_f16_f32 v187, v190, v191\n" \
"v_add_u32 v184, s91, v184\n" \
"buffer_load_dwordx4 v232, s[36:39], s80 offen offset:1024 sc1 lds\n" \
"v_mfma_f32_16x16x32_f16 v[44:47], v[140:143], v[176:179], v[44:47]\n" \
"v_add_u32 v185, s91, v185\n" \
"v_add_u32 v186, s91, v186\n" \
"ds_read_b64_tr_b16 v[126:127], v242 offset:8192\n" \
"v_mfma_f32_16x16x32_f16 v[48:51], v[128:131], v[160:163], v[48:51]\n" \
"v_add_u32 v187, s91, v187\n" \
"v_and_b32 v184, s92, v184\n" \
"ds_read_b128 v[176:179], v240 offset:8192\n" \
"v_mfma_f32_16x16x32_f16 v[60:63], v[132:135], v[160:163], v[60:63]\n" \
"s_waitcnt lgkmcnt(5)\n" \
"v_pk_add_f16 v160, v160, v164\n" \
"v_pk_add_f16 v161, v161, v165\n" \
"v_and_b32 v185, s92, v185\n" \
"s_waitcnt lgkmcnt(4)\n" \
"v_mfma_f32_16x16x32_bf16 v[204:207], v[200:203], v[196:199], v[204:207]\n" \
"v_mfma_f32_16x16x32_f16 v[52:55], v[152:155], v[164:167], v[52:55]\n" \
"v_pk_add_f16 v162, v162, v166\n" \
"v_pk_add_f16 v163, v163, v167\n" \
"v_and_b32 v186, s92, v186\n" \
"v_and_b32 v187, s92, v187\n" \
"v_mfma_f32_16x16x32_f16 v[64:67], v[156:159], v[164:167], v[64:67]\n" \
"ds_read_b128 v[180:183], v240 offset:8704\n" \
"buffer_store_dwordx4 v[184:187], v234, s[36:39], s82 offen offset:1024 sc1\n" \
"ds_read_b64_tr_b16 v[144:145], v241 offset:512\n" \
"v_mfma_f32_16x16x32_f16 v[56:59], v[136:139], v[160:163], v[56:59]\n" \
"buffer_load_dwordx4 v232, s[36:39], s80 offen offset:2048 sc1 lds\n" \
"ds_read_b64_tr_b16 v[146:147], v241 offset:8704\n" \
"v_mfma_f32_16x16x32_f16 v[68:71], v[140:143], v[160:163], v[68:71]\n" \
"ds_read_b128 v[196:199], v249 offset:3072\n" \
"ds_read_b64_tr_b16 v[200:201], v247 offset:12288\n" \
"ds_read_b64_tr_b16 v[148:149], v242 offset:512\n" \
"v_mfma_f32_16x16x32_f16 v[72:75], v[128:131], v[168:171], v[72:75]\n" \
"ds_read_b128 v[160:163], v240 offset:0\n" \
"ds_read_b64_tr_b16 v[202:203], v247 offset:12800\n" \
"v_mfma_f32_16x16x32_f16 v[84:87], v[132:135], v[168:171], v[84:87]\n" \
"s_waitcnt lgkmcnt(8)\n" \
"v_pk_add_f16 v168, v168, v172\n" \
"v_pk_add_f16 v169, v169, v173\n" \
"v_mfma_f32_16x16x32_f16 v[76:79], v[152:155], v[172:175], v[76:79]\n" \
"v_pk_add_f16 v170, v170, v174\n" \
"v_pk_add_f16 v171, v171, v175\n" \
"v_mfma_f32_16x16x32_f16 v[88:91], v[156:159], v[172:175], v[88:91]\n" \
"ds_read_b128 v[164:167], v240 offset:512\n" \
"ds_read_b64_tr_b16 v[150:151], v242 offset:8704\n" \
"v_mfma_f32_16x16x32_f16 v[80:83], v[136:139], v[168:171], v[80:83]\n" \
"buffer_load_dwordx4 v232, s[36:39], s80 offen offset:3072 sc1 lds\n" \
"v_mfma_f32_16x16x32_f16 v[92:95], v[140:143], v[168:171], v[92:95]\n" \
"v_mfma_f32_16x16x32_f16 v[96:99], v[128:131], v[176:179], v[96:99]\n" \
"s_waitcnt lgkmcnt(8)\n" \
"v_pk_add_f16 v128, v120, v144\n" \
"v_pk_add_f16 v129, v121, v145\n" \
"ds_read_b128 v[168:171], v240 offset:2048\n" \
"s_add_i32 m0, s45, 0x1000\n" \
"s_mov_b32 s83, 0x38000\n" \
"s_waitcnt lgkmcnt(3)\n" \
"v_mfma_f32_16x16x32_bf16 v[204:207], v[200:203], v[196:199], v[204:207]\n" \
"v_mfma_f32_16x16x32_f16 v[108:111], v[132:135], v[176:179], v[108:111]\n" \
"v_pk_add_f16 v176, v176, v180\n" \
"v_pk_add_f16 v177, v177, v181\n" \
"ds_read_b128 v[172:175], v240 offset:2560\n" \
"buffer_load_dwordx4 v233, s[36:39], s83 offen sc1 lds\n" \
"v_mfma_f32_16x16x32_f16 v[100:103], v[152:155], v[180:183], v[100:103]\n" \
"v_pk_add_f16 v178, v178, v182\n" \
"v_pk_add_f16 v179, v179, v183\n" \
"v_mfma_f32_16x16x32_f16 v[112:115], v[156:159], v[180:183], v[112:115]\n" \
"v_pk_add_f16 v130, v122, v146\n" \
"v_pk_add_f16 v131, v123, v147\n" \
"v_mfma_f32_16x16x32_f16 v[104:107], v[136:139], v[176:179], v[104:107]\n" \
"v_pk_add_f16 v132, v124, v148\n" \
"v_pk_add_f16 v133, v125, v149\n" \
"v_mfma_f32_16x16x32_f16 v[116:119], v[140:143], v[176:179], v[116:119]\n" \
"s_waitcnt lgkmcnt(2)\n" \
"v_pk_add_f16 v134, v126, v150\n" \
"v_pk_add_f16 v135, v127, v151\n" \
"s_waitcnt vmcnt(8) lgkmcnt(0)\n" \
"s_barrier\n" \
"v_mfma_f32_16x16x32_f16 v[0:3], v[120:123], v[160:163], v[0:3]\n" \
"ds_read_b128 v[176:179], v240 offset:24576\n" \
"s_mov_b32 s81, 0x8012000\n" \
"ds_read_b64_tr_b16 v[184:185], v236 offset:36864\n" \
"s_add_i32 m0, s45, 0x2000\n" \
"s_mov_b32 s83, 0x40000\n" \
"v_mfma_f32_16x16x32_f16 v[12:15], v[124:127], v[160:163], v[12:15]\n" \
"v_pk_add_f16 v160, v160, v164\n" \
"v_pk_add_f16 v161, v161, v165\n" \
"buffer_load_dword v253, v252, s[36:39], s81 offen sc1\n" \
"buffer_load_dwordx4 v233, s[36:39], s83 offen sc1 lds\n" \
"v_mfma_f32_16x16x32_f16 v[4:7], v[144:147], v[164:167], v[4:7]\n" \
"v_pk_add_f16 v162, v162, v166\n" \
"v_pk_add_f16 v163, v163, v167\n" \
"ds_read_b64_tr_b16 v[188:189], v237 offset:36864\n" \
"v_mfma_f32_16x16x32_f16 v[16:19], v[148:151], v[164:167], v[16:19]\n" \
"ds_read_b128 v[180:183], v240 offset:25088\n" \
"s_add_i32 m0, s44, 0x0\n" \
"s_add_i32 s80, s46, 0x5000000\n" \
"v_mfma_f32_16x16x32_f16 v[8:11], v[128:131], v[160:163], v[8:11]\n" \
"buffer_load_dwordx4 v232, s[36:39], s80 offen sc1 lds\n" \
"s_waitcnt lgkmcnt(2)\n" \
"v_mfma_f32_16x16x16_bf16 v[184:187], v[184:185], v[192:193], 0\n" \
"v_mfma_f32_16x16x32_f16 v[20:23], v[132:135], v[160:163], v[20:23]\n" \
"ds_read_b128 v[196:199], v249 offset:4096\n" \
"ds_read_b64_tr_b16 v[200:201], v247 offset:16384\n" \
"s_waitcnt lgkmcnt(3)\n" \
"v_mfma_f32_16x16x16_bf16 v[188:191], v[188:189], v[192:193], 0\n" \
"v_mfma_f32_16x16x32_f16 v[24:27], v[120:123], v[168:171], v[24:27]\n" \
"ds_read_b128 v[160:163], v240 offset:26624\n" \
"ds_read_b64_tr_b16 v[136:137], v241 offset:16384\n" \
"ds_read_b64_tr_b16 v[138:139], v241 offset:24576\n" \
"v_mfma_f32_16x16x32_f16 v[36:39], v[124:127], v[168:171], v[36:39]\n" \
"v_pk_add_f16 v168, v168, v172\n" \
"v_pk_add_f16 v169, v169, v173\n" \
"ds_read_b64_tr_b16 v[202:203], v247 offset:16896\n" \
"ds_read_b64_tr_b16 v[140:141], v242 offset:16384\n" \
"v_mfma_f32_16x16x32_f16 v[28:31], v[144:147], v[172:175], v[28:31]\n" \
"v_pk_add_f16 v170, v170, v174\n" \
"v_pk_add_f16 v171, v171, v175\n" \
"v_cvt_pk_f16_f32 v184, v184, v185\n" \
"v_mfma_f32_16x16x32_f16 v[40:43], v[148:151], v[172:175], v[40:43]\n" \
"v_cvt_pk_f16_f32 v185, v186, v187\n" \
"v_cvt_pk_f16_f32 v186, v188, v189\n" \
"ds_read_b128 v[164:167], v240 offset:27136\n" \
"v_mfma_f32_16x16x32_f16 v[32:35], v[128:131], v[168:171], v[32:35]\n" \
"v_cvt_pk_f16_f32 v187, v190, v191\n" \
"v_add_u32 v184, s91, v184\n" \
"buffer_load_dwordx4 v232, s[36:39], s80 offen offset:1024 sc1 lds\n" \
"v_mfma_f32_16x16x32_f16 v[44:47], v[132:135], v[168:171], v[44:47]\n" \
"v_add_u32 v185, s91, v185\n" \
"v_add_u32 v186, s91, v186\n" \
"ds_read_b64_tr_b16 v[142:143], v242 offset:24576\n" \
"v_mfma_f32_16x16x32_f16 v[48:51], v[120:123], v[176:179], v[48:51]\n" \
"v_add_u32 v187, s91, v187\n" \
"v_and_b32 v184, s92, v184\n" \
"ds_read_b128 v[168:171], v240 offset:28672\n" \
"v_mfma_f32_16x16x32_f16 v[60:63], v[124:127], v[176:179], v[60:63]\n" \
"s_waitcnt lgkmcnt(5)\n" \
"v_pk_add_f16 v176, v176, v180\n" \
"v_pk_add_f16 v177, v177, v181\n" \
"v_and_b32 v185, s92, v185\n" \
"s_add_i32 s82, s50, 0x7000000\n" \
"s_waitcnt lgkmcnt(3)\n" \
"v_mfma_f32_16x16x32_bf16 v[204:207], v[200:203], v[196:199], v[204:207]\n" \
"v_mfma_f32_16x16x32_f16 v[52:55], v[144:147], v[180:183], v[52:55]\n" \
"v_pk_add_f16 v178, v178, v182\n" \
"v_pk_add_f16 v179, v179, v183\n" \
"v_and_b32 v186, s92, v186\n" \
"v_and_b32 v187, s92, v187\n" \
"v_mfma_f32_16x16x32_f16 v[64:67], v[148:151], v[180:183], v[64:67]\n" \
"ds_read_b128 v[172:175], v240 offset:29184\n" \
"buffer_store_dwordx4 v[184:187], v234, s[36:39], s82 offen sc1\n" \
"ds_read_b64_tr_b16 v[152:153], v241 offset:16896\n" \
"v_mfma_f32_16x16x32_f16 v[56:59], v[128:131], v[176:179], v[56:59]\n" \
"buffer_load_dwordx4 v232, s[36:39], s80 offen offset:2048 sc1 lds\n" \
"ds_read_b64_tr_b16 v[154:155], v241 offset:25088\n" \
"v_mfma_f32_16x16x32_f16 v[68:71], v[132:135], v[176:179], v[68:71]\n" \
"ds_read_b128 v[196:199], v249 offset:5120\n" \
"ds_read_b64_tr_b16 v[200:201], v247 offset:20480\n" \
"ds_read_b64_tr_b16 v[156:157], v242 offset:16896\n" \
"v_mfma_f32_16x16x32_f16 v[72:75], v[120:123], v[160:163], v[72:75]\n" \
"ds_read_b128 v[176:179], v240 offset:20480\n" \
"ds_read_b64_tr_b16 v[202:203], v247 offset:20992\n" \
"v_mfma_f32_16x16x32_f16 v[84:87], v[124:127], v[160:163], v[84:87]\n" \
"s_waitcnt lgkmcnt(8)\n" \
"v_pk_add_f16 v160, v160, v164\n" \
"v_pk_add_f16 v161, v161, v165\n" \
"v_mfma_f32_16x16x32_f16 v[76:79], v[144:147], v[164:167], v[76:79]\n" \
"v_pk_add_f16 v162, v162, v166\n" \
"v_pk_add_f16 v163, v163, v167\n" \
"v_mfma_f32_16x16x32_f16 v[88:91], v[148:151], v[164:167], v[88:91]\n" \
"ds_read_b128 v[180:183], v240 offset:20992\n" \
"ds_read_b64_tr_b16 v[158:159], v242 offset:25088\n" \
"v_mfma_f32_16x16x32_f16 v[80:83], v[128:131], v[160:163], v[80:83]\n" \
"buffer_load_dwordx4 v232, s[36:39], s80 offen offset:3072 sc1 lds\n" \
"v_mfma_f32_16x16x32_f16 v[92:95], v[132:135], v[160:163], v[92:95]\n" \
"v_mfma_f32_16x16x32_f16 v[96:99], v[120:123], v[168:171], v[96:99]\n" \
"s_waitcnt lgkmcnt(8)\n" \
"v_pk_add_f16 v120, v136, v152\n" \
"v_pk_add_f16 v121, v137, v153\n" \
"ds_read_b128 v[160:163], v240 offset:22528\n" \
"s_add_i32 m0, s45, 0x3000\n" \
"s_mov_b32 s83, 0x48000\n" \
"s_waitcnt lgkmcnt(3)\n" \
"v_mfma_f32_16x16x32_bf16 v[204:207], v[200:203], v[196:199], v[204:207]\n" \
"v_mfma_f32_16x16x32_f16 v[108:111], v[124:127], v[168:171], v[108:111]\n" \
"v_pk_add_f16 v168, v168, v172\n" \
"v_pk_add_f16 v169, v169, v173\n" \
"ds_read_b128 v[164:167], v240 offset:23040\n" \
"buffer_load_dwordx4 v233, s[36:39], s83 offen sc1 lds\n" \
"v_mfma_f32_16x16x32_f16 v[100:103], v[144:147], v[172:175], v[100:103]\n" \
"v_pk_add_f16 v170, v170, v174\n" \
"v_pk_add_f16 v171, v171, v175\n" \
"v_mfma_f32_16x16x32_f16 v[112:115], v[148:151], v[172:175], v[112:115]\n" \
"v_pk_add_f16 v122, v138, v154\n" \
"v_pk_add_f16 v123, v139, v155\n" \
"v_mfma_f32_16x16x32_f16 v[104:107], v[128:131], v[168:171], v[104:107]\n" \
"v_pk_add_f16 v124, v140, v156\n" \
"v_pk_add_f16 v125, v141, v157\n" \
"v_mfma_f32_16x16x32_f16 v[116:119], v[132:135], v[168:171], v[116:119]\n" \
"s_waitcnt lgkmcnt(2)\n" \
"v_pk_add_f16 v126, v142, v158\n" \
"v_pk_add_f16 v127, v143, v159\n" \
"s_waitcnt vmcnt(7) lgkmcnt(0)\n" \
"s_barrier\n" \
"v_mfma_f32_16x16x32_f16 v[0:3], v[136:139], v[176:179], v[0:3]\n" \
"ds_read_b128 v[168:171], v240 offset:45056\n" \
"ds_read_b64_tr_b16 v[184:185], v238 offset:36864\n" \
"ds_read_b64_tr_b16 v[188:189], v239 offset:36864\n" \
"s_add_i32 m0, s45, 0x4000\n" \
"s_mov_b32 s83, 0x50000\n" \
"v_mfma_f32_16x16x32_f16 v[12:15], v[140:143], v[176:179], v[12:15]\n" \
"v_pk_add_f16 v176, v176, v180\n" \
"v_pk_add_f16 v177, v177, v181\n" \
"buffer_load_dwordx4 v233, s[36:39], s83 offen sc1 lds\n" \
"v_mfma_f32_16x16x32_f16 v[4:7], v[152:155], v[180:183], v[4:7]\n" \
"v_pk_add_f16 v178, v178, v182\n" \
"v_pk_add_f16 v179, v179, v183\n" \
"v_cmp_eq_u32 vcc, 0, v253\n" \
"s_cbranch_vccnz .Ls09_%=\n" \
".Lr09_%=:\n" \
"v_mfma_f32_16x16x32_f16 v[16:19], v[156:159], v[180:183], v[16:19]\n" \
"ds_read_b128 v[172:175], v240 offset:45568\n" \
"s_add_i32 m0, s44, 0x4000\n" \
"s_add_i32 s80, s46, 0x5400000\n" \
"v_mfma_f32_16x16x32_f16 v[8:11], v[120:123], v[176:179], v[8:11]\n" \
"buffer_load_dwordx4 v232, s[36:39], s80 offen sc1 lds\n" \
"ds_read_b64_tr_b16 v[128:129], v241 offset:32768\n" \
"s_waitcnt lgkmcnt(3)\n" \
"v_mfma_f32_16x16x16_bf16 v[184:187], v[184:185], v[192:193], 0\n" \
"v_mfma_f32_16x16x32_f16 v[20:23], v[124:127], v[176:179], v[20:23]\n" \
"ds_read_b128 v[196:199], v249 offset:6144\n" \
"ds_read_b64_tr_b16 v[200:201], v247 offset:0\n" \
"s_waitcnt lgkmcnt(4)\n" \
"v_mfma_f32_16x16x16_bf16 v[188:191], v[188:189], v[192:193], 0\n" \
"v_mfma_f32_16x16x32_f16 v[24:27], v[136:139], v[160:163], v[24:27]\n" \
"ds_read_b128 v[176:179], v240 offset:47104\n" \
"s_add_i32 s84, s53, 0x8016000\n" \
"ds_read_b64_tr_b16 v[130:131], v241 offset:40960\n" \
"v_mfma_f32_16x16x32_f16 v[36:39], v[140:143], v[160:163], v[36:39]\n" \
"v_pk_add_f16 v160, v160, v164\n" \
"v_pk_add_f16 v161, v161, v165\n" \
"buffer_store_dword v240, v243, s[36:39], s84 offen sc1\n" \
"v_mfma_f32_16x16x32_f16 v[28:31], v[152:155], v[164:167], v[28:31]\n" \
"v_pk_add_f16 v162, v162, v166\n" \
"v_pk_add_f16 v163, v163, v167\n" \
"v_cvt_pk_f16_f32 v184, v184, v185\n" \
"ds_read_b64_tr_b16 v[202:203], v247 offset:512\n" \
"v_mfma_f32_16x16x32_f16 v[40:43], v[156:159], v[164:167], v[40:43]\n" \
"v_cvt_pk_f16_f32 v185, v186, v187\n" \
"v_cvt_pk_f16_f32 v186, v188, v189\n" \
"ds_read_b128 v[180:183], v240 offset:47616\n" \
"ds_read_b64_tr_b16 v[132:133], v242 offset:32768\n" \
"v_mfma_f32_16x16x32_f16 v[32:35], v[120:123], v[160:163], v[32:35]\n" \
"v_cvt_pk_f16_f32 v187, v190, v191\n" \
"v_add_u32 v184, s91, v184\n" \
"buffer_load_dwordx4 v232, s[36:39], s80 offen offset:1024 sc1 lds\n" \
"v_mfma_f32_16x16x32_f16 v[44:47], v[124:127], v[160:163], v[44:47]\n" \
"v_add_u32 v185, s91, v185\n" \
"v_add_u32 v186, s91, v186\n" \
"ds_read_b64_tr_b16 v[134:135], v242 offset:40960\n" \
"v_mfma_f32_16x16x32_f16 v[48:51], v[136:139], v[168:171], v[48:51]\n" \
"v_add_u32 v187, s91, v187\n" \
"v_and_b32 v184, s92, v184\n" \
"ds_read_b128 v[160:163], v240 offset:49152\n" \
"v_mfma_f32_16x16x32_f16 v[60:63], v[140:143], v[168:171], v[60:63]\n" \
"s_waitcnt lgkmcnt(5)\n" \
"v_pk_add_f16 v168, v168, v172\n" \
"v_pk_add_f16 v169, v169, v173\n" \
"v_and_b32 v185, s92, v185\n" \
"s_waitcnt lgkmcnt(4)\n" \
"v_mfma_f32_16x16x32_bf16 v[204:207], v[200:203], v[196:199], v[204:207]\n" \
"v_mfma_f32_16x16x32_f16 v[52:55], v[152:155], v[172:175], v[52:55]\n" \
"v_pk_add_f16 v170, v170, v174\n" \
"v_pk_add_f16 v171, v171, v175\n" \
"v_and_b32 v186, s92, v186\n" \
"v_and_b32 v187, s92, v187\n" \
"v_mfma_f32_16x16x32_f16 v[64:67], v[156:159], v[172:175], v[64:67]\n" \
"ds_read_b128 v[164:167], v240 offset:49664\n" \
"buffer_store_dwordx4 v[184:187], v234, s[36:39], s82 offen offset:1024 sc1\n" \
"ds_read_b64_tr_b16 v[144:145], v241 offset:33280\n" \
"v_mfma_f32_16x16x32_f16 v[56:59], v[120:123], v[168:171], v[56:59]\n" \
"buffer_load_dwordx4 v232, s[36:39], s80 offen offset:2048 sc1 lds\n" \
"ds_read_b64_tr_b16 v[146:147], v241 offset:41472\n" \
"v_mfma_f32_16x16x32_f16 v[68:71], v[124:127], v[168:171], v[68:71]\n" \
"ds_read_b128 v[196:199], v249 offset:7168\n" \
"ds_read_b64_tr_b16 v[200:201], v247 offset:4096\n" \
"ds_read_b64_tr_b16 v[148:149], v242 offset:33280\n" \
"v_mfma_f32_16x16x32_f16 v[72:75], v[136:139], v[176:179], v[72:75]\n" \
"ds_read_b128 v[168:171], v240 offset:40960\n" \
"ds_read_b64_tr_b16 v[202:203], v247 offset:4608\n" \
"v_mfma_f32_16x16x32_f16 v[84:87], v[140:143], v[176:179], v[84:87]\n" \
"s_waitcnt lgkmcnt(8)\n" \
"v_pk_add_f16 v176, v176, v180\n" \
"v_pk_add_f16 v177, v177, v181\n" \
"v_mfma_f32_16x16x32_f16 v[76:79], v[152:155], v[180:183], v[76:79]\n" \
"v_pk_add_f16 v178, v178, v182\n" \
"v_pk_add_f16 v179, v179, v183\n" \
"v_mfma_f32_16x16x32_f16 v[88:91], v[156:159], v[180:183], v[88:91]\n" \
"ds_read_b128 v[172:175], v240 offset:41472\n" \
"ds_read_b64_tr_b16 v[150:151], v242 offset:41472\n" \
"v_mfma_f32_16x16x32_f16 v[80:83], v[120:123], v[176:179], v[80:83]\n" \
"buffer_load_dwordx4 v232, s[36:39], s80 offen offset:3072 sc1 lds\n" \
"v_mfma_f32_16x16x32_f16 v[92:95], v[124:127], v[176:179], v[92:95]\n" \
"v_mfma_f32_16x16x32_f16 v[96:99], v[136:139], v[160:163], v[96:99]\n" \
"s_waitcnt lgkmcnt(8)\n" \
"v_pk_add_f16 v136, v128, v144\n" \
"v_pk_add_f16 v137, v129, v145\n" \
"ds_read_b128 v[176:179], v240 offset:43008\n" \
"s_add_i32 m0, s45, 0x5000\n" \
"s_mov_b32 s83, 0x58000\n" \
"s_waitcnt lgkmcnt(3)\n" \
"v_mfma_f32_16x16x32_bf16 v[204:207], v[200:203], v[196:199], v[204:207]\n" \
"v_mfma_f32_16x16x32_f16 v[108:111], v[140:143], v[160:163], v[108:111]\n" \
"v_pk_add_f16 v160, v160, v164\n" \
"v_pk_add_f16 v161, v161, v165\n" \
"ds_read_b128 v[180:183], v240 offset:43520\n" \
"buffer_load_dwordx4 v233, s[36:39], s83 offen sc1 lds\n" \
"v_mfma_f32_16x16x32_f16 v[100:103], v[152:155], v[164:167], v[100:103]\n" \
"v_pk_add_f16 v162, v162, v166\n" \
"v_pk_add_f16 v163, v163, v167\n" \
"v_mfma_f32_16x16x32_f16 v[112:115], v[156:159], v[164:167], v[112:115]\n" \
"v_pk_add_f16 v138, v130, v146\n" \
"v_pk_add_f16 v139, v131, v147\n" \
"v_mfma_f32_16x16x32_f16 v[104:107], v[120:123], v[160:163], v[104:107]\n" \
"v_pk_add_f16 v140, v132, v148\n" \
"v_pk_add_f16 v141, v133, v149\n" \
"v_mfma_f32_16x16x32_f16 v[116:119], v[124:127], v[160:163], v[116:119]\n" \
"s_waitcnt lgkmcnt(2)\n" \
"v_pk_add_f16 v142, v134, v150\n" \
"v_pk_add_f16 v143, v135, v151\n" \
"s_waitcnt vmcnt(8) lgkmcnt(0)\n" \
"s_barrier\n" \
"v_mfma_f32_16x16x32_f16 v[0:3], v[128:131], v[168:171], v[0:3]\n" \
"ds_read_b128 v[160:163], v240 offset:4096\n" \
"ds_read_b64_tr_b16 v[184:185], v236 offset:40960\n" \
"s_add_i32 m0, s45, 0x0\n" \
"s_mov_b32 s83, 0x60000\n" \
"v_mfma_f32_16x16x32_f16 v[12:15], v[132:135], v[168:171], v[12:15]\n" \
"v_pk_add_f16 v168, v168, v172\n" \
"v_pk_add_f16 v169, v169, v173\n" \
"ds_read_b64_tr_b16 v[188:189], v237 offset:40960\n" \
"buffer_load_dwordx4 v233, s[36:39], s83 offen sc1 lds\n" \
"v_mfma_f32_16x16x32_f16 v[4:7], v[144:147], v[172:175], v[4:7]\n" \
"v_pk_add_f16 v170, v170, v174\n" \
"v_pk_add_f16 v171, v171, v175\n" \
"v_mfma_f32_16x16x32_f16 v[16:19], v[148:151], v[172:175], v[16:19]\n" \
"ds_read_b128 v[164:167], v240 offset:4608\n" \
"s_add_i32 m0, s44, 0x8000\n" \
"s_add_i32 s80, s46, 0x5800000\n" \
"v_mfma_f32_16x16x32_f16 v[8:11], v[136:139], v[168:171], v[8:11]\n" \
"buffer_load_dwordx4 v232, s[36:39], s80 offen sc1 lds\n" \
"v_mfma_f32_16x16x32_f16 v[20:23], v[140:143], v[168:171], v[20:23]\n" \
"ds_read_b128 v[196:199], v249 offset:0\n" \
"ds_read_b64_tr_b16 v[200:201], v247 offset:8192\n" \
"s_waitcnt lgkmcnt(4)\n" \
"v_mfma_f32_16x16x16_bf16 v[184:187], v[184:185], v[192:193], 0\n" \
"v_mfma_f32_16x16x32_f16 v[24:27], v[128:131], v[176:179], v[24:27]\n" \
"ds_read_b128 v[168:171], v240 offset:6144\n" \
"ds_read_b64_tr_b16 v[120:121], v241 offset:0\n" \
"ds_read_b64_tr_b16 v[122:123], v241 offset:8192\n" \
"s_waitcnt lgkmcnt(6)\n" \
"v_mfma_f32_16x16x16_bf16 v[188:191], v[188:189], v[192:193], 0\n" \
"v_mfma_f32_16x16x32_f16 v[36:39], v[132:135], v[176:179], v[36:39]\n" \
"v_pk_add_f16 v176, v176, v180\n" \
"v_pk_add_f16 v177, v177, v181\n" \
"ds_read_b64_tr_b16 v[202:203], v247 offset:8704\n" \
"v_mfma_f32_16x16x32_f16 v[28:31], v[144:147], v[180:183], v[28:31]\n" \
"v_pk_add_f16 v178, v178, v182\n" \
"v_pk_add_f16 v179, v179, v183\n" \
"ds_read_b64_tr_b16 v[124:125], v242 offset:0\n" \
"v_mfma_f32_16x16x32_f16 v[40:43], v[148:151], v[180:183], v[40:43]\n" \
"v_cvt_pk_f16_f32 v184, v184, v185\n" \
"ds_read_b128 v[172:175], v240 offset:6656\n" \
"ds_read_b64_tr_b16 v[126:127], v242 offset:8192\n" \
"v_mfma_f32_16x16x32_f16 v[32:35], v[136:139], v[176:179], v[32:35]\n" \
"v_cvt_pk_f16_f32 v185, v186, v187\n" \
"v_cvt_pk_f16_f32 v186, v188, v189\n" \
"buffer_load_dwordx4 v232, s[36:39], s80 offen offset:1024 sc1 lds\n" \
"v_mfma_f32_16x16x32_f16 v[44:47], v[140:143], v[176:179], v[44:47]\n" \
"v_cvt_pk_f16_f32 v187, v190, v191\n" \
"v_add_u32 v184, s91, v184\n" \
"v_mfma_f32_16x16x32_f16 v[48:51], v[128:131], v[160:163], v[48:51]\n" \
"v_add_u32 v185, s91, v185\n" \
"v_add_u32 v186, s91, v186\n" \
"ds_read_b128 v[176:179], v240 offset:8192\n" \
"v_mfma_f32_16x16x32_f16 v[60:63], v[132:135], v[160:163], v[60:63]\n" \
"s_waitcnt lgkmcnt(5)\n" \
"v_pk_add_f16 v160, v160, v164\n" \
"v_pk_add_f16 v161, v161, v165\n" \
"v_add_u32 v187, s91, v187\n" \
"v_and_b32 v184, s92, v184\n" \
"s_add_i32 s82, s51, 0x7800000\n" \
"s_waitcnt lgkmcnt(4)\n" \
"v_mfma_f32_16x16x32_bf16 v[204:207], v[200:203], v[196:199], v[204:207]\n" \
"v_mfma_f32_16x16x32_f16 v[52:55], v[144:147], v[164:167], v[52:55]\n" \
"v_pk_add_f16 v162, v162, v166\n" \
"v_pk_add_f16 v163, v163, v167\n" \
"v_and_b32 v185, s92, v185\n" \
"v_and_b32 v186, s92, v186\n" \
"v_and_b32 v187, s92, v187\n" \
"v_mfma_f32_16x16x32_f16 v[64:67], v[148:151], v[164:167], v[64:67]\n" \
"ds_read_b128 v[180:183], v240 offset:8704\n" \
"buffer_store_dwordx4 v[184:187], v234, s[36:39], s82 offen sc1\n" \
"ds_read_b64_tr_b16 v[152:153], v241 offset:512\n" \
"v_mfma_f32_16x16x32_f16 v[56:59], v[136:139], v[160:163], v[56:59]\n" \
"buffer_load_dwordx4 v232, s[36:39], s80 offen offset:2048 sc1 lds\n" \
"ds_read_b128 v[196:199], v249 offset:1024\n" \
"v_mfma_f32_16x16x32_f16 v[68:71], v[140:143], v[160:163], v[68:71]\n" \
"ds_read_b64_tr_b16 v[184:185], v238 offset:40960\n" \
"ds_read_b64_tr_b16 v[188:189], v239 offset:40960\n" \
"ds_read_b64_tr_b16 v[154:155], v241 offset:8704\n" \
"v_mfma_f32_16x16x32_f16 v[72:75], v[128:131], v[168:171], v[72:75]\n" \
"ds_read_b128 v[160:163], v240 offset:0\n" \
"ds_read_b64_tr_b16 v[200:201], v247 offset:12288\n" \
"ds_read_b64_tr_b16 v[202:203], v247 offset:12800\n" \
"v_mfma_f32_16x16x32_f16 v[84:87], v[132:135], v[168:171], v[84:87]\n" \
"s_waitcnt lgkmcnt(9)\n" \
"v_pk_add_f16 v168, v168, v172\n" \
"v_pk_add_f16 v169, v169, v173\n" \
"ds_read_b64_tr_b16 v[156:157], v242 offset:512\n" \
"v_mfma_f32_16x16x32_f16 v[76:79], v[144:147], v[172:175], v[76:79]\n" \
"v_pk_add_f16 v170, v170, v174\n" \
"v_pk_add_f16 v171, v171, v175\n" \
"s_waitcnt lgkmcnt(6)\n" \
"v_mfma_f32_16x16x16_bf16 v[184:187], v[184:185], v[192:193], 0\n" \
"v_mfma_f32_16x16x32_f16 v[88:91], v[148:151], v[172:175], v[88:91]\n" \
"ds_read_b128 v[164:167], v240 offset:512\n" \
"ds_read_b64_tr_b16 v[158:159], v242 offset:8704\n" \
"s_waitcnt lgkmcnt(7)\n" \
"v_mfma_f32_16x16x16_bf16 v[188:191], v[188:189], v[192:193], 0\n" \
"v_mfma_f32_16x16x32_f16 v[80:83], v[136:139], v[168:171], v[80:83]\n" \
"buffer_load_dwordx4 v232, s[36:39], s80 offen offset:3072 sc1 lds\n" \
"v_mfma_f32_16x16x32_f16 v[92:95], v[140:143], v[168:171], v[92:95]\n" \
"v_mfma_f32_16x16x32_f16 v[96:99], v[128:131], v[176:179], v[96:99]\n" \
"v_pk_add_f16 v128, v120, v152\n" \
"v_pk_add_f16 v129, v121, v153\n" \
"v_cvt_pk_f16_f32 v184, v184, v185\n" \
"v_cvt_pk_f16_f32 v185, v186, v187\n" \
"ds_read_b128 v[168:171], v240 offset:2048\n" \
"s_add_i32 m0, s45, 0x1000\n" \
"s_mov_b32 s83, 0x68000\n" \
"s_waitcnt lgkmcnt(4)\n" \
"v_mfma_f32_16x16x32_bf16 v[204:207], v[200:203], v[196:199], v[204:207]\n" \
"v_mfma_f32_16x16x32_f16 v[108:111], v[132:135], v[176:179], v[108:111]\n" \
"v_pk_add_f16 v176, v176, v180\n" \
"v_pk_add_f16 v177, v177, v181\n" \
"v_cvt_pk_f16_f32 v186, v188, v189\n" \
"v_cvt_pk_f16_f32 v187, v190, v191\n" \
"ds_read_b128 v[172:175], v240 offset:2560\n" \
"buffer_load_dwordx4 v233, s[36:39], s83 offen sc1 lds\n" \
"v_mfma_f32_16x16x32_f16 v[100:103], v[144:147], v[180:183], v[100:103]\n" \
"v_pk_add_f16 v178, v178, v182\n" \
"v_pk_add_f16 v179, v179, v183\n" \
"v_add_u32 v184, s91, v184\n" \
"v_add_u32 v185, s91, v185\n" \
"v_add_u32 v186, s91, v186\n" \
"v_mfma_f32_16x16x32_f16 v[112:115], v[148:151], v[180:183], v[112:115]\n" \
"v_pk_add_f16 v130, v122, v154\n" \
"v_pk_add_f16 v131, v123, v155\n" \
"v_add_u32 v187, s91, v187\n" \
"v_and_b32 v184, s92, v184\n" \
"v_mfma_f32_16x16x32_f16 v[104:107], v[136:139], v[176:179], v[104:107]\n" \
"s_waitcnt lgkmcnt(1)\n" \
"v_pk_add_f16 v132, v124, v156\n" \
"v_pk_add_f16 v133, v125, v157\n" \
"v_and_b32 v185, s92, v185\n" \
"v_and_b32 v186, s92, v186\n" \
"v_and_b32 v187, s92, v187\n" \
"v_mfma_f32_16x16x32_f16 v[116:119], v[140:143], v[176:179], v[116:119]\n" \
"v_pk_add_f16 v134, v126, v158\n" \
"v_pk_add_f16 v135, v127, v159\n" \
"buffer_store_dwordx4 v[184:187], v234, s[36:39], s82 offen offset:1024 sc1\n" \
"s_waitcnt vmcnt(8) lgkmcnt(0)\n" \
"s_barrier\n" \
"v_mfma_f32_16x16x32_f16 v[0:3], v[120:123], v[160:163], v[0:3]\n" \
"ds_read_b128 v[176:179], v240 offset:24576\n" \
"s_add_i32 m0, s45, 0x2000\n" \
"s_mov_b32 s83, 0x70000\n" \
"v_mfma_f32_16x16x32_f16 v[12:15], v[124:127], v[160:163], v[12:15]\n" \
"v_pk_add_f16 v160, v160, v164\n" \
"v_pk_add_f16 v161, v161, v165\n" \
"buffer_load_dwordx4 v233, s[36:39], s83 offen sc1 lds\n" \
"v_mfma_f32_16x16x32_f16 v[4:7], v[152:155], v[164:167], v[4:7]\n" \
"v_pk_add_f16 v162, v162, v166\n" \
"v_pk_add_f16 v163, v163, v167\n" \
"v_mfma_f32_16x16x32_f16 v[16:19], v[156:159], v[164:167], v[16:19]\n" \
"ds_read_b128 v[180:183], v240 offset:25088\n" \
"s_add_i32 m0, s44, 0x0\n" \
"s_add_i32 s80, s46, 0x5c00000\n" \
"v_mfma_f32_16x16x32_f16 v[8:11], v[128:131], v[160:163], v[8:11]\n" \
"buffer_load_dwordx4 v232, s[36:39], s80 offen sc1 lds\n" \
"ds_read_b64_tr_b16 v[136:137], v241 offset:16384\n" \
"v_mfma_f32_16x16x32_f16 v[20:23], v[132:135], v[160:163], v[20:23]\n" \
"ds_read_b128 v[196:199], v249 offset:2048\n" \
"ds_read_b64_tr_b16 v[200:201], v247 offset:16384\n" \
"v_mfma_f32_16x16x32_f16 v[24:27], v[120:123], v[168:171], v[24:27]\n" \
"ds_read_b128 v[160:163], v240 offset:26624\n" \
"s_add_i32 s84, s54, 0x8018000\n" \
"ds_read_b64_tr_b16 v[138:139], v241 offset:24576\n" \
"v_mfma_f32_16x16x32_f16 v[36:39], v[124:127], v[168:171], v[36:39]\n" \
"v_pk_add_f16 v168, v168, v172\n" \
"v_pk_add_f16 v169, v169, v173\n" \
"buffer_store_dword v240, v243, s[36:39], s84 offen sc1\n" \
"v_mfma_f32_16x16x32_f16 v[28:31], v[152:155], v[172:175], v[28:31]\n" \
"v_pk_add_f16 v170, v170, v174\n" \
"v_pk_add_f16 v171, v171, v175\n" \
"v_mfma_f32_16x16x32_f16 v[40:43], v[156:159], v[172:175], v[40:43]\n" \
"ds_read_b128 v[164:167], v240 offset:27136\n" \
"ds_read_b64_tr_b16 v[202:203], v247 offset:16896\n" \
"v_mfma_f32_16x16x32_f16 v[32:35], v[128:131], v[168:171], v[32:35]\n" \
"buffer_load_dwordx4 v232, s[36:39], s80 offen offset:1024 sc1 lds\n" \
"ds_read_b64_tr_b16 v[140:141], v242 offset:16384\n" \
"v_mfma_f32_16x16x32_f16 v[44:47], v[132:135], v[168:171], v[44:47]\n" \
"ds_read_b64_tr_b16 v[142:143], v242 offset:24576\n" \
"s_waitcnt lgkmcnt(9)\n" \
"v_mfma_f32_16x16x32_f16 v[48:51], v[120:123], v[176:179], v[48:51]\n" \
"ds_read_b128 v[168:171], v240 offset:28672\n" \
"ds_read_b64_tr_b16 v[144:145], v241 offset:16896\n" \
"v_mfma_f32_16x16x32_f16 v[60:63], v[124:127], v[176:179], v[60:63]\n" \
"v_pk_add_f16 v176, v176, v180\n" \
"v_pk_add_f16 v177, v177, v181\n" \
"s_waitcnt lgkmcnt(4)\n" \
"v_mfma_f32_16x16x32_bf16 v[204:207], v[200:203], v[196:199], v[204:207]\n" \
"v_mfma_f32_16x16x32_f16 v[52:55], v[152:155], v[180:183], v[52:55]\n" \
"v_pk_add_f16 v178, v178, v182\n" \
"v_pk_add_f16 v179, v179, v183\n" \
"v_mfma_f32_16x16x32_f16 v[64:67], v[156:159], v[180:183], v[64:67]\n" \
"ds_read_b128 v[172:175], v240 offset:29184\n" \
"ds_read_b64_tr_b16 v[146:147], v241 offset:25088\n" \
"v_mfma_f32_16x16x32_f16 v[56:59], v[128:131], v[176:179], v[56:59]\n" \
"buffer_load_dwordx4 v232, s[36:39], s80 offen offset:2048 sc1 lds\n" \
"ds_read_b64_tr_b16 v[148:149], v242 offset:16896\n" \
"v_mfma_f32_16x16x32_f16 v[68:71], v[132:135], v[176:179], v[68:71]\n" \
"ds_read_b128 v[196:199], v249 offset:3072\n" \
"ds_read_b64_tr_b16 v[200:201], v247 offset:20480\n" \
"v_mfma_f32_16x16x32_f16 v[72:75], v[120:123], v[160:163], v[72:75]\n" \
"ds_read_b128 v[176:179], v240 offset:20480\n" \
"ds_read_b64_tr_b16 v[202:203], v247 offset:20992\n" \
"v_mfma_f32_16x16x32_f16 v[84:87], v[124:127], v[160:163], v[84:87]\n" \
"v_pk_add_f16 v160, v160, v164\n" \
"v_pk_add_f16 v161, v161, v165\n" \
"v_mfma_f32_16x16x32_f16 v[76:79], v[152:155], v[164:167], v[76:79]\n" \
"v_pk_add_f16 v162, v162, v166\n" \
"v_pk_add_f16 v163, v163, v167\n" \
"v_mfma_f32_16x16x32_f16 v[88:91], v[156:159], v[164:167], v[88:91]\n" \
"ds_read_b128 v[180:183], v240 offset:20992\n" \
"ds_read_b64_tr_b16 v[150:151], v242 offset:25088\n" \
"v_mfma_f32_16x16x32_f16 v[80:83], v[128:131], v[160:163], v[80:83]\n" \
"buffer_load_dwordx4 v232, s[36:39], s80 offen offset:3072 sc1 lds\n" \
"v_mfma_f32_16x16x32_f16 v[92:95], v[132:135], v[160:163], v[92:95]\n" \
"s_waitcnt lgkmcnt(9)\n" \
"v_mfma_f32_16x16x32_f16 v[96:99], v[120:123], v[168:171], v[96:99]\n" \
"v_pk_add_f16 v120, v136, v144\n" \
"v_pk_add_f16 v121, v137, v145\n" \
"ds_read_b128 v[160:163], v240 offset:22528\n" \
"s_add_i32 m0, s45, 0x3000\n" \
"s_mov_b32 s83, 0x78000\n" \
"s_waitcnt lgkmcnt(3)\n" \
"v_mfma_f32_16x16x32_bf16 v[204:207], v[200:203], v[196:199], v[204:207]\n" \
"v_mfma_f32_16x16x32_f16 v[108:111], v[124:127], v[168:171], v[108:111]\n" \
"v_pk_add_f16 v168, v168, v172\n" \
"v_pk_add_f16 v169, v169, v173\n" \
"ds_read_b128 v[164:167], v240 offset:23040\n" \
"buffer_load_dwordx4 v233, s[36:39], s83 offen sc1 lds\n" \
"v_mfma_f32_16x16x32_f16 v[100:103], v[152:155], v[172:175], v[100:103]\n" \
"v_pk_add_f16 v170, v170, v174\n" \
"v_pk_add_f16 v171, v171, v175\n" \
"v_mfma_f32_16x16x32_f16 v[112:115], v[156:159], v[172:175], v[112:115]\n" \
"v_pk_add_f16 v122, v138, v146\n" \
"v_pk_add_f16 v123, v139, v147\n" \
"v_mfma_f32_16x16x32_f16 v[104:107], v[128:131], v[168:171], v[104:107]\n" \
"v_pk_add_f16 v124, v140, v148\n" \
"v_pk_add_f16 v125, v141, v149\n" \
"v_mfma_f32_16x16x32_f16 v[116:119], v[132:135], v[168:171], v[116:119]\n" \
"s_waitcnt lgkmcnt(2)\n" \
"v_pk_add_f16 v126, v142, v150\n" \
"v_pk_add_f16 v127, v143, v151\n" \
"s_waitcnt vmcnt(7) lgkmcnt(0)\n" \
"s_barrier\n" \
"v_mfma_f32_16x16x32_f16 v[0:3], v[136:139], v[176:179], v[0:3]\n" \
"ds_read_b128 v[168:171], v240 offset:45056\n" \
"s_mov_b32 s81, 0x8016000\n" \
"v_mfma_f32_16x16x32_f16 v[12:15], v[140:143], v[176:179], v[12:15]\n" \
"v_pk_add_f16 v176, v176, v180\n" \
"v_pk_add_f16 v177, v177, v181\n" \
"buffer_load_dword v253, v252, s[36:39], s81 offen sc1\n" \
"v_mfma_f32_16x16x32_f16 v[4:7], v[144:147], v[180:183], v[4:7]\n" \
"v_pk_add_f16 v178, v178, v182\n" \
"v_pk_add_f16 v179, v179, v183\n" \
"v_mfma_f32_16x16x32_f16 v[16:19], v[148:151], v[180:183], v[16:19]\n" \
"ds_read_b128 v[172:175], v240 offset:45568\n" \
"s_add_i32 m0, s44, 0x4000\n" \
"s_add_i32 s80, s46, 0x6000000\n" \
"v_mfma_f32_16x16x32_f16 v[8:11], v[120:123], v[176:179], v[8:11]\n" \
"buffer_load_dwordx4 v232, s[36:39], s80 offen sc1 lds\n" \
"ds_read_b64_tr_b16 v[128:129], v241 offset:32768\n" \
"v_mfma_f32_16x16x32_f16 v[20:23], v[124:127], v[176:179], v[20:23]\n" \
"ds_read_b128 v[196:199], v249 offset:4096\n" \
"ds_read_b64_tr_b16 v[200:201], v247 offset:0\n" \
"v_mfma_f32_16x16x32_f16 v[24:27], v[136:139], v[160:163], v[24:27]\n" \
"ds_read_b128 v[176:179], v240 offset:47104\n" \
"s_add_i32 s84, s55, 0x801a000\n" \
"ds_read_b64_tr_b16 v[130:131], v241 offset:40960\n" \
"v_mfma_f32_16x16x32_f16 v[36:39], v[140:143], v[160:163], v[36:39]\n" \
"v_pk_add_f16 v160, v160, v164\n" \
"v_pk_add_f16 v161, v161, v165\n" \
"buffer_store_dword v240, v243, s[36:39], s84 offen sc1\n" \
"v_mfma_f32_16x16x32_f16 v[28:31], v[144:147], v[164:167], v[28:31]\n" \
"v_pk_add_f16 v162, v162, v166\n" \
"v_pk_add_f16 v163, v163, v167\n" \
"v_mfma_f32_16x16x32_f16 v[40:43], v[148:151], v[164:167], v[40:43]\n" \
"ds_read_b128 v[180:183], v240 offset:47616\n" \
"ds_read_b64_tr_b16 v[202:203], v247 offset:512\n" \
"v_mfma_f32_16x16x32_f16 v[32:35], v[120:123], v[160:163], v[32:35]\n" \
"buffer_load_dwordx4 v232, s[36:39], s80 offen offset:1024 sc1 lds\n" \
"ds_read_b64_tr_b16 v[132:133], v242 offset:32768\n" \
"v_mfma_f32_16x16x32_f16 v[44:47], v[124:127], v[160:163], v[44:47]\n" \
"ds_read_b64_tr_b16 v[134:135], v242 offset:40960\n" \
"s_waitcnt lgkmcnt(9)\n" \
"v_mfma_f32_16x16x32_f16 v[48:51], v[136:139], v[168:171], v[48:51]\n" \
"ds_read_b128 v[160:163], v240 offset:49152\n" \
"ds_read_b64_tr_b16 v[152:153], v241 offset:33280\n" \
"v_mfma_f32_16x16x32_f16 v[60:63], v[140:143], v[168:171], v[60:63]\n" \
"v_pk_add_f16 v168, v168, v172\n" \
"v_pk_add_f16 v169, v169, v173\n" \
"s_waitcnt lgkmcnt(4)\n" \
"v_mfma_f32_16x16x32_bf16 v[204:207], v[200:203], v[196:199], v[204:207]\n" \
"v_mfma_f32_16x16x32_f16 v[52:55], v[144:147], v[172:175], v[52:55]\n" \
"v_pk_add_f16 v170, v170, v174\n" \
"v_pk_add_f16 v171, v171, v175\n" \
"v_mfma_f32_16x16x32_f16 v[64:67], v[148:151], v[172:175], v[64:67]\n" \
"ds_read_b128 v[164:167], v240 offset:49664\n" \
"ds_read_b64_tr_b16 v[154:155], v241 offset:41472\n" \
"v_mfma_f32_16x16x32_f16 v[56:59], v[120:123], v[168:171], v[56:59]\n" \
"buffer_load_dwordx4 v232, s[36:39], s80 offen offset:2048 sc1 lds\n" \
"ds_read_b64_tr_b16 v[156:157], v242 offset:33280\n" \
"v_mfma_f32_16x16x32_f16 v[68:71], v[124:127], v[168:171], v[68:71]\n" \
"ds_read_b128 v[196:199], v249 offset:5120\n" \
"ds_read_b64_tr_b16 v[200:201], v247 offset:4096\n" \
"v_mfma_f32_16x16x32_f16 v[72:75], v[136:139], v[176:179], v[72:75]\n" \
"ds_read_b128 v[168:171], v240 offset:40960\n" \
"ds_read_b64_tr_b16 v[202:203], v247 offset:4608\n" \
"v_mfma_f32_16x16x32_f16 v[84:87], v[140:143], v[176:179], v[84:87]\n" \
"v_pk_add_f16 v176, v176, v180\n" \
"v_pk_add_f16 v177, v177, v181\n" \
"v_mfma_f32_16x16x32_f16 v[76:79], v[144:147], v[180:183], v[76:79]\n" \
"v_pk_add_f16 v178, v178, v182\n" \
"v_pk_add_f16 v179, v179, v183\n" \
"v_mfma_f32_16x16x32_f16 v[88:91], v[148:151], v[180:183], v[88:91]\n" \
"ds_read_b128 v[172:175], v240 offset:41472\n" \
"ds_read_b64_tr_b16 v[158:159], v242 offset:41472\n" \
"v_mfma_f32_16x16x32_f16 v[80:83], v[120:123], v[176:179], v[80:83]\n" \
"buffer_load_dwordx4 v232, s[36:39], s80 offen offset:3072 sc1 lds\n" \
"v_mfma_f32_16x16x32_f16 v[92:95], v[124:127], v[176:179], v[92:95]\n" \
"s_waitcnt lgkmcnt(9)\n" \
"v_mfma_f32_16x16x32_f16 v[96:99], v[136:139], v[160:163], v[96:99]\n" \
"v_pk_add_f16 v136, v128, v152\n" \
"v_pk_add_f16 v137, v129, v153\n" \
"ds_read_b128 v[176:179], v240 offset:43008\n" \
"s_waitcnt lgkmcnt(3)\n" \
"v_mfma_f32_16x16x32_bf16 v[204:207], v[200:203], v[196:199], v[204:207]\n" \
"v_mfma_f32_16x16x32_f16 v[108:111], v[140:143], v[160:163], v[108:111]\n" \
"v_pk_add_f16 v160, v160, v164\n" \
"v_pk_add_f16 v161, v161, v165\n" \
"ds_read_b128 v[180:183], v240 offset:43520\n" \
"v_mfma_f32_16x16x32_f16 v[100:103], v[144:147], v[164:167], v[100:103]\n" \
"v_pk_add_f16 v162, v162, v166\n" \
"v_pk_add_f16 v163, v163, v167\n" \
"v_mfma_f32_16x16x32_f16 v[112:115], v[148:151], v[164:167], v[112:115]\n" \
"v_pk_add_f16 v138, v130, v154\n" \
"v_pk_add_f16 v139, v131, v155\n" \
"v_mfma_f32_16x16x32_f16 v[104:107], v[120:123], v[160:163], v[104:107]\n" \
"v_pk_add_f16 v140, v132, v156\n" \
"v_pk_add_f16 v141, v133, v157\n" \
"v_mfma_f32_16x16x32_f16 v[116:119], v[124:127], v[160:163], v[116:119]\n" \
"s_waitcnt lgkmcnt(2)\n" \
"v_pk_add_f16 v142, v134, v158\n" \
"v_pk_add_f16 v143, v135, v159\n" \
"s_waitcnt vmcnt(5) lgkmcnt(0)\n" \
"s_barrier\n" \
"v_mfma_f32_16x16x32_f16 v[0:3], v[128:131], v[168:171], v[0:3]\n" \
"ds_read_b128 v[160:163], v240 offset:4096\n" \
"v_mfma_f32_16x16x32_f16 v[12:15], v[132:135], v[168:171], v[12:15]\n" \
"v_pk_add_f16 v168, v168, v172\n" \
"v_pk_add_f16 v169, v169, v173\n" \
"v_mfma_f32_16x16x32_f16 v[4:7], v[152:155], v[172:175], v[4:7]\n" \
"v_pk_add_f16 v170, v170, v174\n" \
"v_pk_add_f16 v171, v171, v175\n" \
"v_cmp_eq_u32 vcc, 0, v253\n" \
"s_cbranch_vccnz .Ls011_%=\n" \
".Lr011_%=:\n" \
"v_mfma_f32_16x16x32_f16 v[16:19], v[156:159], v[172:175], v[16:19]\n" \
"ds_read_b128 v[164:167], v240 offset:4608\n" \
"s_add_i32 m0, s44, 0x8000\n" \
"s_add_i32 s80, s46, 0x6400000\n" \
"v_mfma_f32_16x16x32_f16 v[8:11], v[136:139], v[168:171], v[8:11]\n" \
"buffer_load_dwordx4 v232, s[36:39], s80 offen sc1 lds\n" \
"v_mfma_f32_16x16x32_f16 v[20:23], v[140:143], v[168:171], v[20:23]\n" \
"ds_read_b128 v[196:199], v249 offset:6144\n" \
"ds_read_b64_tr_b16 v[200:201], v247 offset:8192\n" \
"v_mfma_f32_16x16x32_f16 v[24:27], v[128:131], v[176:179], v[24:27]\n" \
"ds_read_b128 v[168:171], v240 offset:6144\n" \
"ds_read_b64_tr_b16 v[120:121], v241 offset:0\n" \
"ds_read_b64_tr_b16 v[122:123], v241 offset:8192\n" \
"v_mfma_f32_16x16x32_f16 v[36:39], v[132:135], v[176:179], v[36:39]\n" \
"v_pk_add_f16 v176, v176, v180\n" \
"v_pk_add_f16 v177, v177, v181\n" \
"v_mfma_f32_16x16x32_f16 v[28:31], v[152:155], v[180:183], v[28:31]\n" \
"v_pk_add_f16 v178, v178, v182\n" \
"v_pk_add_f16 v179, v179, v183\n" \
"v_mfma_f32_16x16x32_f16 v[40:43], v[156:159], v[180:183], v[40:43]\n" \
"ds_read_b128 v[172:175], v240 offset:6656\n" \
"ds_read_b64_tr_b16 v[202:203], v247 offset:8704\n" \
"v_mfma_f32_16x16x32_f16 v[32:35], v[136:139], v[176:179], v[32:35]\n" \
"buffer_load_dwordx4 v232, s[36:39], s80 offen offset:1024 sc1 lds\n" \
"ds_read_b64_tr_b16 v[124:125], v242 offset:0\n" \
"v_mfma_f32_16x16x32_f16 v[44:47], v[140:143], v[176:179], v[44:47]\n" \
"ds_read_b64_tr_b16 v[126:127], v242 offset:8192\n" \
"s_waitcnt lgkmcnt(9)\n" \
"v_mfma_f32_16x16x32_f16 v[48:51], v[128:131], v[160:163], v[48:51]\n" \
"ds_read_b128 v[176:179], v240 offset:8192\n" \
"ds_read_b64_tr_b16 v[144:145], v241 offset:512\n" \
"v_mfma_f32_16x16x32_f16 v[60:63], v[132:135], v[160:163], v[60:63]\n" \
"v_pk_add_f16 v160, v160, v164\n" \
"v_pk_add_f16 v161, v161, v165\n" \
"s_waitcnt lgkmcnt(4)\n" \
"v_mfma_f32_16x16x32_bf16 v[204:207], v[200:203], v[196:199], v[204:207]\n" \
"v_mfma_f32_16x16x32_f16 v[52:55], v[152:155], v[164:167], v[52:55]\n" \
"v_pk_add_f16 v162, v162, v166\n" \
"v_pk_add_f16 v163, v163, v167\n" \
"v_mfma_f32_16x16x32_f16 v[64:67], v[156:159], v[164:167], v[64:67]\n" \
"ds_read_b128 v[180:183], v240 offset:8704\n" \
"ds_read_b64_tr_b16 v[146:147], v241 offset:8704\n" \
"v_mfma_f32_16x16x32_f16 v[56:59], v[136:139], v[160:163], v[56:59]\n" \
"buffer_load_dwordx4 v232, s[36:39], s80 offen offset:2048 sc1 lds\n" \
"ds_read_b64_tr_b16 v[148:149], v242 offset:512\n" \
"v_mfma_f32_16x16x32_f16 v[68:71], v[140:143], v[160:163], v[68:71]\n" \
"ds_read_b128 v[196:199], v249 offset:7168\n" \
"ds_read_b64_tr_b16 v[200:201], v247 offset:12288\n" \
"v_mfma_f32_16x16x32_f16 v[72:75], v[128:131], v[168:171], v[72:75]\n" \
"ds_read_b128 v[160:163], v240 offset:0\n" \
"ds_read_b64_tr_b16 v[202:203], v247 offset:12800\n" \
"v_mfma_f32_16x16x32_f16 v[84:87], v[132:135], v[168:171], v[84:87]\n" \
"v_pk_add_f16 v168, v168, v172\n" \
"v_pk_add_f16 v169, v169, v173\n" \
"v_mfma_f32_16x16x32_f16 v[76:79], v[152:155], v[172:175], v[76:79]\n" \
"v_pk_add_f16 v170, v170, v174\n" \
"v_pk_add_f16 v171, v171, v175\n" \
"v_mfma_f32_16x16x32_f16 v[88:91], v[156:159], v[172:175], v[88:91]\n" \
"ds_read_b128 v[164:167], v240 offset:512\n" \
"ds_read_b64_tr_b16 v[150:151], v242 offset:8704\n" \
"v_mfma_f32_16x16x32_f16 v[80:83], v[136:139], v[168:171], v[80:83]\n" \
"buffer_load_dwordx4 v232, s[36:39], s80 offen offset:3072 sc1 lds\n" \
"v_mfma_f32_16x16x32_f16 v[92:95], v[140:143], v[168:171], v[92:95]\n" \
"s_waitcnt lgkmcnt(9)\n" \
"v_mfma_f32_16x16x32_f16 v[96:99], v[128:131], v[176:179], v[96:99]\n" \
"v_pk_add_f16 v128, v120, v144\n" \
"v_pk_add_f16 v129, v121, v145\n" \
"ds_read_b128 v[168:171], v240 offset:2048\n" \
"s_waitcnt lgkmcnt(3)\n" \
"v_mfma_f32_16x16x32_bf16 v[204:207], v[200:203], v[196:199], v[204:207]\n" \
"v_mfma_f32_16x16x32_f16 v[108:111], v[132:135], v[176:179], v[108:111]\n" \
"v_pk_add_f16 v176, v176, v180\n" \
"v_pk_add_f16 v177, v177, v181\n" \
"ds_read_b128 v[172:175], v240 offset:2560\n" \
"v_mfma_f32_16x16x32_f16 v[100:103], v[152:155], v[180:183], v[100:103]\n" \
"v_pk_add_f16 v178, v178, v182\n" \
"v_pk_add_f16 v179, v179, v183\n" \
"v_mfma_f32_16x16x32_f16 v[112:115], v[156:159], v[180:183], v[112:115]\n" \
"v_pk_add_f16 v130, v122, v146\n" \
"v_pk_add_f16 v131, v123, v147\n" \
"v_mfma_f32_16x16x32_f16 v[104:107], v[136:139], v[176:179], v[104:107]\n" \
"v_pk_add_f16 v132, v124, v148\n" \
"v_pk_add_f16 v133, v125, v149\n" \
"v_mfma_f32_16x16x32_f16 v[116:119], v[140:143], v[176:179], v[116:119]\n" \
"s_waitcnt lgkmcnt(2)\n" \
"v_pk_add_f16 v134, v126, v150\n" \
"v_pk_add_f16 v135, v127, v151\n" \
"s_waitcnt vmcnt(4) lgkmcnt(0)\n" \
"s_barrier\n" \
"v_mfma_f32_16x16x32_f16 v[0:3], v[120:123], v[160:163], v[0:3]\n" \
"ds_read_b128 v[176:179], v240 offset:24576\n" \
"s_mov_b32 s81, 0x8018000\n" \
"v_mfma_f32_16x16x32_f16 v[12:15], v[124:127], v[160:163], v[12:15]\n" \
"v_pk_add_f16 v160, v160, v164\n" \
"v_pk_add_f16 v161, v161, v165\n" \
"buffer_load_dword v253, v252, s[36:39], s81 offen sc1\n" \
"v_mfma_f32_16x16x32_f16 v[4:7], v[144:147], v[164:167], v[4:7]\n" \
"v_pk_add_f16 v162, v162, v166\n" \
"v_pk_add_f16 v163, v163, v167\n" \
"v_mfma_f32_16x16x32_f16 v[16:19], v[148:151], v[164:167], v[16:19]\n" \
"ds_read_b128 v[180:183], v240 offset:25088\n" \
"s_add_i32 m0, s44, 0x0\n" \
"s_add_i32 s80, s46, 0x6800000\n" \
"v_mfma_f32_16x16x32_f16 v[8:11], v[128:131], v[160:163], v[8:11]\n" \
"buffer_load_dwordx4 v232, s[36:39], s80 offen sc1 lds\n" \
"v_mfma_f32_16x16x32_f16 v[20:23], v[132:135], v[160:163], v[20:23]\n" \
"ds_write_b128 v248, v[204:207]\n" \
"ds_read_b64_tr_b16 v[136:137], v241 offset:16384\n" \
"v_mfma_f32_16x16x32_f16 v[24:27], v[120:123], v[168:171], v[24:27]\n" \
"ds_read_b128 v[160:163], v240 offset:26624\n" \
"ds_read_b64_tr_b16 v[138:139], v241 offset:24576\n" \
"v_mfma_f32_16x16x32_f16 v[36:39], v[124:127], v[168:171], v[36:39]\n" \
"v_pk_add_f16 v168, v168, v172\n" \
"v_pk_add_f16 v169, v169, v173\n" \
"v_mfma_f32_16x16x32_f16 v[28:31], v[144:147], v[172:175], v[28:31]\n" \
"v_pk_add_f16 v170, v170, v174\n" \
"v_pk_add_f16 v171, v171, v175\n" \
"v_mfma_f32_16x16x32_f16 v[40:43], v[148:151], v[172:175], v[40:43]\n" \
"ds_read_b128 v[164:167], v240 offset:27136\n" \
"ds_read_b64_tr_b16 v[140:141], v242 offset:16384\n" \
"v_mfma_f32_16x16x32_f16 v[32:35], v[128:131], v[168:171], v[32:35]\n" \
"buffer_load_dwordx4 v232, s[36:39], s80 offen offset:1024 sc1 lds\n" \
"v_mfma_f32_16x16x32_f16 v[44:47], v[132:135], v[168:171], v[44:47]\n" \
"ds_read_b64_tr_b16 v[142:143], v242 offset:24576\n" \
"s_waitcnt lgkmcnt(7)\n" \
"v_mfma_f32_16x16x32_f16 v[48:51], v[120:123], v[176:179], v[48:51]\n" \
"ds_read_b128 v[168:171], v240 offset:28672\n" \
"ds_read_b64_tr_b16 v[152:153], v241 offset:16896\n" \
"v_mfma_f32_16x16x32_f16 v[60:63], v[124:127], v[176:179], v[60:63]\n" \
"v_pk_add_f16 v176, v176, v180\n" \
"v_pk_add_f16 v177, v177, v181\n" \
"v_mfma_f32_16x16x32_f16 v[52:55], v[144:147], v[180:183], v[52:55]\n" \
"v_pk_add_f16 v178, v178, v182\n" \
"v_pk_add_f16 v179, v179, v183\n" \
"v_mfma_f32_16x16x32_f16 v[64:67], v[148:151], v[180:183], v[64:67]\n" \
"ds_read_b128 v[172:175], v240 offset:29184\n" \
"v_mfma_f32_16x16x32_f16 v[56:59], v[128:131], v[176:179], v[56:59]\n" \
"buffer_load_dwordx4 v232, s[36:39], s80 offen offset:2048 sc1 lds\n" \
"v_mfma_f32_16x16x32_f16 v[68:71], v[132:135], v[176:179], v[68:71]\n" \
"ds_read_b64_tr_b16 v[154:155], v241 offset:25088\n" \
"ds_read_b64_tr_b16 v[156:157], v242 offset:16896\n" \
"s_waitcnt lgkmcnt(8)\n" \
"v_mfma_f32_16x16x32_f16 v[72:75], v[120:123], v[160:163], v[72:75]\n" \
"ds_read_b128 v[176:179], v240 offset:20480\n" \
"ds_read_b64_tr_b16 v[158:159], v242 offset:25088\n" \
"v_mfma_f32_16x16x32_f16 v[84:87], v[124:127], v[160:163], v[84:87]\n" \
"s_waitcnt lgkmcnt(8)\n" \
"v_pk_add_f16 v160, v160, v164\n" \
"v_pk_add_f16 v161, v161, v165\n" \
"v_mfma_f32_16x16x32_f16 v[76:79], v[144:147], v[164:167], v[76:79]\n" \
"v_pk_add_f16 v162, v162, v166\n" \
"v_pk_add_f16 v163, v163, v167\n" \
"v_mfma_f32_16x16x32_f16 v[88:91], v[148:151], v[164:167], v[88:91]\n" \
"ds_read_b128 v[180:183], v240 offset:20992\n" \
"v_mfma_f32_16x16x32_f16 v[80:83], v[128:131], v[160:163], v[80:83]\n" \
"buffer_load_dwordx4 v232, s[36:39], s80 offen offset:3072 sc1 lds\n" \
"v_mfma_f32_16x16x32_f16 v[92:95], v[132:135], v[160:163], v[92:95]\n" \
"s_waitcnt lgkmcnt(6)\n" \
"v_mfma_f32_16x16x32_f16 v[96:99], v[120:123], v[168:171], v[96:99]\n" \
"v_pk_add_f16 v120, v136, v152\n" \
"v_pk_add_f16 v121, v137, v153\n" \
"ds_read_b128 v[160:163], v240 offset:22528\n" \
"v_mfma_f32_16x16x32_f16 v[108:111], v[124:127], v[168:171], v[108:111]\n" \
"s_waitcnt lgkmcnt(5)\n" \
"v_pk_add_f16 v168, v168, v172\n" \
"v_pk_add_f16 v169, v169, v173\n" \
"ds_read_b128 v[164:167], v240 offset:23040\n" \
"v_mfma_f32_16x16x32_f16 v[100:103], v[144:147], v[172:175], v[100:103]\n" \
"v_pk_add_f16 v170, v170, v174\n" \
"v_pk_add_f16 v171, v171, v175\n" \
"v_mfma_f32_16x16x32_f16 v[112:115], v[148:151], v[172:175], v[112:115]\n" \
"v_pk_add_f16 v122, v138, v154\n" \
"v_pk_add_f16 v123, v139, v155\n" \
"v_mfma_f32_16x16x32_f16 v[104:107], v[128:131], v[168:171], v[104:107]\n" \
"s_waitcnt lgkmcnt(3)\n" \
"v_pk_add_f16 v124, v140, v156\n" \
"v_pk_add_f16 v125, v141, v157\n" \
"v_mfma_f32_16x16x32_f16 v[116:119], v[132:135], v[168:171], v[116:119]\n" \
"v_pk_add_f16 v126, v142, v158\n" \
"v_pk_add_f16 v127, v143, v159\n" \
".Lcf_t:\n" \
"s_waitcnt vmcnt(4) lgkmcnt(0)\n" \
"s_barrier\n" \
"v_mfma_f32_16x16x32_f16 v[0:3], v[136:139], v[176:179], v[0:3]\n" \
"ds_read_b128 v[168:171], v240 offset:45056\n" \
"v_mfma_f32_16x16x32_f16 v[12:15], v[140:143], v[176:179], v[12:15]\n" \
"v_pk_add_f16 v176, v176, v180\n" \
"v_pk_add_f16 v177, v177, v181\n" \
"v_mfma_f32_16x16x32_f16 v[4:7], v[152:155], v[180:183], v[4:7]\n" \
"v_pk_add_f16 v178, v178, v182\n" \
"v_pk_add_f16 v179, v179, v183\n" \
"v_cmp_eq_u32 vcc, 0, v253\n" \
"s_cbranch_vccnz .Ls012_%=\n" \
".Lr012_%=:\n" \
"s_mov_b32 s81, 0x801a000\n" \
"v_mfma_f32_16x16x32_f16 v[16:19], v[156:159], v[180:183], v[16:19]\n" \
"ds_read_b128 v[172:175], v240 offset:45568\n" \
"buffer_load_dword v253, v252, s[36:39], s81 offen sc1\n" \
"s_add_i32 m0, s44, 0x4000\n" \
"s_add_i32 s80, s46, 0x6c00000\n" \
"v_mfma_f32_16x16x32_f16 v[8:11], v[120:123], v[176:179], v[8:11]\n" \
"buffer_load_dwordx4 v232, s[36:39], s80 offen sc1 lds\n" \
"v_mfma_f32_16x16x32_f16 v[20:23], v[124:127], v[176:179], v[20:23]\n" \
"ds_read_b64_tr_b16 v[128:129], v241 offset:32768\n" \
"ds_read_b64_tr_b16 v[130:131], v241 offset:40960\n" \
"v_mfma_f32_16x16x32_f16 v[24:27], v[136:139], v[160:163], v[24:27]\n" \
"ds_read_b128 v[176:179], v240 offset:47104\n" \
"v_mfma_f32_16x16x32_f16 v[36:39], v[140:143], v[160:163], v[36:39]\n" \
"v_pk_add_f16 v160, v160, v164\n" \
"v_pk_add_f16 v161, v161, v165\n" \
"v_mfma_f32_16x16x32_f16 v[28:31], v[152:155], v[164:167], v[28:31]\n" \
"v_pk_add_f16 v162, v162, v166\n" \
"v_pk_add_f16 v163, v163, v167\n" \
"v_mfma_f32_16x16x32_f16 v[40:43], v[156:159], v[164:167], v[40:43]\n" \
"ds_read_b128 v[180:183], v240 offset:47616\n" \
"ds_read_b64_tr_b16 v[132:133], v242 offset:32768\n" \
"v_mfma_f32_16x16x32_f16 v[32:35], v[120:123], v[160:163], v[32:35]\n" \
"buffer_load_dwordx4 v232, s[36:39], s80 offen offset:1024 sc1 lds\n" \
"v_mfma_f32_16x16x32_f16 v[44:47], v[124:127], v[160:163], v[44:47]\n" \
"ds_read_b64_tr_b16 v[134:135], v242 offset:40960\n" \
"s_waitcnt lgkmcnt(6)\n" \
"v_mfma_f32_16x16x32_f16 v[48:51], v[136:139], v[168:171], v[48:51]\n" \
"ds_read_b128 v[160:163], v240 offset:49152\n" \
"ds_read_b64_tr_b16 v[144:145], v241 offset:33280\n" \
"v_mfma_f32_16x16x32_f16 v[60:63], v[140:143], v[168:171], v[60:63]\n" \
"v_pk_add_f16 v168, v168, v172\n" \
"v_pk_add_f16 v169, v169, v173\n" \
"v_mfma_f32_16x16x32_f16 v[52:55], v[152:155], v[172:175], v[52:55]\n" \
"v_pk_add_f16 v170, v170, v174\n" \
"v_pk_add_f16 v171, v171, v175\n" \
"v_mfma_f32_16x16x32_f16 v[64:67], v[156:159], v[172:175], v[64:67]\n" \
"ds_read_b128 v[164:167], v240 offset:49664\n" \
"v_mfma_f32_16x16x32_f16 v[56:59], v[120:123], v[168:171], v[56:59]\n" \
"buffer_load_dwordx4 v232, s[36:39], s80 offen offset:2048 sc1 lds\n" \
"v_mfma_f32_16x16x32_f16 v[68:71], v[124:127], v[168:171], v[68:71]\n" \
"ds_read_b64_tr_b16 v[146:147], v241 offset:41472\n" \
"ds_read_b64_tr_b16 v[148:149], v242 offset:33280\n" \
"s_waitcnt lgkmcnt(8)\n" \
"v_mfma_f32_16x16x32_f16 v[72:75], v[136:139], v[176:179], v[72:75]\n" \
"ds_read_b128 v[168:171], v240 offset:40960\n" \
"ds_read_b64_tr_b16 v[150:151], v242 offset:41472\n" \
"v_mfma_f32_16x16x32_f16 v[84:87], v[140:143], v[176:179], v[84:87]\n" \
"s_waitcnt lgkmcnt(8)\n" \
"v_pk_add_f16 v176, v176, v180\n" \
"v_pk_add_f16 v177, v177, v181\n" \
"v_mfma_f32_16x16x32_f16 v[76:79], v[152:155], v[180:183], v[76:79]\n" \
"v_pk_add_f16 v178, v178, v182\n" \
"v_pk_add_f16 v179, v179, v183\n" \
"v_mfma_f32_16x16x32_f16 v[88:91], v[156:159], v[180:183], v[88:91]\n" \
"ds_read_b128 v[172:175], v240 offset:41472\n" \
"v_mfma_f32_16x16x32_f16 v[80:83], v[120:123], v[176:179], v[80:83]\n" \
"buffer_load_dwordx4 v232, s[36:39], s80 offen offset:3072 sc1 lds\n" \
"v_mfma_f32_16x16x32_f16 v[92:95], v[124:127], v[176:179], v[92:95]\n" \
"s_waitcnt lgkmcnt(6)\n" \
"v_mfma_f32_16x16x32_f16 v[96:99], v[136:139], v[160:163], v[96:99]\n" \
"v_pk_add_f16 v136, v128, v144\n" \
"v_pk_add_f16 v137, v129, v145\n" \
"ds_read_b128 v[176:179], v240 offset:43008\n" \
"v_mfma_f32_16x16x32_f16 v[108:111], v[140:143], v[160:163], v[108:111]\n" \
"s_waitcnt lgkmcnt(5)\n" \
"v_pk_add_f16 v160, v160, v164\n" \
"v_pk_add_f16 v161, v161, v165\n" \
"ds_read_b128 v[180:183], v240 offset:43520\n" \
"v_mfma_f32_16x16x32_f16 v[100:103], v[152:155], v[164:167], v[100:103]\n" \
"v_pk_add_f16 v162, v162, v166\n" \
"v_pk_add_f16 v163, v163, v167\n" \
"v_mfma_f32_16x16x32_f16 v[112:115], v[156:159], v[164:167], v[112:115]\n" \
"v_pk_add_f16 v138, v130, v146\n" \
"v_pk_add_f16 v139, v131, v147\n" \
"v_mfma_f32_16x16x32_f16 v[104:107], v[120:123], v[160:163], v[104:107]\n" \
"s_waitcnt lgkmcnt(3)\n" \
"v_pk_add_f16 v140, v132, v148\n" \
"v_pk_add_f16 v141, v133, v149\n" \
"v_mfma_f32_16x16x32_f16 v[116:119], v[124:127], v[160:163], v[116:119]\n" \
"v_pk_add_f16 v142, v134, v150\n" \
"v_pk_add_f16 v143, v135, v151\n" \
"s_waitcnt vmcnt(4) lgkmcnt(0)\n" \
"s_barrier\n" \
"v_mfma_f32_16x16x32_f16 v[0:3], v[128:131], v[168:171], v[0:3]\n" \
"ds_read_b128 v[160:163], v240 offset:4096\n" \
"v_mfma_f32_16x16x32_f16 v[12:15], v[132:135], v[168:171], v[12:15]\n" \
"v_pk_add_f16 v168, v168, v172\n" \
"v_pk_add_f16 v169, v169, v173\n" \
"s_add_i32 m0, s44, 0x8000\n" \
"s_add_i32 s80, s46, 0x7000000\n" \
"v_mfma_f32_16x16x32_f16 v[4:7], v[144:147], v[172:175], v[4:7]\n" \
"v_pk_add_f16 v170, v170, v174\n" \
"v_pk_add_f16 v171, v171, v175\n" \
"buffer_load_dwordx4 v232, s[36:39], s80 offen sc1 lds\n" \
"v_mfma_f32_16x16x32_f16 v[16:19], v[148:151], v[172:175], v[16:19]\n" \
"ds_read_b128 v[164:167], v240 offset:4608\n" \
"v_mfma_f32_16x16x32_f16 v[8:11], v[136:139], v[168:171], v[8:11]\n" \
"buffer_load_dwordx4 v232, s[36:39], s80 offen offset:1024 sc1 lds\n" \
"v_mfma_f32_16x16x32_f16 v[20:23], v[140:143], v[168:171], v[20:23]\n" \
"ds_read_b64_tr_b16 v[120:121], v241 offset:0\n" \
"ds_read_b64_tr_b16 v[122:123], v241 offset:8192\n" \
"v_mfma_f32_16x16x32_f16 v[24:27], v[128:131], v[176:179], v[24:27]\n" \
"ds_read_b128 v[168:171], v240 offset:6144\n" \
"buffer_load_dwordx4 v232, s[36:39], s80 offen offset:2048 sc1 lds\n" \
"v_mfma_f32_16x16x32_f16 v[36:39], v[132:135], v[176:179], v[36:39]\n" \
"v_pk_add_f16 v176, v176, v180\n" \
"v_pk_add_f16 v177, v177, v181\n" \
"ds_read_b64_tr_b16 v[124:125], v242 offset:0\n" \
"v_mfma_f32_16x16x32_f16 v[28:31], v[144:147], v[180:183], v[28:31]\n" \
"v_pk_add_f16 v178, v178, v182\n" \
"v_pk_add_f16 v179, v179, v183\n" \
"v_mfma_f32_16x16x32_f16 v[40:43], v[148:151], v[180:183], v[40:43]\n" \
"ds_read_b128 v[172:175], v240 offset:6656\n" \
"buffer_load_dwordx4 v232, s[36:39], s80 offen offset:3072 sc1 lds\n" \
"v_mfma_f32_16x16x32_f16 v[32:35], v[136:139], v[176:179], v[32:35]\n" \
"v_cmp_eq_u32 vcc, 0, v253\n" \
"s_cbranch_vccnz .Ls013_%=\n" \
".Lr013_%=:\n" \
"v_mfma_f32_16x16x32_f16 v[44:47], v[140:143], v[176:179], v[44:47]\n" \
"s_add_i32 m0, s44, 0x1b000\n" \
"s_add_i32 s80, s46, 0x7400000\n" \
"ds_read_b64_tr_b16 v[126:127], v242 offset:8192\n" \
"s_waitcnt lgkmcnt(6)\n" \
"v_mfma_f32_16x16x32_f16 v[48:51], v[128:131], v[160:163], v[48:51]\n" \
"ds_read_b128 v[176:179], v240 offset:8192\n" \
"buffer_load_dwordx4 v232, s[36:39], s80 offen sc1 lds\n" \
"v_mfma_f32_16x16x32_f16 v[60:63], v[132:135], v[160:163], v[60:63]\n" \
"v_pk_add_f16 v160, v160, v164\n" \
"v_pk_add_f16 v161, v161, v165\n" \
"v_mfma_f32_16x16x32_f16 v[52:55], v[144:147], v[164:167], v[52:55]\n" \
"v_pk_add_f16 v162, v162, v166\n" \
"v_pk_add_f16 v163, v163, v167\n" \
"buffer_load_dwordx4 v232, s[36:39], s80 offen offset:1024 sc1 lds\n" \
"v_mfma_f32_16x16x32_f16 v[64:67], v[148:151], v[164:167], v[64:67]\n" \
"ds_read_b128 v[180:183], v240 offset:8704\n" \
"ds_read_b64_tr_b16 v[152:153], v241 offset:512\n" \
"v_mfma_f32_16x16x32_f16 v[56:59], v[136:139], v[160:163], v[56:59]\n" \
"buffer_load_dwordx4 v232, s[36:39], s80 offen offset:2048 sc1 lds\n" \
"v_mfma_f32_16x16x32_f16 v[68:71], v[140:143], v[160:163], v[68:71]\n" \
"ds_read_b64_tr_b16 v[154:155], v241 offset:8704\n" \
"ds_read_b64_tr_b16 v[156:157], v242 offset:512\n" \
"s_waitcnt lgkmcnt(6)\n" \
"v_mfma_f32_16x16x32_f16 v[72:75], v[128:131], v[168:171], v[72:75]\n" \
"ds_read_b128 v[160:163], v240 offset:0\n" \
"buffer_load_dwordx4 v232, s[36:39], s80 offen offset:3072 sc1 lds\n" \
"v_mfma_f32_16x16x32_f16 v[84:87], v[132:135], v[168:171], v[84:87]\n" \
"v_pk_add_f16 v168, v168, v172\n" \
"v_pk_add_f16 v169, v169, v173\n" \
"ds_read_b64_tr_b16 v[158:159], v242 offset:8704\n" \
"v_mfma_f32_16x16x32_f16 v[76:79], v[144:147], v[172:175], v[76:79]\n" \
"v_pk_add_f16 v170, v170, v174\n" \
"v_pk_add_f16 v171, v171, v175\n" \
"s_add_i32 m0, s44, 0x1f000\n" \
"s_add_i32 s80, s46, 0x7800000\n" \
"v_mfma_f32_16x16x32_f16 v[88:91], v[148:151], v[172:175], v[88:91]\n" \
"ds_read_b128 v[164:167], v240 offset:512\n" \
"buffer_load_dwordx4 v232, s[36:39], s80 offen sc1 lds\n" \
"v_mfma_f32_16x16x32_f16 v[80:83], v[136:139], v[168:171], v[80:83]\n" \
"v_mfma_f32_16x16x32_f16 v[92:95], v[140:143], v[168:171], v[92:95]\n" \
"buffer_load_dwordx4 v232, s[36:39], s80 offen offset:1024 sc1 lds\n" \
"s_waitcnt lgkmcnt(5)\n" \
"v_mfma_f32_16x16x32_f16 v[96:99], v[128:131], v[176:179], v[96:99]\n" \
"v_pk_add_f16 v128, v120, v152\n" \
"v_pk_add_f16 v129, v121, v153\n" \
"ds_read_b128 v[168:171], v240 offset:2048\n" \
"v_mfma_f32_16x16x32_f16 v[108:111], v[132:135], v[176:179], v[108:111]\n" \
"v_pk_add_f16 v176, v176, v180\n" \
"v_pk_add_f16 v177, v177, v181\n" \
"ds_read_b128 v[172:175], v240 offset:2560\n" \
"buffer_load_dwordx4 v232, s[36:39], s80 offen offset:2048 sc1 lds\n" \
"v_mfma_f32_16x16x32_f16 v[100:103], v[144:147], v[180:183], v[100:103]\n" \
"v_pk_add_f16 v178, v178, v182\n" \
"v_pk_add_f16 v179, v179, v183\n" \
"v_mfma_f32_16x16x32_f16 v[112:115], v[148:151], v[180:183], v[112:115]\n" \
"s_waitcnt lgkmcnt(3)\n" \
"v_pk_add_f16 v130, v122, v154\n" \
"v_pk_add_f16 v131, v123, v155\n" \
"buffer_load_dwordx4 v232, s[36:39], s80 offen offset:3072 sc1 lds\n" \
"v_mfma_f32_16x16x32_f16 v[104:107], v[136:139], v[176:179], v[104:107]\n" \
"v_pk_add_f16 v132, v124, v156\n" \
"v_pk_add_f16 v133, v125, v157\n" \
"v_mfma_f32_16x16x32_f16 v[116:119], v[140:143], v[176:179], v[116:119]\n" \
"v_pk_add_f16 v134, v126, v158\n" \
"v_pk_add_f16 v135, v127, v159\n" \
"s_waitcnt vmcnt(12) lgkmcnt(0)\n" \
"s_barrier\n" \
"v_mfma_f32_16x16x32_f16 v[0:3], v[120:123], v[160:163], v[0:3]\n" \
"ds_read_b128 v[176:179], v240 offset:24576\n" \
"v_mfma_f32_16x16x32_f16 v[12:15], v[124:127], v[160:163], v[12:15]\n" \
"v_pk_add_f16 v160, v160, v164\n" \
"v_pk_add_f16 v161, v161, v165\n" \
"s_lshr_b32 m0, s44, 1\n" \
"v_mfma_f32_16x16x32_f16 v[4:7], v[152:155], v[164:167], v[4:7]\n" \
"v_pk_add_f16 v162, v162, v166\n" \
"v_pk_add_f16 v163, v163, v167\n" \
"v_add_u32 v252, m0, v244\n" \
"v_mfma_f32_16x16x32_f16 v[16:19], v[156:159], v[164:167], v[16:19]\n" \
"ds_read_b128 v[180:183], v240 offset:25088\n" \
"v_mfma_f32_16x16x32_f16 v[8:11], v[128:131], v[160:163], v[8:11]\n" \
"global_load_lds_dwordx4 v252, s[76:77]\n" \
"v_mfma_f32_16x16x32_f16 v[20:23], v[132:135], v[160:163], v[20:23]\n" \
"ds_read_b64_tr_b16 v[136:137], v241 offset:16384\n" \
"ds_read_b64_tr_b16 v[138:139], v241 offset:24576\n" \
"v_mfma_f32_16x16x32_f16 v[24:27], v[120:123], v[168:171], v[24:27]\n" \
"ds_read_b128 v[160:163], v240 offset:26624\n" \
"v_mfma_f32_16x16x32_f16 v[36:39], v[124:127], v[168:171], v[36:39]\n" \
"v_pk_add_f16 v168, v168, v172\n" \
"v_pk_add_f16 v169, v169, v173\n" \
"v_mfma_f32_16x16x32_f16 v[28:31], v[152:155], v[172:175], v[28:31]\n" \
"v_pk_add_f16 v170, v170, v174\n" \
"v_pk_add_f16 v171, v171, v175\n" \
"v_mfma_f32_16x16x32_f16 v[40:43], v[156:159], v[172:175], v[40:43]\n" \
"ds_read_b128 v[164:167], v240 offset:27136\n" \
"ds_read_b64_tr_b16 v[140:141], v242 offset:16384\n" \
"v_mfma_f32_16x16x32_f16 v[32:35], v[128:131], v[168:171], v[32:35]\n" \
"global_load_lds_dwordx4 v252, s[76:77] offset:1024\n" \
"v_mfma_f32_16x16x32_f16 v[44:47], v[132:135], v[168:171], v[44:47]\n" \
"ds_read_b64_tr_b16 v[142:143], v242 offset:24576\n" \
"s_waitcnt lgkmcnt(7)\n" \
"v_mfma_f32_16x16x32_f16 v[48:51], v[120:123], v[176:179], v[48:51]\n" \
"ds_read_b128 v[168:171], v240 offset:28672\n" \
"ds_read_b64_tr_b16 v[144:145], v241 offset:16896\n" \
"v_mfma_f32_16x16x32_f16 v[60:63], v[124:127], v[176:179], v[60:63]\n" \
"s_waitcnt lgkmcnt(8)\n" \
"v_pk_add_f16 v176, v176, v180\n" \
"v_pk_add_f16 v177, v177, v181\n" \
"v_mfma_f32_16x16x32_f16 v[52:55], v[152:155], v[180:183], v[52:55]\n" \
"v_pk_add_f16 v178, v178, v182\n" \
"v_pk_add_f16 v179, v179, v183\n" \
"v_mov_b32 v248, v194\n" \
"v_mfma_f32_16x16x32_f16 v[64:67], v[156:159], v[180:183], v[64:67]\n" \
"v_mov_b32 v249, v195\n" \
"ds_read_b128 v[172:175], v240 offset:29184\n" \
"v_mfma_f32_16x16x32_f16 v[56:59], v[128:131], v[176:179], v[56:59]\n" \
"ds_read_b64_tr_b16 v[146:147], v241 offset:25088\n" \
"v_mfma_f32_16x16x32_f16 v[68:71], v[132:135], v[176:179], v[68:71]\n" \
"ds_read_b64_tr_b16 v[148:149], v242 offset:16896\n" \
"s_waitcnt lgkmcnt(6)\n" \
"v_mfma_f32_16x16x32_f16 v[72:75], v[120:123], v[160:163], v[72:75]\n" \
"ds_read_b128 v[176:179], v240 offset:20480\n" \
"ds_read_b64_tr_b16 v[150:151], v242 offset:25088\n" \
"v_mfma_f32_16x16x32_f16 v[84:87], v[124:127], v[160:163], v[84:87]\n" \
"v_pk_add_f16 v160, v160, v164\n" \
"v_pk_add_f16 v161, v161, v165\n" \
"v_mfma_f32_16x16x32_f16 v[76:79], v[152:155], v[164:167], v[76:79]\n" \
"v_pk_add_f16 v162, v162, v166\n" \
"v_pk_add_f16 v163, v163, v167\n" \
"v_add_u32 v243, 0x1b000, v241\n" \
"v_mfma_f32_16x16x32_f16 v[88:91], v[156:159], v[164:167], v[88:91]\n" \
"v_xor_b32 v244, 32, v243\n" \
"ds_read_b128 v[180:183], v240 offset:20992\n" \
"v_mfma_f32_16x16x32_f16 v[80:83], v[128:131], v[160:163], v[80:83]\n" \
"v_mfma_f32_16x16x32_f16 v[92:95], v[132:135], v[160:163], v[92:95]\n" \
"v_add_u32 v246, 0x17000, v240\n" \
"s_waitcnt lgkmcnt(6)\n" \
"v_mfma_f32_16x16x32_f16 v[96:99], v[120:123], v[168:171], v[96:99]\n" \
"v_pk_add_f16 v120, v136, v144\n" \
"v_pk_add_f16 v121, v137, v145\n" \
"ds_read_b128 v[160:163], v240 offset:22528\n" \
"v_mfma_f32_16x16x32_f16 v[108:111], v[124:127], v[168:171], v[108:111]\n" \
"s_waitcnt lgkmcnt(4)\n" \
"v_pk_add_f16 v168, v168, v172\n" \
"v_pk_add_f16 v169, v169, v173\n" \
"ds_read_b128 v[164:167], v240 offset:23040\n" \
"v_mfma_f32_16x16x32_f16 v[100:103], v[152:155], v[172:175], v[100:103]\n" \
"v_pk_add_f16 v170, v170, v174\n" \
"v_pk_add_f16 v171, v171, v175\n" \
"v_mfma_f32_16x16x32_f16 v[112:115], v[156:159], v[172:175], v[112:115]\n" \
"v_pk_add_f16 v122, v138, v146\n" \
"v_pk_add_f16 v123, v139, v147\n" \
"v_mfma_f32_16x16x32_f16 v[104:107], v[128:131], v[168:171], v[104:107]\n" \
"v_pk_add_f16 v124, v140, v148\n" \
"v_pk_add_f16 v125, v141, v149\n" \
"v_mfma_f32_16x16x32_f16 v[116:119], v[132:135], v[168:171], v[116:119]\n" \
"s_waitcnt lgkmcnt(2)\n" \
"v_pk_add_f16 v126, v142, v150\n" \
"v_pk_add_f16 v127, v143, v151\n" \
"s_waitcnt vmcnt(2) lgkmcnt(0)\n" \
"s_barrier\n" \
"v_mfma_f32_16x16x32_f16 v[0:3], v[136:139], v[176:179], v[0:3]\n" \
"ds_read_b128 v[168:171], v240 offset:40960\n" \
"ds_read_b64_tr_b16 v[128:129], v241 offset:32768\n" \
"ds_read_b64_tr_b16 v[130:131], v241 offset:40960\n" \
"v_mfma_f32_16x16x32_f16 v[12:15], v[140:143], v[176:179], v[12:15]\n" \
"v_pk_add_f16 v176, v176, v180\n" \
"v_pk_add_f16 v177, v177, v181\n" \
"ds_read_b64_tr_b16 v[132:133], v242 offset:32768\n" \
"v_mfma_f32_16x16x32_f16 v[4:7], v[144:147], v[180:183], v[4:7]\n" \
"v_pk_add_f16 v178, v178, v182\n" \
"v_pk_add_f16 v179, v179, v183\n" \
"v_mfma_f32_16x16x32_f16 v[16:19], v[148:151], v[180:183], v[16:19]\n" \
"ds_read_b128 v[172:175], v240 offset:41472\n" \
"ds_read_b64_tr_b16 v[134:135], v242 offset:40960\n" \
"ds_read_b64_tr_b16 v[152:153], v241 offset:33280\n" \
"v_mfma_f32_16x16x32_f16 v[8:11], v[120:123], v[176:179], v[8:11]\n" \
"ds_read_b64_tr_b16 v[154:155], v241 offset:41472\n" \
"v_mfma_f32_16x16x32_f16 v[20:23], v[124:127], v[176:179], v[20:23]\n" \
"ds_read_b64_tr_b16 v[156:157], v242 offset:33280\n" \
"ds_read_b64_tr_b16 v[158:159], v242 offset:41472\n" \
"v_mfma_f32_16x16x32_f16 v[24:27], v[136:139], v[160:163], v[24:27]\n" \
"ds_read_b128 v[176:179], v246 offset:0\n" \
"ds_read_b64_tr_b16 v[184:185], v243 offset:0\n" \
"ds_read_b64_tr_b16 v[186:187], v243 offset:8192\n" \
"v_mfma_f32_16x16x32_f16 v[36:39], v[140:143], v[160:163], v[36:39]\n" \
"v_pk_add_f16 v160, v160, v164\n" \
"v_pk_add_f16 v161, v161, v165\n" \
"ds_read_b64_tr_b16 v[188:189], v244 offset:0\n" \
"v_mfma_f32_16x16x32_f16 v[28:31], v[144:147], v[164:167], v[28:31]\n" \
"v_pk_add_f16 v162, v162, v166\n" \
"v_pk_add_f16 v163, v163, v167\n" \
"v_mfma_f32_16x16x32_f16 v[40:43], v[148:151], v[164:167], v[40:43]\n" \
"ds_read_b128 v[180:183], v246 offset:512\n" \
"ds_read_b64_tr_b16 v[190:191], v244 offset:8192\n" \
"ds_read_b64_tr_b16 v[192:193], v243 offset:512\n" \
"v_mfma_f32_16x16x32_f16 v[32:35], v[120:123], v[160:163], v[32:35]\n" \
"ds_read_b64_tr_b16 v[194:195], v243 offset:8704\n" \
"v_mfma_f32_16x16x32_f16 v[44:47], v[124:127], v[160:163], v[44:47]\n" \
"s_waitcnt lgkmcnt(11)\n" \
"v_pk_add_f16 v232, v128, v152\n" \
"v_pk_add_f16 v233, v129, v153\n" \
"ds_read_b64_tr_b16 v[196:197], v244 offset:512\n" \
"ds_read_b64_tr_b16 v[198:199], v244 offset:8704\n" \
"v_mfma_f32_16x16x32_f16 v[0:3], v[128:131], v[168:171], v[0:3]\n" \
"s_waitcnt lgkmcnt(10)\n" \
"v_pk_add_f16 v234, v130, v154\n" \
"ds_read_b128 v[160:163], v246 offset:8192\n" \
"ds_read_b64_tr_b16 v[208:209], v243 offset:16384\n" \
"ds_read_b64_tr_b16 v[210:211], v243 offset:24576\n" \
"v_mfma_f32_16x16x32_f16 v[12:15], v[132:135], v[168:171], v[12:15]\n" \
"v_pk_add_f16 v168, v168, v172\n" \
"v_pk_add_f16 v169, v169, v173\n" \
"v_pk_add_f16 v235, v131, v155\n" \
"v_pk_add_f16 v236, v132, v156\n" \
"ds_read_b64_tr_b16 v[212:213], v244 offset:16384\n" \
"v_mfma_f32_16x16x32_f16 v[4:7], v[152:155], v[172:175], v[4:7]\n" \
"v_pk_add_f16 v170, v170, v174\n" \
"v_pk_add_f16 v171, v171, v175\n" \
"v_pk_add_f16 v237, v133, v157\n" \
"v_pk_add_f16 v238, v134, v158\n" \
"v_pk_add_f16 v239, v135, v159\n" \
"v_mfma_f32_16x16x32_f16 v[16:19], v[156:159], v[172:175], v[16:19]\n" \
"s_waitcnt lgkmcnt(6)\n" \
"v_pk_add_f16 v200, v184, v192\n" \
"ds_read_b128 v[164:167], v246 offset:8704\n" \
"ds_read_b64_tr_b16 v[214:215], v244 offset:24576\n" \
"ds_read_b64_tr_b16 v[216:217], v243 offset:16896\n" \
"v_mfma_f32_16x16x32_f16 v[8:11], v[232:235], v[168:171], v[8:11]\n" \
"v_pk_add_f16 v201, v185, v193\n" \
"v_pk_add_f16 v202, v186, v194\n" \
"ds_read_b64_tr_b16 v[218:219], v243 offset:25088\n" \
"v_mfma_f32_16x16x32_f16 v[20:23], v[236:239], v[168:171], v[20:23]\n" \
"ds_read_b64_tr_b16 v[220:221], v244 offset:16896\n" \
"ds_read_b64_tr_b16 v[222:223], v244 offset:25088\n" \
"v_mfma_f32_16x16x32_f16 v[0:3], v[184:187], v[176:179], v[0:3]\n" \
"v_pk_add_f16 v203, v187, v195\n" \
"s_waitcnt lgkmcnt(7)\n" \
"v_pk_add_f16 v204, v188, v196\n" \
"ds_read_b128 v[168:171], v240 offset:43008\n" \
"v_mfma_f32_16x16x32_f16 v[12:15], v[188:191], v[176:179], v[12:15]\n" \
"v_pk_add_f16 v176, v176, v180\n" \
"v_pk_add_f16 v177, v177, v181\n" \
"v_pk_add_f16 v205, v189, v197\n" \
"v_mfma_f32_16x16x32_f16 v[4:7], v[192:195], v[180:183], v[4:7]\n" \
"v_pk_add_f16 v178, v178, v182\n" \
"v_pk_add_f16 v179, v179, v183\n" \
"v_pk_add_f16 v206, v190, v198\n" \
"v_pk_add_f16 v207, v191, v199\n" \
"v_mfma_f32_16x16x32_f16 v[16:19], v[196:199], v[180:183], v[16:19]\n" \
"s_waitcnt lgkmcnt(4)\n" \
"v_pk_add_f16 v224, v208, v216\n" \
"ds_read_b128 v[172:175], v240 offset:43520\n" \
"v_mfma_f32_16x16x32_f16 v[8:11], v[200:203], v[176:179], v[8:11]\n" \
"v_pk_add_f16 v225, v209, v217\n" \
"v_mfma_f32_16x16x32_f16 v[20:23], v[204:207], v[176:179], v[20:23]\n" \
"s_waitcnt lgkmcnt(4)\n" \
"v_pk_add_f16 v226, v210, v218\n" \
"v_pk_add_f16 v227, v211, v219\n" \
"v_mfma_f32_16x16x32_f16 v[0:3], v[208:211], v[160:163], v[0:3]\n" \
"s_waitcnt lgkmcnt(2)\n" \
"v_pk_add_f16 v228, v212, v220\n" \
"v_pk_add_f16 v229, v213, v221\n" \
"ds_read_b128 v[176:179], v246 offset:2048\n" \
"v_mfma_f32_16x16x32_f16 v[12:15], v[212:215], v[160:163], v[12:15]\n" \
"v_pk_add_f16 v160, v160, v164\n" \
"v_pk_add_f16 v161, v161, v165\n" \
"v_pk_add_f16 v230, v214, v222\n" \
"v_mfma_f32_16x16x32_f16 v[4:7], v[216:219], v[164:167], v[4:7]\n" \
"v_pk_add_f16 v162, v162, v166\n" \
"v_pk_add_f16 v163, v163, v167\n" \
"v_pk_add_f16 v231, v215, v223\n" \
"v_mfma_f32_16x16x32_f16 v[16:19], v[220:223], v[164:167], v[16:19]\n" \
"ds_read_b128 v[180:183], v246 offset:2560\n" \
"s_mov_b32 vcc_lo, 0xaaaaaaaa\n" \
"v_mfma_f32_16x16x32_f16 v[8:11], v[224:227], v[160:163], v[8:11]\n" \
"s_mov_b32 vcc_hi, 0xaaaaaaaa\n" \
"v_mfma_f32_16x16x32_f16 v[20:23], v[228:231], v[160:163], v[20:23]\n" \
"v_add_u32 v241, 0xffff7000, v240\n" \
"s_waitcnt lgkmcnt(2)\n" \
"v_mfma_f32_16x16x32_f16 v[24:27], v[128:131], v[168:171], v[24:27]\n" \
"ds_read_b128 v[160:163], v246 offset:10240\n" \
"v_mfma_f32_16x16x32_f16 v[36:39], v[132:135], v[168:171], v[36:39]\n" \
"v_pk_add_f16 v168, v168, v172\n" \
"v_pk_add_f16 v169, v169, v173\n" \
"v_mfma_f32_16x16x32_f16 v[28:31], v[152:155], v[172:175], v[28:31]\n" \
"v_pk_add_f16 v170, v170, v174\n" \
"v_pk_add_f16 v171, v171, v175\n" \
"v_add_f32 v252, v4, v8\n" \
"v_add_f32 v253, v5, v9\n" \
"v_add_f32 v254, v6, v10\n" \
"v_add_f32 v255, v7, v11\n" \
"v_mfma_f32_16x16x32_f16 v[40:43], v[156:159], v[172:175], v[40:43]\n" \
"ds_read_b128 v[164:167], v246 offset:10752\n" \
"v_fmac_f32 v4, s56, v8\n" \
"v_fmac_f32 v5, s56, v9\n" \
"v_fmac_f32 v6, s56, v10\n" \
"v_fmac_f32 v7, s56, v11\n" \
"v_mfma_f32_16x16x32_f16 v[32:35], v[232:235], v[168:171], v[32:35]\n" \
"v_fmac_f32 v0, s57, v252\n" \
"v_fmac_f32 v1, s57, v253\n" \
"v_fmac_f32 v2, s57, v254\n" \
"v_fmac_f32 v3, s57, v255\n" \
"v_add_f32 v252, v16, v20\n" \
"v_mfma_f32_16x16x32_f16 v[44:47], v[236:239], v[168:171], v[44:47]\n" \
"v_add_f32 v253, v17, v21\n" \
"v_add_f32 v254, v18, v22\n" \
"v_add_f32 v255, v19, v23\n" \
"v_fmac_f32 v16, s56, v20\n" \
"s_waitcnt lgkmcnt(1)\n" \
"v_mfma_f32_16x16x32_f16 v[24:27], v[184:187], v[176:179], v[24:27]\n" \
"ds_read_b128 v[168:171], v240 offset:45056\n" \
"v_fmac_f32 v17, s56, v21\n" \
"v_fmac_f32 v18, s56, v22\n" \
"v_fmac_f32 v19, s56, v23\n" \
"v_fmac_f32 v12, s57, v252\n" \
"v_fmac_f32 v13, s57, v253\n" \
"v_mfma_f32_16x16x32_f16 v[36:39], v[188:191], v[176:179], v[36:39]\n" \
"v_pk_add_f16 v176, v176, v180\n" \
"v_pk_add_f16 v177, v177, v181\n" \
"v_fmac_f32 v14, s57, v254\n" \
"v_fmac_f32 v15, s57, v255\n" \
"v_cvt_pk_bf16_f32 v8, v0, v1\n" \
"v_cvt_pk_bf16_f32 v20, v4, v5\n" \
"v_mfma_f32_16x16x32_f16 v[28:31], v[192:195], v[180:183], v[28:31]\n" \
"v_pk_add_f16 v178, v178, v182\n" \
"v_pk_add_f16 v179, v179, v183\n" \
"v_cvt_pk_bf16_f32 v9, v2, v3\n" \
"v_cvt_pk_bf16_f32 v21, v6, v7\n" \
"v_cvt_pk_bf16_f32 v10, v12, v13\n" \
"v_cvt_pk_bf16_f32 v22, v16, v17\n" \
"v_cvt_pk_bf16_f32 v11, v14, v15\n" \
"v_mfma_f32_16x16x32_f16 v[40:43], v[196:199], v[180:183], v[40:43]\n" \
"ds_read_b128 v[172:175], v240 offset:45568\n" \
"v_cvt_pk_bf16_f32 v23, v18, v19\n" \
"s_not_b64 vcc, vcc\n" \
"v_cndmask_b32_dpp v0, v20, v8, vcc quad_perm:[1,0,3,2] row_mask:0xf bank_mask:0xf\n" \
"v_cndmask_b32_dpp v1, v21, v9, vcc quad_perm:[1,0,3,2] row_mask:0xf bank_mask:0xf\n" \
"v_mfma_f32_16x16x32_f16 v[32:35], v[200:203], v[176:179], v[32:35]\n" \
"s_waitcnt vmcnt(0)\n" \
"s_barrier\n" \
"v_cndmask_b32_dpp v2, v22, v10, vcc quad_perm:[1,0,3,2] row_mask:0xf bank_mask:0xf\n" \
"v_cndmask_b32_dpp v3, v23, v11, vcc quad_perm:[1,0,3,2] row_mask:0xf bank_mask:0xf\n" \
"buffer_store_dwordx4 v[0:3], v245, s[40:43], s69 offen sc1\n" \
"s_not_b64 vcc, vcc\n" \
"s_add_i32 s85, s69, 0x10000\n" \
"v_mfma_f32_16x16x32_f16 v[44:47], v[204:207], v[176:179], v[44:47]\n" \
"v_cndmask_b32_dpp v12, v8, v20, vcc quad_perm:[1,0,3,2] row_mask:0xf bank_mask:0xf\n" \
"v_cndmask_b32_dpp v13, v9, v21, vcc quad_perm:[1,0,3,2] row_mask:0xf bank_mask:0xf\n" \
"v_cndmask_b32_dpp v14, v10, v22, vcc quad_perm:[1,0,3,2] row_mask:0xf bank_mask:0xf\n" \
"v_cndmask_b32_dpp v15, v11, v23, vcc quad_perm:[1,0,3,2] row_mask:0xf bank_mask:0xf\n" \
"buffer_store_dwordx4 v[12:15], v245, s[40:43], s85 offen sc1\n" \
"v_mfma_f32_16x16x32_f16 v[24:27], v[208:211], v[160:163], v[24:27]\n" \
"ds_read_b128 v[176:179], v240 offset:4096\n" \
"v_mfma_f32_16x16x32_f16 v[36:39], v[212:215], v[160:163], v[36:39]\n" \
"s_waitcnt lgkmcnt(2)\n" \
"v_pk_add_f16 v160, v160, v164\n" \
"v_pk_add_f16 v161, v161, v165\n" \
"v_mfma_f32_16x16x32_f16 v[28:31], v[216:219], v[164:167], v[28:31]\n" \
"v_pk_add_f16 v162, v162, v166\n" \
"v_pk_add_f16 v163, v163, v167\n" \
"v_mfma_f32_16x16x32_f16 v[40:43], v[220:223], v[164:167], v[40:43]\n" \
"ds_read_b128 v[180:183], v240 offset:4608\n" \
"v_mfma_f32_16x16x32_f16 v[32:35], v[224:227], v[160:163], v[32:35]\n" \
"v_mfma_f32_16x16x32_f16 v[44:47], v[228:231], v[160:163], v[44:47]\n" \
"v_mfma_f32_16x16x32_f16 v[48:51], v[136:139], v[168:171], v[48:51]\n" \
"ds_read_b128 v[160:163], v240 offset:24576\n" \
"v_mfma_f32_16x16x32_f16 v[60:63], v[140:143], v[168:171], v[60:63]\n" \
"s_waitcnt lgkmcnt(3)\n" \
"v_pk_add_f16 v168, v168, v172\n" \
"v_pk_add_f16 v169, v169, v173\n" \
"v_mfma_f32_16x16x32_f16 v[52:55], v[144:147], v[172:175], v[52:55]\n" \
"v_pk_add_f16 v170, v170, v174\n" \
"v_pk_add_f16 v171, v171, v175\n" \
"v_add_f32 v252, v28, v32\n" \
"v_add_f32 v253, v29, v33\n" \
"v_mfma_f32_16x16x32_f16 v[64:67], v[148:151], v[172:175], v[64:67]\n" \
"ds_read_b128 v[164:167], v240 offset:25088\n" \
"v_add_f32 v254, v30, v34\n" \
"v_add_f32 v255, v31, v35\n" \
"v_fmac_f32 v28, s56, v32\n" \
"v_mfma_f32_16x16x32_f16 v[56:59], v[120:123], v[168:171], v[56:59]\n" \
"v_fmac_f32 v29, s56, v33\n" \
"v_fmac_f32 v30, s56, v34\n" \
"v_fmac_f32 v31, s56, v35\n" \
"v_mfma_f32_16x16x32_f16 v[68:71], v[124:127], v[168:171], v[68:71]\n" \
"v_fmac_f32 v24, s57, v252\n" \
"v_fmac_f32 v25, s57, v253\n" \
"v_fmac_f32 v26, s57, v254\n" \
"s_waitcnt lgkmcnt(2)\n" \
"v_mfma_f32_16x16x32_f16 v[48:51], v[128:131], v[176:179], v[48:51]\n" \
"ds_read_b128 v[168:171], v241 offset:4096\n" \
"v_fmac_f32 v27, s57, v255\n" \
"v_add_f32 v252, v40, v44\n" \
"v_add_f32 v253, v41, v45\n" \
"v_mfma_f32_16x16x32_f16 v[60:63], v[132:135], v[176:179], v[60:63]\n" \
"v_pk_add_f16 v176, v176, v180\n" \
"v_pk_add_f16 v177, v177, v181\n" \
"v_add_f32 v254, v42, v46\n" \
"v_add_f32 v255, v43, v47\n" \
"v_mfma_f32_16x16x32_f16 v[52:55], v[152:155], v[180:183], v[52:55]\n" \
"v_pk_add_f16 v178, v178, v182\n" \
"v_pk_add_f16 v179, v179, v183\n" \
"v_fmac_f32 v40, s56, v44\n" \
"v_fmac_f32 v41, s56, v45\n" \
"v_fmac_f32 v42, s56, v46\n" \
"v_mfma_f32_16x16x32_f16 v[64:67], v[156:159], v[180:183], v[64:67]\n" \
"ds_read_b128 v[172:175], v241 offset:4608\n" \
"v_fmac_f32 v43, s56, v47\n" \
"v_fmac_f32 v36, s57, v252\n" \
"v_fmac_f32 v37, s57, v253\n" \
"v_mfma_f32_16x16x32_f16 v[56:59], v[232:235], v[176:179], v[56:59]\n" \
"v_fmac_f32 v38, s57, v254\n" \
"v_fmac_f32 v39, s57, v255\n" \
"v_cvt_pk_bf16_f32 v32, v24, v25\n" \
"v_mfma_f32_16x16x32_f16 v[68:71], v[236:239], v[176:179], v[68:71]\n" \
"v_cvt_pk_bf16_f32 v44, v28, v29\n" \
"v_cvt_pk_bf16_f32 v33, v26, v27\n" \
"v_cvt_pk_bf16_f32 v45, v30, v31\n" \
"s_waitcnt lgkmcnt(1)\n" \
"v_mfma_f32_16x16x32_f16 v[48:51], v[184:187], v[160:163], v[48:51]\n" \
"ds_read_b128 v[176:179], v240 offset:47104\n" \
"v_cvt_pk_bf16_f32 v34, v36, v37\n" \
"v_cvt_pk_bf16_f32 v46, v40, v41\n" \
"v_mfma_f32_16x16x32_f16 v[60:63], v[188:191], v[160:163], v[60:63]\n" \
"v_pk_add_f16 v160, v160, v164\n" \
"v_pk_add_f16 v161, v161, v165\n" \
"v_cvt_pk_bf16_f32 v35, v38, v39\n" \
"v_cvt_pk_bf16_f32 v47, v42, v43\n" \
"s_not_b64 vcc, vcc\n" \
"v_mfma_f32_16x16x32_f16 v[52:55], v[192:195], v[164:167], v[52:55]\n" \
"v_pk_add_f16 v162, v162, v166\n" \
"v_pk_add_f16 v163, v163, v167\n" \
"v_cndmask_b32_dpp v24, v44, v32, vcc quad_perm:[1,0,3,2] row_mask:0xf bank_mask:0xf\n" \
"v_cndmask_b32_dpp v25, v45, v33, vcc quad_perm:[1,0,3,2] row_mask:0xf bank_mask:0xf\n" \
"v_cndmask_b32_dpp v26, v46, v34, vcc quad_perm:[1,0,3,2] row_mask:0xf bank_mask:0xf\n" \
"v_mfma_f32_16x16x32_f16 v[64:67], v[196:199], v[164:167], v[64:67]\n" \
"ds_read_b128 v[180:183], v240 offset:47616\n" \
"v_cndmask_b32_dpp v27, v47, v35, vcc quad_perm:[1,0,3,2] row_mask:0xf bank_mask:0xf\n" \
"buffer_store_dwordx4 v[24:27], v245, s[40:43], s70 offen sc1\n" \
"s_not_b64 vcc, vcc\n" \
"v_mfma_f32_16x16x32_f16 v[56:59], v[200:203], v[160:163], v[56:59]\n" \
"s_add_i32 s85, s70, 0x10000\n" \
"v_cndmask_b32_dpp v36, v32, v44, vcc quad_perm:[1,0,3,2] row_mask:0xf bank_mask:0xf\n" \
"v_cndmask_b32_dpp v37, v33, v45, vcc quad_perm:[1,0,3,2] row_mask:0xf bank_mask:0xf\n" \
"v_mfma_f32_16x16x32_f16 v[68:71], v[204:207], v[160:163], v[68:71]\n" \
"v_cndmask_b32_dpp v38, v34, v46, vcc quad_perm:[1,0,3,2] row_mask:0xf bank_mask:0xf\n" \
"v_cndmask_b32_dpp v39, v35, v47, vcc quad_perm:[1,0,3,2] row_mask:0xf bank_mask:0xf\n" \
"buffer_store_dwordx4 v[36:39], v245, s[40:43], s85 offen sc1\n" \
"v_mfma_f32_16x16x32_f16 v[48:51], v[208:211], v[168:171], v[48:51]\n" \
"ds_read_b128 v[160:163], v240 offset:6144\n" \
"v_mfma_f32_16x16x32_f16 v[60:63], v[212:215], v[168:171], v[60:63]\n" \
"s_waitcnt lgkmcnt(2)\n" \
"v_pk_add_f16 v168, v168, v172\n" \
"v_pk_add_f16 v169, v169, v173\n" \
"v_mfma_f32_16x16x32_f16 v[52:55], v[216:219], v[172:175], v[52:55]\n" \
"v_pk_add_f16 v170, v170, v174\n" \
"v_pk_add_f16 v171, v171, v175\n" \
"v_mfma_f32_16x16x32_f16 v[64:67], v[220:223], v[172:175], v[64:67]\n" \
"ds_read_b128 v[164:167], v240 offset:6656\n" \
"v_mfma_f32_16x16x32_f16 v[56:59], v[224:227], v[168:171], v[56:59]\n" \
"v_mfma_f32_16x16x32_f16 v[68:71], v[228:231], v[168:171], v[68:71]\n" \
"v_mfma_f32_16x16x32_f16 v[72:75], v[136:139], v[176:179], v[72:75]\n" \
"ds_read_b128 v[168:171], v240 offset:26624\n" \
"v_mfma_f32_16x16x32_f16 v[84:87], v[140:143], v[176:179], v[84:87]\n" \
"s_waitcnt lgkmcnt(3)\n" \
"v_pk_add_f16 v176, v176, v180\n" \
"v_pk_add_f16 v177, v177, v181\n" \
"v_mfma_f32_16x16x32_f16 v[76:79], v[144:147], v[180:183], v[76:79]\n" \
"v_pk_add_f16 v178, v178, v182\n" \
"v_pk_add_f16 v179, v179, v183\n" \
"v_add_f32 v252, v52, v56\n" \
"v_add_f32 v253, v53, v57\n" \
"v_mfma_f32_16x16x32_f16 v[88:91], v[148:151], v[180:183], v[88:91]\n" \
"ds_read_b128 v[172:175], v240 offset:27136\n" \
"v_add_f32 v254, v54, v58\n" \
"v_add_f32 v255, v55, v59\n" \
"v_mfma_f32_16x16x32_f16 v[80:83], v[120:123], v[176:179], v[80:83]\n" \
"v_fmac_f32 v52, s56, v56\n" \
"v_fmac_f32 v53, s56, v57\n" \
"v_mfma_f32_16x16x32_f16 v[92:95], v[124:127], v[176:179], v[92:95]\n" \
"v_fmac_f32 v54, s56, v58\n" \
"v_fmac_f32 v55, s56, v59\n" \
"s_waitcnt lgkmcnt(2)\n" \
"v_mfma_f32_16x16x32_f16 v[72:75], v[128:131], v[160:163], v[72:75]\n" \
"ds_read_b128 v[176:179], v241 offset:6144\n" \
"v_fmac_f32 v48, s57, v252\n" \
"v_fmac_f32 v49, s57, v253\n" \
"v_mfma_f32_16x16x32_f16 v[84:87], v[132:135], v[160:163], v[84:87]\n" \
"v_pk_add_f16 v160, v160, v164\n" \
"v_pk_add_f16 v161, v161, v165\n" \
"v_fmac_f32 v50, s57, v254\n" \
"v_fmac_f32 v51, s57, v255\n" \
"v_mfma_f32_16x16x32_f16 v[76:79], v[152:155], v[164:167], v[76:79]\n" \
"v_pk_add_f16 v162, v162, v166\n" \
"v_pk_add_f16 v163, v163, v167\n" \
"v_add_f32 v252, v64, v68\n" \
"v_add_f32 v253, v65, v69\n" \
"v_mfma_f32_16x16x32_f16 v[88:91], v[156:159], v[164:167], v[88:91]\n" \
"ds_read_b128 v[180:183], v241 offset:6656\n" \
"v_add_f32 v254, v66, v70\n" \
"v_add_f32 v255, v67, v71\n" \
"v_mfma_f32_16x16x32_f16 v[80:83], v[232:235], v[160:163], v[80:83]\n" \
"v_fmac_f32 v64, s56, v68\n" \
"v_fmac_f32 v65, s56, v69\n" \
"v_mfma_f32_16x16x32_f16 v[92:95], v[236:239], v[160:163], v[92:95]\n" \
"v_fmac_f32 v66, s56, v70\n" \
"v_fmac_f32 v67, s56, v71\n" \
"s_waitcnt lgkmcnt(2)\n" \
"v_mfma_f32_16x16x32_f16 v[72:75], v[184:187], v[168:171], v[72:75]\n" \
"ds_read_b128 v[160:163], v240 offset:49152\n" \
"v_fmac_f32 v60, s57, v252\n" \
"v_fmac_f32 v61, s57, v253\n" \
"v_mfma_f32_16x16x32_f16 v[84:87], v[188:191], v[168:171], v[84:87]\n" \
"v_pk_add_f16 v168, v168, v172\n" \
"v_pk_add_f16 v169, v169, v173\n" \
"v_fmac_f32 v62, s57, v254\n" \
"v_fmac_f32 v63, s57, v255\n" \
"v_mfma_f32_16x16x32_f16 v[76:79], v[192:195], v[172:175], v[76:79]\n" \
"v_pk_add_f16 v170, v170, v174\n" \
"v_pk_add_f16 v171, v171, v175\n" \
"v_cvt_pk_bf16_f32 v56, v48, v49\n" \
"v_cvt_pk_bf16_f32 v68, v52, v53\n" \
"v_mfma_f32_16x16x32_f16 v[88:91], v[196:199], v[172:175], v[88:91]\n" \
"ds_read_b128 v[164:167], v240 offset:49664\n" \
"v_cvt_pk_bf16_f32 v57, v50, v51\n" \
"v_cvt_pk_bf16_f32 v69, v54, v55\n" \
"v_mfma_f32_16x16x32_f16 v[80:83], v[200:203], v[168:171], v[80:83]\n" \
"v_cvt_pk_bf16_f32 v58, v60, v61\n" \
"v_cvt_pk_bf16_f32 v70, v64, v65\n" \
"v_mfma_f32_16x16x32_f16 v[92:95], v[204:207], v[168:171], v[92:95]\n" \
"v_cvt_pk_bf16_f32 v59, v62, v63\n" \
"v_cvt_pk_bf16_f32 v71, v66, v67\n" \
"s_waitcnt lgkmcnt(2)\n" \
"v_mfma_f32_16x16x32_f16 v[72:75], v[208:211], v[176:179], v[72:75]\n" \
"ds_read_b128 v[168:171], v240 offset:8192\n" \
"s_not_b64 vcc, vcc\n" \
"v_cndmask_b32_dpp v48, v68, v56, vcc quad_perm:[1,0,3,2] row_mask:0xf bank_mask:0xf\n" \
"v_mfma_f32_16x16x32_f16 v[84:87], v[212:215], v[176:179], v[84:87]\n" \
"v_pk_add_f16 v176, v176, v180\n" \
"v_pk_add_f16 v177, v177, v181\n" \
"v_cndmask_b32_dpp v49, v69, v57, vcc quad_perm:[1,0,3,2] row_mask:0xf bank_mask:0xf\n" \
"v_cndmask_b32_dpp v50, v70, v58, vcc quad_perm:[1,0,3,2] row_mask:0xf bank_mask:0xf\n" \
"v_mfma_f32_16x16x32_f16 v[76:79], v[216:219], v[180:183], v[76:79]\n" \
"v_pk_add_f16 v178, v178, v182\n" \
"v_pk_add_f16 v179, v179, v183\n" \
"v_cndmask_b32_dpp v51, v71, v59, vcc quad_perm:[1,0,3,2] row_mask:0xf bank_mask:0xf\n" \
"buffer_store_dwordx4 v[48:51], v245, s[40:43], s71 offen sc1\n" \
"v_mfma_f32_16x16x32_f16 v[88:91], v[220:223], v[180:183], v[88:91]\n" \
"ds_read_b128 v[172:175], v240 offset:8704\n" \
"s_not_b64 vcc, vcc\n" \
"s_add_i32 s85, s71, 0x10000\n" \
"v_mfma_f32_16x16x32_f16 v[80:83], v[224:227], v[176:179], v[80:83]\n" \
"v_cndmask_b32_dpp v60, v56, v68, vcc quad_perm:[1,0,3,2] row_mask:0xf bank_mask:0xf\n" \
"v_cndmask_b32_dpp v61, v57, v69, vcc quad_perm:[1,0,3,2] row_mask:0xf bank_mask:0xf\n" \
"v_mfma_f32_16x16x32_f16 v[92:95], v[228:231], v[176:179], v[92:95]\n" \
"v_cndmask_b32_dpp v62, v58, v70, vcc quad_perm:[1,0,3,2] row_mask:0xf bank_mask:0xf\n" \
"v_cndmask_b32_dpp v63, v59, v71, vcc quad_perm:[1,0,3,2] row_mask:0xf bank_mask:0xf\n" \
"buffer_store_dwordx4 v[60:63], v245, s[40:43], s85 offen sc1\n" \
"s_waitcnt lgkmcnt(1)\n" \
"v_mfma_f32_16x16x32_f16 v[96:99], v[136:139], v[160:163], v[96:99]\n" \
"ds_read_b128 v[176:179], v240 offset:28672\n" \
"v_mfma_f32_16x16x32_f16 v[108:111], v[140:143], v[160:163], v[108:111]\n" \
"v_pk_add_f16 v160, v160, v164\n" \
"v_pk_add_f16 v161, v161, v165\n" \
"v_mfma_f32_16x16x32_f16 v[100:103], v[144:147], v[164:167], v[100:103]\n" \
"v_pk_add_f16 v162, v162, v166\n" \
"v_pk_add_f16 v163, v163, v167\n" \
"v_add_f32 v252, v76, v80\n" \
"v_add_f32 v253, v77, v81\n" \
"v_mfma_f32_16x16x32_f16 v[112:115], v[148:151], v[164:167], v[112:115]\n" \
"ds_read_b128 v[180:183], v240 offset:29184\n" \
"v_add_f32 v254, v78, v82\n" \
"v_add_f32 v255, v79, v83\n" \
"v_mfma_f32_16x16x32_f16 v[104:107], v[120:123], v[160:163], v[104:107]\n" \
"v_fmac_f32 v76, s56, v80\n" \
"v_fmac_f32 v77, s56, v81\n" \
"v_mfma_f32_16x16x32_f16 v[116:119], v[124:127], v[160:163], v[116:119]\n" \
"v_fmac_f32 v78, s56, v82\n" \
"v_fmac_f32 v79, s56, v83\n" \
"v_mfma_f32_16x16x32_f16 v[96:99], v[128:131], v[168:171], v[96:99]\n" \
"ds_read_b128 v[160:163], v241 offset:8192\n" \
"v_fmac_f32 v72, s57, v252\n" \
"v_fmac_f32 v73, s57, v253\n" \
"v_mfma_f32_16x16x32_f16 v[108:111], v[132:135], v[168:171], v[108:111]\n" \
"s_waitcnt lgkmcnt(2)\n" \
"v_pk_add_f16 v168, v168, v172\n" \
"v_pk_add_f16 v169, v169, v173\n" \
"v_fmac_f32 v74, s57, v254\n" \
"v_fmac_f32 v75, s57, v255\n" \
"v_mfma_f32_16x16x32_f16 v[100:103], v[152:155], v[172:175], v[100:103]\n" \
"v_pk_add_f16 v170, v170, v174\n" \
"v_pk_add_f16 v171, v171, v175\n" \
"v_add_f32 v252, v88, v92\n" \
"v_add_f32 v253, v89, v93\n" \
"v_mfma_f32_16x16x32_f16 v[112:115], v[156:159], v[172:175], v[112:115]\n" \
"ds_read_b128 v[164:167], v241 offset:8704\n" \
"v_add_f32 v254, v90, v94\n" \
"v_add_f32 v255, v91, v95\n" \
"v_mfma_f32_16x16x32_f16 v[104:107], v[232:235], v[168:171], v[104:107]\n" \
"v_fmac_f32 v88, s56, v92\n" \
"v_fmac_f32 v89, s56, v93\n" \
"v_mfma_f32_16x16x32_f16 v[116:119], v[236:239], v[168:171], v[116:119]\n" \
"v_fmac_f32 v90, s56, v94\n" \
"v_fmac_f32 v91, s56, v95\n" \
"v_mfma_f32_16x16x32_f16 v[96:99], v[184:187], v[176:179], v[96:99]\n" \
"v_fmac_f32 v84, s57, v252\n" \
"v_fmac_f32 v85, s57, v253\n" \
"v_fmac_f32 v86, s57, v254\n" \
"v_mfma_f32_16x16x32_f16 v[108:111], v[188:191], v[176:179], v[108:111]\n" \
"s_waitcnt lgkmcnt(1)\n" \
"v_pk_add_f16 v176, v176, v180\n" \
"v_pk_add_f16 v177, v177, v181\n" \
"v_fmac_f32 v87, s57, v255\n" \
"v_mfma_f32_16x16x32_f16 v[100:103], v[192:195], v[180:183], v[100:103]\n" \
"v_pk_add_f16 v178, v178, v182\n" \
"v_pk_add_f16 v179, v179, v183\n" \
"v_cvt_pk_bf16_f32 v80, v72, v73\n" \
"v_cvt_pk_bf16_f32 v92, v76, v77\n" \
"v_mfma_f32_16x16x32_f16 v[112:115], v[196:199], v[180:183], v[112:115]\n" \
"v_cvt_pk_bf16_f32 v81, v74, v75\n" \
"v_cvt_pk_bf16_f32 v93, v78, v79\n" \
"v_mfma_f32_16x16x32_f16 v[104:107], v[200:203], v[176:179], v[104:107]\n" \
"v_cvt_pk_bf16_f32 v82, v84, v85\n" \
"v_cvt_pk_bf16_f32 v94, v88, v89\n" \
"v_mfma_f32_16x16x32_f16 v[116:119], v[204:207], v[176:179], v[116:119]\n" \
"v_cvt_pk_bf16_f32 v83, v86, v87\n" \
"v_cvt_pk_bf16_f32 v95, v90, v91\n" \
"v_mfma_f32_16x16x32_f16 v[96:99], v[208:211], v[160:163], v[96:99]\n" \
"s_not_b64 vcc, vcc\n" \
"v_cndmask_b32_dpp v72, v92, v80, vcc quad_perm:[1,0,3,2] row_mask:0xf bank_mask:0xf\n" \
"v_cndmask_b32_dpp v73, v93, v81, vcc quad_perm:[1,0,3,2] row_mask:0xf bank_mask:0xf\n" \
"v_mfma_f32_16x16x32_f16 v[108:111], v[212:215], v[160:163], v[108:111]\n" \
"s_waitcnt lgkmcnt(0)\n" \
"v_pk_add_f16 v160, v160, v164\n" \
"v_pk_add_f16 v161, v161, v165\n" \
"v_cndmask_b32_dpp v74, v94, v82, vcc quad_perm:[1,0,3,2] row_mask:0xf bank_mask:0xf\n" \
"v_mfma_f32_16x16x32_f16 v[100:103], v[216:219], v[164:167], v[100:103]\n" \
"v_pk_add_f16 v162, v162, v166\n" \
"v_pk_add_f16 v163, v163, v167\n" \
"v_cndmask_b32_dpp v75, v95, v83, vcc quad_perm:[1,0,3,2] row_mask:0xf bank_mask:0xf\n" \
"buffer_store_dwordx4 v[72:75], v245, s[40:43], s72 offen sc1\n" \
"v_mfma_f32_16x16x32_f16 v[112:115], v[220:223], v[164:167], v[112:115]\n" \
"s_not_b64 vcc, vcc\n" \
"s_add_i32 s85, s72, 0x10000\n" \
"v_mfma_f32_16x16x32_f16 v[104:107], v[224:227], v[160:163], v[104:107]\n" \
"v_cndmask_b32_dpp v84, v80, v92, vcc quad_perm:[1,0,3,2] row_mask:0xf bank_mask:0xf\n" \
"v_cndmask_b32_dpp v85, v81, v93, vcc quad_perm:[1,0,3,2] row_mask:0xf bank_mask:0xf\n" \
"v_mfma_f32_16x16x32_f16 v[116:119], v[228:231], v[160:163], v[116:119]\n" \
"v_cndmask_b32_dpp v86, v82, v94, vcc quad_perm:[1,0,3,2] row_mask:0xf bank_mask:0xf\n" \
"v_cndmask_b32_dpp v87, v83, v95, vcc quad_perm:[1,0,3,2] row_mask:0xf bank_mask:0xf\n" \
"buffer_store_dwordx4 v[84:87], v245, s[40:43], s85 offen sc1\n" \
"s_nop 7\n" \
"v_add_f32 v252, v100, v104\n" \
"v_add_f32 v253, v101, v105\n" \
"v_add_f32 v254, v102, v106\n" \
"v_add_f32 v255, v103, v107\n" \
"v_fmac_f32 v100, s56, v104\n" \
"v_fmac_f32 v101, s56, v105\n" \
"v_fmac_f32 v102, s56, v106\n" \
"v_fmac_f32 v103, s56, v107\n" \
"v_fmac_f32 v96, s57, v252\n" \
"v_fmac_f32 v97, s57, v253\n" \
"v_fmac_f32 v98, s57, v254\n" \
"v_fmac_f32 v99, s57, v255\n" \
"v_add_f32 v252, v112, v116\n" \
"v_add_f32 v253, v113, v117\n" \
"v_add_f32 v254, v114, v118\n" \
"v_add_f32 v255, v115, v119\n" \
"v_fmac_f32 v112, s56, v116\n" \
"v_fmac_f32 v113, s56, v117\n" \
"v_fmac_f32 v114, s56, v118\n" \
"v_fmac_f32 v115, s56, v119\n" \
"v_fmac_f32 v108, s57, v252\n" \
"v_fmac_f32 v109, s57, v253\n" \
"v_fmac_f32 v110, s57, v254\n" \
"v_fmac_f32 v111, s57, v255\n" \
"v_cvt_pk_bf16_f32 v104, v96, v97\n" \
"v_cvt_pk_bf16_f32 v116, v100, v101\n" \
"v_cvt_pk_bf16_f32 v105, v98, v99\n" \
"v_cvt_pk_bf16_f32 v117, v102, v103\n" \
"v_cvt_pk_bf16_f32 v106, v108, v109\n" \
"v_cvt_pk_bf16_f32 v118, v112, v113\n" \
"v_cvt_pk_bf16_f32 v107, v110, v111\n" \
"v_cvt_pk_bf16_f32 v119, v114, v115\n" \
"s_not_b64 vcc, vcc\n" \
"v_cndmask_b32_dpp v96, v116, v104, vcc quad_perm:[1,0,3,2] row_mask:0xf bank_mask:0xf\n" \
"v_cndmask_b32_dpp v97, v117, v105, vcc quad_perm:[1,0,3,2] row_mask:0xf bank_mask:0xf\n" \
"v_cndmask_b32_dpp v98, v118, v106, vcc quad_perm:[1,0,3,2] row_mask:0xf bank_mask:0xf\n" \
"v_cndmask_b32_dpp v99, v119, v107, vcc quad_perm:[1,0,3,2] row_mask:0xf bank_mask:0xf\n" \
"buffer_store_dwordx4 v[96:99], v245, s[40:43], s73 offen sc1\n" \
"s_not_b64 vcc, vcc\n" \
"s_add_i32 s85, s73, 0x10000\n" \
"v_cndmask_b32_dpp v108, v104, v116, vcc quad_perm:[1,0,3,2] row_mask:0xf bank_mask:0xf\n" \
"v_cndmask_b32_dpp v109, v105, v117, vcc quad_perm:[1,0,3,2] row_mask:0xf bank_mask:0xf\n" \
"v_cndmask_b32_dpp v110, v106, v118, vcc quad_perm:[1,0,3,2] row_mask:0xf bank_mask:0xf\n" \
"v_cndmask_b32_dpp v111, v107, v119, vcc quad_perm:[1,0,3,2] row_mask:0xf bank_mask:0xf\n" \
"buffer_store_dwordx4 v[108:111], v245, s[40:43], s85 offen sc1\n" \
"s_waitcnt vmcnt(0)\n" \
"s_barrier\n" \
"buffer_store_dword v240, v248, s[36:39], 0 offen sc1\n" \
"buffer_load_dword v249, v249, s[36:39], 0 offen sc1\n" \
"s_branch .Lcf_e\n" \
".Ls03_%=:\n" \
"s_mov_b32 s87, 0x200000\n" \
"s_mov_b32 s86, 0x8006000\n" \
".Lt03_%=:\n" \
"s_sleep 3\n" \
"buffer_load_dword v253, v252, s[36:39], s86 offen sc1\n" \
"s_waitcnt vmcnt(0)\n" \
"s_sub_u32 s87, s87, 1\n" \
"s_cbranch_scc1 .Lr03_%=\n" \
"v_cmp_eq_u32 vcc, 0, v253\n" \
"s_cbranch_vccnz .Lt03_%=\n" \
"s_branch .Lr03_%=\n" \
".Ls04_%=:\n" \
"s_mov_b32 s87, 0x200000\n" \
"s_mov_b32 s86, 0x8008000\n" \
".Lt04_%=:\n" \
"s_sleep 3\n" \
"buffer_load_dword v253, v252, s[36:39], s86 offen sc1\n" \
"s_waitcnt vmcnt(0)\n" \
"s_sub_u32 s87, s87, 1\n" \
"s_cbranch_scc1 .Lr04_%=\n" \
"v_cmp_eq_u32 vcc, 0, v253\n" \
"s_cbranch_vccnz .Lt04_%=\n" \
"s_branch .Lr04_%=\n" \
".Ls05_%=:\n" \
"s_mov_b32 s87, 0x200000\n" \
"s_mov_b32 s86, 0x800a000\n" \
".Lt05_%=:\n" \
"s_sleep 3\n" \
"buffer_load_dword v253, v252, s[36:39], s86 offen sc1\n" \
"s_waitcnt vmcnt(0)\n" \
"s_sub_u32 s87, s87, 1\n" \
"s_cbranch_scc1 .Lr05_%=\n" \
"v_cmp_eq_u32 vcc, 0, v253\n" \
"s_cbranch_vccnz .Lt05_%=\n" \
"s_branch .Lr05_%=\n" \
".Ls07_%=:\n" \
"s_mov_b32 s87, 0x200000\n" \
"s_mov_b32 s86, 0x800e000\n" \
".Lt07_%=:\n" \
"s_sleep 3\n" \
"buffer_load_dword v253, v252, s[36:39], s86 offen sc1\n" \
"s_waitcnt vmcnt(0)\n" \
"s_sub_u32 s87, s87, 1\n" \
"s_cbranch_scc1 .Lr07_%=\n" \
"v_cmp_eq_u32 vcc, 0, v253\n" \
"s_cbranch_vccnz .Lt07_%=\n" \
"s_branch .Lr07_%=\n" \
".Ls08_%=:\n" \
"s_mov_b32 s87, 0x200000\n" \
"s_mov_b32 s86, 0x8010000\n" \
".Lt08_%=:\n" \
"s_sleep 3\n" \
"buffer_load_dword v253, v252, s[36:39], s86 offen sc1\n" \
"s_waitcnt vmcnt(0)\n" \
"s_sub_u32 s87, s87, 1\n" \
"s_cbranch_scc1 .Lr08_%=\n" \
"v_cmp_eq_u32 vcc, 0, v253\n" \
"s_cbranch_vccnz .Lt08_%=\n" \
"s_branch .Lr08_%=\n" \
".Ls09_%=:\n" \
"s_mov_b32 s87, 0x200000\n" \
"s_mov_b32 s86, 0x8012000\n" \
".Lt09_%=:\n" \
"s_sleep 3\n" \
"buffer_load_dword v253, v252, s[36:39], s86 offen sc1\n" \
"s_waitcnt vmcnt(0)\n" \
"s_sub_u32 s87, s87, 1\n" \
"s_cbranch_scc1 .Lr09_%=\n" \
"v_cmp_eq_u32 vcc, 0, v253\n" \
"s_cbranch_vccnz .Lt09_%=\n" \
"s_branch .Lr09_%=\n" \
".Ls011_%=:\n" \
"s_mov_b32 s87, 0x200000\n" \
"s_mov_b32 s86, 0x8016000\n" \
".Lt011_%=:\n" \
"s_sleep 3\n" \
"buffer_load_dword v253, v252, s[36:39], s86 offen sc1\n" \
"s_waitcnt vmcnt(0)\n" \
"s_sub_u32 s87, s87, 1\n" \
"s_cbranch_scc1 .Lr011_%=\n" \
"v_cmp_eq_u32 vcc, 0, v253\n" \
"s_cbranch_vccnz .Lt011_%=\n" \
"s_branch .Lr011_%=\n" \
".Ls012_%=:\n" \
"s_mov_b32 s87, 0x200000\n" \
"s_mov_b32 s86, 0x8018000\n" \
".Lt012_%=:\n" \
"s_sleep 3\n" \
"buffer_load_dword v253, v252, s[36:39], s86 offen sc1\n" \
"s_waitcnt vmcnt(0)\n" \
"s_sub_u32 s87, s87, 1\n" \
"s_cbranch_scc1 .Lr012_%=\n" \
"v_cmp_eq_u32 vcc, 0, v253\n" \
"s_cbranch_vccnz .Lt012_%=\n" \
"s_branch .Lr012_%=\n" \
".Ls013_%=:\n" \
"s_mov_b32 s87, 0x200000\n" \
"s_mov_b32 s86, 0x801a000\n" \
".Lt013_%=:\n" \
"s_sleep 3\n" \
"buffer_load_dword v253, v252, s[36:39], s86 offen sc1\n" \
"s_waitcnt vmcnt(0)\n" \
"s_sub_u32 s87, s87, 1\n" \
"s_cbranch_scc1 .Lr013_%=\n" \
"v_cmp_eq_u32 vcc, 0, v253\n" \
"s_cbranch_vccnz .Lt013_%=\n" \
"s_branch .Lr013_%=\n" \
""
#define LOOP1 \
"s_mov_b32 s91, 0x40004\n" \
"s_mov_b32 s92, 0xfff8fff8\n" \
"ds_read_b64_tr_b16 v[120:121], v241 offset:0\n" \
"ds_read_b64_tr_b16 v[122:123], v241 offset:8192\n" \
"ds_read_b64_tr_b16 v[124:125], v242 offset:0\n" \
"ds_read_b64_tr_b16 v[126:127], v242 offset:8192\n" \
"ds_read_b64_tr_b16 v[128:129], v241 offset:512\n" \
"ds_read_b64_tr_b16 v[130:131], v241 offset:8704\n" \
"ds_read_b64_tr_b16 v[132:133], v242 offset:512\n" \
"ds_read_b64_tr_b16 v[134:135], v242 offset:8704\n" \
"ds_read_b128 v[160:163], v240 offset:2048\n" \
"ds_read_b128 v[164:167], v240 offset:2560\n" \
"ds_read_b128 v[168:171], v240 offset:4096\n" \
"ds_read_b128 v[172:175], v240 offset:4608\n" \
"s_mov_b32 s94, s53\n" \
"s_mov_b32 s95, s74\n" \
"s_setprio 1\n" \
"s_waitcnt lgkmcnt(0)\n" \
"s_cmp_eq_u32 s94, 0\n" \
"s_cbranch_scc1 .Lp0_%=\n" \
".Lq0_%=:\n" \
"s_memrealtime s[86:87]\n" \
"s_waitcnt lgkmcnt(0)\n" \
"s_and_b32 s87, s94, 0xff\n" \
"s_add_i32 s75, s86, s87\n" \
".Lp0_%=:\n" \
"v_mfma_f32_16x16x32_f16 v[0:3], v[120:123], v[160:163], 0\n" \
"v_pk_add_f16 v136, v120, v128\n" \
"v_pk_add_f16 v137, v121, v129\n" \
"v_pk_add_f16 v138, v122, v130\n" \
"v_pk_add_f16 v139, v123, v131\n" \
"s_add_i32 m0, s44, 0xa000\n" \
"s_add_i32 s80, s46, 0x400000\n" \
"v_mfma_f32_16x16x32_f16 v[12:15], v[124:127], v[160:163], 0\n" \
"v_pk_add_f16 v160, v160, v164\n" \
"v_pk_add_f16 v161, v161, v165\n" \
"v_pk_add_f16 v140, v124, v132\n" \
"buffer_load_dwordx4 v232, s[36:39], s80 offen sc1 lds\n" \
"v_mfma_f32_16x16x32_f16 v[4:7], v[128:131], v[164:167], 0\n" \
"v_pk_add_f16 v162, v162, v166\n" \
"v_pk_add_f16 v163, v163, v167\n" \
"v_pk_add_f16 v141, v125, v133\n" \
"v_mfma_f32_16x16x32_f16 v[16:19], v[132:135], v[164:167], 0\n" \
"v_pk_add_f16 v142, v126, v134\n" \
"v_pk_add_f16 v143, v127, v135\n" \
"buffer_load_dwordx4 v232, s[36:39], s80 offen offset:1024 sc1 lds\n" \
"v_mfma_f32_16x16x32_f16 v[8:11], v[136:139], v[160:163], 0\n" \
"ds_write_b64 v246, v[96:97] offset:0\n" \
"ds_write_b64 v246, v[98:99] offset:4096\n" \
"v_mfma_f32_16x16x32_f16 v[20:23], v[140:143], v[160:163], 0\n" \
"s_waitcnt vmcnt(5)\n" \
"s_barrier\n" \
"v_mfma_f32_16x16x32_f16 v[24:27], v[120:123], v[168:171], 0\n" \
"ds_read_b128 v[176:179], v240 offset:26624\n" \
"ds_write_b64 v246, v[100:101] offset:8192\n" \
"buffer_store_dwordx4 v[72:75], v235, s[36:39], s93 offen sc1\n" \
"v_mfma_f32_16x16x32_f16 v[36:39], v[124:127], v[168:171], 0\n" \
"v_pk_add_f16 v168, v168, v172\n" \
"v_pk_add_f16 v169, v169, v173\n" \
"ds_read_b128 v[180:183], v240 offset:27136\n" \
"buffer_load_dwordx4 v232, s[36:39], s80 offen offset:2048 sc1 lds\n" \
"v_mfma_f32_16x16x32_f16 v[28:31], v[128:131], v[172:175], 0\n" \
"v_pk_add_f16 v170, v170, v174\n" \
"v_pk_add_f16 v171, v171, v175\n" \
"ds_read_b128 v[160:163], v240 offset:28672\n" \
"v_mfma_f32_16x16x32_f16 v[40:43], v[132:135], v[172:175], 0\n" \
"ds_read_b128 v[164:167], v240 offset:29184\n" \
"buffer_load_dwordx4 v232, s[36:39], s80 offen offset:3072 sc1 lds\n" \
"v_mfma_f32_16x16x32_f16 v[32:35], v[136:139], v[168:171], 0\n" \
"s_add_i32 m0, s45, 0xa000\n" \
"s_add_i32 s81, s47, 0x400000\n" \
"ds_write_b64 v246, v[102:103] offset:12288\n" \
"v_mfma_f32_16x16x32_f16 v[44:47], v[140:143], v[168:171], 0\n" \
"buffer_load_dwordx4 v232, s[36:39], s81 offen sc1 lds\n" \
"ds_read_b64_tr_b16 v[144:145], v241 offset:16384\n" \
"ds_read_b64_tr_b16 v[146:147], v241 offset:24576\n" \
"s_waitcnt lgkmcnt(7)\n" \
"v_mfma_f32_16x16x32_f16 v[48:51], v[120:123], v[176:179], 0\n" \
"ds_read_b128 v[168:171], v240 offset:20480\n" \
"ds_write_b64 v246, v[104:105] offset:16384\n" \
"ds_write_b64 v246, v[106:107] offset:20480\n" \
"buffer_store_dwordx4 v[196:199], v235, s[36:39], s60 offen sc1\n" \
"v_mfma_f32_16x16x32_f16 v[60:63], v[124:127], v[176:179], 0\n" \
"s_waitcnt lgkmcnt(8)\n" \
"v_pk_add_f16 v176, v176, v180\n" \
"v_pk_add_f16 v177, v177, v181\n" \
"s_add_i32 m0, s44, 0x0\n" \
"s_add_i32 s80, s46, 0x800000\n" \
"v_mfma_f32_16x16x32_f16 v[52:55], v[128:131], v[180:183], 0\n" \
"v_pk_add_f16 v178, v178, v182\n" \
"v_pk_add_f16 v179, v179, v183\n" \
"buffer_load_dwordx4 v232, s[36:39], s80 offen sc1 lds\n" \
"ds_read_b64_tr_b16 v[148:149], v242 offset:16384\n" \
"v_mfma_f32_16x16x32_f16 v[64:67], v[132:135], v[180:183], 0\n" \
"ds_read_b128 v[172:175], v240 offset:20992\n" \
"ds_write_b64 v246, v[108:109] offset:24576\n" \
"ds_read_b64_tr_b16 v[150:151], v242 offset:24576\n" \
"v_mfma_f32_16x16x32_f16 v[56:59], v[136:139], v[176:179], 0\n" \
"ds_write_b64 v246, v[110:111] offset:28672\n" \
"ds_write_b64 v246, v[112:113] offset:32768\n" \
"v_mfma_f32_16x16x32_f16 v[68:71], v[140:143], v[176:179], 0\n" \
"s_setprio 0\n" \
"buffer_load_dwordx4 v232, s[36:39], s80 offen offset:1024 sc1 lds\n" \
"ds_read_b64_tr_b16 v[152:153], v241 offset:16896\n" \
"s_waitcnt lgkmcnt(7)\n" \
"v_mfma_f32_16x16x32_f16 v[72:75], v[120:123], v[160:163], 0\n" \
"ds_read_b128 v[176:179], v240 offset:22528\n" \
"ds_write_b64 v246, v[114:115] offset:36864\n" \
"buffer_store_dwordx4 v[200:203], v235, s[36:39], s61 offen sc1\n" \
"v_mfma_f32_16x16x32_f16 v[84:87], v[124:127], v[160:163], 0\n" \
"v_pk_add_f16 v160, v160, v164\n" \
"v_pk_add_f16 v161, v161, v165\n" \
"ds_read_b64_tr_b16 v[154:155], v241 offset:25088\n" \
"ds_read_b64_tr_b16 v[156:157], v242 offset:16896\n" \
"v_mfma_f32_16x16x32_f16 v[76:79], v[128:131], v[164:167], 0\n" \
"v_pk_add_f16 v162, v162, v166\n" \
"v_pk_add_f16 v163, v163, v167\n" \
"buffer_load_dwordx4 v232, s[36:39], s80 offen offset:2048 sc1 lds\n" \
"v_mfma_f32_16x16x32_f16 v[88:91], v[132:135], v[164:167], 0\n" \
"ds_read_b128 v[180:183], v240 offset:23040\n" \
"ds_read_b64_tr_b16 v[158:159], v242 offset:25088\n" \
"v_mfma_f32_16x16x32_f16 v[80:83], v[136:139], v[160:163], 0\n" \
"ds_write_b64 v246, v[116:117] offset:40960\n" \
"v_mfma_f32_16x16x32_f16 v[92:95], v[140:143], v[160:163], 0\n" \
"buffer_load_dwordx4 v232, s[36:39], s80 offen offset:3072 sc1 lds\n" \
"v_mfma_f32_16x16x32_f16 v[96:99], v[120:123], v[168:171], 0\n" \
"s_waitcnt lgkmcnt(7)\n" \
"v_pk_add_f16 v120, v144, v152\n" \
"v_pk_add_f16 v121, v145, v153\n" \
"ds_read_b128 v[160:163], v240 offset:24576\n" \
"v_mfma_f32_16x16x32_f16 v[108:111], v[124:127], v[168:171], 0\n" \
"v_pk_add_f16 v168, v168, v172\n" \
"v_pk_add_f16 v169, v169, v173\n" \
"ds_read_b128 v[164:167], v240 offset:25088\n" \
"v_mfma_f32_16x16x32_f16 v[100:103], v[128:131], v[172:175], 0\n" \
"v_pk_add_f16 v170, v170, v174\n" \
"v_pk_add_f16 v171, v171, v175\n" \
"s_add_i32 m0, s45, 0x0\n" \
"s_add_i32 s81, s47, 0x800000\n" \
"v_mfma_f32_16x16x32_f16 v[112:115], v[132:135], v[172:175], 0\n" \
"s_waitcnt lgkmcnt(5)\n" \
"v_pk_add_f16 v122, v146, v154\n" \
"v_pk_add_f16 v123, v147, v155\n" \
"buffer_load_dwordx4 v232, s[36:39], s81 offen sc1 lds\n" \
"v_mfma_f32_16x16x32_f16 v[104:107], v[136:139], v[168:171], 0\n" \
"v_pk_add_f16 v124, v148, v156\n" \
"v_pk_add_f16 v125, v149, v157\n" \
"v_mfma_f32_16x16x32_f16 v[116:119], v[140:143], v[168:171], 0\n" \
"s_setprio 1\n" \
"s_waitcnt lgkmcnt(2)\n" \
"v_pk_add_f16 v126, v150, v158\n" \
"v_pk_add_f16 v127, v151, v159\n" \
"s_waitcnt vmcnt(7) lgkmcnt(0)\n" \
"s_cmp_eq_u32 s94, 0\n" \
"s_cbranch_scc1 .Lp1_%=\n" \
".Lq1_%=:\n" \
"s_memrealtime s[86:87]\n" \
"s_waitcnt lgkmcnt(0)\n" \
"s_sub_i32 s87, s86, s75\n" \
"s_cmp_lt_i32 s87, 0\n" \
"s_cbranch_scc1 .Lq1_%=\n" \
"s_lshr_b64 s[94:95], s[94:95], 8\n" \
"s_and_b32 s87, s94, 0xff\n" \
"s_add_i32 s75, s86, s87\n" \
".Lp1_%=:\n" \
"s_barrier\n" \
"v_mfma_f32_16x16x32_f16 v[0:3], v[144:147], v[176:179], v[0:3]\n" \
"ds_read_b128 v[168:171], v240 offset:47104\n" \
"ds_read_b64_tr_b16 v[184:185], v236 offset:0\n" \
"v_mfma_f32_16x16x32_f16 v[12:15], v[148:151], v[176:179], v[12:15]\n" \
"v_pk_add_f16 v176, v176, v180\n" \
"v_pk_add_f16 v177, v177, v181\n" \
"ds_read_b64_tr_b16 v[188:189], v237 offset:0\n" \
"v_mfma_f32_16x16x32_f16 v[4:7], v[152:155], v[180:183], v[4:7]\n" \
"v_pk_add_f16 v178, v178, v182\n" \
"v_pk_add_f16 v179, v179, v183\n" \
"v_mfma_f32_16x16x32_f16 v[16:19], v[156:159], v[180:183], v[16:19]\n" \
"ds_read_b128 v[172:175], v240 offset:47616\n" \
"s_add_i32 m0, s44, 0x5000\n" \
"s_add_i32 s80, s46, 0xc00000\n" \
"v_mfma_f32_16x16x32_f16 v[8:11], v[120:123], v[176:179], v[8:11]\n" \
"buffer_load_dwordx4 v232, s[36:39], s80 offen sc1 lds\n" \
"v_mfma_f32_16x16x32_f16 v[20:23], v[124:127], v[176:179], v[20:23]\n" \
"ds_read_b64_tr_b16 v[128:129], v241 offset:32768\n" \
"ds_read_b64_tr_b16 v[130:131], v241 offset:40960\n" \
"s_waitcnt lgkmcnt(4)\n" \
"v_mfma_f32_16x16x16_bf16 v[184:187], v[184:185], v[192:193], 0\n" \
"v_mfma_f32_16x16x32_f16 v[24:27], v[144:147], v[160:163], v[24:27]\n" \
"ds_read_b128 v[176:179], v240 offset:49152\n" \
"s_waitcnt lgkmcnt(4)\n" \
"v_mfma_f32_16x16x16_bf16 v[188:191], v[188:189], v[192:193], 0\n" \
"v_mfma_f32_16x16x32_f16 v[36:39], v[148:151], v[160:163], v[36:39]\n" \
"v_pk_add_f16 v160, v160, v164\n" \
"v_pk_add_f16 v161, v161, v165\n" \
"ds_read_b64_tr_b16 v[132:133], v242 offset:32768\n" \
"v_mfma_f32_16x16x32_f16 v[28:31], v[152:155], v[164:167], v[28:31]\n" \
"v_pk_add_f16 v162, v162, v166\n" \
"v_pk_add_f16 v163, v163, v167\n" \
"v_mfma_f32_16x16x32_f16 v[40:43], v[156:159], v[164:167], v[40:43]\n" \
"v_cvt_pk_f16_f32 v184, v184, v185\n" \
"ds_read_b128 v[180:183], v240 offset:49664\n" \
"ds_read_b64_tr_b16 v[134:135], v242 offset:40960\n" \
"v_mfma_f32_16x16x32_f16 v[32:35], v[120:123], v[160:163], v[32:35]\n" \
"v_cvt_pk_f16_f32 v185, v186, v187\n" \
"v_cvt_pk_f16_f32 v186, v188, v189\n" \
"buffer_load_dwordx4 v232, s[36:39], s80 offen offset:1024 sc1 lds\n" \
"v_mfma_f32_16x16x32_f16 v[44:47], v[124:127], v[160:163], v[44:47]\n" \
"v_cvt_pk_f16_f32 v187, v190, v191\n" \
"v_add_u32 v184, s91, v184\n" \
"v_mfma_f32_16x16x32_f16 v[48:51], v[144:147], v[168:171], v[48:51]\n" \
"v_add_u32 v185, s91, v185\n" \
"v_add_u32 v186, s91, v186\n" \
"ds_read_b128 v[160:163], v240 offset:40960\n" \
"v_mfma_f32_16x16x32_f16 v[60:63], v[148:151], v[168:171], v[60:63]\n" \
"s_waitcnt lgkmcnt(4)\n" \
"v_pk_add_f16 v168, v168, v172\n" \
"v_pk_add_f16 v169, v169, v173\n" \
"v_add_u32 v187, s91, v187\n" \
"v_and_b32 v184, s92, v184\n" \
"s_add_i32 s82, s49, 0x2800000\n" \
"v_mfma_f32_16x16x32_f16 v[52:55], v[152:155], v[172:175], v[52:55]\n" \
"v_pk_add_f16 v170, v170, v174\n" \
"v_pk_add_f16 v171, v171, v175\n" \
"v_and_b32 v185, s92, v185\n" \
"v_and_b32 v186, s92, v186\n" \
"v_and_b32 v187, s92, v187\n" \
"v_mfma_f32_16x16x32_f16 v[64:67], v[156:159], v[172:175], v[64:67]\n" \
"ds_read_b128 v[164:167], v240 offset:41472\n" \
"buffer_store_dwordx4 v[184:187], v234, s[36:39], s82 offen sc1\n" \
"ds_read_b64_tr_b16 v[136:137], v241 offset:33280\n" \
"v_mfma_f32_16x16x32_f16 v[56:59], v[120:123], v[168:171], v[56:59]\n" \
"buffer_load_dwordx4 v232, s[36:39], s80 offen offset:2048 sc1 lds\n" \
"ds_read_b64_tr_b16 v[138:139], v241 offset:41472\n" \
"v_mfma_f32_16x16x32_f16 v[68:71], v[124:127], v[168:171], v[68:71]\n" \
"s_setprio 0\n" \
"ds_read_b64_tr_b16 v[184:185], v238 offset:0\n" \
"ds_read_b64_tr_b16 v[188:189], v239 offset:0\n" \
"v_mfma_f32_16x16x32_f16 v[72:75], v[144:147], v[176:179], v[72:75]\n" \
"ds_read_b128 v[168:171], v240 offset:43008\n" \
"ds_read_b64_tr_b16 v[140:141], v242 offset:33280\n" \
"v_mfma_f32_16x16x32_f16 v[84:87], v[148:151], v[176:179], v[84:87]\n" \
"s_waitcnt lgkmcnt(7)\n" \
"v_pk_add_f16 v176, v176, v180\n" \
"v_pk_add_f16 v177, v177, v181\n" \
"v_mfma_f32_16x16x32_f16 v[76:79], v[152:155], v[180:183], v[76:79]\n" \
"v_pk_add_f16 v178, v178, v182\n" \
"v_pk_add_f16 v179, v179, v183\n" \
"s_waitcnt lgkmcnt(3)\n" \
"v_mfma_f32_16x16x16_bf16 v[184:187], v[184:185], v[192:193], 0\n" \
"v_mfma_f32_16x16x32_f16 v[88:91], v[156:159], v[180:183], v[88:91]\n" \
"ds_read_b128 v[172:175], v240 offset:43520\n" \
"ds_read_b64_tr_b16 v[142:143], v242 offset:41472\n" \
"s_waitcnt lgkmcnt(4)\n" \
"v_mfma_f32_16x16x16_bf16 v[188:191], v[188:189], v[192:193], 0\n" \
"v_mfma_f32_16x16x32_f16 v[80:83], v[120:123], v[176:179], v[80:83]\n" \
"buffer_load_dwordx4 v232, s[36:39], s80 offen offset:3072 sc1 lds\n" \
"v_mfma_f32_16x16x32_f16 v[92:95], v[124:127], v[176:179], v[92:95]\n" \
"v_mfma_f32_16x16x32_f16 v[96:99], v[144:147], v[160:163], v[96:99]\n" \
"v_pk_add_f16 v144, v128, v136\n" \
"v_pk_add_f16 v145, v129, v137\n" \
"v_cvt_pk_f16_f32 v184, v184, v185\n" \
"v_cvt_pk_f16_f32 v185, v186, v187\n" \
"ds_read_b128 v[176:179], v240 offset:45056\n" \
"v_mfma_f32_16x16x32_f16 v[108:111], v[148:151], v[160:163], v[108:111]\n" \
"v_pk_add_f16 v160, v160, v164\n" \
"v_pk_add_f16 v161, v161, v165\n" \
"v_cvt_pk_f16_f32 v186, v188, v189\n" \
"v_cvt_pk_f16_f32 v187, v190, v191\n" \
"ds_read_b128 v[180:183], v240 offset:45568\n" \
"v_mfma_f32_16x16x32_f16 v[100:103], v[152:155], v[164:167], v[100:103]\n" \
"v_pk_add_f16 v162, v162, v166\n" \
"v_pk_add_f16 v163, v163, v167\n" \
"v_add_u32 v184, s91, v184\n" \
"v_add_u32 v185, s91, v185\n" \
"v_add_u32 v186, s91, v186\n" \
"s_add_i32 m0, s45, 0x5000\n" \
"s_add_i32 s81, s47, 0xc00000\n" \
"v_mfma_f32_16x16x32_f16 v[112:115], v[156:159], v[164:167], v[112:115]\n" \
"v_pk_add_f16 v146, v130, v138\n" \
"v_pk_add_f16 v147, v131, v139\n" \
"v_add_u32 v187, s91, v187\n" \
"v_and_b32 v184, s92, v184\n" \
"buffer_load_dwordx4 v232, s[36:39], s81 offen sc1 lds\n" \
"v_mfma_f32_16x16x32_f16 v[104:107], v[120:123], v[160:163], v[104:107]\n" \
"s_waitcnt lgkmcnt(2)\n" \
"v_pk_add_f16 v148, v132, v140\n" \
"v_pk_add_f16 v149, v133, v141\n" \
"v_and_b32 v185, s92, v185\n" \
"v_and_b32 v186, s92, v186\n" \
"v_and_b32 v187, s92, v187\n" \
"v_mfma_f32_16x16x32_f16 v[116:119], v[124:127], v[160:163], v[116:119]\n" \
"s_setprio 1\n" \
"v_pk_add_f16 v150, v134, v142\n" \
"v_pk_add_f16 v151, v135, v143\n" \
"buffer_store_dwordx4 v[184:187], v234, s[36:39], s82 offen offset:1024 sc1\n" \
"s_waitcnt vmcnt(7) lgkmcnt(0)\n" \
"s_cmp_eq_u32 s94, 0\n" \
"s_cbranch_scc1 .Lp2_%=\n" \
".Lq2_%=:\n" \
"s_memrealtime s[86:87]\n" \
"s_waitcnt lgkmcnt(0)\n" \
"s_sub_i32 s87, s86, s75\n" \
"s_cmp_lt_i32 s87, 0\n" \
"s_cbranch_scc1 .Lq2_%=\n" \
"s_lshr_b64 s[94:95], s[94:95], 8\n" \
"s_and_b32 s87, s94, 0xff\n" \
"s_add_i32 s75, s86, s87\n" \
".Lp2_%=:\n" \
"s_barrier\n" \
"v_mfma_f32_16x16x32_f16 v[0:3], v[128:131], v[168:171], v[0:3]\n" \
"ds_read_b128 v[160:163], v240 offset:6144\n" \
"ds_read_b64_tr_b16 v[184:185], v236 offset:4096\n" \
"ds_read_b64_tr_b16 v[188:189], v237 offset:4096\n" \
"v_mfma_f32_16x16x32_f16 v[12:15], v[132:135], v[168:171], v[12:15]\n" \
"v_pk_add_f16 v168, v168, v172\n" \
"v_pk_add_f16 v169, v169, v173\n" \
"v_mfma_f32_16x16x32_f16 v[4:7], v[136:139], v[172:175], v[4:7]\n" \
"v_pk_add_f16 v170, v170, v174\n" \
"v_pk_add_f16 v171, v171, v175\n" \
"buffer_store_dwordx4 v[204:207], v235, s[36:39], s62 offen sc1\n" \
"v_mfma_f32_16x16x32_f16 v[16:19], v[140:143], v[172:175], v[16:19]\n" \
"ds_read_b128 v[164:167], v240 offset:6656\n" \
"s_add_i32 m0, s44, 0xa000\n" \
"s_add_i32 s80, s46, 0x1000000\n" \
"v_mfma_f32_16x16x32_f16 v[8:11], v[144:147], v[168:171], v[8:11]\n" \
"buffer_load_dwordx4 v232, s[36:39], s80 offen sc1 lds\n" \
"s_waitcnt lgkmcnt(2)\n" \
"v_mfma_f32_16x16x16_bf16 v[184:187], v[184:185], v[192:193], 0\n" \
"v_mfma_f32_16x16x32_f16 v[20:23], v[148:151], v[168:171], v[20:23]\n" \
"ds_read_b64_tr_b16 v[120:121], v241 offset:0\n" \
"ds_read_b64_tr_b16 v[122:123], v241 offset:8192\n" \
"s_waitcnt lgkmcnt(3)\n" \
"v_mfma_f32_16x16x16_bf16 v[188:191], v[188:189], v[192:193], 0\n" \
"v_mfma_f32_16x16x32_f16 v[24:27], v[128:131], v[176:179], v[24:27]\n" \
"ds_read_b128 v[168:171], v240 offset:8192\n" \
"v_mfma_f32_16x16x32_f16 v[36:39], v[132:135], v[176:179], v[36:39]\n" \
"v_pk_add_f16 v176, v176, v180\n" \
"v_pk_add_f16 v177, v177, v181\n" \
"ds_read_b64_tr_b16 v[124:125], v242 offset:0\n" \
"v_mfma_f32_16x16x32_f16 v[28:31], v[136:139], v[180:183], v[28:31]\n" \
"v_pk_add_f16 v178, v178, v182\n" \
"v_pk_add_f16 v179, v179, v183\n" \
"v_cvt_pk_f16_f32 v184, v184, v185\n" \
"v_mfma_f32_16x16x32_f16 v[40:43], v[140:143], v[180:183], v[40:43]\n" \
"v_cvt_pk_f16_f32 v185, v186, v187\n" \
"v_cvt_pk_f16_f32 v186, v188, v189\n" \
"ds_read_b128 v[172:175], v240 offset:8704\n" \
"v_mfma_f32_16x16x32_f16 v[32:35], v[144:147], v[176:179], v[32:35]\n" \
"v_cvt_pk_f16_f32 v187, v190, v191\n" \
"v_add_u32 v184, s91, v184\n" \
"buffer_load_dwordx4 v232, s[36:39], s80 offen offset:1024 sc1 lds\n" \
"v_mfma_f32_16x16x32_f16 v[44:47], v[148:151], v[176:179], v[44:47]\n" \
"v_add_u32 v185, s91, v185\n" \
"v_add_u32 v186, s91, v186\n" \
"ds_read_b64_tr_b16 v[126:127], v242 offset:8192\n" \
"v_mfma_f32_16x16x32_f16 v[48:51], v[128:131], v[160:163], v[48:51]\n" \
"v_add_u32 v187, s91, v187\n" \
"v_and_b32 v184, s92, v184\n" \
"ds_read_b128 v[176:179], v240 offset:0\n" \
"v_mfma_f32_16x16x32_f16 v[60:63], v[132:135], v[160:163], v[60:63]\n" \
"s_waitcnt lgkmcnt(4)\n" \
"v_pk_add_f16 v160, v160, v164\n" \
"v_pk_add_f16 v161, v161, v165\n" \
"v_and_b32 v185, s92, v185\n" \
"s_add_i32 s82, s50, 0x3000000\n" \
"v_mfma_f32_16x16x32_f16 v[52:55], v[136:139], v[164:167], v[52:55]\n" \
"v_pk_add_f16 v162, v162, v166\n" \
"v_pk_add_f16 v163, v163, v167\n" \
"v_and_b32 v186, s92, v186\n" \
"v_and_b32 v187, s92, v187\n" \
"v_mfma_f32_16x16x32_f16 v[64:67], v[140:143], v[164:167], v[64:67]\n" \
"ds_read_b128 v[180:183], v240 offset:512\n" \
"buffer_store_dwordx4 v[184:187], v234, s[36:39], s82 offen sc1\n" \
"ds_read_b64_tr_b16 v[152:153], v241 offset:512\n" \
"v_mfma_f32_16x16x32_f16 v[56:59], v[144:147], v[160:163], v[56:59]\n" \
"buffer_load_dwordx4 v232, s[36:39], s80 offen offset:2048 sc1 lds\n" \
"v_mfma_f32_16x16x32_f16 v[68:71], v[148:151], v[160:163], v[68:71]\n" \
"s_setprio 0\n" \
"ds_read_b64_tr_b16 v[154:155], v241 offset:8704\n" \
"ds_read_b64_tr_b16 v[156:157], v242 offset:512\n" \
"v_mfma_f32_16x16x32_f16 v[72:75], v[128:131], v[168:171], v[72:75]\n" \
"ds_read_b128 v[160:163], v240 offset:2048\n" \
"ds_read_b64_tr_b16 v[158:159], v242 offset:8704\n" \
"v_mfma_f32_16x16x32_f16 v[84:87], v[132:135], v[168:171], v[84:87]\n" \
"s_waitcnt lgkmcnt(6)\n" \
"v_pk_add_f16 v168, v168, v172\n" \
"v_pk_add_f16 v169, v169, v173\n" \
"v_mfma_f32_16x16x32_f16 v[76:79], v[136:139], v[172:175], v[76:79]\n" \
"v_pk_add_f16 v170, v170, v174\n" \
"v_pk_add_f16 v171, v171, v175\n" \
"v_mfma_f32_16x16x32_f16 v[88:91], v[140:143], v[172:175], v[88:91]\n" \
"ds_read_b128 v[164:167], v240 offset:2560\n" \
"v_mfma_f32_16x16x32_f16 v[80:83], v[144:147], v[168:171], v[80:83]\n" \
"buffer_load_dwordx4 v232, s[36:39], s80 offen offset:3072 sc1 lds\n" \
"v_mfma_f32_16x16x32_f16 v[92:95], v[148:151], v[168:171], v[92:95]\n" \
"v_mfma_f32_16x16x32_f16 v[96:99], v[128:131], v[176:179], v[96:99]\n" \
"s_waitcnt lgkmcnt(5)\n" \
"v_pk_add_f16 v128, v120, v152\n" \
"v_pk_add_f16 v129, v121, v153\n" \
"ds_read_b128 v[168:171], v240 offset:4096\n" \
"v_mfma_f32_16x16x32_f16 v[108:111], v[132:135], v[176:179], v[108:111]\n" \
"v_pk_add_f16 v176, v176, v180\n" \
"v_pk_add_f16 v177, v177, v181\n" \
"ds_read_b128 v[172:175], v240 offset:4608\n" \
"v_mfma_f32_16x16x32_f16 v[100:103], v[136:139], v[180:183], v[100:103]\n" \
"v_pk_add_f16 v178, v178, v182\n" \
"v_pk_add_f16 v179, v179, v183\n" \
"s_add_i32 m0, s45, 0xa000\n" \
"s_add_i32 s81, s47, 0x1000000\n" \
"v_mfma_f32_16x16x32_f16 v[112:115], v[140:143], v[180:183], v[112:115]\n" \
"s_waitcnt lgkmcnt(3)\n" \
"v_pk_add_f16 v130, v122, v154\n" \
"v_pk_add_f16 v131, v123, v155\n" \
"buffer_load_dwordx4 v232, s[36:39], s81 offen sc1 lds\n" \
"v_mfma_f32_16x16x32_f16 v[104:107], v[144:147], v[176:179], v[104:107]\n" \
"v_pk_add_f16 v132, v124, v156\n" \
"v_pk_add_f16 v133, v125, v157\n" \
"v_mfma_f32_16x16x32_f16 v[116:119], v[148:151], v[176:179], v[116:119]\n" \
"s_setprio 1\n" \
"v_pk_add_f16 v134, v126, v158\n" \
"v_pk_add_f16 v135, v127, v159\n" \
"s_waitcnt vmcnt(7) lgkmcnt(0)\n" \
"s_cmp_eq_u32 s94, 0\n" \
"s_cbranch_scc1 .Lp3_%=\n" \
".Lq3_%=:\n" \
"s_memrealtime s[86:87]\n" \
"s_waitcnt lgkmcnt(0)\n" \
"s_sub_i32 s87, s86, s75\n" \
"s_cmp_lt_i32 s87, 0\n" \
"s_cbranch_scc1 .Lq3_%=\n" \
"s_lshr_b64 s[94:95], s[94:95], 8\n" \
"s_and_b32 s87, s94, 0xff\n" \
"s_add_i32 s75, s86, s87\n" \
".Lp3_%=:\n" \
"s_barrier\n" \
"v_mfma_f32_16x16x32_f16 v[0:3], v[120:123], v[160:163], v[0:3]\n" \
"ds_read_b128 v[176:179], v240 offset:26624\n" \
"ds_read_b64_tr_b16 v[184:185], v238 offset:4096\n" \
"ds_read_b64_tr_b16 v[188:189], v239 offset:4096\n" \
"v_mfma_f32_16x16x32_f16 v[12:15], v[124:127], v[160:163], v[12:15]\n" \
"v_pk_add_f16 v160, v160, v164\n" \
"v_pk_add_f16 v161, v161, v165\n" \
"v_mfma_f32_16x16x32_f16 v[4:7], v[152:155], v[164:167], v[4:7]\n" \
"v_pk_add_f16 v162, v162, v166\n" \
"v_pk_add_f16 v163, v163, v167\n" \
"v_mfma_f32_16x16x32_f16 v[16:19], v[156:159], v[164:167], v[16:19]\n" \
"ds_read_b128 v[180:183], v240 offset:27136\n" \
"s_add_i32 m0, s44, 0x0\n" \
"s_add_i32 s80, s46, 0x1400000\n" \
"v_mfma_f32_16x16x32_f16 v[8:11], v[128:131], v[160:163], v[8:11]\n" \
"buffer_load_dwordx4 v232, s[36:39], s80 offen sc1 lds\n" \
"s_waitcnt lgkmcnt(2)\n" \
"v_mfma_f32_16x16x16_bf16 v[184:187], v[184:185], v[192:193], 0\n" \
"v_mfma_f32_16x16x32_f16 v[20:23], v[132:135], v[160:163], v[20:23]\n" \
"ds_read_b64_tr_b16 v[136:137], v241 offset:16384\n" \
"ds_read_b64_tr_b16 v[138:139], v241 offset:24576\n" \
"s_waitcnt lgkmcnt(3)\n" \
"v_mfma_f32_16x16x16_bf16 v[188:191], v[188:189], v[192:193], 0\n" \
"v_mfma_f32_16x16x32_f16 v[24:27], v[120:123], v[168:171], v[24:27]\n" \
"ds_read_b128 v[160:163], v240 offset:28672\n" \
"v_mfma_f32_16x16x32_f16 v[36:39], v[124:127], v[168:171], v[36:39]\n" \
"v_pk_add_f16 v168, v168, v172\n" \
"v_pk_add_f16 v169, v169, v173\n" \
"ds_read_b64_tr_b16 v[140:141], v242 offset:16384\n" \
"v_mfma_f32_16x16x32_f16 v[28:31], v[152:155], v[172:175], v[28:31]\n" \
"v_pk_add_f16 v170, v170, v174\n" \
"v_pk_add_f16 v171, v171, v175\n" \
"v_cvt_pk_f16_f32 v184, v184, v185\n" \
"v_mfma_f32_16x16x32_f16 v[40:43], v[156:159], v[172:175], v[40:43]\n" \
"v_cvt_pk_f16_f32 v185, v186, v187\n" \
"v_cvt_pk_f16_f32 v186, v188, v189\n" \
"ds_read_b128 v[164:167], v240 offset:29184\n" \
"v_mfma_f32_16x16x32_f16 v[32:35], v[128:131], v[168:171], v[32:35]\n" \
"v_cvt_pk_f16_f32 v187, v190, v191\n" \
"v_add_u32 v184, s91, v184\n" \
"buffer_load_dwordx4 v232, s[36:39], s80 offen offset:1024 sc1 lds\n" \
"v_mfma_f32_16x16x32_f16 v[44:47], v[132:135], v[168:171], v[44:47]\n" \
"v_add_u32 v185, s91, v185\n" \
"v_add_u32 v186, s91, v186\n" \
"ds_read_b64_tr_b16 v[142:143], v242 offset:24576\n" \
"v_mfma_f32_16x16x32_f16 v[48:51], v[120:123], v[176:179], v[48:51]\n" \
"v_add_u32 v187, s91, v187\n" \
"v_and_b32 v184, s92, v184\n" \
"ds_read_b128 v[168:171], v240 offset:20480\n" \
"v_mfma_f32_16x16x32_f16 v[60:63], v[124:127], v[176:179], v[60:63]\n" \
"s_waitcnt lgkmcnt(4)\n" \
"v_pk_add_f16 v176, v176, v180\n" \
"v_pk_add_f16 v177, v177, v181\n" \
"v_and_b32 v185, s92, v185\n" \
"v_mfma_f32_16x16x32_f16 v[52:55], v[152:155], v[180:183], v[52:55]\n" \
"v_pk_add_f16 v178, v178, v182\n" \
"v_pk_add_f16 v179, v179, v183\n" \
"v_and_b32 v186, s92, v186\n" \
"v_and_b32 v187, s92, v187\n" \
"v_mfma_f32_16x16x32_f16 v[64:67], v[156:159], v[180:183], v[64:67]\n" \
"ds_read_b128 v[172:175], v240 offset:20992\n" \
"buffer_store_dwordx4 v[184:187], v234, s[36:39], s82 offen offset:1024 sc1\n" \
"ds_read_b64_tr_b16 v[144:145], v241 offset:16896\n" \
"v_mfma_f32_16x16x32_f16 v[56:59], v[128:131], v[176:179], v[56:59]\n" \
"buffer_load_dwordx4 v232, s[36:39], s80 offen offset:2048 sc1 lds\n" \
"v_mfma_f32_16x16x32_f16 v[68:71], v[132:135], v[176:179], v[68:71]\n" \
"s_setprio 0\n" \
"ds_read_b64_tr_b16 v[146:147], v241 offset:25088\n" \
"ds_read_b64_tr_b16 v[148:149], v242 offset:16896\n" \
"v_mfma_f32_16x16x32_f16 v[72:75], v[120:123], v[160:163], v[72:75]\n" \
"ds_read_b128 v[176:179], v240 offset:22528\n" \
"ds_read_b64_tr_b16 v[150:151], v242 offset:25088\n" \
"v_mfma_f32_16x16x32_f16 v[84:87], v[124:127], v[160:163], v[84:87]\n" \
"s_waitcnt lgkmcnt(6)\n" \
"v_pk_add_f16 v160, v160, v164\n" \
"v_pk_add_f16 v161, v161, v165\n" \
"v_mfma_f32_16x16x32_f16 v[76:79], v[152:155], v[164:167], v[76:79]\n" \
"v_pk_add_f16 v162, v162, v166\n" \
"v_pk_add_f16 v163, v163, v167\n" \
"v_mfma_f32_16x16x32_f16 v[88:91], v[156:159], v[164:167], v[88:91]\n" \
"ds_read_b128 v[180:183], v240 offset:23040\n" \
"v_mfma_f32_16x16x32_f16 v[80:83], v[128:131], v[160:163], v[80:83]\n" \
"buffer_load_dwordx4 v232, s[36:39], s80 offen offset:3072 sc1 lds\n" \
"v_mfma_f32_16x16x32_f16 v[92:95], v[132:135], v[160:163], v[92:95]\n" \
"v_mfma_f32_16x16x32_f16 v[96:99], v[120:123], v[168:171], v[96:99]\n" \
"s_waitcnt lgkmcnt(5)\n" \
"v_pk_add_f16 v120, v136, v144\n" \
"v_pk_add_f16 v121, v137, v145\n" \
"ds_read_b128 v[160:163], v240 offset:24576\n" \
"v_mfma_f32_16x16x32_f16 v[108:111], v[124:127], v[168:171], v[108:111]\n" \
"v_pk_add_f16 v168, v168, v172\n" \
"v_pk_add_f16 v169, v169, v173\n" \
"ds_read_b128 v[164:167], v240 offset:25088\n" \
"v_mfma_f32_16x16x32_f16 v[100:103], v[152:155], v[172:175], v[100:103]\n" \
"v_pk_add_f16 v170, v170, v174\n" \
"v_pk_add_f16 v171, v171, v175\n" \
"s_add_i32 m0, s45, 0x0\n" \
"s_add_i32 s81, s47, 0x1400000\n" \
"v_mfma_f32_16x16x32_f16 v[112:115], v[156:159], v[172:175], v[112:115]\n" \
"s_waitcnt lgkmcnt(3)\n" \
"v_pk_add_f16 v122, v138, v146\n" \
"v_pk_add_f16 v123, v139, v147\n" \
"buffer_load_dwordx4 v232, s[36:39], s81 offen sc1 lds\n" \
"v_mfma_f32_16x16x32_f16 v[104:107], v[128:131], v[168:171], v[104:107]\n" \
"v_pk_add_f16 v124, v140, v148\n" \
"v_pk_add_f16 v125, v141, v149\n" \
"v_mfma_f32_16x16x32_f16 v[116:119], v[132:135], v[168:171], v[116:119]\n" \
"s_setprio 1\n" \
"v_pk_add_f16 v126, v142, v150\n" \
"v_pk_add_f16 v127, v143, v151\n" \
"s_waitcnt vmcnt(6) lgkmcnt(0)\n" \
"s_cmp_eq_u32 s94, 0\n" \
"s_cbranch_scc1 .Lp4_%=\n" \
".Lq4_%=:\n" \
"s_memrealtime s[86:87]\n" \
"s_waitcnt lgkmcnt(0)\n" \
"s_sub_i32 s87, s86, s75\n" \
"s_cmp_lt_i32 s87, 0\n" \
"s_cbranch_scc1 .Lq4_%=\n" \
"s_lshr_b64 s[94:95], s[94:95], 8\n" \
"s_and_b32 s87, s94, 0xff\n" \
"s_add_i32 s75, s86, s87\n" \
".Lp4_%=:\n" \
"s_barrier\n" \
"v_mfma_f32_16x16x32_f16 v[0:3], v[136:139], v[176:179], v[0:3]\n" \
"ds_read_b128 v[168:171], v240 offset:47104\n" \
"ds_read_b64_tr_b16 v[184:185], v236 offset:8192\n" \
"ds_read_b64_tr_b16 v[188:189], v237 offset:8192\n" \
"v_mfma_f32_16x16x32_f16 v[12:15], v[140:143], v[176:179], v[12:15]\n" \
"v_pk_add_f16 v176, v176, v180\n" \
"v_pk_add_f16 v177, v177, v181\n" \
"s_mov_b32 s84, 0x8004000\n" \
"v_mfma_f32_16x16x32_f16 v[4:7], v[144:147], v[180:183], v[4:7]\n" \
"v_pk_add_f16 v178, v178, v182\n" \
"v_pk_add_f16 v179, v179, v183\n" \
"buffer_load_dword v253, v252, s[36:39], s84 offen sc1\n" \
"buffer_store_dwordx4 v[208:211], v235, s[36:39], s63 offen sc1\n" \
"v_mfma_f32_16x16x32_f16 v[16:19], v[148:151], v[180:183], v[16:19]\n" \
"ds_read_b128 v[172:175], v240 offset:47616\n" \
"s_add_i32 m0, s44, 0x5000\n" \
"s_add_i32 s80, s46, 0x1800000\n" \
"v_mfma_f32_16x16x32_f16 v[8:11], v[120:123], v[176:179], v[8:11]\n" \
"buffer_load_dwordx4 v232, s[36:39], s80 offen sc1 lds\n" \
"s_waitcnt lgkmcnt(2)\n" \
"v_mfma_f32_16x16x16_bf16 v[184:187], v[184:185], v[192:193], 0\n" \
"v_mfma_f32_16x16x32_f16 v[20:23], v[124:127], v[176:179], v[20:23]\n" \
"ds_read_b64_tr_b16 v[128:129], v241 offset:32768\n" \
"ds_read_b64_tr_b16 v[130:131], v241 offset:40960\n" \
"s_waitcnt lgkmcnt(3)\n" \
"v_mfma_f32_16x16x16_bf16 v[188:191], v[188:189], v[192:193], 0\n" \
"v_mfma_f32_16x16x32_f16 v[24:27], v[136:139], v[160:163], v[24:27]\n" \
"ds_read_b128 v[176:179], v240 offset:49152\n" \
"v_mfma_f32_16x16x32_f16 v[36:39], v[140:143], v[160:163], v[36:39]\n" \
"v_pk_add_f16 v160, v160, v164\n" \
"v_pk_add_f16 v161, v161, v165\n" \
"ds_read_b64_tr_b16 v[132:133], v242 offset:32768\n" \
"v_mfma_f32_16x16x32_f16 v[28:31], v[144:147], v[164:167], v[28:31]\n" \
"v_pk_add_f16 v162, v162, v166\n" \
"v_pk_add_f16 v163, v163, v167\n" \
"v_cvt_pk_f16_f32 v184, v184, v185\n" \
"v_mfma_f32_16x16x32_f16 v[40:43], v[148:151], v[164:167], v[40:43]\n" \
"v_cvt_pk_f16_f32 v185, v186, v187\n" \
"v_cvt_pk_f16_f32 v186, v188, v189\n" \
"ds_read_b128 v[180:183], v240 offset:49664\n" \
"v_mfma_f32_16x16x32_f16 v[32:35], v[120:123], v[160:163], v[32:35]\n" \
"v_cvt_pk_f16_f32 v187, v190, v191\n" \
"v_add_u32 v184, s91, v184\n" \
"buffer_load_dwordx4 v232, s[36:39], s80 offen offset:1024 sc1 lds\n" \
"v_mfma_f32_16x16x32_f16 v[44:47], v[124:127], v[160:163], v[44:47]\n" \
"v_add_u32 v185, s91, v185\n" \
"v_add_u32 v186, s91, v186\n" \
"ds_read_b64_tr_b16 v[134:135], v242 offset:40960\n" \
"v_mfma_f32_16x16x32_f16 v[48:51], v[136:139], v[168:171], v[48:51]\n" \
"v_add_u32 v187, s91, v187\n" \
"v_and_b32 v184, s92, v184\n" \
"ds_read_b128 v[160:163], v240 offset:40960\n" \
"v_mfma_f32_16x16x32_f16 v[60:63], v[140:143], v[168:171], v[60:63]\n" \
"s_waitcnt lgkmcnt(4)\n" \
"v_pk_add_f16 v168, v168, v172\n" \
"v_pk_add_f16 v169, v169, v173\n" \
"v_and_b32 v185, s92, v185\n" \
"s_add_i32 s82, s51, 0x3800000\n" \
"v_mfma_f32_16x16x32_f16 v[52:55], v[144:147], v[172:175], v[52:55]\n" \
"v_pk_add_f16 v170, v170, v174\n" \
"v_pk_add_f16 v171, v171, v175\n" \
"v_and_b32 v186, s92, v186\n" \
"v_and_b32 v187, s92, v187\n" \
"v_mfma_f32_16x16x32_f16 v[64:67], v[148:151], v[172:175], v[64:67]\n" \
"ds_read_b128 v[164:167], v240 offset:41472\n" \
"buffer_store_dwordx4 v[184:187], v234, s[36:39], s82 offen sc1\n" \
"ds_read_b64_tr_b16 v[152:153], v241 offset:33280\n" \
"v_mfma_f32_16x16x32_f16 v[56:59], v[120:123], v[168:171], v[56:59]\n" \
"buffer_load_dwordx4 v232, s[36:39], s80 offen offset:2048 sc1 lds\n" \
"v_mfma_f32_16x16x32_f16 v[68:71], v[124:127], v[168:171], v[68:71]\n" \
"s_setprio 0\n" \
"ds_read_b64_tr_b16 v[154:155], v241 offset:41472\n" \
"ds_read_b64_tr_b16 v[156:157], v242 offset:33280\n" \
"v_mfma_f32_16x16x32_f16 v[72:75], v[136:139], v[176:179], v[72:75]\n" \
"ds_read_b128 v[168:171], v240 offset:43008\n" \
"ds_read_b64_tr_b16 v[158:159], v242 offset:41472\n" \
"v_mfma_f32_16x16x32_f16 v[84:87], v[140:143], v[176:179], v[84:87]\n" \
"s_waitcnt lgkmcnt(6)\n" \
"v_pk_add_f16 v176, v176, v180\n" \
"v_pk_add_f16 v177, v177, v181\n" \
"v_mfma_f32_16x16x32_f16 v[76:79], v[144:147], v[180:183], v[76:79]\n" \
"v_pk_add_f16 v178, v178, v182\n" \
"v_pk_add_f16 v179, v179, v183\n" \
"v_mfma_f32_16x16x32_f16 v[88:91], v[148:151], v[180:183], v[88:91]\n" \
"ds_read_b128 v[172:175], v240 offset:43520\n" \
"v_mfma_f32_16x16x32_f16 v[80:83], v[120:123], v[176:179], v[80:83]\n" \
"buffer_load_dwordx4 v232, s[36:39], s80 offen offset:3072 sc1 lds\n" \
"v_mfma_f32_16x16x32_f16 v[92:95], v[124:127], v[176:179], v[92:95]\n" \
"v_mfma_f32_16x16x32_f16 v[96:99], v[136:139], v[160:163], v[96:99]\n" \
"s_waitcnt lgkmcnt(5)\n" \
"v_pk_add_f16 v136, v128, v152\n" \
"v_pk_add_f16 v137, v129, v153\n" \
"ds_read_b128 v[176:179], v240 offset:45056\n" \
"v_mfma_f32_16x16x32_f16 v[108:111], v[140:143], v[160:163], v[108:111]\n" \
"v_pk_add_f16 v160, v160, v164\n" \
"v_pk_add_f16 v161, v161, v165\n" \
"ds_read_b128 v[180:183], v240 offset:45568\n" \
"v_mfma_f32_16x16x32_f16 v[100:103], v[144:147], v[164:167], v[100:103]\n" \
"v_pk_add_f16 v162, v162, v166\n" \
"v_pk_add_f16 v163, v163, v167\n" \
"s_waitcnt vmcnt(6)\n" \
"v_cmp_eq_u32 vcc, 0, v253\n" \
"s_cbranch_vccnz .Ls12_%=\n" \
".Lr12_%=:\n" \
"s_add_i32 m0, s45, 0x5000\n" \
"s_add_i32 s81, s47, 0x1800000\n" \
"v_mfma_f32_16x16x32_f16 v[112:115], v[148:151], v[164:167], v[112:115]\n" \
"s_waitcnt lgkmcnt(3)\n" \
"v_pk_add_f16 v138, v130, v154\n" \
"v_pk_add_f16 v139, v131, v155\n" \
"buffer_load_dwordx4 v232, s[36:39], s81 offen sc1 lds\n" \
"v_mfma_f32_16x16x32_f16 v[104:107], v[120:123], v[160:163], v[104:107]\n" \
"v_pk_add_f16 v140, v132, v156\n" \
"v_pk_add_f16 v141, v133, v157\n" \
"v_mfma_f32_16x16x32_f16 v[116:119], v[124:127], v[160:163], v[116:119]\n" \
"s_setprio 1\n" \
"v_pk_add_f16 v142, v134, v158\n" \
"v_pk_add_f16 v143, v135, v159\n" \
"s_waitcnt vmcnt(7) lgkmcnt(0)\n" \
"s_cmp_eq_u32 s94, 0\n" \
"s_cbranch_scc1 .Lp5_%=\n" \
".Lq5_%=:\n" \
"s_memrealtime s[86:87]\n" \
"s_waitcnt lgkmcnt(0)\n" \
"s_sub_i32 s87, s86, s75\n" \
"s_cmp_lt_i32 s87, 0\n" \
"s_cbranch_scc1 .Lq5_%=\n" \
"s_lshr_b64 s[94:95], s[94:95], 8\n" \
"s_and_b32 s87, s94, 0xff\n" \
"s_add_i32 s75, s86, s87\n" \
".Lp5_%=:\n" \
"s_barrier\n" \
"v_mfma_f32_16x16x32_f16 v[0:3], v[128:131], v[168:171], v[0:3]\n" \
"ds_read_b128 v[160:163], v240 offset:6144\n" \
"ds_read_b64_tr_b16 v[184:185], v238 offset:8192\n" \
"ds_read_b64_tr_b16 v[188:189], v239 offset:8192\n" \
"v_mfma_f32_16x16x32_f16 v[12:15], v[132:135], v[168:171], v[12:15]\n" \
"v_pk_add_f16 v168, v168, v172\n" \
"v_pk_add_f16 v169, v169, v173\n" \
"v_mfma_f32_16x16x32_f16 v[4:7], v[152:155], v[172:175], v[4:7]\n" \
"v_pk_add_f16 v170, v170, v174\n" \
"v_pk_add_f16 v171, v171, v175\n" \
"v_mfma_f32_16x16x32_f16 v[16:19], v[156:159], v[172:175], v[16:19]\n" \
"ds_read_b128 v[164:167], v240 offset:6656\n" \
"s_add_i32 m0, s44, 0xa000\n" \
"s_add_i32 s80, s46, 0x1c00000\n" \
"v_mfma_f32_16x16x32_f16 v[8:11], v[136:139], v[168:171], v[8:11]\n" \
"buffer_load_dwordx4 v232, s[36:39], s80 offen sc1 lds\n" \
"s_waitcnt lgkmcnt(2)\n" \
"v_mfma_f32_16x16x16_bf16 v[184:187], v[184:185], v[192:193], 0\n" \
"v_mfma_f32_16x16x32_f16 v[20:23], v[140:143], v[168:171], v[20:23]\n" \
"ds_read_b64_tr_b16 v[120:121], v241 offset:0\n" \
"ds_read_b64_tr_b16 v[122:123], v241 offset:8192\n" \
"s_waitcnt lgkmcnt(3)\n" \
"v_mfma_f32_16x16x16_bf16 v[188:191], v[188:189], v[192:193], 0\n" \
"v_mfma_f32_16x16x32_f16 v[24:27], v[128:131], v[176:179], v[24:27]\n" \
"ds_read_b128 v[168:171], v240 offset:8192\n" \
"v_mfma_f32_16x16x32_f16 v[36:39], v[132:135], v[176:179], v[36:39]\n" \
"v_pk_add_f16 v176, v176, v180\n" \
"v_pk_add_f16 v177, v177, v181\n" \
"ds_read_b64_tr_b16 v[124:125], v242 offset:0\n" \
"v_mfma_f32_16x16x32_f16 v[28:31], v[152:155], v[180:183], v[28:31]\n" \
"v_pk_add_f16 v178, v178, v182\n" \
"v_pk_add_f16 v179, v179, v183\n" \
"v_cvt_pk_f16_f32 v184, v184, v185\n" \
"v_mfma_f32_16x16x32_f16 v[40:43], v[156:159], v[180:183], v[40:43]\n" \
"v_cvt_pk_f16_f32 v185, v186, v187\n" \
"v_cvt_pk_f16_f32 v186, v188, v189\n" \
"ds_read_b128 v[172:175], v240 offset:8704\n" \
"v_mfma_f32_16x16x32_f16 v[32:35], v[136:139], v[176:179], v[32:35]\n" \
"v_cvt_pk_f16_f32 v187, v190, v191\n" \
"v_add_u32 v184, s91, v184\n" \
"buffer_load_dwordx4 v232, s[36:39], s80 offen offset:1024 sc1 lds\n" \
"v_mfma_f32_16x16x32_f16 v[44:47], v[140:143], v[176:179], v[44:47]\n" \
"v_add_u32 v185, s91, v185\n" \
"v_add_u32 v186, s91, v186\n" \
"ds_read_b64_tr_b16 v[126:127], v242 offset:8192\n" \
"v_mfma_f32_16x16x32_f16 v[48:51], v[128:131], v[160:163], v[48:51]\n" \
"v_add_u32 v187, s91, v187\n" \
"v_and_b32 v184, s92, v184\n" \
"ds_read_b128 v[176:179], v240 offset:0\n" \
"v_mfma_f32_16x16x32_f16 v[60:63], v[132:135], v[160:163], v[60:63]\n" \
"s_waitcnt lgkmcnt(4)\n" \
"v_pk_add_f16 v160, v160, v164\n" \
"v_pk_add_f16 v161, v161, v165\n" \
"v_and_b32 v185, s92, v185\n" \
"v_mfma_f32_16x16x32_f16 v[52:55], v[152:155], v[164:167], v[52:55]\n" \
"v_pk_add_f16 v162, v162, v166\n" \
"v_pk_add_f16 v163, v163, v167\n" \
"v_and_b32 v186, s92, v186\n" \
"v_and_b32 v187, s92, v187\n" \
"v_mfma_f32_16x16x32_f16 v[64:67], v[156:159], v[164:167], v[64:67]\n" \
"ds_read_b128 v[180:183], v240 offset:512\n" \
"buffer_store_dwordx4 v[184:187], v234, s[36:39], s82 offen offset:1024 sc1\n" \
"ds_read_b64_tr_b16 v[144:145], v241 offset:512\n" \
"v_mfma_f32_16x16x32_f16 v[56:59], v[136:139], v[160:163], v[56:59]\n" \
"buffer_load_dwordx4 v232, s[36:39], s80 offen offset:2048 sc1 lds\n" \
"v_mfma_f32_16x16x32_f16 v[68:71], v[140:143], v[160:163], v[68:71]\n" \
"s_setprio 0\n" \
"ds_read_b64_tr_b16 v[146:147], v241 offset:8704\n" \
"ds_read_b64_tr_b16 v[148:149], v242 offset:512\n" \
"v_mfma_f32_16x16x32_f16 v[72:75], v[128:131], v[168:171], v[72:75]\n" \
"ds_read_b128 v[160:163], v240 offset:2048\n" \
"ds_read_b64_tr_b16 v[150:151], v242 offset:8704\n" \
"v_mfma_f32_16x16x32_f16 v[84:87], v[132:135], v[168:171], v[84:87]\n" \
"s_waitcnt lgkmcnt(6)\n" \
"v_pk_add_f16 v168, v168, v172\n" \
"v_pk_add_f16 v169, v169, v173\n" \
"v_mfma_f32_16x16x32_f16 v[76:79], v[152:155], v[172:175], v[76:79]\n" \
"v_pk_add_f16 v170, v170, v174\n" \
"v_pk_add_f16 v171, v171, v175\n" \
"v_mfma_f32_16x16x32_f16 v[88:91], v[156:159], v[172:175], v[88:91]\n" \
"ds_read_b128 v[164:167], v240 offset:2560\n" \
"v_mfma_f32_16x16x32_f16 v[80:83], v[136:139], v[168:171], v[80:83]\n" \
"buffer_load_dwordx4 v232, s[36:39], s80 offen offset:3072 sc1 lds\n" \
"v_mfma_f32_16x16x32_f16 v[92:95], v[140:143], v[168:171], v[92:95]\n" \
"v_mfma_f32_16x16x32_f16 v[96:99], v[128:131], v[176:179], v[96:99]\n" \
"s_waitcnt lgkmcnt(5)\n" \
"v_pk_add_f16 v128, v120, v144\n" \
"v_pk_add_f16 v129, v121, v145\n" \
"ds_read_b128 v[168:171], v240 offset:4096\n" \
"v_mfma_f32_16x16x32_f16 v[108:111], v[132:135], v[176:179], v[108:111]\n" \
"v_pk_add_f16 v176, v176, v180\n" \
"v_pk_add_f16 v177, v177, v181\n" \
"ds_read_b128 v[172:175], v240 offset:4608\n" \
"v_mfma_f32_16x16x32_f16 v[100:103], v[152:155], v[180:183], v[100:103]\n" \
"v_pk_add_f16 v178, v178, v182\n" \
"v_pk_add_f16 v179, v179, v183\n" \
"s_add_i32 m0, s45, 0xa000\n" \
"s_add_i32 s81, s47, 0x1c00000\n" \
"v_mfma_f32_16x16x32_f16 v[112:115], v[156:159], v[180:183], v[112:115]\n" \
"s_waitcnt lgkmcnt(3)\n" \
"v_pk_add_f16 v130, v122, v146\n" \
"v_pk_add_f16 v131, v123, v147\n" \
"buffer_load_dwordx4 v232, s[36:39], s81 offen sc1 lds\n" \
"v_mfma_f32_16x16x32_f16 v[104:107], v[136:139], v[176:179], v[104:107]\n" \
"v_pk_add_f16 v132, v124, v148\n" \
"v_pk_add_f16 v133, v125, v149\n" \
"v_mfma_f32_16x16x32_f16 v[116:119], v[140:143], v[176:179], v[116:119]\n" \
"s_setprio 1\n" \
"v_pk_add_f16 v134, v126, v150\n" \
"v_pk_add_f16 v135, v127, v151\n" \
"s_waitcnt vmcnt(6) lgkmcnt(0)\n" \
"s_cmp_eq_u32 s94, 0\n" \
"s_cbranch_scc1 .Lp6_%=\n" \
".Lq6_%=:\n" \
"s_memrealtime s[86:87]\n" \
"s_waitcnt lgkmcnt(0)\n" \
"s_sub_i32 s87, s86, s75\n" \
"s_cmp_lt_i32 s87, 0\n" \
"s_cbranch_scc1 .Lq6_%=\n" \
"s_lshr_b64 s[94:95], s[94:95], 8\n" \
"s_and_b32 s87, s94, 0xff\n" \
"s_add_i32 s75, s86, s87\n" \
".Lp6_%=:\n" \
"s_barrier\n" \
"v_mfma_f32_16x16x32_f16 v[0:3], v[120:123], v[160:163], v[0:3]\n" \
"ds_read_b128 v[176:179], v240 offset:26624\n" \
"ds_read_b64_tr_b16 v[184:185], v236 offset:12288\n" \
"ds_read_b64_tr_b16 v[188:189], v237 offset:12288\n" \
"v_mfma_f32_16x16x32_f16 v[12:15], v[124:127], v[160:163], v[12:15]\n" \
"v_pk_add_f16 v160, v160, v164\n" \
"v_pk_add_f16 v161, v161, v165\n" \
"v_mfma_f32_16x16x32_f16 v[4:7], v[144:147], v[164:167], v[4:7]\n" \
"v_pk_add_f16 v162, v162, v166\n" \
"v_pk_add_f16 v163, v163, v167\n" \
"buffer_store_dwordx4 v[212:215], v235, s[36:39], s64 offen sc1\n" \
"v_mfma_f32_16x16x32_f16 v[16:19], v[148:151], v[164:167], v[16:19]\n" \
"ds_read_b128 v[180:183], v240 offset:27136\n" \
"s_add_i32 m0, s44, 0x0\n" \
"s_add_i32 s80, s46, 0x2000000\n" \
"v_mfma_f32_16x16x32_f16 v[8:11], v[128:131], v[160:163], v[8:11]\n" \
"buffer_load_dwordx4 v232, s[36:39], s80 offen sc1 lds\n" \
"s_waitcnt lgkmcnt(2)\n" \
"v_mfma_f32_16x16x16_bf16 v[184:187], v[184:185], v[192:193], 0\n" \
"v_mfma_f32_16x16x32_f16 v[20:23], v[132:135], v[160:163], v[20:23]\n" \
"ds_read_b64_tr_b16 v[136:137], v241 offset:16384\n" \
"ds_read_b64_tr_b16 v[138:139], v241 offset:24576\n" \
"s_waitcnt lgkmcnt(3)\n" \
"v_mfma_f32_16x16x16_bf16 v[188:191], v[188:189], v[192:193], 0\n" \
"v_mfma_f32_16x16x32_f16 v[24:27], v[120:123], v[168:171], v[24:27]\n" \
"ds_read_b128 v[160:163], v240 offset:28672\n" \
"v_mfma_f32_16x16x32_f16 v[36:39], v[124:127], v[168:171], v[36:39]\n" \
"v_pk_add_f16 v168, v168, v172\n" \
"v_pk_add_f16 v169, v169, v173\n" \
"ds_read_b64_tr_b16 v[140:141], v242 offset:16384\n" \
"v_mfma_f32_16x16x32_f16 v[28:31], v[144:147], v[172:175], v[28:31]\n" \
"v_pk_add_f16 v170, v170, v174\n" \
"v_pk_add_f16 v171, v171, v175\n" \
"v_cvt_pk_f16_f32 v184, v184, v185\n" \
"v_mfma_f32_16x16x32_f16 v[40:43], v[148:151], v[172:175], v[40:43]\n" \
"v_cvt_pk_f16_f32 v185, v186, v187\n" \
"v_cvt_pk_f16_f32 v186, v188, v189\n" \
"ds_read_b128 v[164:167], v240 offset:29184\n" \
"v_mfma_f32_16x16x32_f16 v[32:35], v[128:131], v[168:171], v[32:35]\n" \
"v_cvt_pk_f16_f32 v187, v190, v191\n" \
"v_add_u32 v184, s91, v184\n" \
"buffer_load_dwordx4 v232, s[36:39], s80 offen offset:1024 sc1 lds\n" \
"v_mfma_f32_16x16x32_f16 v[44:47], v[132:135], v[168:171], v[44:47]\n" \
"v_add_u32 v185, s91, v185\n" \
"v_add_u32 v186, s91, v186\n" \
"ds_read_b64_tr_b16 v[142:143], v242 offset:24576\n" \
"v_mfma_f32_16x16x32_f16 v[48:51], v[120:123], v[176:179], v[48:51]\n" \
"v_add_u32 v187, s91, v187\n" \
"v_and_b32 v184, s92, v184\n" \
"ds_read_b128 v[168:171], v240 offset:20480\n" \
"v_mfma_f32_16x16x32_f16 v[60:63], v[124:127], v[176:179], v[60:63]\n" \
"s_waitcnt lgkmcnt(4)\n" \
"v_pk_add_f16 v176, v176, v180\n" \
"v_pk_add_f16 v177, v177, v181\n" \
"v_and_b32 v185, s92, v185\n" \
"s_add_i32 s82, s48, 0x4000000\n" \
"v_mfma_f32_16x16x32_f16 v[52:55], v[144:147], v[180:183], v[52:55]\n" \
"v_pk_add_f16 v178, v178, v182\n" \
"v_pk_add_f16 v179, v179, v183\n" \
"v_and_b32 v186, s92, v186\n" \
"v_and_b32 v187, s92, v187\n" \
"v_mfma_f32_16x16x32_f16 v[64:67], v[148:151], v[180:183], v[64:67]\n" \
"ds_read_b128 v[172:175], v240 offset:20992\n" \
"buffer_store_dwordx4 v[184:187], v234, s[36:39], s82 offen sc1\n" \
"ds_read_b64_tr_b16 v[152:153], v241 offset:16896\n" \
"v_mfma_f32_16x16x32_f16 v[56:59], v[128:131], v[176:179], v[56:59]\n" \
"buffer_load_dwordx4 v232, s[36:39], s80 offen offset:2048 sc1 lds\n" \
"v_mfma_f32_16x16x32_f16 v[68:71], v[132:135], v[176:179], v[68:71]\n" \
"s_setprio 0\n" \
"ds_read_b64_tr_b16 v[154:155], v241 offset:25088\n" \
"ds_read_b64_tr_b16 v[156:157], v242 offset:16896\n" \
"v_mfma_f32_16x16x32_f16 v[72:75], v[120:123], v[160:163], v[72:75]\n" \
"ds_read_b128 v[176:179], v240 offset:22528\n" \
"ds_read_b64_tr_b16 v[158:159], v242 offset:25088\n" \
"v_mfma_f32_16x16x32_f16 v[84:87], v[124:127], v[160:163], v[84:87]\n" \
"s_waitcnt lgkmcnt(6)\n" \
"v_pk_add_f16 v160, v160, v164\n" \
"v_pk_add_f16 v161, v161, v165\n" \
"v_mfma_f32_16x16x32_f16 v[76:79], v[144:147], v[164:167], v[76:79]\n" \
"v_pk_add_f16 v162, v162, v166\n" \
"v_pk_add_f16 v163, v163, v167\n" \
"v_mfma_f32_16x16x32_f16 v[88:91], v[148:151], v[164:167], v[88:91]\n" \
"ds_read_b128 v[180:183], v240 offset:23040\n" \
"v_mfma_f32_16x16x32_f16 v[80:83], v[128:131], v[160:163], v[80:83]\n" \
"buffer_load_dwordx4 v232, s[36:39], s80 offen offset:3072 sc1 lds\n" \
"v_mfma_f32_16x16x32_f16 v[92:95], v[132:135], v[160:163], v[92:95]\n" \
"v_mfma_f32_16x16x32_f16 v[96:99], v[120:123], v[168:171], v[96:99]\n" \
"s_waitcnt lgkmcnt(5)\n" \
"v_pk_add_f16 v120, v136, v152\n" \
"v_pk_add_f16 v121, v137, v153\n" \
"ds_read_b128 v[160:163], v240 offset:24576\n" \
"v_mfma_f32_16x16x32_f16 v[108:111], v[124:127], v[168:171], v[108:111]\n" \
"v_pk_add_f16 v168, v168, v172\n" \
"v_pk_add_f16 v169, v169, v173\n" \
"ds_read_b128 v[164:167], v240 offset:25088\n" \
"v_mfma_f32_16x16x32_f16 v[100:103], v[144:147], v[172:175], v[100:103]\n" \
"v_pk_add_f16 v170, v170, v174\n" \
"v_pk_add_f16 v171, v171, v175\n" \
"s_add_i32 m0, s45, 0x0\n" \
"s_add_i32 s81, s47, 0x2000000\n" \
"v_mfma_f32_16x16x32_f16 v[112:115], v[148:151], v[172:175], v[112:115]\n" \
"s_waitcnt lgkmcnt(3)\n" \
"v_pk_add_f16 v122, v138, v154\n" \
"v_pk_add_f16 v123, v139, v155\n" \
"buffer_load_dwordx4 v232, s[36:39], s81 offen sc1 lds\n" \
"v_mfma_f32_16x16x32_f16 v[104:107], v[128:131], v[168:171], v[104:107]\n" \
"v_pk_add_f16 v124, v140, v156\n" \
"v_pk_add_f16 v125, v141, v157\n" \
"v_mfma_f32_16x16x32_f16 v[116:119], v[132:135], v[168:171], v[116:119]\n" \
"s_setprio 1\n" \
"v_pk_add_f16 v126, v142, v158\n" \
"v_pk_add_f16 v127, v143, v159\n" \
"s_waitcnt vmcnt(7) lgkmcnt(0)\n" \
"s_cmp_eq_u32 s94, 0\n" \
"s_cbranch_scc1 .Lp7_%=\n" \
".Lq7_%=:\n" \
"s_memrealtime s[86:87]\n" \
"s_waitcnt lgkmcnt(0)\n" \
"s_sub_i32 s87, s86, s75\n" \
"s_cmp_lt_i32 s87, 0\n" \
"s_cbranch_scc1 .Lq7_%=\n" \
"s_lshr_b64 s[94:95], s[94:95], 8\n" \
"s_and_b32 s87, s94, 0xff\n" \
"s_add_i32 s75, s86, s87\n" \
".Lp7_%=:\n" \
"s_barrier\n" \
"v_mfma_f32_16x16x32_f16 v[0:3], v[136:139], v[176:179], v[0:3]\n" \
"ds_read_b128 v[168:171], v240 offset:47104\n" \
"ds_read_b64_tr_b16 v[184:185], v238 offset:12288\n" \
"ds_read_b64_tr_b16 v[188:189], v239 offset:12288\n" \
"v_mfma_f32_16x16x32_f16 v[12:15], v[140:143], v[176:179], v[12:15]\n" \
"v_pk_add_f16 v176, v176, v180\n" \
"v_pk_add_f16 v177, v177, v181\n" \
"v_mfma_f32_16x16x32_f16 v[4:7], v[152:155], v[180:183], v[4:7]\n" \
"v_pk_add_f16 v178, v178, v182\n" \
"v_pk_add_f16 v179, v179, v183\n" \
"v_mfma_f32_16x16x32_f16 v[16:19], v[156:159], v[180:183], v[16:19]\n" \
"ds_read_b128 v[172:175], v240 offset:47616\n" \
"s_add_i32 m0, s44, 0x5000\n" \
"s_add_i32 s80, s46, 0x2400000\n" \
"v_mfma_f32_16x16x32_f16 v[8:11], v[120:123], v[176:179], v[8:11]\n" \
"buffer_load_dwordx4 v232, s[36:39], s80 offen sc1 lds\n" \
"s_waitcnt lgkmcnt(2)\n" \
"v_mfma_f32_16x16x16_bf16 v[184:187], v[184:185], v[192:193], 0\n" \
"v_mfma_f32_16x16x32_f16 v[20:23], v[124:127], v[176:179], v[20:23]\n" \
"ds_read_b64_tr_b16 v[128:129], v241 offset:32768\n" \
"ds_read_b64_tr_b16 v[130:131], v241 offset:40960\n" \
"s_waitcnt lgkmcnt(3)\n" \
"v_mfma_f32_16x16x16_bf16 v[188:191], v[188:189], v[192:193], 0\n" \
"v_mfma_f32_16x16x32_f16 v[24:27], v[136:139], v[160:163], v[24:27]\n" \
"ds_read_b128 v[176:179], v240 offset:49152\n" \
"v_mfma_f32_16x16x32_f16 v[36:39], v[140:143], v[160:163], v[36:39]\n" \
"v_pk_add_f16 v160, v160, v164\n" \
"v_pk_add_f16 v161, v161, v165\n" \
"ds_read_b64_tr_b16 v[132:133], v242 offset:32768\n" \
"v_mfma_f32_16x16x32_f16 v[28:31], v[152:155], v[164:167], v[28:31]\n" \
"v_pk_add_f16 v162, v162, v166\n" \
"v_pk_add_f16 v163, v163, v167\n" \
"v_cvt_pk_f16_f32 v184, v184, v185\n" \
"v_mfma_f32_16x16x32_f16 v[40:43], v[156:159], v[164:167], v[40:43]\n" \
"v_cvt_pk_f16_f32 v185, v186, v187\n" \
"v_cvt_pk_f16_f32 v186, v188, v189\n" \
"ds_read_b128 v[180:183], v240 offset:49664\n" \
"v_mfma_f32_16x16x32_f16 v[32:35], v[120:123], v[160:163], v[32:35]\n" \
"v_cvt_pk_f16_f32 v187, v190, v191\n" \
"v_add_u32 v184, s91, v184\n" \
"buffer_load_dwordx4 v232, s[36:39], s80 offen offset:1024 sc1 lds\n" \
"v_mfma_f32_16x16x32_f16 v[44:47], v[124:127], v[160:163], v[44:47]\n" \
"v_add_u32 v185, s91, v185\n" \
"v_add_u32 v186, s91, v186\n" \
"ds_read_b64_tr_b16 v[134:135], v242 offset:40960\n" \
"v_mfma_f32_16x16x32_f16 v[48:51], v[136:139], v[168:171], v[48:51]\n" \
"v_add_u32 v187, s91, v187\n" \
"v_and_b32 v184, s92, v184\n" \
"ds_read_b128 v[160:163], v240 offset:40960\n" \
"v_mfma_f32_16x16x32_f16 v[60:63], v[140:143], v[168:171], v[60:63]\n" \
"s_waitcnt lgkmcnt(4)\n" \
"v_pk_add_f16 v168, v168, v172\n" \
"v_pk_add_f16 v169, v169, v173\n" \
"v_and_b32 v185, s92, v185\n" \
"v_mfma_f32_16x16x32_f16 v[52:55], v[152:155], v[172:175], v[52:55]\n" \
"v_pk_add_f16 v170, v170, v174\n" \
"v_pk_add_f16 v171, v171, v175\n" \
"v_and_b32 v186, s92, v186\n" \
"v_and_b32 v187, s92, v187\n" \
"v_mfma_f32_16x16x32_f16 v[64:67], v[156:159], v[172:175], v[64:67]\n" \
"ds_read_b128 v[164:167], v240 offset:41472\n" \
"buffer_store_dwordx4 v[184:187], v234, s[36:39], s82 offen offset:1024 sc1\n" \
"ds_read_b64_tr_b16 v[144:145], v241 offset:33280\n" \
"v_mfma_f32_16x16x32_f16 v[56:59], v[120:123], v[168:171], v[56:59]\n" \
"buffer_load_dwordx4 v232, s[36:39], s80 offen offset:2048 sc1 lds\n" \
"v_mfma_f32_16x16x32_f16 v[68:71], v[124:127], v[168:171], v[68:71]\n" \
"s_setprio 0\n" \
"ds_read_b64_tr_b16 v[146:147], v241 offset:41472\n" \
"ds_read_b64_tr_b16 v[148:149], v242 offset:33280\n" \
"v_mfma_f32_16x16x32_f16 v[72:75], v[136:139], v[176:179], v[72:75]\n" \
"ds_read_b128 v[168:171], v240 offset:43008\n" \
"ds_read_b64_tr_b16 v[150:151], v242 offset:41472\n" \
"v_mfma_f32_16x16x32_f16 v[84:87], v[140:143], v[176:179], v[84:87]\n" \
"s_waitcnt lgkmcnt(6)\n" \
"v_pk_add_f16 v176, v176, v180\n" \
"v_pk_add_f16 v177, v177, v181\n" \
"v_mfma_f32_16x16x32_f16 v[76:79], v[152:155], v[180:183], v[76:79]\n" \
"v_pk_add_f16 v178, v178, v182\n" \
"v_pk_add_f16 v179, v179, v183\n" \
"v_mfma_f32_16x16x32_f16 v[88:91], v[156:159], v[180:183], v[88:91]\n" \
"ds_read_b128 v[172:175], v240 offset:43520\n" \
"v_mfma_f32_16x16x32_f16 v[80:83], v[120:123], v[176:179], v[80:83]\n" \
"buffer_load_dwordx4 v232, s[36:39], s80 offen offset:3072 sc1 lds\n" \
"v_mfma_f32_16x16x32_f16 v[92:95], v[124:127], v[176:179], v[92:95]\n" \
"v_mfma_f32_16x16x32_f16 v[96:99], v[136:139], v[160:163], v[96:99]\n" \
"s_waitcnt lgkmcnt(5)\n" \
"v_pk_add_f16 v136, v128, v144\n" \
"v_pk_add_f16 v137, v129, v145\n" \
"ds_read_b128 v[176:179], v240 offset:45056\n" \
"v_mfma_f32_16x16x32_f16 v[108:111], v[140:143], v[160:163], v[108:111]\n" \
"v_pk_add_f16 v160, v160, v164\n" \
"v_pk_add_f16 v161, v161, v165\n" \
"ds_read_b128 v[180:183], v240 offset:45568\n" \
"v_mfma_f32_16x16x32_f16 v[100:103], v[152:155], v[164:167], v[100:103]\n" \
"v_pk_add_f16 v162, v162, v166\n" \
"v_pk_add_f16 v163, v163, v167\n" \
"s_add_i32 m0, s45, 0x5000\n" \
"s_add_i32 s81, s47, 0x2400000\n" \
"v_mfma_f32_16x16x32_f16 v[112:115], v[156:159], v[164:167], v[112:115]\n" \
"s_waitcnt lgkmcnt(3)\n" \
"v_pk_add_f16 v138, v130, v146\n" \
"v_pk_add_f16 v139, v131, v147\n" \
"buffer_load_dwordx4 v232, s[36:39], s81 offen sc1 lds\n" \
"v_mfma_f32_16x16x32_f16 v[104:107], v[120:123], v[160:163], v[104:107]\n" \
"v_pk_add_f16 v140, v132, v148\n" \
"v_pk_add_f16 v141, v133, v149\n" \
"v_mfma_f32_16x16x32_f16 v[116:119], v[124:127], v[160:163], v[116:119]\n" \
"s_setprio 1\n" \
"v_pk_add_f16 v142, v134, v150\n" \
"v_pk_add_f16 v143, v135, v151\n" \
"s_waitcnt vmcnt(6) lgkmcnt(0)\n" \
"s_barrier\n" \
"v_mfma_f32_16x16x32_f16 v[0:3], v[128:131], v[168:171], v[0:3]\n" \
"ds_read_b128 v[160:163], v240 offset:6144\n" \
"ds_read_b64_tr_b16 v[184:185], v236 offset:16384\n" \
"ds_read_b64_tr_b16 v[188:189], v237 offset:16384\n" \
"v_mfma_f32_16x16x32_f16 v[12:15], v[132:135], v[168:171], v[12:15]\n" \
"v_pk_add_f16 v168, v168, v172\n" \
"v_pk_add_f16 v169, v169, v173\n" \
"v_mfma_f32_16x16x32_f16 v[4:7], v[144:147], v[172:175], v[4:7]\n" \
"v_pk_add_f16 v170, v170, v174\n" \
"v_pk_add_f16 v171, v171, v175\n" \
"buffer_store_dwordx4 v[216:219], v235, s[36:39], s65 offen sc1\n" \
"v_mfma_f32_16x16x32_f16 v[16:19], v[148:151], v[172:175], v[16:19]\n" \
"ds_read_b128 v[164:167], v240 offset:6656\n" \
"s_add_i32 m0, s44, 0xa000\n" \
"s_add_i32 s80, s46, 0x2800000\n" \
"v_mfma_f32_16x16x32_f16 v[8:11], v[136:139], v[168:171], v[8:11]\n" \
"buffer_load_dwordx4 v232, s[36:39], s80 offen sc1 lds\n" \
"s_waitcnt lgkmcnt(2)\n" \
"v_mfma_f32_16x16x16_bf16 v[184:187], v[184:185], v[192:193], 0\n" \
"v_mfma_f32_16x16x32_f16 v[20:23], v[140:143], v[168:171], v[20:23]\n" \
"ds_read_b64_tr_b16 v[120:121], v241 offset:0\n" \
"ds_read_b64_tr_b16 v[122:123], v241 offset:8192\n" \
"s_waitcnt lgkmcnt(3)\n" \
"v_mfma_f32_16x16x16_bf16 v[188:191], v[188:189], v[192:193], 0\n" \
"v_mfma_f32_16x16x32_f16 v[24:27], v[128:131], v[176:179], v[24:27]\n" \
"ds_read_b128 v[168:171], v240 offset:8192\n" \
"v_mfma_f32_16x16x32_f16 v[36:39], v[132:135], v[176:179], v[36:39]\n" \
"v_pk_add_f16 v176, v176, v180\n" \
"v_pk_add_f16 v177, v177, v181\n" \
"ds_read_b64_tr_b16 v[124:125], v242 offset:0\n" \
"v_mfma_f32_16x16x32_f16 v[28:31], v[144:147], v[180:183], v[28:31]\n" \
"v_pk_add_f16 v178, v178, v182\n" \
"v_pk_add_f16 v179, v179, v183\n" \
"v_cvt_pk_f16_f32 v184, v184, v185\n" \
"v_mfma_f32_16x16x32_f16 v[40:43], v[148:151], v[180:183], v[40:43]\n" \
"v_cvt_pk_f16_f32 v185, v186, v187\n" \
"v_cvt_pk_f16_f32 v186, v188, v189\n" \
"ds_read_b128 v[172:175], v240 offset:8704\n" \
"v_mfma_f32_16x16x32_f16 v[32:35], v[136:139], v[176:179], v[32:35]\n" \
"v_cvt_pk_f16_f32 v187, v190, v191\n" \
"v_add_u32 v184, s91, v184\n" \
"buffer_load_dwordx4 v232, s[36:39], s80 offen offset:1024 sc1 lds\n" \
"v_mfma_f32_16x16x32_f16 v[44:47], v[140:143], v[176:179], v[44:47]\n" \
"v_add_u32 v185, s91, v185\n" \
"v_add_u32 v186, s91, v186\n" \
"ds_read_b64_tr_b16 v[126:127], v242 offset:8192\n" \
"v_mfma_f32_16x16x32_f16 v[48:51], v[128:131], v[160:163], v[48:51]\n" \
"v_add_u32 v187, s91, v187\n" \
"v_and_b32 v184, s92, v184\n" \
"ds_read_b128 v[176:179], v240 offset:0\n" \
"v_mfma_f32_16x16x32_f16 v[60:63], v[132:135], v[160:163], v[60:63]\n" \
"s_waitcnt lgkmcnt(4)\n" \
"v_pk_add_f16 v160, v160, v164\n" \
"v_pk_add_f16 v161, v161, v165\n" \
"v_and_b32 v185, s92, v185\n" \
"s_add_i32 s82, s49, 0x4800000\n" \
"v_mfma_f32_16x16x32_f16 v[52:55], v[144:147], v[164:167], v[52:55]\n" \
"v_pk_add_f16 v162, v162, v166\n" \
"v_pk_add_f16 v163, v163, v167\n" \
"v_and_b32 v186, s92, v186\n" \
"v_and_b32 v187, s92, v187\n" \
"v_mfma_f32_16x16x32_f16 v[64:67], v[148:151], v[164:167], v[64:67]\n" \
"ds_read_b128 v[180:183], v240 offset:512\n" \
"buffer_store_dwordx4 v[184:187], v234, s[36:39], s82 offen sc1\n" \
"ds_read_b64_tr_b16 v[152:153], v241 offset:512\n" \
"v_mfma_f32_16x16x32_f16 v[56:59], v[136:139], v[160:163], v[56:59]\n" \
"buffer_load_dwordx4 v232, s[36:39], s80 offen offset:2048 sc1 lds\n" \
"v_mfma_f32_16x16x32_f16 v[68:71], v[140:143], v[160:163], v[68:71]\n" \
"s_setprio 0\n" \
"ds_read_b64_tr_b16 v[154:155], v241 offset:8704\n" \
"ds_read_b64_tr_b16 v[156:157], v242 offset:512\n" \
"v_mfma_f32_16x16x32_f16 v[72:75], v[128:131], v[168:171], v[72:75]\n" \
"ds_read_b128 v[160:163], v240 offset:2048\n" \
"ds_read_b64_tr_b16 v[158:159], v242 offset:8704\n" \
"v_mfma_f32_16x16x32_f16 v[84:87], v[132:135], v[168:171], v[84:87]\n" \
"s_waitcnt lgkmcnt(6)\n" \
"v_pk_add_f16 v168, v168, v172\n" \
"v_pk_add_f16 v169, v169, v173\n" \
"v_mfma_f32_16x16x32_f16 v[76:79], v[144:147], v[172:175], v[76:79]\n" \
"v_pk_add_f16 v170, v170, v174\n" \
"v_pk_add_f16 v171, v171, v175\n" \
"v_mfma_f32_16x16x32_f16 v[88:91], v[148:151], v[172:175], v[88:91]\n" \
"ds_read_b128 v[164:167], v240 offset:2560\n" \
"v_mfma_f32_16x16x32_f16 v[80:83], v[136:139], v[168:171], v[80:83]\n" \
"buffer_load_dwordx4 v232, s[36:39], s80 offen offset:3072 sc1 lds\n" \
"v_mfma_f32_16x16x32_f16 v[92:95], v[140:143], v[168:171], v[92:95]\n" \
"v_mfma_f32_16x16x32_f16 v[96:99], v[128:131], v[176:179], v[96:99]\n" \
"s_waitcnt lgkmcnt(5)\n" \
"v_pk_add_f16 v128, v120, v152\n" \
"v_pk_add_f16 v129, v121, v153\n" \
"ds_read_b128 v[168:171], v240 offset:4096\n" \
"v_mfma_f32_16x16x32_f16 v[108:111], v[132:135], v[176:179], v[108:111]\n" \
"v_pk_add_f16 v176, v176, v180\n" \
"v_pk_add_f16 v177, v177, v181\n" \
"ds_read_b128 v[172:175], v240 offset:4608\n" \
"v_mfma_f32_16x16x32_f16 v[100:103], v[144:147], v[180:183], v[100:103]\n" \
"v_pk_add_f16 v178, v178, v182\n" \
"v_pk_add_f16 v179, v179, v183\n" \
"s_add_i32 m0, s45, 0xa000\n" \
"s_add_i32 s81, s47, 0x2800000\n" \
"v_mfma_f32_16x16x32_f16 v[112:115], v[148:151], v[180:183], v[112:115]\n" \
"s_waitcnt lgkmcnt(3)\n" \
"v_pk_add_f16 v130, v122, v154\n" \
"v_pk_add_f16 v131, v123, v155\n" \
"buffer_load_dwordx4 v232, s[36:39], s81 offen sc1 lds\n" \
"v_mfma_f32_16x16x32_f16 v[104:107], v[136:139], v[176:179], v[104:107]\n" \
"v_pk_add_f16 v132, v124, v156\n" \
"v_pk_add_f16 v133, v125, v157\n" \
"v_mfma_f32_16x16x32_f16 v[116:119], v[140:143], v[176:179], v[116:119]\n" \
"s_setprio 1\n" \
"v_pk_add_f16 v134, v126, v158\n" \
"v_pk_add_f16 v135, v127, v159\n" \
"s_waitcnt vmcnt(7) lgkmcnt(0)\n" \
"s_barrier\n" \
"v_mfma_f32_16x16x32_f16 v[0:3], v[120:123], v[160:163], v[0:3]\n" \
"ds_read_b128 v[176:179], v240 offset:26624\n" \
"ds_read_b64_tr_b16 v[184:185], v238 offset:16384\n" \
"ds_read_b64_tr_b16 v[188:189], v239 offset:16384\n" \
"v_mfma_f32_16x16x32_f16 v[12:15], v[124:127], v[160:163], v[12:15]\n" \
"v_pk_add_f16 v160, v160, v164\n" \
"v_pk_add_f16 v161, v161, v165\n" \
"v_mfma_f32_16x16x32_f16 v[4:7], v[152:155], v[164:167], v[4:7]\n" \
"v_pk_add_f16 v162, v162, v166\n" \
"v_pk_add_f16 v163, v163, v167\n" \
"v_mfma_f32_16x16x32_f16 v[16:19], v[156:159], v[164:167], v[16:19]\n" \
"ds_read_b128 v[180:183], v240 offset:27136\n" \
"s_add_i32 m0, s44, 0x0\n" \
"s_add_i32 s80, s46, 0x2c00000\n" \
"v_mfma_f32_16x16x32_f16 v[8:11], v[128:131], v[160:163], v[8:11]\n" \
"buffer_load_dwordx4 v232, s[36:39], s80 offen sc1 lds\n" \
"s_waitcnt lgkmcnt(2)\n" \
"v_mfma_f32_16x16x16_bf16 v[184:187], v[184:185], v[192:193], 0\n" \
"v_mfma_f32_16x16x32_f16 v[20:23], v[132:135], v[160:163], v[20:23]\n" \
"ds_read_b64_tr_b16 v[136:137], v241 offset:16384\n" \
"ds_read_b64_tr_b16 v[138:139], v241 offset:24576\n" \
"s_waitcnt lgkmcnt(3)\n" \
"v_mfma_f32_16x16x16_bf16 v[188:191], v[188:189], v[192:193], 0\n" \
"v_mfma_f32_16x16x32_f16 v[24:27], v[120:123], v[168:171], v[24:27]\n" \
"ds_read_b128 v[160:163], v240 offset:28672\n" \
"v_mfma_f32_16x16x32_f16 v[36:39], v[124:127], v[168:171], v[36:39]\n" \
"v_pk_add_f16 v168, v168, v172\n" \
"v_pk_add_f16 v169, v169, v173\n" \
"ds_read_b64_tr_b16 v[140:141], v242 offset:16384\n" \
"v_mfma_f32_16x16x32_f16 v[28:31], v[152:155], v[172:175], v[28:31]\n" \
"v_pk_add_f16 v170, v170, v174\n" \
"v_pk_add_f16 v171, v171, v175\n" \
"v_cvt_pk_f16_f32 v184, v184, v185\n" \
"v_mfma_f32_16x16x32_f16 v[40:43], v[156:159], v[172:175], v[40:43]\n" \
"v_cvt_pk_f16_f32 v185, v186, v187\n" \
"v_cvt_pk_f16_f32 v186, v188, v189\n" \
"ds_read_b128 v[164:167], v240 offset:29184\n" \
"v_mfma_f32_16x16x32_f16 v[32:35], v[128:131], v[168:171], v[32:35]\n" \
"v_cvt_pk_f16_f32 v187, v190, v191\n" \
"v_add_u32 v184, s91, v184\n" \
"buffer_load_dwordx4 v232, s[36:39], s80 offen offset:1024 sc1 lds\n" \
"v_mfma_f32_16x16x32_f16 v[44:47], v[132:135], v[168:171], v[44:47]\n" \
"v_add_u32 v185, s91, v185\n" \
"v_add_u32 v186, s91, v186\n" \
"ds_read_b64_tr_b16 v[142:143], v242 offset:24576\n" \
"v_mfma_f32_16x16x32_f16 v[48:51], v[120:123], v[176:179], v[48:51]\n" \
"v_add_u32 v187, s91, v187\n" \
"v_and_b32 v184, s92, v184\n" \
"ds_read_b128 v[168:171], v240 offset:20480\n" \
"v_mfma_f32_16x16x32_f16 v[60:63], v[124:127], v[176:179], v[60:63]\n" \
"s_waitcnt lgkmcnt(4)\n" \
"v_pk_add_f16 v176, v176, v180\n" \
"v_pk_add_f16 v177, v177, v181\n" \
"v_and_b32 v185, s92, v185\n" \
"v_mfma_f32_16x16x32_f16 v[52:55], v[152:155], v[180:183], v[52:55]\n" \
"v_pk_add_f16 v178, v178, v182\n" \
"v_pk_add_f16 v179, v179, v183\n" \
"v_and_b32 v186, s92, v186\n" \
"v_and_b32 v187, s92, v187\n" \
"v_mfma_f32_16x16x32_f16 v[64:67], v[156:159], v[180:183], v[64:67]\n" \
"ds_read_b128 v[172:175], v240 offset:20992\n" \
"buffer_store_dwordx4 v[184:187], v234, s[36:39], s82 offen offset:1024 sc1\n" \
"ds_read_b64_tr_b16 v[144:145], v241 offset:16896\n" \
"v_mfma_f32_16x16x32_f16 v[56:59], v[128:131], v[176:179], v[56:59]\n" \
"buffer_load_dwordx4 v232, s[36:39], s80 offen offset:2048 sc1 lds\n" \
"v_mfma_f32_16x16x32_f16 v[68:71], v[132:135], v[176:179], v[68:71]\n" \
"s_setprio 0\n" \
"ds_read_b64_tr_b16 v[146:147], v241 offset:25088\n" \
"ds_read_b64_tr_b16 v[148:149], v242 offset:16896\n" \
"v_mfma_f32_16x16x32_f16 v[72:75], v[120:123], v[160:163], v[72:75]\n" \
"ds_read_b128 v[176:179], v240 offset:22528\n" \
"ds_read_b64_tr_b16 v[150:151], v242 offset:25088\n" \
"v_mfma_f32_16x16x32_f16 v[84:87], v[124:127], v[160:163], v[84:87]\n" \
"s_waitcnt lgkmcnt(6)\n" \
"v_pk_add_f16 v160, v160, v164\n" \
"v_pk_add_f16 v161, v161, v165\n" \
"v_mfma_f32_16x16x32_f16 v[76:79], v[152:155], v[164:167], v[76:79]\n" \
"v_pk_add_f16 v162, v162, v166\n" \
"v_pk_add_f16 v163, v163, v167\n" \
"v_mfma_f32_16x16x32_f16 v[88:91], v[156:159], v[164:167], v[88:91]\n" \
"ds_read_b128 v[180:183], v240 offset:23040\n" \
"v_mfma_f32_16x16x32_f16 v[80:83], v[128:131], v[160:163], v[80:83]\n" \
"buffer_load_dwordx4 v232, s[36:39], s80 offen offset:3072 sc1 lds\n" \
"v_mfma_f32_16x16x32_f16 v[92:95], v[132:135], v[160:163], v[92:95]\n" \
"v_mfma_f32_16x16x32_f16 v[96:99], v[120:123], v[168:171], v[96:99]\n" \
"s_waitcnt lgkmcnt(5)\n" \
"v_pk_add_f16 v120, v136, v144\n" \
"v_pk_add_f16 v121, v137, v145\n" \
"ds_read_b128 v[160:163], v240 offset:24576\n" \
"v_mfma_f32_16x16x32_f16 v[108:111], v[124:127], v[168:171], v[108:111]\n" \
"v_pk_add_f16 v168, v168, v172\n" \
"v_pk_add_f16 v169, v169, v173\n" \
"ds_read_b128 v[164:167], v240 offset:25088\n" \
"v_mfma_f32_16x16x32_f16 v[100:103], v[152:155], v[172:175], v[100:103]\n" \
"v_pk_add_f16 v170, v170, v174\n" \
"v_pk_add_f16 v171, v171, v175\n" \
"s_add_i32 m0, s45, 0x0\n" \
"s_add_i32 s81, s47, 0x2c00000\n" \
"v_mfma_f32_16x16x32_f16 v[112:115], v[156:159], v[172:175], v[112:115]\n" \
"s_waitcnt lgkmcnt(3)\n" \
"v_pk_add_f16 v122, v138, v146\n" \
"v_pk_add_f16 v123, v139, v147\n" \
"buffer_load_dwordx4 v232, s[36:39], s81 offen sc1 lds\n" \
"v_mfma_f32_16x16x32_f16 v[104:107], v[128:131], v[168:171], v[104:107]\n" \
"v_pk_add_f16 v124, v140, v148\n" \
"v_pk_add_f16 v125, v141, v149\n" \
"v_mfma_f32_16x16x32_f16 v[116:119], v[132:135], v[168:171], v[116:119]\n" \
"s_setprio 1\n" \
"v_pk_add_f16 v126, v142, v150\n" \
"v_pk_add_f16 v127, v143, v151\n" \
"s_waitcnt vmcnt(6) lgkmcnt(0)\n" \
"s_barrier\n" \
"v_mfma_f32_16x16x32_f16 v[0:3], v[136:139], v[176:179], v[0:3]\n" \
"ds_read_b128 v[168:171], v240 offset:47104\n" \
"ds_read_b64_tr_b16 v[184:185], v236 offset:20480\n" \
"ds_read_b64_tr_b16 v[188:189], v237 offset:20480\n" \
"v_mfma_f32_16x16x32_f16 v[12:15], v[140:143], v[176:179], v[12:15]\n" \
"v_pk_add_f16 v176, v176, v180\n" \
"v_pk_add_f16 v177, v177, v181\n" \
"v_mfma_f32_16x16x32_f16 v[4:7], v[144:147], v[180:183], v[4:7]\n" \
"v_pk_add_f16 v178, v178, v182\n" \
"v_pk_add_f16 v179, v179, v183\n" \
"buffer_store_dwordx4 v[220:223], v235, s[36:39], s66 offen sc1\n" \
"v_mfma_f32_16x16x32_f16 v[16:19], v[148:151], v[180:183], v[16:19]\n" \
"ds_read_b128 v[172:175], v240 offset:47616\n" \
"s_add_i32 m0, s44, 0x5000\n" \
"s_add_i32 s80, s46, 0x3000000\n" \
"v_mfma_f32_16x16x32_f16 v[8:11], v[120:123], v[176:179], v[8:11]\n" \
"buffer_load_dwordx4 v232, s[36:39], s80 offen sc1 lds\n" \
"s_waitcnt lgkmcnt(2)\n" \
"v_mfma_f32_16x16x16_bf16 v[184:187], v[184:185], v[192:193], 0\n" \
"v_mfma_f32_16x16x32_f16 v[20:23], v[124:127], v[176:179], v[20:23]\n" \
"ds_read_b64_tr_b16 v[128:129], v241 offset:32768\n" \
"ds_read_b64_tr_b16 v[130:131], v241 offset:40960\n" \
"s_waitcnt lgkmcnt(3)\n" \
"v_mfma_f32_16x16x16_bf16 v[188:191], v[188:189], v[192:193], 0\n" \
"v_mfma_f32_16x16x32_f16 v[24:27], v[136:139], v[160:163], v[24:27]\n" \
"ds_read_b128 v[176:179], v240 offset:49152\n" \
"v_mfma_f32_16x16x32_f16 v[36:39], v[140:143], v[160:163], v[36:39]\n" \
"v_pk_add_f16 v160, v160, v164\n" \
"v_pk_add_f16 v161, v161, v165\n" \
"ds_read_b64_tr_b16 v[132:133], v242 offset:32768\n" \
"v_mfma_f32_16x16x32_f16 v[28:31], v[144:147], v[164:167], v[28:31]\n" \
"v_pk_add_f16 v162, v162, v166\n" \
"v_pk_add_f16 v163, v163, v167\n" \
"v_cvt_pk_f16_f32 v184, v184, v185\n" \
"v_mfma_f32_16x16x32_f16 v[40:43], v[148:151], v[164:167], v[40:43]\n" \
"v_cvt_pk_f16_f32 v185, v186, v187\n" \
"v_cvt_pk_f16_f32 v186, v188, v189\n" \
"ds_read_b128 v[180:183], v240 offset:49664\n" \
"v_mfma_f32_16x16x32_f16 v[32:35], v[120:123], v[160:163], v[32:35]\n" \
"v_cvt_pk_f16_f32 v187, v190, v191\n" \
"v_add_u32 v184, s91, v184\n" \
"buffer_load_dwordx4 v232, s[36:39], s80 offen offset:1024 sc1 lds\n" \
"v_mfma_f32_16x16x32_f16 v[44:47], v[124:127], v[160:163], v[44:47]\n" \
"v_add_u32 v185, s91, v185\n" \
"v_add_u32 v186, s91, v186\n" \
"ds_read_b64_tr_b16 v[134:135], v242 offset:40960\n" \
"v_mfma_f32_16x16x32_f16 v[48:51], v[136:139], v[168:171], v[48:51]\n" \
"v_add_u32 v187, s91, v187\n" \
"v_and_b32 v184, s92, v184\n" \
"ds_read_b128 v[160:163], v240 offset:40960\n" \
"v_mfma_f32_16x16x32_f16 v[60:63], v[140:143], v[168:171], v[60:63]\n" \
"s_waitcnt lgkmcnt(4)\n" \
"v_pk_add_f16 v168, v168, v172\n" \
"v_pk_add_f16 v169, v169, v173\n" \
"v_and_b32 v185, s92, v185\n" \
"s_add_i32 s82, s50, 0x5000000\n" \
"v_mfma_f32_16x16x32_f16 v[52:55], v[144:147], v[172:175], v[52:55]\n" \
"v_pk_add_f16 v170, v170, v174\n" \
"v_pk_add_f16 v171, v171, v175\n" \
"v_and_b32 v186, s92, v186\n" \
"v_and_b32 v187, s92, v187\n" \
"v_mfma_f32_16x16x32_f16 v[64:67], v[148:151], v[172:175], v[64:67]\n" \
"ds_read_b128 v[164:167], v240 offset:41472\n" \
"buffer_store_dwordx4 v[184:187], v234, s[36:39], s82 offen sc1\n" \
"ds_read_b64_tr_b16 v[152:153], v241 offset:33280\n" \
"v_mfma_f32_16x16x32_f16 v[56:59], v[120:123], v[168:171], v[56:59]\n" \
"buffer_load_dwordx4 v232, s[36:39], s80 offen offset:2048 sc1 lds\n" \
"v_mfma_f32_16x16x32_f16 v[68:71], v[124:127], v[168:171], v[68:71]\n" \
"s_setprio 0\n" \
"ds_read_b64_tr_b16 v[154:155], v241 offset:41472\n" \
"ds_read_b64_tr_b16 v[156:157], v242 offset:33280\n" \
"v_mfma_f32_16x16x32_f16 v[72:75], v[136:139], v[176:179], v[72:75]\n" \
"ds_read_b128 v[168:171], v240 offset:43008\n" \
"ds_read_b64_tr_b16 v[158:159], v242 offset:41472\n" \
"v_mfma_f32_16x16x32_f16 v[84:87], v[140:143], v[176:179], v[84:87]\n" \
"s_waitcnt lgkmcnt(6)\n" \
"v_pk_add_f16 v176, v176, v180\n" \
"v_pk_add_f16 v177, v177, v181\n" \
"v_mfma_f32_16x16x32_f16 v[76:79], v[144:147], v[180:183], v[76:79]\n" \
"v_pk_add_f16 v178, v178, v182\n" \
"v_pk_add_f16 v179, v179, v183\n" \
"v_mfma_f32_16x16x32_f16 v[88:91], v[148:151], v[180:183], v[88:91]\n" \
"ds_read_b128 v[172:175], v240 offset:43520\n" \
"v_mfma_f32_16x16x32_f16 v[80:83], v[120:123], v[176:179], v[80:83]\n" \
"buffer_load_dwordx4 v232, s[36:39], s80 offen offset:3072 sc1 lds\n" \
"v_mfma_f32_16x16x32_f16 v[92:95], v[124:127], v[176:179], v[92:95]\n" \
"v_mfma_f32_16x16x32_f16 v[96:99], v[136:139], v[160:163], v[96:99]\n" \
"s_waitcnt lgkmcnt(5)\n" \
"v_pk_add_f16 v136, v128, v152\n" \
"v_pk_add_f16 v137, v129, v153\n" \
"ds_read_b128 v[176:179], v240 offset:45056\n" \
"v_mfma_f32_16x16x32_f16 v[108:111], v[140:143], v[160:163], v[108:111]\n" \
"v_pk_add_f16 v160, v160, v164\n" \
"v_pk_add_f16 v161, v161, v165\n" \
"ds_read_b128 v[180:183], v240 offset:45568\n" \
"v_mfma_f32_16x16x32_f16 v[100:103], v[144:147], v[164:167], v[100:103]\n" \
"v_pk_add_f16 v162, v162, v166\n" \
"v_pk_add_f16 v163, v163, v167\n" \
"s_add_i32 m0, s45, 0x5000\n" \
"s_add_i32 s81, s47, 0x3000000\n" \
"v_mfma_f32_16x16x32_f16 v[112:115], v[148:151], v[164:167], v[112:115]\n" \
"s_waitcnt lgkmcnt(3)\n" \
"v_pk_add_f16 v138, v130, v154\n" \
"v_pk_add_f16 v139, v131, v155\n" \
"buffer_load_dwordx4 v232, s[36:39], s81 offen sc1 lds\n" \
"v_mfma_f32_16x16x32_f16 v[104:107], v[120:123], v[160:163], v[104:107]\n" \
"v_pk_add_f16 v140, v132, v156\n" \
"v_pk_add_f16 v141, v133, v157\n" \
"v_mfma_f32_16x16x32_f16 v[116:119], v[124:127], v[160:163], v[116:119]\n" \
"s_setprio 1\n" \
"v_pk_add_f16 v142, v134, v158\n" \
"v_pk_add_f16 v143, v135, v159\n" \
"s_waitcnt vmcnt(7) lgkmcnt(0)\n" \
"s_barrier\n" \
"v_mfma_f32_16x16x32_f16 v[0:3], v[128:131], v[168:171], v[0:3]\n" \
"ds_read_b128 v[160:163], v240 offset:6144\n" \
"ds_read_b64_tr_b16 v[184:185], v238 offset:20480\n" \
"ds_read_b64_tr_b16 v[188:189], v239 offset:20480\n" \
"v_mfma_f32_16x16x32_f16 v[12:15], v[132:135], v[168:171], v[12:15]\n" \
"v_pk_add_f16 v168, v168, v172\n" \
"v_pk_add_f16 v169, v169, v173\n" \
"v_mfma_f32_16x16x32_f16 v[4:7], v[152:155], v[172:175], v[4:7]\n" \
"v_pk_add_f16 v170, v170, v174\n" \
"v_pk_add_f16 v171, v171, v175\n" \
"v_mfma_f32_16x16x32_f16 v[16:19], v[156:159], v[172:175], v[16:19]\n" \
"ds_read_b128 v[164:167], v240 offset:6656\n" \
"s_add_i32 m0, s44, 0xa000\n" \
"s_add_i32 s80, s46, 0x3400000\n" \
"v_mfma_f32_16x16x32_f16 v[8:11], v[136:139], v[168:171], v[8:11]\n" \
"buffer_load_dwordx4 v232, s[36:39], s80 offen sc1 lds\n" \
"s_waitcnt lgkmcnt(2)\n" \
"v_mfma_f32_16x16x16_bf16 v[184:187], v[184:185], v[192:193], 0\n" \
"v_mfma_f32_16x16x32_f16 v[20:23], v[140:143], v[168:171], v[20:23]\n" \
"ds_read_b64_tr_b16 v[120:121], v241 offset:0\n" \
"ds_read_b64_tr_b16 v[122:123], v241 offset:8192\n" \
"s_waitcnt lgkmcnt(3)\n" \
"v_mfma_f32_16x16x16_bf16 v[188:191], v[188:189], v[192:193], 0\n" \
"v_mfma_f32_16x16x32_f16 v[24:27], v[128:131], v[176:179], v[24:27]\n" \
"ds_read_b128 v[168:171], v240 offset:8192\n" \
"v_mfma_f32_16x16x32_f16 v[36:39], v[132:135], v[176:179], v[36:39]\n" \
"v_pk_add_f16 v176, v176, v180\n" \
"v_pk_add_f16 v177, v177, v181\n" \
"ds_read_b64_tr_b16 v[124:125], v242 offset:0\n" \
"v_mfma_f32_16x16x32_f16 v[28:31], v[152:155], v[180:183], v[28:31]\n" \
"v_pk_add_f16 v178, v178, v182\n" \
"v_pk_add_f16 v179, v179, v183\n" \
"v_cvt_pk_f16_f32 v184, v184, v185\n" \
"v_mfma_f32_16x16x32_f16 v[40:43], v[156:159], v[180:183], v[40:43]\n" \
"v_cvt_pk_f16_f32 v185, v186, v187\n" \
"v_cvt_pk_f16_f32 v186, v188, v189\n" \
"ds_read_b128 v[172:175], v240 offset:8704\n" \
"v_mfma_f32_16x16x32_f16 v[32:35], v[136:139], v[176:179], v[32:35]\n" \
"v_cvt_pk_f16_f32 v187, v190, v191\n" \
"v_add_u32 v184, s91, v184\n" \
"buffer_load_dwordx4 v232, s[36:39], s80 offen offset:1024 sc1 lds\n" \
"v_mfma_f32_16x16x32_f16 v[44:47], v[140:143], v[176:179], v[44:47]\n" \
"v_add_u32 v185, s91, v185\n" \
"v_add_u32 v186, s91, v186\n" \
"ds_read_b64_tr_b16 v[126:127], v242 offset:8192\n" \
"v_mfma_f32_16x16x32_f16 v[48:51], v[128:131], v[160:163], v[48:51]\n" \
"v_add_u32 v187, s91, v187\n" \
"v_and_b32 v184, s92, v184\n" \
"ds_read_b128 v[176:179], v240 offset:0\n" \
"v_mfma_f32_16x16x32_f16 v[60:63], v[132:135], v[160:163], v[60:63]\n" \
"s_waitcnt lgkmcnt(4)\n" \
"v_pk_add_f16 v160, v160, v164\n" \
"v_pk_add_f16 v161, v161, v165\n" \
"v_and_b32 v185, s92, v185\n" \
"v_mfma_f32_16x16x32_f16 v[52:55], v[152:155], v[164:167], v[52:55]\n" \
"v_pk_add_f16 v162, v162, v166\n" \
"v_pk_add_f16 v163, v163, v167\n" \
"v_and_b32 v186, s92, v186\n" \
"v_and_b32 v187, s92, v187\n" \
"v_mfma_f32_16x16x32_f16 v[64:67], v[156:159], v[164:167], v[64:67]\n" \
"ds_read_b128 v[180:183], v240 offset:512\n" \
"buffer_store_dwordx4 v[184:187], v234, s[36:39], s82 offen offset:1024 sc1\n" \
"ds_read_b64_tr_b16 v[144:145], v241 offset:512\n" \
"v_mfma_f32_16x16x32_f16 v[56:59], v[136:139], v[160:163], v[56:59]\n" \
"buffer_load_dwordx4 v232, s[36:39], s80 offen offset:2048 sc1 lds\n" \
"v_mfma_f32_16x16x32_f16 v[68:71], v[140:143], v[160:163], v[68:71]\n" \
"s_setprio 0\n" \
"ds_read_b64_tr_b16 v[146:147], v241 offset:8704\n" \
"ds_read_b64_tr_b16 v[148:149], v242 offset:512\n" \
"v_mfma_f32_16x16x32_f16 v[72:75], v[128:131], v[168:171], v[72:75]\n" \
"ds_read_b128 v[160:163], v240 offset:2048\n" \
"ds_read_b64_tr_b16 v[150:151], v242 offset:8704\n" \
"v_mfma_f32_16x16x32_f16 v[84:87], v[132:135], v[168:171], v[84:87]\n" \
"s_waitcnt lgkmcnt(6)\n" \
"v_pk_add_f16 v168, v168, v172\n" \
"v_pk_add_f16 v169, v169, v173\n" \
"v_mfma_f32_16x16x32_f16 v[76:79], v[152:155], v[172:175], v[76:79]\n" \
"v_pk_add_f16 v170, v170, v174\n" \
"v_pk_add_f16 v171, v171, v175\n" \
"v_mfma_f32_16x16x32_f16 v[88:91], v[156:159], v[172:175], v[88:91]\n" \
"ds_read_b128 v[164:167], v240 offset:2560\n" \
"v_mfma_f32_16x16x32_f16 v[80:83], v[136:139], v[168:171], v[80:83]\n" \
"buffer_load_dwordx4 v232, s[36:39], s80 offen offset:3072 sc1 lds\n" \
"v_mfma_f32_16x16x32_f16 v[92:95], v[140:143], v[168:171], v[92:95]\n" \
"v_mfma_f32_16x16x32_f16 v[96:99], v[128:131], v[176:179], v[96:99]\n" \
"s_waitcnt lgkmcnt(5)\n" \
"v_pk_add_f16 v128, v120, v144\n" \
"v_pk_add_f16 v129, v121, v145\n" \
"ds_read_b128 v[168:171], v240 offset:4096\n" \
"v_mfma_f32_16x16x32_f16 v[108:111], v[132:135], v[176:179], v[108:111]\n" \
"v_pk_add_f16 v176, v176, v180\n" \
"v_pk_add_f16 v177, v177, v181\n" \
"ds_read_b128 v[172:175], v240 offset:4608\n" \
"v_mfma_f32_16x16x32_f16 v[100:103], v[152:155], v[180:183], v[100:103]\n" \
"v_pk_add_f16 v178, v178, v182\n" \
"v_pk_add_f16 v179, v179, v183\n" \
"s_add_i32 m0, s45, 0xa000\n" \
"s_add_i32 s81, s47, 0x3400000\n" \
"v_mfma_f32_16x16x32_f16 v[112:115], v[156:159], v[180:183], v[112:115]\n" \
"s_waitcnt lgkmcnt(3)\n" \
"v_pk_add_f16 v130, v122, v146\n" \
"v_pk_add_f16 v131, v123, v147\n" \
"buffer_load_dwordx4 v232, s[36:39], s81 offen sc1 lds\n" \
"v_mfma_f32_16x16x32_f16 v[104:107], v[136:139], v[176:179], v[104:107]\n" \
"v_pk_add_f16 v132, v124, v148\n" \
"v_pk_add_f16 v133, v125, v149\n" \
"v_mfma_f32_16x16x32_f16 v[116:119], v[140:143], v[176:179], v[116:119]\n" \
"s_setprio 1\n" \
"v_pk_add_f16 v134, v126, v150\n" \
"v_pk_add_f16 v135, v127, v151\n" \
"s_waitcnt vmcnt(6) lgkmcnt(0)\n" \
"s_barrier\n" \
"v_mfma_f32_16x16x32_f16 v[0:3], v[120:123], v[160:163], v[0:3]\n" \
"ds_read_b128 v[176:179], v240 offset:26624\n" \
"ds_read_b64_tr_b16 v[184:185], v236 offset:24576\n" \
"ds_read_b64_tr_b16 v[188:189], v237 offset:24576\n" \
"v_mfma_f32_16x16x32_f16 v[12:15], v[124:127], v[160:163], v[12:15]\n" \
"v_pk_add_f16 v160, v160, v164\n" \
"v_pk_add_f16 v161, v161, v165\n" \
"s_mov_b32 s84, 0x800c000\n" \
"v_mfma_f32_16x16x32_f16 v[4:7], v[144:147], v[164:167], v[4:7]\n" \
"v_pk_add_f16 v162, v162, v166\n" \
"v_pk_add_f16 v163, v163, v167\n" \
"buffer_load_dword v253, v252, s[36:39], s84 offen sc1\n" \
"buffer_store_dwordx4 v[224:227], v235, s[36:39], s67 offen sc1\n" \
"v_mfma_f32_16x16x32_f16 v[16:19], v[148:151], v[164:167], v[16:19]\n" \
"ds_read_b128 v[180:183], v240 offset:27136\n" \
"s_add_i32 m0, s44, 0x0\n" \
"s_add_i32 s80, s46, 0x3800000\n" \
"v_mfma_f32_16x16x32_f16 v[8:11], v[128:131], v[160:163], v[8:11]\n" \
"buffer_load_dwordx4 v232, s[36:39], s80 offen sc1 lds\n" \
"s_waitcnt lgkmcnt(2)\n" \
"v_mfma_f32_16x16x16_bf16 v[184:187], v[184:185], v[192:193], 0\n" \
"v_mfma_f32_16x16x32_f16 v[20:23], v[132:135], v[160:163], v[20:23]\n" \
"ds_read_b64_tr_b16 v[136:137], v241 offset:16384\n" \
"ds_read_b64_tr_b16 v[138:139], v241 offset:24576\n" \
"s_waitcnt lgkmcnt(3)\n" \
"v_mfma_f32_16x16x16_bf16 v[188:191], v[188:189], v[192:193], 0\n" \
"v_mfma_f32_16x16x32_f16 v[24:27], v[120:123], v[168:171], v[24:27]\n" \
"ds_read_b128 v[160:163], v240 offset:28672\n" \
"v_mfma_f32_16x16x32_f16 v[36:39], v[124:127], v[168:171], v[36:39]\n" \
"v_pk_add_f16 v168, v168, v172\n" \
"v_pk_add_f16 v169, v169, v173\n" \
"ds_read_b64_tr_b16 v[140:141], v242 offset:16384\n" \
"v_mfma_f32_16x16x32_f16 v[28:31], v[144:147], v[172:175], v[28:31]\n" \
"v_pk_add_f16 v170, v170, v174\n" \
"v_pk_add_f16 v171, v171, v175\n" \
"v_cvt_pk_f16_f32 v184, v184, v185\n" \
"v_mfma_f32_16x16x32_f16 v[40:43], v[148:151], v[172:175], v[40:43]\n" \
"v_cvt_pk_f16_f32 v185, v186, v187\n" \
"v_cvt_pk_f16_f32 v186, v188, v189\n" \
"ds_read_b128 v[164:167], v240 offset:29184\n" \
"v_mfma_f32_16x16x32_f16 v[32:35], v[128:131], v[168:171], v[32:35]\n" \
"v_cvt_pk_f16_f32 v187, v190, v191\n" \
"v_add_u32 v184, s91, v184\n" \
"buffer_load_dwordx4 v232, s[36:39], s80 offen offset:1024 sc1 lds\n" \
"v_mfma_f32_16x16x32_f16 v[44:47], v[132:135], v[168:171], v[44:47]\n" \
"v_add_u32 v185, s91, v185\n" \
"v_add_u32 v186, s91, v186\n" \
"ds_read_b64_tr_b16 v[142:143], v242 offset:24576\n" \
"v_mfma_f32_16x16x32_f16 v[48:51], v[120:123], v[176:179], v[48:51]\n" \
"v_add_u32 v187, s91, v187\n" \
"v_and_b32 v184, s92, v184\n" \
"ds_read_b128 v[168:171], v240 offset:20480\n" \
"v_mfma_f32_16x16x32_f16 v[60:63], v[124:127], v[176:179], v[60:63]\n" \
"s_waitcnt lgkmcnt(4)\n" \
"v_pk_add_f16 v176, v176, v180\n" \
"v_pk_add_f16 v177, v177, v181\n" \
"v_and_b32 v185, s92, v185\n" \
"s_add_i32 s82, s51, 0x5800000\n" \
"v_mfma_f32_16x16x32_f16 v[52:55], v[144:147], v[180:183], v[52:55]\n" \
"v_pk_add_f16 v178, v178, v182\n" \
"v_pk_add_f16 v179, v179, v183\n" \
"v_and_b32 v186, s92, v186\n" \
"v_and_b32 v187, s92, v187\n" \
"v_mfma_f32_16x16x32_f16 v[64:67], v[148:151], v[180:183], v[64:67]\n" \
"ds_read_b128 v[172:175], v240 offset:20992\n" \
"buffer_store_dwordx4 v[184:187], v234, s[36:39], s82 offen sc1\n" \
"ds_read_b64_tr_b16 v[152:153], v241 offset:16896\n" \
"v_mfma_f32_16x16x32_f16 v[56:59], v[128:131], v[176:179], v[56:59]\n" \
"buffer_load_dwordx4 v232, s[36:39], s80 offen offset:2048 sc1 lds\n" \
"v_mfma_f32_16x16x32_f16 v[68:71], v[132:135], v[176:179], v[68:71]\n" \
"s_setprio 0\n" \
"ds_read_b64_tr_b16 v[154:155], v241 offset:25088\n" \
"ds_read_b64_tr_b16 v[156:157], v242 offset:16896\n" \
"v_mfma_f32_16x16x32_f16 v[72:75], v[120:123], v[160:163], v[72:75]\n" \
"ds_read_b128 v[176:179], v240 offset:22528\n" \
"ds_read_b64_tr_b16 v[158:159], v242 offset:25088\n" \
"v_mfma_f32_16x16x32_f16 v[84:87], v[124:127], v[160:163], v[84:87]\n" \
"s_waitcnt lgkmcnt(6)\n" \
"v_pk_add_f16 v160, v160, v164\n" \
"v_pk_add_f16 v161, v161, v165\n" \
"v_mfma_f32_16x16x32_f16 v[76:79], v[144:147], v[164:167], v[76:79]\n" \
"v_pk_add_f16 v162, v162, v166\n" \
"v_pk_add_f16 v163, v163, v167\n" \
"v_mfma_f32_16x16x32_f16 v[88:91], v[148:151], v[164:167], v[88:91]\n" \
"ds_read_b128 v[180:183], v240 offset:23040\n" \
"v_mfma_f32_16x16x32_f16 v[80:83], v[128:131], v[160:163], v[80:83]\n" \
"buffer_load_dwordx4 v232, s[36:39], s80 offen offset:3072 sc1 lds\n" \
"v_mfma_f32_16x16x32_f16 v[92:95], v[132:135], v[160:163], v[92:95]\n" \
"v_mfma_f32_16x16x32_f16 v[96:99], v[120:123], v[168:171], v[96:99]\n" \
"s_waitcnt lgkmcnt(5)\n" \
"v_pk_add_f16 v120, v136, v152\n" \
"v_pk_add_f16 v121, v137, v153\n" \
"ds_read_b128 v[160:163], v240 offset:24576\n" \
"v_mfma_f32_16x16x32_f16 v[108:111], v[124:127], v[168:171], v[108:111]\n" \
"v_pk_add_f16 v168, v168, v172\n" \
"v_pk_add_f16 v169, v169, v173\n" \
"ds_read_b128 v[164:167], v240 offset:25088\n" \
"v_mfma_f32_16x16x32_f16 v[100:103], v[144:147], v[172:175], v[100:103]\n" \
"v_pk_add_f16 v170, v170, v174\n" \
"v_pk_add_f16 v171, v171, v175\n" \
"s_waitcnt vmcnt(6)\n" \
"v_cmp_eq_u32 vcc, 0, v253\n" \
"s_cbranch_vccnz .Ls16_%=\n" \
".Lr16_%=:\n" \
"s_add_i32 m0, s45, 0x0\n" \
"s_add_i32 s81, s47, 0x3800000\n" \
"v_mfma_f32_16x16x32_f16 v[112:115], v[148:151], v[172:175], v[112:115]\n" \
"s_waitcnt lgkmcnt(3)\n" \
"v_pk_add_f16 v122, v138, v154\n" \
"v_pk_add_f16 v123, v139, v155\n" \
"buffer_load_dwordx4 v232, s[36:39], s81 offen sc1 lds\n" \
"v_mfma_f32_16x16x32_f16 v[104:107], v[128:131], v[168:171], v[104:107]\n" \
"v_pk_add_f16 v124, v140, v156\n" \
"v_pk_add_f16 v125, v141, v157\n" \
"v_mfma_f32_16x16x32_f16 v[116:119], v[132:135], v[168:171], v[116:119]\n" \
"s_setprio 1\n" \
"v_pk_add_f16 v126, v142, v158\n" \
"v_pk_add_f16 v127, v143, v159\n" \
"s_waitcnt vmcnt(7) lgkmcnt(0)\n" \
"s_barrier\n" \
"v_mfma_f32_16x16x32_f16 v[0:3], v[136:139], v[176:179], v[0:3]\n" \
"ds_read_b128 v[168:171], v240 offset:47104\n" \
"ds_read_b64_tr_b16 v[184:185], v238 offset:24576\n" \
"ds_read_b64_tr_b16 v[188:189], v239 offset:24576\n" \
"v_mfma_f32_16x16x32_f16 v[12:15], v[140:143], v[176:179], v[12:15]\n" \
"v_pk_add_f16 v176, v176, v180\n" \
"v_pk_add_f16 v177, v177, v181\n" \
"v_mfma_f32_16x16x32_f16 v[4:7], v[152:155], v[180:183], v[4:7]\n" \
"v_pk_add_f16 v178, v178, v182\n" \
"v_pk_add_f16 v179, v179, v183\n" \
"v_mfma_f32_16x16x32_f16 v[16:19], v[156:159], v[180:183], v[16:19]\n" \
"ds_read_b128 v[172:175], v240 offset:47616\n" \
"s_add_i32 m0, s44, 0x5000\n" \
"s_add_i32 s80, s46, 0x3c00000\n" \
"v_mfma_f32_16x16x32_f16 v[8:11], v[120:123], v[176:179], v[8:11]\n" \
"buffer_load_dwordx4 v232, s[36:39], s80 offen sc1 lds\n" \
"s_waitcnt lgkmcnt(2)\n" \
"v_mfma_f32_16x16x16_bf16 v[184:187], v[184:185], v[192:193], 0\n" \
"v_mfma_f32_16x16x32_f16 v[20:23], v[124:127], v[176:179], v[20:23]\n" \
"ds_read_b64_tr_b16 v[128:129], v241 offset:32768\n" \
"ds_read_b64_tr_b16 v[130:131], v241 offset:40960\n" \
"s_waitcnt lgkmcnt(3)\n" \
"v_mfma_f32_16x16x16_bf16 v[188:191], v[188:189], v[192:193], 0\n" \
"v_mfma_f32_16x16x32_f16 v[24:27], v[136:139], v[160:163], v[24:27]\n" \
"ds_read_b128 v[176:179], v240 offset:49152\n" \
"v_mfma_f32_16x16x32_f16 v[36:39], v[140:143], v[160:163], v[36:39]\n" \
"v_pk_add_f16 v160, v160, v164\n" \
"v_pk_add_f16 v161, v161, v165\n" \
"ds_read_b64_tr_b16 v[132:133], v242 offset:32768\n" \
"v_mfma_f32_16x16x32_f16 v[28:31], v[152:155], v[164:167], v[28:31]\n" \
"v_pk_add_f16 v162, v162, v166\n" \
"v_pk_add_f16 v163, v163, v167\n" \
"v_cvt_pk_f16_f32 v184, v184, v185\n" \
"v_mfma_f32_16x16x32_f16 v[40:43], v[156:159], v[164:167], v[40:43]\n" \
"v_cvt_pk_f16_f32 v185, v186, v187\n" \
"v_cvt_pk_f16_f32 v186, v188, v189\n" \
"ds_read_b128 v[180:183], v240 offset:49664\n" \
"v_mfma_f32_16x16x32_f16 v[32:35], v[120:123], v[160:163], v[32:35]\n" \
"v_cvt_pk_f16_f32 v187, v190, v191\n" \
"v_add_u32 v184, s91, v184\n" \
"buffer_load_dwordx4 v232, s[36:39], s80 offen offset:1024 sc1 lds\n" \
"v_mfma_f32_16x16x32_f16 v[44:47], v[124:127], v[160:163], v[44:47]\n" \
"v_add_u32 v185, s91, v185\n" \
"v_add_u32 v186, s91, v186\n" \
"ds_read_b64_tr_b16 v[134:135], v242 offset:40960\n" \
"v_mfma_f32_16x16x32_f16 v[48:51], v[136:139], v[168:171], v[48:51]\n" \
"v_add_u32 v187, s91, v187\n" \
"v_and_b32 v184, s92, v184\n" \
"ds_read_b128 v[160:163], v240 offset:40960\n" \
"v_mfma_f32_16x16x32_f16 v[60:63], v[140:143], v[168:171], v[60:63]\n" \
"s_waitcnt lgkmcnt(4)\n" \
"v_pk_add_f16 v168, v168, v172\n" \
"v_pk_add_f16 v169, v169, v173\n" \
"v_and_b32 v185, s92, v185\n" \
"v_mfma_f32_16x16x32_f16 v[52:55], v[152:155], v[172:175], v[52:55]\n" \
"v_pk_add_f16 v170, v170, v174\n" \
"v_pk_add_f16 v171, v171, v175\n" \
"v_and_b32 v186, s92, v186\n" \
"v_and_b32 v187, s92, v187\n" \
"v_mfma_f32_16x16x32_f16 v[64:67], v[156:159], v[172:175], v[64:67]\n" \
"ds_read_b128 v[164:167], v240 offset:41472\n" \
"buffer_store_dwordx4 v[184:187], v234, s[36:39], s82 offen offset:1024 sc1\n" \
"ds_read_b64_tr_b16 v[144:145], v241 offset:33280\n" \
"v_mfma_f32_16x16x32_f16 v[56:59], v[120:123], v[168:171], v[56:59]\n" \
"buffer_load_dwordx4 v232, s[36:39], s80 offen offset:2048 sc1 lds\n" \
"v_mfma_f32_16x16x32_f16 v[68:71], v[124:127], v[168:171], v[68:71]\n" \
"s_setprio 0\n" \
"ds_read_b64_tr_b16 v[146:147], v241 offset:41472\n" \
"ds_read_b64_tr_b16 v[148:149], v242 offset:33280\n" \
"v_mfma_f32_16x16x32_f16 v[72:75], v[136:139], v[176:179], v[72:75]\n" \
"ds_read_b128 v[168:171], v240 offset:43008\n" \
"ds_read_b64_tr_b16 v[150:151], v242 offset:41472\n" \
"v_mfma_f32_16x16x32_f16 v[84:87], v[140:143], v[176:179], v[84:87]\n" \
"s_waitcnt lgkmcnt(6)\n" \
"v_pk_add_f16 v176, v176, v180\n" \
"v_pk_add_f16 v177, v177, v181\n" \
"v_mfma_f32_16x16x32_f16 v[76:79], v[152:155], v[180:183], v[76:79]\n" \
"v_pk_add_f16 v178, v178, v182\n" \
"v_pk_add_f16 v179, v179, v183\n" \
"v_mfma_f32_16x16x32_f16 v[88:91], v[156:159], v[180:183], v[88:91]\n" \
"ds_read_b128 v[172:175], v240 offset:43520\n" \
"v_mfma_f32_16x16x32_f16 v[80:83], v[120:123], v[176:179], v[80:83]\n" \
"buffer_load_dwordx4 v232, s[36:39], s80 offen offset:3072 sc1 lds\n" \
"v_mfma_f32_16x16x32_f16 v[92:95], v[124:127], v[176:179], v[92:95]\n" \
"v_mfma_f32_16x16x32_f16 v[96:99], v[136:139], v[160:163], v[96:99]\n" \
"s_waitcnt lgkmcnt(5)\n" \
"v_pk_add_f16 v136, v128, v144\n" \
"v_pk_add_f16 v137, v129, v145\n" \
"ds_read_b128 v[176:179], v240 offset:45056\n" \
"v_mfma_f32_16x16x32_f16 v[108:111], v[140:143], v[160:163], v[108:111]\n" \
"v_pk_add_f16 v160, v160, v164\n" \
"v_pk_add_f16 v161, v161, v165\n" \
"ds_read_b128 v[180:183], v240 offset:45568\n" \
"v_mfma_f32_16x16x32_f16 v[100:103], v[152:155], v[164:167], v[100:103]\n" \
"v_pk_add_f16 v162, v162, v166\n" \
"v_pk_add_f16 v163, v163, v167\n" \
"s_add_i32 m0, s45, 0x5000\n" \
"s_add_i32 s81, s47, 0x3c00000\n" \
"v_mfma_f32_16x16x32_f16 v[112:115], v[156:159], v[164:167], v[112:115]\n" \
"s_waitcnt lgkmcnt(3)\n" \
"v_pk_add_f16 v138, v130, v146\n" \
"v_pk_add_f16 v139, v131, v147\n" \
"buffer_load_dwordx4 v232, s[36:39], s81 offen sc1 lds\n" \
"v_mfma_f32_16x16x32_f16 v[104:107], v[120:123], v[160:163], v[104:107]\n" \
"v_pk_add_f16 v140, v132, v148\n" \
"v_pk_add_f16 v141, v133, v149\n" \
"v_mfma_f32_16x16x32_f16 v[116:119], v[124:127], v[160:163], v[116:119]\n" \
"s_setprio 1\n" \
"v_pk_add_f16 v142, v134, v150\n" \
"v_pk_add_f16 v143, v135, v151\n" \
"s_waitcnt vmcnt(6) lgkmcnt(0)\n" \
"s_barrier\n" \
"v_mfma_f32_16x16x32_f16 v[0:3], v[128:131], v[168:171], v[0:3]\n" \
"ds_read_b128 v[160:163], v240 offset:6144\n" \
"ds_read_b64_tr_b16 v[184:185], v236 offset:28672\n" \
"ds_read_b64_tr_b16 v[188:189], v237 offset:28672\n" \
"s_add_i32 m0, s54, 0x0\n" \
"s_mov_b32 s83, 0x0\n" \
"v_mfma_f32_16x16x32_f16 v[12:15], v[132:135], v[168:171], v[12:15]\n" \
"v_pk_add_f16 v168, v168, v172\n" \
"v_pk_add_f16 v169, v169, v173\n" \
"buffer_load_dwordx4 v233, s[36:39], s83 offen sc1 lds\n" \
"v_mfma_f32_16x16x32_f16 v[4:7], v[144:147], v[172:175], v[4:7]\n" \
"v_pk_add_f16 v170, v170, v174\n" \
"v_pk_add_f16 v171, v171, v175\n" \
"buffer_store_dwordx4 v[228:231], v235, s[36:39], s68 offen sc1\n" \
"v_mfma_f32_16x16x32_f16 v[16:19], v[148:151], v[172:175], v[16:19]\n" \
"ds_read_b128 v[164:167], v240 offset:6656\n" \
"s_add_i32 m0, s44, 0xa000\n" \
"s_add_i32 s80, s46, 0x4000000\n" \
"v_mfma_f32_16x16x32_f16 v[8:11], v[136:139], v[168:171], v[8:11]\n" \
"buffer_load_dwordx4 v232, s[36:39], s80 offen sc1 lds\n" \
"s_waitcnt lgkmcnt(2)\n" \
"v_mfma_f32_16x16x16_bf16 v[184:187], v[184:185], v[192:193], 0\n" \
"v_mfma_f32_16x16x32_f16 v[20:23], v[140:143], v[168:171], v[20:23]\n" \
"ds_read_b64_tr_b16 v[120:121], v241 offset:0\n" \
"ds_read_b64_tr_b16 v[122:123], v241 offset:8192\n" \
"s_waitcnt lgkmcnt(3)\n" \
"v_mfma_f32_16x16x16_bf16 v[188:191], v[188:189], v[192:193], 0\n" \
"v_mfma_f32_16x16x32_f16 v[24:27], v[128:131], v[176:179], v[24:27]\n" \
"ds_read_b128 v[168:171], v240 offset:8192\n" \
"v_mfma_f32_16x16x32_f16 v[36:39], v[132:135], v[176:179], v[36:39]\n" \
"v_pk_add_f16 v176, v176, v180\n" \
"v_pk_add_f16 v177, v177, v181\n" \
"ds_read_b64_tr_b16 v[124:125], v242 offset:0\n" \
"v_mfma_f32_16x16x32_f16 v[28:31], v[144:147], v[180:183], v[28:31]\n" \
"v_pk_add_f16 v178, v178, v182\n" \
"v_pk_add_f16 v179, v179, v183\n" \
"v_cvt_pk_f16_f32 v184, v184, v185\n" \
"v_mfma_f32_16x16x32_f16 v[40:43], v[148:151], v[180:183], v[40:43]\n" \
"v_cvt_pk_f16_f32 v185, v186, v187\n" \
"v_cvt_pk_f16_f32 v186, v188, v189\n" \
"ds_read_b128 v[172:175], v240 offset:8704\n" \
"v_mfma_f32_16x16x32_f16 v[32:35], v[136:139], v[176:179], v[32:35]\n" \
"v_cvt_pk_f16_f32 v187, v190, v191\n" \
"v_add_u32 v184, s91, v184\n" \
"buffer_load_dwordx4 v232, s[36:39], s80 offen offset:1024 sc1 lds\n" \
"v_mfma_f32_16x16x32_f16 v[44:47], v[140:143], v[176:179], v[44:47]\n" \
"v_add_u32 v185, s91, v185\n" \
"v_add_u32 v186, s91, v186\n" \
"ds_read_b64_tr_b16 v[126:127], v242 offset:8192\n" \
"v_mfma_f32_16x16x32_f16 v[48:51], v[128:131], v[160:163], v[48:51]\n" \
"v_add_u32 v187, s91, v187\n" \
"v_and_b32 v184, s92, v184\n" \
"ds_read_b128 v[176:179], v240 offset:0\n" \
"v_mfma_f32_16x16x32_f16 v[60:63], v[132:135], v[160:163], v[60:63]\n" \
"s_waitcnt lgkmcnt(4)\n" \
"v_pk_add_f16 v160, v160, v164\n" \
"v_pk_add_f16 v161, v161, v165\n" \
"v_and_b32 v185, s92, v185\n" \
"s_add_i32 s82, s48, 0x6000000\n" \
"v_mfma_f32_16x16x32_f16 v[52:55], v[144:147], v[164:167], v[52:55]\n" \
"v_pk_add_f16 v162, v162, v166\n" \
"v_pk_add_f16 v163, v163, v167\n" \
"v_and_b32 v186, s92, v186\n" \
"v_and_b32 v187, s92, v187\n" \
"v_mfma_f32_16x16x32_f16 v[64:67], v[148:151], v[164:167], v[64:67]\n" \
"ds_read_b128 v[180:183], v240 offset:512\n" \
"buffer_store_dwordx4 v[184:187], v234, s[36:39], s82 offen sc1\n" \
"ds_read_b64_tr_b16 v[152:153], v241 offset:512\n" \
"v_mfma_f32_16x16x32_f16 v[56:59], v[136:139], v[160:163], v[56:59]\n" \
"buffer_load_dwordx4 v232, s[36:39], s80 offen offset:2048 sc1 lds\n" \
"v_mfma_f32_16x16x32_f16 v[68:71], v[140:143], v[160:163], v[68:71]\n" \
"s_setprio 0\n" \
"ds_read_b64_tr_b16 v[154:155], v241 offset:8704\n" \
"ds_read_b64_tr_b16 v[156:157], v242 offset:512\n" \
"v_mfma_f32_16x16x32_f16 v[72:75], v[128:131], v[168:171], v[72:75]\n" \
"ds_read_b128 v[160:163], v240 offset:2048\n" \
"ds_read_b64_tr_b16 v[158:159], v242 offset:8704\n" \
"v_mfma_f32_16x16x32_f16 v[84:87], v[132:135], v[168:171], v[84:87]\n" \
"s_waitcnt lgkmcnt(6)\n" \
"v_pk_add_f16 v168, v168, v172\n" \
"v_pk_add_f16 v169, v169, v173\n" \
"v_mfma_f32_16x16x32_f16 v[76:79], v[144:147], v[172:175], v[76:79]\n" \
"v_pk_add_f16 v170, v170, v174\n" \
"v_pk_add_f16 v171, v171, v175\n" \
"v_mfma_f32_16x16x32_f16 v[88:91], v[148:151], v[172:175], v[88:91]\n" \
"ds_read_b128 v[164:167], v240 offset:2560\n" \
"v_mfma_f32_16x16x32_f16 v[80:83], v[136:139], v[168:171], v[80:83]\n" \
"buffer_load_dwordx4 v232, s[36:39], s80 offen offset:3072 sc1 lds\n" \
"v_mfma_f32_16x16x32_f16 v[92:95], v[140:143], v[168:171], v[92:95]\n" \
"v_mfma_f32_16x16x32_f16 v[96:99], v[128:131], v[176:179], v[96:99]\n" \
"s_waitcnt lgkmcnt(5)\n" \
"v_pk_add_f16 v128, v120, v152\n" \
"v_pk_add_f16 v129, v121, v153\n" \
"ds_read_b128 v[168:171], v240 offset:4096\n" \
"v_mfma_f32_16x16x32_f16 v[108:111], v[132:135], v[176:179], v[108:111]\n" \
"v_pk_add_f16 v176, v176, v180\n" \
"v_pk_add_f16 v177, v177, v181\n" \
"ds_read_b128 v[172:175], v240 offset:4608\n" \
"v_mfma_f32_16x16x32_f16 v[100:103], v[144:147], v[180:183], v[100:103]\n" \
"v_pk_add_f16 v178, v178, v182\n" \
"v_pk_add_f16 v179, v179, v183\n" \
"s_add_i32 m0, s45, 0xa000\n" \
"s_add_i32 s81, s47, 0x4000000\n" \
"v_mfma_f32_16x16x32_f16 v[112:115], v[148:151], v[180:183], v[112:115]\n" \
"s_waitcnt lgkmcnt(3)\n" \
"v_pk_add_f16 v130, v122, v154\n" \
"v_pk_add_f16 v131, v123, v155\n" \
"buffer_load_dwordx4 v232, s[36:39], s81 offen sc1 lds\n" \
"v_mfma_f32_16x16x32_f16 v[104:107], v[136:139], v[176:179], v[104:107]\n" \
"v_pk_add_f16 v132, v124, v156\n" \
"v_pk_add_f16 v133, v125, v157\n" \
"v_mfma_f32_16x16x32_f16 v[116:119], v[140:143], v[176:179], v[116:119]\n" \
"s_setprio 1\n" \
"v_pk_add_f16 v134, v126, v158\n" \
"v_pk_add_f16 v135, v127, v159\n" \
"s_waitcnt vmcnt(8) lgkmcnt(0)\n" \
"s_barrier\n" \
"v_mfma_f32_16x16x32_f16 v[0:3], v[120:123], v[160:163], v[0:3]\n" \
"ds_read_b128 v[176:179], v240 offset:26624\n" \
"ds_read_b64_tr_b16 v[184:185], v238 offset:28672\n" \
"ds_read_b64_tr_b16 v[188:189], v239 offset:28672\n" \
"v_mfma_f32_16x16x32_f16 v[12:15], v[124:127], v[160:163], v[12:15]\n" \
"v_pk_add_f16 v160, v160, v164\n" \
"v_pk_add_f16 v161, v161, v165\n" \
"v_mfma_f32_16x16x32_f16 v[4:7], v[152:155], v[164:167], v[4:7]\n" \
"v_pk_add_f16 v162, v162, v166\n" \
"v_pk_add_f16 v163, v163, v167\n" \
"v_mfma_f32_16x16x32_f16 v[16:19], v[156:159], v[164:167], v[16:19]\n" \
"ds_read_b128 v[180:183], v240 offset:27136\n" \
"s_add_i32 m0, s44, 0x0\n" \
"s_add_i32 s80, s46, 0x4400000\n" \
"v_mfma_f32_16x16x32_f16 v[8:11], v[128:131], v[160:163], v[8:11]\n" \
"buffer_load_dwordx4 v232, s[36:39], s80 offen sc1 lds\n" \
"s_waitcnt lgkmcnt(2)\n" \
"v_mfma_f32_16x16x16_bf16 v[184:187], v[184:185], v[192:193], 0\n" \
"v_mfma_f32_16x16x32_f16 v[20:23], v[132:135], v[160:163], v[20:23]\n" \
"ds_read_b64_tr_b16 v[136:137], v241 offset:16384\n" \
"ds_read_b64_tr_b16 v[138:139], v241 offset:24576\n" \
"s_waitcnt lgkmcnt(3)\n" \
"v_mfma_f32_16x16x16_bf16 v[188:191], v[188:189], v[192:193], 0\n" \
"v_mfma_f32_16x16x32_f16 v[24:27], v[120:123], v[168:171], v[24:27]\n" \
"ds_read_b128 v[160:163], v240 offset:28672\n" \
"v_mfma_f32_16x16x32_f16 v[36:39], v[124:127], v[168:171], v[36:39]\n" \
"v_pk_add_f16 v168, v168, v172\n" \
"v_pk_add_f16 v169, v169, v173\n" \
"ds_read_b64_tr_b16 v[140:141], v242 offset:16384\n" \
"v_mfma_f32_16x16x32_f16 v[28:31], v[152:155], v[172:175], v[28:31]\n" \
"v_pk_add_f16 v170, v170, v174\n" \
"v_pk_add_f16 v171, v171, v175\n" \
"v_cvt_pk_f16_f32 v184, v184, v185\n" \
"v_mfma_f32_16x16x32_f16 v[40:43], v[156:159], v[172:175], v[40:43]\n" \
"v_cvt_pk_f16_f32 v185, v186, v187\n" \
"v_cvt_pk_f16_f32 v186, v188, v189\n" \
"ds_read_b128 v[164:167], v240 offset:29184\n" \
"v_mfma_f32_16x16x32_f16 v[32:35], v[128:131], v[168:171], v[32:35]\n" \
"v_cvt_pk_f16_f32 v187, v190, v191\n" \
"v_add_u32 v184, s91, v184\n" \
"buffer_load_dwordx4 v232, s[36:39], s80 offen offset:1024 sc1 lds\n" \
"v_mfma_f32_16x16x32_f16 v[44:47], v[132:135], v[168:171], v[44:47]\n" \
"v_add_u32 v185, s91, v185\n" \
"v_add_u32 v186, s91, v186\n" \
"ds_read_b64_tr_b16 v[142:143], v242 offset:24576\n" \
"v_mfma_f32_16x16x32_f16 v[48:51], v[120:123], v[176:179], v[48:51]\n" \
"v_add_u32 v187, s91, v187\n" \
"v_and_b32 v184, s92, v184\n" \
"ds_read_b128 v[168:171], v240 offset:20480\n" \
"v_mfma_f32_16x16x32_f16 v[60:63], v[124:127], v[176:179], v[60:63]\n" \
"s_waitcnt lgkmcnt(4)\n" \
"v_pk_add_f16 v176, v176, v180\n" \
"v_pk_add_f16 v177, v177, v181\n" \
"v_and_b32 v185, s92, v185\n" \
"v_mfma_f32_16x16x32_f16 v[52:55], v[152:155], v[180:183], v[52:55]\n" \
"v_pk_add_f16 v178, v178, v182\n" \
"v_pk_add_f16 v179, v179, v183\n" \
"v_and_b32 v186, s92, v186\n" \
"v_and_b32 v187, s92, v187\n" \
"v_mfma_f32_16x16x32_f16 v[64:67], v[156:159], v[180:183], v[64:67]\n" \
"ds_read_b128 v[172:175], v240 offset:20992\n" \
"buffer_store_dwordx4 v[184:187], v234, s[36:39], s82 offen offset:1024 sc1\n" \
"ds_read_b64_tr_b16 v[144:145], v241 offset:16896\n" \
"v_mfma_f32_16x16x32_f16 v[56:59], v[128:131], v[176:179], v[56:59]\n" \
"buffer_load_dwordx4 v232, s[36:39], s80 offen offset:2048 sc1 lds\n" \
"v_mfma_f32_16x16x32_f16 v[68:71], v[132:135], v[176:179], v[68:71]\n" \
"s_setprio 0\n" \
"ds_read_b64_tr_b16 v[146:147], v241 offset:25088\n" \
"ds_read_b64_tr_b16 v[148:149], v242 offset:16896\n" \
"v_mfma_f32_16x16x32_f16 v[72:75], v[120:123], v[160:163], v[72:75]\n" \
"ds_read_b128 v[176:179], v240 offset:22528\n" \
"ds_read_b64_tr_b16 v[150:151], v242 offset:25088\n" \
"v_mfma_f32_16x16x32_f16 v[84:87], v[124:127], v[160:163], v[84:87]\n" \
"s_waitcnt lgkmcnt(6)\n" \
"v_pk_add_f16 v160, v160, v164\n" \
"v_pk_add_f16 v161, v161, v165\n" \
"v_mfma_f32_16x16x32_f16 v[76:79], v[152:155], v[164:167], v[76:79]\n" \
"v_pk_add_f16 v162, v162, v166\n" \
"v_pk_add_f16 v163, v163, v167\n" \
"v_mfma_f32_16x16x32_f16 v[88:91], v[156:159], v[164:167], v[88:91]\n" \
"ds_read_b128 v[180:183], v240 offset:23040\n" \
"v_mfma_f32_16x16x32_f16 v[80:83], v[128:131], v[160:163], v[80:83]\n" \
"buffer_load_dwordx4 v232, s[36:39], s80 offen offset:3072 sc1 lds\n" \
"v_mfma_f32_16x16x32_f16 v[92:95], v[132:135], v[160:163], v[92:95]\n" \
"v_mfma_f32_16x16x32_f16 v[96:99], v[120:123], v[168:171], v[96:99]\n" \
"s_waitcnt lgkmcnt(5)\n" \
"v_pk_add_f16 v120, v136, v144\n" \
"v_pk_add_f16 v121, v137, v145\n" \
"ds_read_b128 v[160:163], v240 offset:24576\n" \
"v_mfma_f32_16x16x32_f16 v[108:111], v[124:127], v[168:171], v[108:111]\n" \
"v_pk_add_f16 v168, v168, v172\n" \
"v_pk_add_f16 v169, v169, v173\n" \
"ds_read_b128 v[164:167], v240 offset:25088\n" \
"v_mfma_f32_16x16x32_f16 v[100:103], v[152:155], v[172:175], v[100:103]\n" \
"v_pk_add_f16 v170, v170, v174\n" \
"v_pk_add_f16 v171, v171, v175\n" \
"s_add_i32 m0, s45, 0x0\n" \
"s_add_i32 s81, s47, 0x4400000\n" \
"v_mfma_f32_16x16x32_f16 v[112:115], v[156:159], v[172:175], v[112:115]\n" \
"s_waitcnt lgkmcnt(3)\n" \
"v_pk_add_f16 v122, v138, v146\n" \
"v_pk_add_f16 v123, v139, v147\n" \
"buffer_load_dwordx4 v232, s[36:39], s81 offen sc1 lds\n" \
"v_mfma_f32_16x16x32_f16 v[104:107], v[128:131], v[168:171], v[104:107]\n" \
"v_pk_add_f16 v124, v140, v148\n" \
"v_pk_add_f16 v125, v141, v149\n" \
"v_mfma_f32_16x16x32_f16 v[116:119], v[132:135], v[168:171], v[116:119]\n" \
"s_setprio 1\n" \
"v_pk_add_f16 v126, v142, v150\n" \
"v_pk_add_f16 v127, v143, v151\n" \
"s_waitcnt vmcnt(6) lgkmcnt(0)\n" \
"s_barrier\n" \
"v_mfma_f32_16x16x32_f16 v[0:3], v[136:139], v[176:179], v[0:3]\n" \
"ds_read_b128 v[168:171], v240 offset:47104\n" \
"ds_read_b64_tr_b16 v[184:185], v236 offset:32768\n" \
"ds_read_b64_tr_b16 v[188:189], v237 offset:32768\n" \
"s_add_i32 m0, s54, 0x1000\n" \
"s_mov_b32 s83, 0x100\n" \
"v_mfma_f32_16x16x32_f16 v[12:15], v[140:143], v[176:179], v[12:15]\n" \
"v_pk_add_f16 v176, v176, v180\n" \
"v_pk_add_f16 v177, v177, v181\n" \
"buffer_load_dwordx4 v233, s[36:39], s83 offen sc1 lds\n" \
"v_mfma_f32_16x16x32_f16 v[4:7], v[144:147], v[180:183], v[4:7]\n" \
"v_pk_add_f16 v178, v178, v182\n" \
"v_pk_add_f16 v179, v179, v183\n" \
"v_mfma_f32_16x16x32_f16 v[16:19], v[148:151], v[180:183], v[16:19]\n" \
"ds_read_b128 v[172:175], v240 offset:47616\n" \
"s_add_i32 m0, s44, 0x5000\n" \
"s_add_i32 s80, s46, 0x4800000\n" \
"v_mfma_f32_16x16x32_f16 v[8:11], v[120:123], v[176:179], v[8:11]\n" \
"buffer_load_dwordx4 v232, s[36:39], s80 offen sc1 lds\n" \
"s_waitcnt lgkmcnt(2)\n" \
"v_mfma_f32_16x16x16_bf16 v[184:187], v[184:185], v[192:193], 0\n" \
"v_mfma_f32_16x16x32_f16 v[20:23], v[124:127], v[176:179], v[20:23]\n" \
"ds_read_b64_tr_b16 v[128:129], v241 offset:32768\n" \
"ds_read_b64_tr_b16 v[130:131], v241 offset:40960\n" \
"s_waitcnt lgkmcnt(3)\n" \
"v_mfma_f32_16x16x16_bf16 v[188:191], v[188:189], v[192:193], 0\n" \
"v_mfma_f32_16x16x32_f16 v[24:27], v[136:139], v[160:163], v[24:27]\n" \
"ds_read_b128 v[176:179], v240 offset:49152\n" \
"v_mfma_f32_16x16x32_f16 v[36:39], v[140:143], v[160:163], v[36:39]\n" \
"v_pk_add_f16 v160, v160, v164\n" \
"v_pk_add_f16 v161, v161, v165\n" \
"ds_read_b64_tr_b16 v[132:133], v242 offset:32768\n" \
"v_mfma_f32_16x16x32_f16 v[28:31], v[144:147], v[164:167], v[28:31]\n" \
"v_pk_add_f16 v162, v162, v166\n" \
"v_pk_add_f16 v163, v163, v167\n" \
"v_cvt_pk_f16_f32 v184, v184, v185\n" \
"v_mfma_f32_16x16x32_f16 v[40:43], v[148:151], v[164:167], v[40:43]\n" \
"v_cvt_pk_f16_f32 v185, v186, v187\n" \
"v_cvt_pk_f16_f32 v186, v188, v189\n" \
"ds_read_b128 v[180:183], v240 offset:49664\n" \
"v_mfma_f32_16x16x32_f16 v[32:35], v[120:123], v[160:163], v[32:35]\n" \
"v_cvt_pk_f16_f32 v187, v190, v191\n" \
"v_add_u32 v184, s91, v184\n" \
"buffer_load_dwordx4 v232, s[36:39], s80 offen offset:1024 sc1 lds\n" \
"v_mfma_f32_16x16x32_f16 v[44:47], v[124:127], v[160:163], v[44:47]\n" \
"v_add_u32 v185, s91, v185\n" \
"v_add_u32 v186, s91, v186\n" \
"ds_read_b64_tr_b16 v[134:135], v242 offset:40960\n" \
"v_mfma_f32_16x16x32_f16 v[48:51], v[136:139], v[168:171], v[48:51]\n" \
"v_add_u32 v187, s91, v187\n" \
"v_and_b32 v184, s92, v184\n" \
"ds_read_b128 v[160:163], v240 offset:40960\n" \
"v_mfma_f32_16x16x32_f16 v[60:63], v[140:143], v[168:171], v[60:63]\n" \
"s_waitcnt lgkmcnt(4)\n" \
"v_pk_add_f16 v168, v168, v172\n" \
"v_pk_add_f16 v169, v169, v173\n" \
"v_and_b32 v185, s92, v185\n" \
"s_add_i32 s82, s49, 0x6800000\n" \
"v_mfma_f32_16x16x32_f16 v[52:55], v[144:147], v[172:175], v[52:55]\n" \
"v_pk_add_f16 v170, v170, v174\n" \
"v_pk_add_f16 v171, v171, v175\n" \
"v_and_b32 v186, s92, v186\n" \
"v_and_b32 v187, s92, v187\n" \
"v_mfma_f32_16x16x32_f16 v[64:67], v[148:151], v[172:175], v[64:67]\n" \
"ds_read_b128 v[164:167], v240 offset:41472\n" \
"buffer_store_dwordx4 v[184:187], v234, s[36:39], s82 offen sc1\n" \
"ds_read_b64_tr_b16 v[152:153], v241 offset:33280\n" \
"v_mfma_f32_16x16x32_f16 v[56:59], v[120:123], v[168:171], v[56:59]\n" \
"buffer_load_dwordx4 v232, s[36:39], s80 offen offset:2048 sc1 lds\n" \
"v_mfma_f32_16x16x32_f16 v[68:71], v[124:127], v[168:171], v[68:71]\n" \
"s_setprio 0\n" \
"ds_read_b64_tr_b16 v[154:155], v241 offset:41472\n" \
"ds_read_b64_tr_b16 v[156:157], v242 offset:33280\n" \
"v_mfma_f32_16x16x32_f16 v[72:75], v[136:139], v[176:179], v[72:75]\n" \
"ds_read_b128 v[168:171], v240 offset:43008\n" \
"ds_read_b64_tr_b16 v[158:159], v242 offset:41472\n" \
"v_mfma_f32_16x16x32_f16 v[84:87], v[140:143], v[176:179], v[84:87]\n" \
"s_waitcnt lgkmcnt(6)\n" \
"v_pk_add_f16 v176, v176, v180\n" \
"v_pk_add_f16 v177, v177, v181\n" \
"v_mfma_f32_16x16x32_f16 v[76:79], v[144:147], v[180:183], v[76:79]\n" \
"v_pk_add_f16 v178, v178, v182\n" \
"v_pk_add_f16 v179, v179, v183\n" \
"v_mfma_f32_16x16x32_f16 v[88:91], v[148:151], v[180:183], v[88:91]\n" \
"ds_read_b128 v[172:175], v240 offset:43520\n" \
"v_mfma_f32_16x16x32_f16 v[80:83], v[120:123], v[176:179], v[80:83]\n" \
"buffer_load_dwordx4 v232, s[36:39], s80 offen offset:3072 sc1 lds\n" \
"v_mfma_f32_16x16x32_f16 v[92:95], v[124:127], v[176:179], v[92:95]\n" \
"v_mfma_f32_16x16x32_f16 v[96:99], v[136:139], v[160:163], v[96:99]\n" \
"s_waitcnt lgkmcnt(5)\n" \
"v_pk_add_f16 v136, v128, v152\n" \
"v_pk_add_f16 v137, v129, v153\n" \
"ds_read_b128 v[176:179], v240 offset:45056\n" \
"v_mfma_f32_16x16x32_f16 v[108:111], v[140:143], v[160:163], v[108:111]\n" \
"v_pk_add_f16 v160, v160, v164\n" \
"v_pk_add_f16 v161, v161, v165\n" \
"ds_read_b128 v[180:183], v240 offset:45568\n" \
"v_mfma_f32_16x16x32_f16 v[100:103], v[144:147], v[164:167], v[100:103]\n" \
"v_pk_add_f16 v162, v162, v166\n" \
"v_pk_add_f16 v163, v163, v167\n" \
"s_add_i32 m0, s45, 0x5000\n" \
"s_add_i32 s81, s47, 0x4800000\n" \
"v_mfma_f32_16x16x32_f16 v[112:115], v[148:151], v[164:167], v[112:115]\n" \
"s_waitcnt lgkmcnt(3)\n" \
"v_pk_add_f16 v138, v130, v154\n" \
"v_pk_add_f16 v139, v131, v155\n" \
"buffer_load_dwordx4 v232, s[36:39], s81 offen sc1 lds\n" \
"v_mfma_f32_16x16x32_f16 v[104:107], v[120:123], v[160:163], v[104:107]\n" \
"v_pk_add_f16 v140, v132, v156\n" \
"v_pk_add_f16 v141, v133, v157\n" \
"v_mfma_f32_16x16x32_f16 v[116:119], v[124:127], v[160:163], v[116:119]\n" \
"s_setprio 1\n" \
"v_pk_add_f16 v142, v134, v158\n" \
"v_pk_add_f16 v143, v135, v159\n" \
"s_waitcnt vmcnt(7) lgkmcnt(0)\n" \
"s_barrier\n" \
"v_mfma_f32_16x16x32_f16 v[0:3], v[128:131], v[168:171], v[0:3]\n" \
"ds_read_b128 v[160:163], v240 offset:6144\n" \
"ds_read_b64_tr_b16 v[184:185], v238 offset:32768\n" \
"ds_read_b64_tr_b16 v[188:189], v239 offset:32768\n" \
"v_mfma_f32_16x16x32_f16 v[12:15], v[132:135], v[168:171], v[12:15]\n" \
"v_pk_add_f16 v168, v168, v172\n" \
"v_pk_add_f16 v169, v169, v173\n" \
"v_mfma_f32_16x16x32_f16 v[4:7], v[152:155], v[172:175], v[4:7]\n" \
"v_pk_add_f16 v170, v170, v174\n" \
"v_pk_add_f16 v171, v171, v175\n" \
"v_mfma_f32_16x16x32_f16 v[16:19], v[156:159], v[172:175], v[16:19]\n" \
"ds_read_b128 v[164:167], v240 offset:6656\n" \
"s_add_i32 m0, s44, 0xa000\n" \
"s_add_i32 s80, s46, 0x4c00000\n" \
"v_mfma_f32_16x16x32_f16 v[8:11], v[136:139], v[168:171], v[8:11]\n" \
"buffer_load_dwordx4 v232, s[36:39], s80 offen sc1 lds\n" \
"s_waitcnt lgkmcnt(2)\n" \
"v_mfma_f32_16x16x16_bf16 v[184:187], v[184:185], v[192:193], 0\n" \
"v_mfma_f32_16x16x32_f16 v[20:23], v[140:143], v[168:171], v[20:23]\n" \
"ds_read_b64_tr_b16 v[120:121], v241 offset:0\n" \
"ds_read_b64_tr_b16 v[122:123], v241 offset:8192\n" \
"s_waitcnt lgkmcnt(3)\n" \
"v_mfma_f32_16x16x16_bf16 v[188:191], v[188:189], v[192:193], 0\n" \
"v_mfma_f32_16x16x32_f16 v[24:27], v[128:131], v[176:179], v[24:27]\n" \
"ds_read_b128 v[168:171], v240 offset:8192\n" \
"v_mfma_f32_16x16x32_f16 v[36:39], v[132:135], v[176:179], v[36:39]\n" \
"v_pk_add_f16 v176, v176, v180\n" \
"v_pk_add_f16 v177, v177, v181\n" \
"ds_read_b64_tr_b16 v[124:125], v242 offset:0\n" \
"v_mfma_f32_16x16x32_f16 v[28:31], v[152:155], v[180:183], v[28:31]\n" \
"v_pk_add_f16 v178, v178, v182\n" \
"v_pk_add_f16 v179, v179, v183\n" \
"v_cvt_pk_f16_f32 v184, v184, v185\n" \
"v_mfma_f32_16x16x32_f16 v[40:43], v[156:159], v[180:183], v[40:43]\n" \
"v_cvt_pk_f16_f32 v185, v186, v187\n" \
"v_cvt_pk_f16_f32 v186, v188, v189\n" \
"ds_read_b128 v[172:175], v240 offset:8704\n" \
"v_mfma_f32_16x16x32_f16 v[32:35], v[136:139], v[176:179], v[32:35]\n" \
"v_cvt_pk_f16_f32 v187, v190, v191\n" \
"v_add_u32 v184, s91, v184\n" \
"buffer_load_dwordx4 v232, s[36:39], s80 offen offset:1024 sc1 lds\n" \
"v_mfma_f32_16x16x32_f16 v[44:47], v[140:143], v[176:179], v[44:47]\n" \
"v_add_u32 v185, s91, v185\n" \
"v_add_u32 v186, s91, v186\n" \
"ds_read_b64_tr_b16 v[126:127], v242 offset:8192\n" \
"v_mfma_f32_16x16x32_f16 v[48:51], v[128:131], v[160:163], v[48:51]\n" \
"v_add_u32 v187, s91, v187\n" \
"v_and_b32 v184, s92, v184\n" \
"ds_read_b128 v[176:179], v240 offset:0\n" \
"v_mfma_f32_16x16x32_f16 v[60:63], v[132:135], v[160:163], v[60:63]\n" \
"s_waitcnt lgkmcnt(4)\n" \
"v_pk_add_f16 v160, v160, v164\n" \
"v_pk_add_f16 v161, v161, v165\n" \
"v_and_b32 v185, s92, v185\n" \
"v_mfma_f32_16x16x32_f16 v[52:55], v[152:155], v[164:167], v[52:55]\n" \
"v_pk_add_f16 v162, v162, v166\n" \
"v_pk_add_f16 v163, v163, v167\n" \
"v_and_b32 v186, s92, v186\n" \
"v_and_b32 v187, s92, v187\n" \
"v_mfma_f32_16x16x32_f16 v[64:67], v[156:159], v[164:167], v[64:67]\n" \
"ds_read_b128 v[180:183], v240 offset:512\n" \
"buffer_store_dwordx4 v[184:187], v234, s[36:39], s82 offen offset:1024 sc1\n" \
"ds_read_b64_tr_b16 v[144:145], v241 offset:512\n" \
"v_mfma_f32_16x16x32_f16 v[56:59], v[136:139], v[160:163], v[56:59]\n" \
"buffer_load_dwordx4 v232, s[36:39], s80 offen offset:2048 sc1 lds\n" \
"v_mfma_f32_16x16x32_f16 v[68:71], v[140:143], v[160:163], v[68:71]\n" \
"s_setprio 0\n" \
"ds_read_b64_tr_b16 v[146:147], v241 offset:8704\n" \
"ds_read_b64_tr_b16 v[148:149], v242 offset:512\n" \
"v_mfma_f32_16x16x32_f16 v[72:75], v[128:131], v[168:171], v[72:75]\n" \
"ds_read_b128 v[160:163], v240 offset:2048\n" \
"ds_read_b64_tr_b16 v[150:151], v242 offset:8704\n" \
"v_mfma_f32_16x16x32_f16 v[84:87], v[132:135], v[168:171], v[84:87]\n" \
"s_waitcnt lgkmcnt(6)\n" \
"v_pk_add_f16 v168, v168, v172\n" \
"v_pk_add_f16 v169, v169, v173\n" \
"v_mfma_f32_16x16x32_f16 v[76:79], v[152:155], v[172:175], v[76:79]\n" \
"v_pk_add_f16 v170, v170, v174\n" \
"v_pk_add_f16 v171, v171, v175\n" \
"v_mfma_f32_16x16x32_f16 v[88:91], v[156:159], v[172:175], v[88:91]\n" \
"ds_read_b128 v[164:167], v240 offset:2560\n" \
"v_mfma_f32_16x16x32_f16 v[80:83], v[136:139], v[168:171], v[80:83]\n" \
"buffer_load_dwordx4 v232, s[36:39], s80 offen offset:3072 sc1 lds\n" \
"v_mfma_f32_16x16x32_f16 v[92:95], v[140:143], v[168:171], v[92:95]\n" \
"v_mfma_f32_16x16x32_f16 v[96:99], v[128:131], v[176:179], v[96:99]\n" \
"s_waitcnt lgkmcnt(5)\n" \
"v_pk_add_f16 v128, v120, v144\n" \
"v_pk_add_f16 v129, v121, v145\n" \
"ds_read_b128 v[168:171], v240 offset:4096\n" \
"v_mfma_f32_16x16x32_f16 v[108:111], v[132:135], v[176:179], v[108:111]\n" \
"v_pk_add_f16 v176, v176, v180\n" \
"v_pk_add_f16 v177, v177, v181\n" \
"ds_read_b128 v[172:175], v240 offset:4608\n" \
"v_mfma_f32_16x16x32_f16 v[100:103], v[152:155], v[180:183], v[100:103]\n" \
"v_pk_add_f16 v178, v178, v182\n" \
"v_pk_add_f16 v179, v179, v183\n" \
"s_add_i32 m0, s45, 0xa000\n" \
"s_add_i32 s81, s47, 0x4c00000\n" \
"v_mfma_f32_16x16x32_f16 v[112:115], v[156:159], v[180:183], v[112:115]\n" \
"s_waitcnt lgkmcnt(3)\n" \
"v_pk_add_f16 v130, v122, v146\n" \
"v_pk_add_f16 v131, v123, v147\n" \
"buffer_load_dwordx4 v232, s[36:39], s81 offen sc1 lds\n" \
"v_mfma_f32_16x16x32_f16 v[104:107], v[136:139], v[176:179], v[104:107]\n" \
"v_pk_add_f16 v132, v124, v148\n" \
"v_pk_add_f16 v133, v125, v149\n" \
"v_mfma_f32_16x16x32_f16 v[116:119], v[140:143], v[176:179], v[116:119]\n" \
"s_setprio 1\n" \
"v_pk_add_f16 v134, v126, v150\n" \
"v_pk_add_f16 v135, v127, v151\n" \
"s_waitcnt vmcnt(6) lgkmcnt(0)\n" \
"s_barrier\n" \
"v_mfma_f32_16x16x32_f16 v[0:3], v[120:123], v[160:163], v[0:3]\n" \
"ds_read_b128 v[176:179], v240 offset:26624\n" \
"ds_read_b64_tr_b16 v[184:185], v236 offset:36864\n" \
"ds_read_b64_tr_b16 v[188:189], v237 offset:36864\n" \
"s_add_i32 m0, s54, 0x0\n" \
"s_mov_b32 s83, 0x200\n" \
"v_mfma_f32_16x16x32_f16 v[12:15], v[124:127], v[160:163], v[12:15]\n" \
"v_pk_add_f16 v160, v160, v164\n" \
"v_pk_add_f16 v161, v161, v165\n" \
"buffer_load_dwordx4 v233, s[36:39], s83 offen sc1 lds\n" \
"v_mfma_f32_16x16x32_f16 v[4:7], v[144:147], v[164:167], v[4:7]\n" \
"v_pk_add_f16 v162, v162, v166\n" \
"v_pk_add_f16 v163, v163, v167\n" \
"v_mfma_f32_16x16x32_f16 v[16:19], v[148:151], v[164:167], v[16:19]\n" \
"ds_read_b128 v[180:183], v240 offset:27136\n" \
"s_add_i32 m0, s44, 0x0\n" \
"s_add_i32 s80, s46, 0x5000000\n" \
"v_mfma_f32_16x16x32_f16 v[8:11], v[128:131], v[160:163], v[8:11]\n" \
"buffer_load_dwordx4 v232, s[36:39], s80 offen sc1 lds\n" \
"s_waitcnt lgkmcnt(2)\n" \
"v_mfma_f32_16x16x16_bf16 v[184:187], v[184:185], v[192:193], 0\n" \
"v_mfma_f32_16x16x32_f16 v[20:23], v[132:135], v[160:163], v[20:23]\n" \
"ds_read_b64_tr_b16 v[136:137], v241 offset:16384\n" \
"ds_read_b64_tr_b16 v[138:139], v241 offset:24576\n" \
"s_waitcnt lgkmcnt(3)\n" \
"v_mfma_f32_16x16x16_bf16 v[188:191], v[188:189], v[192:193], 0\n" \
"v_mfma_f32_16x16x32_f16 v[24:27], v[120:123], v[168:171], v[24:27]\n" \
"ds_read_b128 v[160:163], v240 offset:28672\n" \
"v_mfma_f32_16x16x32_f16 v[36:39], v[124:127], v[168:171], v[36:39]\n" \
"v_pk_add_f16 v168, v168, v172\n" \
"v_pk_add_f16 v169, v169, v173\n" \
"ds_read_b64_tr_b16 v[140:141], v242 offset:16384\n" \
"v_mfma_f32_16x16x32_f16 v[28:31], v[144:147], v[172:175], v[28:31]\n" \
"v_pk_add_f16 v170, v170, v174\n" \
"v_pk_add_f16 v171, v171, v175\n" \
"v_cvt_pk_f16_f32 v184, v184, v185\n" \
"v_mfma_f32_16x16x32_f16 v[40:43], v[148:151], v[172:175], v[40:43]\n" \
"v_cvt_pk_f16_f32 v185, v186, v187\n" \
"v_cvt_pk_f16_f32 v186, v188, v189\n" \
"ds_read_b128 v[164:167], v240 offset:29184\n" \
"v_mfma_f32_16x16x32_f16 v[32:35], v[128:131], v[168:171], v[32:35]\n" \
"v_cvt_pk_f16_f32 v187, v190, v191\n" \
"v_add_u32 v184, s91, v184\n" \
"buffer_load_dwordx4 v232, s[36:39], s80 offen offset:1024 sc1 lds\n" \
"v_mfma_f32_16x16x32_f16 v[44:47], v[132:135], v[168:171], v[44:47]\n" \
"v_add_u32 v185, s91, v185\n" \
"v_add_u32 v186, s91, v186\n" \
"ds_read_b64_tr_b16 v[142:143], v242 offset:24576\n" \
"v_mfma_f32_16x16x32_f16 v[48:51], v[120:123], v[176:179], v[48:51]\n" \
"v_add_u32 v187, s91, v187\n" \
"v_and_b32 v184, s92, v184\n" \
"ds_read_b128 v[168:171], v240 offset:20480\n" \
"v_mfma_f32_16x16x32_f16 v[60:63], v[124:127], v[176:179], v[60:63]\n" \
"s_waitcnt lgkmcnt(4)\n" \
"v_pk_add_f16 v176, v176, v180\n" \
"v_pk_add_f16 v177, v177, v181\n" \
"v_and_b32 v185, s92, v185\n" \
"s_add_i32 s82, s50, 0x7000000\n" \
"v_mfma_f32_16x16x32_f16 v[52:55], v[144:147], v[180:183], v[52:55]\n" \
"v_pk_add_f16 v178, v178, v182\n" \
"v_pk_add_f16 v179, v179, v183\n" \
"v_and_b32 v186, s92, v186\n" \
"v_and_b32 v187, s92, v187\n" \
"v_mfma_f32_16x16x32_f16 v[64:67], v[148:151], v[180:183], v[64:67]\n" \
"ds_read_b128 v[172:175], v240 offset:20992\n" \
"buffer_store_dwordx4 v[184:187], v234, s[36:39], s82 offen sc1\n" \
"ds_read_b64_tr_b16 v[152:153], v241 offset:16896\n" \
"v_mfma_f32_16x16x32_f16 v[56:59], v[128:131], v[176:179], v[56:59]\n" \
"buffer_load_dwordx4 v232, s[36:39], s80 offen offset:2048 sc1 lds\n" \
"v_mfma_f32_16x16x32_f16 v[68:71], v[132:135], v[176:179], v[68:71]\n" \
"s_setprio 0\n" \
"ds_read_b64_tr_b16 v[154:155], v241 offset:25088\n" \
"ds_read_b64_tr_b16 v[156:157], v242 offset:16896\n" \
"v_mfma_f32_16x16x32_f16 v[72:75], v[120:123], v[160:163], v[72:75]\n" \
"ds_read_b128 v[176:179], v240 offset:22528\n" \
"ds_read_b64_tr_b16 v[158:159], v242 offset:25088\n" \
"v_mfma_f32_16x16x32_f16 v[84:87], v[124:127], v[160:163], v[84:87]\n" \
"s_waitcnt lgkmcnt(6)\n" \
"v_pk_add_f16 v160, v160, v164\n" \
"v_pk_add_f16 v161, v161, v165\n" \
"v_mfma_f32_16x16x32_f16 v[76:79], v[144:147], v[164:167], v[76:79]\n" \
"v_pk_add_f16 v162, v162, v166\n" \
"v_pk_add_f16 v163, v163, v167\n" \
"v_mfma_f32_16x16x32_f16 v[88:91], v[148:151], v[164:167], v[88:91]\n" \
"ds_read_b128 v[180:183], v240 offset:23040\n" \
"v_mfma_f32_16x16x32_f16 v[80:83], v[128:131], v[160:163], v[80:83]\n" \
"buffer_load_dwordx4 v232, s[36:39], s80 offen offset:3072 sc1 lds\n" \
"v_mfma_f32_16x16x32_f16 v[92:95], v[132:135], v[160:163], v[92:95]\n" \
"v_mfma_f32_16x16x32_f16 v[96:99], v[120:123], v[168:171], v[96:99]\n" \
"s_waitcnt lgkmcnt(5)\n" \
"v_pk_add_f16 v120, v136, v152\n" \
"v_pk_add_f16 v121, v137, v153\n" \
"ds_read_b128 v[160:163], v240 offset:24576\n" \
"v_mfma_f32_16x16x32_f16 v[108:111], v[124:127], v[168:171], v[108:111]\n" \
"v_pk_add_f16 v168, v168, v172\n" \
"v_pk_add_f16 v169, v169, v173\n" \
"ds_read_b128 v[164:167], v240 offset:25088\n" \
"v_mfma_f32_16x16x32_f16 v[100:103], v[144:147], v[172:175], v[100:103]\n" \
"v_pk_add_f16 v170, v170, v174\n" \
"v_pk_add_f16 v171, v171, v175\n" \
"s_add_i32 m0, s45, 0x0\n" \
"s_add_i32 s81, s47, 0x5000000\n" \
"v_mfma_f32_16x16x32_f16 v[112:115], v[148:151], v[172:175], v[112:115]\n" \
"s_waitcnt lgkmcnt(3)\n" \
"v_pk_add_f16 v122, v138, v154\n" \
"v_pk_add_f16 v123, v139, v155\n" \
"buffer_load_dwordx4 v232, s[36:39], s81 offen sc1 lds\n" \
"v_mfma_f32_16x16x32_f16 v[104:107], v[128:131], v[168:171], v[104:107]\n" \
"v_pk_add_f16 v124, v140, v156\n" \
"v_pk_add_f16 v125, v141, v157\n" \
"v_mfma_f32_16x16x32_f16 v[116:119], v[132:135], v[168:171], v[116:119]\n" \
"s_setprio 1\n" \
"v_pk_add_f16 v126, v142, v158\n" \
"v_pk_add_f16 v127, v143, v159\n" \
"s_waitcnt vmcnt(7) lgkmcnt(0)\n" \
"s_barrier\n" \
"v_mfma_f32_16x16x32_f16 v[0:3], v[136:139], v[176:179], v[0:3]\n" \
"ds_read_b128 v[168:171], v240 offset:47104\n" \
"ds_read_b64_tr_b16 v[184:185], v238 offset:36864\n" \
"ds_read_b64_tr_b16 v[188:189], v239 offset:36864\n" \
"v_mfma_f32_16x16x32_f16 v[12:15], v[140:143], v[176:179], v[12:15]\n" \
"v_pk_add_f16 v176, v176, v180\n" \
"v_pk_add_f16 v177, v177, v181\n" \
"v_mfma_f32_16x16x32_f16 v[4:7], v[152:155], v[180:183], v[4:7]\n" \
"v_pk_add_f16 v178, v178, v182\n" \
"v_pk_add_f16 v179, v179, v183\n" \
"v_mfma_f32_16x16x32_f16 v[16:19], v[156:159], v[180:183], v[16:19]\n" \
"ds_read_b128 v[172:175], v240 offset:47616\n" \
"s_add_i32 m0, s44, 0x5000\n" \
"s_add_i32 s80, s46, 0x5400000\n" \
"v_mfma_f32_16x16x32_f16 v[8:11], v[120:123], v[176:179], v[8:11]\n" \
"buffer_load_dwordx4 v232, s[36:39], s80 offen sc1 lds\n" \
"s_waitcnt lgkmcnt(2)\n" \
"v_mfma_f32_16x16x16_bf16 v[184:187], v[184:185], v[192:193], 0\n" \
"v_mfma_f32_16x16x32_f16 v[20:23], v[124:127], v[176:179], v[20:23]\n" \
"ds_read_b64_tr_b16 v[128:129], v241 offset:32768\n" \
"ds_read_b64_tr_b16 v[130:131], v241 offset:40960\n" \
"s_waitcnt lgkmcnt(3)\n" \
"v_mfma_f32_16x16x16_bf16 v[188:191], v[188:189], v[192:193], 0\n" \
"v_mfma_f32_16x16x32_f16 v[24:27], v[136:139], v[160:163], v[24:27]\n" \
"ds_read_b128 v[176:179], v240 offset:49152\n" \
"v_mfma_f32_16x16x32_f16 v[36:39], v[140:143], v[160:163], v[36:39]\n" \
"v_pk_add_f16 v160, v160, v164\n" \
"v_pk_add_f16 v161, v161, v165\n" \
"ds_read_b64_tr_b16 v[132:133], v242 offset:32768\n" \
"v_mfma_f32_16x16x32_f16 v[28:31], v[152:155], v[164:167], v[28:31]\n" \
"v_pk_add_f16 v162, v162, v166\n" \
"v_pk_add_f16 v163, v163, v167\n" \
"v_cvt_pk_f16_f32 v184, v184, v185\n" \
"v_mfma_f32_16x16x32_f16 v[40:43], v[156:159], v[164:167], v[40:43]\n" \
"v_cvt_pk_f16_f32 v185, v186, v187\n" \
"v_cvt_pk_f16_f32 v186, v188, v189\n" \
"ds_read_b128 v[180:183], v240 offset:49664\n" \
"v_mfma_f32_16x16x32_f16 v[32:35], v[120:123], v[160:163], v[32:35]\n" \
"v_cvt_pk_f16_f32 v187, v190, v191\n" \
"v_add_u32 v184, s91, v184\n" \
"buffer_load_dwordx4 v232, s[36:39], s80 offen offset:1024 sc1 lds\n" \
"v_mfma_f32_16x16x32_f16 v[44:47], v[124:127], v[160:163], v[44:47]\n" \
"v_add_u32 v185, s91, v185\n" \
"v_add_u32 v186, s91, v186\n" \
"ds_read_b64_tr_b16 v[134:135], v242 offset:40960\n" \
"v_mfma_f32_16x16x32_f16 v[48:51], v[136:139], v[168:171], v[48:51]\n" \
"v_add_u32 v187, s91, v187\n" \
"v_and_b32 v184, s92, v184\n" \
"ds_read_b128 v[160:163], v240 offset:40960\n" \
"v_mfma_f32_16x16x32_f16 v[60:63], v[140:143], v[168:171], v[60:63]\n" \
"s_waitcnt lgkmcnt(4)\n" \
"v_pk_add_f16 v168, v168, v172\n" \
"v_pk_add_f16 v169, v169, v173\n" \
"v_and_b32 v185, s92, v185\n" \
"v_mfma_f32_16x16x32_f16 v[52:55], v[152:155], v[172:175], v[52:55]\n" \
"v_pk_add_f16 v170, v170, v174\n" \
"v_pk_add_f16 v171, v171, v175\n" \
"v_and_b32 v186, s92, v186\n" \
"v_and_b32 v187, s92, v187\n" \
"v_mfma_f32_16x16x32_f16 v[64:67], v[156:159], v[172:175], v[64:67]\n" \
"ds_read_b128 v[164:167], v240 offset:41472\n" \
"buffer_store_dwordx4 v[184:187], v234, s[36:39], s82 offen offset:1024 sc1\n" \
"ds_read_b64_tr_b16 v[144:145], v241 offset:33280\n" \
"v_mfma_f32_16x16x32_f16 v[56:59], v[120:123], v[168:171], v[56:59]\n" \
"buffer_load_dwordx4 v232, s[36:39], s80 offen offset:2048 sc1 lds\n" \
"v_mfma_f32_16x16x32_f16 v[68:71], v[124:127], v[168:171], v[68:71]\n" \
"s_setprio 0\n" \
"ds_read_b64_tr_b16 v[146:147], v241 offset:41472\n" \
"ds_read_b64_tr_b16 v[148:149], v242 offset:33280\n" \
"v_mfma_f32_16x16x32_f16 v[72:75], v[136:139], v[176:179], v[72:75]\n" \
"ds_read_b128 v[168:171], v240 offset:43008\n" \
"ds_read_b64_tr_b16 v[150:151], v242 offset:41472\n" \
"v_mfma_f32_16x16x32_f16 v[84:87], v[140:143], v[176:179], v[84:87]\n" \
"s_waitcnt lgkmcnt(6)\n" \
"v_pk_add_f16 v176, v176, v180\n" \
"v_pk_add_f16 v177, v177, v181\n" \
"v_mfma_f32_16x16x32_f16 v[76:79], v[152:155], v[180:183], v[76:79]\n" \
"v_pk_add_f16 v178, v178, v182\n" \
"v_pk_add_f16 v179, v179, v183\n" \
"v_mfma_f32_16x16x32_f16 v[88:91], v[156:159], v[180:183], v[88:91]\n" \
"ds_read_b128 v[172:175], v240 offset:43520\n" \
"v_mfma_f32_16x16x32_f16 v[80:83], v[120:123], v[176:179], v[80:83]\n" \
"buffer_load_dwordx4 v232, s[36:39], s80 offen offset:3072 sc1 lds\n" \
"v_mfma_f32_16x16x32_f16 v[92:95], v[124:127], v[176:179], v[92:95]\n" \
"v_mfma_f32_16x16x32_f16 v[96:99], v[136:139], v[160:163], v[96:99]\n" \
"s_waitcnt lgkmcnt(5)\n" \
"v_pk_add_f16 v136, v128, v144\n" \
"v_pk_add_f16 v137, v129, v145\n" \
"ds_read_b128 v[176:179], v240 offset:45056\n" \
"v_mfma_f32_16x16x32_f16 v[108:111], v[140:143], v[160:163], v[108:111]\n" \
"v_pk_add_f16 v160, v160, v164\n" \
"v_pk_add_f16 v161, v161, v165\n" \
"ds_read_b128 v[180:183], v240 offset:45568\n" \
"v_mfma_f32_16x16x32_f16 v[100:103], v[152:155], v[164:167], v[100:103]\n" \
"v_pk_add_f16 v162, v162, v166\n" \
"v_pk_add_f16 v163, v163, v167\n" \
"s_add_i32 m0, s45, 0x5000\n" \
"s_add_i32 s81, s47, 0x5400000\n" \
"v_mfma_f32_16x16x32_f16 v[112:115], v[156:159], v[164:167], v[112:115]\n" \
"s_waitcnt lgkmcnt(3)\n" \
"v_pk_add_f16 v138, v130, v146\n" \
"v_pk_add_f16 v139, v131, v147\n" \
"buffer_load_dwordx4 v232, s[36:39], s81 offen sc1 lds\n" \
"v_mfma_f32_16x16x32_f16 v[104:107], v[120:123], v[160:163], v[104:107]\n" \
"v_pk_add_f16 v140, v132, v148\n" \
"v_pk_add_f16 v141, v133, v149\n" \
"v_mfma_f32_16x16x32_f16 v[116:119], v[124:127], v[160:163], v[116:119]\n" \
"s_setprio 1\n" \
"v_pk_add_f16 v142, v134, v150\n" \
"v_pk_add_f16 v143, v135, v151\n" \
"s_waitcnt vmcnt(6) lgkmcnt(0)\n" \
"s_barrier\n" \
"v_mfma_f32_16x16x32_f16 v[0:3], v[128:131], v[168:171], v[0:3]\n" \
"ds_read_b128 v[160:163], v240 offset:6144\n" \
"ds_read_b64_tr_b16 v[184:185], v236 offset:40960\n" \
"s_add_i32 m0, s54, 0x1000\n" \
"s_mov_b32 s83, 0x300\n" \
"v_mfma_f32_16x16x32_f16 v[12:15], v[132:135], v[168:171], v[12:15]\n" \
"v_pk_add_f16 v168, v168, v172\n" \
"v_pk_add_f16 v169, v169, v173\n" \
"s_mov_b32 s84, 0x8014000\n" \
"buffer_load_dwordx4 v233, s[36:39], s83 offen sc1 lds\n" \
"v_mfma_f32_16x16x32_f16 v[4:7], v[144:147], v[172:175], v[4:7]\n" \
"v_pk_add_f16 v170, v170, v174\n" \
"v_pk_add_f16 v171, v171, v175\n" \
"buffer_load_dword v253, v252, s[36:39], s84 offen sc1\n" \
"v_mfma_f32_16x16x32_f16 v[16:19], v[148:151], v[172:175], v[16:19]\n" \
"ds_read_b128 v[164:167], v240 offset:6656\n" \
"s_add_i32 m0, s44, 0xa000\n" \
"s_add_i32 s80, s46, 0x5800000\n" \
"ds_read_b64_tr_b16 v[188:189], v237 offset:40960\n" \
"v_mfma_f32_16x16x32_f16 v[8:11], v[136:139], v[168:171], v[8:11]\n" \
"buffer_load_dwordx4 v232, s[36:39], s80 offen sc1 lds\n" \
"v_mfma_f32_16x16x32_f16 v[20:23], v[140:143], v[168:171], v[20:23]\n" \
"ds_read_b64_tr_b16 v[120:121], v241 offset:0\n" \
"ds_read_b64_tr_b16 v[122:123], v241 offset:8192\n" \
"s_waitcnt lgkmcnt(4)\n" \
"v_mfma_f32_16x16x16_bf16 v[184:187], v[184:185], v[192:193], 0\n" \
"v_mfma_f32_16x16x32_f16 v[24:27], v[128:131], v[176:179], v[24:27]\n" \
"ds_read_b128 v[168:171], v240 offset:8192\n" \
"s_waitcnt lgkmcnt(3)\n" \
"v_mfma_f32_16x16x16_bf16 v[188:191], v[188:189], v[192:193], 0\n" \
"v_mfma_f32_16x16x32_f16 v[36:39], v[132:135], v[176:179], v[36:39]\n" \
"v_pk_add_f16 v176, v176, v180\n" \
"v_pk_add_f16 v177, v177, v181\n" \
"ds_read_b64_tr_b16 v[124:125], v242 offset:0\n" \
"v_mfma_f32_16x16x32_f16 v[28:31], v[144:147], v[180:183], v[28:31]\n" \
"v_pk_add_f16 v178, v178, v182\n" \
"v_pk_add_f16 v179, v179, v183\n" \
"v_mfma_f32_16x16x32_f16 v[40:43], v[148:151], v[180:183], v[40:43]\n" \
"v_cvt_pk_f16_f32 v184, v184, v185\n" \
"ds_read_b128 v[172:175], v240 offset:8704\n" \
"ds_read_b64_tr_b16 v[126:127], v242 offset:8192\n" \
"v_mfma_f32_16x16x32_f16 v[32:35], v[136:139], v[176:179], v[32:35]\n" \
"v_cvt_pk_f16_f32 v185, v186, v187\n" \
"v_cvt_pk_f16_f32 v186, v188, v189\n" \
"buffer_load_dwordx4 v232, s[36:39], s80 offen offset:1024 sc1 lds\n" \
"v_mfma_f32_16x16x32_f16 v[44:47], v[140:143], v[176:179], v[44:47]\n" \
"v_cvt_pk_f16_f32 v187, v190, v191\n" \
"v_add_u32 v184, s91, v184\n" \
"v_mfma_f32_16x16x32_f16 v[48:51], v[128:131], v[160:163], v[48:51]\n" \
"v_add_u32 v185, s91, v185\n" \
"v_add_u32 v186, s91, v186\n" \
"ds_read_b128 v[176:179], v240 offset:0\n" \
"v_mfma_f32_16x16x32_f16 v[60:63], v[132:135], v[160:163], v[60:63]\n" \
"v_pk_add_f16 v160, v160, v164\n" \
"v_pk_add_f16 v161, v161, v165\n" \
"v_add_u32 v187, s91, v187\n" \
"v_and_b32 v184, s92, v184\n" \
"s_add_i32 s82, s51, 0x7800000\n" \
"v_mfma_f32_16x16x32_f16 v[52:55], v[144:147], v[164:167], v[52:55]\n" \
"v_pk_add_f16 v162, v162, v166\n" \
"v_pk_add_f16 v163, v163, v167\n" \
"v_and_b32 v185, s92, v185\n" \
"v_and_b32 v186, s92, v186\n" \
"v_and_b32 v187, s92, v187\n" \
"v_mfma_f32_16x16x32_f16 v[64:67], v[148:151], v[164:167], v[64:67]\n" \
"ds_read_b128 v[180:183], v240 offset:512\n" \
"buffer_store_dwordx4 v[184:187], v234, s[36:39], s82 offen sc1\n" \
"ds_read_b64_tr_b16 v[152:153], v241 offset:512\n" \
"v_mfma_f32_16x16x32_f16 v[56:59], v[136:139], v[160:163], v[56:59]\n" \
"buffer_load_dwordx4 v232, s[36:39], s80 offen offset:2048 sc1 lds\n" \
"ds_read_b64_tr_b16 v[154:155], v241 offset:8704\n" \
"v_mfma_f32_16x16x32_f16 v[68:71], v[140:143], v[160:163], v[68:71]\n" \
"s_setprio 0\n" \
"ds_read_b64_tr_b16 v[184:185], v238 offset:40960\n" \
"ds_read_b64_tr_b16 v[188:189], v239 offset:40960\n" \
"s_waitcnt lgkmcnt(5)\n" \
"v_mfma_f32_16x16x32_f16 v[72:75], v[128:131], v[168:171], v[72:75]\n" \
"ds_read_b128 v[160:163], v240 offset:2048\n" \
"ds_read_b64_tr_b16 v[156:157], v242 offset:512\n" \
"v_mfma_f32_16x16x32_f16 v[84:87], v[132:135], v[168:171], v[84:87]\n" \
"v_pk_add_f16 v168, v168, v172\n" \
"v_pk_add_f16 v169, v169, v173\n" \
"v_mfma_f32_16x16x32_f16 v[76:79], v[144:147], v[172:175], v[76:79]\n" \
"v_pk_add_f16 v170, v170, v174\n" \
"v_pk_add_f16 v171, v171, v175\n" \
"s_waitcnt lgkmcnt(3)\n" \
"v_mfma_f32_16x16x16_bf16 v[184:187], v[184:185], v[192:193], 0\n" \
"v_mfma_f32_16x16x32_f16 v[88:91], v[148:151], v[172:175], v[88:91]\n" \
"ds_read_b128 v[164:167], v240 offset:2560\n" \
"ds_read_b64_tr_b16 v[158:159], v242 offset:8704\n" \
"s_waitcnt lgkmcnt(4)\n" \
"v_mfma_f32_16x16x16_bf16 v[188:191], v[188:189], v[192:193], 0\n" \
"v_mfma_f32_16x16x32_f16 v[80:83], v[136:139], v[168:171], v[80:83]\n" \
"buffer_load_dwordx4 v232, s[36:39], s80 offen offset:3072 sc1 lds\n" \
"v_mfma_f32_16x16x32_f16 v[92:95], v[140:143], v[168:171], v[92:95]\n" \
"v_mfma_f32_16x16x32_f16 v[96:99], v[128:131], v[176:179], v[96:99]\n" \
"v_pk_add_f16 v128, v120, v152\n" \
"v_pk_add_f16 v129, v121, v153\n" \
"v_cvt_pk_f16_f32 v184, v184, v185\n" \
"v_cvt_pk_f16_f32 v185, v186, v187\n" \
"ds_read_b128 v[168:171], v240 offset:4096\n" \
"v_mfma_f32_16x16x32_f16 v[108:111], v[132:135], v[176:179], v[108:111]\n" \
"v_pk_add_f16 v176, v176, v180\n" \
"v_pk_add_f16 v177, v177, v181\n" \
"v_cvt_pk_f16_f32 v186, v188, v189\n" \
"v_cvt_pk_f16_f32 v187, v190, v191\n" \
"v_add_u32 v184, s91, v184\n" \
"v_add_u32 v185, s91, v185\n" \
"v_add_u32 v186, s91, v186\n" \
"ds_read_b128 v[172:175], v240 offset:4608\n" \
"v_mfma_f32_16x16x32_f16 v[100:103], v[144:147], v[180:183], v[100:103]\n" \
"v_pk_add_f16 v178, v178, v182\n" \
"v_pk_add_f16 v179, v179, v183\n" \
"s_waitcnt vmcnt(5)\n" \
"v_cmp_eq_u32 vcc, 0, v253\n" \
"s_cbranch_vccnz .Ls110_%=\n" \
".Lr110_%=:\n" \
"s_add_i32 m0, s45, 0xa000\n" \
"s_add_i32 s81, s47, 0x5800000\n" \
"v_mfma_f32_16x16x32_f16 v[112:115], v[148:151], v[180:183], v[112:115]\n" \
"v_pk_add_f16 v130, v122, v154\n" \
"v_pk_add_f16 v131, v123, v155\n" \
"v_add_u32 v187, s91, v187\n" \
"v_and_b32 v184, s92, v184\n" \
"buffer_load_dwordx4 v232, s[36:39], s81 offen sc1 lds\n" \
"v_mfma_f32_16x16x32_f16 v[104:107], v[136:139], v[176:179], v[104:107]\n" \
"s_waitcnt lgkmcnt(1)\n" \
"v_pk_add_f16 v132, v124, v156\n" \
"v_pk_add_f16 v133, v125, v157\n" \
"v_and_b32 v185, s92, v185\n" \
"v_and_b32 v186, s92, v186\n" \
"v_and_b32 v187, s92, v187\n" \
"v_mfma_f32_16x16x32_f16 v[116:119], v[140:143], v[176:179], v[116:119]\n" \
"s_setprio 1\n" \
"v_pk_add_f16 v134, v126, v158\n" \
"v_pk_add_f16 v135, v127, v159\n" \
"buffer_store_dwordx4 v[184:187], v234, s[36:39], s82 offen offset:1024 sc1\n" \
"s_waitcnt vmcnt(7) lgkmcnt(0)\n" \
"s_barrier\n" \
"v_mfma_f32_16x16x32_f16 v[0:3], v[120:123], v[160:163], v[0:3]\n" \
"ds_read_b128 v[176:179], v240 offset:26624\n" \
"v_mfma_f32_16x16x32_f16 v[12:15], v[124:127], v[160:163], v[12:15]\n" \
"v_pk_add_f16 v160, v160, v164\n" \
"v_pk_add_f16 v161, v161, v165\n" \
"v_mfma_f32_16x16x32_f16 v[4:7], v[152:155], v[164:167], v[4:7]\n" \
"v_pk_add_f16 v162, v162, v166\n" \
"v_pk_add_f16 v163, v163, v167\n" \
"v_mfma_f32_16x16x32_f16 v[16:19], v[156:159], v[164:167], v[16:19]\n" \
"ds_read_b128 v[180:183], v240 offset:27136\n" \
"s_add_i32 m0, s44, 0x0\n" \
"s_add_i32 s80, s46, 0x5c00000\n" \
"v_mfma_f32_16x16x32_f16 v[8:11], v[128:131], v[160:163], v[8:11]\n" \
"buffer_load_dwordx4 v232, s[36:39], s80 offen sc1 lds\n" \
"v_mfma_f32_16x16x32_f16 v[20:23], v[132:135], v[160:163], v[20:23]\n" \
"ds_read_b64_tr_b16 v[136:137], v241 offset:16384\n" \
"ds_read_b64_tr_b16 v[138:139], v241 offset:24576\n" \
"v_mfma_f32_16x16x32_f16 v[24:27], v[120:123], v[168:171], v[24:27]\n" \
"ds_read_b128 v[160:163], v240 offset:28672\n" \
"v_mfma_f32_16x16x32_f16 v[36:39], v[124:127], v[168:171], v[36:39]\n" \
"v_pk_add_f16 v168, v168, v172\n" \
"v_pk_add_f16 v169, v169, v173\n" \
"v_mfma_f32_16x16x32_f16 v[28:31], v[152:155], v[172:175], v[28:31]\n" \
"v_pk_add_f16 v170, v170, v174\n" \
"v_pk_add_f16 v171, v171, v175\n" \
"v_mfma_f32_16x16x32_f16 v[40:43], v[156:159], v[172:175], v[40:43]\n" \
"ds_read_b128 v[164:167], v240 offset:29184\n" \
"ds_read_b64_tr_b16 v[140:141], v242 offset:16384\n" \
"v_mfma_f32_16x16x32_f16 v[32:35], v[128:131], v[168:171], v[32:35]\n" \
"buffer_load_dwordx4 v232, s[36:39], s80 offen offset:1024 sc1 lds\n" \
"v_mfma_f32_16x16x32_f16 v[44:47], v[132:135], v[168:171], v[44:47]\n" \
"ds_read_b64_tr_b16 v[142:143], v242 offset:24576\n" \
"s_waitcnt lgkmcnt(7)\n" \
"v_mfma_f32_16x16x32_f16 v[48:51], v[120:123], v[176:179], v[48:51]\n" \
"ds_read_b128 v[168:171], v240 offset:20480\n" \
"ds_read_b64_tr_b16 v[144:145], v241 offset:16896\n" \
"v_mfma_f32_16x16x32_f16 v[60:63], v[124:127], v[176:179], v[60:63]\n" \
"s_waitcnt lgkmcnt(8)\n" \
"v_pk_add_f16 v176, v176, v180\n" \
"v_pk_add_f16 v177, v177, v181\n" \
"v_mfma_f32_16x16x32_f16 v[52:55], v[152:155], v[180:183], v[52:55]\n" \
"v_pk_add_f16 v178, v178, v182\n" \
"v_pk_add_f16 v179, v179, v183\n" \
"v_mfma_f32_16x16x32_f16 v[64:67], v[156:159], v[180:183], v[64:67]\n" \
"ds_read_b128 v[172:175], v240 offset:20992\n" \
"v_mfma_f32_16x16x32_f16 v[56:59], v[128:131], v[176:179], v[56:59]\n" \
"buffer_load_dwordx4 v232, s[36:39], s80 offen offset:2048 sc1 lds\n" \
"v_mfma_f32_16x16x32_f16 v[68:71], v[132:135], v[176:179], v[68:71]\n" \
"s_setprio 0\n" \
"ds_read_b64_tr_b16 v[146:147], v241 offset:25088\n" \
"ds_read_b64_tr_b16 v[148:149], v242 offset:16896\n" \
"s_waitcnt lgkmcnt(6)\n" \
"v_mfma_f32_16x16x32_f16 v[72:75], v[120:123], v[160:163], v[72:75]\n" \
"ds_read_b128 v[176:179], v240 offset:22528\n" \
"ds_read_b64_tr_b16 v[150:151], v242 offset:25088\n" \
"v_mfma_f32_16x16x32_f16 v[84:87], v[124:127], v[160:163], v[84:87]\n" \
"v_pk_add_f16 v160, v160, v164\n" \
"v_pk_add_f16 v161, v161, v165\n" \
"v_mfma_f32_16x16x32_f16 v[76:79], v[152:155], v[164:167], v[76:79]\n" \
"v_pk_add_f16 v162, v162, v166\n" \
"v_pk_add_f16 v163, v163, v167\n" \
"v_mfma_f32_16x16x32_f16 v[88:91], v[156:159], v[164:167], v[88:91]\n" \
"ds_read_b128 v[180:183], v240 offset:23040\n" \
"v_mfma_f32_16x16x32_f16 v[80:83], v[128:131], v[160:163], v[80:83]\n" \
"buffer_load_dwordx4 v232, s[36:39], s80 offen offset:3072 sc1 lds\n" \
"v_mfma_f32_16x16x32_f16 v[92:95], v[132:135], v[160:163], v[92:95]\n" \
"s_waitcnt lgkmcnt(6)\n" \
"v_mfma_f32_16x16x32_f16 v[96:99], v[120:123], v[168:171], v[96:99]\n" \
"v_pk_add_f16 v120, v136, v144\n" \
"v_pk_add_f16 v121, v137, v145\n" \
"ds_read_b128 v[160:163], v240 offset:24576\n" \
"v_mfma_f32_16x16x32_f16 v[108:111], v[124:127], v[168:171], v[108:111]\n" \
"s_waitcnt lgkmcnt(6)\n" \
"v_pk_add_f16 v168, v168, v172\n" \
"v_pk_add_f16 v169, v169, v173\n" \
"ds_read_b128 v[164:167], v240 offset:25088\n" \
"v_mfma_f32_16x16x32_f16 v[100:103], v[152:155], v[172:175], v[100:103]\n" \
"v_pk_add_f16 v170, v170, v174\n" \
"v_pk_add_f16 v171, v171, v175\n" \
"s_add_i32 m0, s45, 0x0\n" \
"s_add_i32 s81, s47, 0x5c00000\n" \
"v_mfma_f32_16x16x32_f16 v[112:115], v[156:159], v[172:175], v[112:115]\n" \
"s_waitcnt lgkmcnt(3)\n" \
"v_pk_add_f16 v122, v138, v146\n" \
"v_pk_add_f16 v123, v139, v147\n" \
"buffer_load_dwordx4 v232, s[36:39], s81 offen sc1 lds\n" \
"v_mfma_f32_16x16x32_f16 v[104:107], v[128:131], v[168:171], v[104:107]\n" \
"v_pk_add_f16 v124, v140, v148\n" \
"v_pk_add_f16 v125, v141, v149\n" \
"v_mfma_f32_16x16x32_f16 v[116:119], v[132:135], v[168:171], v[116:119]\n" \
"s_setprio 1\n" \
"v_pk_add_f16 v126, v142, v150\n" \
"v_pk_add_f16 v127, v143, v151\n" \
"s_waitcnt vmcnt(5) lgkmcnt(0)\n" \
"s_barrier\n" \
"v_mfma_f32_16x16x32_f16 v[0:3], v[136:139], v[176:179], v[0:3]\n" \
"ds_read_b128 v[168:171], v240 offset:47104\n" \
"v_mfma_f32_16x16x32_f16 v[12:15], v[140:143], v[176:179], v[12:15]\n" \
"v_pk_add_f16 v176, v176, v180\n" \
"v_pk_add_f16 v177, v177, v181\n" \
"v_mfma_f32_16x16x32_f16 v[4:7], v[144:147], v[180:183], v[4:7]\n" \
"v_pk_add_f16 v178, v178, v182\n" \
"v_pk_add_f16 v179, v179, v183\n" \
"v_mfma_f32_16x16x32_f16 v[16:19], v[148:151], v[180:183], v[16:19]\n" \
"ds_read_b128 v[172:175], v240 offset:47616\n" \
"s_add_i32 m0, s44, 0x5000\n" \
"s_add_i32 s80, s46, 0x6000000\n" \
"v_mfma_f32_16x16x32_f16 v[8:11], v[120:123], v[176:179], v[8:11]\n" \
"buffer_load_dwordx4 v232, s[36:39], s80 offen sc1 lds\n" \
"v_mfma_f32_16x16x32_f16 v[20:23], v[124:127], v[176:179], v[20:23]\n" \
"ds_read_b64_tr_b16 v[128:129], v241 offset:32768\n" \
"ds_read_b64_tr_b16 v[130:131], v241 offset:40960\n" \
"v_mfma_f32_16x16x32_f16 v[24:27], v[136:139], v[160:163], v[24:27]\n" \
"ds_read_b128 v[176:179], v240 offset:49152\n" \
"v_mfma_f32_16x16x32_f16 v[36:39], v[140:143], v[160:163], v[36:39]\n" \
"v_pk_add_f16 v160, v160, v164\n" \
"v_pk_add_f16 v161, v161, v165\n" \
"v_mfma_f32_16x16x32_f16 v[28:31], v[144:147], v[164:167], v[28:31]\n" \
"v_pk_add_f16 v162, v162, v166\n" \
"v_pk_add_f16 v163, v163, v167\n" \
"v_mfma_f32_16x16x32_f16 v[40:43], v[148:151], v[164:167], v[40:43]\n" \
"ds_read_b128 v[180:183], v240 offset:49664\n" \
"ds_read_b64_tr_b16 v[132:133], v242 offset:32768\n" \
"v_mfma_f32_16x16x32_f16 v[32:35], v[120:123], v[160:163], v[32:35]\n" \
"buffer_load_dwordx4 v232, s[36:39], s80 offen offset:1024 sc1 lds\n" \
"v_mfma_f32_16x16x32_f16 v[44:47], v[124:127], v[160:163], v[44:47]\n" \
"ds_read_b64_tr_b16 v[134:135], v242 offset:40960\n" \
"s_waitcnt lgkmcnt(7)\n" \
"v_mfma_f32_16x16x32_f16 v[48:51], v[136:139], v[168:171], v[48:51]\n" \
"ds_read_b128 v[160:163], v240 offset:40960\n" \
"ds_read_b64_tr_b16 v[152:153], v241 offset:33280\n" \
"v_mfma_f32_16x16x32_f16 v[60:63], v[140:143], v[168:171], v[60:63]\n" \
"s_waitcnt lgkmcnt(8)\n" \
"v_pk_add_f16 v168, v168, v172\n" \
"v_pk_add_f16 v169, v169, v173\n" \
"v_mfma_f32_16x16x32_f16 v[52:55], v[144:147], v[172:175], v[52:55]\n" \
"v_pk_add_f16 v170, v170, v174\n" \
"v_pk_add_f16 v171, v171, v175\n" \
"v_mfma_f32_16x16x32_f16 v[64:67], v[148:151], v[172:175], v[64:67]\n" \
"ds_read_b128 v[164:167], v240 offset:41472\n" \
"v_mfma_f32_16x16x32_f16 v[56:59], v[120:123], v[168:171], v[56:59]\n" \
"buffer_load_dwordx4 v232, s[36:39], s80 offen offset:2048 sc1 lds\n" \
"v_mfma_f32_16x16x32_f16 v[68:71], v[124:127], v[168:171], v[68:71]\n" \
"s_setprio 0\n" \
"ds_read_b64_tr_b16 v[154:155], v241 offset:41472\n" \
"ds_read_b64_tr_b16 v[156:157], v242 offset:33280\n" \
"s_waitcnt lgkmcnt(6)\n" \
"v_mfma_f32_16x16x32_f16 v[72:75], v[136:139], v[176:179], v[72:75]\n" \
"ds_read_b128 v[168:171], v240 offset:43008\n" \
"ds_read_b64_tr_b16 v[158:159], v242 offset:41472\n" \
"v_mfma_f32_16x16x32_f16 v[84:87], v[140:143], v[176:179], v[84:87]\n" \
"v_pk_add_f16 v176, v176, v180\n" \
"v_pk_add_f16 v177, v177, v181\n" \
"v_mfma_f32_16x16x32_f16 v[76:79], v[144:147], v[180:183], v[76:79]\n" \
"v_pk_add_f16 v178, v178, v182\n" \
"v_pk_add_f16 v179, v179, v183\n" \
"v_mfma_f32_16x16x32_f16 v[88:91], v[148:151], v[180:183], v[88:91]\n" \
"ds_read_b128 v[172:175], v240 offset:43520\n" \
"v_mfma_f32_16x16x32_f16 v[80:83], v[120:123], v[176:179], v[80:83]\n" \
"buffer_load_dwordx4 v232, s[36:39], s80 offen offset:3072 sc1 lds\n" \
"v_mfma_f32_16x16x32_f16 v[92:95], v[124:127], v[176:179], v[92:95]\n" \
"s_waitcnt lgkmcnt(6)\n" \
"v_mfma_f32_16x16x32_f16 v[96:99], v[136:139], v[160:163], v[96:99]\n" \
"v_pk_add_f16 v136, v128, v152\n" \
"v_pk_add_f16 v137, v129, v153\n" \
"ds_read_b128 v[176:179], v240 offset:45056\n" \
"v_mfma_f32_16x16x32_f16 v[108:111], v[140:143], v[160:163], v[108:111]\n" \
"s_waitcnt lgkmcnt(6)\n" \
"v_pk_add_f16 v160, v160, v164\n" \
"v_pk_add_f16 v161, v161, v165\n" \
"ds_read_b128 v[180:183], v240 offset:45568\n" \
"v_mfma_f32_16x16x32_f16 v[100:103], v[144:147], v[164:167], v[100:103]\n" \
"v_pk_add_f16 v162, v162, v166\n" \
"v_pk_add_f16 v163, v163, v167\n" \
"s_add_i32 m0, s45, 0x5000\n" \
"s_add_i32 s81, s47, 0x6000000\n" \
"v_mfma_f32_16x16x32_f16 v[112:115], v[148:151], v[164:167], v[112:115]\n" \
"s_waitcnt lgkmcnt(3)\n" \
"v_pk_add_f16 v138, v130, v154\n" \
"v_pk_add_f16 v139, v131, v155\n" \
"buffer_load_dwordx4 v232, s[36:39], s81 offen sc1 lds\n" \
"v_mfma_f32_16x16x32_f16 v[104:107], v[120:123], v[160:163], v[104:107]\n" \
"v_pk_add_f16 v140, v132, v156\n" \
"v_pk_add_f16 v141, v133, v157\n" \
"v_mfma_f32_16x16x32_f16 v[116:119], v[124:127], v[160:163], v[116:119]\n" \
"s_setprio 1\n" \
"v_pk_add_f16 v142, v134, v158\n" \
"v_pk_add_f16 v143, v135, v159\n" \
"s_waitcnt vmcnt(5) lgkmcnt(0)\n" \
"s_barrier\n" \
"v_mfma_f32_16x16x32_f16 v[0:3], v[128:131], v[168:171], v[0:3]\n" \
"ds_read_b128 v[160:163], v240 offset:6144\n" \
"v_mfma_f32_16x16x32_f16 v[12:15], v[132:135], v[168:171], v[12:15]\n" \
"v_pk_add_f16 v168, v168, v172\n" \
"v_pk_add_f16 v169, v169, v173\n" \
"v_mfma_f32_16x16x32_f16 v[4:7], v[152:155], v[172:175], v[4:7]\n" \
"v_pk_add_f16 v170, v170, v174\n" \
"v_pk_add_f16 v171, v171, v175\n" \
"v_mfma_f32_16x16x32_f16 v[16:19], v[156:159], v[172:175], v[16:19]\n" \
"ds_read_b128 v[164:167], v240 offset:6656\n" \
"s_add_i32 m0, s44, 0xa000\n" \
"s_add_i32 s80, s46, 0x6400000\n" \
"v_mfma_f32_16x16x32_f16 v[8:11], v[136:139], v[168:171], v[8:11]\n" \
"buffer_load_dwordx4 v232, s[36:39], s80 offen sc1 lds\n" \
"v_mfma_f32_16x16x32_f16 v[20:23], v[140:143], v[168:171], v[20:23]\n" \
"v_lshlrev_b32 v254, 3, v244\n" \
"ds_read_b64_tr_b16 v[120:121], v241 offset:0\n" \
"ds_read_b64_tr_b16 v[122:123], v241 offset:8192\n" \
"v_mfma_f32_16x16x32_f16 v[24:27], v[128:131], v[176:179], v[24:27]\n" \
"ds_read_b128 v[168:171], v240 offset:8192\n" \
"v_mfma_f32_16x16x32_f16 v[36:39], v[132:135], v[176:179], v[36:39]\n" \
"v_pk_add_f16 v176, v176, v180\n" \
"v_pk_add_f16 v177, v177, v181\n" \
"v_mfma_f32_16x16x32_f16 v[28:31], v[152:155], v[180:183], v[28:31]\n" \
"v_pk_add_f16 v178, v178, v182\n" \
"v_pk_add_f16 v179, v179, v183\n" \
"v_mfma_f32_16x16x32_f16 v[40:43], v[156:159], v[180:183], v[40:43]\n" \
"ds_read_b128 v[172:175], v240 offset:8704\n" \
"ds_read_b64_tr_b16 v[124:125], v242 offset:0\n" \
"v_mfma_f32_16x16x32_f16 v[32:35], v[136:139], v[176:179], v[32:35]\n" \
"buffer_load_dwordx4 v232, s[36:39], s80 offen offset:1024 sc1 lds\n" \
"v_mfma_f32_16x16x32_f16 v[44:47], v[140:143], v[176:179], v[44:47]\n" \
"ds_read_b64_tr_b16 v[126:127], v242 offset:8192\n" \
"s_mov_b64 exec, s[58:59]\n" \
"global_load_dword v255, v254, s[78:79]\n" \
"s_mov_b64 exec, -1\n" \
"s_waitcnt lgkmcnt(6)\n" \
"v_mfma_f32_16x16x32_f16 v[48:51], v[128:131], v[160:163], v[48:51]\n" \
"ds_read_b128 v[176:179], v240 offset:0\n" \
"ds_read_b64_tr_b16 v[144:145], v241 offset:512\n" \
"v_mfma_f32_16x16x32_f16 v[60:63], v[132:135], v[160:163], v[60:63]\n" \
"v_pk_add_f16 v160, v160, v164\n" \
"v_pk_add_f16 v161, v161, v165\n" \
"v_mfma_f32_16x16x32_f16 v[52:55], v[152:155], v[164:167], v[52:55]\n" \
"v_pk_add_f16 v162, v162, v166\n" \
"v_pk_add_f16 v163, v163, v167\n" \
"v_mfma_f32_16x16x32_f16 v[64:67], v[156:159], v[164:167], v[64:67]\n" \
"ds_read_b128 v[180:183], v240 offset:512\n" \
"v_mfma_f32_16x16x32_f16 v[56:59], v[136:139], v[160:163], v[56:59]\n" \
"buffer_load_dwordx4 v232, s[36:39], s80 offen offset:2048 sc1 lds\n" \
"v_mfma_f32_16x16x32_f16 v[68:71], v[140:143], v[160:163], v[68:71]\n" \
"s_setprio 0\n" \
"ds_read_b64_tr_b16 v[146:147], v241 offset:8704\n" \
"ds_read_b64_tr_b16 v[148:149], v242 offset:512\n" \
"s_waitcnt lgkmcnt(6)\n" \
"v_mfma_f32_16x16x32_f16 v[72:75], v[128:131], v[168:171], v[72:75]\n" \
"ds_read_b128 v[160:163], v240 offset:2048\n" \
"ds_read_b64_tr_b16 v[150:151], v242 offset:8704\n" \
"v_mfma_f32_16x16x32_f16 v[84:87], v[132:135], v[168:171], v[84:87]\n" \
"v_pk_add_f16 v168, v168, v172\n" \
"v_pk_add_f16 v169, v169, v173\n" \
"v_mfma_f32_16x16x32_f16 v[76:79], v[152:155], v[172:175], v[76:79]\n" \
"v_pk_add_f16 v170, v170, v174\n" \
"v_pk_add_f16 v171, v171, v175\n" \
"v_mfma_f32_16x16x32_f16 v[88:91], v[156:159], v[172:175], v[88:91]\n" \
"ds_read_b128 v[164:167], v240 offset:2560\n" \
"v_mfma_f32_16x16x32_f16 v[80:83], v[136:139], v[168:171], v[80:83]\n" \
"buffer_load_dwordx4 v232, s[36:39], s80 offen offset:3072 sc1 lds\n" \
"v_mfma_f32_16x16x32_f16 v[92:95], v[140:143], v[168:171], v[92:95]\n" \
"s_waitcnt lgkmcnt(6)\n" \
"v_mfma_f32_16x16x32_f16 v[96:99], v[128:131], v[176:179], v[96:99]\n" \
"v_pk_add_f16 v128, v120, v144\n" \
"v_pk_add_f16 v129, v121, v145\n" \
"ds_read_b128 v[168:171], v240 offset:4096\n" \
"v_mfma_f32_16x16x32_f16 v[108:111], v[132:135], v[176:179], v[108:111]\n" \
"s_waitcnt lgkmcnt(6)\n" \
"v_pk_add_f16 v176, v176, v180\n" \
"v_pk_add_f16 v177, v177, v181\n" \
"ds_read_b128 v[172:175], v240 offset:4608\n" \
"v_mfma_f32_16x16x32_f16 v[100:103], v[152:155], v[180:183], v[100:103]\n" \
"v_pk_add_f16 v178, v178, v182\n" \
"v_pk_add_f16 v179, v179, v183\n" \
"s_add_i32 m0, s45, 0xa000\n" \
"s_add_i32 s81, s47, 0x6400000\n" \
"v_mfma_f32_16x16x32_f16 v[112:115], v[156:159], v[180:183], v[112:115]\n" \
"s_waitcnt lgkmcnt(3)\n" \
"v_pk_add_f16 v130, v122, v146\n" \
"v_pk_add_f16 v131, v123, v147\n" \
"buffer_load_dwordx4 v232, s[36:39], s81 offen sc1 lds\n" \
"v_mfma_f32_16x16x32_f16 v[104:107], v[136:139], v[176:179], v[104:107]\n" \
"v_pk_add_f16 v132, v124, v148\n" \
"v_pk_add_f16 v133, v125, v149\n" \
"v_mfma_f32_16x16x32_f16 v[116:119], v[140:143], v[176:179], v[116:119]\n" \
"s_setprio 1\n" \
"v_pk_add_f16 v134, v126, v150\n" \
"v_pk_add_f16 v135, v127, v151\n" \
"s_waitcnt vmcnt(6) lgkmcnt(0)\n" \
"s_barrier\n" \
"v_mfma_f32_16x16x32_f16 v[0:3], v[120:123], v[160:163], v[0:3]\n" \
"ds_read_b128 v[176:179], v240 offset:26624\n" \
"v_mfma_f32_16x16x32_f16 v[12:15], v[124:127], v[160:163], v[12:15]\n" \
"v_pk_add_f16 v160, v160, v164\n" \
"v_pk_add_f16 v161, v161, v165\n" \
"v_mfma_f32_16x16x32_f16 v[4:7], v[144:147], v[164:167], v[4:7]\n" \
"v_pk_add_f16 v162, v162, v166\n" \
"v_pk_add_f16 v163, v163, v167\n" \
"v_mfma_f32_16x16x32_f16 v[16:19], v[148:151], v[164:167], v[16:19]\n" \
"ds_read_b128 v[180:183], v240 offset:27136\n" \
"s_add_i32 m0, s44, 0x0\n" \
"s_add_i32 s80, s46, 0x6800000\n" \
"v_mfma_f32_16x16x32_f16 v[8:11], v[128:131], v[160:163], v[8:11]\n" \
"buffer_load_dwordx4 v232, s[36:39], s80 offen sc1 lds\n" \
"v_mfma_f32_16x16x32_f16 v[20:23], v[132:135], v[160:163], v[20:23]\n" \
"ds_read_b64_tr_b16 v[136:137], v241 offset:16384\n" \
"ds_read_b64_tr_b16 v[138:139], v241 offset:24576\n" \
"v_mfma_f32_16x16x32_f16 v[24:27], v[120:123], v[168:171], v[24:27]\n" \
"ds_read_b128 v[160:163], v240 offset:28672\n" \
"v_mfma_f32_16x16x32_f16 v[36:39], v[124:127], v[168:171], v[36:39]\n" \
"v_pk_add_f16 v168, v168, v172\n" \
"v_pk_add_f16 v169, v169, v173\n" \
"v_mfma_f32_16x16x32_f16 v[28:31], v[144:147], v[172:175], v[28:31]\n" \
"v_pk_add_f16 v170, v170, v174\n" \
"v_pk_add_f16 v171, v171, v175\n" \
"v_mfma_f32_16x16x32_f16 v[40:43], v[148:151], v[172:175], v[40:43]\n" \
"ds_read_b128 v[164:167], v240 offset:29184\n" \
"ds_read_b64_tr_b16 v[140:141], v242 offset:16384\n" \
"v_mfma_f32_16x16x32_f16 v[32:35], v[128:131], v[168:171], v[32:35]\n" \
"buffer_load_dwordx4 v232, s[36:39], s80 offen offset:1024 sc1 lds\n" \
"v_mfma_f32_16x16x32_f16 v[44:47], v[132:135], v[168:171], v[44:47]\n" \
"ds_read_b64_tr_b16 v[142:143], v242 offset:24576\n" \
"s_waitcnt lgkmcnt(7)\n" \
"v_mfma_f32_16x16x32_f16 v[48:51], v[120:123], v[176:179], v[48:51]\n" \
"ds_read_b128 v[168:171], v240 offset:20480\n" \
"ds_read_b64_tr_b16 v[152:153], v241 offset:16896\n" \
"v_mfma_f32_16x16x32_f16 v[60:63], v[124:127], v[176:179], v[60:63]\n" \
"s_waitcnt lgkmcnt(8)\n" \
"v_pk_add_f16 v176, v176, v180\n" \
"v_pk_add_f16 v177, v177, v181\n" \
"v_mfma_f32_16x16x32_f16 v[52:55], v[144:147], v[180:183], v[52:55]\n" \
"v_pk_add_f16 v178, v178, v182\n" \
"v_pk_add_f16 v179, v179, v183\n" \
"v_mfma_f32_16x16x32_f16 v[64:67], v[148:151], v[180:183], v[64:67]\n" \
"ds_read_b128 v[172:175], v240 offset:20992\n" \
"v_mfma_f32_16x16x32_f16 v[56:59], v[128:131], v[176:179], v[56:59]\n" \
"buffer_load_dwordx4 v232, s[36:39], s80 offen offset:2048 sc1 lds\n" \
"v_mfma_f32_16x16x32_f16 v[68:71], v[132:135], v[176:179], v[68:71]\n" \
"s_setprio 0\n" \
"ds_read_b64_tr_b16 v[154:155], v241 offset:25088\n" \
"ds_read_b64_tr_b16 v[156:157], v242 offset:16896\n" \
"s_waitcnt lgkmcnt(6)\n" \
"v_mfma_f32_16x16x32_f16 v[72:75], v[120:123], v[160:163], v[72:75]\n" \
"ds_read_b128 v[176:179], v240 offset:22528\n" \
"ds_read_b64_tr_b16 v[158:159], v242 offset:25088\n" \
"v_mfma_f32_16x16x32_f16 v[84:87], v[124:127], v[160:163], v[84:87]\n" \
"v_pk_add_f16 v160, v160, v164\n" \
"v_pk_add_f16 v161, v161, v165\n" \
"v_mfma_f32_16x16x32_f16 v[76:79], v[144:147], v[164:167], v[76:79]\n" \
"v_pk_add_f16 v162, v162, v166\n" \
"v_pk_add_f16 v163, v163, v167\n" \
"v_mfma_f32_16x16x32_f16 v[88:91], v[148:151], v[164:167], v[88:91]\n" \
"ds_read_b128 v[180:183], v240 offset:23040\n" \
"v_mfma_f32_16x16x32_f16 v[80:83], v[128:131], v[160:163], v[80:83]\n" \
"buffer_load_dwordx4 v232, s[36:39], s80 offen offset:3072 sc1 lds\n" \
"v_mfma_f32_16x16x32_f16 v[92:95], v[132:135], v[160:163], v[92:95]\n" \
"s_waitcnt lgkmcnt(6)\n" \
"v_mfma_f32_16x16x32_f16 v[96:99], v[120:123], v[168:171], v[96:99]\n" \
"v_pk_add_f16 v120, v136, v152\n" \
"v_pk_add_f16 v121, v137, v153\n" \
"ds_read_b128 v[160:163], v240 offset:24576\n" \
"v_mfma_f32_16x16x32_f16 v[108:111], v[124:127], v[168:171], v[108:111]\n" \
"s_waitcnt lgkmcnt(6)\n" \
"v_pk_add_f16 v168, v168, v172\n" \
"v_pk_add_f16 v169, v169, v173\n" \
"ds_read_b128 v[164:167], v240 offset:25088\n" \
"v_mfma_f32_16x16x32_f16 v[100:103], v[144:147], v[172:175], v[100:103]\n" \
"v_pk_add_f16 v170, v170, v174\n" \
"v_pk_add_f16 v171, v171, v175\n" \
"s_add_i32 m0, s45, 0x0\n" \
"s_add_i32 s81, s47, 0x6800000\n" \
"v_mfma_f32_16x16x32_f16 v[112:115], v[148:151], v[172:175], v[112:115]\n" \
"s_waitcnt lgkmcnt(3)\n" \
"v_pk_add_f16 v122, v138, v154\n" \
"v_pk_add_f16 v123, v139, v155\n" \
"buffer_load_dwordx4 v232, s[36:39], s81 offen sc1 lds\n" \
"v_mfma_f32_16x16x32_f16 v[104:107], v[128:131], v[168:171], v[104:107]\n" \
"v_pk_add_f16 v124, v140, v156\n" \
"v_pk_add_f16 v125, v141, v157\n" \
"v_mfma_f32_16x16x32_f16 v[116:119], v[132:135], v[168:171], v[116:119]\n" \
"s_setprio 1\n" \
"v_pk_add_f16 v126, v142, v158\n" \
"v_pk_add_f16 v127, v143, v159\n" \
"s_waitcnt vmcnt(5) lgkmcnt(0)\n" \
"s_barrier\n" \
"v_mfma_f32_16x16x32_f16 v[0:3], v[136:139], v[176:179], v[0:3]\n" \
"ds_read_b128 v[168:171], v240 offset:47104\n" \
"v_mfma_f32_16x16x32_f16 v[12:15], v[140:143], v[176:179], v[12:15]\n" \
"v_pk_add_f16 v176, v176, v180\n" \
"v_pk_add_f16 v177, v177, v181\n" \
"v_mfma_f32_16x16x32_f16 v[4:7], v[152:155], v[180:183], v[4:7]\n" \
"v_pk_add_f16 v178, v178, v182\n" \
"v_pk_add_f16 v179, v179, v183\n" \
"s_add_i32 m0, s44, 0x5000\n" \
"s_add_i32 s80, s46, 0x6c00000\n" \
"v_mfma_f32_16x16x32_f16 v[16:19], v[156:159], v[180:183], v[16:19]\n" \
"ds_read_b128 v[172:175], v240 offset:47616\n" \
"buffer_load_dwordx4 v232, s[36:39], s80 offen sc1 lds\n" \
"v_mfma_f32_16x16x32_f16 v[8:11], v[120:123], v[176:179], v[8:11]\n" \
"ds_read_b64_tr_b16 v[128:129], v241 offset:32768\n" \
"v_mfma_f32_16x16x32_f16 v[20:23], v[124:127], v[176:179], v[20:23]\n" \
"ds_read_b64_tr_b16 v[130:131], v241 offset:40960\n" \
"v_mfma_f32_16x16x32_f16 v[24:27], v[136:139], v[160:163], v[24:27]\n" \
"ds_read_b128 v[176:179], v240 offset:49152\n" \
"buffer_load_dwordx4 v232, s[36:39], s80 offen offset:1024 sc1 lds\n" \
"v_mfma_f32_16x16x32_f16 v[36:39], v[140:143], v[160:163], v[36:39]\n" \
"v_pk_add_f16 v160, v160, v164\n" \
"v_pk_add_f16 v161, v161, v165\n" \
"v_mfma_f32_16x16x32_f16 v[28:31], v[152:155], v[164:167], v[28:31]\n" \
"v_pk_add_f16 v162, v162, v166\n" \
"v_pk_add_f16 v163, v163, v167\n" \
"v_mfma_f32_16x16x32_f16 v[40:43], v[156:159], v[164:167], v[40:43]\n" \
"ds_read_b128 v[180:183], v240 offset:49664\n" \
"buffer_load_dwordx4 v232, s[36:39], s80 offen offset:2048 sc1 lds\n" \
"v_mfma_f32_16x16x32_f16 v[32:35], v[120:123], v[160:163], v[32:35]\n" \
"ds_read_b64_tr_b16 v[132:133], v242 offset:32768\n" \
"v_mfma_f32_16x16x32_f16 v[44:47], v[124:127], v[160:163], v[44:47]\n" \
"ds_read_b64_tr_b16 v[134:135], v242 offset:40960\n" \
"s_waitcnt lgkmcnt(7)\n" \
"v_mfma_f32_16x16x32_f16 v[48:51], v[136:139], v[168:171], v[48:51]\n" \
"ds_read_b128 v[160:163], v240 offset:40960\n" \
"buffer_load_dwordx4 v232, s[36:39], s80 offen offset:3072 sc1 lds\n" \
"v_mfma_f32_16x16x32_f16 v[60:63], v[140:143], v[168:171], v[60:63]\n" \
"s_waitcnt lgkmcnt(6)\n" \
"v_pk_add_f16 v168, v168, v172\n" \
"v_pk_add_f16 v169, v169, v173\n" \
"ds_read_b64_tr_b16 v[144:145], v241 offset:33280\n" \
"v_mfma_f32_16x16x32_f16 v[52:55], v[152:155], v[172:175], v[52:55]\n" \
"v_pk_add_f16 v170, v170, v174\n" \
"v_pk_add_f16 v171, v171, v175\n" \
"s_add_i32 m0, s45, 0x5000\n" \
"s_add_i32 s81, s47, 0x6c00000\n" \
"v_mfma_f32_16x16x32_f16 v[64:67], v[156:159], v[172:175], v[64:67]\n" \
"ds_read_b128 v[164:167], v240 offset:41472\n" \
"buffer_load_dwordx4 v232, s[36:39], s81 offen sc1 lds\n" \
"v_mfma_f32_16x16x32_f16 v[56:59], v[120:123], v[168:171], v[56:59]\n" \
"ds_read_b64_tr_b16 v[146:147], v241 offset:41472\n" \
"ds_read_b64_tr_b16 v[148:149], v242 offset:33280\n" \
"v_mfma_f32_16x16x32_f16 v[68:71], v[124:127], v[168:171], v[68:71]\n" \
"s_setprio 0\n" \
"s_add_i32 m0, s52, 0x0\n" \
"s_add_i32 s80, s55, 0x0\n" \
"s_waitcnt lgkmcnt(5)\n" \
"v_mfma_f32_16x16x32_f16 v[72:75], v[136:139], v[176:179], v[72:75]\n" \
"ds_read_b128 v[168:171], v240 offset:43008\n" \
"buffer_load_dwordx4 v232, s[36:39], s80 offen sc1 lds\n" \
"v_mfma_f32_16x16x32_f16 v[84:87], v[140:143], v[176:179], v[84:87]\n" \
"v_pk_add_f16 v176, v176, v180\n" \
"v_pk_add_f16 v177, v177, v181\n" \
"ds_read_b64_tr_b16 v[150:151], v242 offset:41472\n" \
"v_mfma_f32_16x16x32_f16 v[76:79], v[152:155], v[180:183], v[76:79]\n" \
"v_pk_add_f16 v178, v178, v182\n" \
"v_pk_add_f16 v179, v179, v183\n" \
"v_mfma_f32_16x16x32_f16 v[88:91], v[156:159], v[180:183], v[88:91]\n" \
"ds_read_b128 v[172:175], v240 offset:43520\n" \
"buffer_load_dwordx4 v232, s[36:39], s80 offen offset:1024 sc1 lds\n" \
"v_mfma_f32_16x16x32_f16 v[80:83], v[120:123], v[176:179], v[80:83]\n" \
"v_mfma_f32_16x16x32_f16 v[92:95], v[124:127], v[176:179], v[92:95]\n" \
"s_waitcnt lgkmcnt(5)\n" \
"v_mfma_f32_16x16x32_f16 v[96:99], v[136:139], v[160:163], v[96:99]\n" \
"v_pk_add_f16 v136, v128, v144\n" \
"v_pk_add_f16 v137, v129, v145\n" \
"ds_read_b128 v[176:179], v240 offset:45056\n" \
"buffer_load_dwordx4 v232, s[36:39], s80 offen offset:2048 sc1 lds\n" \
"v_mfma_f32_16x16x32_f16 v[108:111], v[140:143], v[160:163], v[108:111]\n" \
"v_pk_add_f16 v160, v160, v164\n" \
"v_pk_add_f16 v161, v161, v165\n" \
"ds_read_b128 v[180:183], v240 offset:45568\n" \
"v_mfma_f32_16x16x32_f16 v[100:103], v[152:155], v[164:167], v[100:103]\n" \
"v_pk_add_f16 v162, v162, v166\n" \
"v_pk_add_f16 v163, v163, v167\n" \
"v_mfma_f32_16x16x32_f16 v[112:115], v[156:159], v[164:167], v[112:115]\n" \
"s_waitcnt lgkmcnt(4)\n" \
"v_pk_add_f16 v138, v130, v146\n" \
"v_pk_add_f16 v139, v131, v147\n" \
"buffer_load_dwordx4 v232, s[36:39], s80 offen offset:3072 sc1 lds\n" \
"v_mfma_f32_16x16x32_f16 v[104:107], v[120:123], v[160:163], v[104:107]\n" \
"v_pk_add_f16 v140, v132, v148\n" \
"v_pk_add_f16 v141, v133, v149\n" \
"v_mfma_f32_16x16x32_f16 v[116:119], v[124:127], v[160:163], v[116:119]\n" \
"s_setprio 1\n" \
"s_waitcnt lgkmcnt(2)\n" \
"v_pk_add_f16 v142, v134, v150\n" \
"v_pk_add_f16 v143, v135, v151\n" \
"s_waitcnt vmcnt(9) lgkmcnt(0)\n" \
"s_barrier\n" \
"v_mfma_f32_16x16x32_f16 v[0:3], v[128:131], v[168:171], v[0:3]\n" \
"ds_read_b128 v[160:163], v240 offset:6144\n" \
"v_mfma_f32_16x16x32_f16 v[12:15], v[132:135], v[168:171], v[12:15]\n" \
"v_pk_add_f16 v168, v168, v172\n" \
"v_pk_add_f16 v169, v169, v173\n" \
"v_mfma_f32_16x16x32_f16 v[4:7], v[144:147], v[172:175], v[4:7]\n" \
"v_pk_add_f16 v170, v170, v174\n" \
"v_pk_add_f16 v171, v171, v175\n" \
"v_mfma_f32_16x16x32_f16 v[16:19], v[148:151], v[172:175], v[16:19]\n" \
"ds_read_b128 v[164:167], v240 offset:6656\n" \
"s_add_i32 m0, s44, 0xa000\n" \
"s_add_i32 s80, s46, 0x7000000\n" \
"v_mfma_f32_16x16x32_f16 v[8:11], v[136:139], v[168:171], v[8:11]\n" \
"buffer_load_dwordx4 v232, s[36:39], s80 offen sc1 lds\n" \
"v_mfma_f32_16x16x32_f16 v[20:23], v[140:143], v[168:171], v[20:23]\n" \
"ds_read_b64_tr_b16 v[120:121], v241 offset:0\n" \
"ds_read_b64_tr_b16 v[122:123], v241 offset:8192\n" \
"v_mfma_f32_16x16x32_f16 v[24:27], v[128:131], v[176:179], v[24:27]\n" \
"ds_read_b128 v[168:171], v240 offset:8192\n" \
"v_mfma_f32_16x16x32_f16 v[36:39], v[132:135], v[176:179], v[36:39]\n" \
"v_pk_add_f16 v176, v176, v180\n" \
"v_pk_add_f16 v177, v177, v181\n" \
"v_mfma_f32_16x16x32_f16 v[28:31], v[144:147], v[180:183], v[28:31]\n" \
"v_pk_add_f16 v178, v178, v182\n" \
"v_pk_add_f16 v179, v179, v183\n" \
"v_mfma_f32_16x16x32_f16 v[40:43], v[148:151], v[180:183], v[40:43]\n" \
"ds_read_b128 v[172:175], v240 offset:8704\n" \
"ds_read_b64_tr_b16 v[124:125], v242 offset:0\n" \
"v_mfma_f32_16x16x32_f16 v[32:35], v[136:139], v[176:179], v[32:35]\n" \
"buffer_load_dwordx4 v232, s[36:39], s80 offen offset:1024 sc1 lds\n" \
"v_mfma_f32_16x16x32_f16 v[44:47], v[140:143], v[176:179], v[44:47]\n" \
"ds_read_b64_tr_b16 v[126:127], v242 offset:8192\n" \
"s_waitcnt lgkmcnt(7)\n" \
"v_mfma_f32_16x16x32_f16 v[48:51], v[128:131], v[160:163], v[48:51]\n" \
"ds_read_b128 v[176:179], v240 offset:0\n" \
"ds_read_b64_tr_b16 v[152:153], v241 offset:512\n" \
"v_mfma_f32_16x16x32_f16 v[60:63], v[132:135], v[160:163], v[60:63]\n" \
"s_waitcnt lgkmcnt(8)\n" \
"v_pk_add_f16 v160, v160, v164\n" \
"v_pk_add_f16 v161, v161, v165\n" \
"v_mfma_f32_16x16x32_f16 v[52:55], v[144:147], v[164:167], v[52:55]\n" \
"v_pk_add_f16 v162, v162, v166\n" \
"v_pk_add_f16 v163, v163, v167\n" \
"v_mfma_f32_16x16x32_f16 v[64:67], v[148:151], v[164:167], v[64:67]\n" \
"ds_read_b128 v[180:183], v240 offset:512\n" \
"v_mfma_f32_16x16x32_f16 v[56:59], v[136:139], v[160:163], v[56:59]\n" \
"buffer_load_dwordx4 v232, s[36:39], s80 offen offset:2048 sc1 lds\n" \
"v_mfma_f32_16x16x32_f16 v[68:71], v[140:143], v[160:163], v[68:71]\n" \
"s_setprio 0\n" \
"ds_read_b64_tr_b16 v[154:155], v241 offset:8704\n" \
"ds_read_b64_tr_b16 v[156:157], v242 offset:512\n" \
"s_waitcnt lgkmcnt(6)\n" \
"v_mfma_f32_16x16x32_f16 v[72:75], v[128:131], v[168:171], v[72:75]\n" \
"ds_read_b128 v[160:163], v240 offset:2048\n" \
"ds_read_b64_tr_b16 v[158:159], v242 offset:8704\n" \
"v_mfma_f32_16x16x32_f16 v[84:87], v[132:135], v[168:171], v[84:87]\n" \
"v_pk_add_f16 v168, v168, v172\n" \
"v_pk_add_f16 v169, v169, v173\n" \
"v_mfma_f32_16x16x32_f16 v[76:79], v[144:147], v[172:175], v[76:79]\n" \
"v_pk_add_f16 v170, v170, v174\n" \
"v_pk_add_f16 v171, v171, v175\n" \
"v_mfma_f32_16x16x32_f16 v[88:91], v[148:151], v[172:175], v[88:91]\n" \
"ds_read_b128 v[164:167], v240 offset:2560\n" \
"v_mfma_f32_16x16x32_f16 v[80:83], v[136:139], v[168:171], v[80:83]\n" \
"buffer_load_dwordx4 v232, s[36:39], s80 offen offset:3072 sc1 lds\n" \
"v_mfma_f32_16x16x32_f16 v[92:95], v[140:143], v[168:171], v[92:95]\n" \
"s_waitcnt lgkmcnt(6)\n" \
"v_mfma_f32_16x16x32_f16 v[96:99], v[128:131], v[176:179], v[96:99]\n" \
"v_pk_add_f16 v128, v120, v152\n" \
"v_pk_add_f16 v129, v121, v153\n" \
"ds_read_b128 v[168:171], v240 offset:4096\n" \
"v_mfma_f32_16x16x32_f16 v[108:111], v[132:135], v[176:179], v[108:111]\n" \
"s_waitcnt lgkmcnt(6)\n" \
"v_pk_add_f16 v176, v176, v180\n" \
"v_pk_add_f16 v177, v177, v181\n" \
"ds_read_b128 v[172:175], v240 offset:4608\n" \
"v_mfma_f32_16x16x32_f16 v[100:103], v[144:147], v[180:183], v[100:103]\n" \
"v_pk_add_f16 v178, v178, v182\n" \
"v_pk_add_f16 v179, v179, v183\n" \
"s_add_i32 m0, s45, 0xa000\n" \
"s_add_i32 s81, s47, 0x7000000\n" \
"v_mfma_f32_16x16x32_f16 v[112:115], v[148:151], v[180:183], v[112:115]\n" \
"s_waitcnt lgkmcnt(3)\n" \
"v_pk_add_f16 v130, v122, v154\n" \
"v_pk_add_f16 v131, v123, v155\n" \
"buffer_load_dwordx4 v232, s[36:39], s81 offen sc1 lds\n" \
"v_mfma_f32_16x16x32_f16 v[104:107], v[136:139], v[176:179], v[104:107]\n" \
"v_pk_add_f16 v132, v124, v156\n" \
"v_pk_add_f16 v133, v125, v157\n" \
"v_mfma_f32_16x16x32_f16 v[116:119], v[140:143], v[176:179], v[116:119]\n" \
"s_setprio 1\n" \
"v_pk_add_f16 v134, v126, v158\n" \
"v_pk_add_f16 v135, v127, v159\n" \
"s_waitcnt vmcnt(9) lgkmcnt(0)\n" \
"s_barrier\n" \
"v_mfma_f32_16x16x32_f16 v[0:3], v[120:123], v[160:163], v[0:3]\n" \
"ds_read_b128 v[176:179], v240 offset:26624\n" \
"v_mfma_f32_16x16x32_f16 v[12:15], v[124:127], v[160:163], v[12:15]\n" \
"v_pk_add_f16 v160, v160, v164\n" \
"v_pk_add_f16 v161, v161, v165\n" \
"v_mfma_f32_16x16x32_f16 v[4:7], v[152:155], v[164:167], v[4:7]\n" \
"v_pk_add_f16 v162, v162, v166\n" \
"v_pk_add_f16 v163, v163, v167\n" \
"v_mfma_f32_16x16x32_f16 v[16:19], v[156:159], v[164:167], v[16:19]\n" \
"ds_read_b128 v[180:183], v240 offset:27136\n" \
"s_add_i32 m0, s44, 0x0\n" \
"s_add_i32 s80, s46, 0x7400000\n" \
"s_bitcmp1_b32 s46, 22\n" \
"s_cselect_b32 s80, 0x80000000, s80\n" \
"v_mfma_f32_16x16x32_f16 v[8:11], v[128:131], v[160:163], v[8:11]\n" \
"buffer_load_dwordx4 v232, s[36:39], s80 offen sc1 lds\n" \
"v_mfma_f32_16x16x32_f16 v[20:23], v[132:135], v[160:163], v[20:23]\n" \
"ds_read_b64_tr_b16 v[136:137], v241 offset:16384\n" \
"ds_read_b64_tr_b16 v[138:139], v241 offset:24576\n" \
"v_mfma_f32_16x16x32_f16 v[24:27], v[120:123], v[168:171], v[24:27]\n" \
"ds_read_b128 v[160:163], v240 offset:28672\n" \
"v_mfma_f32_16x16x32_f16 v[36:39], v[124:127], v[168:171], v[36:39]\n" \
"v_pk_add_f16 v168, v168, v172\n" \
"v_pk_add_f16 v169, v169, v173\n" \
"v_mfma_f32_16x16x32_f16 v[28:31], v[152:155], v[172:175], v[28:31]\n" \
"v_pk_add_f16 v170, v170, v174\n" \
"v_pk_add_f16 v171, v171, v175\n" \
"v_mfma_f32_16x16x32_f16 v[40:43], v[156:159], v[172:175], v[40:43]\n" \
"ds_read_b128 v[164:167], v240 offset:29184\n" \
"ds_read_b64_tr_b16 v[140:141], v242 offset:16384\n" \
"v_mfma_f32_16x16x32_f16 v[32:35], v[128:131], v[168:171], v[32:35]\n" \
"buffer_load_dwordx4 v232, s[36:39], s80 offen offset:1024 sc1 lds\n" \
"v_mfma_f32_16x16x32_f16 v[44:47], v[132:135], v[168:171], v[44:47]\n" \
"ds_read_b64_tr_b16 v[142:143], v242 offset:24576\n" \
"s_waitcnt lgkmcnt(6)\n" \
"v_mfma_f32_16x16x32_f16 v[48:51], v[120:123], v[176:179], v[48:51]\n" \
"ds_read_b128 v[168:171], v240 offset:20480\n" \
"ds_read_b64_tr_b16 v[144:145], v241 offset:16896\n" \
"v_mfma_f32_16x16x32_f16 v[60:63], v[124:127], v[176:179], v[60:63]\n" \
"v_pk_add_f16 v176, v176, v180\n" \
"v_pk_add_f16 v177, v177, v181\n" \
"v_mfma_f32_16x16x32_f16 v[52:55], v[152:155], v[180:183], v[52:55]\n" \
"v_pk_add_f16 v178, v178, v182\n" \
"v_pk_add_f16 v179, v179, v183\n" \
"v_mfma_f32_16x16x32_f16 v[64:67], v[156:159], v[180:183], v[64:67]\n" \
"ds_read_b128 v[172:175], v240 offset:20992\n" \
"v_mfma_f32_16x16x32_f16 v[56:59], v[128:131], v[176:179], v[56:59]\n" \
"buffer_load_dwordx4 v232, s[36:39], s80 offen offset:2048 sc1 lds\n" \
"v_mfma_f32_16x16x32_f16 v[68:71], v[132:135], v[176:179], v[68:71]\n" \
"s_setprio 0\n" \
"ds_read_b64_tr_b16 v[146:147], v241 offset:25088\n" \
"ds_read_b64_tr_b16 v[148:149], v242 offset:16896\n" \
"s_waitcnt lgkmcnt(7)\n" \
"v_mfma_f32_16x16x32_f16 v[72:75], v[120:123], v[160:163], v[72:75]\n" \
"ds_read_b128 v[176:179], v240 offset:22528\n" \
"ds_read_b64_tr_b16 v[150:151], v242 offset:25088\n" \
"v_mfma_f32_16x16x32_f16 v[84:87], v[124:127], v[160:163], v[84:87]\n" \
"v_pk_add_f16 v160, v160, v164\n" \
"v_pk_add_f16 v161, v161, v165\n" \
"v_mfma_f32_16x16x32_f16 v[76:79], v[152:155], v[164:167], v[76:79]\n" \
"v_pk_add_f16 v162, v162, v166\n" \
"v_pk_add_f16 v163, v163, v167\n" \
"v_add_u32 v243, 0x1b000, v241\n" \
"v_mfma_f32_16x16x32_f16 v[88:91], v[156:159], v[164:167], v[88:91]\n" \
"v_xor_b32 v244, 32, v243\n" \
"ds_read_b128 v[180:183], v240 offset:23040\n" \
"v_mfma_f32_16x16x32_f16 v[80:83], v[128:131], v[160:163], v[80:83]\n" \
"buffer_load_dwordx4 v232, s[36:39], s80 offen offset:3072 sc1 lds\n" \
"v_mfma_f32_16x16x32_f16 v[92:95], v[132:135], v[160:163], v[92:95]\n" \
"v_add_u32 v246, 0x15000, v240\n" \
"s_waitcnt lgkmcnt(5)\n" \
"v_mfma_f32_16x16x32_f16 v[96:99], v[120:123], v[168:171], v[96:99]\n" \
"v_pk_add_f16 v120, v136, v144\n" \
"v_pk_add_f16 v121, v137, v145\n" \
"ds_read_b128 v[160:163], v240 offset:24576\n" \
"v_mfma_f32_16x16x32_f16 v[108:111], v[124:127], v[168:171], v[108:111]\n" \
"v_pk_add_f16 v168, v168, v172\n" \
"v_pk_add_f16 v169, v169, v173\n" \
"ds_read_b128 v[164:167], v240 offset:25088\n" \
"v_mfma_f32_16x16x32_f16 v[100:103], v[152:155], v[172:175], v[100:103]\n" \
"v_pk_add_f16 v170, v170, v174\n" \
"v_pk_add_f16 v171, v171, v175\n" \
"s_add_i32 m0, s45, 0x0\n" \
"s_add_i32 s81, s47, 0x7400000\n" \
"v_mfma_f32_16x16x32_f16 v[112:115], v[156:159], v[172:175], v[112:115]\n" \
"s_waitcnt lgkmcnt(3)\n" \
"v_pk_add_f16 v122, v138, v146\n" \
"v_pk_add_f16 v123, v139, v147\n" \
"buffer_load_dwordx4 v232, s[36:39], s81 offen sc1 lds\n" \
"v_mfma_f32_16x16x32_f16 v[104:107], v[128:131], v[168:171], v[104:107]\n" \
"v_pk_add_f16 v124, v140, v148\n" \
"v_pk_add_f16 v125, v141, v149\n" \
"s_mov_b32 m0, 0x27c00\n" \
"v_mfma_f32_16x16x32_f16 v[116:119], v[132:135], v[168:171], v[116:119]\n" \
"v_pk_add_f16 v126, v142, v150\n" \
"v_pk_add_f16 v127, v143, v151\n" \
"global_load_lds_dword v[248:249], off\n" \
"s_waitcnt vmcnt(6) lgkmcnt(0)\n" \
"s_barrier\n" \
"v_mfma_f32_16x16x32_f16 v[0:3], v[136:139], v[176:179], v[0:3]\n" \
"ds_read_b128 v[168:171], v240 offset:43008\n" \
"s_add_i32 m0, s44, 0x5000\n" \
"s_add_i32 s80, s46, 0x7800000\n" \
"s_bitcmp1_b32 s46, 22\n" \
"s_cselect_b32 s80, 0x80000000, s80\n" \
"ds_read_b64_tr_b16 v[128:129], v241 offset:32768\n" \
"v_mfma_f32_16x16x32_f16 v[12:15], v[140:143], v[176:179], v[12:15]\n" \
"v_pk_add_f16 v176, v176, v180\n" \
"v_pk_add_f16 v177, v177, v181\n" \
"buffer_load_dwordx4 v232, s[36:39], s80 offen sc1 lds\n" \
"ds_read_b64_tr_b16 v[130:131], v241 offset:40960\n" \
"v_mfma_f32_16x16x32_f16 v[4:7], v[144:147], v[180:183], v[4:7]\n" \
"v_pk_add_f16 v178, v178, v182\n" \
"v_pk_add_f16 v179, v179, v183\n" \
"buffer_load_dwordx4 v232, s[36:39], s80 offen offset:1024 sc1 lds\n" \
"ds_read_b64_tr_b16 v[132:133], v242 offset:32768\n" \
"v_mfma_f32_16x16x32_f16 v[16:19], v[148:151], v[180:183], v[16:19]\n" \
"ds_read_b128 v[172:175], v240 offset:43520\n" \
"buffer_load_dwordx4 v232, s[36:39], s80 offen offset:2048 sc1 lds\n" \
"ds_read_b64_tr_b16 v[134:135], v242 offset:40960\n" \
"ds_read_b64_tr_b16 v[152:153], v241 offset:33280\n" \
"v_mfma_f32_16x16x32_f16 v[8:11], v[120:123], v[176:179], v[8:11]\n" \
"buffer_load_dwordx4 v232, s[36:39], s80 offen offset:3072 sc1 lds\n" \
"ds_read_b64_tr_b16 v[154:155], v241 offset:41472\n" \
"v_mfma_f32_16x16x32_f16 v[20:23], v[124:127], v[176:179], v[20:23]\n" \
"s_add_i32 m0, s45, 0x5000\n" \
"s_add_i32 s81, s47, 0x7800000\n" \
"ds_read_b64_tr_b16 v[156:157], v242 offset:33280\n" \
"ds_read_b64_tr_b16 v[158:159], v242 offset:41472\n" \
"v_mfma_f32_16x16x32_f16 v[24:27], v[136:139], v[160:163], v[24:27]\n" \
"ds_read_b128 v[176:179], v246 offset:2048\n" \
"buffer_load_dwordx4 v232, s[36:39], s81 offen sc1 lds\n" \
"ds_read_b64_tr_b16 v[184:185], v243 offset:0\n" \
"v_mfma_f32_16x16x32_f16 v[36:39], v[140:143], v[160:163], v[36:39]\n" \
"v_pk_add_f16 v160, v160, v164\n" \
"v_pk_add_f16 v161, v161, v165\n" \
"ds_read_b64_tr_b16 v[186:187], v243 offset:8192\n" \
"ds_read_b64_tr_b16 v[188:189], v244 offset:0\n" \
"v_mfma_f32_16x16x32_f16 v[28:31], v[144:147], v[164:167], v[28:31]\n" \
"v_pk_add_f16 v162, v162, v166\n" \
"v_pk_add_f16 v163, v163, v167\n" \
"s_add_i32 m0, s44, 0xffff7000\n" \
"s_add_i32 s80, s46, 0x7c00000\n" \
"s_bitcmp0_b32 s46, 22\n" \
"s_cselect_b64 exec, -1, 0\n" \
"buffer_load_dwordx4 v232, s[36:39], s80 offen sc1 lds\n" \
"buffer_load_dwordx4 v232, s[36:39], s80 offen offset:1024 sc1 lds\n" \
"buffer_load_dwordx4 v232, s[36:39], s80 offen offset:2048 sc1 lds\n" \
"buffer_load_dwordx4 v232, s[36:39], s80 offen offset:3072 sc1 lds\n" \
"s_mov_b64 exec, -1\n" \
"v_mfma_f32_16x16x32_f16 v[40:43], v[148:151], v[164:167], v[40:43]\n" \
"ds_read_b128 v[180:183], v246 offset:2560\n" \
"s_add_i32 m0, s45, 0xffff7000\n" \
"s_add_i32 s81, s47, 0x7c00000\n" \
"ds_read_b64_tr_b16 v[190:191], v244 offset:8192\n" \
"ds_read_b64_tr_b16 v[192:193], v243 offset:512\n" \
"v_mfma_f32_16x16x32_f16 v[32:35], v[120:123], v[160:163], v[32:35]\n" \
"buffer_load_dwordx4 v232, s[36:39], s81 offen sc1 lds\n" \
"ds_read_b64_tr_b16 v[194:195], v243 offset:8704\n" \
"v_mfma_f32_16x16x32_f16 v[44:47], v[124:127], v[160:163], v[44:47]\n" \
"s_waitcnt lgkmcnt(5)\n" \
"v_pk_add_f16 v232, v128, v152\n" \
"v_pk_add_f16 v233, v129, v153\n" \
"ds_read_b64_tr_b16 v[196:197], v244 offset:512\n" \
"ds_read_b64_tr_b16 v[198:199], v244 offset:8704\n" \
"v_mfma_f32_16x16x32_f16 v[0:3], v[128:131], v[168:171], v[0:3]\n" \
"v_pk_add_f16 v234, v130, v154\n" \
"ds_read_b128 v[160:163], v246 offset:10240\n" \
"ds_read_b64_tr_b16 v[208:209], v243 offset:16384\n" \
"ds_read_b64_tr_b16 v[210:211], v243 offset:24576\n" \
"v_mfma_f32_16x16x32_f16 v[12:15], v[132:135], v[168:171], v[12:15]\n" \
"v_pk_add_f16 v168, v168, v172\n" \
"v_pk_add_f16 v169, v169, v173\n" \
"v_pk_add_f16 v235, v131, v155\n" \
"v_pk_add_f16 v236, v132, v156\n" \
"ds_read_b64_tr_b16 v[212:213], v244 offset:16384\n" \
"v_mfma_f32_16x16x32_f16 v[4:7], v[152:155], v[172:175], v[4:7]\n" \
"v_pk_add_f16 v170, v170, v174\n" \
"v_pk_add_f16 v171, v171, v175\n" \
"v_pk_add_f16 v237, v133, v157\n" \
"v_pk_add_f16 v238, v134, v158\n" \
"v_pk_add_f16 v239, v135, v159\n" \
"v_mfma_f32_16x16x32_f16 v[16:19], v[156:159], v[172:175], v[16:19]\n" \
"s_waitcnt lgkmcnt(6)\n" \
"v_pk_add_f16 v200, v184, v192\n" \
"ds_read_b128 v[164:167], v246 offset:10752\n" \
"ds_read_b64_tr_b16 v[214:215], v244 offset:24576\n" \
"ds_read_b64_tr_b16 v[216:217], v243 offset:16896\n" \
"v_mfma_f32_16x16x32_f16 v[8:11], v[232:235], v[168:171], v[8:11]\n" \
"v_pk_add_f16 v201, v185, v193\n" \
"v_pk_add_f16 v202, v186, v194\n" \
"ds_read_b64_tr_b16 v[218:219], v243 offset:25088\n" \
"v_mfma_f32_16x16x32_f16 v[20:23], v[236:239], v[168:171], v[20:23]\n" \
"ds_read_b64_tr_b16 v[220:221], v244 offset:16896\n" \
"ds_read_b64_tr_b16 v[222:223], v244 offset:25088\n" \
"v_mfma_f32_16x16x32_f16 v[0:3], v[184:187], v[176:179], v[0:3]\n" \
"v_pk_add_f16 v203, v187, v195\n" \
"s_waitcnt lgkmcnt(7)\n" \
"v_pk_add_f16 v204, v188, v196\n" \
"ds_read_b128 v[168:171], v240 offset:45056\n" \
"v_mfma_f32_16x16x32_f16 v[12:15], v[188:191], v[176:179], v[12:15]\n" \
"v_pk_add_f16 v176, v176, v180\n" \
"v_pk_add_f16 v177, v177, v181\n" \
"v_pk_add_f16 v205, v189, v197\n" \
"v_mfma_f32_16x16x32_f16 v[4:7], v[192:195], v[180:183], v[4:7]\n" \
"v_pk_add_f16 v178, v178, v182\n" \
"v_pk_add_f16 v179, v179, v183\n" \
"v_pk_add_f16 v206, v190, v198\n" \
"v_pk_add_f16 v207, v191, v199\n" \
"v_mfma_f32_16x16x32_f16 v[16:19], v[196:199], v[180:183], v[16:19]\n" \
"s_waitcnt lgkmcnt(4)\n" \
"v_pk_add_f16 v224, v208, v216\n" \
"ds_read_b128 v[172:175], v240 offset:45568\n" \
"v_mfma_f32_16x16x32_f16 v[8:11], v[200:203], v[176:179], v[8:11]\n" \
"v_pk_add_f16 v225, v209, v217\n" \
"v_mfma_f32_16x16x32_f16 v[20:23], v[204:207], v[176:179], v[20:23]\n" \
"s_waitcnt lgkmcnt(4)\n" \
"v_pk_add_f16 v226, v210, v218\n" \
"v_pk_add_f16 v227, v211, v219\n" \
"v_mfma_f32_16x16x32_f16 v[0:3], v[208:211], v[160:163], v[0:3]\n" \
"s_waitcnt lgkmcnt(2)\n" \
"v_pk_add_f16 v228, v212, v220\n" \
"v_pk_add_f16 v229, v213, v221\n" \
"ds_read_b128 v[176:179], v246 offset:4096\n" \
"v_mfma_f32_16x16x32_f16 v[12:15], v[212:215], v[160:163], v[12:15]\n" \
"v_pk_add_f16 v160, v160, v164\n" \
"v_pk_add_f16 v161, v161, v165\n" \
"v_pk_add_f16 v230, v214, v222\n" \
"v_mfma_f32_16x16x32_f16 v[4:7], v[216:219], v[164:167], v[4:7]\n" \
"v_pk_add_f16 v162, v162, v166\n" \
"v_pk_add_f16 v163, v163, v167\n" \
"v_pk_add_f16 v231, v215, v223\n" \
"v_mfma_f32_16x16x32_f16 v[16:19], v[220:223], v[164:167], v[16:19]\n" \
"ds_read_b128 v[180:183], v246 offset:4608\n" \
"s_mov_b32 vcc_lo, 0xaaaaaaaa\n" \
"v_mfma_f32_16x16x32_f16 v[8:11], v[224:227], v[160:163], v[8:11]\n" \
"s_mov_b32 vcc_hi, 0xaaaaaaaa\n" \
"v_mfma_f32_16x16x32_f16 v[20:23], v[228:231], v[160:163], v[20:23]\n" \
"v_add_u32 v241, 0xffff7000, v240\n" \
"s_waitcnt lgkmcnt(2)\n" \
"v_mfma_f32_16x16x32_f16 v[24:27], v[128:131], v[168:171], v[24:27]\n" \
"ds_read_b128 v[160:163], v246 offset:12288\n" \
"v_mfma_f32_16x16x32_f16 v[36:39], v[132:135], v[168:171], v[36:39]\n" \
"v_pk_add_f16 v168, v168, v172\n" \
"v_pk_add_f16 v169, v169, v173\n" \
"v_mfma_f32_16x16x32_f16 v[28:31], v[152:155], v[172:175], v[28:31]\n" \
"v_pk_add_f16 v170, v170, v174\n" \
"v_pk_add_f16 v171, v171, v175\n" \
"v_add_f32 v252, v4, v8\n" \
"v_add_f32 v253, v5, v9\n" \
"v_add_f32 v254, v6, v10\n" \
"v_add_f32 v255, v7, v11\n" \
"v_mfma_f32_16x16x32_f16 v[40:43], v[156:159], v[172:175], v[40:43]\n" \
"ds_read_b128 v[164:167], v246 offset:12800\n" \
"v_fmac_f32 v4, s56, v8\n" \
"v_fmac_f32 v5, s56, v9\n" \
"v_fmac_f32 v6, s56, v10\n" \
"v_fmac_f32 v7, s56, v11\n" \
"v_mfma_f32_16x16x32_f16 v[32:35], v[232:235], v[168:171], v[32:35]\n" \
"v_fmac_f32 v0, s57, v252\n" \
"v_fmac_f32 v1, s57, v253\n" \
"v_fmac_f32 v2, s57, v254\n" \
"v_fmac_f32 v3, s57, v255\n" \
"v_add_f32 v252, v16, v20\n" \
"v_mfma_f32_16x16x32_f16 v[44:47], v[236:239], v[168:171], v[44:47]\n" \
"v_add_f32 v253, v17, v21\n" \
"v_add_f32 v254, v18, v22\n" \
"v_add_f32 v255, v19, v23\n" \
"v_fmac_f32 v16, s56, v20\n" \
"s_waitcnt lgkmcnt(1)\n" \
"v_mfma_f32_16x16x32_f16 v[24:27], v[184:187], v[176:179], v[24:27]\n" \
"ds_read_b128 v[168:171], v240 offset:47104\n" \
"v_fmac_f32 v17, s56, v21\n" \
"v_fmac_f32 v18, s56, v22\n" \
"v_fmac_f32 v19, s56, v23\n" \
"v_fmac_f32 v12, s57, v252\n" \
"v_fmac_f32 v13, s57, v253\n" \
"v_mfma_f32_16x16x32_f16 v[36:39], v[188:191], v[176:179], v[36:39]\n" \
"v_pk_add_f16 v176, v176, v180\n" \
"v_pk_add_f16 v177, v177, v181\n" \
"v_fmac_f32 v14, s57, v254\n" \
"v_fmac_f32 v15, s57, v255\n" \
"v_cvt_pk_bf16_f32 v8, v0, v1\n" \
"v_cvt_pk_bf16_f32 v20, v4, v5\n" \
"v_mfma_f32_16x16x32_f16 v[28:31], v[192:195], v[180:183], v[28:31]\n" \
"v_pk_add_f16 v178, v178, v182\n" \
"v_pk_add_f16 v179, v179, v183\n" \
"v_cvt_pk_bf16_f32 v9, v2, v3\n" \
"v_cvt_pk_bf16_f32 v21, v6, v7\n" \
"v_cvt_pk_bf16_f32 v10, v12, v13\n" \
"v_cvt_pk_bf16_f32 v22, v16, v17\n" \
"v_cvt_pk_bf16_f32 v11, v14, v15\n" \
"v_mfma_f32_16x16x32_f16 v[40:43], v[196:199], v[180:183], v[40:43]\n" \
"ds_read_b128 v[172:175], v240 offset:47616\n" \
"v_cvt_pk_bf16_f32 v23, v18, v19\n" \
"s_not_b64 vcc, vcc\n" \
"v_cndmask_b32_dpp v0, v20, v8, vcc quad_perm:[1,0,3,2] row_mask:0xf bank_mask:0xf\n" \
"v_cndmask_b32_dpp v1, v21, v9, vcc quad_perm:[1,0,3,2] row_mask:0xf bank_mask:0xf\n" \
"v_mfma_f32_16x16x32_f16 v[32:35], v[200:203], v[176:179], v[32:35]\n" \
"s_waitcnt vmcnt(0)\n" \
"s_barrier\n" \
"v_cndmask_b32_dpp v2, v22, v10, vcc quad_perm:[1,0,3,2] row_mask:0xf bank_mask:0xf\n" \
"v_cndmask_b32_dpp v3, v23, v11, vcc quad_perm:[1,0,3,2] row_mask:0xf bank_mask:0xf\n" \
"buffer_store_dwordx4 v[0:3], v245, s[40:43], s69 offen sc1\n" \
"s_not_b64 vcc, vcc\n" \
"s_add_i32 s85, s69, 0x10000\n" \
"v_mfma_f32_16x16x32_f16 v[44:47], v[204:207], v[176:179], v[44:47]\n" \
"v_cndmask_b32_dpp v12, v8, v20, vcc quad_perm:[1,0,3,2] row_mask:0xf bank_mask:0xf\n" \
"v_cndmask_b32_dpp v13, v9, v21, vcc quad_perm:[1,0,3,2] row_mask:0xf bank_mask:0xf\n" \
"v_cndmask_b32_dpp v14, v10, v22, vcc quad_perm:[1,0,3,2] row_mask:0xf bank_mask:0xf\n" \
"v_cndmask_b32_dpp v15, v11, v23, vcc quad_perm:[1,0,3,2] row_mask:0xf bank_mask:0xf\n" \
"buffer_store_dwordx4 v[12:15], v245, s[40:43], s85 offen sc1\n" \
"v_mfma_f32_16x16x32_f16 v[24:27], v[208:211], v[160:163], v[24:27]\n" \
"ds_read_b128 v[176:179], v240 offset:6144\n" \
"v_mfma_f32_16x16x32_f16 v[36:39], v[212:215], v[160:163], v[36:39]\n" \
"s_waitcnt lgkmcnt(2)\n" \
"v_pk_add_f16 v160, v160, v164\n" \
"v_pk_add_f16 v161, v161, v165\n" \
"v_mfma_f32_16x16x32_f16 v[28:31], v[216:219], v[164:167], v[28:31]\n" \
"v_pk_add_f16 v162, v162, v166\n" \
"v_pk_add_f16 v163, v163, v167\n" \
"v_mfma_f32_16x16x32_f16 v[40:43], v[220:223], v[164:167], v[40:43]\n" \
"ds_read_b128 v[180:183], v240 offset:6656\n" \
"v_mfma_f32_16x16x32_f16 v[32:35], v[224:227], v[160:163], v[32:35]\n" \
"v_mfma_f32_16x16x32_f16 v[44:47], v[228:231], v[160:163], v[44:47]\n" \
"v_mfma_f32_16x16x32_f16 v[48:51], v[136:139], v[168:171], v[48:51]\n" \
"ds_read_b128 v[160:163], v240 offset:26624\n" \
"v_mfma_f32_16x16x32_f16 v[60:63], v[140:143], v[168:171], v[60:63]\n" \
"s_waitcnt lgkmcnt(3)\n" \
"v_pk_add_f16 v168, v168, v172\n" \
"v_pk_add_f16 v169, v169, v173\n" \
"v_mfma_f32_16x16x32_f16 v[52:55], v[144:147], v[172:175], v[52:55]\n" \
"v_pk_add_f16 v170, v170, v174\n" \
"v_pk_add_f16 v171, v171, v175\n" \
"v_add_f32 v252, v28, v32\n" \
"v_add_f32 v253, v29, v33\n" \
"v_mfma_f32_16x16x32_f16 v[64:67], v[148:151], v[172:175], v[64:67]\n" \
"ds_read_b128 v[164:167], v240 offset:27136\n" \
"v_add_f32 v254, v30, v34\n" \
"v_add_f32 v255, v31, v35\n" \
"v_fmac_f32 v28, s56, v32\n" \
"v_mfma_f32_16x16x32_f16 v[56:59], v[120:123], v[168:171], v[56:59]\n" \
"v_fmac_f32 v29, s56, v33\n" \
"v_fmac_f32 v30, s56, v34\n" \
"v_fmac_f32 v31, s56, v35\n" \
"v_mfma_f32_16x16x32_f16 v[68:71], v[124:127], v[168:171], v[68:71]\n" \
"v_fmac_f32 v24, s57, v252\n" \
"v_fmac_f32 v25, s57, v253\n" \
"v_fmac_f32 v26, s57, v254\n" \
"s_waitcnt lgkmcnt(2)\n" \
"v_mfma_f32_16x16x32_f16 v[48:51], v[128:131], v[176:179], v[48:51]\n" \
"ds_read_b128 v[168:171], v241 offset:6144\n" \
"v_fmac_f32 v27, s57, v255\n" \
"v_add_f32 v252, v40, v44\n" \
"v_add_f32 v253, v41, v45\n" \
"v_mfma_f32_16x16x32_f16 v[60:63], v[132:135], v[176:179], v[60:63]\n" \
"v_pk_add_f16 v176, v176, v180\n" \
"v_pk_add_f16 v177, v177, v181\n" \
"v_add_f32 v254, v42, v46\n" \
"v_add_f32 v255, v43, v47\n" \
"v_mfma_f32_16x16x32_f16 v[52:55], v[152:155], v[180:183], v[52:55]\n" \
"v_pk_add_f16 v178, v178, v182\n" \
"v_pk_add_f16 v179, v179, v183\n" \
"v_fmac_f32 v40, s56, v44\n" \
"v_fmac_f32 v41, s56, v45\n" \
"v_fmac_f32 v42, s56, v46\n" \
"v_mfma_f32_16x16x32_f16 v[64:67], v[156:159], v[180:183], v[64:67]\n" \
"ds_read_b128 v[172:175], v241 offset:6656\n" \
"v_fmac_f32 v43, s56, v47\n" \
"v_fmac_f32 v36, s57, v252\n" \
"v_fmac_f32 v37, s57, v253\n" \
"v_mfma_f32_16x16x32_f16 v[56:59], v[232:235], v[176:179], v[56:59]\n" \
"v_fmac_f32 v38, s57, v254\n" \
"v_fmac_f32 v39, s57, v255\n" \
"v_cvt_pk_bf16_f32 v32, v24, v25\n" \
"v_mfma_f32_16x16x32_f16 v[68:71], v[236:239], v[176:179], v[68:71]\n" \
"v_cvt_pk_bf16_f32 v44, v28, v29\n" \
"v_cvt_pk_bf16_f32 v33, v26, v27\n" \
"v_cvt_pk_bf16_f32 v45, v30, v31\n" \
"s_waitcnt lgkmcnt(1)\n" \
"v_mfma_f32_16x16x32_f16 v[48:51], v[184:187], v[160:163], v[48:51]\n" \
"ds_read_b128 v[176:179], v240 offset:49152\n" \
"v_cvt_pk_bf16_f32 v34, v36, v37\n" \
"v_cvt_pk_bf16_f32 v46, v40, v41\n" \
"v_mfma_f32_16x16x32_f16 v[60:63], v[188:191], v[160:163], v[60:63]\n" \
"v_pk_add_f16 v160, v160, v164\n" \
"v_pk_add_f16 v161, v161, v165\n" \
"v_cvt_pk_bf16_f32 v35, v38, v39\n" \
"v_cvt_pk_bf16_f32 v47, v42, v43\n" \
"s_not_b64 vcc, vcc\n" \
"v_mfma_f32_16x16x32_f16 v[52:55], v[192:195], v[164:167], v[52:55]\n" \
"v_pk_add_f16 v162, v162, v166\n" \
"v_pk_add_f16 v163, v163, v167\n" \
"v_cndmask_b32_dpp v24, v44, v32, vcc quad_perm:[1,0,3,2] row_mask:0xf bank_mask:0xf\n" \
"v_cndmask_b32_dpp v25, v45, v33, vcc quad_perm:[1,0,3,2] row_mask:0xf bank_mask:0xf\n" \
"v_cndmask_b32_dpp v26, v46, v34, vcc quad_perm:[1,0,3,2] row_mask:0xf bank_mask:0xf\n" \
"v_mfma_f32_16x16x32_f16 v[64:67], v[196:199], v[164:167], v[64:67]\n" \
"ds_read_b128 v[180:183], v240 offset:49664\n" \
"v_cndmask_b32_dpp v27, v47, v35, vcc quad_perm:[1,0,3,2] row_mask:0xf bank_mask:0xf\n" \
"buffer_store_dwordx4 v[24:27], v245, s[40:43], s70 offen sc1\n" \
"s_not_b64 vcc, vcc\n" \
"v_mfma_f32_16x16x32_f16 v[56:59], v[200:203], v[160:163], v[56:59]\n" \
"s_add_i32 s85, s70, 0x10000\n" \
"v_cndmask_b32_dpp v36, v32, v44, vcc quad_perm:[1,0,3,2] row_mask:0xf bank_mask:0xf\n" \
"v_cndmask_b32_dpp v37, v33, v45, vcc quad_perm:[1,0,3,2] row_mask:0xf bank_mask:0xf\n" \
"v_mfma_f32_16x16x32_f16 v[68:71], v[204:207], v[160:163], v[68:71]\n" \
"v_cndmask_b32_dpp v38, v34, v46, vcc quad_perm:[1,0,3,2] row_mask:0xf bank_mask:0xf\n" \
"v_cndmask_b32_dpp v39, v35, v47, vcc quad_perm:[1,0,3,2] row_mask:0xf bank_mask:0xf\n" \
"buffer_store_dwordx4 v[36:39], v245, s[40:43], s85 offen sc1\n" \
"v_mfma_f32_16x16x32_f16 v[48:51], v[208:211], v[168:171], v[48:51]\n" \
"ds_read_b128 v[160:163], v240 offset:8192\n" \
"v_mfma_f32_16x16x32_f16 v[60:63], v[212:215], v[168:171], v[60:63]\n" \
"s_waitcnt lgkmcnt(2)\n" \
"v_pk_add_f16 v168, v168, v172\n" \
"v_pk_add_f16 v169, v169, v173\n" \
"v_mfma_f32_16x16x32_f16 v[52:55], v[216:219], v[172:175], v[52:55]\n" \
"v_pk_add_f16 v170, v170, v174\n" \
"v_pk_add_f16 v171, v171, v175\n" \
"v_mfma_f32_16x16x32_f16 v[64:67], v[220:223], v[172:175], v[64:67]\n" \
"ds_read_b128 v[164:167], v240 offset:8704\n" \
"v_mfma_f32_16x16x32_f16 v[56:59], v[224:227], v[168:171], v[56:59]\n" \
"v_mfma_f32_16x16x32_f16 v[68:71], v[228:231], v[168:171], v[68:71]\n" \
"v_mfma_f32_16x16x32_f16 v[72:75], v[136:139], v[176:179], v[72:75]\n" \
"ds_read_b128 v[168:171], v240 offset:28672\n" \
"v_mfma_f32_16x16x32_f16 v[84:87], v[140:143], v[176:179], v[84:87]\n" \
"s_waitcnt lgkmcnt(3)\n" \
"v_pk_add_f16 v176, v176, v180\n" \
"v_pk_add_f16 v177, v177, v181\n" \
"v_mfma_f32_16x16x32_f16 v[76:79], v[144:147], v[180:183], v[76:79]\n" \
"v_pk_add_f16 v178, v178, v182\n" \
"v_pk_add_f16 v179, v179, v183\n" \
"v_add_f32 v252, v52, v56\n" \
"v_add_f32 v253, v53, v57\n" \
"v_mfma_f32_16x16x32_f16 v[88:91], v[148:151], v[180:183], v[88:91]\n" \
"ds_read_b128 v[172:175], v240 offset:29184\n" \
"v_add_f32 v254, v54, v58\n" \
"v_add_f32 v255, v55, v59\n" \
"v_mfma_f32_16x16x32_f16 v[80:83], v[120:123], v[176:179], v[80:83]\n" \
"v_fmac_f32 v52, s56, v56\n" \
"v_fmac_f32 v53, s56, v57\n" \
"v_mfma_f32_16x16x32_f16 v[92:95], v[124:127], v[176:179], v[92:95]\n" \
"v_fmac_f32 v54, s56, v58\n" \
"v_fmac_f32 v55, s56, v59\n" \
"s_waitcnt lgkmcnt(2)\n" \
"v_mfma_f32_16x16x32_f16 v[72:75], v[128:131], v[160:163], v[72:75]\n" \
"ds_read_b128 v[176:179], v241 offset:8192\n" \
"v_fmac_f32 v48, s57, v252\n" \
"v_fmac_f32 v49, s57, v253\n" \
"v_mfma_f32_16x16x32_f16 v[84:87], v[132:135], v[160:163], v[84:87]\n" \
"v_pk_add_f16 v160, v160, v164\n" \
"v_pk_add_f16 v161, v161, v165\n" \
"v_fmac_f32 v50, s57, v254\n" \
"v_fmac_f32 v51, s57, v255\n" \
"v_mfma_f32_16x16x32_f16 v[76:79], v[152:155], v[164:167], v[76:79]\n" \
"v_pk_add_f16 v162, v162, v166\n" \
"v_pk_add_f16 v163, v163, v167\n" \
"v_add_f32 v252, v64, v68\n" \
"v_add_f32 v253, v65, v69\n" \
"v_mfma_f32_16x16x32_f16 v[88:91], v[156:159], v[164:167], v[88:91]\n" \
"ds_read_b128 v[180:183], v241 offset:8704\n" \
"v_add_f32 v254, v66, v70\n" \
"v_add_f32 v255, v67, v71\n" \
"v_mfma_f32_16x16x32_f16 v[80:83], v[232:235], v[160:163], v[80:83]\n" \
"v_fmac_f32 v64, s56, v68\n" \
"v_fmac_f32 v65, s56, v69\n" \
"v_mfma_f32_16x16x32_f16 v[92:95], v[236:239], v[160:163], v[92:95]\n" \
"v_fmac_f32 v66, s56, v70\n" \
"v_fmac_f32 v67, s56, v71\n" \
"s_waitcnt lgkmcnt(2)\n" \
"v_mfma_f32_16x16x32_f16 v[72:75], v[184:187], v[168:171], v[72:75]\n" \
"ds_read_b128 v[160:163], v240 offset:40960\n" \
"v_fmac_f32 v60, s57, v252\n" \
"v_fmac_f32 v61, s57, v253\n" \
"v_mfma_f32_16x16x32_f16 v[84:87], v[188:191], v[168:171], v[84:87]\n" \
"v_pk_add_f16 v168, v168, v172\n" \
"v_pk_add_f16 v169, v169, v173\n" \
"v_fmac_f32 v62, s57, v254\n" \
"v_fmac_f32 v63, s57, v255\n" \
"v_mfma_f32_16x16x32_f16 v[76:79], v[192:195], v[172:175], v[76:79]\n" \
"v_pk_add_f16 v170, v170, v174\n" \
"v_pk_add_f16 v171, v171, v175\n" \
"v_cvt_pk_bf16_f32 v56, v48, v49\n" \
"v_cvt_pk_bf16_f32 v68, v52, v53\n" \
"v_mfma_f32_16x16x32_f16 v[88:91], v[196:199], v[172:175], v[88:91]\n" \
"ds_read_b128 v[164:167], v240 offset:41472\n" \
"v_cvt_pk_bf16_f32 v57, v50, v51\n" \
"v_cvt_pk_bf16_f32 v69, v54, v55\n" \
"v_mfma_f32_16x16x32_f16 v[80:83], v[200:203], v[168:171], v[80:83]\n" \
"v_cvt_pk_bf16_f32 v58, v60, v61\n" \
"v_cvt_pk_bf16_f32 v70, v64, v65\n" \
"v_mfma_f32_16x16x32_f16 v[92:95], v[204:207], v[168:171], v[92:95]\n" \
"v_cvt_pk_bf16_f32 v59, v62, v63\n" \
"v_cvt_pk_bf16_f32 v71, v66, v67\n" \
"s_waitcnt lgkmcnt(2)\n" \
"v_mfma_f32_16x16x32_f16 v[72:75], v[208:211], v[176:179], v[72:75]\n" \
"ds_read_b128 v[168:171], v240 offset:0\n" \
"s_not_b64 vcc, vcc\n" \
"v_cndmask_b32_dpp v48, v68, v56, vcc quad_perm:[1,0,3,2] row_mask:0xf bank_mask:0xf\n" \
"v_mfma_f32_16x16x32_f16 v[84:87], v[212:215], v[176:179], v[84:87]\n" \
"v_pk_add_f16 v176, v176, v180\n" \
"v_pk_add_f16 v177, v177, v181\n" \
"v_cndmask_b32_dpp v49, v69, v57, vcc quad_perm:[1,0,3,2] row_mask:0xf bank_mask:0xf\n" \
"v_cndmask_b32_dpp v50, v70, v58, vcc quad_perm:[1,0,3,2] row_mask:0xf bank_mask:0xf\n" \
"v_mfma_f32_16x16x32_f16 v[76:79], v[216:219], v[180:183], v[76:79]\n" \
"v_pk_add_f16 v178, v178, v182\n" \
"v_pk_add_f16 v179, v179, v183\n" \
"v_cndmask_b32_dpp v51, v71, v59, vcc quad_perm:[1,0,3,2] row_mask:0xf bank_mask:0xf\n" \
"buffer_store_dwordx4 v[48:51], v245, s[40:43], s71 offen sc1\n" \
"v_mfma_f32_16x16x32_f16 v[88:91], v[220:223], v[180:183], v[88:91]\n" \
"ds_read_b128 v[172:175], v240 offset:512\n" \
"s_not_b64 vcc, vcc\n" \
"s_add_i32 s85, s71, 0x10000\n" \
"v_mfma_f32_16x16x32_f16 v[80:83], v[224:227], v[176:179], v[80:83]\n" \
"v_cndmask_b32_dpp v60, v56, v68, vcc quad_perm:[1,0,3,2] row_mask:0xf bank_mask:0xf\n" \
"v_cndmask_b32_dpp v61, v57, v69, vcc quad_perm:[1,0,3,2] row_mask:0xf bank_mask:0xf\n" \
"v_mfma_f32_16x16x32_f16 v[92:95], v[228:231], v[176:179], v[92:95]\n" \
"v_cndmask_b32_dpp v62, v58, v70, vcc quad_perm:[1,0,3,2] row_mask:0xf bank_mask:0xf\n" \
"v_cndmask_b32_dpp v63, v59, v71, vcc quad_perm:[1,0,3,2] row_mask:0xf bank_mask:0xf\n" \
"buffer_store_dwordx4 v[60:63], v245, s[40:43], s85 offen sc1\n" \
"s_waitcnt lgkmcnt(1)\n" \
"v_mfma_f32_16x16x32_f16 v[96:99], v[136:139], v[160:163], v[96:99]\n" \
"ds_read_b128 v[176:179], v240 offset:20480\n" \
"v_mfma_f32_16x16x32_f16 v[108:111], v[140:143], v[160:163], v[108:111]\n" \
"v_pk_add_f16 v160, v160, v164\n" \
"v_pk_add_f16 v161, v161, v165\n" \
"v_mfma_f32_16x16x32_f16 v[100:103], v[144:147], v[164:167], v[100:103]\n" \
"v_pk_add_f16 v162, v162, v166\n" \
"v_pk_add_f16 v163, v163, v167\n" \
"v_add_f32 v252, v76, v80\n" \
"v_add_f32 v253, v77, v81\n" \
"v_mfma_f32_16x16x32_f16 v[112:115], v[148:151], v[164:167], v[112:115]\n" \
"ds_read_b128 v[180:183], v240 offset:20992\n" \
"v_add_f32 v254, v78, v82\n" \
"v_add_f32 v255, v79, v83\n" \
"v_mfma_f32_16x16x32_f16 v[104:107], v[120:123], v[160:163], v[104:107]\n" \
"v_fmac_f32 v76, s56, v80\n" \
"v_fmac_f32 v77, s56, v81\n" \
"v_mfma_f32_16x16x32_f16 v[116:119], v[124:127], v[160:163], v[116:119]\n" \
"v_fmac_f32 v78, s56, v82\n" \
"v_fmac_f32 v79, s56, v83\n" \
"v_mfma_f32_16x16x32_f16 v[96:99], v[128:131], v[168:171], v[96:99]\n" \
"ds_read_b128 v[160:163], v241 offset:0\n" \
"v_fmac_f32 v72, s57, v252\n" \
"v_fmac_f32 v73, s57, v253\n" \
"v_mfma_f32_16x16x32_f16 v[108:111], v[132:135], v[168:171], v[108:111]\n" \
"s_waitcnt lgkmcnt(2)\n" \
"v_pk_add_f16 v168, v168, v172\n" \
"v_pk_add_f16 v169, v169, v173\n" \
"v_fmac_f32 v74, s57, v254\n" \
"v_fmac_f32 v75, s57, v255\n" \
"v_mfma_f32_16x16x32_f16 v[100:103], v[152:155], v[172:175], v[100:103]\n" \
"v_pk_add_f16 v170, v170, v174\n" \
"v_pk_add_f16 v171, v171, v175\n" \
"v_add_f32 v252, v88, v92\n" \
"v_add_f32 v253, v89, v93\n" \
"v_mfma_f32_16x16x32_f16 v[112:115], v[156:159], v[172:175], v[112:115]\n" \
"ds_read_b128 v[164:167], v241 offset:512\n" \
"v_add_f32 v254, v90, v94\n" \
"v_add_f32 v255, v91, v95\n" \
"v_mfma_f32_16x16x32_f16 v[104:107], v[232:235], v[168:171], v[104:107]\n" \
"v_fmac_f32 v88, s56, v92\n" \
"v_fmac_f32 v89, s56, v93\n" \
"v_mfma_f32_16x16x32_f16 v[116:119], v[236:239], v[168:171], v[116:119]\n" \
"v_fmac_f32 v90, s56, v94\n" \
"v_fmac_f32 v91, s56, v95\n" \
"v_mfma_f32_16x16x32_f16 v[96:99], v[184:187], v[176:179], v[96:99]\n" \
"v_fmac_f32 v84, s57, v252\n" \
"v_fmac_f32 v85, s57, v253\n" \
"v_fmac_f32 v86, s57, v254\n" \
"v_mfma_f32_16x16x32_f16 v[108:111], v[188:191], v[176:179], v[108:111]\n" \
"s_waitcnt lgkmcnt(1)\n" \
"v_pk_add_f16 v176, v176, v180\n" \
"v_pk_add_f16 v177, v177, v181\n" \
"v_fmac_f32 v87, s57, v255\n" \
"v_mfma_f32_16x16x32_f16 v[100:103], v[192:195], v[180:183], v[100:103]\n" \
"v_pk_add_f16 v178, v178, v182\n" \
"v_pk_add_f16 v179, v179, v183\n" \
"v_cvt_pk_bf16_f32 v80, v72, v73\n" \
"v_cvt_pk_bf16_f32 v92, v76, v77\n" \
"v_mfma_f32_16x16x32_f16 v[112:115], v[196:199], v[180:183], v[112:115]\n" \
"v_cvt_pk_bf16_f32 v81, v74, v75\n" \
"v_cvt_pk_bf16_f32 v93, v78, v79\n" \
"v_mfma_f32_16x16x32_f16 v[104:107], v[200:203], v[176:179], v[104:107]\n" \
"v_cvt_pk_bf16_f32 v82, v84, v85\n" \
"v_cvt_pk_bf16_f32 v94, v88, v89\n" \
"v_mfma_f32_16x16x32_f16 v[116:119], v[204:207], v[176:179], v[116:119]\n" \
"v_cvt_pk_bf16_f32 v83, v86, v87\n" \
"v_cvt_pk_bf16_f32 v95, v90, v91\n" \
"v_mfma_f32_16x16x32_f16 v[96:99], v[208:211], v[160:163], v[96:99]\n" \
"s_not_b64 vcc, vcc\n" \
"v_cndmask_b32_dpp v72, v92, v80, vcc quad_perm:[1,0,3,2] row_mask:0xf bank_mask:0xf\n" \
"v_cndmask_b32_dpp v73, v93, v81, vcc quad_perm:[1,0,3,2] row_mask:0xf bank_mask:0xf\n" \
"v_mfma_f32_16x16x32_f16 v[108:111], v[212:215], v[160:163], v[108:111]\n" \
"s_waitcnt lgkmcnt(0)\n" \
"v_pk_add_f16 v160, v160, v164\n" \
"v_pk_add_f16 v161, v161, v165\n" \
"v_cndmask_b32_dpp v74, v94, v82, vcc quad_perm:[1,0,3,2] row_mask:0xf bank_mask:0xf\n" \
"v_mfma_f32_16x16x32_f16 v[100:103], v[216:219], v[164:167], v[100:103]\n" \
"v_pk_add_f16 v162, v162, v166\n" \
"v_pk_add_f16 v163, v163, v167\n" \
"v_cndmask_b32_dpp v75, v95, v83, vcc quad_perm:[1,0,3,2] row_mask:0xf bank_mask:0xf\n" \
"buffer_store_dwordx4 v[72:75], v245, s[40:43], s72 offen sc1\n" \
"v_mfma_f32_16x16x32_f16 v[112:115], v[220:223], v[164:167], v[112:115]\n" \
"s_not_b64 vcc, vcc\n" \
"s_add_i32 s85, s72, 0x10000\n" \
"v_mfma_f32_16x16x32_f16 v[104:107], v[224:227], v[160:163], v[104:107]\n" \
"v_cndmask_b32_dpp v84, v80, v92, vcc quad_perm:[1,0,3,2] row_mask:0xf bank_mask:0xf\n" \
"v_cndmask_b32_dpp v85, v81, v93, vcc quad_perm:[1,0,3,2] row_mask:0xf bank_mask:0xf\n" \
"v_mfma_f32_16x16x32_f16 v[116:119], v[228:231], v[160:163], v[116:119]\n" \
"v_cndmask_b32_dpp v86, v82, v94, vcc quad_perm:[1,0,3,2] row_mask:0xf bank_mask:0xf\n" \
"v_cndmask_b32_dpp v87, v83, v95, vcc quad_perm:[1,0,3,2] row_mask:0xf bank_mask:0xf\n" \
"buffer_store_dwordx4 v[84:87], v245, s[40:43], s85 offen sc1\n" \
"s_nop 7\n" \
"v_add_f32 v252, v100, v104\n" \
"v_add_f32 v253, v101, v105\n" \
"v_add_f32 v254, v102, v106\n" \
"v_add_f32 v255, v103, v107\n" \
"v_fmac_f32 v100, s56, v104\n" \
"v_fmac_f32 v101, s56, v105\n" \
"v_fmac_f32 v102, s56, v106\n" \
"v_fmac_f32 v103, s56, v107\n" \
"v_fmac_f32 v96, s57, v252\n" \
"v_fmac_f32 v97, s57, v253\n" \
"v_fmac_f32 v98, s57, v254\n" \
"v_fmac_f32 v99, s57, v255\n" \
"v_add_f32 v252, v112, v116\n" \
"v_add_f32 v253, v113, v117\n" \
"v_add_f32 v254, v114, v118\n" \
"v_add_f32 v255, v115, v119\n" \
"v_fmac_f32 v112, s56, v116\n" \
"v_fmac_f32 v113, s56, v117\n" \
"v_fmac_f32 v114, s56, v118\n" \
"v_fmac_f32 v115, s56, v119\n" \
"v_fmac_f32 v108, s57, v252\n" \
"v_fmac_f32 v109, s57, v253\n" \
"v_fmac_f32 v110, s57, v254\n" \
"v_fmac_f32 v111, s57, v255\n" \
"v_cvt_pk_bf16_f32 v104, v96, v97\n" \
"v_cvt_pk_bf16_f32 v116, v100, v101\n" \
"v_cvt_pk_bf16_f32 v105, v98, v99\n" \
"v_cvt_pk_bf16_f32 v117, v102, v103\n" \
"v_cvt_pk_bf16_f32 v106, v108, v109\n" \
"v_cvt_pk_bf16_f32 v118, v112, v113\n" \
"v_cvt_pk_bf16_f32 v107, v110, v111\n" \
"v_cvt_pk_bf16_f32 v119, v114, v115\n" \
"s_not_b64 vcc, vcc\n" \
"v_cndmask_b32_dpp v96, v116, v104, vcc quad_perm:[1,0,3,2] row_mask:0xf bank_mask:0xf\n" \
"v_cndmask_b32_dpp v97, v117, v105, vcc quad_perm:[1,0,3,2] row_mask:0xf bank_mask:0xf\n" \
"v_cndmask_b32_dpp v98, v118, v106, vcc quad_perm:[1,0,3,2] row_mask:0xf bank_mask:0xf\n" \
"v_cndmask_b32_dpp v99, v119, v107, vcc quad_perm:[1,0,3,2] row_mask:0xf bank_mask:0xf\n" \
"buffer_store_dwordx4 v[96:99], v245, s[40:43], s73 offen sc1\n" \
"s_not_b64 vcc, vcc\n" \
"s_add_i32 s85, s73, 0x10000\n" \
"v_cndmask_b32_dpp v108, v104, v116, vcc quad_perm:[1,0,3,2] row_mask:0xf bank_mask:0xf\n" \
"v_cndmask_b32_dpp v109, v105, v117, vcc quad_perm:[1,0,3,2] row_mask:0xf bank_mask:0xf\n" \
"v_cndmask_b32_dpp v110, v106, v118, vcc quad_perm:[1,0,3,2] row_mask:0xf bank_mask:0xf\n" \
"v_cndmask_b32_dpp v111, v107, v119, vcc quad_perm:[1,0,3,2] row_mask:0xf bank_mask:0xf\n" \
"buffer_store_dwordx4 v[108:111], v245, s[40:43], s85 offen sc1\n" \
"s_waitcnt vmcnt(0)\n" \
"s_barrier\n" \
"s_branch .Lcf_e\n" \
".Ls12_%=:\n" \
"s_mov_b32 s87, 0x200000\n" \
"s_mov_b32 s86, 0x8004000\n" \
".Lt12_%=:\n" \
"s_sleep 3\n" \
"buffer_load_dword v253, v252, s[36:39], s86 offen sc1\n" \
"s_waitcnt vmcnt(0)\n" \
"s_sub_u32 s87, s87, 1\n" \
"s_cbranch_scc1 .Lr12_%=\n" \
"v_cmp_eq_u32 vcc, 0, v253\n" \
"s_cbranch_vccnz .Lt12_%=\n" \
"s_branch .Lr12_%=\n" \
".Ls16_%=:\n" \
"s_mov_b32 s87, 0x200000\n" \
"s_mov_b32 s86, 0x800c000\n" \
".Lt16_%=:\n" \
"s_sleep 3\n" \
"buffer_load_dword v253, v252, s[36:39], s86 offen sc1\n" \
"s_waitcnt vmcnt(0)\n" \
"s_sub_u32 s87, s87, 1\n" \
"s_cbranch_scc1 .Lr16_%=\n" \
"v_cmp_eq_u32 vcc, 0, v253\n" \
"s_cbranch_vccnz .Lt16_%=\n" \
"s_branch .Lr16_%=\n" \
".Ls110_%=:\n" \
"s_mov_b32 s87, 0x200000\n" \
"s_mov_b32 s86, 0x8014000\n" \
".Lt110_%=:\n" \
"s_sleep 3\n" \
"buffer_load_dword v253, v252, s[36:39], s86 offen sc1\n" \
"s_waitcnt vmcnt(0)\n" \
"s_sub_u32 s87, s87, 1\n" \
"s_cbranch_scc1 .Lr110_%=\n" \
"v_cmp_eq_u32 vcc, 0, v253\n" \
"s_cbranch_vccnz .Lt110_%=\n" \
"s_branch .Lr110_%=\n" \
""
#define CLOBBERS "v0", "v1", "v2", "v3", "v4", "v5", "v6", "v7", "v8", "v9", "v10", "v11", "v12", "v13", "v14", "v15", "v16", "v17", "v18", "v19", "v20", "v21", "v22", "v23", "v24", "v25", "v26", "v27", "v28", "v29", "v30", "v31", "v32", "v33", "v34", "v35", "v36", "v37", "v38", "v39", "v40", "v41", "v42", "v43", "v44", "v45", "v46", "v47", "v48", "v49", "v50", "v51", "v52", "v53", "v54", "v55", "v56", "v57", "v58", "v59", "v60", "v61", "v62", "v63", "v64", "v65", "v66", "v67", "v68", "v69", "v70", "v71", "v84", "v85", "v86", "v87", "v88", "v89", "v90", "v91", "v92", "v93", "v94", "v95", "v120", "v121", "v122", "v123", "v124", "v125", "v126", "v127", "v128", "v129", "v130", "v131", "v132", "v133", "v134", "v135", "v136", "v137", "v138", "v139", "v140", "v141", "v142", "v143", "v144", "v145", "v146", "v147", "v148", "v149", "v150", "v151", "v152", "v153", "v154", "v155", "v156", "v157", "v158", "v159", "v160", "v161", "v162", "v163", "v164", "v165", "v166", "v167", "v168", "v169", "v170", "v171", "v172", "v173", "v174", "v175", "v176", "v177", "v178", "v179", "v180", "v181", "v182", "v183", "v184", "v185", "v186", "v187", "v188", "v189", "v190", "v191", "v76", "v77", "v78", "v79", "v80", "v81", "v82", "v83", "v253", "v254", "v255", "s80", "s81", "s82", "s83", "s84", "s85", "s86", "s87", "s88", "s89", "s90", "s91", "s92", "s75", "s94", "s95"
template <int NM>
__device__ __forceinline__ void run1(Ctx& cx, const unsigned short* __restrict__ x, const unsigned short* __restrict__ w, RQ& rq) {
  unsigned xs;
  phase1<NM>(cx, x, w, rq, xs, rq.vb, [&]() __attribute__((always_inline)) {
  static_assert(NS - st_first(PG) == 3 && NH == 10 && NQW == 11 && RAWL == 110592, "constants of the generated code");
  const unsigned bid = cx.vb, idx = bid >> 3, slot = (bid & 7) * 4 + (idx >> 3), lane = cx.lane4 >> 2, h = (idx >> 2) & 1, wv = cx.wv;
  const unsigned lds0 = (unsigned)(uintptr_t)(lds_void*)cx.smem;
  unsigned own = ((wv ^ idx) & 3) - 1u;
  asm volatile("s_ashr_i32 %0, %0, 31" : "+s"(own) :: "scc");
  unsigned* sa = rq.s, *sb = rq.s + 16;
  sa[0] = cx.ldg; sa[1] = cx.ldf; sa[2] = cx.sg; sa[3] = cx.sf;
#pragma unroll
  for (int j = 0; j < 4; ++j) {
    sa[4 + j] = ((bid + j) & 3) * 16384;
    sa[8 + j] = ((bid + j) & 3) * 256;
  }
  if (NM == 5) {
    sa[8] = lds0 + RAWL + 32768 + (wv & 1) * 8192 + (wv & 2) * 2048;
    sa[11] = (30 + (wv & 1)) * SBLK + (wv & 2) * 10240;
    const unsigned long long pace = wv == 7 ? (bid & 1 ? bytes(105, 100, 90, 88, 86, 84, 82) : bytes(150, 140, 120, 100, 90, 88, 86)) : 0ull;
    sa[9] = (unsigned)pace;
    sb[14] = (unsigned)(pace >> 32);
    sa[10] = lds0 + CORD + (wv & 3) * 1024;
  }
  sa[12] = min(slot, 1u) * 0xbf800000u;
  sa[13] = min(slot, 1u) * 0xbf000000u;
  sa[14] = sa[15] = own;
#pragma unroll
  for (int n = 0; n < NH; ++n) {
    const unsigned blk = (xs & 255u) + n % 5;
    rq.s[n ? 15 + n : 32] = (xs & ~255u) + (n / 5) * (8 * SBLK) + (blk + (blk & 192u)) * 128u;
  }
#pragma unroll
  for (int m = 0; m < 5; ++m) {
    const unsigned t = NM == 5 ? (m + 1) % 5 : m;
    sb[9 + m] = t * (16 * 65536) + ((h * 10 + (wv >> 2) * 5 + (wv & 2) + t) & 2) * 4096;
  }
#pragma unroll
  for (int i = 0; i < 33; ++i) if (i != 31) rq.s[i] = __builtin_amdgcn_readfirstlane(rq.s[i]);
  const unsigned prd = cx.prd + (RAWL - RAWB);
  const unsigned yo = (h * NBLK + (wv >> 2) * 80 + (lane & 14)) * 65536 + ((idx & 3) * 4 + (wv & 1)) * 4096 + (lane >> 4) * 16 + (2 * slot + (lane & 1)) * 64;
  unsigned* v = rq.v;
  v[0] = cx.voff; v[1] = cx.voff5; v[2] = cx.poff; v[3] = cx.xo; v[4] = prd; v[5] = prd ^ 32u; v[6] = prd ^ 64u; v[7] = prd ^ 96u;
  v[8] = cx.xbase; v[9] = cx.wbase; v[10] = cx.wbase ^ 32u; v[11] = cx.fvo; v[12] = lane * 16; v[13] = yo; v[14] = cx.pwr + (RAWL - RAWB);
  if (NM == 6) {
    v[15] = cx.crd;
    v[16] = CBUF + min(lane & 15, 10u) * 272 + wv * 64 + (cx.lane4 >> 6) * 16;
    v[17] = CORD + lane * 16;
    v[18] = (bid & 7) * 1024 + (idx & 3) * 256 + lane * 4;
    const unsigned fg = WS_GRP - WS_S + (idx & 3) * 256 + h * 128;
    v[19] = wv == 1 && lane < 8 ? fg + (lane + (lane & 4)) * 1024 + slot * 4 : 0x80000000u;
    v[20] = wv == 0 ? fg + ((bid & 7) + (bid & 4)) * 1024 + ((lane & 31) ^ ((lane & 31) == slot)) * 4 : 0x80000000u;
  } else {
    const unsigned co = (((bid >> 3) & 3) * 2 + ((bid >> 2) & 1)) * 128u + (cx.lane4 & 127u);
    const unsigned long long p = (unsigned long long)((const char*)(cx.lane4 < 128 ? cx.gamma : cx.beta) + co);
    v[15] = 0u;
    v[16] = (unsigned)p;
    v[17] = (unsigned)(p >> 32);
    v[18] = cx.fvo;
  }
  rq.s[31] = __builtin_amdgcn_readfirstlane(wv & 2 ? cx.pft + (wv & 1) * 8192 : cx.pfo - 8192 - (wv & 1) * 8192);
#pragma unroll
  for (int i = 0; i < 33; ++i) asm volatile("" : "+s"(rq.s[i]));
#pragma unroll
  for (int i = 0; i < (NM == 6 ? 21 : 19); ++i) asm volatile("" : "+v"(rq.v[i]));
  SB0;
  });
}

template <int CTRL>
__device__ __forceinline__ float bfly(float a, float b, bool hi) {
  const float give = hi ? a : b, keep = hi ? b : a;
  return keep + __builtin_bit_cast(float, __builtin_amdgcn_update_dpp(0, __builtin_bit_cast(int, give), CTRL, 0xf, 0xf, false));
}
__device__ __forceinline__ float halves_sum(float v) {
  const auto r = __builtin_amdgcn_permlane32_swap(__float_as_uint(v), __float_as_uint(v), false, false);
  return __uint_as_float(r[0]) + __uint_as_float(r[1]);
}

extern "C" __global__ __launch_bounds__(512, 2) void conv_fft(char* ws, const unsigned short* __restrict__ x,
                                                   const unsigned short* __restrict__ w,
                                                   const unsigned short* __restrict__ gamma,
                                                   const unsigned short* __restrict__ beta,
                                                   unsigned short* __restrict__ out) {
  __shared__ __attribute__((aligned(16))) char smem[160 * 1024];
  const unsigned lds0 = (unsigned)(uintptr_t)(lds_void*)smem;
  const int wv = __builtin_amdgcn_readfirstlane(threadIdx.x >> 6);
  Ctx cx;
  {
    unsigned len;
    asm volatile("s_getpc_b64 %0\n.Lcf_a:\n\ts_mov_b32 %1, .Lcf_b-.Lcf_a\n\ts_mov_b32 %2, .Lcf_t-.Lcf_a" : "=s"(cx.pc), "=s"(len), "=s"(cx.pft));
    cx.wv = wv;
    cx.pfo = len;
    unsigned xcc;
    asm volatile("s_getreg_b32 %0, hwreg(HW_REG_XCC_ID, 0, 3)" : "=s"(xcc));
    unsigned id = blockIdx.x;
    asm volatile("" : "+s"(id));
    cx.id = id;
    cx.vb = (int)((id & 248u) | (id & 256u ? (id >> 10) & 7u : xcc));
    const char* pb = ws + WS_PUB;
    u32x4 sx, sw;
    if ((id | wv) == 0) {
      const unsigned l = threadIdx.x;
      const u32x4 z = {0u, 0u, 0u, 0u};
      {
#pragma unroll
        for (int k = 0; k < 2; ++k)
          asm volatile("global_store_dwordx4 %0, %1, %2 sc1" :: "v"(32768 + (l & 7) * 16 + (l & 8) * 512 + (l & 16) * 1024 + (l & 32) * 32 + k * 8192), "v"(z), "s"(pb) : "memory");
      }
      {
        const unsigned so = l * (31u * 1024u + 16u) + (4u << 20);
        asm volatile("global_store_dwordx4 %2, %3, %4 sc1\n\tglobal_load_dwordx4 %0, %5, %6\n\tglobal_load_dwordx4 %1, %7, %8"
                     : "=&v"(sx), "=&v"(sw) : "v"(l * 16 + 1024), "v"(z), "s"(pb), "v"(so + (l & 3u) * (8u << 20)), "s"(x), "v"(so + (l & 1u) * (6u << 20)), "s"(w) : "memory");
      }
    }
    if (id) code_touch(cx);
    SB0;
    issue_x(0, wv, x_addr(cx.vb, 0, wv), x_rsrc(x, cx.vb), lds0);
    SB0;
    if ((id | wv) == 0) {
      wait_vm<9>();
      asm volatile("" : "+v"(sx), "+v"(sw));
      unsigned m = expo(amax(amax(amax(amax(0, sx[0]), sx[1]), sx[2]), sx[3])) | expo(amax(amax(amax(amax(0, sw[0]), sw[1]), sw[2]), sw[3])) << 16;
      m = amax(m, (unsigned)__builtin_amdgcn_update_dpp(0, (int)m, 0x128, 0xf, 0xf, false));
      m = amax(m, (unsigned)__builtin_amdgcn_update_dpp(0, (int)m, 0x124, 0xf, 0xf, false));
      m = amax(m, (unsigned)__builtin_amdgcn_update_dpp(0, (int)m, 0x122, 0xf, 0xf, false));
      m = amax(m, (unsigned)__builtin_amdgcn_update_dpp(0, (int)m, 0x121, 0xf, 0xf, false));
      unsigned ex = 28, ew = 28;
#pragma unroll
      for (int l = 0; l < 64; l += 16) {
        const unsigned v = (unsigned)__builtin_amdgcn_readlane((int)m, l);
        ex = max(ex + 2, v & 255u) - 2;
        ew = max(ew + 3, v >> 16) - 3;
      }
      const unsigned long long tg = __builtin_amdgcn_s_memrealtime();
      const unsigned lo = (unsigned)tg << 4 | xcc << 1, hi = ((unsigned)(tg >> 28) & 0xffffu) | min(ex, 220u) << 16 | min(ew, 220u) << 24;
      asm volatile("global_store_dwordx4 %0, %1, %2 sc1\n\ts_nop 1" :: "v"(threadIdx.x * 16), "v"(u32x4{lo, hi, ~lo, ~hi}), "s"(pb) : "memory");
      cx.tag[0] = lo;
      cx.tag[1] = hi;
    }
  }
  cx.smem = smem;
  cx.ws = ws;
  cx.gamma = gamma;
  cx.beta = beta;
  bf16x8 gb[8];
  unsigned fv;
  {
    RQ rq;
    if (wv < 4) run1<6>(cx, x, w, rq); else run1<5>(cx, x, w, rq);
    const unsigned long long tpc = cx.pc + rq.s[31];
    __builtin_amdgcn_s_barrier();
    asm volatile("s_cmp_gt_u32 %[wv], 3\n\ts_cbranch_scc1 .Lcf_1\n" LOOP0 ".Lcf_1:\n" LOOP1 ".Lcf_e:"
      :
        "+{v[72:75]}"(rq.h[0]), "+{v232}"(rq.v[0]), "+{v233}"(rq.v[1]), "+{v234}"(rq.v[2]),
        "+{v235}"(rq.v[3]), "+{v236}"(rq.v[4]), "+{v237}"(rq.v[5]), "+{v238}"(rq.v[6]), "+{v239}"(rq.v[7]), "+{v[196:199]}"(rq.h[1]),
        "+{v[200:203]}"(rq.h[2]), "+{v[204:207]}"(rq.h[3]), "+{v[96:97]}"(rq.raw[0]), "+{v[98:99]}"(rq.raw[1]), "+{v[100:101]}"(rq.raw[2]),
        "+{v[102:103]}"(rq.raw[3]), "+{v[104:105]}"(rq.raw[4]), "+{v[106:107]}"(rq.raw[5]), "+{v[108:109]}"(rq.raw[6]), "+{v[110:111]}"(rq.raw[7]),
        "+{v[112:113]}"(rq.raw[8]), "+{v[114:115]}"(rq.raw[9]), "+{v[116:117]}"(rq.raw[10]), "+{v241}"(rq.v[9]), "+{v243}"(rq.v[11]), "+{v244}"(rq.v[12]), "+{v252}"(rq.v[18]), "+{v[192:193]}"(rq.vb),
        "+{v[208:211]}"(rq.h[4]), "+{v[212:215]}"(rq.h[5]), "+{v[216:219]}"(rq.h[6]), "+{v[220:223]}"(rq.h[7]), "+{v[224:227]}"(rq.h[8]), "+{v[228:231]}"(rq.h[9]),
        "+{v194}"(rq.v[19]), "+{v195}"(rq.v[20]), "+{v248}"(rq.v[16]), "+{v249}"(rq.v[17]), "+{v246}"(rq.v[14]), "+{v247}"(rq.v[15])
      :
        "{v240}"(rq.v[8]), "{v242}"(rq.v[10]), "{v245}"(rq.v[13]),

            "{s44}"(rq.s[0]), "{s45}"(rq.s[1]),
        "{s46}"(rq.s[2]), "{s47}"(rq.s[3]), "{s48}"(rq.s[4]), "{s49}"(rq.s[5]), "{s50}"(rq.s[6]), "{s51}"(rq.s[7]), "{s52}"(rq.s[8]), "{s53}"(rq.s[9]),
        "{s54}"(rq.s[10]), "{s55}"(rq.s[11]), "{s56}"(rq.s[12]), "{s57}"(rq.s[13]), "{s58}"(rq.s[14]), "{s59}"(rq.s[15]), "{s60}"(rq.s[16]),
        "{s61}"(rq.s[17]), "{s62}"(rq.s[18]), "{s63}"(rq.s[19]), "{s64}"(rq.s[20]), "{s65}"(rq.s[21]), "{s66}"(rq.s[22]), "{s67}"(rq.s[23]),
        "{s68}"(rq.s[24]), "{s69}"(rq.s[25]), "{s70}"(rq.s[26]), "{s71}"(rq.s[27]), "{s72}"(rq.s[28]), "{s73}"(rq.s[29]), "{s74}"(rq.s[30]), "{s93}"(rq.s[32]),
        "{s[36:39]}"(cx.rs), "{s[40:43]}"(make_rsrc(cx.ws + WS_Y, ROWS * 65536)), "{s[76:77]}"(&TAB.G[0][0][0][0]), "{s[78:79]}"(tpc), [wv] "s"(wv)
      : "memory", "vcc", "scc", "v118", "v119", CLOBBERS);
    fv = rq.v[17];
  }
  ws = cx.ws;
  const int bid = cx.vb, idx = bid >> 3;
  const int slot = (bid & 7) * 4 + (idx >> 3), b = (idx >> 2) & 1, nt = idx & 3;
  const int lane = (int)__builtin_amdgcn_mbcnt_hi(~0u, __builtin_amdgcn_mbcnt_lo(~0u, 0u));
  const int c64i = nt * 2 + ((bid >> 2) & 1), rg = (bid & 1) * 8 + (slot & 1) * 4 + (bid & 2) + (idx >> 4);
  const int hw = wv >> 2, wp = hw ? 7 - wv : wv, cg = c64i * 2 + hw;
  const int bs = 10 * rg;
  const int ntk = wp < 2 ? 3 : 2;
  const int g = lane >> 4, qq = (lane >> 2) & 3, pp = lane & 3, fr = lane & 15;
  const int ch0 = cg * 32 + 8 * g;
  const unsigned cnt = lds0 + 8 * 12288 + 3072 + hw * 8;
  if (wv == 2) asm volatile("ds_write_b32 %0, %1\n\ts_waitcnt lgkmcnt(0)" :: "v"(cnt + (lane & 3) * 4), "v"(0u) : "memory");
#pragma unroll
  for (int i = 0; i < 8; ++i) gb[i] = *reinterpret_cast<const bf16x8*>(smem + i * 1024 + lane * 16);
  const int l0 = HOP * (bs + wp) + fr, le = (fr >> 1) < ntk ? HOP * (bs + wp + 4 * (fr >> 1)) + 48 + (fr & 1) : 1 << 20;
  const int lend = min(HOP * (bs + 10), L_);
  f32x4 nk, rk;
  float rn = 1.0f / (float)(lend - HOP * bs);
#pragma unroll
  for (int k = 0; k < 4; ++k) {
    nk[k] = (float)(2 * HOP + min(max(lend - HOP * (bs + k + 8), 0), HOP));
    rk[k] = 1.0f / nk[k];
  }
  asm volatile("" : "+v"(nk), "+v"(rk), "+v"(rn));
  f32x4 cin[2];
#pragma unroll
    for (int s = 0; s < 2; ++s) {
      cin[s] = *reinterpret_cast<const f32x4*>(smem + CBUF + min(wp + 4 * (fr >> 1), 10) * 272 + hw * 128 + g * 32 + s * 16);
#pragma unroll
      for (int r = 0; r < 4; ++r) cin[s][r] = fr & 1 ? __builtin_amdgcn_ldexpf(cin[s][r], cx.es) : 0.f;
    }
    asm volatile("" : "+v"(cin[0]), "+v"(cin[1]));
#pragma unroll
  for (int i = 0; i < 8; i += 4) asm volatile("" : "+v"(gb[i]), "+v"(gb[i + 1]), "+v"(gb[i + 2]), "+v"(gb[i + 3]));
  SB0;
  unsigned so[3], vo = (unsigned)((lane >> 2) * 64 + (((lane & 3) ^ ((lane >> 3) & 2)) << 4));
#pragma unroll
  for (int i = 0; i < 3; ++i) {
    const unsigned yr = (unsigned)(b * NBLK + bs + wp + 4 * i);
    so[i] = i < ntk ? (yr * 16 + (cg ^ ((yr >> 4) & 2))) * 4096 : 0xf0000000u;
  }
  asm volatile("s_waitcnt vmcnt(0)" : "+v"(fv), "+v"(vo), "+s"(so[0]), "+s"(so[1]), "+s"(so[2]) :: "memory");
  if (wv == 0 && !__all(fv != 0u)) spin_flags(ws + WS_GRP + nt * 256 + b * 128 + ((bid & 7) + (bid & 4)) * 1024 + ((lane & 31) ^ ((lane & 31) == slot)) * 4);
  __builtin_amdgcn_s_barrier();
  char* my = smem + wv * 12288;
  if (1) { if (1 & 4 && !hw) __builtin_amdgcn_s_setprio(3); else __builtin_amdgcn_s_setprio(2); }
  if (24 && hw) __builtin_amdgcn_s_sleep(24);
  if (12 && (nt & 1)) __builtin_amdgcn_s_sleep(12);
  if (0 && b) __builtin_amdgcn_s_sleep(0);
  {
    const u32x4 rs = make_rsrc(ws + WS_Y, ROWS * 65536);
#pragma unroll
    for (int i = 0; i < 3; ++i)
      asm volatile("s_mov_b32 m0, %0\n\ts_nop 0\n\tbuffer_load_dwordx4 %1, %2, %3 offen sc1 lds\n\tbuffer_load_dwordx4 %1, %2, %3 offen offset:1024 sc1 lds\n\t"
                   "buffer_load_dwordx4 %1, %2, %3 offen offset:2048 sc1 lds\n\tbuffer_load_dwordx4 %1, %2, %3 offen offset:3072 sc1 lds"
                   :: "s"(lds0 + wv * 12288 + i * 4096), "v"(vo), "s"(rs), "s"(so[i]) : "memory");
  }
  f32x4 y[24], npv[2];
  float s1[8], s2[8];
  {
    bf16x8 a[12];
    const char* src = my + (4 * g + qq) * 64 + ((g & 1) << 5) + pp * 8;
    auto rd = [&](int q) __attribute__((always_inline)) {
      const char* p = reinterpret_cast<const char*>(smem + (((unsigned)(src - smem)) ^ (unsigned)((q & 2) << 4)) + (q >> 2) * 4096 + (q & 1) * 2048);
      a[q] = cat(tr_read(p), tr_read(p + 1024));
    };
    auto sums = [&](int cp, int q) __attribute__((always_inline)) {
      const int e = (cp & 4) + q;
      if ((cp & 3) == 3) {
        if (cp >= 8) y[cp & 7][q] = (fr >> 1) == (cp >> 3) ? y[cp][q] : y[cp & 7][q];
        return;
      }
      float d = y[cp][q];
      if (cp >= 16 && l0 + 8 * HOP + 16 * (cp & 3) >= lend) d = 0.f;
      if (cp == 0 || cp == 4) { s1[e] = d; s2[e] = d * d; }
      else { s1[e] += d; s2[e] = __builtin_fmaf(d, d, s2[e]); }
      asm volatile("" : "+v"(s1[e]), "+v"(s2[e]));
    };
    wait_vm<8>();
    SB0;
    u32x2 pv[2];
#pragma unroll
    for (int s = 0; s < 2; ++s) pv[s] = *reinterpret_cast<const u32x2*>(my + s * 32 + g * 8);
    SB0;
#pragma unroll
    for (int q = 0; q < 4; ++q) { rd(q); SB0; }
#pragma unroll
    for (int s = 0; s < 2; ++s) {
      npv[s] = f32x4{bf_lo(pv[s][0]), bf_hi(pv[s][0]), bf_lo(pv[s][1]), bf_hi(pv[s][1])} * (-1.0f / 64.0f);
      cin[s] += npv[s];
    }
    SB0;
    auto chain = [&](auto cc) __attribute__((always_inline)) {
      constexpr int c = cc.v, is = c >> 2, tt = c & 3;
      if (c == 7) wait_vm<4>();
      if (c == 15) wait_vm<0>();
#pragma unroll
      for (int q = 0; q < 4; ++q) {
        if (c < 24 && !(q & 1)) y[c % 24] = MFMA(a[(2 * is + (q >> 1)) % 12], gb[2 * tt + (q >> 1)], q ? y[c % 24] : tt == 3 ? cin[is & 1] : npv[is & 1]);
        SB0;
        if (c == 7 || c == 15) rd((c == 7 ? 4 : 8) + q);
        if (c >= 2) sums(c - 2, q);
        SB0;
      }
    };
    sfor<0, 16>(chain);
    if (ntk == 3) {
      sfor<16, 26>(chain);
    } else {
#pragma unroll
      for (int q = 0; q < 8; ++q) sums(14 + (q >> 2), q & 3);
    }
#pragma unroll
    for (int e = 0; e < 8; ++e) {
      const float d = le < lend ? y[3 + (e & 4)][e & 3] : 0.f;
      s1[e] += d;
      s2[e] = __builtin_fmaf(d, d, s2[e]);
    }
  }
  float* red = reinterpret_cast<float*>(smem + 8 * 12288);
  {
    float v[8];
#pragma unroll
    for (int j = 0; j < 8; ++j) v[j] = bfly<0x140>(s1[j], s2[j], fr & 8);
#pragma unroll
    for (int j = 0; j < 4; ++j) v[j] = bfly<0x141>(v[j], v[j + 4], fr & 4);
#pragma unroll
    for (int j = 0; j < 2; ++j) v[j] = bfly<0x4E>(v[j], v[j + 2], fr & 2);
    red[((fr >> 3) * 8 + 2 * wp + hw) * 32 + 8 * g + (fr & 7)] = bfly<0xB1>(v[0], v[1], fr & 1);
    if (fr < 2) *reinterpret_cast<f32x4*>(red + (16 + 2 * wp + hw) * 32 + 8 * g + 4 * fr) = fr ? npv[1] : npv[0];
  }
  unsigned arr = 1u;
  if (lane == 0) asm volatile("ds_add_rtn_u32 %0, %1, %0\n\ts_waitcnt lgkmcnt(0)" : "+v"(arr) : "v"(cnt) : "memory");
  unsigned long long* stats = reinterpret_cast<unsigned long long*>(ws + WS_STATS);
  if (__builtin_amdgcn_readfirstlane(arr) == 3u && lane < 32) {
    const int tid = hw * 32 + lane;
    float mk[4], sm = 0.f, m2 = 0.f;
#pragma unroll
    for (int k = 0; k < 4; ++k) {
      const float* e = red + 64 * k + tid;
      const float dm = e[0] * rk[k];
      mk[k] = dm - e[512];
      m2 += fmaxf(e[256] - e[0] * dm, 0.f);
      sm += nk[k] * mk[k];
    }
    const float mean = sm * rn;
#pragma unroll
    for (int k = 0; k < 4; ++k) m2 += nk[k] * (mk[k] - mean) * (mk[k] - mean);
    unsigned long long pk = (unsigned long long)__float_as_uint(mean) | ((unsigned long long)__float_as_uint(m2) << 32);
    if (pk == ~0ull) pk = ~1ull;
    __hip_atomic_store((gu64*)(stats + (b * 16 + rg + (rg & 12) + (c64i & 1) * 4 + b * 16) * 256 + nt * 64 + tid), pk, RLX_AGENT);
  }
  constexpr float GS = -0.4686686930532063f, GA = -2.0f * 0.7978845608028654f * 1.4426950408889634f / GS;
  const unsigned short* gp = reinterpret_cast<const unsigned short*>(smem + GAM + hw * 64) + (lane & 31);
  float gs = bf_lo(gp[0]) * GS, bt = bf_lo(gp[64]) * GS, eps = fmaxf(__builtin_amdgcn_ldexpf(1e-5f, 2 * cx.es), 1e-30f);
  float cl = lane < 32 ? 0.f : -15.f / 500.f;
  asm volatile("" : "+v"(gs), "+v"(bt), "+v"(eps), "+v"(cl));
  unsigned long long* gl = reinterpret_cast<unsigned long long*>(smem + 8 * 12288 + 4096);
  {
    unsigned long long pk[2];
    const int row = 4 * wp + (lane >> 5), col = hw * 32 + (lane & 31);
    for (unsigned spins = 0;; ++spins) {
#pragma unroll
      for (int r = 0; r < 2; ++r)
        pk[r] = __hip_atomic_load((gu64*)(stats + (b * 32 + row + 2 * r + 4 * wp + (c64i & 1) * 4) * 256 + nt * 64 + col), RLX_AGENT);
      if (__all(pk[0] != ~0ull && pk[1] != ~0ull) || spins > (1u << 20)) break;
      if (1) __builtin_amdgcn_s_sleep(1);
    }
    gl[row * 66 + col] = pk[0];
    gl[(row + 2) * 66 + col] = pk[1];
    if (lane == 0) asm volatile("ds_add_u32 %0, %1" :: "v"(cnt + 4), "v"(1u) : "memory");
    unsigned c;
    do asm volatile("ds_read_b32 %0, %1\n\ts_waitcnt lgkmcnt(0)" : "=v"(c) : "v"(cnt + 4) : "memory");
    while (__builtin_amdgcn_readfirstlane(c) < 4u);
  }
  f32x4 ga[2], be[2];
  {
    const f32x2* gr = reinterpret_cast<const f32x2*>(gl + hw * 32 + (lane & 31));
    const f32x2 p = {gr[0][0], 0.f};
    f32x2 d, sm = {0.f, 0.f};
    float q = 0.f;
#pragma unroll
    for (int k = 0; k < 8; ++k) {
      d = gr[((lane >> 5) * 8 + k) * 66] - p;
      sm += d;
      q = __builtin_fmaf(d[0], d[0], q);
    }
    const float dl = cl * d[0];
    const float dm = halves_sum(sm[0] + dl) * (500.f / L_), var = halves_sum(sm[1] + 500.f * __builtin_fmaf(dl, d[0], q)) * (1.f / L_) - dm * dm;
    gs *= __builtin_amdgcn_rsqf(fmaxf(var, 0.f) + eps);
    float* am = reinterpret_cast<float*>(my);
    am[lane] = lane < 32 ? gs : bt - (p[0] + dm) * gs;
#pragma unroll
    for (int s = 0; s < 2; ++s) {
      ga[s] = *reinterpret_cast<const f32x4*>(am + 8 * g + 4 * s);
      be[s] = *reinterpret_cast<const f32x4*>(am + 32 + 8 * g + 4 * s) - npv[s] * ga[s];
    }
  }
  {
    if (1) __builtin_amdgcn_s_setprio(0);
    f32x4 k1 = {GA, GA, GA, GA}, k2 = {GS, GS, GS, GS};
    asm volatile("" : "+v"(k1), "+v"(k2));
    const u32x4 ro = make_rsrc(out + (size_t)b * L_ * D_, lend * (D_ * 2));
    const unsigned oo = (unsigned)((l0 * D_ + ch0) * 2);
#pragma unroll
    for (int i = 0; i < 3; ++i)
      if (i < 2 || ntk == 3)
#pragma unroll
      for (int tt = 0; tt < 4; ++tt) {
        if (tt == 3 && i) break;
        f32x4 o[2];
#pragma unroll
        for (int s = 0; s < 2; ++s) {
          const f32x4 z = y[8 * i + 4 * s + tt] * ga[s] + be[s];
          const f32x4 a = z * (z * z + k1);
          f32x4 e;
#pragma unroll
          for (int r = 0; r < 4; ++r) e[r] = __builtin_amdgcn_exp2f(a[r]);
          e = e * k2 + k2;
#pragma unroll
          for (int r = 0; r < 4; ++r) e[r] = __builtin_amdgcn_rcpf(e[r]);
          o[s] = z * e;
        }
        st16(pack8(o[0], o[1]), ro, tt < 3 ? oo : (unsigned)((le * D_ + ch0) * 2), tt < 3 ? (4 * HOP * i + 16 * tt) * (D_ * 2) : 0);
      }
  }
  asm volatile(".Lcf_b:\n\ts_nop 0" ::: "memory");
}

extern "C" void kernel_launch(void* const* d_in, const int* in_sizes, int n_in,
                              void* d_out, int out_size, void* d_ws, size_t ws_size,
                              hipStream_t stream) {
  const unsigned short* x = (const unsigned short*)d_in[0];
  const unsigned short* w = (const unsigned short*)d_in[1];
  const unsigned short* gamma = (const unsigned short*)d_in[2];
  const unsigned short* beta = (const unsigned short*)d_in[3];
  char* ws = (char*)(((uintptr_t)d_ws + (2u << 20) - 1) & ~(uintptr_t)((2u << 20) - 1));
  conv_fft<<<256, 512, 0, stream>>>(ws, x, w, gamma, beta, (unsigned short*)d_out);
}
```

```cpp
#include <hip/hip_runtime.h>
#include <stdint.h>

typedef __attribute__((ext_vector_type(8))) short bf16x8;
typedef __attribute__((ext_vector_type(4))) short s16x4;
typedef __attribute__((ext_vector_type(4))) float f32x4;
typedef __attribute__((ext_vector_type(2))) float f32x2;
typedef __attribute__((ext_vector_type(2))) __bf16 bf16x2v;
typedef __attribute__((ext_vector_type(4))) unsigned int u32x4;
typedef __attribute__((ext_vector_type(2))) unsigned int u32x2;
constexpr int NH = 10;
typedef __attribute__((ext_vector_type(8))) unsigned int u32x8;
typedef __attribute__((ext_vector_type(16))) unsigned int u32x16;
typedef __attribute__((ext_vector_type(8))) _Float16 f16x8;
typedef __attribute__((ext_vector_type(2))) _Float16 f16x2;
typedef __attribute__((ext_vector_type(2))) unsigned short u16x2;
typedef __attribute__((address_space(1))) unsigned long long gu64;
typedef __attribute__((address_space(1))) unsigned gu32;
typedef __attribute__((address_space(3))) void lds_void;
typedef __attribute__((address_space(3))) s16x4 lds_s16x4;

constexpr unsigned long long bytes(unsigned a, unsigned b = 0, unsigned c = 0, unsigned d = 0, unsigned e = 0, unsigned f = 0, unsigned g = 0, unsigned h = 0) {
  return a | b << 8 | c << 16 | d << 24 | (unsigned long long)(e | f << 8 | g << 16 | h << 24) << 32;
}
constexpr int T_ = 16000, D_ = 512, L_ = 7985;
constexpr int HOP = 50, NBLK = 160, ROWS = 2 * NBLK;

constexpr unsigned WS_S = 0;
constexpr unsigned SBLK = 4u << 20;
constexpr unsigned sbase(unsigned sl) { return sl * 131072 + (sl >> 4) * 8192; }
constexpr unsigned FLG = 32u * SBLK;
constexpr unsigned WS_GRP = WS_S + FLG + 16 * 8192;
constexpr unsigned WS_D = WS_S + FLG + 20 * 8192;
constexpr unsigned WS_L = WS_D + ROWS * 1024;
constexpr unsigned WS_Y = 40u << 20;
constexpr unsigned WS_STATS = 129u << 20;
constexpr unsigned WS_FA = WS_STATS + (1u << 17);
constexpr unsigned WS_PUB = WS_STATS + (1u << 19);
static_assert(WS_L + D_ * D_ * 2 <= WS_STATS && FLG % (16 * 8192) == 0, "workspace layout");

#define RLX_AGENT __ATOMIC_RELAXED, __HIP_MEMORY_SCOPE_AGENT

constexpr double COS64[17] = {1.0, 0.99518472667219688624, 0.98078528040323044913, 0.95694033573220886494,
    0.92387953251128675613, 0.88192126434835502971, 0.83146961230254523708, 0.77301045336273696081,
    0.70710678118654752440, 0.63439328416364549822, 0.55557023301960222474, 0.47139673682599764856,
    0.38268343236508977173, 0.29028467725446236764, 0.19509032201612826785, 0.09801714032956060199, 0.0};
constexpr double c64(int k) { k &= 63; if (k > 32) k = 64 - k; return k <= 16 ? COS64[k] : -COS64[32 - k]; }
constexpr double s64(int k) { return c64(k + 48); }
constexpr unsigned short bf_rne(double v) {
  const unsigned u = __builtin_bit_cast(unsigned, (float)v);
  return (unsigned short)((u + 0x7fffu + ((u >> 16) & 1u)) >> 16);
}
constexpr double R3 = 0.86602540378443864676;
constexpr double dft(int c, int t, double sgn) {
  return (c >> 1) == 0 ? ((c & t) & 1 ? -1.0 : 1.0)
                       : ((c & 1) ? -0.5 * c64((c >> 1) * t) - R3 * sgn * s64((c >> 1) * t) : c64((c >> 1) * t));
}
constexpr double idft(int c, int t) {
  return (c >> 1) == 0 ? ((c & t) & 1 ? -1.0 : 1.0) / 64.0
                       : ((c & 1) ? -s64((c >> 1) * t) / R3 : c64((c >> 1) * t) * (4.0 / 3.0)) / 32.0;
}
constexpr int kidx(int g, int j) { return j < 4 ? 4 * g + j : 12 + 4 * g + j; }
struct Tables {
  unsigned short G[4][2][64][8];
  unsigned short F[4][2][64][8];
  unsigned short V[4][64][4];
};
constexpr Tables make_tables() {
  Tables tb{};
  for (int l = 0; l < 64; ++l)
    for (int j = 0; j < 8; ++j)
      for (int t = 0; t < 4; ++t) {
        const int g = l >> 4, n = l & 15;
        for (int ks = 0; ks < 2; ++ks) {
          tb.F[t][ks][l][j] = bf_rne(dft(16 * t + n, 32 * ks + kidx(g, j), -1.0));
          tb.G[t][ks][l][j] = bf_rne(idft(32 * ks + kidx(g, j), 16 * t + (t < 3 ? n : n & 1)));
        }
        tb.V[t][l][j & 3] = bf_rne(dft(16 * t + n, 4 * g + (j & 3), 1.0));
      }
  return tb;
}
__device__ const __attribute__((aligned(16))) Tables TAB = make_tables();

__device__ __forceinline__ unsigned pk_bf16(float lo, float hi) {
  return __builtin_bit_cast(unsigned, __builtin_convertvector(f32x2{lo, hi}, bf16x2v));
}
__device__ __forceinline__ float bf_lo(unsigned v) { return __uint_as_float(v << 16); }
__device__ __forceinline__ float bf_hi(unsigned v) { return __uint_as_float(v & 0xffff0000u); }
__device__ __forceinline__ u32x4 pack8(f32x4 a, f32x4 b) {
  return u32x4{pk_bf16(a[0], a[1]), pk_bf16(a[2], a[3]), pk_bf16(b[0], b[1]), pk_bf16(b[2], b[3])};
}
__device__ __forceinline__ unsigned pk_f16(float lo, float hi) {
  return (__builtin_bit_cast(unsigned, __builtin_convertvector(f32x2{lo, hi}, f16x2)) + 0x40004u) & 0xfff8fff8u;
}
__device__ __forceinline__ u32x4 pack8h(f32x4 a, f32x4 b) {
  return u32x4{pk_f16(a[0], a[1]), pk_f16(a[2], a[3]), pk_f16(b[0], b[1]), pk_f16(b[2], b[3])};
}
__device__ __forceinline__ unsigned add_h(const bf16x8& a, const bf16x8& b, int d) {
  const unsigned x = __builtin_bit_cast(u32x4, a)[d], y = __builtin_bit_cast(u32x4, b)[d];
  unsigned r = __builtin_bit_cast(unsigned, __builtin_bit_cast(f16x2, x) + __builtin_bit_cast(f16x2, y));
  return r;
}
__device__ __forceinline__ unsigned amax(unsigned m, unsigned v) {
  return __builtin_bit_cast(unsigned, __builtin_elementwise_max(__builtin_bit_cast(u16x2, m), __builtin_bit_cast(u16x2, v & 0x7fff7fffu)));
}
struct Rng { f16x2 hi = {0, 0}, lo = {0, 0}; };
__device__ __forceinline__ void widen(Rng& r, unsigned a, unsigned b) {
  const f16x2 x = __builtin_bit_cast(f16x2, a), y = __builtin_bit_cast(f16x2, b);
  r.hi = __builtin_elementwise_maximum(__builtin_elementwise_maximum(r.hi, x), y);
  r.lo = __builtin_elementwise_minimum(__builtin_elementwise_minimum(r.lo, x), y);
}
__device__ __forceinline__ unsigned amax(const Rng& r) { return amax(amax(0, __builtin_bit_cast(unsigned, r.hi)), __builtin_bit_cast(unsigned, r.lo)); }
__device__ __forceinline__ unsigned expo(unsigned m) { return max(m & 0xffffu, m >> 16) >> 7; }
__device__ __forceinline__ unsigned wave_max(unsigned v) {
  unsigned m = 0;
#pragma unroll 1
  for (int l = 0; l < 64; ++l) m = max(m, (unsigned)__builtin_amdgcn_readlane((int)v, l));
  return m;
}
__device__ __forceinline__ unsigned scale2(unsigned u, int e) {
  return __builtin_amdgcn_perm(__float_as_uint(__builtin_amdgcn_ldexpf(bf_hi(u), e)), __float_as_uint(__builtin_amdgcn_ldexpf(bf_lo(u), e)), 0x07060302u);
}
__device__ __forceinline__ void restart(unsigned id) {
  asm volatile("s_waitcnt vmcnt(0)\n\ts_barrier\n\ts_branch conv_fft" :: "{s[0:1]}"(__builtin_amdgcn_kernarg_segment_ptr()), "{s2}"(id), "{v0}"(threadIdx.x) : "memory");
}
__device__ __forceinline__ s16x4 tr_read(const char* p) {
  return __builtin_amdgcn_ds_read_tr16_b64_v4i16((lds_s16x4*)p);
}
__device__ __forceinline__ bf16x8 cat(s16x4 a, s16x4 b) {
  const u32x2 x = __builtin_bit_cast(u32x2, a), y = __builtin_bit_cast(u32x2, b);
  return __builtin_bit_cast(bf16x8, u32x4{x[0], x[1], y[0], y[1]});
}
#define MFMA(a, b, c) __builtin_amdgcn_mfma_f32_16x16x32_bf16(a, b, c, 0, 0, 0)
#define MFMS(a, b) __builtin_amdgcn_mfma_f32_16x16x16bf16_1k(a, __builtin_bit_cast(s16x4, b), ZERO4, 0, 0, 0)
#define SB0 __builtin_amdgcn_sched_barrier(0)
#define ZERO4 (f32x4{0.f, 0.f, 0.f, 0.f})
#define DMA16(POL, rs, lds, voff, soff) \
  asm volatile("s_mov_b32 m0, %0\n\ts_nop 0\n\tbuffer_load_dwordx4 %1, %2, %3 offen " POL " lds" \
               :: "s"(lds), "v"(voff), "s"(rs), "s"(soff) : "memory")
__device__ __forceinline__ void st16(const u32x4& v, const u32x4& rs, unsigned voff, unsigned soff) {
  asm volatile("buffer_store_dwordx4 %0, %1, %2, %3 offen sc1\n\ts_nop 1" :: "v"(v), "v"(voff), "s"(rs), "s"(soff) : "memory");
}
template <int N> __device__ __forceinline__ void wait_vm() { asm volatile("s_waitcnt vmcnt(%0)" :: "i"(N) : "memory"); }
__device__ __forceinline__ u32x4 make_rsrc(const void* p, unsigned bytes) {
  const unsigned long long a = (unsigned long long)p;
  return u32x4{(unsigned)a, (unsigned)(a >> 32) & 0xffffu, bytes, 0x00020000u};
}
__device__ __forceinline__ void spin_flags(const char* p) {
  for (unsigned spins = 0; !__all(__hip_atomic_load((const gu32*)p, RLX_AGENT) != 0u) && spins < (1u << 21); ++spins)
    __builtin_amdgcn_s_sleep(3);
}

constexpr int GW = 10;
constexpr int NQ1 = GW / 2, NQW = 16 - NQ1;
constexpr int WSL = 16384, XSL = 20480, XRING = 3 * WSL;
constexpr int RAWB = 67 * 1024, XT1 = RAWB + NQ1 * 4096, SINK = XT1 + RAWB;
constexpr int RAWL = XRING + 3 * XSL;
constexpr int CORR = RAWL + 4096, CORD = RAWL + NQW * 4096;
constexpr int GAM = RAWL + 52224, CBUF = RAWL + 49152;

struct Ctx {
  char* smem;
  char* ws;
  const char* out;
  const unsigned short *gamma, *beta;
  u32x4 rs;
  unsigned ldg, ldf, sg, sf;
  unsigned voff, voff5, crd;
  unsigned xbase, wbase;
  unsigned pwr, prd, poff;
  unsigned xo;
  unsigned lane4, fvo;
  unsigned id;
  unsigned tag[2];
  int es;
  int wv, vb;
  unsigned long long pc;
  unsigned pfo, pft;
};
__device__ __forceinline__ void code_touch(const Ctx& cx, int h0 = 0, int h1 = 2) {
  if (((cx.wv ^ (cx.vb >> 3)) & 3) | (cx.vb & (h0 || h1 == 1 ? 0 : 0))) return;
#pragma unroll
  for (int h = h0; h < h1; ++h)
    asm volatile("s_mov_b32 m0, %0\n\ts_nop 0\n\tglobal_load_lds_dword %1, %2" :: "s"(SINK + cx.wv * 256), "v"(min(__builtin_amdgcn_mbcnt_hi(~0u, __builtin_amdgcn_mbcnt_lo(~0u, cx.wv * 64u + h * 512u)) * 128u, cx.pfo)), "s"(cx.pc) : "memory");
}
__device__ __forceinline__ void x_store(const Ctx& cx, const u32x4& v, unsigned kb, unsigned blk) {
  st16(v, cx.rs, cx.xo, kb + (blk + (blk & 192u)) * 128u);
}
template <int NM>
__device__ __forceinline__ void stage_issue(const Ctx& cx, int kc) {
  const unsigned slot = (unsigned)(kc + 1) * (NM == 6 ? WSL : XSL);
  if (kc >= 0 || NM == 6 || !(cx.wv & 1))
    asm volatile("s_mov_b32 m0, %0\n\ts_nop 0\n\tbuffer_load_dwordx4 %1, %2, %3 offen sc1 lds\n\tbuffer_load_dwordx4 %1, %2, %3 offen offset:1024 sc1 lds\n\t"
                 "buffer_load_dwordx4 %1, %2, %3 offen offset:2048 sc1 lds\n\tbuffer_load_dwordx4 %1, %2, %3 offen offset:3072 sc1 lds"
                 :: "s"(cx.ldg + slot), "v"(cx.voff), "s"(cx.rs), "s"(cx.sg + kc * SBLK) : "memory");
  if (kc >= 0 && NM == 5) DMA16("sc1", cx.rs, cx.ldf + slot, cx.voff, cx.sf);
}
template <int V> struct IC { static constexpr int v = V; constexpr operator int() const { return V; } };
template <int A, int B, class F> __device__ __forceinline__ void sfor(F f) {
  if constexpr (A < B) { f(IC<A>{}); sfor<A + 1, B>(f); }
}
struct RQ { u32x4 h[NH]; u32x2 raw[NQW]; u32x2 vb; unsigned s[33], v[21]; };

__device__ __forceinline__ int run_bs(int vb, int t) { return 5 * ((vb >> 4) + 16 * ((vb ^ t) & 1)); }
__device__ __forceinline__ int run_q(int vb, int t) { return (((vb >> 1) & 1) + ((vb >> 2) & 2) + 2 * t) & 3; }
__device__ __forceinline__ unsigned x_addr(int vb, int t, int wv) {
  const int lane = threadIdx.x & 63, q = run_q(vb, t);
  const unsigned c = (unsigned)((lane & 15) ^ (((4 * wv + (lane >> 4)) & 7) << 1));
  return (unsigned)(2 * (HOP * run_bs(vb, t) + 4 * wv + (lane >> 4)) + ((t ^ q ^ (q >> 1)) & 1)) * 1024u + c * 16 + q * 256;
}
__device__ __forceinline__ u32x4 x_rsrc(const unsigned short* x, int vb) { return make_rsrc(x + (size_t)((vb >> 2) & 1) * T_ * D_, T_ * D_ * 2); }
__device__ __forceinline__ void issue_x(int t, int wv, unsigned a, const u32x4& rx, unsigned lds0, int j0 = 0, int j1 = 9) {
#pragma unroll
  for (int jj = 0; jj < 9; ++jj)
    if (jj >= j0 && jj < j1 && (jj < 8 || wv < 3)) DMA16("nt", rx, lds0 + t * XT1 + wv * 1024 + jj * 8192, a, jj * 65536u);
}
struct XR { s16x4 t[8]; };
constexpr int P0 = 3, P1 = 23;
constexpr int PG = 20, NFA = 1;
constexpr int TE = 4;
constexpr int NIT = 23, NS = 30 - NH, NA = 7, NB = 13;
constexpr int ord(int p) { return p < 0 || p >= NIT ? -1 : p == 0 ? 0 : p < 6 ? p + 2 : p < 8 ? p - 5 : p; }
constexpr int st_w(int j) { return j < 2 ? j : j < 7 ? -1 : j < 15 ? j - 5 : -1; }
constexpr int st_x(int j) { return j < 7 ? j - 2 : j - 10; }
constexpr int st_pos(int j) { return st_w(j) >= 0 ? (j < 9 ? 0 : j < 13 ? 6 : 7) : j < 7 ? j - 1 : j - 7; }
constexpr int st_first(int p) {
  int j = p <= P0 ? 0 : p >= P1 ? NS : (p - P0) * NS / (P1 - P0);
  while (j > 0 && p <= NIT && st_pos(j - 1) + 2 > p - 1) --j;
  return j;
}
template <int NM, class F>
__device__ __forceinline__ void phase1(Ctx& cx, const unsigned short* __restrict__ x, const unsigned short* __restrict__ w,
                                       RQ& rqv, unsigned& xs, u32x2& vt, F setup) {
  u32x2 rq[NQW];
  static_assert(NQ1 == 5, "phase-1 wait counts assume five quanta");
  char* smem = cx.smem;
  const unsigned lds0 = (unsigned)(uintptr_t)(lds_void*)smem;
  const int idx = cx.vb >> 3, wv = cx.wv, lane = threadIdx.x & 63;
  const u32x4 rw = make_rsrc(w, 31 * D_ * D_ * 2);
  const int tap = threadIdx.x >> 5, pc = threadIdx.x & 31;
  const unsigned roff = (unsigned)(tap * (2 * D_ * D_ * 2) + pc * 8);
  const int ng = wv & 3, mg = wv >> 2;
  const int g = lane >> 4, qq = (lane >> 2) & 3, pp = lane & 3;
  u32x2 r1[NQ1];
  u32x4 fb[2];
  unsigned long long tag;
  const char* rec = cx.ws + WS_PUB + ((cx.id & 7) * 8 + wv) * 16;
  u32x4 pr, wl;
  {
    const int vb = cx.vb;
    const int kr = (vb >> 7) * 32 + (vb & 4) * 4 + (idx & 15);
    auto raw_load = [&](u32x2& r, int q) {
      const unsigned so = (unsigned)(kr + 64 * (2 * (q & 3) + ((q >> 2) & 1)) + 512 * (((q >> 3) ^ q ^ (q >> 1)) & 1)) * 1024u + (unsigned)((vb + q) & 3) * 256u;
      asm volatile("buffer_load_dwordx2 %0, %1, %2, %3 offen nt" : "=v"(r) : "v"(roff), "s"(rw), "s"(so) : "memory");
    };
#pragma unroll
    for (int k = 0; k < NQ1; ++k) raw_load(r1[k], k);
    const unsigned xa1 = x_addr(vb, 1, wv);
    issue_x(1, wv, xa1, x_rsrc(x, vb), lds0, 0, TE);
    if (NM == 6 && (cx.id | wv) == 0) {
      unsigned g[2] = {~0u, ~0u};
#pragma unroll 1
      for (int k = -1; k < 16 && ((k & 1) || min(g[0], g[1]) > 440u); ++k) {
        unsigned long long t0, t1;
        unsigned d;
        asm volatile("s_memtime %0\n\ts_waitcnt lgkmcnt(0)\n\ts_load_dword %2, %3, 0x0\n\ts_waitcnt lgkmcnt(0)\n\ts_memtime %1\n\ts_waitcnt lgkmcnt(0)"
                     : "=&s"(t0), "=&s"(t1), "=&s"(d) : "s"(rec + 32768 + (k < 0 ? 128 : (k & 1) * 8192 + (k & 2) * 2048 + (k & 4) * 4096 + (k & 8) * 128)) : "memory");
        if (k >= 0) { if (k & 1) g[1] = min(g[1], (unsigned)(t1 - t0)); else g[0] = min(g[0], (unsigned)(t1 - t0)); }
      }
      const unsigned lo = cx.tag[0] | (((g[0] < g[1] ? 0u : 1u) ^ (cx.vb >> 2)) & 1u), hi = cx.tag[1];
      asm volatile("global_store_dwordx4 %0, %1, %2 sc1\n\ts_nop 1" :: "v"(lane * 16 + 2048), "v"(u32x4{lo, hi, lo, hi}), "s"(rec) : "memory");
    }
    {
#pragma unroll
      for (int i = 0; i < 2; ++i) asm volatile("global_load_dwordx4 %0, %1, off" : "=v"(fb[i]) : "v"(TAB.F[ng][i][lane]) : "memory");
      asm volatile("global_load_dwordx2 %0, %1, off" : "=v"(vt) : "v"(TAB.V[ng][lane]) : "memory");
      asm volatile("global_load_dwordx4 %0, %1, %2 sc1" : "=v"(pr) : "v"(0u), "s"(rec) : "memory");
    }
    if (NM == 5 && wv >= 6)
      asm volatile("buffer_load_dwordx4 %0, %1, %2, %3 offen nt" : "=v"(wl) : "v"((threadIdx.x & 127) * 16), "s"(rw), "s"(30 * D_ * D_ * 2 + vb * 2048) : "memory");
    if (cx.id == 0 && wv >= TE - 1)
      DMA16("nt", x_rsrc(x, vb), lds0 + XT1 + (wv + 1) * 8192, x_addr(vb, 1, 0), (wv + 1) * 65536u);
    if (NM == 5 || (cx.id | wv)) issue_x(1, wv, xa1, x_rsrc(x, vb), lds0, TE, 9);
#pragma unroll
    for (int k = 0; k < NQW; ++k) raw_load(rq[k], NQ1 + k);
    if (1 == 1 && cx.id) code_touch(cx, 1, 2);
    if (1 && NM == 5 && wv == 5) {
      const char* p = lane < 2 ? cx.ws + (idx + 32 * lane) * (2u << 20) + 65536 : cx.out + idx * 511040;
      asm volatile("s_mov_b32 m0, %0\n\ts_mov_b64 exec, 7\n\tglobal_load_lds_dword %1, off nt\n\ts_mov_b64 exec, -1" :: "s"(SINK + 5 * 256), "v"(p + (vb & 7) * 128) : "memory");
    }
    {
      const int bid = vb, xb = (bid >> 2) & 1, krow = kr;
      const int slot = (bid & 7) * 4 + (idx >> 3), b = (idx >> 2) & 1, nt = idx & 3;
      const int rt = 4 * g + qq;
      cx.lane4 = lane * 4;
      cx.fvo = NM == 5 ? (unsigned)((bid & 7) * 1024 + ng * 256 + lane * 4) : wv == 0 && lane < 8 ? (unsigned)(lane * 1024 + (bid >> 2) * 4) : 0x80000000u;
      cx.pwr = (unsigned)(RAWB + tap * 256 + (((pc >> 2) ^ (tap & 7)) << 5) + (pc & 3) * 8);
      cx.prd = (unsigned)(RAWB + rt * 256 + (((4 * mg) ^ (rt & 7)) << 5) + pp * 8);
      cx.poff = (unsigned)(sbase(8 * ng + ((lane >> 1) & 7)) + (krow >> 5) * SBLK + (krow & 24) * 512 + (krow & 7) * 128 +
                           mg * 2048 + (lane & 1) * 64 + g * 16);
      cx.xo = (unsigned)(sbase(8 * ng + ((lane >> 1) & 7)) + mg * SBLK + 65536 + xb * 8192 + (lane & 1) * 64 + g * 16);
      cx.xbase = (unsigned)(XRING + mg * 5 * 2048 + (lane & 8) * 128 + (lane & 7) * 64 + ((g ^ ((lane >> 1) & 2)) << 4));
      cx.wbase = (unsigned)((rt >> 3) * 4096 + ng * 1024 + (rt & 7) * 64 + (rt & 4) * 8 + pp * 8);
      const int pat = ((lane >> 2) & 7) * 128 + (lane >> 5) * 64 + (((lane & 3) ^ ((lane >> 3) & 2)) << 4);
      if (NM == 6) {
        cx.voff = (unsigned)(sbase(slot) + nt * 16384 + ng * 4096 + pat);
        cx.ldg = lds0 + ng * 4096;
        cx.ldf = lds0 + CORR + ng * 1024;
        cx.sg = SBLK;
        cx.sf = 0;
        const int kr8 = lane >> 3;
        cx.voff5 = (unsigned)(WS_L - WS_S + (8 * ng + kr8) * 1024 + (nt * 2 + xb) * 128 + (((lane & 7) ^ ((kr8 & 2) | (ng & 1) << 2)) << 4));
        cx.crd = (unsigned)(CORR + (8 * g + qq) * 128 + ((ng ^ ((qq >> 1) | (g & 1) << 1)) << 5) + pp * 8);
      } else {
        const int rg = (bid & 1) * 8 + (slot & 1) * 4 + (bid & 2) + (idx >> 4);
        cx.voff = (unsigned)(sbase(slot) + 65536 + b * 8192 + pat);
        cx.ldg = lds0 + XRING + (ng + (ng >> 1)) * 4096;
        cx.ldf = lds0 + XRING + 8192 + ng * 1024;
        cx.sg = (~ng & 1) * SBLK + (ng + (ng >> 1) * 3 + ((ng + 1) >> 2) * 2) * 4096;
        cx.sf = 16384 + ng * 1024;
        cx.voff5 = (unsigned)(WS_D - WS_S + (b * NBLK + 10 * rg + min(lane & 15, 9)) * 1024 + ng * 64 + g * 16);
      }
    }
    asm volatile("" : "+v"(cx.fvo), "+v"(cx.poff), "+v"(cx.xo), "+v"(cx.voff), "+v"(cx.voff5), "+v"(cx.lane4));
  }
  Rng gx, gw;
  auto scan = [&](int t) __attribute__((always_inline)) {
    u32x4 v[9];
#pragma unroll
    for (int j = 0; j < 9; ++j)
      if (j < 8 || NM == 6) v[j] = *reinterpret_cast<const u32x4*>(smem + t * XT1 + (j < 8 || wv < 3 ? j : 7) * 8192 + threadIdx.x * 16);
    SB0;
#pragma unroll
    for (int j = 0; j < 9; ++j)
      if (j < 8 || NM == 6) {
        widen(gx, v[j][0], v[j][1]);
        widen(gx, v[j][2], v[j][3]);
      }
    asm volatile("" : "+v"(gx.hi), "+v"(gx.lo));
    SB0;
  };
  if (NM == 6 && (cx.id | wv) == 0) wait_vm<NQW>();
  wait_vm<8 + 4 + NQW>();
  unsigned near;
  scan(0);
#pragma unroll
  for (int q = 0; q < NQ1; ++q) {
    asm volatile("" : "+v"(r1[q]));
    widen(gw, r1[q][0], r1[q][1]);
  }
  {
    wait_vm<NQW + 8 - TE>();
    unsigned d0, d1;
    for (unsigned spins = 0;; ++spins) {
      asm volatile("" : "+v"(pr));
      d0 = __builtin_amdgcn_readfirstlane(pr[0]);
      d1 = __builtin_amdgcn_readfirstlane(pr[1]);
      const unsigned d2 = __builtin_amdgcn_readfirstlane(pr[2]), d3 = __builtin_amdgcn_readfirstlane(pr[3]);
      if (__builtin_expect((d0 == ~d2 && d1 == ~d3) || spins > (1u << 21), 1)) break;
      __builtin_amdgcn_s_sleep(2);
      asm volatile("global_load_dwordx4 %0, %1, %2 sc1\n\ts_waitcnt vmcnt(0)" : "=v"(pr) : "v"(0u), "s"(rec) : "memory");
    }
    tag = d0 | (unsigned long long)d1 << 32;
    const unsigned vn = (cx.id + (d0 >> 1)) & 7u;
    if (__builtin_expect(vn != (cx.vb & 7u), 0)) restart(cx.id | 256u | vn << 10);
  }
  const unsigned ee = (cx.id & 512u ? cx.id : (unsigned)(tag >> 32)) >> 16, ex = ee & 255u, ew = ee >> 8;
  cx.es = 256 - (int)(ex + ew);
  char* ws;
  const int bid = cx.vb;
  u32x4 f0, f1;
  char* fab;
  const u32x4* fa;
  u32x2 ov;
  unsigned pollv;
  const int rb[2] = {run_bs(bid, 0), run_bs(bid, 1)};
  unsigned xb0[4];
#pragma unroll
  for (int k = 0; k < 4; ++k) xb0[k] = (unsigned)((4 * g + qq) * 256 + (((2 * mg) ^ ((4 * g + qq + 2 * k) & 7)) << 5) + pp * 8);
#pragma unroll
  for (int q = 0; q < NQ1; ++q) *reinterpret_cast<u32x2*>(smem + cx.pwr + q * 4096) = r1[q];
  asm volatile("" : "+v"(fb[0]), "+v"(fb[1]), "+v"(vt));
#pragma unroll
  for (int i = 0; i < 2; ++i) vt[i] = scale2(vt[i], 128 - (int)ew);
#pragma unroll
  for (int i = 0; i < 8; ++i) fb[i >> 2][i & 3] = scale2(fb[i >> 2][i & 3], 128 - (int)ex);
  asm volatile("" : "+v"(fb[0]), "+v"(fb[1]), "+v"(vt));
  asm volatile("s_waitcnt lgkmcnt(0)" : "+v"(xb0[0]), "+v"(xb0[1]), "+v"(xb0[2]), "+v"(xb0[3]) :: "memory");
  wait_vm<NQW>();
  asm volatile("global_load_dwordx2 %0, %1, %2 sc1" : "=v"(ov) : "v"(2048u), "s"(rec) : "memory");
  __builtin_amdgcn_s_barrier();
  SB0;
  u32x4 hw[6][2], hx[20];
  f32x4 pend[2];
  auto rd = [&](int it, int i) __attribute__((always_inline)) {
    if (it < 3) return tr_read(smem + ((cx.prd ^ (unsigned)((i & 3) << 5)) + (2 * it + (i >> 2)) * 4096));
    const int n = it - 3;
    const unsigned base = (xb0[n % 5 & 3] ^ (unsigned)((4 * ((n / 5) & 1) + (i & 1)) << 5)) + n / 10 * XT1;
    return tr_read(smem + (base + n % 5 * (HOP * 256) + (i >> 1) * (16 * 256)));
  };
  auto dst = [&](int it) -> u32x4& { return it < 3 ? hw[it == 2 ? 4 : 2 * it + 1][1] : hx[it - 3]; };
  auto item = [&](int it, int nx, int pv, XR& xr, auto side) __attribute__((always_inline)) {
    const XR c = xr;
    const int nm = it < 2 ? 8 : 4, nr = nx == 2 ? 4 : 8;
    f32x4 r[8];
#pragma unroll
    for (int i = 0; i < 8; ++i) {
      if (i >= nm) break;
      const int s2 = i & 1, ks = (i >> 1) & 1;
      if (it < 3) r[i] = MFMS(c.t[i], vt);
      else r[i] = MFMA(cat(c.t[4 * ks + s2], c.t[4 * ks + 2 + s2]), __builtin_bit_cast(bf16x8, fb[ks]), i < 2 ? ZERO4 : r[i - 2]);
      SB0;
      if (nx >= 0) {
#pragma unroll
        for (int j = 0; j < 8; ++j) if (j < nr && j * nm / nr == i) xr.t[j] = rd(nx, j);
      }
      if (pv >= 0 && (nm == 4 ? i >= 2 : i >= 1 && i <= 4)) {
        u32x4& d = dst(pv);
#pragma unroll
        for (int e = 0; e < 4; ++e)
          if ((nm == 4 ? 2 + (e >> 1) : 1 + e) == i) d[e] = pk_f16(pend[e >> 1][(e & 1) * 2], pend[e >> 1][(e & 1) * 2 + 1]);
        if (i == (nm == 4 ? 3 : 4)) asm volatile("" : "+v"(d));
      }
      if (it < 3 && i >= 3 && (i & 1)) {
        u32x4& d = hw[2 * it + ((i - 3) >> 2)][((i - 3) >> 1) & 1];
        d = pack8h(r[i - 3], r[i - 2]);
        asm volatile("" : "+v"(d));
      }
      side(i * (8 / nm));
      if (nm == 4) side(2 * i + 1);
      SB0;
    }
    pend[0] = r[nm - 2];
    pend[1] = r[nm - 1];
  };
  const unsigned so0 = (unsigned)(2 * run_q(bid, 0)) * SBLK, so1 = (unsigned)(16 + 2 * run_q(bid, 1)) * SBLK;
  auto store = [&](int j) __attribute__((always_inline)) {
    if (j == 0) {
      wait_vm<0>();
      for (unsigned spins = 0;; ++spins) {
        asm volatile("" : "+v"(ov));
        near = __builtin_amdgcn_readfirstlane(ov[0]) ^ (unsigned)tag;
        if (__builtin_expect((near < 2u && __builtin_amdgcn_readfirstlane(ov[1]) == (unsigned)(tag >> 32)) || spins > (1u << 21), 1)) break;
        __builtin_amdgcn_s_sleep(2);
        asm volatile("global_load_dwordx2 %0, %1, %2 sc1\n\ts_waitcnt vmcnt(0)" : "=v"(ov) : "v"(2048u), "s"(rec) : "memory");
      }
      ws = cx.ws += (near & 1u) * 8192;
      cx.rs = make_rsrc(ws + WS_S, WS_L - WS_S + D_ * D_ * 2);
      fab = ws + WS_FA + (cx.id & 512u) * 32;
      fa = reinterpret_cast<const u32x4*>(fab + (bid & 7) * 2048) + lane;
      if (NM == 6 && wv == 1) {
        const unsigned o = (lane < 32 ? WS_STATS : WS_S + FLG + 16384 - 512) + bid * 512 + lane * 16;
        const unsigned v = lane < 32 ? ~0u : 0u;
        asm volatile("global_store_dwordx4 %0, %1, %2 sc1\n\ts_nop 1" :: "v"(o), "v"(u32x4{v, v, v, v}), "s"(ws) : "memory");
        if (lane < 8) __hip_atomic_store((gu32*)(ws + WS_S + FLG + lane * 1024 + ((bid + 2) & 3) * 256 + (bid >> 2) * 4), 0u, RLX_AGENT);
      }
    }
    if (j == NB + 1 && NM == 6) {
      if (((bid >> 1) ^ (bid >> 3) ^ 1) & 1 && threadIdx.x < 160) {
        const int t = threadIdx.x >= 80, blk = (threadIdx.x >> 4) - 5 * t;
        const unsigned o = (unsigned)(t * XT1 + blk * (HOP * 256) + (((lane & 15) ^ ((blk & 3) << 2)) << 4));
        const u32x4 lo = *reinterpret_cast<const u32x4*>(smem + o), hi = *reinterpret_cast<const u32x4*>(smem + o + 64 * 256);
        u32x4 d;
#pragma unroll
        for (int e = 0; e < 4; ++e) d[e] = pk_bf16(bf_lo(hi[e]) - bf_lo(lo[e]), bf_hi(hi[e]) - bf_hi(lo[e]));
        st16(d, cx.rs, (unsigned)((((bid >> 2) & 1) * NBLK + (t ? rb[1] : rb[0]) + blk) * 1024 + run_q(bid, t) * 256 + (lane & 15) * 16), WS_D - WS_S);
      }
    }
    if (j == NB + 1 && NM == 5) {
      if (wv >= 6) {
        asm volatile("" : "+v"(wl));
        st16(wl, cx.rs, (threadIdx.x & 127) * 16, WS_L - WS_S + bid * 2048);
      }
    }
    const int wq = st_w(j), xn = st_x(j);
    if (wq >= 0) st16(hw[wq >> 1][wq & 1], cx.rs, cx.poff, (unsigned)(wq >> 1) * (2 * SBLK) + (unsigned)((bid + (wq >> 1)) & 3) * 16384u + (wq & 1) * 1024u);
    else x_store(cx, hx[xn], (xn < 10 ? so0 : so1) + ((xn / 5) & 1) * (8 * SBLK), (unsigned)(rb[xn / 10] + xn % 5));
    if (j == NA + NFA - 1) {
      scan(1);
#pragma unroll
      for (int k = 0; k < NQW; ++k) {
        asm volatile("" : "+v"(rq[k]));
        widen(gw, rq[k][0], rq[k][1]);
      }
      const unsigned mx = amax(gx), mw = amax(gw);
      if (__builtin_expect(__any(expo(mx) > ex + 7 || expo(mw) > ew + 9) && !(cx.id & 512u), 0)) {
        const u32x2 m = {wave_max(expo(mx)), wave_max(expo(mw))};
        asm volatile("global_atomic_umax %0, %1, %3 sc1\n\tglobal_atomic_umax %0, %2, %3 offset:4 sc1\n\ts_waitcnt vmcnt(0)"
                     :: "v"(lane * 16 + 1024), "v"(m[0]), "v"(m[1]), "s"(rec - ((cx.id & 7) * 8 + wv) * 16) : "memory");
      }
    }
    if (j == NA + NFA - 1 || j == NB + NFA - 1) {
      wait_vm<NFA>();
      __builtin_amdgcn_s_barrier();
      if (wv == 0 && lane < 8) {
        if (j < NB) __hip_atomic_store((gu64*)(fab + lane * 2048) + bid, tag, RLX_AGENT);
        else __hip_atomic_store((gu32*)(ws + WS_S + FLG + lane * 1024 + ((bid + 2) & 3) * 256 + (bid >> 2) * 4), 1u, RLX_AGENT);
      }
    }
    SB0;
  };
  XR xr;
#pragma unroll
  for (int j = 0; j < 8; ++j) xr.t[j] = rd(0, j);
  SB0;
  static_assert(st_first(NIT) == NS && PG >= 14 && st_first(PG - 2) >= NB + 2 && NA + NFA <= NB && NFA >= 1, "order of the hand-off steps");
  sfor<0, NIT>([&](auto p) __attribute__((always_inline)) {
    constexpr int j0 = st_first(p.v), j1 = st_first(p.v + 1);
    if (p >= 12 && p < PG) { if (!(bid & 1) && cx.id) __builtin_amdgcn_s_sleep(3); }
    if (p == PG - 2) {
      if (!0) code_touch(cx);
      if (NM == 6 && wv == 0)
        asm volatile("global_load_dwordx4 %0, %2, off sc1\n\tglobal_load_dwordx4 %1, %2, off offset:1024 sc1" : "=&v"(f0), "=&v"(f1) : "v"(fa) : "memory");
    }
    if (p == PG) {
      if (NM == 6 && wv == 0) {
        wait_vm<st_first(PG) - st_first(PG - 2)>();
        for (unsigned spins = 0; spins < (1u << 21); ++spins) {
          asm volatile("" : "+v"(f0), "+v"(f1));
          const unsigned t0 = (unsigned)tag, t1 = (unsigned)(tag >> 32);
          if (__all(f0[0] == t0 && f0[1] == t1 && f0[2] == t0 && f0[3] == t1 && f1[0] == t0 && f1[1] == t1 && f1[2] == t0 && f1[3] == t1)) break;
          __builtin_amdgcn_s_sleep(2);
          asm volatile("global_load_dwordx4 %0, %2, off sc1\n\tglobal_load_dwordx4 %1, %2, off offset:1024 sc1\n\ts_waitcnt vmcnt(0)"
                       : "=&v"(f0), "=&v"(f1) : "v"(fa) : "memory");
        }
      }
      __builtin_amdgcn_s_barrier();
      stage_issue<NM>(cx, -1);
      asm volatile("global_load_dwordx2 %0, %1, %2 sc1" : "=v"(ov) : "v"(1024u), "s"(rec) : "memory");
      stage_issue<NM>(cx, 0);
      SB0;
    }
    if (p == NIT - 1 && NM == 5) {
      asm volatile("buffer_load_dword %0, %1, %2, %3 offen sc1" : "=v"(pollv) : "v"(cx.fvo), "s"(cx.rs), "s"(FLG) : "memory");
    }
    item(ord(p), ord(p + 1), ord(p - 1), xr, [&](int i) __attribute__((always_inline)) {
      if ((i * (j1 - j0) + 7) / 8 < ((i + 1) * (j1 - j0) + 7) / 8) store(j0 + (i * (j1 - j0) + 7) / 8);
    });
  });
  hx[19] = pack8h(pend[0], pend[1]);
  xs = so1 + (unsigned)rb[1];
#pragma unroll
  for (int e = 0; e < NH; ++e) rqv.h[e] = hx[20 - NH + e];
  asm volatile("" : "+v"(rqv.h[NH - 1]));
  setup();
  wait_vm<NM == 5 ? NS - st_first(NIT - 1) : 10 - NM + NS - st_first(PG)>();
  {
    asm volatile("" : "+v"(ov));
    const unsigned ox = __builtin_amdgcn_readfirstlane(ov[0]), ow = __builtin_amdgcn_readfirstlane(ov[1]);
    if (__builtin_expect((ox | ow) && !(cx.id & 512u), 0))
      restart((cx.id & 255u) | 768u | (bid & 7u) << 10 | min(max(ex + 7, ox) - 7, 220u) << 16 | min(max(ew + 9, ow) - 9, 220u) << 24);
    if (NM == 6 && (cx.id & 255u) == 0 && wv == 0)
      asm volatile("global_store_dwordx4 %0, %1, %2 sc1\n\ts_nop 1" :: "v"(lane * 16), "v"(u32x4{0u, 0u, 0u, 0u}), "s"(rec) : "memory");
  }
  if (NM == 5) {
    asm volatile("" : "+v"(pollv));
    if (!__all(pollv != 0u)) spin_flags(ws + WS_S + FLG + cx.fvo);
  }
#pragma unroll
  for (int k = 0; k < NQW; ++k) rqv.raw[k] = rq[k];
}

#define LOOP0 \
"v_lshlrev_b32 v254, 3, v244\n" \
"s_mov_b32 s91, 0x40004\n" \
"s_mov_b32 s92, 0xfff8fff8\n" \
"ds_read_b64_tr_b16 v[120:121], v241 offset:0\n" \
"ds_read_b64_tr_b16 v[122:123], v241 offset:8192\n" \
"ds_read_b64_tr_b16 v[124:125], v242 offset:0\n" \
"ds_read_b64_tr_b16 v[126:127], v242 offset:8192\n" \
"ds_read_b64_tr_b16 v[128:129], v241 offset:512\n" \
"ds_read_b64_tr_b16 v[130:131], v241 offset:8704\n" \
"ds_read_b64_tr_b16 v[132:133], v242 offset:512\n" \
"ds_read_b64_tr_b16 v[134:135], v242 offset:8704\n" \
"ds_read_b128 v[160:163], v240 offset:0\n" \
"ds_read_b128 v[164:167], v240 offset:512\n" \
"ds_read_b128 v[168:171], v240 offset:2048\n" \
"ds_read_b128 v[172:175], v240 offset:2560\n" \
"s_waitcnt lgkmcnt(0)\n" \
"v_mfma_f32_16x16x32_f16 v[0:3], v[120:123], v[160:163], 0\n" \
"v_pk_add_f16 v136, v120, v128\n" \
"v_pk_add_f16 v137, v121, v129\n" \
"v_pk_add_f16 v138, v122, v130\n" \
"v_pk_add_f16 v139, v123, v131\n" \
"s_add_i32 m0, s44, 0x8000\n" \
"s_add_i32 s80, s46, 0x400000\n" \
"v_mfma_f32_16x16x32_f16 v[12:15], v[124:127], v[160:163], 0\n" \
"v_pk_add_f16 v160, v160, v164\n" \
"v_pk_add_f16 v161, v161, v165\n" \
"v_pk_add_f16 v140, v124, v132\n" \
"buffer_load_dwordx4 v232, s[36:39], s80 offen sc1 lds\n" \
"v_mfma_f32_16x16x32_f16 v[4:7], v[128:131], v[164:167], 0\n" \
"v_pk_add_f16 v162, v162, v166\n" \
"v_pk_add_f16 v163, v163, v167\n" \
"v_pk_add_f16 v141, v125, v133\n" \
"v_mfma_f32_16x16x32_f16 v[16:19], v[132:135], v[164:167], 0\n" \
"v_pk_add_f16 v142, v126, v134\n" \
"v_pk_add_f16 v143, v127, v135\n" \
"buffer_load_dwordx4 v232, s[36:39], s80 offen offset:1024 sc1 lds\n" \
"v_mfma_f32_16x16x32_f16 v[8:11], v[136:139], v[160:163], 0\n" \
"ds_write_b64 v246, v[96:97] offset:0\n" \
"ds_write_b64 v246, v[98:99] offset:4096\n" \
"v_mfma_f32_16x16x32_f16 v[20:23], v[140:143], v[160:163], 0\n" \
"s_waitcnt vmcnt(5)\n" \
"s_barrier\n" \
"v_mfma_f32_16x16x32_f16 v[24:27], v[120:123], v[168:171], 0\n" \
"ds_read_b128 v[176:179], v240 offset:24576\n" \
"ds_write_b64 v246, v[100:101] offset:8192\n" \
"buffer_store_dwordx4 v[72:75], v235, s[36:39], s93 offen sc1\n" \
"v_mfma_f32_16x16x32_f16 v[36:39], v[124:127], v[168:171], 0\n" \
"v_pk_add_f16 v168, v168, v172\n" \
"v_pk_add_f16 v169, v169, v173\n" \
"ds_read_b128 v[180:183], v240 offset:25088\n" \
"buffer_load_dwordx4 v232, s[36:39], s80 offen offset:2048 sc1 lds\n" \
"v_mfma_f32_16x16x32_f16 v[28:31], v[128:131], v[172:175], 0\n" \
"v_pk_add_f16 v170, v170, v174\n" \
"v_pk_add_f16 v171, v171, v175\n" \
"ds_read_b128 v[160:163], v240 offset:26624\n" \
"v_mfma_f32_16x16x32_f16 v[40:43], v[132:135], v[172:175], 0\n" \
"ds_read_b128 v[164:167], v240 offset:27136\n" \
"buffer_load_dwordx4 v232, s[36:39], s80 offen offset:3072 sc1 lds\n" \
"v_mfma_f32_16x16x32_f16 v[32:35], v[136:139], v[168:171], 0\n" \
"ds_write_b64 v246, v[102:103] offset:12288\n" \
"ds_read_b64_tr_b16 v[144:145], v241 offset:16384\n" \
"ds_read_b64_tr_b16 v[146:147], v241 offset:24576\n" \
"v_mfma_f32_16x16x32_f16 v[44:47], v[140:143], v[168:171], 0\n" \
"ds_write_b64 v246, v[104:105] offset:16384\n" \
"ds_write_b64 v246, v[106:107] offset:20480\n" \
"s_waitcnt lgkmcnt(9)\n" \
"v_mfma_f32_16x16x32_f16 v[48:51], v[120:123], v[176:179], 0\n" \
"ds_read_b128 v[168:171], v240 offset:28672\n" \
"ds_write_b64 v246, v[108:109] offset:24576\n" \
"buffer_store_dwordx4 v[196:199], v235, s[36:39], s60 offen sc1\n" \
"ds_read_b64_tr_b16 v[148:149], v242 offset:16384\n" \
"v_mfma_f32_16x16x32_f16 v[60:63], v[124:127], v[176:179], 0\n" \
"s_waitcnt lgkmcnt(10)\n" \
"v_pk_add_f16 v176, v176, v180\n" \
"v_pk_add_f16 v177, v177, v181\n" \
"s_add_i32 m0, s44, 0x0\n" \
"s_add_i32 s80, s46, 0x800000\n" \
"v_mfma_f32_16x16x32_f16 v[52:55], v[128:131], v[180:183], 0\n" \
"v_pk_add_f16 v178, v178, v182\n" \
"v_pk_add_f16 v179, v179, v183\n" \
"buffer_load_dwordx4 v232, s[36:39], s80 offen sc1 lds\n" \
"v_mfma_f32_16x16x32_f16 v[64:67], v[132:135], v[180:183], 0\n" \
"ds_read_b128 v[172:175], v240 offset:29184\n" \
"ds_read_b64_tr_b16 v[150:151], v242 offset:24576\n" \
"v_mfma_f32_16x16x32_f16 v[56:59], v[136:139], v[176:179], 0\n" \
"ds_write_b64 v246, v[110:111] offset:28672\n" \
"ds_write_b64 v246, v[112:113] offset:32768\n" \
"v_mfma_f32_16x16x32_f16 v[68:71], v[140:143], v[176:179], 0\n" \
"buffer_load_dwordx4 v232, s[36:39], s80 offen offset:1024 sc1 lds\n" \
"ds_read_b64_tr_b16 v[152:153], v241 offset:16896\n" \
"s_waitcnt lgkmcnt(8)\n" \
"v_mfma_f32_16x16x32_f16 v[72:75], v[120:123], v[160:163], 0\n" \
"ds_read_b128 v[176:179], v240 offset:20480\n" \
"ds_write_b64 v246, v[114:115] offset:36864\n" \
"buffer_store_dwordx4 v[200:203], v235, s[36:39], s61 offen sc1\n" \
"v_mfma_f32_16x16x32_f16 v[84:87], v[124:127], v[160:163], 0\n" \
"v_pk_add_f16 v160, v160, v164\n" \
"v_pk_add_f16 v161, v161, v165\n" \
"ds_read_b64_tr_b16 v[154:155], v241 offset:25088\n" \
"ds_read_b64_tr_b16 v[156:157], v242 offset:16896\n" \
"v_mfma_f32_16x16x32_f16 v[76:79], v[128:131], v[164:167], 0\n" \
"v_pk_add_f16 v162, v162, v166\n" \
"v_pk_add_f16 v163, v163, v167\n" \
"buffer_load_dwordx4 v232, s[36:39], s80 offen offset:2048 sc1 lds\n" \
"v_mfma_f32_16x16x32_f16 v[88:91], v[132:135], v[164:167], 0\n" \
"ds_read_b128 v[180:183], v240 offset:20992\n" \
"ds_read_b64_tr_b16 v[158:159], v242 offset:25088\n" \
"v_mfma_f32_16x16x32_f16 v[80:83], v[136:139], v[160:163], 0\n" \
"ds_write_b64 v246, v[116:117] offset:40960\n" \
"v_mfma_f32_16x16x32_f16 v[92:95], v[140:143], v[160:163], 0\n" \
"buffer_load_dwordx4 v232, s[36:39], s80 offen offset:3072 sc1 lds\n" \
"s_waitcnt lgkmcnt(8)\n" \
"v_mfma_f32_16x16x32_f16 v[96:99], v[120:123], v[168:171], 0\n" \
"s_waitcnt lgkmcnt(7)\n" \
"v_pk_add_f16 v120, v144, v152\n" \
"v_pk_add_f16 v121, v145, v153\n" \
"ds_read_b128 v[160:163], v240 offset:22528\n" \
"v_mfma_f32_16x16x32_f16 v[108:111], v[124:127], v[168:171], 0\n" \
"v_pk_add_f16 v168, v168, v172\n" \
"v_pk_add_f16 v169, v169, v173\n" \
"ds_read_b128 v[164:167], v240 offset:23040\n" \
"v_mfma_f32_16x16x32_f16 v[100:103], v[128:131], v[172:175], 0\n" \
"v_pk_add_f16 v170, v170, v174\n" \
"v_pk_add_f16 v171, v171, v175\n" \
"v_mfma_f32_16x16x32_f16 v[112:115], v[132:135], v[172:175], 0\n" \
"s_waitcnt lgkmcnt(5)\n" \
"v_pk_add_f16 v122, v146, v154\n" \
"v_pk_add_f16 v123, v147, v155\n" \
"v_mfma_f32_16x16x32_f16 v[104:107], v[136:139], v[168:171], 0\n" \
"v_pk_add_f16 v124, v148, v156\n" \
"v_pk_add_f16 v125, v149, v157\n" \
"v_mfma_f32_16x16x32_f16 v[116:119], v[140:143], v[168:171], 0\n" \
"s_waitcnt lgkmcnt(2)\n" \
"v_pk_add_f16 v126, v150, v158\n" \
"v_pk_add_f16 v127, v151, v159\n" \
"s_waitcnt vmcnt(6) lgkmcnt(0)\n" \
"s_barrier\n" \
"v_mfma_f32_16x16x32_f16 v[0:3], v[144:147], v[176:179], v[0:3]\n" \
"ds_read_b128 v[168:171], v240 offset:45056\n" \
"ds_read_b64_tr_b16 v[184:185], v236 offset:0\n" \
"v_mfma_f32_16x16x32_f16 v[12:15], v[148:151], v[176:179], v[12:15]\n" \
"v_pk_add_f16 v176, v176, v180\n" \
"v_pk_add_f16 v177, v177, v181\n" \
"ds_read_b64_tr_b16 v[188:189], v237 offset:0\n" \
"v_mfma_f32_16x16x32_f16 v[4:7], v[152:155], v[180:183], v[4:7]\n" \
"v_pk_add_f16 v178, v178, v182\n" \
"v_pk_add_f16 v179, v179, v183\n" \
"v_mfma_f32_16x16x32_f16 v[16:19], v[156:159], v[180:183], v[16:19]\n" \
"ds_read_b128 v[172:175], v240 offset:45568\n" \
"s_add_i32 m0, s44, 0x4000\n" \
"s_add_i32 s80, s46, 0xc00000\n" \
"v_mfma_f32_16x16x32_f16 v[8:11], v[120:123], v[176:179], v[8:11]\n" \
"buffer_load_dwordx4 v232, s[36:39], s80 offen sc1 lds\n" \
"v_mfma_f32_16x16x32_f16 v[20:23], v[124:127], v[176:179], v[20:23]\n" \
"ds_read_b64_tr_b16 v[128:129], v241 offset:32768\n" \
"ds_read_b64_tr_b16 v[130:131], v241 offset:40960\n" \
"s_waitcnt lgkmcnt(4)\n" \
"v_mfma_f32_16x16x16_bf16 v[184:187], v[184:185], v[192:193], 0\n" \
"v_mfma_f32_16x16x32_f16 v[24:27], v[144:147], v[160:163], v[24:27]\n" \
"ds_read_b128 v[176:179], v240 offset:47104\n" \
"s_add_i32 s84, s52, 0x8004000\n" \
"s_waitcnt lgkmcnt(4)\n" \
"v_mfma_f32_16x16x16_bf16 v[188:191], v[188:189], v[192:193], 0\n" \
"v_mfma_f32_16x16x32_f16 v[36:39], v[148:151], v[160:163], v[36:39]\n" \
"v_pk_add_f16 v160, v160, v164\n" \
"v_pk_add_f16 v161, v161, v165\n" \
"buffer_store_dword v240, v243, s[36:39], s84 offen sc1\n" \
"v_mfma_f32_16x16x32_f16 v[28:31], v[152:155], v[164:167], v[28:31]\n" \
"v_pk_add_f16 v162, v162, v166\n" \
"v_pk_add_f16 v163, v163, v167\n" \
"ds_read_b64_tr_b16 v[132:133], v242 offset:32768\n" \
"v_mfma_f32_16x16x32_f16 v[40:43], v[156:159], v[164:167], v[40:43]\n" \
"v_cvt_pk_f16_f32 v184, v184, v185\n" \
"ds_read_b128 v[180:183], v240 offset:47616\n" \
"ds_read_b64_tr_b16 v[134:135], v242 offset:40960\n" \
"v_mfma_f32_16x16x32_f16 v[32:35], v[120:123], v[160:163], v[32:35]\n" \
"v_cvt_pk_f16_f32 v185, v186, v187\n" \
"v_cvt_pk_f16_f32 v186, v188, v189\n" \
"buffer_load_dwordx4 v232, s[36:39], s80 offen offset:1024 sc1 lds\n" \
"v_mfma_f32_16x16x32_f16 v[44:47], v[124:127], v[160:163], v[44:47]\n" \
"v_cvt_pk_f16_f32 v187, v190, v191\n" \
"v_add_u32 v184, s91, v184\n" \
"v_mfma_f32_16x16x32_f16 v[48:51], v[144:147], v[168:171], v[48:51]\n" \
"v_add_u32 v185, s91, v185\n" \
"v_add_u32 v186, s91, v186\n" \
"ds_read_b128 v[160:163], v240 offset:49152\n" \
"v_mfma_f32_16x16x32_f16 v[60:63], v[148:151], v[168:171], v[60:63]\n" \
"s_waitcnt lgkmcnt(4)\n" \
"v_pk_add_f16 v168, v168, v172\n" \
"v_pk_add_f16 v169, v169, v173\n" \
"v_add_u32 v187, s91, v187\n" \
"v_and_b32 v184, s92, v184\n" \
"s_add_i32 s82, s49, 0x2800000\n" \
"v_mfma_f32_16x16x32_f16 v[52:55], v[152:155], v[172:175], v[52:55]\n" \
"v_pk_add_f16 v170, v170, v174\n" \
"v_pk_add_f16 v171, v171, v175\n" \
"v_and_b32 v185, s92, v185\n" \
"v_and_b32 v186, s92, v186\n" \
"v_and_b32 v187, s92, v187\n" \
"v_mfma_f32_16x16x32_f16 v[64:67], v[156:159], v[172:175], v[64:67]\n" \
"ds_read_b128 v[164:167], v240 offset:49664\n" \
"buffer_store_dwordx4 v[184:187], v234, s[36:39], s82 offen sc1\n" \
"ds_read_b64_tr_b16 v[136:137], v241 offset:33280\n" \
"v_mfma_f32_16x16x32_f16 v[56:59], v[120:123], v[168:171], v[56:59]\n" \
"buffer_load_dwordx4 v232, s[36:39], s80 offen offset:2048 sc1 lds\n" \
"ds_read_b64_tr_b16 v[138:139], v241 offset:41472\n" \
"v_mfma_f32_16x16x32_f16 v[68:71], v[124:127], v[168:171], v[68:71]\n" \
"ds_read_b64_tr_b16 v[184:185], v238 offset:0\n" \
"ds_read_b64_tr_b16 v[188:189], v239 offset:0\n" \
"v_mfma_f32_16x16x32_f16 v[72:75], v[144:147], v[176:179], v[72:75]\n" \
"ds_read_b128 v[168:171], v240 offset:40960\n" \
"ds_read_b64_tr_b16 v[140:141], v242 offset:33280\n" \
"v_mfma_f32_16x16x32_f16 v[84:87], v[148:151], v[176:179], v[84:87]\n" \
"s_waitcnt lgkmcnt(7)\n" \
"v_pk_add_f16 v176, v176, v180\n" \
"v_pk_add_f16 v177, v177, v181\n" \
"v_mfma_f32_16x16x32_f16 v[76:79], v[152:155], v[180:183], v[76:79]\n" \
"v_pk_add_f16 v178, v178, v182\n" \
"v_pk_add_f16 v179, v179, v183\n" \
"s_waitcnt lgkmcnt(3)\n" \
"v_mfma_f32_16x16x16_bf16 v[184:187], v[184:185], v[192:193], 0\n" \
"v_mfma_f32_16x16x32_f16 v[88:91], v[156:159], v[180:183], v[88:91]\n" \
"ds_read_b128 v[172:175], v240 offset:41472\n" \
"ds_read_b64_tr_b16 v[142:143], v242 offset:41472\n" \
"s_waitcnt lgkmcnt(4)\n" \
"v_mfma_f32_16x16x16_bf16 v[188:191], v[188:189], v[192:193], 0\n" \
"v_mfma_f32_16x16x32_f16 v[80:83], v[120:123], v[176:179], v[80:83]\n" \
"buffer_load_dwordx4 v232, s[36:39], s80 offen offset:3072 sc1 lds\n" \
"v_mfma_f32_16x16x32_f16 v[92:95], v[124:127], v[176:179], v[92:95]\n" \
"v_mfma_f32_16x16x32_f16 v[96:99], v[144:147], v[160:163], v[96:99]\n" \
"v_pk_add_f16 v144, v128, v136\n" \
"v_pk_add_f16 v145, v129, v137\n" \
"v_cvt_pk_f16_f32 v184, v184, v185\n" \
"v_cvt_pk_f16_f32 v185, v186, v187\n" \
"ds_read_b128 v[176:179], v240 offset:43008\n" \
"v_mfma_f32_16x16x32_f16 v[108:111], v[148:151], v[160:163], v[108:111]\n" \
"v_pk_add_f16 v160, v160, v164\n" \
"v_pk_add_f16 v161, v161, v165\n" \
"v_cvt_pk_f16_f32 v186, v188, v189\n" \
"v_cvt_pk_f16_f32 v187, v190, v191\n" \
"ds_read_b128 v[180:183], v240 offset:43520\n" \
"v_mfma_f32_16x16x32_f16 v[100:103], v[152:155], v[164:167], v[100:103]\n" \
"v_pk_add_f16 v162, v162, v166\n" \
"v_pk_add_f16 v163, v163, v167\n" \
"v_add_u32 v184, s91, v184\n" \
"v_add_u32 v185, s91, v185\n" \
"v_add_u32 v186, s91, v186\n" \
"v_mfma_f32_16x16x32_f16 v[112:115], v[156:159], v[164:167], v[112:115]\n" \
"v_pk_add_f16 v146, v130, v138\n" \
"v_pk_add_f16 v147, v131, v139\n" \
"v_add_u32 v187, s91, v187\n" \
"v_and_b32 v184, s92, v184\n" \
"v_mfma_f32_16x16x32_f16 v[104:107], v[120:123], v[160:163], v[104:107]\n" \
"s_waitcnt lgkmcnt(2)\n" \
"v_pk_add_f16 v148, v132, v140\n" \
"v_pk_add_f16 v149, v133, v141\n" \
"v_and_b32 v185, s92, v185\n" \
"v_and_b32 v186, s92, v186\n" \
"v_and_b32 v187, s92, v187\n" \
"v_mfma_f32_16x16x32_f16 v[116:119], v[124:127], v[160:163], v[116:119]\n" \
"v_pk_add_f16 v150, v134, v142\n" \
"v_pk_add_f16 v151, v135, v143\n" \
"buffer_store_dwordx4 v[184:187], v234, s[36:39], s82 offen offset:1024 sc1\n" \
"s_waitcnt vmcnt(7) lgkmcnt(0)\n" \
"s_barrier\n" \
"v_mfma_f32_16x16x32_f16 v[0:3], v[128:131], v[168:171], v[0:3]\n" \
"ds_read_b128 v[160:163], v240 offset:4096\n" \
"ds_read_b64_tr_b16 v[184:185], v236 offset:4096\n" \
"ds_read_b64_tr_b16 v[188:189], v237 offset:4096\n" \
"v_mfma_f32_16x16x32_f16 v[12:15], v[132:135], v[168:171], v[12:15]\n" \
"v_pk_add_f16 v168, v168, v172\n" \
"v_pk_add_f16 v169, v169, v173\n" \
"v_mfma_f32_16x16x32_f16 v[4:7], v[136:139], v[172:175], v[4:7]\n" \
"v_pk_add_f16 v170, v170, v174\n" \
"v_pk_add_f16 v171, v171, v175\n" \
"buffer_store_dwordx4 v[204:207], v235, s[36:39], s62 offen sc1\n" \
"v_mfma_f32_16x16x32_f16 v[16:19], v[140:143], v[172:175], v[16:19]\n" \
"ds_read_b128 v[164:167], v240 offset:4608\n" \
"s_add_i32 m0, s44, 0x8000\n" \
"s_add_i32 s80, s46, 0x1000000\n" \
"v_mfma_f32_16x16x32_f16 v[8:11], v[144:147], v[168:171], v[8:11]\n" \
"buffer_load_dwordx4 v232, s[36:39], s80 offen sc1 lds\n" \
"s_waitcnt lgkmcnt(2)\n" \
"v_mfma_f32_16x16x16_bf16 v[184:187], v[184:185], v[192:193], 0\n" \
"v_mfma_f32_16x16x32_f16 v[20:23], v[148:151], v[168:171], v[20:23]\n" \
"ds_read_b64_tr_b16 v[120:121], v241 offset:0\n" \
"ds_read_b64_tr_b16 v[122:123], v241 offset:8192\n" \
"s_waitcnt lgkmcnt(3)\n" \
"v_mfma_f32_16x16x16_bf16 v[188:191], v[188:189], v[192:193], 0\n" \
"v_mfma_f32_16x16x32_f16 v[24:27], v[128:131], v[176:179], v[24:27]\n" \
"ds_read_b128 v[168:171], v240 offset:6144\n" \
"v_mfma_f32_16x16x32_f16 v[36:39], v[132:135], v[176:179], v[36:39]\n" \
"v_pk_add_f16 v176, v176, v180\n" \
"v_pk_add_f16 v177, v177, v181\n" \
"ds_read_b64_tr_b16 v[124:125], v242 offset:0\n" \
"v_mfma_f32_16x16x32_f16 v[28:31], v[136:139], v[180:183], v[28:31]\n" \
"v_pk_add_f16 v178, v178, v182\n" \
"v_pk_add_f16 v179, v179, v183\n" \
"v_cvt_pk_f16_f32 v184, v184, v185\n" \
"v_mfma_f32_16x16x32_f16 v[40:43], v[140:143], v[180:183], v[40:43]\n" \
"v_cvt_pk_f16_f32 v185, v186, v187\n" \
"v_cvt_pk_f16_f32 v186, v188, v189\n" \
"ds_read_b128 v[172:175], v240 offset:6656\n" \
"v_mfma_f32_16x16x32_f16 v[32:35], v[144:147], v[176:179], v[32:35]\n" \
"v_cvt_pk_f16_f32 v187, v190, v191\n" \
"v_add_u32 v184, s91, v184\n" \
"buffer_load_dwordx4 v232, s[36:39], s80 offen offset:1024 sc1 lds\n" \
"v_mfma_f32_16x16x32_f16 v[44:47], v[148:151], v[176:179], v[44:47]\n" \
"v_add_u32 v185, s91, v185\n" \
"v_add_u32 v186, s91, v186\n" \
"ds_read_b64_tr_b16 v[126:127], v242 offset:8192\n" \
"v_mfma_f32_16x16x32_f16 v[48:51], v[128:131], v[160:163], v[48:51]\n" \
"v_add_u32 v187, s91, v187\n" \
"v_and_b32 v184, s92, v184\n" \
"ds_read_b128 v[176:179], v240 offset:8192\n" \
"v_mfma_f32_16x16x32_f16 v[60:63], v[132:135], v[160:163], v[60:63]\n" \
"s_waitcnt lgkmcnt(4)\n" \
"v_pk_add_f16 v160, v160, v164\n" \
"v_pk_add_f16 v161, v161, v165\n" \
"v_and_b32 v185, s92, v185\n" \
"s_add_i32 s82, s50, 0x3000000\n" \
"v_mfma_f32_16x16x32_f16 v[52:55], v[136:139], v[164:167], v[52:55]\n" \
"v_pk_add_f16 v162, v162, v166\n" \
"v_pk_add_f16 v163, v163, v167\n" \
"v_and_b32 v186, s92, v186\n" \
"v_and_b32 v187, s92, v187\n" \
"v_mfma_f32_16x16x32_f16 v[64:67], v[140:143], v[164:167], v[64:67]\n" \
"ds_read_b128 v[180:183], v240 offset:8704\n" \
"buffer_store_dwordx4 v[184:187], v234, s[36:39], s82 offen sc1\n" \
"ds_read_b64_tr_b16 v[152:153], v241 offset:512\n" \
"v_mfma_f32_16x16x32_f16 v[56:59], v[144:147], v[160:163], v[56:59]\n" \
"buffer_load_dwordx4 v232, s[36:39], s80 offen offset:2048 sc1 lds\n" \
"v_mfma_f32_16x16x32_f16 v[68:71], v[148:151], v[160:163], v[68:71]\n" \
"ds_read_b64_tr_b16 v[154:155], v241 offset:8704\n" \
"ds_read_b64_tr_b16 v[156:157], v242 offset:512\n" \
"v_mfma_f32_16x16x32_f16 v[72:75], v[128:131], v[168:171], v[72:75]\n" \
"ds_read_b128 v[160:163], v240 offset:0\n" \
"ds_read_b64_tr_b16 v[158:159], v242 offset:8704\n" \
"v_mfma_f32_16x16x32_f16 v[84:87], v[132:135], v[168:171], v[84:87]\n" \
"s_waitcnt lgkmcnt(6)\n" \
"v_pk_add_f16 v168, v168, v172\n" \
"v_pk_add_f16 v169, v169, v173\n" \
"v_mfma_f32_16x16x32_f16 v[76:79], v[136:139], v[172:175], v[76:79]\n" \
"v_pk_add_f16 v170, v170, v174\n" \
"v_pk_add_f16 v171, v171, v175\n" \
"v_mfma_f32_16x16x32_f16 v[88:91], v[140:143], v[172:175], v[88:91]\n" \
"ds_read_b128 v[164:167], v240 offset:512\n" \
"v_mfma_f32_16x16x32_f16 v[80:83], v[144:147], v[168:171], v[80:83]\n" \
"buffer_load_dwordx4 v232, s[36:39], s80 offen offset:3072 sc1 lds\n" \
"v_mfma_f32_16x16x32_f16 v[92:95], v[148:151], v[168:171], v[92:95]\n" \
"v_mfma_f32_16x16x32_f16 v[96:99], v[128:131], v[176:179], v[96:99]\n" \
"s_waitcnt lgkmcnt(5)\n" \
"v_pk_add_f16 v128, v120, v152\n" \
"v_pk_add_f16 v129, v121, v153\n" \
"ds_read_b128 v[168:171], v240 offset:2048\n" \
"v_mfma_f32_16x16x32_f16 v[108:111], v[132:135], v[176:179], v[108:111]\n" \
"v_pk_add_f16 v176, v176, v180\n" \
"v_pk_add_f16 v177, v177, v181\n" \
"ds_read_b128 v[172:175], v240 offset:2560\n" \
"v_mfma_f32_16x16x32_f16 v[100:103], v[136:139], v[180:183], v[100:103]\n" \
"v_pk_add_f16 v178, v178, v182\n" \
"v_pk_add_f16 v179, v179, v183\n" \
"v_mfma_f32_16x16x32_f16 v[112:115], v[140:143], v[180:183], v[112:115]\n" \
"s_waitcnt lgkmcnt(3)\n" \
"v_pk_add_f16 v130, v122, v154\n" \
"v_pk_add_f16 v131, v123, v155\n" \
"v_mfma_f32_16x16x32_f16 v[104:107], v[144:147], v[176:179], v[104:107]\n" \
"v_pk_add_f16 v132, v124, v156\n" \
"v_pk_add_f16 v133, v125, v157\n" \
"v_mfma_f32_16x16x32_f16 v[116:119], v[148:151], v[176:179], v[116:119]\n" \
"v_pk_add_f16 v134, v126, v158\n" \
"v_pk_add_f16 v135, v127, v159\n" \
"s_waitcnt vmcnt(6) lgkmcnt(0)\n" \
"s_barrier\n" \
"v_mfma_f32_16x16x32_f16 v[0:3], v[120:123], v[160:163], v[0:3]\n" \
"ds_read_b128 v[176:179], v240 offset:24576\n" \
"ds_read_b64_tr_b16 v[184:185], v238 offset:4096\n" \
"ds_read_b64_tr_b16 v[188:189], v239 offset:4096\n" \
"v_mfma_f32_16x16x32_f16 v[12:15], v[124:127], v[160:163], v[12:15]\n" \
"v_pk_add_f16 v160, v160, v164\n" \
"v_pk_add_f16 v161, v161, v165\n" \
"v_mfma_f32_16x16x32_f16 v[4:7], v[152:155], v[164:167], v[4:7]\n" \
"v_pk_add_f16 v162, v162, v166\n" \
"v_pk_add_f16 v163, v163, v167\n" \
"v_mfma_f32_16x16x32_f16 v[16:19], v[156:159], v[164:167], v[16:19]\n" \
"ds_read_b128 v[180:183], v240 offset:25088\n" \
"s_add_i32 m0, s44, 0x0\n" \
"s_add_i32 s80, s46, 0x1400000\n" \
"v_mfma_f32_16x16x32_f16 v[8:11], v[128:131], v[160:163], v[8:11]\n" \
"buffer_load_dwordx4 v232, s[36:39], s80 offen sc1 lds\n" \
"s_waitcnt lgkmcnt(2)\n" \
"v_mfma_f32_16x16x16_bf16 v[184:187], v[184:185], v[192:193], 0\n" \
"v_mfma_f32_16x16x32_f16 v[20:23], v[132:135], v[160:163], v[20:23]\n" \
"ds_read_b64_tr_b16 v[136:137], v241 offset:16384\n" \
"ds_read_b64_tr_b16 v[138:139], v241 offset:24576\n" \
"s_waitcnt lgkmcnt(3)\n" \
"v_mfma_f32_16x16x16_bf16 v[188:191], v[188:189], v[192:193], 0\n" \
"v_mfma_f32_16x16x32_f16 v[24:27], v[120:123], v[168:171], v[24:27]\n" \
"ds_read_b128 v[160:163], v240 offset:26624\n" \
"s_add_i32 s84, s53, 0x8006000\n" \
"v_mfma_f32_16x16x32_f16 v[36:39], v[124:127], v[168:171], v[36:39]\n" \
"v_pk_add_f16 v168, v168, v172\n" \
"v_pk_add_f16 v169, v169, v173\n" \
"buffer_store_dword v240, v243, s[36:39], s84 offen sc1\n" \
"v_mfma_f32_16x16x32_f16 v[28:31], v[152:155], v[172:175], v[28:31]\n" \
"v_pk_add_f16 v170, v170, v174\n" \
"v_pk_add_f16 v171, v171, v175\n" \
"v_cvt_pk_f16_f32 v184, v184, v185\n" \
"ds_read_b64_tr_b16 v[140:141], v242 offset:16384\n" \
"v_mfma_f32_16x16x32_f16 v[40:43], v[156:159], v[172:175], v[40:43]\n" \
"v_cvt_pk_f16_f32 v185, v186, v187\n" \
"v_cvt_pk_f16_f32 v186, v188, v189\n" \
"ds_read_b128 v[164:167], v240 offset:27136\n" \
"v_mfma_f32_16x16x32_f16 v[32:35], v[128:131], v[168:171], v[32:35]\n" \
"v_cvt_pk_f16_f32 v187, v190, v191\n" \
"v_add_u32 v184, s91, v184\n" \
"buffer_load_dwordx4 v232, s[36:39], s80 offen offset:1024 sc1 lds\n" \
"v_mfma_f32_16x16x32_f16 v[44:47], v[132:135], v[168:171], v[44:47]\n" \
"v_add_u32 v185, s91, v185\n" \
"v_add_u32 v186, s91, v186\n" \
"ds_read_b64_tr_b16 v[142:143], v242 offset:24576\n" \
"v_mfma_f32_16x16x32_f16 v[48:51], v[120:123], v[176:179], v[48:51]\n" \
"v_add_u32 v187, s91, v187\n" \
"v_and_b32 v184, s92, v184\n" \
"ds_read_b128 v[168:171], v240 offset:28672\n" \
"v_mfma_f32_16x16x32_f16 v[60:63], v[124:127], v[176:179], v[60:63]\n" \
"s_waitcnt lgkmcnt(4)\n" \
"v_pk_add_f16 v176, v176, v180\n" \
"v_pk_add_f16 v177, v177, v181\n" \
"v_and_b32 v185, s92, v185\n" \
"v_mfma_f32_16x16x32_f16 v[52:55], v[152:155], v[180:183], v[52:55]\n" \
"v_pk_add_f16 v178, v178, v182\n" \
"v_pk_add_f16 v179, v179, v183\n" \
"v_and_b32 v186, s92, v186\n" \
"v_and_b32 v187, s92, v187\n" \
"v_mfma_f32_16x16x32_f16 v[64:67], v[156:159], v[180:183], v[64:67]\n" \
"ds_read_b128 v[172:175], v240 offset:29184\n" \
"buffer_store_dwordx4 v[184:187], v234, s[36:39], s82 offen offset:1024 sc1\n" \
"ds_read_b64_tr_b16 v[144:145], v241 offset:16896\n" \
"v_mfma_f32_16x16x32_f16 v[56:59], v[128:131], v[176:179], v[56:59]\n" \
"buffer_load_dwordx4 v232, s[36:39], s80 offen offset:2048 sc1 lds\n" \
"v_mfma_f32_16x16x32_f16 v[68:71], v[132:135], v[176:179], v[68:71]\n" \
"ds_read_b64_tr_b16 v[146:147], v241 offset:25088\n" \
"ds_read_b64_tr_b16 v[148:149], v242 offset:16896\n" \
"v_mfma_f32_16x16x32_f16 v[72:75], v[120:123], v[160:163], v[72:75]\n" \
"ds_read_b128 v[176:179], v240 offset:20480\n" \
"ds_read_b64_tr_b16 v[150:151], v242 offset:25088\n" \
"v_mfma_f32_16x16x32_f16 v[84:87], v[124:127], v[160:163], v[84:87]\n" \
"s_waitcnt lgkmcnt(6)\n" \
"v_pk_add_f16 v160, v160, v164\n" \
"v_pk_add_f16 v161, v161, v165\n" \
"v_mfma_f32_16x16x32_f16 v[76:79], v[152:155], v[164:167], v[76:79]\n" \
"v_pk_add_f16 v162, v162, v166\n" \
"v_pk_add_f16 v163, v163, v167\n" \
"v_mfma_f32_16x16x32_f16 v[88:91], v[156:159], v[164:167], v[88:91]\n" \
"ds_read_b128 v[180:183], v240 offset:20992\n" \
"v_mfma_f32_16x16x32_f16 v[80:83], v[128:131], v[160:163], v[80:83]\n" \
"buffer_load_dwordx4 v232, s[36:39], s80 offen offset:3072 sc1 lds\n" \
"v_mfma_f32_16x16x32_f16 v[92:95], v[132:135], v[160:163], v[92:95]\n" \
"v_mfma_f32_16x16x32_f16 v[96:99], v[120:123], v[168:171], v[96:99]\n" \
"s_waitcnt lgkmcnt(5)\n" \
"v_pk_add_f16 v120, v136, v144\n" \
"v_pk_add_f16 v121, v137, v145\n" \
"ds_read_b128 v[160:163], v240 offset:22528\n" \
"v_mfma_f32_16x16x32_f16 v[108:111], v[124:127], v[168:171], v[108:111]\n" \
"v_pk_add_f16 v168, v168, v172\n" \
"v_pk_add_f16 v169, v169, v173\n" \
"ds_read_b128 v[164:167], v240 offset:23040\n" \
"v_mfma_f32_16x16x32_f16 v[100:103], v[152:155], v[172:175], v[100:103]\n" \
"v_pk_add_f16 v170, v170, v174\n" \
"v_pk_add_f16 v171, v171, v175\n" \
"v_mfma_f32_16x16x32_f16 v[112:115], v[156:159], v[172:175], v[112:115]\n" \
"s_waitcnt lgkmcnt(3)\n" \
"v_pk_add_f16 v122, v138, v146\n" \
"v_pk_add_f16 v123, v139, v147\n" \
"v_mfma_f32_16x16x32_f16 v[104:107], v[128:131], v[168:171], v[104:107]\n" \
"v_pk_add_f16 v124, v140, v148\n" \
"v_pk_add_f16 v125, v141, v149\n" \
"v_mfma_f32_16x16x32_f16 v[116:119], v[132:135], v[168:171], v[116:119]\n" \
"v_pk_add_f16 v126, v142, v150\n" \
"v_pk_add_f16 v127, v143, v151\n" \
"s_waitcnt vmcnt(6) lgkmcnt(0)\n" \
"s_barrier\n" \
"v_mfma_f32_16x16x32_f16 v[0:3], v[136:139], v[176:179], v[0:3]\n" \
"ds_read_b128 v[168:171], v240 offset:45056\n" \
"ds_read_b64_tr_b16 v[184:185], v236 offset:8192\n" \
"ds_read_b64_tr_b16 v[188:189], v237 offset:8192\n" \
"v_mfma_f32_16x16x32_f16 v[12:15], v[140:143], v[176:179], v[12:15]\n" \
"v_pk_add_f16 v176, v176, v180\n" \
"v_pk_add_f16 v177, v177, v181\n" \
"v_mfma_f32_16x16x32_f16 v[4:7], v[144:147], v[180:183], v[4:7]\n" \
"v_pk_add_f16 v178, v178, v182\n" \
"v_pk_add_f16 v179, v179, v183\n" \
"buffer_store_dwordx4 v[208:211], v235, s[36:39], s63 offen sc1\n" \
"v_mfma_f32_16x16x32_f16 v[16:19], v[148:151], v[180:183], v[16:19]\n" \
"ds_read_b128 v[172:175], v240 offset:45568\n" \
"s_add_i32 m0, s44, 0x4000\n" \
"s_add_i32 s80, s46, 0x1800000\n" \
"v_mfma_f32_16x16x32_f16 v[8:11], v[120:123], v[176:179], v[8:11]\n" \
"buffer_load_dwordx4 v232, s[36:39], s80 offen sc1 lds\n" \
"s_waitcnt lgkmcnt(2)\n" \
"v_mfma_f32_16x16x16_bf16 v[184:187], v[184:185], v[192:193], 0\n" \
"v_mfma_f32_16x16x32_f16 v[20:23], v[124:127], v[176:179], v[20:23]\n" \
"ds_read_b64_tr_b16 v[128:129], v241 offset:32768\n" \
"ds_read_b64_tr_b16 v[130:131], v241 offset:40960\n" \
"s_waitcnt lgkmcnt(3)\n" \
"v_mfma_f32_16x16x16_bf16 v[188:191], v[188:189], v[192:193], 0\n" \
"v_mfma_f32_16x16x32_f16 v[24:27], v[136:139], v[160:163], v[24:27]\n" \
"ds_read_b128 v[176:179], v240 offset:47104\n" \
"v_mfma_f32_16x16x32_f16 v[36:39], v[140:143], v[160:163], v[36:39]\n" \
"v_pk_add_f16 v160, v160, v164\n" \
"v_pk_add_f16 v161, v161, v165\n" \
"ds_read_b64_tr_b16 v[132:133], v242 offset:32768\n" \
"v_mfma_f32_16x16x32_f16 v[28:31], v[144:147], v[164:167], v[28:31]\n" \
"v_pk_add_f16 v162, v162, v166\n" \
"v_pk_add_f16 v163, v163, v167\n" \
"v_cvt_pk_f16_f32 v184, v184, v185\n" \
"v_mfma_f32_16x16x32_f16 v[40:43], v[148:151], v[164:167], v[40:43]\n" \
"v_cvt_pk_f16_f32 v185, v186, v187\n" \
"v_cvt_pk_f16_f32 v186, v188, v189\n" \
"ds_read_b128 v[180:183], v240 offset:47616\n" \
"v_mfma_f32_16x16x32_f16 v[32:35], v[120:123], v[160:163], v[32:35]\n" \
"v_cvt_pk_f16_f32 v187, v190, v191\n" \
"v_add_u32 v184, s91, v184\n" \
"buffer_load_dwordx4 v232, s[36:39], s80 offen offset:1024 sc1 lds\n" \
"v_mfma_f32_16x16x32_f16 v[44:47], v[124:127], v[160:163], v[44:47]\n" \
"v_add_u32 v185, s91, v185\n" \
"v_add_u32 v186, s91, v186\n" \
"ds_read_b64_tr_b16 v[134:135], v242 offset:40960\n" \
"v_mfma_f32_16x16x32_f16 v[48:51], v[136:139], v[168:171], v[48:51]\n" \
"v_add_u32 v187, s91, v187\n" \
"v_and_b32 v184, s92, v184\n" \
"ds_read_b128 v[160:163], v240 offset:49152\n" \
"v_mfma_f32_16x16x32_f16 v[60:63], v[140:143], v[168:171], v[60:63]\n" \
"s_waitcnt lgkmcnt(4)\n" \
"v_pk_add_f16 v168, v168, v172\n" \
"v_pk_add_f16 v169, v169, v173\n" \
"v_and_b32 v185, s92, v185\n" \
"s_add_i32 s82, s51, 0x3800000\n" \
"v_mfma_f32_16x16x32_f16 v[52:55], v[144:147], v[172:175], v[52:55]\n" \
"v_pk_add_f16 v170, v170, v174\n" \
"v_pk_add_f16 v171, v171, v175\n" \
"v_and_b32 v186, s92, v186\n" \
"v_and_b32 v187, s92, v187\n" \
"v_mfma_f32_16x16x32_f16 v[64:67], v[148:151], v[172:175], v[64:67]\n" \
"ds_read_b128 v[164:167], v240 offset:49664\n" \
"buffer_store_dwordx4 v[184:187], v234, s[36:39], s82 offen sc1\n" \
"ds_read_b64_tr_b16 v[152:153], v241 offset:33280\n" \
"v_mfma_f32_16x16x32_f16 v[56:59], v[120:123], v[168:171], v[56:59]\n" \
"buffer_load_dwordx4 v232, s[36:39], s80 offen offset:2048 sc1 lds\n" \
"v_mfma_f32_16x16x32_f16 v[68:71], v[124:127], v[168:171], v[68:71]\n" \
"ds_read_b64_tr_b16 v[154:155], v241 offset:41472\n" \
"ds_read_b64_tr_b16 v[156:157], v242 offset:33280\n" \
"v_mfma_f32_16x16x32_f16 v[72:75], v[136:139], v[176:179], v[72:75]\n" \
"ds_read_b128 v[168:171], v240 offset:40960\n" \
"ds_read_b64_tr_b16 v[158:159], v242 offset:41472\n" \
"v_mfma_f32_16x16x32_f16 v[84:87], v[140:143], v[176:179], v[84:87]\n" \
"s_waitcnt lgkmcnt(6)\n" \
"v_pk_add_f16 v176, v176, v180\n" \
"v_pk_add_f16 v177, v177, v181\n" \
"v_mfma_f32_16x16x32_f16 v[76:79], v[144:147], v[180:183], v[76:79]\n" \
"v_pk_add_f16 v178, v178, v182\n" \
"v_pk_add_f16 v179, v179, v183\n" \
"v_mfma_f32_16x16x32_f16 v[88:91], v[148:151], v[180:183], v[88:91]\n" \
"ds_read_b128 v[172:175], v240 offset:41472\n" \
"v_mfma_f32_16x16x32_f16 v[80:83], v[120:123], v[176:179], v[80:83]\n" \
"buffer_load_dwordx4 v232, s[36:39], s80 offen offset:3072 sc1 lds\n" \
"v_mfma_f32_16x16x32_f16 v[92:95], v[124:127], v[176:179], v[92:95]\n" \
"v_mfma_f32_16x16x32_f16 v[96:99], v[136:139], v[160:163], v[96:99]\n" \
"s_waitcnt lgkmcnt(5)\n" \
"v_pk_add_f16 v136, v128, v152\n" \
"v_pk_add_f16 v137, v129, v153\n" \
"ds_read_b128 v[176:179], v240 offset:43008\n" \
"v_mfma_f32_16x16x32_f16 v[108:111], v[140:143], v[160:163], v[108:111]\n" \
"v_pk_add_f16 v160, v160, v164\n" \
"v_pk_add_f16 v161, v161, v165\n" \
"ds_read_b128 v[180:183], v240 offset:43520\n" \
"v_mfma_f32_16x16x32_f16 v[100:103], v[144:147], v[164:167], v[100:103]\n" \
"v_pk_add_f16 v162, v162, v166\n" \
"v_pk_add_f16 v163, v163, v167\n" \
"v_mfma_f32_16x16x32_f16 v[112:115], v[148:151], v[164:167], v[112:115]\n" \
"s_waitcnt lgkmcnt(3)\n" \
"v_pk_add_f16 v138, v130, v154\n" \
"v_pk_add_f16 v139, v131, v155\n" \
"v_mfma_f32_16x16x32_f16 v[104:107], v[120:123], v[160:163], v[104:107]\n" \
"v_pk_add_f16 v140, v132, v156\n" \
"v_pk_add_f16 v141, v133, v157\n" \
"v_mfma_f32_16x16x32_f16 v[116:119], v[124:127], v[160:163], v[116:119]\n" \
"v_pk_add_f16 v142, v134, v158\n" \
"v_pk_add_f16 v143, v135, v159\n" \
"s_waitcnt vmcnt(6) lgkmcnt(0)\n" \
"s_barrier\n" \
"v_mfma_f32_16x16x32_f16 v[0:3], v[128:131], v[168:171], v[0:3]\n" \
"ds_read_b128 v[160:163], v240 offset:4096\n" \
"ds_read_b64_tr_b16 v[184:185], v238 offset:8192\n" \
"ds_read_b64_tr_b16 v[188:189], v239 offset:8192\n" \
"v_mfma_f32_16x16x32_f16 v[12:15], v[132:135], v[168:171], v[12:15]\n" \
"v_pk_add_f16 v168, v168, v172\n" \
"v_pk_add_f16 v169, v169, v173\n" \
"v_mfma_f32_16x16x32_f16 v[4:7], v[152:155], v[172:175], v[4:7]\n" \
"v_pk_add_f16 v170, v170, v174\n" \
"v_pk_add_f16 v171, v171, v175\n" \
"v_mfma_f32_16x16x32_f16 v[16:19], v[156:159], v[172:175], v[16:19]\n" \
"ds_read_b128 v[164:167], v240 offset:4608\n" \
"s_add_i32 m0, s44, 0x8000\n" \
"s_add_i32 s80, s46, 0x1c00000\n" \
"v_mfma_f32_16x16x32_f16 v[8:11], v[136:139], v[168:171], v[8:11]\n" \
"buffer_load_dwordx4 v232, s[36:39], s80 offen sc1 lds\n" \
"s_waitcnt lgkmcnt(2)\n" \
"v_mfma_f32_16x16x16_bf16 v[184:187], v[184:185], v[192:193], 0\n" \
"v_mfma_f32_16x16x32_f16 v[20:23], v[140:143], v[168:171], v[20:23]\n" \
"ds_read_b64_tr_b16 v[120:121], v241 offset:0\n" \
"ds_read_b64_tr_b16 v[122:123], v241 offset:8192\n" \
"s_waitcnt lgkmcnt(3)\n" \
"v_mfma_f32_16x16x16_bf16 v[188:191], v[188:189], v[192:193], 0\n" \
"v_mfma_f32_16x16x32_f16 v[24:27], v[128:131], v[176:179], v[24:27]\n" \
"ds_read_b128 v[168:171], v240 offset:6144\n" \
"s_add_i32 s84, s54, 0x8008000\n" \
"v_mfma_f32_16x16x32_f16 v[36:39], v[132:135], v[176:179], v[36:39]\n" \
"v_pk_add_f16 v176, v176, v180\n" \
"v_pk_add_f16 v177, v177, v181\n" \
"buffer_store_dword v240, v243, s[36:39], s84 offen sc1\n" \
"v_mfma_f32_16x16x32_f16 v[28:31], v[152:155], v[180:183], v[28:31]\n" \
"v_pk_add_f16 v178, v178, v182\n" \
"v_pk_add_f16 v179, v179, v183\n" \
"v_cvt_pk_f16_f32 v184, v184, v185\n" \
"ds_read_b64_tr_b16 v[124:125], v242 offset:0\n" \
"v_mfma_f32_16x16x32_f16 v[40:43], v[156:159], v[180:183], v[40:43]\n" \
"v_cvt_pk_f16_f32 v185, v186, v187\n" \
"v_cvt_pk_f16_f32 v186, v188, v189\n" \
"ds_read_b128 v[172:175], v240 offset:6656\n" \
"v_mfma_f32_16x16x32_f16 v[32:35], v[136:139], v[176:179], v[32:35]\n" \
"v_cvt_pk_f16_f32 v187, v190, v191\n" \
"v_add_u32 v184, s91, v184\n" \
"buffer_load_dwordx4 v232, s[36:39], s80 offen offset:1024 sc1 lds\n" \
"v_mfma_f32_16x16x32_f16 v[44:47], v[140:143], v[176:179], v[44:47]\n" \
"v_add_u32 v185, s91, v185\n" \
"v_add_u32 v186, s91, v186\n" \
"ds_read_b64_tr_b16 v[126:127], v242 offset:8192\n" \
"v_mfma_f32_16x16x32_f16 v[48:51], v[128:131], v[160:163], v[48:51]\n" \
"v_add_u32 v187, s91, v187\n" \
"v_and_b32 v184, s92, v184\n" \
"ds_read_b128 v[176:179], v240 offset:8192\n" \
"v_mfma_f32_16x16x32_f16 v[60:63], v[132:135], v[160:163], v[60:63]\n" \
"s_waitcnt lgkmcnt(4)\n" \
"v_pk_add_f16 v160, v160, v164\n" \
"v_pk_add_f16 v161, v161, v165\n" \
"v_and_b32 v185, s92, v185\n" \
"v_mfma_f32_16x16x32_f16 v[52:55], v[152:155], v[164:167], v[52:55]\n" \
"v_pk_add_f16 v162, v162, v166\n" \
"v_pk_add_f16 v163, v163, v167\n" \
"v_and_b32 v186, s92, v186\n" \
"v_and_b32 v187, s92, v187\n" \
"v_mfma_f32_16x16x32_f16 v[64:67], v[156:159], v[164:167], v[64:67]\n" \
"ds_read_b128 v[180:183], v240 offset:8704\n" \
"buffer_store_dwordx4 v[184:187], v234, s[36:39], s82 offen offset:1024 sc1\n" \
"ds_read_b64_tr_b16 v[144:145], v241 offset:512\n" \
"v_mfma_f32_16x16x32_f16 v[56:59], v[136:139], v[160:163], v[56:59]\n" \
"buffer_load_dwordx4 v232, s[36:39], s80 offen offset:2048 sc1 lds\n" \
"v_mfma_f32_16x16x32_f16 v[68:71], v[140:143], v[160:163], v[68:71]\n" \
"ds_read_b64_tr_b16 v[146:147], v241 offset:8704\n" \
"ds_read_b64_tr_b16 v[148:149], v242 offset:512\n" \
"v_mfma_f32_16x16x32_f16 v[72:75], v[128:131], v[168:171], v[72:75]\n" \
"ds_read_b128 v[160:163], v240 offset:0\n" \
"ds_read_b64_tr_b16 v[150:151], v242 offset:8704\n" \
"v_mfma_f32_16x16x32_f16 v[84:87], v[132:135], v[168:171], v[84:87]\n" \
"s_waitcnt lgkmcnt(6)\n" \
"v_pk_add_f16 v168, v168, v172\n" \
"v_pk_add_f16 v169, v169, v173\n" \
"v_mfma_f32_16x16x32_f16 v[76:79], v[152:155], v[172:175], v[76:79]\n" \
"v_pk_add_f16 v170, v170, v174\n" \
"v_pk_add_f16 v171, v171, v175\n" \
"v_mfma_f32_16x16x32_f16 v[88:91], v[156:159], v[172:175], v[88:91]\n" \
"ds_read_b128 v[164:167], v240 offset:512\n" \
"v_mfma_f32_16x16x32_f16 v[80:83], v[136:139], v[168:171], v[80:83]\n" \
"buffer_load_dwordx4 v232, s[36:39], s80 offen offset:3072 sc1 lds\n" \
"v_mfma_f32_16x16x32_f16 v[92:95], v[140:143], v[168:171], v[92:95]\n" \
"v_mfma_f32_16x16x32_f16 v[96:99], v[128:131], v[176:179], v[96:99]\n" \
"s_waitcnt lgkmcnt(5)\n" \
"v_pk_add_f16 v128, v120, v144\n" \
"v_pk_add_f16 v129, v121, v145\n" \
"ds_read_b128 v[168:171], v240 offset:2048\n" \
"v_mfma_f32_16x16x32_f16 v[108:111], v[132:135], v[176:179], v[108:111]\n" \
"v_pk_add_f16 v176, v176, v180\n" \
"v_pk_add_f16 v177, v177, v181\n" \
"ds_read_b128 v[172:175], v240 offset:2560\n" \
"v_mfma_f32_16x16x32_f16 v[100:103], v[152:155], v[180:183], v[100:103]\n" \
"v_pk_add_f16 v178, v178, v182\n" \
"v_pk_add_f16 v179, v179, v183\n" \
"v_mfma_f32_16x16x32_f16 v[112:115], v[156:159], v[180:183], v[112:115]\n" \
"s_waitcnt lgkmcnt(3)\n" \
"v_pk_add_f16 v130, v122, v146\n" \
"v_pk_add_f16 v131, v123, v147\n" \
"v_mfma_f32_16x16x32_f16 v[104:107], v[136:139], v[176:179], v[104:107]\n" \
"v_pk_add_f16 v132, v124, v148\n" \
"v_pk_add_f16 v133, v125, v149\n" \
"v_mfma_f32_16x16x32_f16 v[116:119], v[140:143], v[176:179], v[116:119]\n" \
"v_pk_add_f16 v134, v126, v150\n" \
"v_pk_add_f16 v135, v127, v151\n" \
"s_waitcnt vmcnt(6) lgkmcnt(0)\n" \
"s_barrier\n" \
"v_mfma_f32_16x16x32_f16 v[0:3], v[120:123], v[160:163], v[0:3]\n" \
"ds_read_b128 v[176:179], v240 offset:24576\n" \
"s_mov_b32 s81, 0x8006000\n" \
"ds_read_b64_tr_b16 v[184:185], v236 offset:12288\n" \
"ds_read_b64_tr_b16 v[188:189], v237 offset:12288\n" \
"v_mfma_f32_16x16x32_f16 v[12:15], v[124:127], v[160:163], v[12:15]\n" \
"v_pk_add_f16 v160, v160, v164\n" \
"v_pk_add_f16 v161, v161, v165\n" \
"buffer_load_dword v253, v252, s[36:39], s81 offen sc1\n" \
"v_mfma_f32_16x16x32_f16 v[4:7], v[144:147], v[164:167], v[4:7]\n" \
"v_pk_add_f16 v162, v162, v166\n" \
"v_pk_add_f16 v163, v163, v167\n" \
"buffer_store_dwordx4 v[212:215], v235, s[36:39], s64 offen sc1\n" \
"v_mfma_f32_16x16x32_f16 v[16:19], v[148:151], v[164:167], v[16:19]\n" \
"ds_read_b128 v[180:183], v240 offset:25088\n" \
"s_add_i32 m0, s44, 0x0\n" \
"s_add_i32 s80, s46, 0x2000000\n" \
"v_mfma_f32_16x16x32_f16 v[8:11], v[128:131], v[160:163], v[8:11]\n" \
"buffer_load_dwordx4 v232, s[36:39], s80 offen sc1 lds\n" \
"s_waitcnt lgkmcnt(2)\n" \
"v_mfma_f32_16x16x16_bf16 v[184:187], v[184:185], v[192:193], 0\n" \
"v_mfma_f32_16x16x32_f16 v[20:23], v[132:135], v[160:163], v[20:23]\n" \
"ds_read_b64_tr_b16 v[136:137], v241 offset:16384\n" \
"ds_read_b64_tr_b16 v[138:139], v241 offset:24576\n" \
"s_waitcnt lgkmcnt(3)\n" \
"v_mfma_f32_16x16x16_bf16 v[188:191], v[188:189], v[192:193], 0\n" \
"v_mfma_f32_16x16x32_f16 v[24:27], v[120:123], v[168:171], v[24:27]\n" \
"ds_read_b128 v[160:163], v240 offset:26624\n" \
"v_mfma_f32_16x16x32_f16 v[36:39], v[124:127], v[168:171], v[36:39]\n" \
"v_pk_add_f16 v168, v168, v172\n" \
"v_pk_add_f16 v169, v169, v173\n" \
"ds_read_b64_tr_b16 v[140:141], v242 offset:16384\n" \
"v_mfma_f32_16x16x32_f16 v[28:31], v[144:147], v[172:175], v[28:31]\n" \
"v_pk_add_f16 v170, v170, v174\n" \
"v_pk_add_f16 v171, v171, v175\n" \
"v_cvt_pk_f16_f32 v184, v184, v185\n" \
"v_mfma_f32_16x16x32_f16 v[40:43], v[148:151], v[172:175], v[40:43]\n" \
"v_cvt_pk_f16_f32 v185, v186, v187\n" \
"v_cvt_pk_f16_f32 v186, v188, v189\n" \
"ds_read_b128 v[164:167], v240 offset:27136\n" \
"v_mfma_f32_16x16x32_f16 v[32:35], v[128:131], v[168:171], v[32:35]\n" \
"v_cvt_pk_f16_f32 v187, v190, v191\n" \
"v_add_u32 v184, s91, v184\n" \
"buffer_load_dwordx4 v232, s[36:39], s80 offen offset:1024 sc1 lds\n" \
"v_mfma_f32_16x16x32_f16 v[44:47], v[132:135], v[168:171], v[44:47]\n" \
"v_add_u32 v185, s91, v185\n" \
"v_add_u32 v186, s91, v186\n" \
"ds_read_b64_tr_b16 v[142:143], v242 offset:24576\n" \
"v_mfma_f32_16x16x32_f16 v[48:51], v[120:123], v[176:179], v[48:51]\n" \
"v_add_u32 v187, s91, v187\n" \
"v_and_b32 v184, s92, v184\n" \
"ds_read_b128 v[168:171], v240 offset:28672\n" \
"v_mfma_f32_16x16x32_f16 v[60:63], v[124:127], v[176:179], v[60:63]\n" \
"s_waitcnt lgkmcnt(4)\n" \
"v_pk_add_f16 v176, v176, v180\n" \
"v_pk_add_f16 v177, v177, v181\n" \
"v_and_b32 v185, s92, v185\n" \
"s_add_i32 s82, s48, 0x4000000\n" \
"v_mfma_f32_16x16x32_f16 v[52:55], v[144:147], v[180:183], v[52:55]\n" \
"v_pk_add_f16 v178, v178, v182\n" \
"v_pk_add_f16 v179, v179, v183\n" \
"v_and_b32 v186, s92, v186\n" \
"v_and_b32 v187, s92, v187\n" \
"v_mfma_f32_16x16x32_f16 v[64:67], v[148:151], v[180:183], v[64:67]\n" \
"ds_read_b128 v[172:175], v240 offset:29184\n" \
"buffer_store_dwordx4 v[184:187], v234, s[36:39], s82 offen sc1\n" \
"ds_read_b64_tr_b16 v[152:153], v241 offset:16896\n" \
"v_mfma_f32_16x16x32_f16 v[56:59], v[128:131], v[176:179], v[56:59]\n" \
"buffer_load_dwordx4 v232, s[36:39], s80 offen offset:2048 sc1 lds\n" \
"v_mfma_f32_16x16x32_f16 v[68:71], v[132:135], v[176:179], v[68:71]\n" \
"ds_read_b64_tr_b16 v[154:155], v241 offset:25088\n" \
"ds_read_b64_tr_b16 v[156:157], v242 offset:16896\n" \
"v_mfma_f32_16x16x32_f16 v[72:75], v[120:123], v[160:163], v[72:75]\n" \
"ds_read_b128 v[176:179], v240 offset:20480\n" \
"ds_read_b64_tr_b16 v[158:159], v242 offset:25088\n" \
"v_mfma_f32_16x16x32_f16 v[84:87], v[124:127], v[160:163], v[84:87]\n" \
"s_waitcnt lgkmcnt(6)\n" \
"v_pk_add_f16 v160, v160, v164\n" \
"v_pk_add_f16 v161, v161, v165\n" \
"v_mfma_f32_16x16x32_f16 v[76:79], v[144:147], v[164:167], v[76:79]\n" \
"v_pk_add_f16 v162, v162, v166\n" \
"v_pk_add_f16 v163, v163, v167\n" \
"v_mfma_f32_16x16x32_f16 v[88:91], v[148:151], v[164:167], v[88:91]\n" \
"ds_read_b128 v[180:183], v240 offset:20992\n" \
"v_mfma_f32_16x16x32_f16 v[80:83], v[128:131], v[160:163], v[80:83]\n" \
"buffer_load_dwordx4 v232, s[36:39], s80 offen offset:3072 sc1 lds\n" \
"v_mfma_f32_16x16x32_f16 v[92:95], v[132:135], v[160:163], v[92:95]\n" \
"v_mfma_f32_16x16x32_f16 v[96:99], v[120:123], v[168:171], v[96:99]\n" \
"s_waitcnt lgkmcnt(5)\n" \
"v_pk_add_f16 v120, v136, v152\n" \
"v_pk_add_f16 v121, v137, v153\n" \
"ds_read_b128 v[160:163], v240 offset:22528\n" \
"v_mfma_f32_16x16x32_f16 v[108:111], v[124:127], v[168:171], v[108:111]\n" \
"v_pk_add_f16 v168, v168, v172\n" \
"v_pk_add_f16 v169, v169, v173\n" \
"ds_read_b128 v[164:167], v240 offset:23040\n" \
"v_mfma_f32_16x16x32_f16 v[100:103], v[144:147], v[172:175], v[100:103]\n" \
"v_pk_add_f16 v170, v170, v174\n" \
"v_pk_add_f16 v171, v171, v175\n" \
"v_mfma_f32_16x16x32_f16 v[112:115], v[148:151], v[172:175], v[112:115]\n" \
"s_waitcnt lgkmcnt(3)\n" \
"v_pk_add_f16 v122, v138, v154\n" \
"v_pk_add_f16 v123, v139, v155\n" \
"v_mfma_f32_16x16x32_f16 v[104:107], v[128:131], v[168:171], v[104:107]\n" \
"v_pk_add_f16 v124, v140, v156\n" \
"v_pk_add_f16 v125, v141, v157\n" \
"v_mfma_f32_16x16x32_f16 v[116:119], v[132:135], v[168:171], v[116:119]\n" \
"v_pk_add_f16 v126, v142, v158\n" \
"v_pk_add_f16 v127, v143, v159\n" \
"s_waitcnt vmcnt(6) lgkmcnt(0)\n" \
"s_barrier\n" \
"v_mfma_f32_16x16x32_f16 v[0:3], v[136:139], v[176:179], v[0:3]\n" \
"ds_read_b128 v[168:171], v240 offset:45056\n" \
"ds_read_b64_tr_b16 v[184:185], v238 offset:12288\n" \
"ds_read_b64_tr_b16 v[188:189], v239 offset:12288\n" \
"v_mfma_f32_16x16x32_f16 v[12:15], v[140:143], v[176:179], v[12:15]\n" \
"v_pk_add_f16 v176, v176, v180\n" \
"v_pk_add_f16 v177, v177, v181\n" \
"v_mfma_f32_16x16x32_f16 v[4:7], v[152:155], v[180:183], v[4:7]\n" \
"v_pk_add_f16 v178, v178, v182\n" \
"v_pk_add_f16 v179, v179, v183\n" \
"v_cmp_eq_u32 vcc, 0, v253\n" \
"s_cbranch_vccnz .Ls03_%=\n" \
".Lr03_%=:\n" \
"v_mfma_f32_16x16x32_f16 v[16:19], v[156:159], v[180:183], v[16:19]\n" \
"ds_read_b128 v[172:175], v240 offset:45568\n" \
"s_add_i32 m0, s44, 0x4000\n" \
"s_add_i32 s80, s46, 0x2400000\n" \
"v_mfma_f32_16x16x32_f16 v[8:11], v[120:123], v[176:179], v[8:11]\n" \
"buffer_load_dwordx4 v232, s[36:39], s80 offen sc1 lds\n" \
"s_waitcnt lgkmcnt(2)\n" \
"v_mfma_f32_16x16x16_bf16 v[184:187], v[184:185], v[192:193], 0\n" \
"v_mfma_f32_16x16x32_f16 v[20:23], v[124:127], v[176:179], v[20:23]\n" \
"ds_read_b64_tr_b16 v[128:129], v241 offset:32768\n" \
"ds_read_b64_tr_b16 v[130:131], v241 offset:40960\n" \
"s_waitcnt lgkmcnt(3)\n" \
"v_mfma_f32_16x16x16_bf16 v[188:191], v[188:189], v[192:193], 0\n" \
"v_mfma_f32_16x16x32_f16 v[24:27], v[136:139], v[160:163], v[24:27]\n" \
"ds_read_b128 v[176:179], v240 offset:47104\n" \
"s_add_i32 s84, s55, 0x800a000\n" \
"v_mfma_f32_16x16x32_f16 v[36:39], v[140:143], v[160:163], v[36:39]\n" \
"v_pk_add_f16 v160, v160, v164\n" \
"v_pk_add_f16 v161, v161, v165\n" \
"buffer_store_dword v240, v243, s[36:39], s84 offen sc1\n" \
"v_mfma_f32_16x16x32_f16 v[28:31], v[152:155], v[164:167], v[28:31]\n" \
"v_pk_add_f16 v162, v162, v166\n" \
"v_pk_add_f16 v163, v163, v167\n" \
"v_cvt_pk_f16_f32 v184, v184, v185\n" \
"ds_read_b64_tr_b16 v[132:133], v242 offset:32768\n" \
"v_mfma_f32_16x16x32_f16 v[40:43], v[156:159], v[164:167], v[40:43]\n" \
"v_cvt_pk_f16_f32 v185, v186, v187\n" \
"v_cvt_pk_f16_f32 v186, v188, v189\n" \
"ds_read_b128 v[180:183], v240 offset:47616\n" \
"v_mfma_f32_16x16x32_f16 v[32:35], v[120:123], v[160:163], v[32:35]\n" \
"v_cvt_pk_f16_f32 v187, v190, v191\n" \
"v_add_u32 v184, s91, v184\n" \
"buffer_load_dwordx4 v232, s[36:39], s80 offen offset:1024 sc1 lds\n" \
"v_mfma_f32_16x16x32_f16 v[44:47], v[124:127], v[160:163], v[44:47]\n" \
"v_add_u32 v185, s91, v185\n" \
"v_add_u32 v186, s91, v186\n" \
"ds_read_b64_tr_b16 v[134:135], v242 offset:40960\n" \
"v_mfma_f32_16x16x32_f16 v[48:51], v[136:139], v[168:171], v[48:51]\n" \
"v_add_u32 v187, s91, v187\n" \
"v_and_b32 v184, s92, v184\n" \
"ds_read_b128 v[160:163], v240 offset:49152\n" \
"v_mfma_f32_16x16x32_f16 v[60:63], v[140:143], v[168:171], v[60:63]\n" \
"s_waitcnt lgkmcnt(4)\n" \
"v_pk_add_f16 v168, v168, v172\n" \
"v_pk_add_f16 v169, v169, v173\n" \
"v_and_b32 v185, s92, v185\n" \
"v_mfma_f32_16x16x32_f16 v[52:55], v[152:155], v[172:175], v[52:55]\n" \
"v_pk_add_f16 v170, v170, v174\n" \
"v_pk_add_f16 v171, v171, v175\n" \
"v_and_b32 v186, s92, v186\n" \
"v_and_b32 v187, s92, v187\n" \
"v_mfma_f32_16x16x32_f16 v[64:67], v[156:159], v[172:175], v[64:67]\n" \
"ds_read_b128 v[164:167], v240 offset:49664\n" \
"buffer_store_dwordx4 v[184:187], v234, s[36:39], s82 offen offset:1024 sc1\n" \
"ds_read_b64_tr_b16 v[144:145], v241 offset:33280\n" \
"v_mfma_f32_16x16x32_f16 v[56:59], v[120:123], v[168:171], v[56:59]\n" \
"buffer_load_dwordx4 v232, s[36:39], s80 offen offset:2048 sc1 lds\n" \
"v_mfma_f32_16x16x32_f16 v[68:71], v[124:127], v[168:171], v[68:71]\n" \
"ds_read_b64_tr_b16 v[146:147], v241 offset:41472\n" \
"ds_read_b64_tr_b16 v[148:149], v242 offset:33280\n" \
"v_mfma_f32_16x16x32_f16 v[72:75], v[136:139], v[176:179], v[72:75]\n" \
"ds_read_b128 v[168:171], v240 offset:40960\n" \
"ds_read_b64_tr_b16 v[150:151], v242 offset:41472\n" \
"v_mfma_f32_16x16x32_f16 v[84:87], v[140:143], v[176:179], v[84:87]\n" \
"s_waitcnt lgkmcnt(6)\n" \
"v_pk_add_f16 v176, v176, v180\n" \
"v_pk_add_f16 v177, v177, v181\n" \
"v_mfma_f32_16x16x32_f16 v[76:79], v[152:155], v[180:183], v[76:79]\n" \
"v_pk_add_f16 v178, v178, v182\n" \
"v_pk_add_f16 v179, v179, v183\n" \
"v_mfma_f32_16x16x32_f16 v[88:91], v[156:159], v[180:183], v[88:91]\n" \
"ds_read_b128 v[172:175], v240 offset:41472\n" \
"v_mfma_f32_16x16x32_f16 v[80:83], v[120:123], v[176:179], v[80:83]\n" \
"buffer_load_dwordx4 v232, s[36:39], s80 offen offset:3072 sc1 lds\n" \
"v_mfma_f32_16x16x32_f16 v[92:95], v[124:127], v[176:179], v[92:95]\n" \
"v_mfma_f32_16x16x32_f16 v[96:99], v[136:139], v[160:163], v[96:99]\n" \
"s_waitcnt lgkmcnt(5)\n" \
"v_pk_add_f16 v136, v128, v144\n" \
"v_pk_add_f16 v137, v129, v145\n" \
"ds_read_b128 v[176:179], v240 offset:43008\n" \
"v_mfma_f32_16x16x32_f16 v[108:111], v[140:143], v[160:163], v[108:111]\n" \
"v_pk_add_f16 v160, v160, v164\n" \
"v_pk_add_f16 v161, v161, v165\n" \
"ds_read_b128 v[180:183], v240 offset:43520\n" \
"v_mfma_f32_16x16x32_f16 v[100:103], v[152:155], v[164:167], v[100:103]\n" \
"v_pk_add_f16 v162, v162, v166\n" \
"v_pk_add_f16 v163, v163, v167\n" \
"v_mfma_f32_16x16x32_f16 v[112:115], v[156:159], v[164:167], v[112:115]\n" \
"s_waitcnt lgkmcnt(3)\n" \
"v_pk_add_f16 v138, v130, v146\n" \
"v_pk_add_f16 v139, v131, v147\n" \
"v_mfma_f32_16x16x32_f16 v[104:107], v[120:123], v[160:163], v[104:107]\n" \
"v_pk_add_f16 v140, v132, v148\n" \
"v_pk_add_f16 v141, v133, v149\n" \
"v_mfma_f32_16x16x32_f16 v[116:119], v[124:127], v[160:163], v[116:119]\n" \
"v_pk_add_f16 v142, v134, v150\n" \
"v_pk_add_f16 v143, v135, v151\n" \
"s_waitcnt vmcnt(6) lgkmcnt(0)\n" \
"s_barrier\n" \
"v_mfma_f32_16x16x32_f16 v[0:3], v[128:131], v[168:171], v[0:3]\n" \
"ds_read_b128 v[160:163], v240 offset:4096\n" \
"s_mov_b32 s81, 0x8008000\n" \
"ds_read_b64_tr_b16 v[184:185], v236 offset:16384\n" \
"ds_read_b64_tr_b16 v[188:189], v237 offset:16384\n" \
"v_mfma_f32_16x16x32_f16 v[12:15], v[132:135], v[168:171], v[12:15]\n" \
"v_pk_add_f16 v168, v168, v172\n" \
"v_pk_add_f16 v169, v169, v173\n" \
"buffer_load_dword v253, v252, s[36:39], s81 offen sc1\n" \
"v_mfma_f32_16x16x32_f16 v[4:7], v[144:147], v[172:175], v[4:7]\n" \
"v_pk_add_f16 v170, v170, v174\n" \
"v_pk_add_f16 v171, v171, v175\n" \
"buffer_store_dwordx4 v[216:219], v235, s[36:39], s65 offen sc1\n" \
"v_mfma_f32_16x16x32_f16 v[16:19], v[148:151], v[172:175], v[16:19]\n" \
"ds_read_b128 v[164:167], v240 offset:4608\n" \
"s_add_i32 m0, s44, 0x8000\n" \
"s_add_i32 s80, s46, 0x2800000\n" \
"v_mfma_f32_16x16x32_f16 v[8:11], v[136:139], v[168:171], v[8:11]\n" \
"buffer_load_dwordx4 v232, s[36:39], s80 offen sc1 lds\n" \
"s_waitcnt lgkmcnt(2)\n" \
"v_mfma_f32_16x16x16_bf16 v[184:187], v[184:185], v[192:193], 0\n" \
"v_mfma_f32_16x16x32_f16 v[20:23], v[140:143], v[168:171], v[20:23]\n" \
"ds_read_b64_tr_b16 v[120:121], v241 offset:0\n" \
"ds_read_b64_tr_b16 v[122:123], v241 offset:8192\n" \
"s_waitcnt lgkmcnt(3)\n" \
"v_mfma_f32_16x16x16_bf16 v[188:191], v[188:189], v[192:193], 0\n" \
"v_mfma_f32_16x16x32_f16 v[24:27], v[128:131], v[176:179], v[24:27]\n" \
"ds_read_b128 v[168:171], v240 offset:6144\n" \
"v_mfma_f32_16x16x32_f16 v[36:39], v[132:135], v[176:179], v[36:39]\n" \
"v_pk_add_f16 v176, v176, v180\n" \
"v_pk_add_f16 v177, v177, v181\n" \
"ds_read_b64_tr_b16 v[124:125], v242 offset:0\n" \
"v_mfma_f32_16x16x32_f16 v[28:31], v[144:147], v[180:183], v[28:31]\n" \
"v_pk_add_f16 v178, v178, v182\n" \
"v_pk_add_f16 v179, v179, v183\n" \
"v_cvt_pk_f16_f32 v184, v184, v185\n" \
"v_mfma_f32_16x16x32_f16 v[40:43], v[148:151], v[180:183], v[40:43]\n" \
"v_cvt_pk_f16_f32 v185, v186, v187\n" \
"v_cvt_pk_f16_f32 v186, v188, v189\n" \
"ds_read_b128 v[172:175], v240 offset:6656\n" \
"v_mfma_f32_16x16x32_f16 v[32:35], v[136:139], v[176:179], v[32:35]\n" \
"v_cvt_pk_f16_f32 v187, v190, v191\n" \
"v_add_u32 v184, s91, v184\n" \
"buffer_load_dwordx4 v232, s[36:39], s80 offen offset:1024 sc1 lds\n" \
"v_mfma_f32_16x16x32_f16 v[44:47], v[140:143], v[176:179], v[44:47]\n" \
"v_add_u32 v185, s91, v185\n" \
"v_add_u32 v186, s91, v186\n" \
"ds_read_b64_tr_b16 v[126:127], v242 offset:8192\n" \
"v_mfma_f32_16x16x32_f16 v[48:51], v[128:131], v[160:163], v[48:51]\n" \
"v_add_u32 v187, s91, v187\n" \
"v_and_b32 v184, s92, v184\n" \
"ds_read_b128 v[176:179], v240 offset:8192\n" \
"v_mfma_f32_16x16x32_f16 v[60:63], v[132:135], v[160:163], v[60:63]\n" \
"s_waitcnt lgkmcnt(4)\n" \
"v_pk_add_f16 v160, v160, v164\n" \
"v_pk_add_f16 v161, v161, v165\n" \
"v_and_b32 v185, s92, v185\n" \
"s_add_i32 s82, s49, 0x4800000\n" \
"v_mfma_f32_16x16x32_f16 v[52:55], v[144:147], v[164:167], v[52:55]\n" \
"v_pk_add_f16 v162, v162, v166\n" \
"v_pk_add_f16 v163, v163, v167\n" \
"v_and_b32 v186, s92, v186\n" \
"v_and_b32 v187, s92, v187\n" \
"v_mfma_f32_16x16x32_f16 v[64:67], v[148:151], v[164:167], v[64:67]\n" \
"ds_read_b128 v[180:183], v240 offset:8704\n" \
"buffer_store_dwordx4 v[184:187], v234, s[36:39], s82 offen sc1\n" \
"ds_read_b64_tr_b16 v[152:153], v241 offset:512\n" \
"v_mfma_f32_16x16x32_f16 v[56:59], v[136:139], v[160:163], v[56:59]\n" \
"buffer_load_dwordx4 v232, s[36:39], s80 offen offset:2048 sc1 lds\n" \
"v_mfma_f32_16x16x32_f16 v[68:71], v[140:143], v[160:163], v[68:71]\n" \
"ds_read_b64_tr_b16 v[154:155], v241 offset:8704\n" \
"ds_read_b64_tr_b16 v[156:157], v242 offset:512\n" \
"v_mfma_f32_16x16x32_f16 v[72:75], v[128:131], v[168:171], v[72:75]\n" \
"ds_read_b128 v[160:163], v240 offset:0\n" \
"ds_read_b64_tr_b16 v[158:159], v242 offset:8704\n" \
"v_mfma_f32_16x16x32_f16 v[84:87], v[132:135], v[168:171], v[84:87]\n" \
"s_waitcnt lgkmcnt(6)\n" \
"v_pk_add_f16 v168, v168, v172\n" \
"v_pk_add_f16 v169, v169, v173\n" \
"v_mfma_f32_16x16x32_f16 v[76:79], v[144:147], v[172:175], v[76:79]\n" \
"v_pk_add_f16 v170, v170, v174\n" \
"v_pk_add_f16 v171, v171, v175\n" \
"v_mfma_f32_16x16x32_f16 v[88:91], v[148:151], v[172:175], v[88:91]\n" \
"ds_read_b128 v[164:167], v240 offset:512\n" \
"v_mfma_f32_16x16x32_f16 v[80:83], v[136:139], v[168:171], v[80:83]\n" \
"buffer_load_dwordx4 v232, s[36:39], s80 offen offset:3072 sc1 lds\n" \
"v_mfma_f32_16x16x32_f16 v[92:95], v[140:143], v[168:171], v[92:95]\n" \
"v_mfma_f32_16x16x32_f16 v[96:99], v[128:131], v[176:179], v[96:99]\n" \
"s_waitcnt lgkmcnt(5)\n" \
"v_pk_add_f16 v128, v120, v152\n" \
"v_pk_add_f16 v129, v121, v153\n" \
"ds_read_b128 v[168:171], v240 offset:2048\n" \
"v_mfma_f32_16x16x32_f16 v[108:111], v[132:135], v[176:179], v[108:111]\n" \
"v_pk_add_f16 v176, v176, v180\n" \
"v_pk_add_f16 v177, v177, v181\n" \
"ds_read_b128 v[172:175], v240 offset:2560\n" \
"v_mfma_f32_16x16x32_f16 v[100:103], v[144:147], v[180:183], v[100:103]\n" \
"v_pk_add_f16 v178, v178, v182\n" \
"v_pk_add_f16 v179, v179, v183\n" \
"v_mfma_f32_16x16x32_f16 v[112:115], v[148:151], v[180:183], v[112:115]\n" \
"s_waitcnt lgkmcnt(3)\n" \
"v_pk_add_f16 v130, v122, v154\n" \
"v_pk_add_f16 v131, v123, v155\n" \
"v_mfma_f32_16x16x32_f16 v[104:107], v[136:139], v[176:179], v[104:107]\n" \
"v_pk_add_f16 v132, v124, v156\n" \
"v_pk_add_f16 v133, v125, v157\n" \
"v_mfma_f32_16x16x32_f16 v[116:119], v[140:143], v[176:179], v[116:119]\n" \
"v_pk_add_f16 v134, v126, v158\n" \
"v_pk_add_f16 v135, v127, v159\n" \
"s_waitcnt vmcnt(6) lgkmcnt(0)\n" \
"s_barrier\n" \
"v_mfma_f32_16x16x32_f16 v[0:3], v[120:123], v[160:163], v[0:3]\n" \
"ds_read_b128 v[176:179], v240 offset:24576\n" \
"ds_read_b64_tr_b16 v[184:185], v238 offset:16384\n" \
"ds_read_b64_tr_b16 v[188:189], v239 offset:16384\n" \
"v_mfma_f32_16x16x32_f16 v[12:15], v[124:127], v[160:163], v[12:15]\n" \
"v_pk_add_f16 v160, v160, v164\n" \
"v_pk_add_f16 v161, v161, v165\n" \
"v_mfma_f32_16x16x32_f16 v[4:7], v[152:155], v[164:167], v[4:7]\n" \
"v_pk_add_f16 v162, v162, v166\n" \
"v_pk_add_f16 v163, v163, v167\n" \
"v_cmp_eq_u32 vcc, 0, v253\n" \
"s_cbranch_vccnz .Ls04_%=\n" \
".Lr04_%=:\n" \
"v_mfma_f32_16x16x32_f16 v[16:19], v[156:159], v[164:167], v[16:19]\n" \
"ds_read_b128 v[180:183], v240 offset:25088\n" \
"s_add_i32 m0, s44, 0x0\n" \
"s_add_i32 s80, s46, 0x2c00000\n" \
"v_mfma_f32_16x16x32_f16 v[8:11], v[128:131], v[160:163], v[8:11]\n" \
"buffer_load_dwordx4 v232, s[36:39], s80 offen sc1 lds\n" \
"s_waitcnt lgkmcnt(2)\n" \
"v_mfma_f32_16x16x16_bf16 v[184:187], v[184:185], v[192:193], 0\n" \
"v_mfma_f32_16x16x32_f16 v[20:23], v[132:135], v[160:163], v[20:23]\n" \
"ds_read_b64_tr_b16 v[136:137], v241 offset:16384\n" \
"ds_read_b64_tr_b16 v[138:139], v241 offset:24576\n" \
"s_waitcnt lgkmcnt(3)\n" \
"v_mfma_f32_16x16x16_bf16 v[188:191], v[188:189], v[192:193], 0\n" \
"v_mfma_f32_16x16x32_f16 v[24:27], v[120:123], v[168:171], v[24:27]\n" \
"ds_read_b128 v[160:163], v240 offset:26624\n" \
"s_add_i32 s84, s52, 0x800c000\n" \
"v_mfma_f32_16x16x32_f16 v[36:39], v[124:127], v[168:171], v[36:39]\n" \
"v_pk_add_f16 v168, v168, v172\n" \
"v_pk_add_f16 v169, v169, v173\n" \
"buffer_store_dword v240, v243, s[36:39], s84 offen sc1\n" \
"v_mfma_f32_16x16x32_f16 v[28:31], v[152:155], v[172:175], v[28:31]\n" \
"v_pk_add_f16 v170, v170, v174\n" \
"v_pk_add_f16 v171, v171, v175\n" \
"v_cvt_pk_f16_f32 v184, v184, v185\n" \
"ds_read_b64_tr_b16 v[140:141], v242 offset:16384\n" \
"v_mfma_f32_16x16x32_f16 v[40:43], v[156:159], v[172:175], v[40:43]\n" \
"v_cvt_pk_f16_f32 v185, v186, v187\n" \
"v_cvt_pk_f16_f32 v186, v188, v189\n" \
"ds_read_b128 v[164:167], v240 offset:27136\n" \
"v_mfma_f32_16x16x32_f16 v[32:35], v[128:131], v[168:171], v[32:35]\n" \
"v_cvt_pk_f16_f32 v187, v190, v191\n" \
"v_add_u32 v184, s91, v184\n" \
"buffer_load_dwordx4 v232, s[36:39], s80 offen offset:1024 sc1 lds\n" \
"v_mfma_f32_16x16x32_f16 v[44:47], v[132:135], v[168:171], v[44:47]\n" \
"v_add_u32 v185, s91, v185\n" \
"v_add_u32 v186, s91, v186\n" \
"ds_read_b64_tr_b16 v[142:143], v242 offset:24576\n" \
"v_mfma_f32_16x16x32_f16 v[48:51], v[120:123], v[176:179], v[48:51]\n" \
"v_add_u32 v187, s91, v187\n" \
"v_and_b32 v184, s92, v184\n" \
"ds_read_b128 v[168:171], v240 offset:28672\n" \
"v_mfma_f32_16x16x32_f16 v[60:63], v[124:127], v[176:179], v[60:63]\n" \
"s_waitcnt lgkmcnt(4)\n" \
"v_pk_add_f16 v176, v176, v180\n" \
"v_pk_add_f16 v177, v177, v181\n" \
"v_and_b32 v185, s92, v185\n" \
"v_mfma_f32_16x16x32_f16 v[52:55], v[152:155], v[180:183], v[52:55]\n" \
"v_pk_add_f16 v178, v178, v182\n" \
"v_pk_add_f16 v179, v179, v183\n" \
"v_and_b32 v186, s92, v186\n" \
"v_and_b32 v187, s92, v187\n" \
"v_mfma_f32_16x16x32_f16 v[64:67], v[156:159], v[180:183], v[64:67]\n" \
"ds_read_b128 v[172:175], v240 offset:29184\n" \
"buffer_store_dwordx4 v[184:187], v234, s[36:39], s82 offen offset:1024 sc1\n" \
"ds_read_b64_tr_b16 v[144:145], v241 offset:16896\n" \
"v_mfma_f32_16x16x32_f16 v[56:59], v[128:131], v[176:179], v[56:59]\n" \
"buffer_load_dwordx4 v232, s[36:39], s80 offen offset:2048 sc1 lds\n" \
"v_mfma_f32_16x16x32_f16 v[68:71], v[132:135], v[176:179], v[68:71]\n" \
"ds_read_b64_tr_b16 v[146:147], v241 offset:25088\n" \
"ds_read_b64_tr_b16 v[148:149], v242 offset:16896\n" \
"v_mfma_f32_16x16x32_f16 v[72:75], v[120:123], v[160:163], v[72:75]\n" \
"ds_read_b128 v[176:179], v240 offset:20480\n" \
"ds_read_b64_tr_b16 v[150:151], v242 offset:25088\n" \
"v_mfma_f32_16x16x32_f16 v[84:87], v[124:127], v[160:163], v[84:87]\n" \
"s_waitcnt lgkmcnt(6)\n" \
"v_pk_add_f16 v160, v160, v164\n" \
"v_pk_add_f16 v161, v161, v165\n" \
"v_mfma_f32_16x16x32_f16 v[76:79], v[152:155], v[164:167], v[76:79]\n" \
"v_pk_add_f16 v162, v162, v166\n" \
"v_pk_add_f16 v163, v163, v167\n" \
"v_mfma_f32_16x16x32_f16 v[88:91], v[156:159], v[164:167], v[88:91]\n" \
"ds_read_b128 v[180:183], v240 offset:20992\n" \
"v_mfma_f32_16x16x32_f16 v[80:83], v[128:131], v[160:163], v[80:83]\n" \
"buffer_load_dwordx4 v232, s[36:39], s80 offen offset:3072 sc1 lds\n" \
"v_mfma_f32_16x16x32_f16 v[92:95], v[132:135], v[160:163], v[92:95]\n" \
"v_mfma_f32_16x16x32_f16 v[96:99], v[120:123], v[168:171], v[96:99]\n" \
"s_waitcnt lgkmcnt(5)\n" \
"v_pk_add_f16 v120, v136, v144\n" \
"v_pk_add_f16 v121, v137, v145\n" \
"ds_read_b128 v[160:163], v240 offset:22528\n" \
"v_mfma_f32_16x16x32_f16 v[108:111], v[124:127], v[168:171], v[108:111]\n" \
"v_pk_add_f16 v168, v168, v172\n" \
"v_pk_add_f16 v169, v169, v173\n" \
"ds_read_b128 v[164:167], v240 offset:23040\n" \
"v_mfma_f32_16x16x32_f16 v[100:103], v[152:155], v[172:175], v[100:103]\n" \
"v_pk_add_f16 v170, v170, v174\n" \
"v_pk_add_f16 v171, v171, v175\n" \
"v_mfma_f32_16x16x32_f16 v[112:115], v[156:159], v[172:175], v[112:115]\n" \
"s_waitcnt lgkmcnt(3)\n" \
"v_pk_add_f16 v122, v138, v146\n" \
"v_pk_add_f16 v123, v139, v147\n" \
"v_mfma_f32_16x16x32_f16 v[104:107], v[128:131], v[168:171], v[104:107]\n" \
"v_pk_add_f16 v124, v140, v148\n" \
"v_pk_add_f16 v125, v141, v149\n" \
"v_mfma_f32_16x16x32_f16 v[116:119], v[132:135], v[168:171], v[116:119]\n" \
"v_pk_add_f16 v126, v142, v150\n" \
"v_pk_add_f16 v127, v143, v151\n" \
"s_waitcnt vmcnt(6) lgkmcnt(0)\n" \
"s_barrier\n" \
"v_mfma_f32_16x16x32_f16 v[0:3], v[136:139], v[176:179], v[0:3]\n" \
"ds_read_b128 v[168:171], v240 offset:45056\n" \
"s_mov_b32 s81, 0x800a000\n" \
"ds_read_b64_tr_b16 v[184:185], v236 offset:20480\n" \
"ds_read_b64_tr_b16 v[188:189], v237 offset:20480\n" \
"v_mfma_f32_16x16x32_f16 v[12:15], v[140:143], v[176:179], v[12:15]\n" \
"v_pk_add_f16 v176, v176, v180\n" \
"v_pk_add_f16 v177, v177, v181\n" \
"buffer_load_dword v253, v252, s[36:39], s81 offen sc1\n" \
"v_mfma_f32_16x16x32_f16 v[4:7], v[144:147], v[180:183], v[4:7]\n" \
"v_pk_add_f16 v178, v178, v182\n" \
"v_pk_add_f16 v179, v179, v183\n" \
"buffer_store_dwordx4 v[220:223], v235, s[36:39], s66 offen sc1\n" \
"v_mfma_f32_16x16x32_f16 v[16:19], v[148:151], v[180:183], v[16:19]\n" \
"ds_read_b128 v[172:175], v240 offset:45568\n" \
"s_add_i32 m0, s44, 0x4000\n" \
"s_add_i32 s80, s46, 0x3000000\n" \
"v_mfma_f32_16x16x32_f16 v[8:11], v[120:123], v[176:179], v[8:11]\n" \
"buffer_load_dwordx4 v232, s[36:39], s80 offen sc1 lds\n" \
"s_waitcnt lgkmcnt(2)\n" \
"v_mfma_f32_16x16x16_bf16 v[184:187], v[184:185], v[192:193], 0\n" \
"v_mfma_f32_16x16x32_f16 v[20:23], v[124:127], v[176:179], v[20:23]\n" \
"ds_read_b64_tr_b16 v[128:129], v241 offset:32768\n" \
"ds_read_b64_tr_b16 v[130:131], v241 offset:40960\n" \
"s_waitcnt lgkmcnt(3)\n" \
"v_mfma_f32_16x16x16_bf16 v[188:191], v[188:189], v[192:193], 0\n" \
"v_mfma_f32_16x16x32_f16 v[24:27], v[136:139], v[160:163], v[24:27]\n" \
"ds_read_b128 v[176:179], v240 offset:47104\n" \
"v_mfma_f32_16x16x32_f16 v[36:39], v[140:143], v[160:163], v[36:39]\n" \
"v_pk_add_f16 v160, v160, v164\n" \
"v_pk_add_f16 v161, v161, v165\n" \
"ds_read_b64_tr_b16 v[132:133], v242 offset:32768\n" \
"v_mfma_f32_16x16x32_f16 v[28:31], v[144:147], v[164:167], v[28:31]\n" \
"v_pk_add_f16 v162, v162, v166\n" \
"v_pk_add_f16 v163, v163, v167\n" \
"v_cvt_pk_f16_f32 v184, v184, v185\n" \
"v_mfma_f32_16x16x32_f16 v[40:43], v[148:151], v[164:167], v[40:43]\n" \
"v_cvt_pk_f16_f32 v185, v186, v187\n" \
"v_cvt_pk_f16_f32 v186, v188, v189\n" \
"ds_read_b128 v[180:183], v240 offset:47616\n" \
"v_mfma_f32_16x16x32_f16 v[32:35], v[120:123], v[160:163], v[32:35]\n" \
"v_cvt_pk_f16_f32 v187, v190, v191\n" \
"v_add_u32 v184, s91, v184\n" \
"buffer_load_dwordx4 v232, s[36:39], s80 offen offset:1024 sc1 lds\n" \
"v_mfma_f32_16x16x32_f16 v[44:47], v[124:127], v[160:163], v[44:47]\n" \
"v_add_u32 v185, s91, v185\n" \
"v_add_u32 v186, s91, v186\n" \
"ds_read_b64_tr_b16 v[134:135], v242 offset:40960\n" \
"v_mfma_f32_16x16x32_f16 v[48:51], v[136:139], v[168:171], v[48:51]\n" \
"v_add_u32 v187, s91, v187\n" \
"v_and_b32 v184, s92, v184\n" \
"ds_read_b128 v[160:163], v240 offset:49152\n" \
"v_mfma_f32_16x16x32_f16 v[60:63], v[140:143], v[168:171], v[60:63]\n" \
"s_waitcnt lgkmcnt(4)\n" \
"v_pk_add_f16 v168, v168, v172\n" \
"v_pk_add_f16 v169, v169, v173\n" \
"v_and_b32 v185, s92, v185\n" \
"s_add_i32 s82, s50, 0x5000000\n" \
"v_mfma_f32_16x16x32_f16 v[52:55], v[144:147], v[172:175], v[52:55]\n" \
"v_pk_add_f16 v170, v170, v174\n" \
"v_pk_add_f16 v171, v171, v175\n" \
"v_and_b32 v186, s92, v186\n" \
"v_and_b32 v187, s92, v187\n" \
"v_mfma_f32_16x16x32_f16 v[64:67], v[148:151], v[172:175], v[64:67]\n" \
"ds_read_b128 v[164:167], v240 offset:49664\n" \
"buffer_store_dwordx4 v[184:187], v234, s[36:39], s82 offen sc1\n" \
"ds_read_b64_tr_b16 v[152:153], v241 offset:33280\n" \
"v_mfma_f32_16x16x32_f16 v[56:59], v[120:123], v[168:171], v[56:59]\n" \
"buffer_load_dwordx4 v232, s[36:39], s80 offen offset:2048 sc1 lds\n" \
"v_mfma_f32_16x16x32_f16 v[68:71], v[124:127], v[168:171], v[68:71]\n" \
"ds_read_b64_tr_b16 v[154:155], v241 offset:41472\n" \
"ds_read_b64_tr_b16 v[156:157], v242 offset:33280\n" \
"v_mfma_f32_16x16x32_f16 v[72:75], v[136:139], v[176:179], v[72:75]\n" \
"ds_read_b128 v[168:171], v240 offset:40960\n" \
"ds_read_b64_tr_b16 v[158:159], v242 offset:41472\n" \
"v_mfma_f32_16x16x32_f16 v[84:87], v[140:143], v[176:179], v[84:87]\n" \
"s_waitcnt lgkmcnt(6)\n" \
"v_pk_add_f16 v176, v176, v180\n" \
"v_pk_add_f16 v177, v177, v181\n" \
"v_mfma_f32_16x16x32_f16 v[76:79], v[144:147], v[180:183], v[76:79]\n" \
"v_pk_add_f16 v178, v178, v182\n" \
"v_pk_add_f16 v179, v179, v183\n" \
"v_mfma_f32_16x16x32_f16 v[88:91], v[148:151], v[180:183], v[88:91]\n" \
"ds_read_b128 v[172:175], v240 offset:41472\n" \
"v_mfma_f32_16x16x32_f16 v[80:83], v[120:123], v[176:179], v[80:83]\n" \
"buffer_load_dwordx4 v232, s[36:39], s80 offen offset:3072 sc1 lds\n" \
"v_mfma_f32_16x16x32_f16 v[92:95], v[124:127], v[176:179], v[92:95]\n" \
"v_mfma_f32_16x16x32_f16 v[96:99], v[136:139], v[160:163], v[96:99]\n" \
"s_waitcnt lgkmcnt(5)\n" \
"v_pk_add_f16 v136, v128, v152\n" \
"v_pk_add_f16 v137, v129, v153\n" \
"ds_read_b128 v[176:179], v240 offset:43008\n" \
"v_mfma_f32_16x16x32_f16 v[108:111], v[140:143], v[160:163], v[108:111]\n" \
"v_pk_add_f16 v160, v160, v164\n" \
"v_pk_add_f16 v161, v161, v165\n" \
"ds_read_b128 v[180:183], v240 offset:43520\n" \
"v_mfma_f32_16x16x32_f16 v[100:103], v[144:147], v[164:167], v[100:103]\n" \
"v_pk_add_f16 v162, v162, v166\n" \
"v_pk_add_f16 v163, v163, v167\n" \
"v_mfma_f32_16x16x32_f16 v[112:115], v[148:151], v[164:167], v[112:115]\n" \
"s_waitcnt lgkmcnt(3)\n" \
"v_pk_add_f16 v138, v130, v154\n" \
"v_pk_add_f16 v139, v131, v155\n" \
"v_mfma_f32_16x16x32_f16 v[104:107], v[120:123], v[160:163], v[104:107]\n" \
"v_pk_add_f16 v140, v132, v156\n" \
"v_pk_add_f16 v141, v133, v157\n" \
"v_mfma_f32_16x16x32_f16 v[116:119], v[124:127], v[160:163], v[116:119]\n" \
"v_pk_add_f16 v142, v134, v158\n" \
"v_pk_add_f16 v143, v135, v159\n" \
"s_waitcnt vmcnt(6) lgkmcnt(0)\n" \
"s_barrier\n" \
"v_mfma_f32_16x16x32_f16 v[0:3], v[128:131], v[168:171], v[0:3]\n" \
"ds_read_b128 v[160:163], v240 offset:4096\n" \
"ds_read_b64_tr_b16 v[184:185], v238 offset:20480\n" \
"ds_read_b64_tr_b16 v[188:189], v239 offset:20480\n" \
"v_mfma_f32_16x16x32_f16 v[12:15], v[132:135], v[168:171], v[12:15]\n" \
"v_pk_add_f16 v168, v168, v172\n" \
"v_pk_add_f16 v169, v169, v173\n" \
"v_mfma_f32_16x16x32_f16 v[4:7], v[152:155], v[172:175], v[4:7]\n" \
"v_pk_add_f16 v170, v170, v174\n" \
"v_pk_add_f16 v171, v171, v175\n" \
"v_cmp_eq_u32 vcc, 0, v253\n" \
"s_cbranch_vccnz .Ls05_%=\n" \
".Lr05_%=:\n" \
"v_mfma_f32_16x16x32_f16 v[16:19], v[156:159], v[172:175], v[16:19]\n" \
"ds_read_b128 v[164:167], v240 offset:4608\n" \
"s_add_i32 m0, s44, 0x8000\n" \
"s_add_i32 s80, s46, 0x3400000\n" \
"v_mfma_f32_16x16x32_f16 v[8:11], v[136:139], v[168:171], v[8:11]\n" \
"buffer_load_dwordx4 v232, s[36:39], s80 offen sc1 lds\n" \
"s_waitcnt lgkmcnt(2)\n" \
"v_mfma_f32_16x16x16_bf16 v[184:187], v[184:185], v[192:193], 0\n" \
"v_mfma_f32_16x16x32_f16 v[20:23], v[140:143], v[168:171], v[20:23]\n" \
"ds_read_b64_tr_b16 v[120:121], v241 offset:0\n" \
"ds_read_b64_tr_b16 v[122:123], v241 offset:8192\n" \
"s_waitcnt lgkmcnt(3)\n" \
"v_mfma_f32_16x16x16_bf16 v[188:191], v[188:189], v[192:193], 0\n" \
"v_mfma_f32_16x16x32_f16 v[24:27], v[128:131], v[176:179], v[24:27]\n" \
"ds_read_b128 v[168:171], v240 offset:6144\n" \
"s_add_i32 s84, s53, 0x800e000\n" \
"v_mfma_f32_16x16x32_f16 v[36:39], v[132:135], v[176:179], v[36:39]\n" \
"v_pk_add_f16 v176, v176, v180\n" \
"v_pk_add_f16 v177, v177, v181\n" \
"buffer_store_dword v240, v243, s[36:39], s84 offen sc1\n" \
"v_mfma_f32_16x16x32_f16 v[28:31], v[152:155], v[180:183], v[28:31]\n" \
"v_pk_add_f16 v178, v178, v182\n" \
"v_pk_add_f16 v179, v179, v183\n" \
"v_cvt_pk_f16_f32 v184, v184, v185\n" \
"ds_read_b64_tr_b16 v[124:125], v242 offset:0\n" \
"v_mfma_f32_16x16x32_f16 v[40:43], v[156:159], v[180:183], v[40:43]\n" \
"v_cvt_pk_f16_f32 v185, v186, v187\n" \
"v_cvt_pk_f16_f32 v186, v188, v189\n" \
"ds_read_b128 v[172:175], v240 offset:6656\n" \
"v_mfma_f32_16x16x32_f16 v[32:35], v[136:139], v[176:179], v[32:35]\n" \
"v_cvt_pk_f16_f32 v187, v190, v191\n" \
"v_add_u32 v184, s91, v184\n" \
"buffer_load_dwordx4 v232, s[36:39], s80 offen offset:1024 sc1 lds\n" \
"v_mfma_f32_16x16x32_f16 v[44:47], v[140:143], v[176:179], v[44:47]\n" \
"v_add_u32 v185, s91, v185\n" \
"v_add_u32 v186, s91, v186\n" \
"ds_read_b64_tr_b16 v[126:127], v242 offset:8192\n" \
"v_mfma_f32_16x16x32_f16 v[48:51], v[128:131], v[160:163], v[48:51]\n" \
"v_add_u32 v187, s91, v187\n" \
"v_and_b32 v184, s92, v184\n" \
"ds_read_b128 v[176:179], v240 offset:8192\n" \
"v_mfma_f32_16x16x32_f16 v[60:63], v[132:135], v[160:163], v[60:63]\n" \
"s_waitcnt lgkmcnt(4)\n" \
"v_pk_add_f16 v160, v160, v164\n" \
"v_pk_add_f16 v161, v161, v165\n" \
"v_and_b32 v185, s92, v185\n" \
"v_mfma_f32_16x16x32_f16 v[52:55], v[152:155], v[164:167], v[52:55]\n" \
"v_pk_add_f16 v162, v162, v166\n" \
"v_pk_add_f16 v163, v163, v167\n" \
"v_and_b32 v186, s92, v186\n" \
"v_and_b32 v187, s92, v187\n" \
"v_mfma_f32_16x16x32_f16 v[64:67], v[156:159], v[164:167], v[64:67]\n" \
"ds_read_b128 v[180:183], v240 offset:8704\n" \
"buffer_store_dwordx4 v[184:187], v234, s[36:39], s82 offen offset:1024 sc1\n" \
"ds_read_b64_tr_b16 v[144:145], v241 offset:512\n" \
"v_mfma_f32_16x16x32_f16 v[56:59], v[136:139], v[160:163], v[56:59]\n" \
"buffer_load_dwordx4 v232, s[36:39], s80 offen offset:2048 sc1 lds\n" \
"v_mfma_f32_16x16x32_f16 v[68:71], v[140:143], v[160:163], v[68:71]\n" \
"ds_read_b64_tr_b16 v[146:147], v241 offset:8704\n" \
"ds_read_b64_tr_b16 v[148:149], v242 offset:512\n" \
"v_mfma_f32_16x16x32_f16 v[72:75], v[128:131], v[168:171], v[72:75]\n" \
"ds_read_b128 v[160:163], v240 offset:0\n" \
"ds_read_b64_tr_b16 v[150:151], v242 offset:8704\n" \
"v_mfma_f32_16x16x32_f16 v[84:87], v[132:135], v[168:171], v[84:87]\n" \
"s_waitcnt lgkmcnt(6)\n" \
"v_pk_add_f16 v168, v168, v172\n" \
"v_pk_add_f16 v169, v169, v173\n" \
"v_mfma_f32_16x16x32_f16 v[76:79], v[152:155], v[172:175], v[76:79]\n" \
"v_pk_add_f16 v170, v170, v174\n" \
"v_pk_add_f16 v171, v171, v175\n" \
"v_mfma_f32_16x16x32_f16 v[88:91], v[156:159], v[172:175], v[88:91]\n" \
"ds_read_b128 v[164:167], v240 offset:512\n" \
"v_mfma_f32_16x16x32_f16 v[80:83], v[136:139], v[168:171], v[80:83]\n" \
"buffer_load_dwordx4 v232, s[36:39], s80 offen offset:3072 sc1 lds\n" \
"v_mfma_f32_16x16x32_f16 v[92:95], v[140:143], v[168:171], v[92:95]\n" \
"v_mfma_f32_16x16x32_f16 v[96:99], v[128:131], v[176:179], v[96:99]\n" \
"s_waitcnt lgkmcnt(5)\n" \
"v_pk_add_f16 v128, v120, v144\n" \
"v_pk_add_f16 v129, v121, v145\n" \
"ds_read_b128 v[168:171], v240 offset:2048\n" \
"v_mfma_f32_16x16x32_f16 v[108:111], v[132:135], v[176:179], v[108:111]\n" \
"v_pk_add_f16 v176, v176, v180\n" \
"v_pk_add_f16 v177, v177, v181\n" \
"ds_read_b128 v[172:175], v240 offset:2560\n" \
"v_mfma_f32_16x16x32_f16 v[100:103], v[152:155], v[180:183], v[100:103]\n" \
"v_pk_add_f16 v178, v178, v182\n" \
"v_pk_add_f16 v179, v179, v183\n" \
"v_mfma_f32_16x16x32_f16 v[112:115], v[156:159], v[180:183], v[112:115]\n" \
"s_waitcnt lgkmcnt(3)\n" \
"v_pk_add_f16 v130, v122, v146\n" \
"v_pk_add_f16 v131, v123, v147\n" \
"v_mfma_f32_16x16x32_f16 v[104:107], v[136:139], v[176:179], v[104:107]\n" \
"v_pk_add_f16 v132, v124, v148\n" \
"v_pk_add_f16 v133, v125, v149\n" \
"v_mfma_f32_16x16x32_f16 v[116:119], v[140:143], v[176:179], v[116:119]\n" \
"v_pk_add_f16 v134, v126, v150\n" \
"v_pk_add_f16 v135, v127, v151\n" \
"s_waitcnt vmcnt(6) lgkmcnt(0)\n" \
"s_barrier\n" \
"v_mfma_f32_16x16x32_f16 v[0:3], v[120:123], v[160:163], v[0:3]\n" \
"ds_read_b128 v[176:179], v240 offset:24576\n" \
"ds_read_b64_tr_b16 v[184:185], v236 offset:24576\n" \
"ds_read_b64_tr_b16 v[188:189], v237 offset:24576\n" \
"v_mfma_f32_16x16x32_f16 v[12:15], v[124:127], v[160:163], v[12:15]\n" \
"v_pk_add_f16 v160, v160, v164\n" \
"v_pk_add_f16 v161, v161, v165\n" \
"v_mfma_f32_16x16x32_f16 v[4:7], v[144:147], v[164:167], v[4:7]\n" \
"v_pk_add_f16 v162, v162, v166\n" \
"v_pk_add_f16 v163, v163, v167\n" \
"buffer_store_dwordx4 v[224:227], v235, s[36:39], s67 offen sc1\n" \
"v_mfma_f32_16x16x32_f16 v[16:19], v[148:151], v[164:167], v[16:19]\n" \
"ds_read_b128 v[180:183], v240 offset:25088\n" \
"s_add_i32 m0, s44, 0x0\n" \
"s_add_i32 s80, s46, 0x3800000\n" \
"v_mfma_f32_16x16x32_f16 v[8:11], v[128:131], v[160:163], v[8:11]\n" \
"buffer_load_dwordx4 v232, s[36:39], s80 offen sc1 lds\n" \
"s_waitcnt lgkmcnt(2)\n" \
"v_mfma_f32_16x16x16_bf16 v[184:187], v[184:185], v[192:193], 0\n" \
"v_mfma_f32_16x16x32_f16 v[20:23], v[132:135], v[160:163], v[20:23]\n" \
"ds_read_b64_tr_b16 v[136:137], v241 offset:16384\n" \
"ds_read_b64_tr_b16 v[138:139], v241 offset:24576\n" \
"s_waitcnt lgkmcnt(3)\n" \
"v_mfma_f32_16x16x16_bf16 v[188:191], v[188:189], v[192:193], 0\n" \
"v_mfma_f32_16x16x32_f16 v[24:27], v[120:123], v[168:171], v[24:27]\n" \
"ds_read_b128 v[160:163], v240 offset:26624\n" \
"v_mfma_f32_16x16x32_f16 v[36:39], v[124:127], v[168:171], v[36:39]\n" \
"v_pk_add_f16 v168, v168, v172\n" \
"v_pk_add_f16 v169, v169, v173\n" \
"ds_read_b64_tr_b16 v[140:141], v242 offset:16384\n" \
"v_mfma_f32_16x16x32_f16 v[28:31], v[144:147], v[172:175], v[28:31]\n" \
"v_pk_add_f16 v170, v170, v174\n" \
"v_pk_add_f16 v171, v171, v175\n" \
"v_cvt_pk_f16_f32 v184, v184, v185\n" \
"v_mfma_f32_16x16x32_f16 v[40:43], v[148:151], v[172:175], v[40:43]\n" \
"v_cvt_pk_f16_f32 v185, v186, v187\n" \
"v_cvt_pk_f16_f32 v186, v188, v189\n" \
"ds_read_b128 v[164:167], v240 offset:27136\n" \
"v_mfma_f32_16x16x32_f16 v[32:35], v[128:131], v[168:171], v[32:35]\n" \
"v_cvt_pk_f16_f32 v187, v190, v191\n" \
"v_add_u32 v184, s91, v184\n" \
"buffer_load_dwordx4 v232, s[36:39], s80 offen offset:1024 sc1 lds\n" \
"v_mfma_f32_16x16x32_f16 v[44:47], v[132:135], v[168:171], v[44:47]\n" \
"v_add_u32 v185, s91, v185\n" \
"v_add_u32 v186, s91, v186\n" \
"ds_read_b64_tr_b16 v[142:143], v242 offset:24576\n" \
"v_mfma_f32_16x16x32_f16 v[48:51], v[120:123], v[176:179], v[48:51]\n" \
"v_add_u32 v187, s91, v187\n" \
"v_and_b32 v184, s92, v184\n" \
"ds_read_b128 v[168:171], v240 offset:28672\n" \
"v_mfma_f32_16x16x32_f16 v[60:63], v[124:127], v[176:179], v[60:63]\n" \
"s_waitcnt lgkmcnt(4)\n" \
"v_pk_add_f16 v176, v176, v180\n" \
"v_pk_add_f16 v177, v177, v181\n" \
"v_and_b32 v185, s92, v185\n" \
"s_add_i32 s82, s51, 0x5800000\n" \
"v_mfma_f32_16x16x32_f16 v[52:55], v[144:147], v[180:183], v[52:55]\n" \
"v_pk_add_f16 v178, v178, v182\n" \
"v_pk_add_f16 v179, v179, v183\n" \
"v_and_b32 v186, s92, v186\n" \
"v_and_b32 v187, s92, v187\n" \
"v_mfma_f32_16x16x32_f16 v[64:67], v[148:151], v[180:183], v[64:67]\n" \
"ds_read_b128 v[172:175], v240 offset:29184\n" \
"buffer_store_dwordx4 v[184:187], v234, s[36:39], s82 offen sc1\n" \
"ds_read_b64_tr_b16 v[152:153], v241 offset:16896\n" \
"v_mfma_f32_16x16x32_f16 v[56:59], v[128:131], v[176:179], v[56:59]\n" \
"buffer_load_dwordx4 v232, s[36:39], s80 offen offset:2048 sc1 lds\n" \
"v_mfma_f32_16x16x32_f16 v[68:71], v[132:135], v[176:179], v[68:71]\n" \
"ds_read_b64_tr_b16 v[154:155], v241 offset:25088\n" \
"ds_read_b64_tr_b16 v[156:157], v242 offset:16896\n" \
"v_mfma_f32_16x16x32_f16 v[72:75], v[120:123], v[160:163], v[72:75]\n" \
"ds_read_b128 v[176:179], v240 offset:20480\n" \
"ds_read_b64_tr_b16 v[158:159], v242 offset:25088\n" \
"v_mfma_f32_16x16x32_f16 v[84:87], v[124:127], v[160:163], v[84:87]\n" \
"s_waitcnt lgkmcnt(6)\n" \
"v_pk_add_f16 v160, v160, v164\n" \
"v_pk_add_f16 v161, v161, v165\n" \
"v_mfma_f32_16x16x32_f16 v[76:79], v[144:147], v[164:167], v[76:79]\n" \
"v_pk_add_f16 v162, v162, v166\n" \
"v_pk_add_f16 v163, v163, v167\n" \
"v_mfma_f32_16x16x32_f16 v[88:91], v[148:151], v[164:167], v[88:91]\n" \
"ds_read_b128 v[180:183], v240 offset:20992\n" \
"v_mfma_f32_16x16x32_f16 v[80:83], v[128:131], v[160:163], v[80:83]\n" \
"buffer_load_dwordx4 v232, s[36:39], s80 offen offset:3072 sc1 lds\n" \
"v_mfma_f32_16x16x32_f16 v[92:95], v[132:135], v[160:163], v[92:95]\n" \
"v_mfma_f32_16x16x32_f16 v[96:99], v[120:123], v[168:171], v[96:99]\n" \
"s_waitcnt lgkmcnt(5)\n" \
"v_pk_add_f16 v120, v136, v152\n" \
"v_pk_add_f16 v121, v137, v153\n" \
"ds_read_b128 v[160:163], v240 offset:22528\n" \
"v_mfma_f32_16x16x32_f16 v[108:111], v[124:127], v[168:171], v[108:111]\n" \
"v_pk_add_f16 v168, v168, v172\n" \
"v_pk_add_f16 v169, v169, v173\n" \
"ds_read_b128 v[164:167], v240 offset:23040\n" \
"v_mfma_f32_16x16x32_f16 v[100:103], v[144:147], v[172:175], v[100:103]\n" \
"v_pk_add_f16 v170, v170, v174\n" \
"v_pk_add_f16 v171, v171, v175\n" \
"v_mfma_f32_16x16x32_f16 v[112:115], v[148:151], v[172:175], v[112:115]\n" \
"s_waitcnt lgkmcnt(3)\n" \
"v_pk_add_f16 v122, v138, v154\n" \
"v_pk_add_f16 v123, v139, v155\n" \
"v_mfma_f32_16x16x32_f16 v[104:107], v[128:131], v[168:171], v[104:107]\n" \
"v_pk_add_f16 v124, v140, v156\n" \
"v_pk_add_f16 v125, v141, v157\n" \
"v_mfma_f32_16x16x32_f16 v[116:119], v[132:135], v[168:171], v[116:119]\n" \
"v_pk_add_f16 v126, v142, v158\n" \
"v_pk_add_f16 v127, v143, v159\n" \
"s_waitcnt vmcnt(6) lgkmcnt(0)\n" \
"s_barrier\n" \
"v_mfma_f32_16x16x32_f16 v[0:3], v[136:139], v[176:179], v[0:3]\n" \
"ds_read_b128 v[168:171], v240 offset:45056\n" \
"ds_read_b64_tr_b16 v[184:185], v238 offset:24576\n" \
"ds_read_b64_tr_b16 v[188:189], v239 offset:24576\n" \
"v_mfma_f32_16x16x32_f16 v[12:15], v[140:143], v[176:179], v[12:15]\n" \
"v_pk_add_f16 v176, v176, v180\n" \
"v_pk_add_f16 v177, v177, v181\n" \
"v_mfma_f32_16x16x32_f16 v[4:7], v[152:155], v[180:183], v[4:7]\n" \
"v_pk_add_f16 v178, v178, v182\n" \
"v_pk_add_f16 v179, v179, v183\n" \
"v_mfma_f32_16x16x32_f16 v[16:19], v[156:159], v[180:183], v[16:19]\n" \
"ds_read_b128 v[172:175], v240 offset:45568\n" \
"s_add_i32 m0, s44, 0x4000\n" \
"s_add_i32 s80, s46, 0x3c00000\n" \
"v_mfma_f32_16x16x32_f16 v[8:11], v[120:123], v[176:179], v[8:11]\n" \
"buffer_load_dwordx4 v232, s[36:39], s80 offen sc1 lds\n" \
"s_waitcnt lgkmcnt(2)\n" \
"v_mfma_f32_16x16x16_bf16 v[184:187], v[184:185], v[192:193], 0\n" \
"v_mfma_f32_16x16x32_f16 v[20:23], v[124:127], v[176:179], v[20:23]\n" \
"ds_read_b64_tr_b16 v[128:129], v241 offset:32768\n" \
"ds_read_b64_tr_b16 v[130:131], v241 offset:40960\n" \
"s_waitcnt lgkmcnt(3)\n" \
"v_mfma_f32_16x16x16_bf16 v[188:191], v[188:189], v[192:193], 0\n" \
"v_mfma_f32_16x16x32_f16 v[24:27], v[136:139], v[160:163], v[24:27]\n" \
"ds_read_b128 v[176:179], v240 offset:47104\n" \
"s_add_i32 s84, s54, 0x8010000\n" \
"v_mfma_f32_16x16x32_f16 v[36:39], v[140:143], v[160:163], v[36:39]\n" \
"v_pk_add_f16 v160, v160, v164\n" \
"v_pk_add_f16 v161, v161, v165\n" \
"buffer_store_dword v240, v243, s[36:39], s84 offen sc1\n" \
"v_mfma_f32_16x16x32_f16 v[28:31], v[152:155], v[164:167], v[28:31]\n" \
"v_pk_add_f16 v162, v162, v166\n" \
"v_pk_add_f16 v163, v163, v167\n" \
"v_cvt_pk_f16_f32 v184, v184, v185\n" \
"ds_read_b64_tr_b16 v[132:133], v242 offset:32768\n" \
"v_mfma_f32_16x16x32_f16 v[40:43], v[156:159], v[164:167], v[40:43]\n" \
"v_cvt_pk_f16_f32 v185, v186, v187\n" \
"v_cvt_pk_f16_f32 v186, v188, v189\n" \
"ds_read_b128 v[180:183], v240 offset:47616\n" \
"v_mfma_f32_16x16x32_f16 v[32:35], v[120:123], v[160:163], v[32:35]\n" \
"v_cvt_pk_f16_f32 v187, v190, v191\n" \
"v_add_u32 v184, s91, v184\n" \
"buffer_load_dwordx4 v232, s[36:39], s80 offen offset:1024 sc1 lds\n" \
"v_mfma_f32_16x16x32_f16 v[44:47], v[124:127], v[160:163], v[44:47]\n" \
"v_add_u32 v185, s91, v185\n" \
"v_add_u32 v186, s91, v186\n" \
"ds_read_b64_tr_b16 v[134:135], v242 offset:40960\n" \
"v_mfma_f32_16x16x32_f16 v[48:51], v[136:139], v[168:171], v[48:51]\n" \
"v_add_u32 v187, s91, v187\n" \
"v_and_b32 v184, s92, v184\n" \
"ds_read_b128 v[160:163], v240 offset:49152\n" \
"v_mfma_f32_16x16x32_f16 v[60:63], v[140:143], v[168:171], v[60:63]\n" \
"s_waitcnt lgkmcnt(4)\n" \
"v_pk_add_f16 v168, v168, v172\n" \
"v_pk_add_f16 v169, v169, v173\n" \
"v_and_b32 v185, s92, v185\n" \
"v_mfma_f32_16x16x32_f16 v[52:55], v[152:155], v[172:175], v[52:55]\n" \
"v_pk_add_f16 v170, v170, v174\n" \
"v_pk_add_f16 v171, v171, v175\n" \
"v_and_b32 v186, s92, v186\n" \
"v_and_b32 v187, s92, v187\n" \
"v_mfma_f32_16x16x32_f16 v[64:67], v[156:159], v[172:175], v[64:67]\n" \
"ds_read_b128 v[164:167], v240 offset:49664\n" \
"buffer_store_dwordx4 v[184:187], v234, s[36:39], s82 offen offset:1024 sc1\n" \
"ds_read_b64_tr_b16 v[144:145], v241 offset:33280\n" \
"v_mfma_f32_16x16x32_f16 v[56:59], v[120:123], v[168:171], v[56:59]\n" \
"buffer_load_dwordx4 v232, s[36:39], s80 offen offset:2048 sc1 lds\n" \
"v_mfma_f32_16x16x32_f16 v[68:71], v[124:127], v[168:171], v[68:71]\n" \
"ds_read_b64_tr_b16 v[146:147], v241 offset:41472\n" \
"ds_read_b64_tr_b16 v[148:149], v242 offset:33280\n" \
"v_mfma_f32_16x16x32_f16 v[72:75], v[136:139], v[176:179], v[72:75]\n" \
"ds_read_b128 v[168:171], v240 offset:40960\n" \
"ds_read_b64_tr_b16 v[150:151], v242 offset:41472\n" \
"v_mfma_f32_16x16x32_f16 v[84:87], v[140:143], v[176:179], v[84:87]\n" \
"s_waitcnt lgkmcnt(6)\n" \
"v_pk_add_f16 v176, v176, v180\n" \
"v_pk_add_f16 v177, v177, v181\n" \
"v_mfma_f32_16x16x32_f16 v[76:79], v[152:155], v[180:183], v[76:79]\n" \
"v_pk_add_f16 v178, v178, v182\n" \
"v_pk_add_f16 v179, v179, v183\n" \
"v_mfma_f32_16x16x32_f16 v[88:91], v[156:159], v[180:183], v[88:91]\n" \
"ds_read_b128 v[172:175], v240 offset:41472\n" \
"v_mfma_f32_16x16x32_f16 v[80:83], v[120:123], v[176:179], v[80:83]\n" \
"buffer_load_dwordx4 v232, s[36:39], s80 offen offset:3072 sc1 lds\n" \
"v_mfma_f32_16x16x32_f16 v[92:95], v[124:127], v[176:179], v[92:95]\n" \
"v_mfma_f32_16x16x32_f16 v[96:99], v[136:139], v[160:163], v[96:99]\n" \
"s_waitcnt lgkmcnt(5)\n" \
"v_pk_add_f16 v136, v128, v144\n" \
"v_pk_add_f16 v137, v129, v145\n" \
"ds_read_b128 v[176:179], v240 offset:43008\n" \
"v_mfma_f32_16x16x32_f16 v[108:111], v[140:143], v[160:163], v[108:111]\n" \
"v_pk_add_f16 v160, v160, v164\n" \
"v_pk_add_f16 v161, v161, v165\n" \
"ds_read_b128 v[180:183], v240 offset:43520\n" \
"v_mfma_f32_16x16x32_f16 v[100:103], v[152:155], v[164:167], v[100:103]\n" \
"v_pk_add_f16 v162, v162, v166\n" \
"v_pk_add_f16 v163, v163, v167\n" \
"v_mfma_f32_16x16x32_f16 v[112:115], v[156:159], v[164:167], v[112:115]\n" \
"s_waitcnt lgkmcnt(3)\n" \
"v_pk_add_f16 v138, v130, v146\n" \
"v_pk_add_f16 v139, v131, v147\n" \
"v_mfma_f32_16x16x32_f16 v[104:107], v[120:123], v[160:163], v[104:107]\n" \
"v_pk_add_f16 v140, v132, v148\n" \
"v_pk_add_f16 v141, v133, v149\n" \
"v_mfma_f32_16x16x32_f16 v[116:119], v[124:127], v[160:163], v[116:119]\n" \
"v_pk_add_f16 v142, v134, v150\n" \
"v_pk_add_f16 v143, v135, v151\n" \
"s_waitcnt vmcnt(6) lgkmcnt(0)\n" \
"s_barrier\n" \
"v_mfma_f32_16x16x32_f16 v[0:3], v[128:131], v[168:171], v[0:3]\n" \
"ds_read_b128 v[160:163], v240 offset:4096\n" \
"s_mov_b32 s81, 0x800e000\n" \
"ds_read_b64_tr_b16 v[184:185], v236 offset:28672\n" \
"ds_read_b64_tr_b16 v[188:189], v237 offset:28672\n" \
"s_add_i32 m0, s45, 0x0\n" \
"s_mov_b32 s83, 0x0\n" \
"v_mfma_f32_16x16x32_f16 v[12:15], v[132:135], v[168:171], v[12:15]\n" \
"v_pk_add_f16 v168, v168, v172\n" \
"v_pk_add_f16 v169, v169, v173\n" \
"buffer_load_dword v253, v252, s[36:39], s81 offen sc1\n" \
"buffer_load_dwordx4 v233, s[36:39], s83 offen sc1 lds\n" \
"v_mfma_f32_16x16x32_f16 v[4:7], v[144:147], v[172:175], v[4:7]\n" \
"v_pk_add_f16 v170, v170, v174\n" \
"v_pk_add_f16 v171, v171, v175\n" \
"buffer_store_dwordx4 v[228:231], v235, s[36:39], s68 offen sc1\n" \
"v_mfma_f32_16x16x32_f16 v[16:19], v[148:151], v[172:175], v[16:19]\n" \
"ds_read_b128 v[164:167], v240 offset:4608\n" \
"s_add_i32 m0, s44, 0x8000\n" \
"s_add_i32 s80, s46, 0x4000000\n" \
"v_mfma_f32_16x16x32_f16 v[8:11], v[136:139], v[168:171], v[8:11]\n" \
"buffer_load_dwordx4 v232, s[36:39], s80 offen sc1 lds\n" \
"s_waitcnt lgkmcnt(2)\n" \
"v_mfma_f32_16x16x16_bf16 v[184:187], v[184:185], v[192:193], 0\n" \
"v_mfma_f32_16x16x32_f16 v[20:23], v[140:143], v[168:171], v[20:23]\n" \
"ds_read_b64_tr_b16 v[120:121], v241 offset:0\n" \
"ds_read_b64_tr_b16 v[122:123], v241 offset:8192\n" \
"s_waitcnt lgkmcnt(3)\n" \
"v_mfma_f32_16x16x16_bf16 v[188:191], v[188:189], v[192:193], 0\n" \
"v_mfma_f32_16x16x32_f16 v[24:27], v[128:131], v[176:179], v[24:27]\n" \
"ds_read_b128 v[168:171], v240 offset:6144\n" \
"v_mfma_f32_16x16x32_f16 v[36:39], v[132:135], v[176:179], v[36:39]\n" \
"v_pk_add_f16 v176, v176, v180\n" \
"v_pk_add_f16 v177, v177, v181\n" \
"ds_read_b64_tr_b16 v[124:125], v242 offset:0\n" \
"v_mfma_f32_16x16x32_f16 v[28:31], v[144:147], v[180:183], v[28:31]\n" \
"v_pk_add_f16 v178, v178, v182\n" \
"v_pk_add_f16 v179, v179, v183\n" \
"v_cvt_pk_f16_f32 v184, v184, v185\n" \
"v_mfma_f32_16x16x32_f16 v[40:43], v[148:151], v[180:183], v[40:43]\n" \
"v_cvt_pk_f16_f32 v185, v186, v187\n" \
"v_cvt_pk_f16_f32 v186, v188, v189\n" \
"ds_read_b128 v[172:175], v240 offset:6656\n" \
"v_mfma_f32_16x16x32_f16 v[32:35], v[136:139], v[176:179], v[32:35]\n" \
"v_cvt_pk_f16_f32 v187, v190, v191\n" \
"v_add_u32 v184, s91, v184\n" \
"buffer_load_dwordx4 v232, s[36:39], s80 offen offset:1024 sc1 lds\n" \
"v_mfma_f32_16x16x32_f16 v[44:47], v[140:143], v[176:179], v[44:47]\n" \
"v_add_u32 v185, s91, v185\n" \
"v_add_u32 v186, s91, v186\n" \
"ds_read_b64_tr_b16 v[126:127], v242 offset:8192\n" \
"v_mfma_f32_16x16x32_f16 v[48:51], v[128:131], v[160:163], v[48:51]\n" \
"v_add_u32 v187, s91, v187\n" \
"v_and_b32 v184, s92, v184\n" \
"ds_read_b128 v[176:179], v240 offset:8192\n" \
"v_mfma_f32_16x16x32_f16 v[60:63], v[132:135], v[160:163], v[60:63]\n" \
"s_waitcnt lgkmcnt(4)\n" \
"v_pk_add_f16 v160, v160, v164\n" \
"v_pk_add_f16 v161, v161, v165\n" \
"v_and_b32 v185, s92, v185\n" \
"s_add_i32 s82, s48, 0x6000000\n" \
"v_mfma_f32_16x16x32_f16 v[52:55], v[144:147], v[164:167], v[52:55]\n" \
"v_pk_add_f16 v162, v162, v166\n" \
"v_pk_add_f16 v163, v163, v167\n" \
"v_and_b32 v186, s92, v186\n" \
"v_and_b32 v187, s92, v187\n" \
"v_mfma_f32_16x16x32_f16 v[64:67], v[148:151], v[164:167], v[64:67]\n" \
"ds_read_b128 v[180:183], v240 offset:8704\n" \
"buffer_store_dwordx4 v[184:187], v234, s[36:39], s82 offen sc1\n" \
"ds_read_b64_tr_b16 v[152:153], v241 offset:512\n" \
"v_mfma_f32_16x16x32_f16 v[56:59], v[136:139], v[160:163], v[56:59]\n" \
"buffer_load_dwordx4 v232, s[36:39], s80 offen offset:2048 sc1 lds\n" \
"v_mfma_f32_16x16x32_f16 v[68:71], v[140:143], v[160:163], v[68:71]\n" \
"ds_read_b64_tr_b16 v[154:155], v241 offset:8704\n" \
"ds_read_b64_tr_b16 v[156:157], v242 offset:512\n" \
"v_mfma_f32_16x16x32_f16 v[72:75], v[128:131], v[168:171], v[72:75]\n" \
"ds_read_b128 v[160:163], v240 offset:0\n" \
"ds_read_b64_tr_b16 v[158:159], v242 offset:8704\n" \
"v_mfma_f32_16x16x32_f16 v[84:87], v[132:135], v[168:171], v[84:87]\n" \
"s_waitcnt lgkmcnt(6)\n" \
"v_pk_add_f16 v168, v168, v172\n" \
"v_pk_add_f16 v169, v169, v173\n" \
"v_mfma_f32_16x16x32_f16 v[76:79], v[144:147], v[172:175], v[76:79]\n" \
"v_pk_add_f16 v170, v170, v174\n" \
"v_pk_add_f16 v171, v171, v175\n" \
"v_mfma_f32_16x16x32_f16 v[88:91], v[148:151], v[172:175], v[88:91]\n" \
"ds_read_b128 v[164:167], v240 offset:512\n" \
"v_mfma_f32_16x16x32_f16 v[80:83], v[136:139], v[168:171], v[80:83]\n" \
"buffer_load_dwordx4 v232, s[36:39], s80 offen offset:3072 sc1 lds\n" \
"v_mfma_f32_16x16x32_f16 v[92:95], v[140:143], v[168:171], v[92:95]\n" \
"v_mfma_f32_16x16x32_f16 v[96:99], v[128:131], v[176:179], v[96:99]\n" \
"s_waitcnt lgkmcnt(5)\n" \
"v_pk_add_f16 v128, v120, v152\n" \
"v_pk_add_f16 v129, v121, v153\n" \
"ds_read_b128 v[168:171], v240 offset:2048\n" \
"s_add_i32 m0, s45, 0x1000\n" \
"s_mov_b32 s83, 0x8000\n" \
"v_mfma_f32_16x16x32_f16 v[108:111], v[132:135], v[176:179], v[108:111]\n" \
"v_pk_add_f16 v176, v176, v180\n" \
"v_pk_add_f16 v177, v177, v181\n" \
"ds_read_b128 v[172:175], v240 offset:2560\n" \
"buffer_load_dwordx4 v233, s[36:39], s83 offen sc1 lds\n" \
"v_mfma_f32_16x16x32_f16 v[100:103], v[144:147], v[180:183], v[100:103]\n" \
"v_pk_add_f16 v178, v178, v182\n" \
"v_pk_add_f16 v179, v179, v183\n" \
"v_mfma_f32_16x16x32_f16 v[112:115], v[148:151], v[180:183], v[112:115]\n" \
"s_waitcnt lgkmcnt(3)\n" \
"v_pk_add_f16 v130, v122, v154\n" \
"v_pk_add_f16 v131, v123, v155\n" \
"v_mfma_f32_16x16x32_f16 v[104:107], v[136:139], v[176:179], v[104:107]\n" \
"v_pk_add_f16 v132, v124, v156\n" \
"v_pk_add_f16 v133, v125, v157\n" \
"v_mfma_f32_16x16x32_f16 v[116:119], v[140:143], v[176:179], v[116:119]\n" \
"v_pk_add_f16 v134, v126, v158\n" \
"v_pk_add_f16 v135, v127, v159\n" \
"s_waitcnt vmcnt(8) lgkmcnt(0)\n" \
"s_barrier\n" \
"v_mfma_f32_16x16x32_f16 v[0:3], v[120:123], v[160:163], v[0:3]\n" \
"ds_read_b128 v[176:179], v240 offset:24576\n" \
"ds_read_b64_tr_b16 v[184:185], v238 offset:28672\n" \
"ds_read_b64_tr_b16 v[188:189], v239 offset:28672\n" \
"s_add_i32 m0, s45, 0x2000\n" \
"s_mov_b32 s83, 0x10000\n" \
"v_mfma_f32_16x16x32_f16 v[12:15], v[124:127], v[160:163], v[12:15]\n" \
"v_pk_add_f16 v160, v160, v164\n" \
"v_pk_add_f16 v161, v161, v165\n" \
"buffer_load_dwordx4 v233, s[36:39], s83 offen sc1 lds\n" \
"v_mfma_f32_16x16x32_f16 v[4:7], v[152:155], v[164:167], v[4:7]\n" \
"v_pk_add_f16 v162, v162, v166\n" \
"v_pk_add_f16 v163, v163, v167\n" \
"v_cmp_eq_u32 vcc, 0, v253\n" \
"s_cbranch_vccnz .Ls07_%=\n" \
".Lr07_%=:\n" \
"v_mfma_f32_16x16x32_f16 v[16:19], v[156:159], v[164:167], v[16:19]\n" \
"ds_read_b128 v[180:183], v240 offset:25088\n" \
"s_add_i32 m0, s44, 0x0\n" \
"s_add_i32 s80, s46, 0x4400000\n" \
"v_mfma_f32_16x16x32_f16 v[8:11], v[128:131], v[160:163], v[8:11]\n" \
"buffer_load_dwordx4 v232, s[36:39], s80 offen sc1 lds\n" \
"s_waitcnt lgkmcnt(2)\n" \
"v_mfma_f32_16x16x16_bf16 v[184:187], v[184:185], v[192:193], 0\n" \
"v_mfma_f32_16x16x32_f16 v[20:23], v[132:135], v[160:163], v[20:23]\n" \
"ds_read_b64_tr_b16 v[136:137], v241 offset:16384\n" \
"ds_read_b64_tr_b16 v[138:139], v241 offset:24576\n" \
"s_waitcnt lgkmcnt(3)\n" \
"v_mfma_f32_16x16x16_bf16 v[188:191], v[188:189], v[192:193], 0\n" \
"v_mfma_f32_16x16x32_f16 v[24:27], v[120:123], v[168:171], v[24:27]\n" \
"ds_read_b128 v[160:163], v240 offset:26624\n" \
"s_add_i32 s84, s55, 0x8012000\n" \
"v_mfma_f32_16x16x32_f16 v[36:39], v[124:127], v[168:171], v[36:39]\n" \
"v_pk_add_f16 v168, v168, v172\n" \
"v_pk_add_f16 v169, v169, v173\n" \
"buffer_store_dword v240, v243, s[36:39], s84 offen sc1\n" \
"v_mfma_f32_16x16x32_f16 v[28:31], v[152:155], v[172:175], v[28:31]\n" \
"v_pk_add_f16 v170, v170, v174\n" \
"v_pk_add_f16 v171, v171, v175\n" \
"v_cvt_pk_f16_f32 v184, v184, v185\n" \
"ds_read_b64_tr_b16 v[140:141], v242 offset:16384\n" \
"v_mfma_f32_16x16x32_f16 v[40:43], v[156:159], v[172:175], v[40:43]\n" \
"v_cvt_pk_f16_f32 v185, v186, v187\n" \
"v_cvt_pk_f16_f32 v186, v188, v189\n" \
"ds_read_b128 v[164:167], v240 offset:27136\n" \
"v_mfma_f32_16x16x32_f16 v[32:35], v[128:131], v[168:171], v[32:35]\n" \
"v_cvt_pk_f16_f32 v187, v190, v191\n" \
"v_add_u32 v184, s91, v184\n" \
"buffer_load_dwordx4 v232, s[36:39], s80 offen offset:1024 sc1 lds\n" \
"v_mfma_f32_16x16x32_f16 v[44:47], v[132:135], v[168:171], v[44:47]\n" \
"v_add_u32 v185, s91, v185\n" \
"v_add_u32 v186, s91, v186\n" \
"ds_read_b64_tr_b16 v[142:143], v242 offset:24576\n" \
"v_mfma_f32_16x16x32_f16 v[48:51], v[120:123], v[176:179], v[48:51]\n" \
"v_add_u32 v187, s91, v187\n" \
"v_and_b32 v184, s92, v184\n" \
"ds_read_b128 v[168:171], v240 offset:28672\n" \
"v_mfma_f32_16x16x32_f16 v[60:63], v[124:127], v[176:179], v[60:63]\n" \
"s_waitcnt lgkmcnt(4)\n" \
"v_pk_add_f16 v176, v176, v180\n" \
"v_pk_add_f16 v177, v177, v181\n" \
"v_and_b32 v185, s92, v185\n" \
"v_mfma_f32_16x16x32_f16 v[52:55], v[152:155], v[180:183], v[52:55]\n" \
"v_pk_add_f16 v178, v178, v182\n" \
"v_pk_add_f16 v179, v179, v183\n" \
"v_and_b32 v186, s92, v186\n" \
"v_and_b32 v187, s92, v187\n" \
"v_mfma_f32_16x16x32_f16 v[64:67], v[156:159], v[180:183], v[64:67]\n" \
"ds_read_b128 v[172:175], v240 offset:29184\n" \
"buffer_store_dwordx4 v[184:187], v234, s[36:39], s82 offen offset:1024 sc1\n" \
"ds_read_b64_tr_b16 v[144:145], v241 offset:16896\n" \
"v_mfma_f32_16x16x32_f16 v[56:59], v[128:131], v[176:179], v[56:59]\n" \
"buffer_load_dwordx4 v232, s[36:39], s80 offen offset:2048 sc1 lds\n" \
"v_mfma_f32_16x16x32_f16 v[68:71], v[132:135], v[176:179], v[68:71]\n" \
"ds_read_b64_tr_b16 v[146:147], v241 offset:25088\n" \
"ds_read_b64_tr_b16 v[148:149], v242 offset:16896\n" \
"v_mfma_f32_16x16x32_f16 v[72:75], v[120:123], v[160:163], v[72:75]\n" \
"ds_read_b128 v[176:179], v240 offset:20480\n" \
"ds_read_b64_tr_b16 v[150:151], v242 offset:25088\n" \
"v_mfma_f32_16x16x32_f16 v[84:87], v[124:127], v[160:163], v[84:87]\n" \
"s_waitcnt lgkmcnt(6)\n" \
"v_pk_add_f16 v160, v160, v164\n" \
"v_pk_add_f16 v161, v161, v165\n" \
"v_mfma_f32_16x16x32_f16 v[76:79], v[152:155], v[164:167], v[76:79]\n" \
"v_pk_add_f16 v162, v162, v166\n" \
"v_pk_add_f16 v163, v163, v167\n" \
"v_mfma_f32_16x16x32_f16 v[88:91], v[156:159], v[164:167], v[88:91]\n" \
"ds_read_b128 v[180:183], v240 offset:20992\n" \
"v_mfma_f32_16x16x32_f16 v[80:83], v[128:131], v[160:163], v[80:83]\n" \
"buffer_load_dwordx4 v232, s[36:39], s80 offen offset:3072 sc1 lds\n" \
"v_mfma_f32_16x16x32_f16 v[92:95], v[132:135], v[160:163], v[92:95]\n" \
"v_mfma_f32_16x16x32_f16 v[96:99], v[120:123], v[168:171], v[96:99]\n" \
"s_waitcnt lgkmcnt(5)\n" \
"v_pk_add_f16 v120, v136, v144\n" \
"v_pk_add_f16 v121, v137, v145\n" \
"ds_read_b128 v[160:163], v240 offset:22528\n" \
"s_add_i32 m0, s45, 0x3000\n" \
"s_mov_b32 s83, 0x18000\n" \
"v_mfma_f32_16x16x32_f16 v[108:111], v[124:127], v[168:171], v[108:111]\n" \
"v_pk_add_f16 v168, v168, v172\n" \
"v_pk_add_f16 v169, v169, v173\n" \
"ds_read_b128 v[164:167], v240 offset:23040\n" \
"buffer_load_dwordx4 v233, s[36:39], s83 offen sc1 lds\n" \
"v_mfma_f32_16x16x32_f16 v[100:103], v[152:155], v[172:175], v[100:103]\n" \
"v_pk_add_f16 v170, v170, v174\n" \
"v_pk_add_f16 v171, v171, v175\n" \
"v_mfma_f32_16x16x32_f16 v[112:115], v[156:159], v[172:175], v[112:115]\n" \
"s_waitcnt lgkmcnt(3)\n" \
"v_pk_add_f16 v122, v138, v146\n" \
"v_pk_add_f16 v123, v139, v147\n" \
"v_mfma_f32_16x16x32_f16 v[104:107], v[128:131], v[168:171], v[104:107]\n" \
"v_pk_add_f16 v124, v140, v148\n" \
"v_pk_add_f16 v125, v141, v149\n" \
"v_mfma_f32_16x16x32_f16 v[116:119], v[132:135], v[168:171], v[116:119]\n" \
"v_pk_add_f16 v126, v142, v150\n" \
"v_pk_add_f16 v127, v143, v151\n" \
"s_waitcnt vmcnt(8) lgkmcnt(0)\n" \
"s_barrier\n" \
"v_mfma_f32_16x16x32_f16 v[0:3], v[136:139], v[176:179], v[0:3]\n" \
"ds_read_b128 v[168:171], v240 offset:45056\n" \
"s_mov_b32 s81, 0x8010000\n" \
"ds_read_b64_tr_b16 v[184:185], v236 offset:32768\n" \
"s_add_i32 m0, s45, 0x4000\n" \
"s_mov_b32 s83, 0x20000\n" \
"v_mfma_f32_16x16x32_f16 v[12:15], v[140:143], v[176:179], v[12:15]\n" \
"v_pk_add_f16 v176, v176, v180\n" \
"v_pk_add_f16 v177, v177, v181\n" \
"buffer_load_dword v253, v252, s[36:39], s81 offen sc1\n" \
"buffer_load_dwordx4 v233, s[36:39], s83 offen sc1 lds\n" \
"v_mfma_f32_16x16x32_f16 v[4:7], v[144:147], v[180:183], v[4:7]\n" \
"v_pk_add_f16 v178, v178, v182\n" \
"v_pk_add_f16 v179, v179, v183\n" \
"ds_read_b64_tr_b16 v[188:189], v237 offset:32768\n" \
"v_mfma_f32_16x16x32_f16 v[16:19], v[148:151], v[180:183], v[16:19]\n" \
"ds_read_b128 v[172:175], v240 offset:45568\n" \
"s_add_i32 m0, s44, 0x4000\n" \
"s_add_i32 s80, s46, 0x4800000\n" \
"v_mfma_f32_16x16x32_f16 v[8:11], v[120:123], v[176:179], v[8:11]\n" \
"buffer_load_dwordx4 v232, s[36:39], s80 offen sc1 lds\n" \
"s_waitcnt lgkmcnt(2)\n" \
"v_mfma_f32_16x16x16_bf16 v[184:187], v[184:185], v[192:193], 0\n" \
"v_mfma_f32_16x16x32_f16 v[20:23], v[124:127], v[176:179], v[20:23]\n" \
"ds_read_b128 v[196:199], v249 offset:0\n" \
"ds_read_b64_tr_b16 v[200:201], v247 offset:0\n" \
"s_waitcnt lgkmcnt(3)\n" \
"v_mfma_f32_16x16x16_bf16 v[188:191], v[188:189], v[192:193], 0\n" \
"v_mfma_f32_16x16x32_f16 v[24:27], v[136:139], v[160:163], v[24:27]\n" \
"ds_read_b128 v[176:179], v240 offset:47104\n" \
"ds_read_b64_tr_b16 v[128:129], v241 offset:32768\n" \
"ds_read_b64_tr_b16 v[130:131], v241 offset:40960\n" \
"v_mfma_f32_16x16x32_f16 v[36:39], v[140:143], v[160:163], v[36:39]\n" \
"v_pk_add_f16 v160, v160, v164\n" \
"v_pk_add_f16 v161, v161, v165\n" \
"ds_read_b64_tr_b16 v[202:203], v247 offset:512\n" \
"ds_read_b64_tr_b16 v[132:133], v242 offset:32768\n" \
"v_mfma_f32_16x16x32_f16 v[28:31], v[144:147], v[164:167], v[28:31]\n" \
"v_pk_add_f16 v162, v162, v166\n" \
"v_pk_add_f16 v163, v163, v167\n" \
"v_cvt_pk_f16_f32 v184, v184, v185\n" \
"v_mfma_f32_16x16x32_f16 v[40:43], v[148:151], v[164:167], v[40:43]\n" \
"v_cvt_pk_f16_f32 v185, v186, v187\n" \
"v_cvt_pk_f16_f32 v186, v188, v189\n" \
"ds_read_b128 v[180:183], v240 offset:47616\n" \
"v_mfma_f32_16x16x32_f16 v[32:35], v[120:123], v[160:163], v[32:35]\n" \
"v_cvt_pk_f16_f32 v187, v190, v191\n" \
"v_add_u32 v184, s91, v184\n" \
"buffer_load_dwordx4 v232, s[36:39], s80 offen offset:1024 sc1 lds\n" \
"v_mfma_f32_16x16x32_f16 v[44:47], v[124:127], v[160:163], v[44:47]\n" \
"v_add_u32 v185, s91, v185\n" \
"v_add_u32 v186, s91, v186\n" \
"ds_read_b64_tr_b16 v[134:135], v242 offset:40960\n" \
"v_mfma_f32_16x16x32_f16 v[48:51], v[136:139], v[168:171], v[48:51]\n" \
"v_add_u32 v187, s91, v187\n" \
"v_and_b32 v184, s92, v184\n" \
"ds_read_b128 v[160:163], v240 offset:49152\n" \
"v_mfma_f32_16x16x32_f16 v[60:63], v[140:143], v[168:171], v[60:63]\n" \
"s_waitcnt lgkmcnt(5)\n" \
"v_pk_add_f16 v168, v168, v172\n" \
"v_pk_add_f16 v169, v169, v173\n" \
"v_and_b32 v185, s92, v185\n" \
"s_add_i32 s82, s49, 0x6800000\n" \
"s_waitcnt lgkmcnt(3)\n" \
"v_mfma_f32_16x16x32_bf16 v[204:207], v[200:203], v[196:199], 0\n" \
"v_mfma_f32_16x16x32_f16 v[52:55], v[144:147], v[172:175], v[52:55]\n" \
"v_pk_add_f16 v170, v170, v174\n" \
"v_pk_add_f16 v171, v171, v175\n" \
"v_and_b32 v186, s92, v186\n" \
"v_and_b32 v187, s92, v187\n" \
"v_mfma_f32_16x16x32_f16 v[64:67], v[148:151], v[172:175], v[64:67]\n" \
"ds_read_b128 v[164:167], v240 offset:49664\n" \
"buffer_store_dwordx4 v[184:187], v234, s[36:39], s82 offen sc1\n" \
"ds_read_b64_tr_b16 v[152:153], v241 offset:33280\n" \
"v_mfma_f32_16x16x32_f16 v[56:59], v[120:123], v[168:171], v[56:59]\n" \
"buffer_load_dwordx4 v232, s[36:39], s80 offen offset:2048 sc1 lds\n" \
"ds_read_b64_tr_b16 v[154:155], v241 offset:41472\n" \
"v_mfma_f32_16x16x32_f16 v[68:71], v[124:127], v[168:171], v[68:71]\n" \
"ds_read_b128 v[196:199], v249 offset:1024\n" \
"ds_read_b64_tr_b16 v[200:201], v247 offset:4096\n" \
"ds_read_b64_tr_b16 v[156:157], v242 offset:33280\n" \
"v_mfma_f32_16x16x32_f16 v[72:75], v[136:139], v[176:179], v[72:75]\n" \
"ds_read_b128 v[168:171], v240 offset:40960\n" \
"ds_read_b64_tr_b16 v[202:203], v247 offset:4608\n" \
"v_mfma_f32_16x16x32_f16 v[84:87], v[140:143], v[176:179], v[84:87]\n" \
"s_waitcnt lgkmcnt(8)\n" \
"v_pk_add_f16 v176, v176, v180\n" \
"v_pk_add_f16 v177, v177, v181\n" \
"v_mfma_f32_16x16x32_f16 v[76:79], v[144:147], v[180:183], v[76:79]\n" \
"v_pk_add_f16 v178, v178, v182\n" \
"v_pk_add_f16 v179, v179, v183\n" \
"v_mfma_f32_16x16x32_f16 v[88:91], v[148:151], v[180:183], v[88:91]\n" \
"ds_read_b128 v[172:175], v240 offset:41472\n" \
"ds_read_b64_tr_b16 v[158:159], v242 offset:41472\n" \
"v_mfma_f32_16x16x32_f16 v[80:83], v[120:123], v[176:179], v[80:83]\n" \
"buffer_load_dwordx4 v232, s[36:39], s80 offen offset:3072 sc1 lds\n" \
"v_mfma_f32_16x16x32_f16 v[92:95], v[124:127], v[176:179], v[92:95]\n" \
"v_mfma_f32_16x16x32_f16 v[96:99], v[136:139], v[160:163], v[96:99]\n" \
"s_waitcnt lgkmcnt(8)\n" \
"v_pk_add_f16 v136, v128, v152\n" \
"v_pk_add_f16 v137, v129, v153\n" \
"ds_read_b128 v[176:179], v240 offset:43008\n" \
"s_add_i32 m0, s45, 0x5000\n" \
"s_mov_b32 s83, 0x28000\n" \
"s_waitcnt lgkmcnt(3)\n" \
"v_mfma_f32_16x16x32_bf16 v[204:207], v[200:203], v[196:199], v[204:207]\n" \
"v_mfma_f32_16x16x32_f16 v[108:111], v[140:143], v[160:163], v[108:111]\n" \
"v_pk_add_f16 v160, v160, v164\n" \
"v_pk_add_f16 v161, v161, v165\n" \
"ds_read_b128 v[180:183], v240 offset:43520\n" \
"buffer_load_dwordx4 v233, s[36:39], s83 offen sc1 lds\n" \
"v_mfma_f32_16x16x32_f16 v[100:103], v[144:147], v[164:167], v[100:103]\n" \
"v_pk_add_f16 v162, v162, v166\n" \
"v_pk_add_f16 v163, v163, v167\n" \
"v_mfma_f32_16x16x32_f16 v[112:115], v[148:151], v[164:167], v[112:115]\n" \
"v_pk_add_f16 v138, v130, v154\n" \
"v_pk_add_f16 v139, v131, v155\n" \
"v_mfma_f32_16x16x32_f16 v[104:107], v[120:123], v[160:163], v[104:107]\n" \
"v_pk_add_f16 v140, v132, v156\n" \
"v_pk_add_f16 v141, v133, v157\n" \
"v_mfma_f32_16x16x32_f16 v[116:119], v[124:127], v[160:163], v[116:119]\n" \
"s_waitcnt lgkmcnt(2)\n" \
"v_pk_add_f16 v142, v134, v158\n" \
"v_pk_add_f16 v143, v135, v159\n" \
"s_waitcnt vmcnt(7) lgkmcnt(0)\n" \
"s_barrier\n" \
"v_mfma_f32_16x16x32_f16 v[0:3], v[128:131], v[168:171], v[0:3]\n" \
"ds_read_b128 v[160:163], v240 offset:4096\n" \
"ds_read_b64_tr_b16 v[184:185], v238 offset:32768\n" \
"ds_read_b64_tr_b16 v[188:189], v239 offset:32768\n" \
"s_add_i32 m0, s45, 0x0\n" \
"s_mov_b32 s83, 0x30000\n" \
"v_mfma_f32_16x16x32_f16 v[12:15], v[132:135], v[168:171], v[12:15]\n" \
"v_pk_add_f16 v168, v168, v172\n" \
"v_pk_add_f16 v169, v169, v173\n" \
"buffer_load_dwordx4 v233, s[36:39], s83 offen sc1 lds\n" \
"v_mfma_f32_16x16x32_f16 v[4:7], v[152:155], v[172:175], v[4:7]\n" \
"v_pk_add_f16 v170, v170, v174\n" \
"v_pk_add_f16 v171, v171, v175\n" \
"v_cmp_eq_u32 vcc, 0, v253\n" \
"s_cbranch_vccnz .Ls08_%=\n" \
".Lr08_%=:\n" \
"v_mfma_f32_16x16x32_f16 v[16:19], v[156:159], v[172:175], v[16:19]\n" \
"ds_read_b128 v[164:167], v240 offset:4608\n" \
"s_add_i32 m0, s44, 0x8000\n" \
"s_add_i32 s80, s46, 0x4c00000\n" \
"v_mfma_f32_16x16x32_f16 v[8:11], v[136:139], v[168:171], v[8:11]\n" \
"buffer_load_dwordx4 v232, s[36:39], s80 offen sc1 lds\n" \
"ds_read_b64_tr_b16 v[120:121], v241 offset:0\n" \
"s_waitcnt lgkmcnt(3)\n" \
"v_mfma_f32_16x16x16_bf16 v[184:187], v[184:185], v[192:193], 0\n" \
"v_mfma_f32_16x16x32_f16 v[20:23], v[140:143], v[168:171], v[20:23]\n" \
"ds_read_b128 v[196:199], v249 offset:2048\n" \
"ds_read_b64_tr_b16 v[200:201], v247 offset:8192\n" \
"s_waitcnt lgkmcnt(4)\n" \
"v_mfma_f32_16x16x16_bf16 v[188:191], v[188:189], v[192:193], 0\n" \
"v_mfma_f32_16x16x32_f16 v[24:27], v[128:131], v[176:179], v[24:27]\n" \
"ds_read_b128 v[168:171], v240 offset:6144\n" \
"s_add_i32 s84, s52, 0x8014000\n" \
"ds_read_b64_tr_b16 v[122:123], v241 offset:8192\n" \
"v_mfma_f32_16x16x32_f16 v[36:39], v[132:135], v[176:179], v[36:39]\n" \
"v_pk_add_f16 v176, v176, v180\n" \
"v_pk_add_f16 v177, v177, v181\n" \
"buffer_store_dword v240, v243, s[36:39], s84 offen sc1\n" \
"v_mfma_f32_16x16x32_f16 v[28:31], v[152:155], v[180:183], v[28:31]\n" \
"v_pk_add_f16 v178, v178, v182\n" \
"v_pk_add_f16 v179, v179, v183\n" \
"v_cvt_pk_f16_f32 v184, v184, v185\n" \
"ds_read_b64_tr_b16 v[202:203], v247 offset:8704\n" \
"v_mfma_f32_16x16x32_f16 v[40:43], v[156:159], v[180:183], v[40:43]\n" \
"v_cvt_pk_f16_f32 v185, v186, v187\n" \
"v_cvt_pk_f16_f32 v186, v188, v189\n" \
"ds_read_b128 v[172:175], v240 offset:6656\n" \
"ds_read_b64_tr_b16 v[124:125], v242 offset:0\n" \
"v_mfma_f32_16x16x32_f16 v[32:35], v[136:139], v[176:179], v[32:35]\n" \
"v_cvt_pk_f16_f32 v187, v190, v191\n" \
"v_add_u32 v184, s91, v184\n" \
"buffer_load_dwordx4 v232, s[36:39], s80 offen offset:1024 sc1 lds\n" \
"v_mfma_f32_16x16x32_f16 v[44:47], v[140:143], v[176:179], v[44:47]\n" \
"v_add_u32 v185, s91, v185\n" \
"v_add_u32 v186, s91, v186\n" \
"ds_read_b64_tr_b16 v[126:127], v242 offset:8192\n" \
"v_mfma_f32_16x16x32_f16 v[48:51], v[128:131], v[160:163], v[48:51]\n" \
"v_add_u32 v187, s91, v187\n" \
"v_and_b32 v184, s92, v184\n" \
"ds_read_b128 v[176:179], v240 offset:8192\n" \
"v_mfma_f32_16x16x32_f16 v[60:63], v[132:135], v[160:163], v[60:63]\n" \
"s_waitcnt lgkmcnt(5)\n" \
"v_pk_add_f16 v160, v160, v164\n" \
"v_pk_add_f16 v161, v161, v165\n" \
"v_and_b32 v185, s92, v185\n" \
"s_waitcnt lgkmcnt(4)\n" \
"v_mfma_f32_16x16x32_bf16 v[204:207], v[200:203], v[196:199], v[204:207]\n" \
"v_mfma_f32_16x16x32_f16 v[52:55], v[152:155], v[164:167], v[52:55]\n" \
"v_pk_add_f16 v162, v162, v166\n" \
"v_pk_add_f16 v163, v163, v167\n" \
"v_and_b32 v186, s92, v186\n" \
"v_and_b32 v187, s92, v187\n" \
"v_mfma_f32_16x16x32_f16 v[64:67], v[156:159], v[164:167], v[64:67]\n" \
"ds_read_b128 v[180:183], v240 offset:8704\n" \
"buffer_store_dwordx4 v[184:187], v234, s[36:39], s82 offen offset:1024 sc1\n" \
"ds_read_b64_tr_b16 v[144:145], v241 offset:512\n" \
"v_mfma_f32_16x16x32_f16 v[56:59], v[136:139], v[160:163], v[56:59]\n" \
"buffer_load_dwordx4 v232, s[36:39], s80 offen offset:2048 sc1 lds\n" \
"ds_read_b64_tr_b16 v[146:147], v241 offset:8704\n" \
"v_mfma_f32_16x16x32_f16 v[68:71], v[140:143], v[160:163], v[68:71]\n" \
"ds_read_b128 v[196:199], v249 offset:3072\n" \
"ds_read_b64_tr_b16 v[200:201], v247 offset:12288\n" \
"ds_read_b64_tr_b16 v[148:149], v242 offset:512\n" \
"v_mfma_f32_16x16x32_f16 v[72:75], v[128:131], v[168:171], v[72:75]\n" \
"ds_read_b128 v[160:163], v240 offset:0\n" \
"ds_read_b64_tr_b16 v[202:203], v247 offset:12800\n" \
"v_mfma_f32_16x16x32_f16 v[84:87], v[132:135], v[168:171], v[84:87]\n" \
"s_waitcnt lgkmcnt(8)\n" \
"v_pk_add_f16 v168, v168, v172\n" \
"v_pk_add_f16 v169, v169, v173\n" \
"v_mfma_f32_16x16x32_f16 v[76:79], v[152:155], v[172:175], v[76:79]\n" \
"v_pk_add_f16 v170, v170, v174\n" \
"v_pk_add_f16 v171, v171, v175\n" \
"v_mfma_f32_16x16x32_f16 v[88:91], v[156:159], v[172:175], v[88:91]\n" \
"ds_read_b128 v[164:167], v240 offset:512\n" \
"ds_read_b64_tr_b16 v[150:151], v242 offset:8704\n" \
"v_mfma_f32_16x16x32_f16 v[80:83], v[136:139], v[168:171], v[80:83]\n" \
"buffer_load_dwordx4 v232, s[36:39], s80 offen offset:3072 sc1 lds\n" \
"v_mfma_f32_16x16x32_f16 v[92:95], v[140:143], v[168:171], v[92:95]\n" \
"v_mfma_f32_16x16x32_f16 v[96:99], v[128:131], v[176:179], v[96:99]\n" \
"s_waitcnt lgkmcnt(8)\n" \
"v_pk_add_f16 v128, v120, v144\n" \
"v_pk_add_f16 v129, v121, v145\n" \
"ds_read_b128 v[168:171], v240 offset:2048\n" \
"s_add_i32 m0, s45, 0x1000\n" \
"s_mov_b32 s83, 0x38000\n" \
"s_waitcnt lgkmcnt(3)\n" \
"v_mfma_f32_16x16x32_bf16 v[204:207], v[200:203], v[196:199], v[204:207]\n" \
"v_mfma_f32_16x16x32_f16 v[108:111], v[132:135], v[176:179], v[108:111]\n" \
"v_pk_add_f16 v176, v176, v180\n" \
"v_pk_add_f16 v177, v177, v181\n" \
"ds_read_b128 v[172:175], v240 offset:2560\n" \
"buffer_load_dwordx4 v233, s[36:39], s83 offen sc1 lds\n" \
"v_mfma_f32_16x16x32_f16 v[100:103], v[152:155], v[180:183], v[100:103]\n" \
"v_pk_add_f16 v178, v178, v182\n" \
"v_pk_add_f16 v179, v179, v183\n" \
"v_mfma_f32_16x16x32_f16 v[112:115], v[156:159], v[180:183], v[112:115]\n" \
"v_pk_add_f16 v130, v122, v146\n" \
"v_pk_add_f16 v131, v123, v147\n" \
"v_mfma_f32_16x16x32_f16 v[104:107], v[136:139], v[176:179], v[104:107]\n" \
"v_pk_add_f16 v132, v124, v148\n" \
"v_pk_add_f16 v133, v125, v149\n" \
"v_mfma_f32_16x16x32_f16 v[116:119], v[140:143], v[176:179], v[116:119]\n" \
"s_waitcnt lgkmcnt(2)\n" \
"v_pk_add_f16 v134, v126, v150\n" \
"v_pk_add_f16 v135, v127, v151\n" \
"s_waitcnt vmcnt(8) lgkmcnt(0)\n" \
"s_barrier\n" \
"v_mfma_f32_16x16x32_f16 v[0:3], v[120:123], v[160:163], v[0:3]\n" \
"ds_read_b128 v[176:179], v240 offset:24576\n" \
"s_mov_b32 s81, 0x8012000\n" \
"ds_read_b64_tr_b16 v[184:185], v236 offset:36864\n" \
"s_add_i32 m0, s45, 0x2000\n" \
"s_mov_b32 s83, 0x40000\n" \
"v_mfma_f32_16x16x32_f16 v[12:15], v[124:127], v[160:163], v[12:15]\n" \
"v_pk_add_f16 v160, v160, v164\n" \
"v_pk_add_f16 v161, v161, v165\n" \
"buffer_load_dword v253, v252, s[36:39], s81 offen sc1\n" \
"buffer_load_dwordx4 v233, s[36:39], s83 offen sc1 lds\n" \
"v_mfma_f32_16x16x32_f16 v[4:7], v[144:147], v[164:167], v[4:7]\n" \
"v_pk_add_f16 v162, v162, v166\n" \
"v_pk_add_f16 v163, v163, v167\n" \
"ds_read_b64_tr_b16 v[188:189], v237 offset:36864\n" \
"v_mfma_f32_16x16x32_f16 v[16:19], v[148:151], v[164:167], v[16:19]\n" \
"ds_read_b128 v[180:183], v240 offset:25088\n" \
"s_add_i32 m0, s44, 0x0\n" \
"s_add_i32 s80, s46, 0x5000000\n" \
"v_mfma_f32_16x16x32_f16 v[8:11], v[128:131], v[160:163], v[8:11]\n" \
"buffer_load_dwordx4 v232, s[36:39], s80 offen sc1 lds\n" \
"s_waitcnt lgkmcnt(2)\n" \
"v_mfma_f32_16x16x16_bf16 v[184:187], v[184:185], v[192:193], 0\n" \
"v_mfma_f32_16x16x32_f16 v[20:23], v[132:135], v[160:163], v[20:23]\n" \
"ds_read_b128 v[196:199], v249 offset:4096\n" \
"ds_read_b64_tr_b16 v[200:201], v247 offset:16384\n" \
"s_waitcnt lgkmcnt(3)\n" \
"v_mfma_f32_16x16x16_bf16 v[188:191], v[188:189], v[192:193], 0\n" \
"v_mfma_f32_16x16x32_f16 v[24:27], v[120:123], v[168:171], v[24:27]\n" \
"ds_read_b128 v[160:163], v240 offset:26624\n" \
"ds_read_b64_tr_b16 v[136:137], v241 offset:16384\n" \
"ds_read_b64_tr_b16 v[138:139], v241 offset:24576\n" \
"v_mfma_f32_16x16x32_f16 v[36:39], v[124:127], v[168:171], v[36:39]\n" \
"v_pk_add_f16 v168, v168, v172\n" \
"v_pk_add_f16 v169, v169, v173\n" \
"ds_read_b64_tr_b16 v[202:203], v247 offset:16896\n" \
"ds_read_b64_tr_b16 v[140:141], v242 offset:16384\n" \
"v_mfma_f32_16x16x32_f16 v[28:31], v[144:147], v[172:175], v[28:31]\n" \
"v_pk_add_f16 v170, v170, v174\n" \
"v_pk_add_f16 v171, v171, v175\n" \
"v_cvt_pk_f16_f32 v184, v184, v185\n" \
"v_mfma_f32_16x16x32_f16 v[40:43], v[148:151], v[172:175], v[40:43]\n" \
"v_cvt_pk_f16_f32 v185, v186, v187\n" \
"v_cvt_pk_f16_f32 v186, v188, v189\n" \
"ds_read_b128 v[164:167], v240 offset:27136\n" \
"v_mfma_f32_16x16x32_f16 v[32:35], v[128:131], v[168:171], v[32:35]\n" \
"v_cvt_pk_f16_f32 v187, v190, v191\n" \
"v_add_u32 v184, s91, v184\n" \
"buffer_load_dwordx4 v232, s[36:39], s80 offen offset:1024 sc1 lds\n" \
"v_mfma_f32_16x16x32_f16 v[44:47], v[132:135], v[168:171], v[44:47]\n" \
"v_add_u32 v185, s91, v185\n" \
"v_add_u32 v186, s91, v186\n" \
"ds_read_b64_tr_b16 v[142:143], v242 offset:24576\n" \
"v_mfma_f32_16x16x32_f16 v[48:51], v[120:123], v[176:179], v[48:51]\n" \
"v_add_u32 v187, s91, v187\n" \
"v_and_b32 v184, s92, v184\n" \
"ds_read_b128 v[168:171], v240 offset:28672\n" \
"v_mfma_f32_16x16x32_f16 v[60:63], v[124:127], v[176:179], v[60:63]\n" \
"s_waitcnt lgkmcnt(5)\n" \
"v_pk_add_f16 v176, v176, v180\n" \
"v_pk_add_f16 v177, v177, v181\n" \
"v_and_b32 v185, s92, v185\n" \
"s_add_i32 s82, s50, 0x7000000\n" \
"s_waitcnt lgkmcnt(3)\n" \
"v_mfma_f32_16x16x32_bf16 v[204:207], v[200:203], v[196:199], v[204:207]\n" \
"v_mfma_f32_16x16x32_f16 v[52:55], v[144:147], v[180:183], v[52:55]\n" \
"v_pk_add_f16 v178, v178, v182\n" \
"v_pk_add_f16 v179, v179, v183\n" \
"v_and_b32 v186, s92, v186\n" \
"v_and_b32 v187, s92, v187\n" \
"v_mfma_f32_16x16x32_f16 v[64:67], v[148:151], v[180:183], v[64:67]\n" \
"ds_read_b128 v[172:175], v240 offset:29184\n" \
"buffer_store_dwordx4 v[184:187], v234, s[36:39], s82 offen sc1\n" \
"ds_read_b64_tr_b16 v[152:153], v241 offset:16896\n" \
"v_mfma_f32_16x16x32_f16 v[56:59], v[128:131], v[176:179], v[56:59]\n" \
"buffer_load_dwordx4 v232, s[36:39], s80 offen offset:2048 sc1 lds\n" \
"ds_read_b64_tr_b16 v[154:155], v241 offset:25088\n" \
"v_mfma_f32_16x16x32_f16 v[68:71], v[132:135], v[176:179], v[68:71]\n" \
"ds_read_b128 v[196:199], v249 offset:5120\n" \
"ds_read_b64_tr_b16 v[200:201], v247 offset:20480\n" \
"ds_read_b64_tr_b16 v[156:157], v242 offset:16896\n" \
"v_mfma_f32_16x16x32_f16 v[72:75], v[120:123], v[160:163], v[72:75]\n" \
"ds_read_b128 v[176:179], v240 offset:20480\n" \
"ds_read_b64_tr_b16 v[202:203], v247 offset:20992\n" \
"v_mfma_f32_16x16x32_f16 v[84:87], v[124:127], v[160:163], v[84:87]\n" \
"s_waitcnt lgkmcnt(8)\n" \
"v_pk_add_f16 v160, v160, v164\n" \
"v_pk_add_f16 v161, v161, v165\n" \
"v_mfma_f32_16x16x32_f16 v[76:79], v[144:147], v[164:167], v[76:79]\n" \
"v_pk_add_f16 v162, v162, v166\n" \
"v_pk_add_f16 v163, v163, v167\n" \
"v_mfma_f32_16x16x32_f16 v[88:91], v[148:151], v[164:167], v[88:91]\n" \
"ds_read_b128 v[180:183], v240 offset:20992\n" \
"ds_read_b64_tr_b16 v[158:159], v242 offset:25088\n" \
"v_mfma_f32_16x16x32_f16 v[80:83], v[128:131], v[160:163], v[80:83]\n" \
"buffer_load_dwordx4 v232, s[36:39], s80 offen offset:3072 sc1 lds\n" \
"v_mfma_f32_16x16x32_f16 v[92:95], v[132:135], v[160:163], v[92:95]\n" \
"v_mfma_f32_16x16x32_f16 v[96:99], v[120:123], v[168:171], v[96:99]\n" \
"s_waitcnt lgkmcnt(8)\n" \
"v_pk_add_f16 v120, v136, v152\n" \
"v_pk_add_f16 v121, v137, v153\n" \
"ds_read_b128 v[160:163], v240 offset:22528\n" \
"s_add_i32 m0, s45, 0x3000\n" \
"s_mov_b32 s83, 0x48000\n" \
"s_waitcnt lgkmcnt(3)\n" \
"v_mfma_f32_16x16x32_bf16 v[204:207], v[200:203], v[196:199], v[204:207]\n" \
"v_mfma_f32_16x16x32_f16 v[108:111], v[124:127], v[168:171], v[108:111]\n" \
"v_pk_add_f16 v168, v168, v172\n" \
"v_pk_add_f16 v169, v169, v173\n" \
"ds_read_b128 v[164:167], v240 offset:23040\n" \
"buffer_load_dwordx4 v233, s[36:39], s83 offen sc1 lds\n" \
"v_mfma_f32_16x16x32_f16 v[100:103], v[144:147], v[172:175], v[100:103]\n" \
"v_pk_add_f16 v170, v170, v174\n" \
"v_pk_add_f16 v171, v171, v175\n" \
"v_mfma_f32_16x16x32_f16 v[112:115], v[148:151], v[172:175], v[112:115]\n" \
"v_pk_add_f16 v122, v138, v154\n" \
"v_pk_add_f16 v123, v139, v155\n" \
"v_mfma_f32_16x16x32_f16 v[104:107], v[128:131], v[168:171], v[104:107]\n" \
"v_pk_add_f16 v124, v140, v156\n" \
"v_pk_add_f16 v125, v141, v157\n" \
"v_mfma_f32_16x16x32_f16 v[116:119], v[132:135], v[168:171], v[116:119]\n" \
"s_waitcnt lgkmcnt(2)\n" \
"v_pk_add_f16 v126, v142, v158\n" \
"v_pk_add_f16 v127, v143, v159\n" \
"s_waitcnt vmcnt(7) lgkmcnt(0)\n" \
"s_barrier\n" \
"v_mfma_f32_16x16x32_f16 v[0:3], v[136:139], v[176:179], v[0:3]\n" \
"ds_read_b128 v[168:171], v240 offset:45056\n" \
"ds_read_b64_tr_b16 v[184:185], v238 offset:36864\n" \
"ds_read_b64_tr_b16 v[188:189], v239 offset:36864\n" \
"s_add_i32 m0, s45, 0x4000\n" \
"s_mov_b32 s83, 0x50000\n" \
"v_mfma_f32_16x16x32_f16 v[12:15], v[140:143], v[176:179], v[12:15]\n" \
"v_pk_add_f16 v176, v176, v180\n" \
"v_pk_add_f16 v177, v177, v181\n" \
"buffer_load_dwordx4 v233, s[36:39], s83 offen sc1 lds\n" \
"v_mfma_f32_16x16x32_f16 v[4:7], v[152:155], v[180:183], v[4:7]\n" \
"v_pk_add_f16 v178, v178, v182\n" \
"v_pk_add_f16 v179, v179, v183\n" \
"v_cmp_eq_u32 vcc, 0, v253\n" \
"s_cbranch_vccnz .Ls09_%=\n" \
".Lr09_%=:\n" \
"v_mfma_f32_16x16x32_f16 v[16:19], v[156:159], v[180:183], v[16:19]\n" \
"ds_read_b128 v[172:175], v240 offset:45568\n" \
"s_add_i32 m0, s44, 0x4000\n" \
"s_add_i32 s80, s46, 0x5400000\n" \
"v_mfma_f32_16x16x32_f16 v[8:11], v[120:123], v[176:179], v[8:11]\n" \
"buffer_load_dwordx4 v232, s[36:39], s80 offen sc1 lds\n" \
"ds_read_b64_tr_b16 v[128:129], v241 offset:32768\n" \
"s_waitcnt lgkmcnt(3)\n" \
"v_mfma_f32_16x16x16_bf16 v[184:187], v[184:185], v[192:193], 0\n" \
"v_mfma_f32_16x16x32_f16 v[20:23], v[124:127], v[176:179], v[20:23]\n" \
"ds_read_b128 v[196:199], v249 offset:6144\n" \
"ds_read_b64_tr_b16 v[200:201], v247 offset:0\n" \
"s_waitcnt lgkmcnt(4)\n" \
"v_mfma_f32_16x16x16_bf16 v[188:191], v[188:189], v[192:193], 0\n" \
"v_mfma_f32_16x16x32_f16 v[24:27], v[136:139], v[160:163], v[24:27]\n" \
"ds_read_b128 v[176:179], v240 offset:47104\n" \
"s_add_i32 s84, s53, 0x8016000\n" \
"ds_read_b64_tr_b16 v[130:131], v241 offset:40960\n" \
"v_mfma_f32_16x16x32_f16 v[36:39], v[140:143], v[160:163], v[36:39]\n" \
"v_pk_add_f16 v160, v160, v164\n" \
"v_pk_add_f16 v161, v161, v165\n" \
"buffer_store_dword v240, v243, s[36:39], s84 offen sc1\n" \
"v_mfma_f32_16x16x32_f16 v[28:31], v[152:155], v[164:167], v[28:31]\n" \
"v_pk_add_f16 v162, v162, v166\n" \
"v_pk_add_f16 v163, v163, v167\n" \
"v_cvt_pk_f16_f32 v184, v184, v185\n" \
"ds_read_b64_tr_b16 v[202:203], v247 offset:512\n" \
"v_mfma_f32_16x16x32_f16 v[40:43], v[156:159], v[164:167], v[40:43]\n" \
"v_cvt_pk_f16_f32 v185, v186, v187\n" \
"v_cvt_pk_f16_f32 v186, v188, v189\n" \
"ds_read_b128 v[180:183], v240 offset:47616\n" \
"ds_read_b64_tr_b16 v[132:133], v242 offset:32768\n" \
"v_mfma_f32_16x16x32_f16 v[32:35], v[120:123], v[160:163], v[32:35]\n" \
"v_cvt_pk_f16_f32 v187, v190, v191\n" \
"v_add_u32 v184, s91, v184\n" \
"buffer_load_dwordx4 v232, s[36:39], s80 offen offset:1024 sc1 lds\n" \
"v_mfma_f32_16x16x32_f16 v[44:47], v[124:127], v[160:163], v[44:47]\n" \
"v_add_u32 v185, s91, v185\n" \
"v_add_u32 v186, s91, v186\n" \
"ds_read_b64_tr_b16 v[134:135], v242 offset:40960\n" \
"v_mfma_f32_16x16x32_f16 v[48:51], v[136:139], v[168:171], v[48:51]\n" \
"v_add_u32 v187, s91, v187\n" \
"v_and_b32 v184, s92, v184\n" \
"ds_read_b128 v[160:163], v240 offset:49152\n" \
"v_mfma_f32_16x16x32_f16 v[60:63], v[140:143], v[168:171], v[60:63]\n" \
"s_waitcnt lgkmcnt(5)\n" \
"v_pk_add_f16 v168, v168, v172\n" \
"v_pk_add_f16 v169, v169, v173\n" \
"v_and_b32 v185, s92, v185\n" \
"s_waitcnt lgkmcnt(4)\n" \
"v_mfma_f32_16x16x32_bf16 v[204:207], v[200:203], v[196:199], v[204:207]\n" \
"v_mfma_f32_16x16x32_f16 v[52:55], v[152:155], v[172:175], v[52:55]\n" \
"v_pk_add_f16 v170, v170, v174\n" \
"v_pk_add_f16 v171, v171, v175\n" \
"v_and_b32 v186, s92, v186\n" \
"v_and_b32 v187, s92, v187\n" \
"v_mfma_f32_16x16x32_f16 v[64:67], v[156:159], v[172:175], v[64:67]\n" \
"ds_read_b128 v[164:167], v240 offset:49664\n" \
"buffer_store_dwordx4 v[184:187], v234, s[36:39], s82 offen offset:1024 sc1\n" \
"ds_read_b64_tr_b16 v[144:145], v241 offset:33280\n" \
"v_mfma_f32_16x16x32_f16 v[56:59], v[120:123], v[168:171], v[56:59]\n" \
"buffer_load_dwordx4 v232, s[36:39], s80 offen offset:2048 sc1 lds\n" \
"ds_read_b64_tr_b16 v[146:147], v241 offset:41472\n" \
"v_mfma_f32_16x16x32_f16 v[68:71], v[124:127], v[168:171], v[68:71]\n" \
"ds_read_b128 v[196:199], v249 offset:7168\n" \
"ds_read_b64_tr_b16 v[200:201], v247 offset:4096\n" \
"ds_read_b64_tr_b16 v[148:149], v242 offset:33280\n" \
"v_mfma_f32_16x16x32_f16 v[72:75], v[136:139], v[176:179], v[72:75]\n" \
"ds_read_b128 v[168:171], v240 offset:40960\n" \
"ds_read_b64_tr_b16 v[202:203], v247 offset:4608\n" \
"v_mfma_f32_16x16x32_f16 v[84:87], v[140:143], v[176:179], v[84:87]\n" \
"s_waitcnt lgkmcnt(8)\n" \
"v_pk_add_f16 v176, v176, v180\n" \
"v_pk_add_f16 v177, v177, v181\n" \
"v_mfma_f32_16x16x32_f16 v[76:79], v[152:155], v[180:183], v[76:79]\n" \
"v_pk_add_f16 v178, v178, v182\n" \
"v_pk_add_f16 v179, v179, v183\n" \
"v_mfma_f32_16x16x32_f16 v[88:91], v[156:159], v[180:183], v[88:91]\n" \
"ds_read_b128 v[172:175], v240 offset:41472\n" \
"ds_read_b64_tr_b16 v[150:151], v242 offset:41472\n" \
"v_mfma_f32_16x16x32_f16 v[80:83], v[120:123], v[176:179], v[80:83]\n" \
"buffer_load_dwordx4 v232, s[36:39], s80 offen offset:3072 sc1 lds\n" \
"v_mfma_f32_16x16x32_f16 v[92:95], v[124:127], v[176:179], v[92:95]\n" \
"v_mfma_f32_16x16x32_f16 v[96:99], v[136:139], v[160:163], v[96:99]\n" \
"s_waitcnt lgkmcnt(8)\n" \
"v_pk_add_f16 v136, v128, v144\n" \
"v_pk_add_f16 v137, v129, v145\n" \
"ds_read_b128 v[176:179], v240 offset:43008\n" \
"s_add_i32 m0, s45, 0x5000\n" \
"s_mov_b32 s83, 0x58000\n" \
"s_waitcnt lgkmcnt(3)\n" \
"v_mfma_f32_16x16x32_bf16 v[204:207], v[200:203], v[196:199], v[204:207]\n" \
"v_mfma_f32_16x16x32_f16 v[108:111], v[140:143], v[160:163], v[108:111]\n" \
"v_pk_add_f16 v160, v160, v164\n" \
"v_pk_add_f16 v161, v161, v165\n" \
"ds_read_b128 v[180:183], v240 offset:43520\n" \
"buffer_load_dwordx4 v233, s[36:39], s83 offen sc1 lds\n" \
"v_mfma_f32_16x16x32_f16 v[100:103], v[152:155], v[164:167], v[100:103]\n" \
"v_pk_add_f16 v162, v162, v166\n" \
"v_pk_add_f16 v163, v163, v167\n" \
"v_mfma_f32_16x16x32_f16 v[112:115], v[156:159], v[164:167], v[112:115]\n" \
"v_pk_add_f16 v138, v130, v146\n" \
"v_pk_add_f16 v139, v131, v147\n" \
"v_mfma_f32_16x16x32_f16 v[104:107], v[120:123], v[160:163], v[104:107]\n" \
"v_pk_add_f16 v140, v132, v148\n" \
"v_pk_add_f16 v141, v133, v149\n" \
"v_mfma_f32_16x16x32_f16 v[116:119], v[124:127], v[160:163], v[116:119]\n" \
"s_waitcnt lgkmcnt(2)\n" \
"v_pk_add_f16 v142, v134, v150\n" \
"v_pk_add_f16 v143, v135, v151\n" \
"s_waitcnt vmcnt(8) lgkmcnt(0)\n" \
"s_barrier\n" \
"v_mfma_f32_16x16x32_f16 v[0:3], v[128:131], v[168:171], v[0:3]\n" \
"ds_read_b128 v[160:163], v240 offset:4096\n" \
"ds_read_b64_tr_b16 v[184:185], v236 offset:40960\n" \
"s_add_i32 m0, s45, 0x0\n" \
"s_mov_b32 s83, 0x60000\n" \
"v_mfma_f32_16x16x32_f16 v[12:15], v[132:135], v[168:171], v[12:15]\n" \
"v_pk_add_f16 v168, v168, v172\n" \
"v_pk_add_f16 v169, v169, v173\n" \
"ds_read_b64_tr_b16 v[188:189], v237 offset:40960\n" \
"buffer_load_dwordx4 v233, s[36:39], s83 offen sc1 lds\n" \
"v_mfma_f32_16x16x32_f16 v[4:7], v[144:147], v[172:175], v[4:7]\n" \
"v_pk_add_f16 v170, v170, v174\n" \
"v_pk_add_f16 v171, v171, v175\n" \
"v_mfma_f32_16x16x32_f16 v[16:19], v[148:151], v[172:175], v[16:19]\n" \
"ds_read_b128 v[164:167], v240 offset:4608\n" \
"s_add_i32 m0, s44, 0x8000\n" \
"s_add_i32 s80, s46, 0x5800000\n" \
"v_mfma_f32_16x16x32_f16 v[8:11], v[136:139], v[168:171], v[8:11]\n" \
"buffer_load_dwordx4 v232, s[36:39], s80 offen sc1 lds\n" \
"v_mfma_f32_16x16x32_f16 v[20:23], v[140:143], v[168:171], v[20:23]\n" \
"ds_read_b128 v[196:199], v249 offset:0\n" \
"ds_read_b64_tr_b16 v[200:201], v247 offset:8192\n" \
"s_waitcnt lgkmcnt(4)\n" \
"v_mfma_f32_16x16x16_bf16 v[184:187], v[184:185], v[192:193], 0\n" \
"v_mfma_f32_16x16x32_f16 v[24:27], v[128:131], v[176:179], v[24:27]\n" \
"ds_read_b128 v[168:171], v240 offset:6144\n" \
"ds_read_b64_tr_b16 v[120:121], v241 offset:0\n" \
"ds_read_b64_tr_b16 v[122:123], v241 offset:8192\n" \
"s_waitcnt lgkmcnt(6)\n" \
"v_mfma_f32_16x16x16_bf16 v[188:191], v[188:189], v[192:193], 0\n" \
"v_mfma_f32_16x16x32_f16 v[36:39], v[132:135], v[176:179], v[36:39]\n" \
"v_pk_add_f16 v176, v176, v180\n" \
"v_pk_add_f16 v177, v177, v181\n" \
"ds_read_b64_tr_b16 v[202:203], v247 offset:8704\n" \
"v_mfma_f32_16x16x32_f16 v[28:31], v[144:147], v[180:183], v[28:31]\n" \
"v_pk_add_f16 v178, v178, v182\n" \
"v_pk_add_f16 v179, v179, v183\n" \
"ds_read_b64_tr_b16 v[124:125], v242 offset:0\n" \
"v_mfma_f32_16x16x32_f16 v[40:43], v[148:151], v[180:183], v[40:43]\n" \
"v_cvt_pk_f16_f32 v184, v184, v185\n" \
"ds_read_b128 v[172:175], v240 offset:6656\n" \
"ds_read_b64_tr_b16 v[126:127], v242 offset:8192\n" \
"v_mfma_f32_16x16x32_f16 v[32:35], v[136:139], v[176:179], v[32:35]\n" \
"v_cvt_pk_f16_f32 v185, v186, v187\n" \
"v_cvt_pk_f16_f32 v186, v188, v189\n" \
"buffer_load_dwordx4 v232, s[36:39], s80 offen offset:1024 sc1 lds\n" \
"v_mfma_f32_16x16x32_f16 v[44:47], v[140:143], v[176:179], v[44:47]\n" \
"v_cvt_pk_f16_f32 v187, v190, v191\n" \
"v_add_u32 v184, s91, v184\n" \
"v_mfma_f32_16x16x32_f16 v[48:51], v[128:131], v[160:163], v[48:51]\n" \
"v_add_u32 v185, s91, v185\n" \
"v_add_u32 v186, s91, v186\n" \
"ds_read_b128 v[176:179], v240 offset:8192\n" \
"v_mfma_f32_16x16x32_f16 v[60:63], v[132:135], v[160:163], v[60:63]\n" \
"s_waitcnt lgkmcnt(5)\n" \
"v_pk_add_f16 v160, v160, v164\n" \
"v_pk_add_f16 v161, v161, v165\n" \
"v_add_u32 v187, s91, v187\n" \
"v_and_b32 v184, s92, v184\n" \
"s_add_i32 s82, s51, 0x7800000\n" \
"s_waitcnt lgkmcnt(4)\n" \
"v_mfma_f32_16x16x32_bf16 v[204:207], v[200:203], v[196:199], v[204:207]\n" \
"v_mfma_f32_16x16x32_f16 v[52:55], v[144:147], v[164:167], v[52:55]\n" \
"v_pk_add_f16 v162, v162, v166\n" \
"v_pk_add_f16 v163, v163, v167\n" \
"v_and_b32 v185, s92, v185\n" \
"v_and_b32 v186, s92, v186\n" \
"v_and_b32 v187, s92, v187\n" \
"v_mfma_f32_16x16x32_f16 v[64:67], v[148:151], v[164:167], v[64:67]\n" \
"ds_read_b128 v[180:183], v240 offset:8704\n" \
"buffer_store_dwordx4 v[184:187], v234, s[36:39], s82 offen sc1\n" \
"ds_read_b64_tr_b16 v[152:153], v241 offset:512\n" \
"v_mfma_f32_16x16x32_f16 v[56:59], v[136:139], v[160:163], v[56:59]\n" \
"buffer_load_dwordx4 v232, s[36:39], s80 offen offset:2048 sc1 lds\n" \
"ds_read_b128 v[196:199], v249 offset:1024\n" \
"v_mfma_f32_16x16x32_f16 v[68:71], v[140:143], v[160:163], v[68:71]\n" \
"ds_read_b64_tr_b16 v[184:185], v238 offset:40960\n" \
"ds_read_b64_tr_b16 v[188:189], v239 offset:40960\n" \
"ds_read_b64_tr_b16 v[154:155], v241 offset:8704\n" \
"v_mfma_f32_16x16x32_f16 v[72:75], v[128:131], v[168:171], v[72:75]\n" \
"ds_read_b128 v[160:163], v240 offset:0\n" \
"ds_read_b64_tr_b16 v[200:201], v247 offset:12288\n" \
"ds_read_b64_tr_b16 v[202:203], v247 offset:12800\n" \
"v_mfma_f32_16x16x32_f16 v[84:87], v[132:135], v[168:171], v[84:87]\n" \
"s_waitcnt lgkmcnt(9)\n" \
"v_pk_add_f16 v168, v168, v172\n" \
"v_pk_add_f16 v169, v169, v173\n" \
"ds_read_b64_tr_b16 v[156:157], v242 offset:512\n" \
"v_mfma_f32_16x16x32_f16 v[76:79], v[144:147], v[172:175], v[76:79]\n" \
"v_pk_add_f16 v170, v170, v174\n" \
"v_pk_add_f16 v171, v171, v175\n" \
"s_waitcnt lgkmcnt(6)\n" \
"v_mfma_f32_16x16x16_bf16 v[184:187], v[184:185], v[192:193], 0\n" \
"v_mfma_f32_16x16x32_f16 v[88:91], v[148:151], v[172:175], v[88:91]\n" \
"ds_read_b128 v[164:167], v240 offset:512\n" \
"ds_read_b64_tr_b16 v[158:159], v242 offset:8704\n" \
"s_waitcnt lgkmcnt(7)\n" \
"v_mfma_f32_16x16x16_bf16 v[188:191], v[188:189], v[192:193], 0\n" \
"v_mfma_f32_16x16x32_f16 v[80:83], v[136:139], v[168:171], v[80:83]\n" \
"buffer_load_dwordx4 v232, s[36:39], s80 offen offset:3072 sc1 lds\n" \
"v_mfma_f32_16x16x32_f16 v[92:95], v[140:143], v[168:171], v[92:95]\n" \
"v_mfma_f32_16x16x32_f16 v[96:99], v[128:131], v[176:179], v[96:99]\n" \
"v_pk_add_f16 v128, v120, v152\n" \
"v_pk_add_f16 v129, v121, v153\n" \
"v_cvt_pk_f16_f32 v184, v184, v185\n" \
"v_cvt_pk_f16_f32 v185, v186, v187\n" \
"ds_read_b128 v[168:171], v240 offset:2048\n" \
"s_add_i32 m0, s45, 0x1000\n" \
"s_mov_b32 s83, 0x68000\n" \
"s_waitcnt lgkmcnt(4)\n" \
"v_mfma_f32_16x16x32_bf16 v[204:207], v[200:203], v[196:199], v[204:207]\n" \
"v_mfma_f32_16x16x32_f16 v[108:111], v[132:135], v[176:179], v[108:111]\n" \
"v_pk_add_f16 v176, v176, v180\n" \
"v_pk_add_f16 v177, v177, v181\n" \
"v_cvt_pk_f16_f32 v186, v188, v189\n" \
"v_cvt_pk_f16_f32 v187, v190, v191\n" \
"ds_read_b128 v[172:175], v240 offset:2560\n" \
"buffer_load_dwordx4 v233, s[36:39], s83 offen sc1 lds\n" \
"v_mfma_f32_16x16x32_f16 v[100:103], v[144:147], v[180:183], v[100:103]\n" \
"v_pk_add_f16 v178, v178, v182\n" \
"v_pk_add_f16 v179, v179, v183\n" \
"v_add_u32 v184, s91, v184\n" \
"v_add_u32 v185, s91, v185\n" \
"v_add_u32 v186, s91, v186\n" \
"v_mfma_f32_16x16x32_f16 v[112:115], v[148:151], v[180:183], v[112:115]\n" \
"v_pk_add_f16 v130, v122, v154\n" \
"v_pk_add_f16 v131, v123, v155\n" \
"v_add_u32 v187, s91, v187\n" \
"v_and_b32 v184, s92, v184\n" \
"v_mfma_f32_16x16x32_f16 v[104:107], v[136:139], v[176:179], v[104:107]\n" \
"s_waitcnt lgkmcnt(1)\n" \
"v_pk_add_f16 v132, v124, v156\n" \
"v_pk_add_f16 v133, v125, v157\n" \
"v_and_b32 v185, s92, v185\n" \
"v_and_b32 v186, s92, v186\n" \
"v_and_b32 v187, s92, v187\n" \
"v_mfma_f32_16x16x32_f16 v[116:119], v[140:143], v[176:179], v[116:119]\n" \
"v_pk_add_f16 v134, v126, v158\n" \
"v_pk_add_f16 v135, v127, v159\n" \
"buffer_store_dwordx4 v[184:187], v234, s[36:39], s82 offen offset:1024 sc1\n" \
"s_waitcnt vmcnt(8) lgkmcnt(0)\n" \
"s_barrier\n" \
"v_mfma_f32_16x16x32_f16 v[0:3], v[120:123], v[160:163], v[0:3]\n" \
"ds_read_b128 v[176:179], v240 offset:24576\n" \
"s_add_i32 m0, s45, 0x2000\n" \
"s_mov_b32 s83, 0x70000\n" \
"v_mfma_f32_16x16x32_f16 v[12:15], v[124:127], v[160:163], v[12:15]\n" \
"v_pk_add_f16 v160, v160, v164\n" \
"v_pk_add_f16 v161, v161, v165\n" \
"buffer_load_dwordx4 v233, s[36:39], s83 offen sc1 lds\n" \
"v_mfma_f32_16x16x32_f16 v[4:7], v[152:155], v[164:167], v[4:7]\n" \
"v_pk_add_f16 v162, v162, v166\n" \
"v_pk_add_f16 v163, v163, v167\n" \
"v_mfma_f32_16x16x32_f16 v[16:19], v[156:159], v[164:167], v[16:19]\n" \
"ds_read_b128 v[180:183], v240 offset:25088\n" \
"s_add_i32 m0, s44, 0x0\n" \
"s_add_i32 s80, s46, 0x5c00000\n" \
"v_mfma_f32_16x16x32_f16 v[8:11], v[128:131], v[160:163], v[8:11]\n" \
"buffer_load_dwordx4 v232, s[36:39], s80 offen sc1 lds\n" \
"ds_read_b64_tr_b16 v[136:137], v241 offset:16384\n" \
"v_mfma_f32_16x16x32_f16 v[20:23], v[132:135], v[160:163], v[20:23]\n" \
"ds_read_b128 v[196:199], v249 offset:2048\n" \
"ds_read_b64_tr_b16 v[200:201], v247 offset:16384\n" \
"v_mfma_f32_16x16x32_f16 v[24:27], v[120:123], v[168:171], v[24:27]\n" \
"ds_read_b128 v[160:163], v240 offset:26624\n" \
"s_add_i32 s84, s54, 0x8018000\n" \
"ds_read_b64_tr_b16 v[138:139], v241 offset:24576\n" \
"v_mfma_f32_16x16x32_f16 v[36:39], v[124:127], v[168:171], v[36:39]\n" \
"v_pk_add_f16 v168, v168, v172\n" \
"v_pk_add_f16 v169, v169, v173\n" \
"buffer_store_dword v240, v243, s[36:39], s84 offen sc1\n" \
"v_mfma_f32_16x16x32_f16 v[28:31], v[152:155], v[172:175], v[28:31]\n" \
"v_pk_add_f16 v170, v170, v174\n" \
"v_pk_add_f16 v171, v171, v175\n" \
"v_mfma_f32_16x16x32_f16 v[40:43], v[156:159], v[172:175], v[40:43]\n" \
"ds_read_b128 v[164:167], v240 offset:27136\n" \
"ds_read_b64_tr_b16 v[202:203], v247 offset:16896\n" \
"v_mfma_f32_16x16x32_f16 v[32:35], v[128:131], v[168:171], v[32:35]\n" \
"buffer_load_dwordx4 v232, s[36:39], s80 offen offset:1024 sc1 lds\n" \
"ds_read_b64_tr_b16 v[140:141], v242 offset:16384\n" \
"v_mfma_f32_16x16x32_f16 v[44:47], v[132:135], v[168:171], v[44:47]\n" \
"ds_read_b64_tr_b16 v[142:143], v242 offset:24576\n" \
"s_waitcnt lgkmcnt(9)\n" \
"v_mfma_f32_16x16x32_f16 v[48:51], v[120:123], v[176:179], v[48:51]\n" \
"ds_read_b128 v[168:171], v240 offset:28672\n" \
"ds_read_b64_tr_b16 v[144:145], v241 offset:16896\n" \
"v_mfma_f32_16x16x32_f16 v[60:63], v[124:127], v[176:179], v[60:63]\n" \
"v_pk_add_f16 v176, v176, v180\n" \
"v_pk_add_f16 v177, v177, v181\n" \
"s_waitcnt lgkmcnt(4)\n" \
"v_mfma_f32_16x16x32_bf16 v[204:207], v[200:203], v[196:199], v[204:207]\n" \
"v_mfma_f32_16x16x32_f16 v[52:55], v[152:155], v[180:183], v[52:55]\n" \
"v_pk_add_f16 v178, v178, v182\n" \
"v_pk_add_f16 v179, v179, v183\n" \
"v_mfma_f32_16x16x32_f16 v[64:67], v[156:159], v[180:183], v[64:67]\n" \
"ds_read_b128 v[172:175], v240 offset:29184\n" \
"ds_read_b64_tr_b16 v[146:147], v241 offset:25088\n" \
"v_mfma_f32_16x16x32_f16 v[56:59], v[128:131], v[176:179], v[56:59]\n" \
"buffer_load_dwordx4 v232, s[36:39], s80 offen offset:2048 sc1 lds\n" \
"ds_read_b64_tr_b16 v[148:149], v242 offset:16896\n" \
"v_mfma_f32_16x16x32_f16 v[68:71], v[132:135], v[176:179], v[68:71]\n" \
"ds_read_b128 v[196:199], v249 offset:3072\n" \
"ds_read_b64_tr_b16 v[200:201], v247 offset:20480\n" \
"v_mfma_f32_16x16x32_f16 v[72:75], v[120:123], v[160:163], v[72:75]\n" \
"ds_read_b128 v[176:179], v240 offset:20480\n" \
"ds_read_b64_tr_b16 v[202:203], v247 offset:20992\n" \
"v_mfma_f32_16x16x32_f16 v[84:87], v[124:127], v[160:163], v[84:87]\n" \
"v_pk_add_f16 v160, v160, v164\n" \
"v_pk_add_f16 v161, v161, v165\n" \
"v_mfma_f32_16x16x32_f16 v[76:79], v[152:155], v[164:167], v[76:79]\n" \
"v_pk_add_f16 v162, v162, v166\n" \
"v_pk_add_f16 v163, v163, v167\n" \
"v_mfma_f32_16x16x32_f16 v[88:91], v[156:159], v[164:167], v[88:91]\n" \
"ds_read_b128 v[180:183], v240 offset:20992\n" \
"ds_read_b64_tr_b16 v[150:151], v242 offset:25088\n" \
"v_mfma_f32_16x16x32_f16 v[80:83], v[128:131], v[160:163], v[80:83]\n" \
"buffer_load_dwordx4 v232, s[36:39], s80 offen offset:3072 sc1 lds\n" \
"v_mfma_f32_16x16x32_f16 v[92:95], v[132:135], v[160:163], v[92:95]\n" \
"s_waitcnt lgkmcnt(9)\n" \
"v_mfma_f32_16x16x32_f16 v[96:99], v[120:123], v[168:171], v[96:99]\n" \
"v_pk_add_f16 v120, v136, v144\n" \
"v_pk_add_f16 v121, v137, v145\n" \
"ds_read_b128 v[160:163], v240 offset:22528\n" \
"s_add_i32 m0, s45, 0x3000\n" \
"s_mov_b32 s83, 0x78000\n" \
"s_waitcnt lgkmcnt(3)\n" \
"v_mfma_f32_16x16x32_bf16 v[204:207], v[200:203], v[196:199], v[204:207]\n" \
"v_mfma_f32_16x16x32_f16 v[108:111], v[124:127], v[168:171], v[108:111]\n" \
"v_pk_add_f16 v168, v168, v172\n" \
"v_pk_add_f16 v169, v169, v173\n" \
"ds_read_b128 v[164:167], v240 offset:23040\n" \
"buffer_load_dwordx4 v233, s[36:39], s83 offen sc1 lds\n" \
"v_mfma_f32_16x16x32_f16 v[100:103], v[152:155], v[172:175], v[100:103]\n" \
"v_pk_add_f16 v170, v170, v174\n" \
"v_pk_add_f16 v171, v171, v175\n" \
"v_mfma_f32_16x16x32_f16 v[112:115], v[156:159], v[172:175], v[112:115]\n" \
"v_pk_add_f16 v122, v138, v146\n" \
"v_pk_add_f16 v123, v139, v147\n" \
"v_mfma_f32_16x16x32_f16 v[104:107], v[128:131], v[168:171], v[104:107]\n" \
"v_pk_add_f16 v124, v140, v148\n" \
"v_pk_add_f16 v125, v141, v149\n" \
"v_mfma_f32_16x16x32_f16 v[116:119], v[132:135], v[168:171], v[116:119]\n" \
"s_waitcnt lgkmcnt(2)\n" \
"v_pk_add_f16 v126, v142, v150\n" \
"v_pk_add_f16 v127, v143, v151\n" \
"s_waitcnt vmcnt(7) lgkmcnt(0)\n" \
"s_barrier\n" \
"v_mfma_f32_16x16x32_f16 v[0:3], v[136:139], v[176:179], v[0:3]\n" \
"ds_read_b128 v[168:171], v240 offset:45056\n" \
"s_mov_b32 s81, 0x8016000\n" \
"v_mfma_f32_16x16x32_f16 v[12:15], v[140:143], v[176:179], v[12:15]\n" \
"v_pk_add_f16 v176, v176, v180\n" \
"v_pk_add_f16 v177, v177, v181\n" \
"buffer_load_dword v253, v252, s[36:39], s81 offen sc1\n" \
"v_mfma_f32_16x16x32_f16 v[4:7], v[144:147], v[180:183], v[4:7]\n" \
"v_pk_add_f16 v178, v178, v182\n" \
"v_pk_add_f16 v179, v179, v183\n" \
"v_mfma_f32_16x16x32_f16 v[16:19], v[148:151], v[180:183], v[16:19]\n" \
"ds_read_b128 v[172:175], v240 offset:45568\n" \
"s_add_i32 m0, s44, 0x4000\n" \
"s_add_i32 s80, s46, 0x6000000\n" \
"v_mfma_f32_16x16x32_f16 v[8:11], v[120:123], v[176:179], v[8:11]\n" \
"buffer_load_dwordx4 v232, s[36:39], s80 offen sc1 lds\n" \
"ds_read_b64_tr_b16 v[128:129], v241 offset:32768\n" \
"v_mfma_f32_16x16x32_f16 v[20:23], v[124:127], v[176:179], v[20:23]\n" \
"ds_read_b128 v[196:199], v249 offset:4096\n" \
"ds_read_b64_tr_b16 v[200:201], v247 offset:0\n" \
"v_mfma_f32_16x16x32_f16 v[24:27], v[136:139], v[160:163], v[24:27]\n" \
"ds_read_b128 v[176:179], v240 offset:47104\n" \
"s_add_i32 s84, s55, 0x801a000\n" \
"ds_read_b64_tr_b16 v[130:131], v241 offset:40960\n" \
"v_mfma_f32_16x16x32_f16 v[36:39], v[140:143], v[160:163], v[36:39]\n" \
"v_pk_add_f16 v160, v160, v164\n" \
"v_pk_add_f16 v161, v161, v165\n" \
"buffer_store_dword v240, v243, s[36:39], s84 offen sc1\n" \
"v_mfma_f32_16x16x32_f16 v[28:31], v[144:147], v[164:167], v[28:31]\n" \
"v_pk_add_f16 v162, v162, v166\n" \
"v_pk_add_f16 v163, v163, v167\n" \
"v_mfma_f32_16x16x32_f16 v[40:43], v[148:151], v[164:167], v[40:43]\n" \
"ds_read_b128 v[180:183], v240 offset:47616\n" \
"ds_read_b64_tr_b16 v[202:203], v247 offset:512\n" \
"v_mfma_f32_16x16x32_f16 v[32:35], v[120:123], v[160:163], v[32:35]\n" \
"buffer_load_dwordx4 v232, s[36:39], s80 offen offset:1024 sc1 lds\n" \
"ds_read_b64_tr_b16 v[132:133], v242 offset:32768\n" \
"v_mfma_f32_16x16x32_f16 v[44:47], v[124:127], v[160:163], v[44:47]\n" \
"ds_read_b64_tr_b16 v[134:135], v242 offset:40960\n" \
"s_waitcnt lgkmcnt(9)\n" \
"v_mfma_f32_16x16x32_f16 v[48:51], v[136:139], v[168:171], v[48:51]\n" \
"ds_read_b128 v[160:163], v240 offset:49152\n" \
"ds_read_b64_tr_b16 v[152:153], v241 offset:33280\n" \
"v_mfma_f32_16x16x32_f16 v[60:63], v[140:143], v[168:171], v[60:63]\n" \
"v_pk_add_f16 v168, v168, v172\n" \
"v_pk_add_f16 v169, v169, v173\n" \
"s_waitcnt lgkmcnt(4)\n" \
"v_mfma_f32_16x16x32_bf16 v[204:207], v[200:203], v[196:199], v[204:207]\n" \
"v_mfma_f32_16x16x32_f16 v[52:55], v[144:147], v[172:175], v[52:55]\n" \
"v_pk_add_f16 v170, v170, v174\n" \
"v_pk_add_f16 v171, v171, v175\n" \
"v_mfma_f32_16x16x32_f16 v[64:67], v[148:151], v[172:175], v[64:67]\n" \
"ds_read_b128 v[164:167], v240 offset:49664\n" \
"ds_read_b64_tr_b16 v[154:155], v241 offset:41472\n" \
"v_mfma_f32_16x16x32_f16 v[56:59], v[120:123], v[168:171], v[56:59]\n" \
"buffer_load_dwordx4 v232, s[36:39], s80 offen offset:2048 sc1 lds\n" \
"ds_read_b64_tr_b16 v[156:157], v242 offset:33280\n" \
"v_mfma_f32_16x16x32_f16 v[68:71], v[124:127], v[168:171], v[68:71]\n" \
"ds_read_b128 v[196:199], v249 offset:5120\n" \
"ds_read_b64_tr_b16 v[200:201], v247 offset:4096\n" \
"v_mfma_f32_16x16x32_f16 v[72:75], v[136:139], v[176:179], v[72:75]\n" \
"ds_read_b128 v[168:171], v240 offset:40960\n" \
"ds_read_b64_tr_b16 v[202:203], v247 offset:4608\n" \
"v_mfma_f32_16x16x32_f16 v[84:87], v[140:143], v[176:179], v[84:87]\n" \
"v_pk_add_f16 v176, v176, v180\n" \
"v_pk_add_f16 v177, v177, v181\n" \
"v_mfma_f32_16x16x32_f16 v[76:79], v[144:147], v[180:183], v[76:79]\n" \
"v_pk_add_f16 v178, v178, v182\n" \
"v_pk_add_f16 v179, v179, v183\n" \
"v_mfma_f32_16x16x32_f16 v[88:91], v[148:151], v[180:183], v[88:91]\n" \
"ds_read_b128 v[172:175], v240 offset:41472\n" \
"ds_read_b64_tr_b16 v[158:159], v242 offset:41472\n" \
"v_mfma_f32_16x16x32_f16 v[80:83], v[120:123], v[176:179], v[80:83]\n" \
"buffer_load_dwordx4 v232, s[36:39], s80 offen offset:3072 sc1 lds\n" \
"v_mfma_f32_16x16x32_f16 v[92:95], v[124:127], v[176:179], v[92:95]\n" \
"s_waitcnt lgkmcnt(9)\n" \
"v_mfma_f32_16x16x32_f16 v[96:99], v[136:139], v[160:163], v[96:99]\n" \
"v_pk_add_f16 v136, v128, v152\n" \
"v_pk_add_f16 v137, v129, v153\n" \
"ds_read_b128 v[176:179], v240 offset:43008\n" \
"s_waitcnt lgkmcnt(3)\n" \
"v_mfma_f32_16x16x32_bf16 v[204:207], v[200:203], v[196:199], v[204:207]\n" \
"v_mfma_f32_16x16x32_f16 v[108:111], v[140:143], v[160:163], v[108:111]\n" \
"v_pk_add_f16 v160, v160, v164\n" \
"v_pk_add_f16 v161, v161, v165\n" \
"ds_read_b128 v[180:183], v240 offset:43520\n" \
"v_mfma_f32_16x16x32_f16 v[100:103], v[144:147], v[164:167], v[100:103]\n" \
"v_pk_add_f16 v162, v162, v166\n" \
"v_pk_add_f16 v163, v163, v167\n" \
"v_mfma_f32_16x16x32_f16 v[112:115], v[148:151], v[164:167], v[112:115]\n" \
"v_pk_add_f16 v138, v130, v154\n" \
"v_pk_add_f16 v139, v131, v155\n" \
"v_mfma_f32_16x16x32_f16 v[104:107], v[120:123], v[160:163], v[104:107]\n" \
"v_pk_add_f16 v140, v132, v156\n" \
"v_pk_add_f16 v141, v133, v157\n" \
"v_mfma_f32_16x16x32_f16 v[116:119], v[124:127], v[160:163], v[116:119]\n" \
"s_waitcnt lgkmcnt(2)\n" \
"v_pk_add_f16 v142, v134, v158\n" \
"v_pk_add_f16 v143, v135, v159\n" \
"s_waitcnt vmcnt(5) lgkmcnt(0)\n" \
"s_barrier\n" \
"v_mfma_f32_16x16x32_f16 v[0:3], v[128:131], v[168:171], v[0:3]\n" \
"ds_read_b128 v[160:163], v240 offset:4096\n" \
"v_mfma_f32_16x16x32_f16 v[12:15], v[132:135], v[168:171], v[12:15]\n" \
"v_pk_add_f16 v168, v168, v172\n" \
"v_pk_add_f16 v169, v169, v173\n" \
"v_mfma_f32_16x16x32_f16 v[4:7], v[152:155], v[172:175], v[4:7]\n" \
"v_pk_add_f16 v170, v170, v174\n" \
"v_pk_add_f16 v171, v171, v175\n" \
"v_cmp_eq_u32 vcc, 0, v253\n" \
"s_cbranch_vccnz .Ls011_%=\n" \
".Lr011_%=:\n" \
"v_mfma_f32_16x16x32_f16 v[16:19], v[156:159], v[172:175], v[16:19]\n" \
"ds_read_b128 v[164:167], v240 offset:4608\n" \
"s_add_i32 m0, s44, 0x8000\n" \
"s_add_i32 s80, s46, 0x6400000\n" \
"v_mfma_f32_16x16x32_f16 v[8:11], v[136:139], v[168:171], v[8:11]\n" \
"buffer_load_dwordx4 v232, s[36:39], s80 offen sc1 lds\n" \
"v_mfma_f32_16x16x32_f16 v[20:23], v[140:143], v[168:171], v[20:23]\n" \
"ds_read_b128 v[196:199], v249 offset:6144\n" \
"ds_read_b64_tr_b16 v[200:201], v247 offset:8192\n" \
"v_mfma_f32_16x16x32_f16 v[24:27], v[128:131], v[176:179], v[24:27]\n" \
"ds_read_b128 v[168:171], v240 offset:6144\n" \
"ds_read_b64_tr_b16 v[120:121], v241 offset:0\n" \
"ds_read_b64_tr_b16 v[122:123], v241 offset:8192\n" \
"v_mfma_f32_16x16x32_f16 v[36:39], v[132:135], v[176:179], v[36:39]\n" \
"v_pk_add_f16 v176, v176, v180\n" \
"v_pk_add_f16 v177, v177, v181\n" \
"v_mfma_f32_16x16x32_f16 v[28:31], v[152:155], v[180:183], v[28:31]\n" \
"v_pk_add_f16 v178, v178, v182\n" \
"v_pk_add_f16 v179, v179, v183\n" \
"v_mfma_f32_16x16x32_f16 v[40:43], v[156:159], v[180:183], v[40:43]\n" \
"ds_read_b128 v[172:175], v240 offset:6656\n" \
"ds_read_b64_tr_b16 v[202:203], v247 offset:8704\n" \
"v_mfma_f32_16x16x32_f16 v[32:35], v[136:139], v[176:179], v[32:35]\n" \
"buffer_load_dwordx4 v232, s[36:39], s80 offen offset:1024 sc1 lds\n" \
"ds_read_b64_tr_b16 v[124:125], v242 offset:0\n" \
"v_mfma_f32_16x16x32_f16 v[44:47], v[140:143], v[176:179], v[44:47]\n" \
"ds_read_b64_tr_b16 v[126:127], v242 offset:8192\n" \
"s_waitcnt lgkmcnt(9)\n" \
"v_mfma_f32_16x16x32_f16 v[48:51], v[128:131], v[160:163], v[48:51]\n" \
"ds_read_b128 v[176:179], v240 offset:8192\n" \
"ds_read_b64_tr_b16 v[144:145], v241 offset:512\n" \
"v_mfma_f32_16x16x32_f16 v[60:63], v[132:135], v[160:163], v[60:63]\n" \
"v_pk_add_f16 v160, v160, v164\n" \
"v_pk_add_f16 v161, v161, v165\n" \
"s_waitcnt lgkmcnt(4)\n" \
"v_mfma_f32_16x16x32_bf16 v[204:207], v[200:203], v[196:199], v[204:207]\n" \
"v_mfma_f32_16x16x32_f16 v[52:55], v[152:155], v[164:167], v[52:55]\n" \
"v_pk_add_f16 v162, v162, v166\n" \
"v_pk_add_f16 v163, v163, v167\n" \
"v_mfma_f32_16x16x32_f16 v[64:67], v[156:159], v[164:167], v[64:67]\n" \
"ds_read_b128 v[180:183], v240 offset:8704\n" \
"ds_read_b64_tr_b16 v[146:147], v241 offset:8704\n" \
"v_mfma_f32_16x16x32_f16 v[56:59], v[136:139], v[160:163], v[56:59]\n" \
"buffer_load_dwordx4 v232, s[36:39], s80 offen offset:2048 sc1 lds\n" \
"ds_read_b64_tr_b16 v[148:149], v242 offset:512\n" \
"v_mfma_f32_16x16x32_f16 v[68:71], v[140:143], v[160:163], v[68:71]\n" \
"ds_read_b128 v[196:199], v249 offset:7168\n" \
"ds_read_b64_tr_b16 v[200:201], v247 offset:12288\n" \
"v_mfma_f32_16x16x32_f16 v[72:75], v[128:131], v[168:171], v[72:75]\n" \
"ds_read_b128 v[160:163], v240 offset:0\n" \
"ds_read_b64_tr_b16 v[202:203], v247 offset:12800\n" \
"v_mfma_f32_16x16x32_f16 v[84:87], v[132:135], v[168:171], v[84:87]\n" \
"v_pk_add_f16 v168, v168, v172\n" \
"v_pk_add_f16 v169, v169, v173\n" \
"v_mfma_f32_16x16x32_f16 v[76:79], v[152:155], v[172:175], v[76:79]\n" \
"v_pk_add_f16 v170, v170, v174\n" \
"v_pk_add_f16 v171, v171, v175\n" \
"v_mfma_f32_16x16x32_f16 v[88:91], v[156:159], v[172:175], v[88:91]\n" \
"ds_read_b128 v[164:167], v240 offset:512\n" \
"ds_read_b64_tr_b16 v[150:151], v242 offset:8704\n" \
"v_mfma_f32_16x16x32_f16 v[80:83], v[136:139], v[168:171], v[80:83]\n" \
"buffer_load_dwordx4 v232, s[36:39], s80 offen offset:3072 sc1 lds\n" \
"v_mfma_f32_16x16x32_f16 v[92:95], v[140:143], v[168:171], v[92:95]\n" \
"s_waitcnt lgkmcnt(9)\n" \
"v_mfma_f32_16x16x32_f16 v[96:99], v[128:131], v[176:179], v[96:99]\n" \
"v_pk_add_f16 v128, v120, v144\n" \
"v_pk_add_f16 v129, v121, v145\n" \
"ds_read_b128 v[168:171], v240 offset:2048\n" \
"s_waitcnt lgkmcnt(3)\n" \
"v_mfma_f32_16x16x32_bf16 v[204:207], v[200:203], v[196:199], v[204:207]\n" \
"v_mfma_f32_16x16x32_f16 v[108:111], v[132:135], v[176:179], v[108:111]\n" \
"v_pk_add_f16 v176, v176, v180\n" \
"v_pk_add_f16 v177, v177, v181\n" \
"ds_read_b128 v[172:175], v240 offset:2560\n" \
"v_mfma_f32_16x16x32_f16 v[100:103], v[152:155], v[180:183], v[100:103]\n" \
"v_pk_add_f16 v178, v178, v182\n" \
"v_pk_add_f16 v179, v179, v183\n" \
"v_mfma_f32_16x16x32_f16 v[112:115], v[156:159], v[180:183], v[112:115]\n" \
"v_pk_add_f16 v130, v122, v146\n" \
"v_pk_add_f16 v131, v123, v147\n" \
"v_mfma_f32_16x16x32_f16 v[104:107], v[136:139], v[176:179], v[104:107]\n" \
"v_pk_add_f16 v132, v124, v148\n" \
"v_pk_add_f16 v133, v125, v149\n" \
"v_mfma_f32_16x16x32_f16 v[116:119], v[140:143], v[176:179], v[116:119]\n" \
"s_waitcnt lgkmcnt(2)\n" \
"v_pk_add_f16 v134, v126, v150\n" \
"v_pk_add_f16 v135, v127, v151\n" \
"s_waitcnt vmcnt(4) lgkmcnt(0)\n" \
"s_barrier\n" \
"v_mfma_f32_16x16x32_f16 v[0:3], v[120:123], v[160:163], v[0:3]\n" \
"ds_read_b128 v[176:179], v240 offset:24576\n" \
"s_mov_b32 s81, 0x8018000\n" \
"v_mfma_f32_16x16x32_f16 v[12:15], v[124:127], v[160:163], v[12:15]\n" \
"v_pk_add_f16 v160, v160, v164\n" \
"v_pk_add_f16 v161, v161, v165\n" \
"buffer_load_dword v253, v252, s[36:39], s81 offen sc1\n" \
"v_mfma_f32_16x16x32_f16 v[4:7], v[144:147], v[164:167], v[4:7]\n" \
"v_pk_add_f16 v162, v162, v166\n" \
"v_pk_add_f16 v163, v163, v167\n" \
"v_mfma_f32_16x16x32_f16 v[16:19], v[148:151], v[164:167], v[16:19]\n" \
"ds_read_b128 v[180:183], v240 offset:25088\n" \
"s_add_i32 m0, s44, 0x0\n" \
"s_add_i32 s80, s46, 0x6800000\n" \
"v_mfma_f32_16x16x32_f16 v[8:11], v[128:131], v[160:163], v[8:11]\n" \
"buffer_load_dwordx4 v232, s[36:39], s80 offen sc1 lds\n" \
"v_mfma_f32_16x16x32_f16 v[20:23], v[132:135], v[160:163], v[20:23]\n" \
"ds_write_b128 v248, v[204:207]\n" \
"ds_read_b64_tr_b16 v[136:137], v241 offset:16384\n" \
"v_mfma_f32_16x16x32_f16 v[24:27], v[120:123], v[168:171], v[24:27]\n" \
"ds_read_b128 v[160:163], v240 offset:26624\n" \
"ds_read_b64_tr_b16 v[138:139], v241 offset:24576\n" \
"v_mfma_f32_16x16x32_f16 v[36:39], v[124:127], v[168:171], v[36:39]\n" \
"v_pk_add_f16 v168, v168, v172\n" \
"v_pk_add_f16 v169, v169, v173\n" \
"v_mfma_f32_16x16x32_f16 v[28:31], v[144:147], v[172:175], v[28:31]\n" \
"v_pk_add_f16 v170, v170, v174\n" \
"v_pk_add_f16 v171, v171, v175\n" \
"v_mfma_f32_16x16x32_f16 v[40:43], v[148:151], v[172:175], v[40:43]\n" \
"ds_read_b128 v[164:167], v240 offset:27136\n" \
"ds_read_b64_tr_b16 v[140:141], v242 offset:16384\n" \
"v_mfma_f32_16x16x32_f16 v[32:35], v[128:131], v[168:171], v[32:35]\n" \
"buffer_load_dwordx4 v232, s[36:39], s80 offen offset:1024 sc1 lds\n" \
"v_mfma_f32_16x16x32_f16 v[44:47], v[132:135], v[168:171], v[44:47]\n" \
"ds_read_b64_tr_b16 v[142:143], v242 offset:24576\n" \
"s_waitcnt lgkmcnt(7)\n" \
"v_mfma_f32_16x16x32_f16 v[48:51], v[120:123], v[176:179], v[48:51]\n" \
"ds_read_b128 v[168:171], v240 offset:28672\n" \
"ds_read_b64_tr_b16 v[152:153], v241 offset:16896\n" \
"v_mfma_f32_16x16x32_f16 v[60:63], v[124:127], v[176:179], v[60:63]\n" \
"v_pk_add_f16 v176, v176, v180\n" \
"v_pk_add_f16 v177, v177, v181\n" \
"v_mfma_f32_16x16x32_f16 v[52:55], v[144:147], v[180:183], v[52:55]\n" \
"v_pk_add_f16 v178, v178, v182\n" \
"v_pk_add_f16 v179, v179, v183\n" \
"v_mfma_f32_16x16x32_f16 v[64:67], v[148:151], v[180:183], v[64:67]\n" \
"ds_read_b128 v[172:175], v240 offset:29184\n" \
"v_mfma_f32_16x16x32_f16 v[56:59], v[128:131], v[176:179], v[56:59]\n" \
"buffer_load_dwordx4 v232, s[36:39], s80 offen offset:2048 sc1 lds\n" \
"v_mfma_f32_16x16x32_f16 v[68:71], v[132:135], v[176:179], v[68:71]\n" \
"ds_read_b64_tr_b16 v[154:155], v241 offset:25088\n" \
"ds_read_b64_tr_b16 v[156:157], v242 offset:16896\n" \
"s_waitcnt lgkmcnt(8)\n" \
"v_mfma_f32_16x16x32_f16 v[72:75], v[120:123], v[160:163], v[72:75]\n" \
"ds_read_b128 v[176:179], v240 offset:20480\n" \
"ds_read_b64_tr_b16 v[158:159], v242 offset:25088\n" \
"v_mfma_f32_16x16x32_f16 v[84:87], v[124:127], v[160:163], v[84:87]\n" \
"s_waitcnt lgkmcnt(8)\n" \
"v_pk_add_f16 v160, v160, v164\n" \
"v_pk_add_f16 v161, v161, v165\n" \
"v_mfma_f32_16x16x32_f16 v[76:79], v[144:147], v[164:167], v[76:79]\n" \
"v_pk_add_f16 v162, v162, v166\n" \
"v_pk_add_f16 v163, v163, v167\n" \
"v_mfma_f32_16x16x32_f16 v[88:91], v[148:151], v[164:167], v[88:91]\n" \
"ds_read_b128 v[180:183], v240 offset:20992\n" \
"v_mfma_f32_16x16x32_f16 v[80:83], v[128:131], v[160:163], v[80:83]\n" \
"buffer_load_dwordx4 v232, s[36:39], s80 offen offset:3072 sc1 lds\n" \
"v_mfma_f32_16x16x32_f16 v[92:95], v[132:135], v[160:163], v[92:95]\n" \
"s_waitcnt lgkmcnt(6)\n" \
"v_mfma_f32_16x16x32_f16 v[96:99], v[120:123], v[168:171], v[96:99]\n" \
"v_pk_add_f16 v120, v136, v152\n" \
"v_pk_add_f16 v121, v137, v153\n" \
"ds_read_b128 v[160:163], v240 offset:22528\n" \
"v_mfma_f32_16x16x32_f16 v[108:111], v[124:127], v[168:171], v[108:111]\n" \
"s_waitcnt lgkmcnt(5)\n" \
"v_pk_add_f16 v168, v168, v172\n" \
"v_pk_add_f16 v169, v169, v173\n" \
"ds_read_b128 v[164:167], v240 offset:23040\n" \
"v_mfma_f32_16x16x32_f16 v[100:103], v[144:147], v[172:175], v[100:103]\n" \
"v_pk_add_f16 v170, v170, v174\n" \
"v_pk_add_f16 v171, v171, v175\n" \
"v_mfma_f32_16x16x32_f16 v[112:115], v[148:151], v[172:175], v[112:115]\n" \
"v_pk_add_f16 v122, v138, v154\n" \
"v_pk_add_f16 v123, v139, v155\n" \
"v_mfma_f32_16x16x32_f16 v[104:107], v[128:131], v[168:171], v[104:107]\n" \
"s_waitcnt lgkmcnt(3)\n" \
"v_pk_add_f16 v124, v140, v156\n" \
"v_pk_add_f16 v125, v141, v157\n" \
"v_mfma_f32_16x16x32_f16 v[116:119], v[132:135], v[168:171], v[116:119]\n" \
"v_pk_add_f16 v126, v142, v158\n" \
"v_pk_add_f16 v127, v143, v159\n" \
".Lcf_t:\n" \
"s_waitcnt vmcnt(4) lgkmcnt(0)\n" \
"s_barrier\n" \
"v_mfma_f32_16x16x32_f16 v[0:3], v[136:139], v[176:179], v[0:3]\n" \
"ds_read_b128 v[168:171], v240 offset:45056\n" \
"v_mfma_f32_16x16x32_f16 v[12:15], v[140:143], v[176:179], v[12:15]\n" \
"v_pk_add_f16 v176, v176, v180\n" \
"v_pk_add_f16 v177, v177, v181\n" \
"v_mfma_f32_16x16x32_f16 v[4:7], v[152:155], v[180:183], v[4:7]\n" \
"v_pk_add_f16 v178, v178, v182\n" \
"v_pk_add_f16 v179, v179, v183\n" \
"v_cmp_eq_u32 vcc, 0, v253\n" \
"s_cbranch_vccnz .Ls012_%=\n" \
".Lr012_%=:\n" \
"s_mov_b32 s81, 0x801a000\n" \
"v_mfma_f32_16x16x32_f16 v[16:19], v[156:159], v[180:183], v[16:19]\n" \
"ds_read_b128 v[172:175], v240 offset:45568\n" \
"buffer_load_dword v253, v252, s[36:39], s81 offen sc1\n" \
"s_add_i32 m0, s44, 0x4000\n" \
"s_add_i32 s80, s46, 0x6c00000\n" \
"v_mfma_f32_16x16x32_f16 v[8:11], v[120:123], v[176:179], v[8:11]\n" \
"buffer_load_dwordx4 v232, s[36:39], s80 offen sc1 lds\n" \
"v_mfma_f32_16x16x32_f16 v[20:23], v[124:127], v[176:179], v[20:23]\n" \
"ds_read_b64_tr_b16 v[128:129], v241 offset:32768\n" \
"ds_read_b64_tr_b16 v[130:131], v241 offset:40960\n" \
"v_mfma_f32_16x16x32_f16 v[24:27], v[136:139], v[160:163], v[24:27]\n" \
"ds_read_b128 v[176:179], v240 offset:47104\n" \
"v_mfma_f32_16x16x32_f16 v[36:39], v[140:143], v[160:163], v[36:39]\n" \
"v_pk_add_f16 v160, v160, v164\n" \
"v_pk_add_f16 v161, v161, v165\n" \
"v_mfma_f32_16x16x32_f16 v[28:31], v[152:155], v[164:167], v[28:31]\n" \
"v_pk_add_f16 v162, v162, v166\n" \
"v_pk_add_f16 v163, v163, v167\n" \
"v_mfma_f32_16x16x32_f16 v[40:43], v[156:159], v[164:167], v[40:43]\n" \
"ds_read_b128 v[180:183], v240 offset:47616\n" \
"ds_read_b64_tr_b16 v[132:133], v242 offset:32768\n" \
"v_mfma_f32_16x16x32_f16 v[32:35], v[120:123], v[160:163], v[32:35]\n" \
"buffer_load_dwordx4 v232, s[36:39], s80 offen offset:1024 sc1 lds\n" \
"v_mfma_f32_16x16x32_f16 v[44:47], v[124:127], v[160:163], v[44:47]\n" \
"ds_read_b64_tr_b16 v[134:135], v242 offset:40960\n" \
"s_waitcnt lgkmcnt(6)\n" \
"v_mfma_f32_16x16x32_f16 v[48:51], v[136:139], v[168:171], v[48:51]\n" \
"ds_read_b128 v[160:163], v240 offset:49152\n" \
"ds_read_b64_tr_b16 v[144:145], v241 offset:33280\n" \
"v_mfma_f32_16x16x32_f16 v[60:63], v[140:143], v[168:171], v[60:63]\n" \
"v_pk_add_f16 v168, v168, v172\n" \
"v_pk_add_f16 v169, v169, v173\n" \
"v_mfma_f32_16x16x32_f16 v[52:55], v[152:155], v[172:175], v[52:55]\n" \
"v_pk_add_f16 v170, v170, v174\n" \
"v_pk_add_f16 v171, v171, v175\n" \
"v_mfma_f32_16x16x32_f16 v[64:67], v[156:159], v[172:175], v[64:67]\n" \
"ds_read_b128 v[164:167], v240 offset:49664\n" \
"v_mfma_f32_16x16x32_f16 v[56:59], v[120:123], v[168:171], v[56:59]\n" \
"buffer_load_dwordx4 v232, s[36:39], s80 offen offset:2048 sc1 lds\n" \
"v_mfma_f32_16x16x32_f16 v[68:71], v[124:127], v[168:171], v[68:71]\n" \
"ds_read_b64_tr_b16 v[146:147], v241 offset:41472\n" \
"ds_read_b64_tr_b16 v[148:149], v242 offset:33280\n" \
"s_waitcnt lgkmcnt(8)\n" \
"v_mfma_f32_16x16x32_f16 v[72:75], v[136:139], v[176:179], v[72:75]\n" \
"ds_read_b128 v[168:171], v240 offset:40960\n" \
"ds_read_b64_tr_b16 v[150:151], v242 offset:41472\n" \
"v_mfma_f32_16x16x32_f16 v[84:87], v[140:143], v[176:179], v[84:87]\n" \
"s_waitcnt lgkmcnt(8)\n" \
"v_pk_add_f16 v176, v176, v180\n" \
"v_pk_add_f16 v177, v177, v181\n" \
"v_mfma_f32_16x16x32_f16 v[76:79], v[152:155], v[180:183], v[76:79]\n" \
"v_pk_add_f16 v178, v178, v182\n" \
"v_pk_add_f16 v179, v179, v183\n" \
"v_mfma_f32_16x16x32_f16 v[88:91], v[156:159], v[180:183], v[88:91]\n" \
"ds_read_b128 v[172:175], v240 offset:41472\n" \
"v_mfma_f32_16x16x32_f16 v[80:83], v[120:123], v[176:179], v[80:83]\n" \
"buffer_load_dwordx4 v232, s[36:39], s80 offen offset:3072 sc1 lds\n" \
"v_mfma_f32_16x16x32_f16 v[92:95], v[124:127], v[176:179], v[92:95]\n" \
"s_waitcnt lgkmcnt(6)\n" \
"v_mfma_f32_16x16x32_f16 v[96:99], v[136:139], v[160:163], v[96:99]\n" \
"v_pk_add_f16 v136, v128, v144\n" \
"v_pk_add_f16 v137, v129, v145\n" \
"ds_read_b128 v[176:179], v240 offset:43008\n" \
"v_mfma_f32_16x16x32_f16 v[108:111], v[140:143], v[160:163], v[108:111]\n" \
"s_waitcnt lgkmcnt(5)\n" \
"v_pk_add_f16 v160, v160, v164\n" \
"v_pk_add_f16 v161, v161, v165\n" \
"ds_read_b128 v[180:183], v240 offset:43520\n" \
"v_mfma_f32_16x16x32_f16 v[100:103], v[152:155], v[164:167], v[100:103]\n" \
"v_pk_add_f16 v162, v162, v166\n" \
"v_pk_add_f16 v163, v163, v167\n" \
"v_mfma_f32_16x16x32_f16 v[112:115], v[156:159], v[164:167], v[112:115]\n" \
"v_pk_add_f16 v138, v130, v146\n" \
"v_pk_add_f16 v139, v131, v147\n" \
"v_mfma_f32_16x16x32_f16 v[104:107], v[120:123], v[160:163], v[104:107]\n" \
"s_waitcnt lgkmcnt(3)\n" \
"v_pk_add_f16 v140, v132, v148\n" \
"v_pk_add_f16 v141, v133, v149\n" \
"v_mfma_f32_16x16x32_f16 v[116:119], v[124:127], v[160:163], v[116:119]\n" \
"v_pk_add_f16 v142, v134, v150\n" \
"v_pk_add_f16 v143, v135, v151\n" \
"s_waitcnt vmcnt(4) lgkmcnt(0)\n" \
"s_barrier\n" \
"v_mfma_f32_16x16x32_f16 v[0:3], v[128:131], v[168:171], v[0:3]\n" \
"ds_read_b128 v[160:163], v240 offset:4096\n" \
"v_mfma_f32_16x16x32_f16 v[12:15], v[132:135], v[168:171], v[12:15]\n" \
"v_pk_add_f16 v168, v168, v172\n" \
"v_pk_add_f16 v169, v169, v173\n" \
"s_add_i32 m0, s44, 0x8000\n" \
"s_add_i32 s80, s46, 0x7000000\n" \
"v_mfma_f32_16x16x32_f16 v[4:7], v[144:147], v[172:175], v[4:7]\n" \
"v_pk_add_f16 v170, v170, v174\n" \
"v_pk_add_f16 v171, v171, v175\n" \
"buffer_load_dwordx4 v232, s[36:39], s80 offen sc1 lds\n" \
"v_mfma_f32_16x16x32_f16 v[16:19], v[148:151], v[172:175], v[16:19]\n" \
"ds_read_b128 v[164:167], v240 offset:4608\n" \
"v_mfma_f32_16x16x32_f16 v[8:11], v[136:139], v[168:171], v[8:11]\n" \
"buffer_load_dwordx4 v232, s[36:39], s80 offen offset:1024 sc1 lds\n" \
"v_mfma_f32_16x16x32_f16 v[20:23], v[140:143], v[168:171], v[20:23]\n" \
"ds_read_b64_tr_b16 v[120:121], v241 offset:0\n" \
"ds_read_b64_tr_b16 v[122:123], v241 offset:8192\n" \
"v_mfma_f32_16x16x32_f16 v[24:27], v[128:131], v[176:179], v[24:27]\n" \
"ds_read_b128 v[168:171], v240 offset:6144\n" \
"buffer_load_dwordx4 v232, s[36:39], s80 offen offset:2048 sc1 lds\n" \
"v_mfma_f32_16x16x32_f16 v[36:39], v[132:135], v[176:179], v[36:39]\n" \
"v_pk_add_f16 v176, v176, v180\n" \
"v_pk_add_f16 v177, v177, v181\n" \
"ds_read_b64_tr_b16 v[124:125], v242 offset:0\n" \
"v_mfma_f32_16x16x32_f16 v[28:31], v[144:147], v[180:183], v[28:31]\n" \
"v_pk_add_f16 v178, v178, v182\n" \
"v_pk_add_f16 v179, v179, v183\n" \
"v_mfma_f32_16x16x32_f16 v[40:43], v[148:151], v[180:183], v[40:43]\n" \
"ds_read_b128 v[172:175], v240 offset:6656\n" \
"buffer_load_dwordx4 v232, s[36:39], s80 offen offset:3072 sc1 lds\n" \
"v_mfma_f32_16x16x32_f16 v[32:35], v[136:139], v[176:179], v[32:35]\n" \
"v_cmp_eq_u32 vcc, 0, v253\n" \
"s_cbranch_vccnz .Ls013_%=\n" \
".Lr013_%=:\n" \
"v_mfma_f32_16x16x32_f16 v[44:47], v[140:143], v[176:179], v[44:47]\n" \
"s_add_i32 m0, s44, 0x1b000\n" \
"s_add_i32 s80, s46, 0x7400000\n" \
"ds_read_b64_tr_b16 v[126:127], v242 offset:8192\n" \
"s_waitcnt lgkmcnt(6)\n" \
"v_mfma_f32_16x16x32_f16 v[48:51], v[128:131], v[160:163], v[48:51]\n" \
"ds_read_b128 v[176:179], v240 offset:8192\n" \
"buffer_load_dwordx4 v232, s[36:39], s80 offen sc1 lds\n" \
"v_mfma_f32_16x16x32_f16 v[60:63], v[132:135], v[160:163], v[60:63]\n" \
"v_pk_add_f16 v160, v160, v164\n" \
"v_pk_add_f16 v161, v161, v165\n" \
"v_mfma_f32_16x16x32_f16 v[52:55], v[144:147], v[164:167], v[52:55]\n" \
"v_pk_add_f16 v162, v162, v166\n" \
"v_pk_add_f16 v163, v163, v167\n" \
"buffer_load_dwordx4 v232, s[36:39], s80 offen offset:1024 sc1 lds\n" \
"v_mfma_f32_16x16x32_f16 v[64:67], v[148:151], v[164:167], v[64:67]\n" \
"ds_read_b128 v[180:183], v240 offset:8704\n" \
"ds_read_b64_tr_b16 v[152:153], v241 offset:512\n" \
"v_mfma_f32_16x16x32_f16 v[56:59], v[136:139], v[160:163], v[56:59]\n" \
"buffer_load_dwordx4 v232, s[36:39], s80 offen offset:2048 sc1 lds\n" \
"v_mfma_f32_16x16x32_f16 v[68:71], v[140:143], v[160:163], v[68:71]\n" \
"ds_read_b64_tr_b16 v[154:155], v241 offset:8704\n" \
"ds_read_b64_tr_b16 v[156:157], v242 offset:512\n" \
"s_waitcnt lgkmcnt(6)\n" \
"v_mfma_f32_16x16x32_f16 v[72:75], v[128:131], v[168:171], v[72:75]\n" \
"ds_read_b128 v[160:163], v240 offset:0\n" \
"buffer_load_dwordx4 v232, s[36:39], s80 offen offset:3072 sc1 lds\n" \
"v_mfma_f32_16x16x32_f16 v[84:87], v[132:135], v[168:171], v[84:87]\n" \
"v_pk_add_f16 v168, v168, v172\n" \
"v_pk_add_f16 v169, v169, v173\n" \
"ds_read_b64_tr_b16 v[158:159], v242 offset:8704\n" \
"v_mfma_f32_16x16x32_f16 v[76:79], v[144:147], v[172:175], v[76:79]\n" \
"v_pk_add_f16 v170, v170, v174\n" \
"v_pk_add_f16 v171, v171, v175\n" \
"s_add_i32 m0, s44, 0x1f000\n" \
"s_add_i32 s80, s46, 0x7800000\n" \
"v_mfma_f32_16x16x32_f16 v[88:91], v[148:151], v[172:175], v[88:91]\n" \
"ds_read_b128 v[164:167], v240 offset:512\n" \
"buffer_load_dwordx4 v232, s[36:39], s80 offen sc1 lds\n" \
"v_mfma_f32_16x16x32_f16 v[80:83], v[136:139], v[168:171], v[80:83]\n" \
"v_mfma_f32_16x16x32_f16 v[92:95], v[140:143], v[168:171], v[92:95]\n" \
"buffer_load_dwordx4 v232, s[36:39], s80 offen offset:1024 sc1 lds\n" \
"s_waitcnt lgkmcnt(5)\n" \
"v_mfma_f32_16x16x32_f16 v[96:99], v[128:131], v[176:179], v[96:99]\n" \
"v_pk_add_f16 v128, v120, v152\n" \
"v_pk_add_f16 v129, v121, v153\n" \
"ds_read_b128 v[168:171], v240 offset:2048\n" \
"v_mfma_f32_16x16x32_f16 v[108:111], v[132:135], v[176:179], v[108:111]\n" \
"v_pk_add_f16 v176, v176, v180\n" \
"v_pk_add_f16 v177, v177, v181\n" \
"ds_read_b128 v[172:175], v240 offset:2560\n" \
"buffer_load_dwordx4 v232, s[36:39], s80 offen offset:2048 sc1 lds\n" \
"v_mfma_f32_16x16x32_f16 v[100:103], v[144:147], v[180:183], v[100:103]\n" \
"v_pk_add_f16 v178, v178, v182\n" \
"v_pk_add_f16 v179, v179, v183\n" \
"v_mfma_f32_16x16x32_f16 v[112:115], v[148:151], v[180:183], v[112:115]\n" \
"s_waitcnt lgkmcnt(3)\n" \
"v_pk_add_f16 v130, v122, v154\n" \
"v_pk_add_f16 v131, v123, v155\n" \
"buffer_load_dwordx4 v232, s[36:39], s80 offen offset:3072 sc1 lds\n" \
"v_mfma_f32_16x16x32_f16 v[104:107], v[136:139], v[176:179], v[104:107]\n" \
"v_pk_add_f16 v132, v124, v156\n" \
"v_pk_add_f16 v133, v125, v157\n" \
"v_mfma_f32_16x16x32_f16 v[116:119], v[140:143], v[176:179], v[116:119]\n" \
"v_pk_add_f16 v134, v126, v158\n" \
"v_pk_add_f16 v135, v127, v159\n" \
"s_waitcnt vmcnt(12) lgkmcnt(0)\n" \
"s_barrier\n" \
"v_mfma_f32_16x16x32_f16 v[0:3], v[120:123], v[160:163], v[0:3]\n" \
"ds_read_b128 v[176:179], v240 offset:24576\n" \
"v_mfma_f32_16x16x32_f16 v[12:15], v[124:127], v[160:163], v[12:15]\n" \
"v_pk_add_f16 v160, v160, v164\n" \
"v_pk_add_f16 v161, v161, v165\n" \
"s_lshr_b32 m0, s44, 1\n" \
"v_mfma_f32_16x16x32_f16 v[4:7], v[152:155], v[164:167], v[4:7]\n" \
"v_pk_add_f16 v162, v162, v166\n" \
"v_pk_add_f16 v163, v163, v167\n" \
"v_add_u32 v252, m0, v244\n" \
"v_mfma_f32_16x16x32_f16 v[16:19], v[156:159], v[164:167], v[16:19]\n" \
"ds_read_b128 v[180:183], v240 offset:25088\n" \
"v_mfma_f32_16x16x32_f16 v[8:11], v[128:131], v[160:163], v[8:11]\n" \
"global_load_lds_dwordx4 v252, s[76:77]\n" \
"v_mfma_f32_16x16x32_f16 v[20:23], v[132:135], v[160:163], v[20:23]\n" \
"ds_read_b64_tr_b16 v[136:137], v241 offset:16384\n" \
"ds_read_b64_tr_b16 v[138:139], v241 offset:24576\n" \
"v_mfma_f32_16x16x32_f16 v[24:27], v[120:123], v[168:171], v[24:27]\n" \
"ds_read_b128 v[160:163], v240 offset:26624\n" \
"v_mfma_f32_16x16x32_f16 v[36:39], v[124:127], v[168:171], v[36:39]\n" \
"v_pk_add_f16 v168, v168, v172\n" \
"v_pk_add_f16 v169, v169, v173\n" \
"v_mfma_f32_16x16x32_f16 v[28:31], v[152:155], v[172:175], v[28:31]\n" \
"v_pk_add_f16 v170, v170, v174\n" \
"v_pk_add_f16 v171, v171, v175\n" \
"v_mfma_f32_16x16x32_f16 v[40:43], v[156:159], v[172:175], v[40:43]\n" \
"ds_read_b128 v[164:167], v240 offset:27136\n" \
"ds_read_b64_tr_b16 v[140:141], v242 offset:16384\n" \
"v_mfma_f32_16x16x32_f16 v[32:35], v[128:131], v[168:171], v[32:35]\n" \
"global_load_lds_dwordx4 v252, s[76:77] offset:1024\n" \
"v_mfma_f32_16x16x32_f16 v[44:47], v[132:135], v[168:171], v[44:47]\n" \
"ds_read_b64_tr_b16 v[142:143], v242 offset:24576\n" \
"s_waitcnt lgkmcnt(7)\n" \
"v_mfma_f32_16x16x32_f16 v[48:51], v[120:123], v[176:179], v[48:51]\n" \
"ds_read_b128 v[168:171], v240 offset:28672\n" \
"ds_read_b64_tr_b16 v[144:145], v241 offset:16896\n" \
"v_mfma_f32_16x16x32_f16 v[60:63], v[124:127], v[176:179], v[60:63]\n" \
"s_waitcnt lgkmcnt(8)\n" \
"v_pk_add_f16 v176, v176, v180\n" \
"v_pk_add_f16 v177, v177, v181\n" \
"v_mfma_f32_16x16x32_f16 v[52:55], v[152:155], v[180:183], v[52:55]\n" \
"v_pk_add_f16 v178, v178, v182\n" \
"v_pk_add_f16 v179, v179, v183\n" \
"v_mov_b32 v248, v194\n" \
"v_mfma_f32_16x16x32_f16 v[64:67], v[156:159], v[180:183], v[64:67]\n" \
"v_mov_b32 v249, v195\n" \
"ds_read_b128 v[172:175], v240 offset:29184\n" \
"v_mfma_f32_16x16x32_f16 v[56:59], v[128:131], v[176:179], v[56:59]\n" \
"ds_read_b64_tr_b16 v[146:147], v241 offset:25088\n" \
"v_mfma_f32_16x16x32_f16 v[68:71], v[132:135], v[176:179], v[68:71]\n" \
"ds_read_b64_tr_b16 v[148:149], v242 offset:16896\n" \
"s_waitcnt lgkmcnt(6)\n" \
"v_mfma_f32_16x16x32_f16 v[72:75], v[120:123], v[160:163], v[72:75]\n" \
"ds_read_b128 v[176:179], v240 offset:20480\n" \
"ds_read_b64_tr_b16 v[150:151], v242 offset:25088\n" \
"v_mfma_f32_16x16x32_f16 v[84:87], v[124:127], v[160:163], v[84:87]\n" \
"v_pk_add_f16 v160, v160, v164\n" \
"v_pk_add_f16 v161, v161, v165\n" \
"v_mfma_f32_16x16x32_f16 v[76:79], v[152:155], v[164:167], v[76:79]\n" \
"v_pk_add_f16 v162, v162, v166\n" \
"v_pk_add_f16 v163, v163, v167\n" \
"v_add_u32 v243, 0x1b000, v241\n" \
"v_mfma_f32_16x16x32_f16 v[88:91], v[156:159], v[164:167], v[88:91]\n" \
"v_xor_b32 v244, 32, v243\n" \
"ds_read_b128 v[180:183], v240 offset:20992\n" \
"v_mfma_f32_16x16x32_f16 v[80:83], v[128:131], v[160:163], v[80:83]\n" \
"v_mfma_f32_16x16x32_f16 v[92:95], v[132:135], v[160:163], v[92:95]\n" \
"v_add_u32 v246, 0x17000, v240\n" \
"s_waitcnt lgkmcnt(6)\n" \
"v_mfma_f32_16x16x32_f16 v[96:99], v[120:123], v[168:171], v[96:99]\n" \
"v_pk_add_f16 v120, v136, v144\n" \
"v_pk_add_f16 v121, v137, v145\n" \
"ds_read_b128 v[160:163], v240 offset:22528\n" \
"v_mfma_f32_16x16x32_f16 v[108:111], v[124:127], v[168:171], v[108:111]\n" \
"s_waitcnt lgkmcnt(4)\n" \
"v_pk_add_f16 v168, v168, v172\n" \
"v_pk_add_f16 v169, v169, v173\n" \
"ds_read_b128 v[164:167], v240 offset:23040\n" \
"v_mfma_f32_16x16x32_f16 v[100:103], v[152:155], v[172:175], v[100:103]\n" \
"v_pk_add_f16 v170, v170, v174\n" \
"v_pk_add_f16 v171, v171, v175\n" \
"v_mfma_f32_16x16x32_f16 v[112:115], v[156:159], v[172:175], v[112:115]\n" \
"v_pk_add_f16 v122, v138, v146\n" \
"v_pk_add_f16 v123, v139, v147\n" \
"v_mfma_f32_16x16x32_f16 v[104:107], v[128:131], v[168:171], v[104:107]\n" \
"v_pk_add_f16 v124, v140, v148\n" \
"v_pk_add_f16 v125, v141, v149\n" \
"v_mfma_f32_16x16x32_f16 v[116:119], v[132:135], v[168:171], v[116:119]\n" \
"s_waitcnt lgkmcnt(2)\n" \
"v_pk_add_f16 v126, v142, v150\n" \
"v_pk_add_f16 v127, v143, v151\n" \
"s_waitcnt vmcnt(2) lgkmcnt(0)\n" \
"s_barrier\n" \
"v_mfma_f32_16x16x32_f16 v[0:3], v[136:139], v[176:179], v[0:3]\n" \
"ds_read_b128 v[168:171], v240 offset:40960\n" \
"ds_read_b64_tr_b16 v[128:129], v241 offset:32768\n" \
"ds_read_b64_tr_b16 v[130:131], v241 offset:40960\n" \
"v_mfma_f32_16x16x32_f16 v[12:15], v[140:143], v[176:179], v[12:15]\n" \
"v_pk_add_f16 v176, v176, v180\n" \
"v_pk_add_f16 v177, v177, v181\n" \
"ds_read_b64_tr_b16 v[132:133], v242 offset:32768\n" \
"v_mfma_f32_16x16x32_f16 v[4:7], v[144:147], v[180:183], v[4:7]\n" \
"v_pk_add_f16 v178, v178, v182\n" \
"v_pk_add_f16 v179, v179, v183\n" \
"v_mfma_f32_16x16x32_f16 v[16:19], v[148:151], v[180:183], v[16:19]\n" \
"ds_read_b128 v[172:175], v240 offset:41472\n" \
"ds_read_b64_tr_b16 v[134:135], v242 offset:40960\n" \
"ds_read_b64_tr_b16 v[152:153], v241 offset:33280\n" \
"v_mfma_f32_16x16x32_f16 v[8:11], v[120:123], v[176:179], v[8:11]\n" \
"ds_read_b64_tr_b16 v[154:155], v241 offset:41472\n" \
"v_mfma_f32_16x16x32_f16 v[20:23], v[124:127], v[176:179], v[20:23]\n" \
"ds_read_b64_tr_b16 v[156:157], v242 offset:33280\n" \
"ds_read_b64_tr_b16 v[158:159], v242 offset:41472\n" \
"v_mfma_f32_16x16x32_f16 v[24:27], v[136:139], v[160:163], v[24:27]\n" \
"ds_read_b128 v[176:179], v246 offset:0\n" \
"ds_read_b64_tr_b16 v[184:185], v243 offset:0\n" \
"ds_read_b64_tr_b16 v[186:187], v243 offset:8192\n" \
"v_mfma_f32_16x16x32_f16 v[36:39], v[140:143], v[160:163], v[36:39]\n" \
"v_pk_add_f16 v160, v160, v164\n" \
"v_pk_add_f16 v161, v161, v165\n" \
"ds_read_b64_tr_b16 v[188:189], v244 offset:0\n" \
"v_mfma_f32_16x16x32_f16 v[28:31], v[144:147], v[164:167], v[28:31]\n" \
"v_pk_add_f16 v162, v162, v166\n" \
"v_pk_add_f16 v163, v163, v167\n" \
"v_mfma_f32_16x16x32_f16 v[40:43], v[148:151], v[164:167], v[40:43]\n" \
"ds_read_b128 v[180:183], v246 offset:512\n" \
"ds_read_b64_tr_b16 v[190:191], v244 offset:8192\n" \
"ds_read_b64_tr_b16 v[192:193], v243 offset:512\n" \
"v_mfma_f32_16x16x32_f16 v[32:35], v[120:123], v[160:163], v[32:35]\n" \
"ds_read_b64_tr_b16 v[194:195], v243 offset:8704\n" \
"v_mfma_f32_16x16x32_f16 v[44:47], v[124:127], v[160:163], v[44:47]\n" \
"s_waitcnt lgkmcnt(11)\n" \
"v_pk_add_f16 v232, v128, v152\n" \
"v_pk_add_f16 v233, v129, v153\n" \
"ds_read_b64_tr_b16 v[196:197], v244 offset:512\n" \
"ds_read_b64_tr_b16 v[198:199], v244 offset:8704\n" \
"v_mfma_f32_16x16x32_f16 v[0:3], v[128:131], v[168:171], v[0:3]\n" \
"s_waitcnt lgkmcnt(10)\n" \
"v_pk_add_f16 v234, v130, v154\n" \
"ds_read_b128 v[160:163], v246 offset:8192\n" \
"ds_read_b64_tr_b16 v[208:209], v243 offset:16384\n" \
"ds_read_b64_tr_b16 v[210:211], v243 offset:24576\n" \
"v_mfma_f32_16x16x32_f16 v[12:15], v[132:135], v[168:171], v[12:15]\n" \
"v_pk_add_f16 v168, v168, v172\n" \
"v_pk_add_f16 v169, v169, v173\n" \
"v_pk_add_f16 v235, v131, v155\n" \
"v_pk_add_f16 v236, v132, v156\n" \
"ds_read_b64_tr_b16 v[212:213], v244 offset:16384\n" \
"v_mfma_f32_16x16x32_f16 v[4:7], v[152:155], v[172:175], v[4:7]\n" \
"v_pk_add_f16 v170, v170, v174\n" \
"v_pk_add_f16 v171, v171, v175\n" \
"v_pk_add_f16 v237, v133, v157\n" \
"v_pk_add_f16 v238, v134, v158\n" \
"v_pk_add_f16 v239, v135, v159\n" \
"v_mfma_f32_16x16x32_f16 v[16:19], v[156:159], v[172:175], v[16:19]\n" \
"s_waitcnt lgkmcnt(6)\n" \
"v_pk_add_f16 v200, v184, v192\n" \
"ds_read_b128 v[164:167], v246 offset:8704\n" \
"ds_read_b64_tr_b16 v[214:215], v244 offset:24576\n" \
"ds_read_b64_tr_b16 v[216:217], v243 offset:16896\n" \
"v_mfma_f32_16x16x32_f16 v[8:11], v[232:235], v[168:171], v[8:11]\n" \
"v_pk_add_f16 v201, v185, v193\n" \
"v_pk_add_f16 v202, v186, v194\n" \
"ds_read_b64_tr_b16 v[218:219], v243 offset:25088\n" \
"v_mfma_f32_16x16x32_f16 v[20:23], v[236:239], v[168:171], v[20:23]\n" \
"ds_read_b64_tr_b16 v[220:221], v244 offset:16896\n" \
"ds_read_b64_tr_b16 v[222:223], v244 offset:25088\n" \
"v_mfma_f32_16x16x32_f16 v[0:3], v[184:187], v[176:179], v[0:3]\n" \
"v_pk_add_f16 v203, v187, v195\n" \
"s_waitcnt lgkmcnt(7)\n" \
"v_pk_add_f16 v204, v188, v196\n" \
"ds_read_b128 v[168:171], v240 offset:43008\n" \
"v_mfma_f32_16x16x32_f16 v[12:15], v[188:191], v[176:179], v[12:15]\n" \
"v_pk_add_f16 v176, v176, v180\n" \
"v_pk_add_f16 v177, v177, v181\n" \
"v_pk_add_f16 v205, v189, v197\n" \
"v_mfma_f32_16x16x32_f16 v[4:7], v[192:195], v[180:183], v[4:7]\n" \
"v_pk_add_f16 v178, v178, v182\n" \
"v_pk_add_f16 v179, v179, v183\n" \
"v_pk_add_f16 v206, v190, v198\n" \
"v_pk_add_f16 v207, v191, v199\n" \
"v_mfma_f32_16x16x32_f16 v[16:19], v[196:199], v[180:183], v[16:19]\n" \
"s_waitcnt lgkmcnt(4)\n" \
"v_pk_add_f16 v224, v208, v216\n" \
"ds_read_b128 v[172:175], v240 offset:43520\n" \
"v_mfma_f32_16x16x32_f16 v[8:11], v[200:203], v[176:179], v[8:11]\n" \
"v_pk_add_f16 v225, v209, v217\n" \
"v_mfma_f32_16x16x32_f16 v[20:23], v[204:207], v[176:179], v[20:23]\n" \
"s_waitcnt lgkmcnt(4)\n" \
"v_pk_add_f16 v226, v210, v218\n" \
"v_pk_add_f16 v227, v211, v219\n" \
"v_mfma_f32_16x16x32_f16 v[0:3], v[208:211], v[160:163], v[0:3]\n" \
"s_waitcnt lgkmcnt(2)\n" \
"v_pk_add_f16 v228, v212, v220\n" \
"v_pk_add_f16 v229, v213, v221\n" \
"ds_read_b128 v[176:179], v246 offset:2048\n" \
"v_mfma_f32_16x16x32_f16 v[12:15], v[212:215], v[160:163], v[12:15]\n" \
"v_pk_add_f16 v160, v160, v164\n" \
"v_pk_add_f16 v161, v161, v165\n" \
"v_pk_add_f16 v230, v214, v222\n" \
"v_mfma_f32_16x16x32_f16 v[4:7], v[216:219], v[164:167], v[4:7]\n" \
"v_pk_add_f16 v162, v162, v166\n" \
"v_pk_add_f16 v163, v163, v167\n" \
"v_pk_add_f16 v231, v215, v223\n" \
"v_mfma_f32_16x16x32_f16 v[16:19], v[220:223], v[164:167], v[16:19]\n" \
"ds_read_b128 v[180:183], v246 offset:2560\n" \
"s_mov_b32 vcc_lo, 0xaaaaaaaa\n" \
"v_mfma_f32_16x16x32_f16 v[8:11], v[224:227], v[160:163], v[8:11]\n" \
"s_mov_b32 vcc_hi, 0xaaaaaaaa\n" \
"v_mfma_f32_16x16x32_f16 v[20:23], v[228:231], v[160:163], v[20:23]\n" \
"v_add_u32 v241, 0xffff7000, v240\n" \
"s_waitcnt lgkmcnt(2)\n" \
"v_mfma_f32_16x16x32_f16 v[24:27], v[128:131], v[168:171], v[24:27]\n" \
"ds_read_b128 v[160:163], v246 offset:10240\n" \
"v_mfma_f32_16x16x32_f16 v[36:39], v[132:135], v[168:171], v[36:39]\n" \
"v_pk_add_f16 v168, v168, v172\n" \
"v_pk_add_f16 v169, v169, v173\n" \
"v_mfma_f32_16x16x32_f16 v[28:31], v[152:155], v[172:175], v[28:31]\n" \
"v_pk_add_f16 v170, v170, v174\n" \
"v_pk_add_f16 v171, v171, v175\n" \
"v_add_f32 v252, v4, v8\n" \
"v_add_f32 v253, v5, v9\n" \
"v_add_f32 v254, v6, v10\n" \
"v_add_f32 v255, v7, v11\n" \
"v_mfma_f32_16x16x32_f16 v[40:43], v[156:159], v[172:175], v[40:43]\n" \
"ds_read_b128 v[164:167], v246 offset:10752\n" \
"v_fmac_f32 v4, s56, v8\n" \
"v_fmac_f32 v5, s56, v9\n" \
"v_fmac_f32 v6, s56, v10\n" \
"v_fmac_f32 v7, s56, v11\n" \
"v_mfma_f32_16x16x32_f16 v[32:35], v[232:235], v[168:171], v[32:35]\n" \
"v_fmac_f32 v0, s57, v252\n" \
"v_fmac_f32 v1, s57, v253\n" \
"v_fmac_f32 v2, s57, v254\n" \
"v_fmac_f32 v3, s57, v255\n" \
"v_add_f32 v252, v16, v20\n" \
"v_mfma_f32_16x16x32_f16 v[44:47], v[236:239], v[168:171], v[44:47]\n" \
"v_add_f32 v253, v17, v21\n" \
"v_add_f32 v254, v18, v22\n" \
"v_add_f32 v255, v19, v23\n" \
"v_fmac_f32 v16, s56, v20\n" \
"s_waitcnt lgkmcnt(1)\n" \
"v_mfma_f32_16x16x32_f16 v[24:27], v[184:187], v[176:179], v[24:27]\n" \
"ds_read_b128 v[168:171], v240 offset:45056\n" \
"v_fmac_f32 v17, s56, v21\n" \
"v_fmac_f32 v18, s56, v22\n" \
"v_fmac_f32 v19, s56, v23\n" \
"v_fmac_f32 v12, s57, v252\n" \
"v_fmac_f32 v13, s57, v253\n" \
"v_mfma_f32_16x16x32_f16 v[36:39], v[188:191], v[176:179], v[36:39]\n" \
"v_pk_add_f16 v176, v176, v180\n" \
"v_pk_add_f16 v177, v177, v181\n" \
"v_fmac_f32 v14, s57, v254\n" \
"v_fmac_f32 v15, s57, v255\n" \
"v_cvt_pk_bf16_f32 v8, v0, v1\n" \
"v_cvt_pk_bf16_f32 v20, v4, v5\n" \
"v_mfma_f32_16x16x32_f16 v[28:31], v[192:195], v[180:183], v[28:31]\n" \
"v_pk_add_f16 v178, v178, v182\n" \
"v_pk_add_f16 v179, v179, v183\n" \
"v_cvt_pk_bf16_f32 v9, v2, v3\n" \
"v_cvt_pk_bf16_f32 v21, v6, v7\n" \
"v_cvt_pk_bf16_f32 v10, v12, v13\n" \
"v_cvt_pk_bf16_f32 v22, v16, v17\n" \
"v_cvt_pk_bf16_f32 v11, v14, v15\n" \
"v_mfma_f32_16x16x32_f16 v[40:43], v[196:199], v[180:183], v[40:43]\n" \
"ds_read_b128 v[172:175], v240 offset:45568\n" \
"v_cvt_pk_bf16_f32 v23, v18, v19\n" \
"s_not_b64 vcc, vcc\n" \
"v_cndmask_b32_dpp v0, v20, v8, vcc quad_perm:[1,0,3,2] row_mask:0xf bank_mask:0xf\n" \
"v_cndmask_b32_dpp v1, v21, v9, vcc quad_perm:[1,0,3,2] row_mask:0xf bank_mask:0xf\n" \
"v_mfma_f32_16x16x32_f16 v[32:35], v[200:203], v[176:179], v[32:35]\n" \
"s_waitcnt vmcnt(0)\n" \
"s_barrier\n" \
"v_cndmask_b32_dpp v2, v22, v10, vcc quad_perm:[1,0,3,2] row_mask:0xf bank_mask:0xf\n" \
"v_cndmask_b32_dpp v3, v23, v11, vcc quad_perm:[1,0,3,2] row_mask:0xf bank_mask:0xf\n" \
"buffer_store_dwordx4 v[0:3], v245, s[40:43], s69 offen sc1\n" \
"s_not_b64 vcc, vcc\n" \
"s_add_i32 s85, s69, 0x10000\n" \
"v_mfma_f32_16x16x32_f16 v[44:47], v[204:207], v[176:179], v[44:47]\n" \
"v_cndmask_b32_dpp v12, v8, v20, vcc quad_perm:[1,0,3,2] row_mask:0xf bank_mask:0xf\n" \
"v_cndmask_b32_dpp v13, v9, v21, vcc quad_perm:[1,0,3,2] row_mask:0xf bank_mask:0xf\n" \
"v_cndmask_b32_dpp v14, v10, v22, vcc quad_perm:[1,0,3,2] row_mask:0xf bank_mask:0xf\n" \
"v_cndmask_b32_dpp v15, v11, v23, vcc quad_perm:[1,0,3,2] row_mask:0xf bank_mask:0xf\n" \
"buffer_store_dwordx4 v[12:15], v245, s[40:43], s85 offen sc1\n" \
"v_mfma_f32_16x16x32_f16 v[24:27], v[208:211], v[160:163], v[24:27]\n" \
"ds_read_b128 v[176:179], v240 offset:4096\n" \
"v_mfma_f32_16x16x32_f16 v[36:39], v[212:215], v[160:163], v[36:39]\n" \
"s_waitcnt lgkmcnt(2)\n" \
"v_pk_add_f16 v160, v160, v164\n" \
"v_pk_add_f16 v161, v161, v165\n" \
"v_mfma_f32_16x16x32_f16 v[28:31], v[216:219], v[164:167], v[28:31]\n" \
"v_pk_add_f16 v162, v162, v166\n" \
"v_pk_add_f16 v163, v163, v167\n" \
"v_mfma_f32_16x16x32_f16 v[40:43], v[220:223], v[164:167], v[40:43]\n" \
"ds_read_b128 v[180:183], v240 offset:4608\n" \
"v_mfma_f32_16x16x32_f16 v[32:35], v[224:227], v[160:163], v[32:35]\n" \
"v_mfma_f32_16x16x32_f16 v[44:47], v[228:231], v[160:163], v[44:47]\n" \
"v_mfma_f32_16x16x32_f16 v[48:51], v[136:139], v[168:171], v[48:51]\n" \
"ds_read_b128 v[160:163], v240 offset:24576\n" \
"v_mfma_f32_16x16x32_f16 v[60:63], v[140:143], v[168:171], v[60:63]\n" \
"s_waitcnt lgkmcnt(3)\n" \
"v_pk_add_f16 v168, v168, v172\n" \
"v_pk_add_f16 v169, v169, v173\n" \
"v_mfma_f32_16x16x32_f16 v[52:55], v[144:147], v[172:175], v[52:55]\n" \
"v_pk_add_f16 v170, v170, v174\n" \
"v_pk_add_f16 v171, v171, v175\n" \
"v_add_f32 v252, v28, v32\n" \
"v_add_f32 v253, v29, v33\n" \
"v_mfma_f32_16x16x32_f16 v[64:67], v[148:151], v[172:175], v[64:67]\n" \
"ds_read_b128 v[164:167], v240 offset:25088\n" \
"v_add_f32 v254, v30, v34\n" \
"v_add_f32 v255, v31, v35\n" \
"v_fmac_f32 v28, s56, v32\n" \
"v_mfma_f32_16x16x32_f16 v[56:59], v[120:123], v[168:171], v[56:59]\n" \
"v_fmac_f32 v29, s56, v33\n" \
"v_fmac_f32 v30, s56, v34\n" \
"v_fmac_f32 v31, s56, v35\n" \
"v_mfma_f32_16x16x32_f16 v[68:71], v[124:127], v[168:171], v[68:71]\n" \
"v_fmac_f32 v24, s57, v252\n" \
"v_fmac_f32 v25, s57, v253\n" \
"v_fmac_f32 v26, s57, v254\n" \
"s_waitcnt lgkmcnt(2)\n" \
"v_mfma_f32_16x16x32_f16 v[48:51], v[128:131], v[176:179], v[48:51]\n" \
"ds_read_b128 v[168:171], v241 offset:4096\n" \
"v_fmac_f32 v27, s57, v255\n" \
"v_add_f32 v252, v40, v44\n" \
"v_add_f32 v253, v41, v45\n" \
"v_mfma_f32_16x16x32_f16 v[60:63], v[132:135], v[176:179], v[60:63]\n" \
"v_pk_add_f16 v176, v176, v180\n" \
"v_pk_add_f16 v177, v177, v181\n" \
"v_add_f32 v254, v42, v46\n" \
"v_add_f32 v255, v43, v47\n" \
"v_mfma_f32_16x16x32_f16 v[52:55], v[152:155], v[180:183], v[52:55]\n" \
"v_pk_add_f16 v178, v178, v182\n" \
"v_pk_add_f16 v179, v179, v183\n" \
"v_fmac_f32 v40, s56, v44\n" \
"v_fmac_f32 v41, s56, v45\n" \
"v_fmac_f32 v42, s56, v46\n" \
"v_mfma_f32_16x16x32_f16 v[64:67], v[156:159], v[180:183], v[64:67]\n" \
"ds_read_b128 v[172:175], v241 offset:4608\n" \
"v_fmac_f32 v43, s56, v47\n" \
"v_fmac_f32 v36, s57, v252\n" \
"v_fmac_f32 v37, s57, v253\n" \
"v_mfma_f32_16x16x32_f16 v[56:59], v[232:235], v[176:179], v[56:59]\n" \
"v_fmac_f32 v38, s57, v254\n" \
"v_fmac_f32 v39, s57, v255\n" \
"v_cvt_pk_bf16_f32 v32, v24, v25\n" \
"v_mfma_f32_16x16x32_f16 v[68:71], v[236:239], v[176:179], v[68:71]\n" \
"v_cvt_pk_bf16_f32 v44, v28, v29\n" \
"v_cvt_pk_bf16_f32 v33, v26, v27\n" \
"v_cvt_pk_bf16_f32 v45, v30, v31\n" \
"s_waitcnt lgkmcnt(1)\n" \
"v_mfma_f32_16x16x32_f16 v[48:51], v[184:187], v[160:163], v[48:51]\n" \
"ds_read_b128 v[176:179], v240 offset:47104\n" \
"v_cvt_pk_bf16_f32 v34, v36, v37\n" \
"v_cvt_pk_bf16_f32 v46, v40, v41\n" \
"v_mfma_f32_16x16x32_f16 v[60:63], v[188:191], v[160:163], v[60:63]\n" \
"v_pk_add_f16 v160, v160, v164\n" \
"v_pk_add_f16 v161, v161, v165\n" \
"v_cvt_pk_bf16_f32 v35, v38, v39\n" \
"v_cvt_pk_bf16_f32 v47, v42, v43\n" \
"s_not_b64 vcc, vcc\n" \
"v_mfma_f32_16x16x32_f16 v[52:55], v[192:195], v[164:167], v[52:55]\n" \
"v_pk_add_f16 v162, v162, v166\n" \
"v_pk_add_f16 v163, v163, v167\n" \
"v_cndmask_b32_dpp v24, v44, v32, vcc quad_perm:[1,0,3,2] row_mask:0xf bank_mask:0xf\n" \
"v_cndmask_b32_dpp v25, v45, v33, vcc quad_perm:[1,0,3,2] row_mask:0xf bank_mask:0xf\n" \
"v_cndmask_b32_dpp v26, v46, v34, vcc quad_perm:[1,0,3,2] row_mask:0xf bank_mask:0xf\n" \
"v_mfma_f32_16x16x32_f16 v[64:67], v[196:199], v[164:167], v[64:67]\n" \
"ds_read_b128 v[180:183], v240 offset:47616\n" \
"v_cndmask_b32_dpp v27, v47, v35, vcc quad_perm:[1,0,3,2] row_mask:0xf bank_mask:0xf\n" \
"buffer_store_dwordx4 v[24:27], v245, s[40:43], s70 offen sc1\n" \
"s_not_b64 vcc, vcc\n" \
"v_mfma_f32_16x16x32_f16 v[56:59], v[200:203], v[160:163], v[56:59]\n" \
"s_add_i32 s85, s70, 0x10000\n" \
"v_cndmask_b32_dpp v36, v32, v44, vcc quad_perm:[1,0,3,2] row_mask:0xf bank_mask:0xf\n" \
"v_cndmask_b32_dpp v37, v33, v45, vcc quad_perm:[1,0,3,2] row_mask:0xf bank_mask:0xf\n" \
"v_mfma_f32_16x16x32_f16 v[68:71], v[204:207], v[160:163], v[68:71]\n" \
"v_cndmask_b32_dpp v38, v34, v46, vcc quad_perm:[1,0,3,2] row_mask:0xf bank_mask:0xf\n" \
"v_cndmask_b32_dpp v39, v35, v47, vcc quad_perm:[1,0,3,2] row_mask:0xf bank_mask:0xf\n" \
"buffer_store_dwordx4 v[36:39], v245, s[40:43], s85 offen sc1\n" \
"v_mfma_f32_16x16x32_f16 v[48:51], v[208:211], v[168:171], v[48:51]\n" \
"ds_read_b128 v[160:163], v240 offset:6144\n" \
"v_mfma_f32_16x16x32_f16 v[60:63], v[212:215], v[168:171], v[60:63]\n" \
"s_waitcnt lgkmcnt(2)\n" \
"v_pk_add_f16 v168, v168, v172\n" \
"v_pk_add_f16 v169, v169, v173\n" \
"v_mfma_f32_16x16x32_f16 v[52:55], v[216:219], v[172:175], v[52:55]\n" \
"v_pk_add_f16 v170, v170, v174\n" \
"v_pk_add_f16 v171, v171, v175\n" \
"v_mfma_f32_16x16x32_f16 v[64:67], v[220:223], v[172:175], v[64:67]\n" \
"ds_read_b128 v[164:167], v240 offset:6656\n" \
"v_mfma_f32_16x16x32_f16 v[56:59], v[224:227], v[168:171], v[56:59]\n" \
"v_mfma_f32_16x16x32_f16 v[68:71], v[228:231], v[168:171], v[68:71]\n" \
"v_mfma_f32_16x16x32_f16 v[72:75], v[136:139], v[176:179], v[72:75]\n" \
"ds_read_b128 v[168:171], v240 offset:26624\n" \
"v_mfma_f32_16x16x32_f16 v[84:87], v[140:143], v[176:179], v[84:87]\n" \
"s_waitcnt lgkmcnt(3)\n" \
"v_pk_add_f16 v176, v176, v180\n" \
"v_pk_add_f16 v177, v177, v181\n" \
"v_mfma_f32_16x16x32_f16 v[76:79], v[144:147], v[180:183], v[76:79]\n" \
"v_pk_add_f16 v178, v178, v182\n" \
"v_pk_add_f16 v179, v179, v183\n" \
"v_add_f32 v252, v52, v56\n" \
"v_add_f32 v253, v53, v57\n" \
"v_mfma_f32_16x16x32_f16 v[88:91], v[148:151], v[180:183], v[88:91]\n" \
"ds_read_b128 v[172:175], v240 offset:27136\n" \
"v_add_f32 v254, v54, v58\n" \
"v_add_f32 v255, v55, v59\n" \
"v_mfma_f32_16x16x32_f16 v[80:83], v[120:123], v[176:179], v[80:83]\n" \
"v_fmac_f32 v52, s56, v56\n" \
"v_fmac_f32 v53, s56, v57\n" \
"v_mfma_f32_16x16x32_f16 v[92:95], v[124:127], v[176:179], v[92:95]\n" \
"v_fmac_f32 v54, s56, v58\n" \
"v_fmac_f32 v55, s56, v59\n" \
"s_waitcnt lgkmcnt(2)\n" \
"v_mfma_f32_16x16x32_f16 v[72:75], v[128:131], v[160:163], v[72:75]\n" \
"ds_read_b128 v[176:179], v241 offset:6144\n" \
"v_fmac_f32 v48, s57, v252\n" \
"v_fmac_f32 v49, s57, v253\n" \
"v_mfma_f32_16x16x32_f16 v[84:87], v[132:135], v[160:163], v[84:87]\n" \
"v_pk_add_f16 v160, v160, v164\n" \
"v_pk_add_f16 v161, v161, v165\n" \
"v_fmac_f32 v50, s57, v254\n" \
"v_fmac_f32 v51, s57, v255\n" \
"v_mfma_f32_16x16x32_f16 v[76:79], v[152:155], v[164:167], v[76:79]\n" \
"v_pk_add_f16 v162, v162, v166\n" \
"v_pk_add_f16 v163, v163, v167\n" \
"v_add_f32 v252, v64, v68\n" \
"v_add_f32 v253, v65, v69\n" \
"v_mfma_f32_16x16x32_f16 v[88:91], v[156:159], v[164:167], v[88:91]\n" \
"ds_read_b128 v[180:183], v241 offset:6656\n" \
"v_add_f32 v254, v66, v70\n" \
"v_add_f32 v255, v67, v71\n" \
"v_mfma_f32_16x16x32_f16 v[80:83], v[232:235], v[160:163], v[80:83]\n" \
"v_fmac_f32 v64, s56, v68\n" \
"v_fmac_f32 v65, s56, v69\n" \
"v_mfma_f32_16x16x32_f16 v[92:95], v[236:239], v[160:163], v[92:95]\n" \
"v_fmac_f32 v66, s56, v70\n" \
"v_fmac_f32 v67, s56, v71\n" \
"s_waitcnt lgkmcnt(2)\n" \
"v_mfma_f32_16x16x32_f16 v[72:75], v[184:187], v[168:171], v[72:75]\n" \
"ds_read_b128 v[160:163], v240 offset:49152\n" \
"v_fmac_f32 v60, s57, v252\n" \
"v_fmac_f32 v61, s57, v253\n" \
"v_mfma_f32_16x16x32_f16 v[84:87], v[188:191], v[168:171], v[84:87]\n" \
"v_pk_add_f16 v168, v168, v172\n" \
"v_pk_add_f16 v169, v169, v173\n" \
"v_fmac_f32 v62, s57, v254\n" \
"v_fmac_f32 v63, s57, v255\n" \
"v_mfma_f32_16x16x32_f16 v[76:79], v[192:195], v[172:175], v[76:79]\n" \
"v_pk_add_f16 v170, v170, v174\n" \
"v_pk_add_f16 v171, v171, v175\n" \
"v_cvt_pk_bf16_f32 v56, v48, v49\n" \
"v_cvt_pk_bf16_f32 v68, v52, v53\n" \
"v_mfma_f32_16x16x32_f16 v[88:91], v[196:199], v[172:175], v[88:91]\n" \
"ds_read_b128 v[164:167], v240 offset:49664\n" \
"v_cvt_pk_bf16_f32 v57, v50, v51\n" \
"v_cvt_pk_bf16_f32 v69, v54, v55\n" \
"v_mfma_f32_16x16x32_f16 v[80:83], v[200:203], v[168:171], v[80:83]\n" \
"v_cvt_pk_bf16_f32 v58, v60, v61\n" \
"v_cvt_pk_bf16_f32 v70, v64, v65\n" \
"v_mfma_f32_16x16x32_f16 v[92:95], v[204:207], v[168:171], v[92:95]\n" \
"v_cvt_pk_bf16_f32 v59, v62, v63\n" \
"v_cvt_pk_bf16_f32 v71, v66, v67\n" \
"s_waitcnt lgkmcnt(2)\n" \
"v_mfma_f32_16x16x32_f16 v[72:75], v[208:211], v[176:179], v[72:75]\n" \
"ds_read_b128 v[168:171], v240 offset:8192\n" \
"s_not_b64 vcc, vcc\n" \
"v_cndmask_b32_dpp v48, v68, v56, vcc quad_perm:[1,0,3,2] row_mask:0xf bank_mask:0xf\n" \
"v_mfma_f32_16x16x32_f16 v[84:87], v[212:215], v[176:179], v[84:87]\n" \
"v_pk_add_f16 v176, v176, v180\n" \
"v_pk_add_f16 v177, v177, v181\n" \
"v_cndmask_b32_dpp v49, v69, v57, vcc quad_perm:[1,0,3,2] row_mask:0xf bank_mask:0xf\n" \
"v_cndmask_b32_dpp v50, v70, v58, vcc quad_perm:[1,0,3,2] row_mask:0xf bank_mask:0xf\n" \
"v_mfma_f32_16x16x32_f16 v[76:79], v[216:219], v[180:183], v[76:79]\n" \
"v_pk_add_f16 v178, v178, v182\n" \
"v_pk_add_f16 v179, v179, v183\n" \
"v_cndmask_b32_dpp v51, v71, v59, vcc quad_perm:[1,0,3,2] row_mask:0xf bank_mask:0xf\n" \
"buffer_store_dwordx4 v[48:51], v245, s[40:43], s71 offen sc1\n" \
"v_mfma_f32_16x16x32_f16 v[88:91], v[220:223], v[180:183], v[88:91]\n" \
"ds_read_b128 v[172:175], v240 offset:8704\n" \
"s_not_b64 vcc, vcc\n" \
"s_add_i32 s85, s71, 0x10000\n" \
"v_mfma_f32_16x16x32_f16 v[80:83], v[224:227], v[176:179], v[80:83]\n" \
"v_cndmask_b32_dpp v60, v56, v68, vcc quad_perm:[1,0,3,2] row_mask:0xf bank_mask:0xf\n" \
"v_cndmask_b32_dpp v61, v57, v69, vcc quad_perm:[1,0,3,2] row_mask:0xf bank_mask:0xf\n" \
"v_mfma_f32_16x16x32_f16 v[92:95], v[228:231], v[176:179], v[92:95]\n" \
"v_cndmask_b32_dpp v62, v58, v70, vcc quad_perm:[1,0,3,2] row_mask:0xf bank_mask:0xf\n" \
"v_cndmask_b32_dpp v63, v59, v71, vcc quad_perm:[1,0,3,2] row_mask:0xf bank_mask:0xf\n" \
"buffer_store_dwordx4 v[60:63], v245, s[40:43], s85 offen sc1\n" \
"s_waitcnt lgkmcnt(1)\n" \
"v_mfma_f32_16x16x32_f16 v[96:99], v[136:139], v[160:163], v[96:99]\n" \
"ds_read_b128 v[176:179], v240 offset:28672\n" \
"v_mfma_f32_16x16x32_f16 v[108:111], v[140:143], v[160:163], v[108:111]\n" \
"v_pk_add_f16 v160, v160, v164\n" \
"v_pk_add_f16 v161, v161, v165\n" \
"v_mfma_f32_16x16x32_f16 v[100:103], v[144:147], v[164:167], v[100:103]\n" \
"v_pk_add_f16 v162, v162, v166\n" \
"v_pk_add_f16 v163, v163, v167\n" \
"v_add_f32 v252, v76, v80\n" \
"v_add_f32 v253, v77, v81\n" \
"v_mfma_f32_16x16x32_f16 v[112:115], v[148:151], v[164:167], v[112:115]\n" \
"ds_read_b128 v[180:183], v240 offset:29184\n" \
"v_add_f32 v254, v78, v82\n" \
"v_add_f32 v255, v79, v83\n" \
"v_mfma_f32_16x16x32_f16 v[104:107], v[120:123], v[160:163], v[104:107]\n" \
"v_fmac_f32 v76, s56, v80\n" \
"v_fmac_f32 v77, s56, v81\n" \
"v_mfma_f32_16x16x32_f16 v[116:119], v[124:127], v[160:163], v[116:119]\n" \
"v_fmac_f32 v78, s56, v82\n" \
"v_fmac_f32 v79, s56, v83\n" \
"v_mfma_f32_16x16x32_f16 v[96:99], v[128:131], v[168:171], v[96:99]\n" \
"ds_read_b128 v[160:163], v241 offset:8192\n" \
"v_fmac_f32 v72, s57, v252\n" \
"v_fmac_f32 v73, s57, v253\n" \
"v_mfma_f32_16x16x32_f16 v[108:111], v[132:135], v[168:171], v[108:111]\n" \
"s_waitcnt lgkmcnt(2)\n" \
"v_pk_add_f16 v168, v168, v172\n" \
"v_pk_add_f16 v169, v169, v173\n" \
"v_fmac_f32 v74, s57, v254\n" \
"v_fmac_f32 v75, s57, v255\n" \
"v_mfma_f32_16x16x32_f16 v[100:103], v[152:155], v[172:175], v[100:103]\n" \
"v_pk_add_f16 v170, v170, v174\n" \
"v_pk_add_f16 v171, v171, v175\n" \
"v_add_f32 v252, v88, v92\n" \
"v_add_f32 v253, v89, v93\n" \
"v_mfma_f32_16x16x32_f16 v[112:115], v[156:159], v[172:175], v[112:115]\n" \
"ds_read_b128 v[164:167], v241 offset:8704\n" \
"v_add_f32 v254, v90, v94\n" \
"v_add_f32 v255, v91, v95\n" \
"v_mfma_f32_16x16x32_f16 v[104:107], v[232:235], v[168:171], v[104:107]\n" \
"v_fmac_f32 v88, s56, v92\n" \
"v_fmac_f32 v89, s56, v93\n" \
"v_mfma_f32_16x16x32_f16 v[116:119], v[236:239], v[168:171], v[116:119]\n" \
"v_fmac_f32 v90, s56, v94\n" \
"v_fmac_f32 v91, s56, v95\n" \
"v_mfma_f32_16x16x32_f16 v[96:99], v[184:187], v[176:179], v[96:99]\n" \
"v_fmac_f32 v84, s57, v252\n" \
"v_fmac_f32 v85, s57, v253\n" \
"v_fmac_f32 v86, s57, v254\n" \
"v_mfma_f32_16x16x32_f16 v[108:111], v[188:191], v[176:179], v[108:111]\n" \
"s_waitcnt lgkmcnt(1)\n" \
"v_pk_add_f16 v176, v176, v180\n" \
"v_pk_add_f16 v177, v177, v181\n" \
"v_fmac_f32 v87, s57, v255\n" \
"v_mfma_f32_16x16x32_f16 v[100:103], v[192:195], v[180:183], v[100:103]\n" \
"v_pk_add_f16 v178, v178, v182\n" \
"v_pk_add_f16 v179, v179, v183\n" \
"v_cvt_pk_bf16_f32 v80, v72, v73\n" \
"v_cvt_pk_bf16_f32 v92, v76, v77\n" \
"v_mfma_f32_16x16x32_f16 v[112:115], v[196:199], v[180:183], v[112:115]\n" \
"v_cvt_pk_bf16_f32 v81, v74, v75\n" \
"v_cvt_pk_bf16_f32 v93, v78, v79\n" \
"v_mfma_f32_16x16x32_f16 v[104:107], v[200:203], v[176:179], v[104:107]\n" \
"v_cvt_pk_bf16_f32 v82, v84, v85\n" \
"v_cvt_pk_bf16_f32 v94, v88, v89\n" \
"v_mfma_f32_16x16x32_f16 v[116:119], v[204:207], v[176:179], v[116:119]\n" \
"v_cvt_pk_bf16_f32 v83, v86, v87\n" \
"v_cvt_pk_bf16_f32 v95, v90, v91\n" \
"v_mfma_f32_16x16x32_f16 v[96:99], v[208:211], v[160:163], v[96:99]\n" \
"s_not_b64 vcc, vcc\n" \
"v_cndmask_b32_dpp v72, v92, v80, vcc quad_perm:[1,0,3,2] row_mask:0xf bank_mask:0xf\n" \
"v_cndmask_b32_dpp v73, v93, v81, vcc quad_perm:[1,0,3,2] row_mask:0xf bank_mask:0xf\n" \
"v_mfma_f32_16x16x32_f16 v[108:111], v[212:215], v[160:163], v[108:111]\n" \
"s_waitcnt lgkmcnt(0)\n" \
"v_pk_add_f16 v160, v160, v164\n" \
"v_pk_add_f16 v161, v161, v165\n" \
"v_cndmask_b32_dpp v74, v94, v82, vcc quad_perm:[1,0,3,2] row_mask:0xf bank_mask:0xf\n" \
"v_mfma_f32_16x16x32_f16 v[100:103], v[216:219], v[164:167], v[100:103]\n" \
"v_pk_add_f16 v162, v162, v166\n" \
"v_pk_add_f16 v163, v163, v167\n" \
"v_cndmask_b32_dpp v75, v95, v83, vcc quad_perm:[1,0,3,2] row_mask:0xf bank_mask:0xf\n" \
"buffer_store_dwordx4 v[72:75], v245, s[40:43], s72 offen sc1\n" \
"v_mfma_f32_16x16x32_f16 v[112:115], v[220:223], v[164:167], v[112:115]\n" \
"s_not_b64 vcc, vcc\n" \
"s_add_i32 s85, s72, 0x10000\n" \
"v_mfma_f32_16x16x32_f16 v[104:107], v[224:227], v[160:163], v[104:107]\n" \
"v_cndmask_b32_dpp v84, v80, v92, vcc quad_perm:[1,0,3,2] row_mask:0xf bank_mask:0xf\n" \
"v_cndmask_b32_dpp v85, v81, v93, vcc quad_perm:[1,0,3,2] row_mask:0xf bank_mask:0xf\n" \
"v_mfma_f32_16x16x32_f16 v[116:119], v[228:231], v[160:163], v[116:119]\n" \
"v_cndmask_b32_dpp v86, v82, v94, vcc quad_perm:[1,0,3,2] row_mask:0xf bank_mask:0xf\n" \
"v_cndmask_b32_dpp v87, v83, v95, vcc quad_perm:[1,0,3,2] row_mask:0xf bank_mask:0xf\n" \
"buffer_store_dwordx4 v[84:87], v245, s[40:43], s85 offen sc1\n" \
"s_nop 7\n" \
"v_add_f32 v252, v100, v104\n" \
"v_add_f32 v253, v101, v105\n" \
"v_add_f32 v254, v102, v106\n" \
"v_add_f32 v255, v103, v107\n" \
"v_fmac_f32 v100, s56, v104\n" \
"v_fmac_f32 v101, s56, v105\n" \
"v_fmac_f32 v102, s56, v106\n" \
"v_fmac_f32 v103, s56, v107\n" \
"v_fmac_f32 v96, s57, v252\n" \
"v_fmac_f32 v97, s57, v253\n" \
"v_fmac_f32 v98, s57, v254\n" \
"v_fmac_f32 v99, s57, v255\n" \
"v_add_f32 v252, v112, v116\n" \
"v_add_f32 v253, v113, v117\n" \
"v_add_f32 v254, v114, v118\n" \
"v_add_f32 v255, v115, v119\n" \
"v_fmac_f32 v112, s56, v116\n" \
"v_fmac_f32 v113, s56, v117\n" \
"v_fmac_f32 v114, s56, v118\n" \
"v_fmac_f32 v115, s56, v119\n" \
"v_fmac_f32 v108, s57, v252\n" \
"v_fmac_f32 v109, s57, v253\n" \
"v_fmac_f32 v110, s57, v254\n" \
"v_fmac_f32 v111, s57, v255\n" \
"v_cvt_pk_bf16_f32 v104, v96, v97\n" \
"v_cvt_pk_bf16_f32 v116, v100, v101\n" \
"v_cvt_pk_bf16_f32 v105, v98, v99\n" \
"v_cvt_pk_bf16_f32 v117, v102, v103\n" \
"v_cvt_pk_bf16_f32 v106, v108, v109\n" \
"v_cvt_pk_bf16_f32 v118, v112, v113\n" \
"v_cvt_pk_bf16_f32 v107, v110, v111\n" \
"v_cvt_pk_bf16_f32 v119, v114, v115\n" \
"s_not_b64 vcc, vcc\n" \
"v_cndmask_b32_dpp v96, v116, v104, vcc quad_perm:[1,0,3,2] row_mask:0xf bank_mask:0xf\n" \
"v_cndmask_b32_dpp v97, v117, v105, vcc quad_perm:[1,0,3,2] row_mask:0xf bank_mask:0xf\n" \
"v_cndmask_b32_dpp v98, v118, v106, vcc quad_perm:[1,0,3,2] row_mask:0xf bank_mask:0xf\n" \
"v_cndmask_b32_dpp v99, v119, v107, vcc quad_perm:[1,0,3,2] row_mask:0xf bank_mask:0xf\n" \
"buffer_store_dwordx4 v[96:99], v245, s[40:43], s73 offen sc1\n" \
"s_not_b64 vcc, vcc\n" \
"s_add_i32 s85, s73, 0x10000\n" \
"v_cndmask_b32_dpp v108, v104, v116, vcc quad_perm:[1,0,3,2] row_mask:0xf bank_mask:0xf\n" \
"v_cndmask_b32_dpp v109, v105, v117, vcc quad_perm:[1,0,3,2] row_mask:0xf bank_mask:0xf\n" \
"v_cndmask_b32_dpp v110, v106, v118, vcc quad_perm:[1,0,3,2] row_mask:0xf bank_mask:0xf\n" \
"v_cndmask_b32_dpp v111, v107, v119, vcc quad_perm:[1,0,3,2] row_mask:0xf bank_mask:0xf\n" \
"buffer_store_dwordx4 v[108:111], v245, s[40:43], s85 offen sc1\n" \
"s_waitcnt vmcnt(0)\n" \
"s_barrier\n" \
"buffer_store_dword v240, v248, s[36:39], 0 offen sc1\n" \
"buffer_load_dword v249, v249, s[36:39], 0 offen sc1\n" \
"s_branch .Lcf_e\n" \
".Ls03_%=:\n" \
"s_mov_b32 s87, 0x200000\n" \
"s_mov_b32 s86, 0x8006000\n" \
".Lt03_%=:\n" \
"s_sleep 3\n" \
"buffer_load_dword v253, v252, s[36:39], s86 offen sc1\n" \
"s_waitcnt vmcnt(0)\n" \
"s_sub_u32 s87, s87, 1\n" \
"s_cbranch_scc1 .Lr03_%=\n" \
"v_cmp_eq_u32 vcc, 0, v253\n" \
"s_cbranch_vccnz .Lt03_%=\n" \
"s_branch .Lr03_%=\n" \
".Ls04_%=:\n" \
"s_mov_b32 s87, 0x200000\n" \
"s_mov_b32 s86, 0x8008000\n" \
".Lt04_%=:\n" \
"s_sleep 3\n" \
"buffer_load_dword v253, v252, s[36:39], s86 offen sc1\n" \
"s_waitcnt vmcnt(0)\n" \
"s_sub_u32 s87, s87, 1\n" \
"s_cbranch_scc1 .Lr04_%=\n" \
"v_cmp_eq_u32 vcc, 0, v253\n" \
"s_cbranch_vccnz .Lt04_%=\n" \
"s_branch .Lr04_%=\n" \
".Ls05_%=:\n" \
"s_mov_b32 s87, 0x200000\n" \
"s_mov_b32 s86, 0x800a000\n" \
".Lt05_%=:\n" \
"s_sleep 3\n" \
"buffer_load_dword v253, v252, s[36:39], s86 offen sc1\n" \
"s_waitcnt vmcnt(0)\n" \
"s_sub_u32 s87, s87, 1\n" \
"s_cbranch_scc1 .Lr05_%=\n" \
"v_cmp_eq_u32 vcc, 0, v253\n" \
"s_cbranch_vccnz .Lt05_%=\n" \
"s_branch .Lr05_%=\n" \
".Ls07_%=:\n" \
"s_mov_b32 s87, 0x200000\n" \
"s_mov_b32 s86, 0x800e000\n" \
".Lt07_%=:\n" \
"s_sleep 3\n" \
"buffer_load_dword v253, v252, s[36:39], s86 offen sc1\n" \
"s_waitcnt vmcnt(0)\n" \
"s_sub_u32 s87, s87, 1\n" \
"s_cbranch_scc1 .Lr07_%=\n" \
"v_cmp_eq_u32 vcc, 0, v253\n" \
"s_cbranch_vccnz .Lt07_%=\n" \
"s_branch .Lr07_%=\n" \
".Ls08_%=:\n" \
"s_mov_b32 s87, 0x200000\n" \
"s_mov_b32 s86, 0x8010000\n" \
".Lt08_%=:\n" \
"s_sleep 3\n" \
"buffer_load_dword v253, v252, s[36:39], s86 offen sc1\n" \
"s_waitcnt vmcnt(0)\n" \
"s_sub_u32 s87, s87, 1\n" \
"s_cbranch_scc1 .Lr08_%=\n" \
"v_cmp_eq_u32 vcc, 0, v253\n" \
"s_cbranch_vccnz .Lt08_%=\n" \
"s_branch .Lr08_%=\n" \
".Ls09_%=:\n" \
"s_mov_b32 s87, 0x200000\n" \
"s_mov_b32 s86, 0x8012000\n" \
".Lt09_%=:\n" \
"s_sleep 3\n" \
"buffer_load_dword v253, v252, s[36:39], s86 offen sc1\n" \
"s_waitcnt vmcnt(0)\n" \
"s_sub_u32 s87, s87, 1\n" \
"s_cbranch_scc1 .Lr09_%=\n" \
"v_cmp_eq_u32 vcc, 0, v253\n" \
"s_cbranch_vccnz .Lt09_%=\n" \
"s_branch .Lr09_%=\n" \
".Ls011_%=:\n" \
"s_mov_b32 s87, 0x200000\n" \
"s_mov_b32 s86, 0x8016000\n" \
".Lt011_%=:\n" \
"s_sleep 3\n" \
"buffer_load_dword v253, v252, s[36:39], s86 offen sc1\n" \
"s_waitcnt vmcnt(0)\n" \
"s_sub_u32 s87, s87, 1\n" \
"s_cbranch_scc1 .Lr011_%=\n" \
"v_cmp_eq_u32 vcc, 0, v253\n" \
"s_cbranch_vccnz .Lt011_%=\n" \
"s_branch .Lr011_%=\n" \
".Ls012_%=:\n" \
"s_mov_b32 s87, 0x200000\n" \
"s_mov_b32 s86, 0x8018000\n" \
".Lt012_%=:\n" \
"s_sleep 3\n" \
"buffer_load_dword v253, v252, s[36:39], s86 offen sc1\n" \
"s_waitcnt vmcnt(0)\n" \
"s_sub_u32 s87, s87, 1\n" \
"s_cbranch_scc1 .Lr012_%=\n" \
"v_cmp_eq_u32 vcc, 0, v253\n" \
"s_cbranch_vccnz .Lt012_%=\n" \
"s_branch .Lr012_%=\n" \
".Ls013_%=:\n" \
"s_mov_b32 s87, 0x200000\n" \
"s_mov_b32 s86, 0x801a000\n" \
".Lt013_%=:\n" \
"s_sleep 3\n" \
"buffer_load_dword v253, v252, s[36:39], s86 offen sc1\n" \
"s_waitcnt vmcnt(0)\n" \
"s_sub_u32 s87, s87, 1\n" \
"s_cbranch_scc1 .Lr013_%=\n" \
"v_cmp_eq_u32 vcc, 0, v253\n" \
"s_cbranch_vccnz .Lt013_%=\n" \
"s_branch .Lr013_%=\n" \
""
#define LOOP1 \
"v_lshlrev_b32 v254, 3, v244\n" \
"s_mov_b32 s91, 0x40004\n" \
"s_mov_b32 s92, 0xfff8fff8\n" \
"ds_read_b64_tr_b16 v[120:121], v241 offset:0\n" \
"ds_read_b64_tr_b16 v[122:123], v241 offset:8192\n" \
"ds_read_b64_tr_b16 v[124:125], v242 offset:0\n" \
"ds_read_b64_tr_b16 v[126:127], v242 offset:8192\n" \
"ds_read_b64_tr_b16 v[128:129], v241 offset:512\n" \
"ds_read_b64_tr_b16 v[130:131], v241 offset:8704\n" \
"ds_read_b64_tr_b16 v[132:133], v242 offset:512\n" \
"ds_read_b64_tr_b16 v[134:135], v242 offset:8704\n" \
"ds_read_b128 v[160:163], v240 offset:2048\n" \
"ds_read_b128 v[164:167], v240 offset:2560\n" \
"ds_read_b128 v[168:171], v240 offset:4096\n" \
"ds_read_b128 v[172:175], v240 offset:4608\n" \
"s_mov_b32 s94, s53\n" \
"s_mov_b32 s95, s74\n" \
"s_setprio 1\n" \
"s_waitcnt lgkmcnt(0)\n" \
"s_cmp_eq_u32 s94, 0\n" \
"s_cbranch_scc1 .Lp0_%=\n" \
".Lq0_%=:\n" \
"s_memrealtime s[86:87]\n" \
"s_waitcnt lgkmcnt(0)\n" \
"s_and_b32 s87, s94, 0xff\n" \
"s_add_i32 s75, s86, s87\n" \
".Lp0_%=:\n" \
"v_mfma_f32_16x16x32_f16 v[0:3], v[120:123], v[160:163], 0\n" \
"v_pk_add_f16 v136, v120, v128\n" \
"v_pk_add_f16 v137, v121, v129\n" \
"v_pk_add_f16 v138, v122, v130\n" \
"v_pk_add_f16 v139, v123, v131\n" \
"s_add_i32 m0, s44, 0xa000\n" \
"s_add_i32 s80, s46, 0x400000\n" \
"v_mfma_f32_16x16x32_f16 v[12:15], v[124:127], v[160:163], 0\n" \
"v_pk_add_f16 v160, v160, v164\n" \
"v_pk_add_f16 v161, v161, v165\n" \
"v_pk_add_f16 v140, v124, v132\n" \
"buffer_load_dwordx4 v232, s[36:39], s80 offen sc1 lds\n" \
"v_mfma_f32_16x16x32_f16 v[4:7], v[128:131], v[164:167], 0\n" \
"v_pk_add_f16 v162, v162, v166\n" \
"v_pk_add_f16 v163, v163, v167\n" \
"v_pk_add_f16 v141, v125, v133\n" \
"v_mfma_f32_16x16x32_f16 v[16:19], v[132:135], v[164:167], 0\n" \
"v_pk_add_f16 v142, v126, v134\n" \
"v_pk_add_f16 v143, v127, v135\n" \
"buffer_load_dwordx4 v232, s[36:39], s80 offen offset:1024 sc1 lds\n" \
"v_mfma_f32_16x16x32_f16 v[8:11], v[136:139], v[160:163], 0\n" \
"ds_write_b64 v246, v[96:97] offset:0\n" \
"ds_write_b64 v246, v[98:99] offset:4096\n" \
"v_mfma_f32_16x16x32_f16 v[20:23], v[140:143], v[160:163], 0\n" \
"s_waitcnt vmcnt(5)\n" \
"s_barrier\n" \
"v_mfma_f32_16x16x32_f16 v[24:27], v[120:123], v[168:171], 0\n" \
"ds_read_b128 v[176:179], v240 offset:26624\n" \
"ds_write_b64 v246, v[100:101] offset:8192\n" \
"buffer_store_dwordx4 v[72:75], v235, s[36:39], s93 offen sc1\n" \
"v_mfma_f32_16x16x32_f16 v[36:39], v[124:127], v[168:171], 0\n" \
"v_pk_add_f16 v168, v168, v172\n" \
"v_pk_add_f16 v169, v169, v173\n" \
"ds_read_b128 v[180:183], v240 offset:27136\n" \
"buffer_load_dwordx4 v232, s[36:39], s80 offen offset:2048 sc1 lds\n" \
"v_mfma_f32_16x16x32_f16 v[28:31], v[128:131], v[172:175], 0\n" \
"v_pk_add_f16 v170, v170, v174\n" \
"v_pk_add_f16 v171, v171, v175\n" \
"ds_read_b128 v[160:163], v240 offset:28672\n" \
"v_mfma_f32_16x16x32_f16 v[40:43], v[132:135], v[172:175], 0\n" \
"ds_read_b128 v[164:167], v240 offset:29184\n" \
"buffer_load_dwordx4 v232, s[36:39], s80 offen offset:3072 sc1 lds\n" \
"v_mfma_f32_16x16x32_f16 v[32:35], v[136:139], v[168:171], 0\n" \
"s_add_i32 m0, s45, 0xa000\n" \
"s_add_i32 s81, s47, 0x400000\n" \
"ds_write_b64 v246, v[102:103] offset:12288\n" \
"v_mfma_f32_16x16x32_f16 v[44:47], v[140:143], v[168:171], 0\n" \
"buffer_load_dwordx4 v232, s[36:39], s81 offen sc1 lds\n" \
"ds_read_b64_tr_b16 v[144:145], v241 offset:16384\n" \
"ds_read_b64_tr_b16 v[146:147], v241 offset:24576\n" \
"s_waitcnt lgkmcnt(7)\n" \
"v_mfma_f32_16x16x32_f16 v[48:51], v[120:123], v[176:179], 0\n" \
"ds_read_b128 v[168:171], v240 offset:20480\n" \
"ds_write_b64 v246, v[104:105] offset:16384\n" \
"ds_write_b64 v246, v[106:107] offset:20480\n" \
"buffer_store_dwordx4 v[196:199], v235, s[36:39], s60 offen sc1\n" \
"v_mfma_f32_16x16x32_f16 v[60:63], v[124:127], v[176:179], 0\n" \
"s_waitcnt lgkmcnt(8)\n" \
"v_pk_add_f16 v176, v176, v180\n" \
"v_pk_add_f16 v177, v177, v181\n" \
"s_add_i32 m0, s44, 0x0\n" \
"s_add_i32 s80, s46, 0x800000\n" \
"v_mfma_f32_16x16x32_f16 v[52:55], v[128:131], v[180:183], 0\n" \
"v_pk_add_f16 v178, v178, v182\n" \
"v_pk_add_f16 v179, v179, v183\n" \
"buffer_load_dwordx4 v232, s[36:39], s80 offen sc1 lds\n" \
"ds_read_b64_tr_b16 v[148:149], v242 offset:16384\n" \
"v_mfma_f32_16x16x32_f16 v[64:67], v[132:135], v[180:183], 0\n" \
"ds_read_b128 v[172:175], v240 offset:20992\n" \
"ds_write_b64 v246, v[108:109] offset:24576\n" \
"ds_read_b64_tr_b16 v[150:151], v242 offset:24576\n" \
"v_mfma_f32_16x16x32_f16 v[56:59], v[136:139], v[176:179], 0\n" \
"ds_write_b64 v246, v[110:111] offset:28672\n" \
"ds_write_b64 v246, v[112:113] offset:32768\n" \
"v_mfma_f32_16x16x32_f16 v[68:71], v[140:143], v[176:179], 0\n" \
"s_setprio 0\n" \
"buffer_load_dwordx4 v232, s[36:39], s80 offen offset:1024 sc1 lds\n" \
"ds_read_b64_tr_b16 v[152:153], v241 offset:16896\n" \
"s_waitcnt lgkmcnt(7)\n" \
"v_mfma_f32_16x16x32_f16 v[72:75], v[120:123], v[160:163], 0\n" \
"ds_read_b128 v[176:179], v240 offset:22528\n" \
"ds_write_b64 v246, v[114:115] offset:36864\n" \
"buffer_store_dwordx4 v[200:203], v235, s[36:39], s61 offen sc1\n" \
"v_mfma_f32_16x16x32_f16 v[84:87], v[124:127], v[160:163], 0\n" \
"v_pk_add_f16 v160, v160, v164\n" \
"v_pk_add_f16 v161, v161, v165\n" \
"ds_read_b64_tr_b16 v[154:155], v241 offset:25088\n" \
"ds_read_b64_tr_b16 v[156:157], v242 offset:16896\n" \
"v_mfma_f32_16x16x32_f16 v[76:79], v[128:131], v[164:167], 0\n" \
"v_pk_add_f16 v162, v162, v166\n" \
"v_pk_add_f16 v163, v163, v167\n" \
"buffer_load_dwordx4 v232, s[36:39], s80 offen offset:2048 sc1 lds\n" \
"v_mfma_f32_16x16x32_f16 v[88:91], v[132:135], v[164:167], 0\n" \
"ds_read_b128 v[180:183], v240 offset:23040\n" \
"ds_read_b64_tr_b16 v[158:159], v242 offset:25088\n" \
"v_mfma_f32_16x16x32_f16 v[80:83], v[136:139], v[160:163], 0\n" \
"ds_write_b64 v246, v[116:117] offset:40960\n" \
"v_mfma_f32_16x16x32_f16 v[92:95], v[140:143], v[160:163], 0\n" \
"buffer_load_dwordx4 v232, s[36:39], s80 offen offset:3072 sc1 lds\n" \
"v_mfma_f32_16x16x32_f16 v[96:99], v[120:123], v[168:171], 0\n" \
"s_waitcnt lgkmcnt(7)\n" \
"v_pk_add_f16 v120, v144, v152\n" \
"v_pk_add_f16 v121, v145, v153\n" \
"ds_read_b128 v[160:163], v240 offset:24576\n" \
"v_mfma_f32_16x16x32_f16 v[108:111], v[124:127], v[168:171], 0\n" \
"v_pk_add_f16 v168, v168, v172\n" \
"v_pk_add_f16 v169, v169, v173\n" \
"ds_read_b128 v[164:167], v240 offset:25088\n" \
"v_mfma_f32_16x16x32_f16 v[100:103], v[128:131], v[172:175], 0\n" \
"v_pk_add_f16 v170, v170, v174\n" \
"v_pk_add_f16 v171, v171, v175\n" \
"s_add_i32 m0, s45, 0x0\n" \
"s_add_i32 s81, s47, 0x800000\n" \
"v_mfma_f32_16x16x32_f16 v[112:115], v[132:135], v[172:175], 0\n" \
"s_waitcnt lgkmcnt(5)\n" \
"v_pk_add_f16 v122, v146, v154\n" \
"v_pk_add_f16 v123, v147, v155\n" \
"buffer_load_dwordx4 v232, s[36:39], s81 offen sc1 lds\n" \
"v_mfma_f32_16x16x32_f16 v[104:107], v[136:139], v[168:171], 0\n" \
"v_pk_add_f16 v124, v148, v156\n" \
"v_pk_add_f16 v125, v149, v157\n" \
"v_mfma_f32_16x16x32_f16 v[116:119], v[140:143], v[168:171], 0\n" \
"s_setprio 1\n" \
"s_waitcnt lgkmcnt(2)\n" \
"v_pk_add_f16 v126, v150, v158\n" \
"v_pk_add_f16 v127, v151, v159\n" \
"s_waitcnt vmcnt(7) lgkmcnt(0)\n" \
"s_cmp_eq_u32 s94, 0\n" \
"s_cbranch_scc1 .Lp1_%=\n" \
".Lq1_%=:\n" \
"s_memrealtime s[86:87]\n" \
"s_waitcnt lgkmcnt(0)\n" \
"s_sub_i32 s87, s86, s75\n" \
"s_cmp_lt_i32 s87, 0\n" \
"s_cbranch_scc1 .Lq1_%=\n" \
"s_lshr_b64 s[94:95], s[94:95], 8\n" \
"s_and_b32 s87, s94, 0xff\n" \
"s_add_i32 s75, s86, s87\n" \
".Lp1_%=:\n" \
"s_barrier\n" \
"v_mfma_f32_16x16x32_f16 v[0:3], v[144:147], v[176:179], v[0:3]\n" \
"ds_read_b128 v[168:171], v240 offset:47104\n" \
"ds_read_b64_tr_b16 v[184:185], v236 offset:0\n" \
"v_mfma_f32_16x16x32_f16 v[12:15], v[148:151], v[176:179], v[12:15]\n" \
"v_pk_add_f16 v176, v176, v180\n" \
"v_pk_add_f16 v177, v177, v181\n" \
"ds_read_b64_tr_b16 v[188:189], v237 offset:0\n" \
"v_mfma_f32_16x16x32_f16 v[4:7], v[152:155], v[180:183], v[4:7]\n" \
"v_pk_add_f16 v178, v178, v182\n" \
"v_pk_add_f16 v179, v179, v183\n" \
"v_mfma_f32_16x16x32_f16 v[16:19], v[156:159], v[180:183], v[16:19]\n" \
"ds_read_b128 v[172:175], v240 offset:47616\n" \
"s_add_i32 m0, s44, 0x5000\n" \
"s_add_i32 s80, s46, 0xc00000\n" \
"v_mfma_f32_16x16x32_f16 v[8:11], v[120:123], v[176:179], v[8:11]\n" \
"buffer_load_dwordx4 v232, s[36:39], s80 offen sc1 lds\n" \
"v_mfma_f32_16x16x32_f16 v[20:23], v[124:127], v[176:179], v[20:23]\n" \
"ds_read_b64_tr_b16 v[128:129], v241 offset:32768\n" \
"ds_read_b64_tr_b16 v[130:131], v241 offset:40960\n" \
"s_waitcnt lgkmcnt(4)\n" \
"v_mfma_f32_16x16x16_bf16 v[184:187], v[184:185], v[192:193], 0\n" \
"v_mfma_f32_16x16x32_f16 v[24:27], v[144:147], v[160:163], v[24:27]\n" \
"ds_read_b128 v[176:179], v240 offset:49152\n" \
"s_waitcnt lgkmcnt(4)\n" \
"v_mfma_f32_16x16x16_bf16 v[188:191], v[188:189], v[192:193], 0\n" \
"v_mfma_f32_16x16x32_f16 v[36:39], v[148:151], v[160:163], v[36:39]\n" \
"v_pk_add_f16 v160, v160, v164\n" \
"v_pk_add_f16 v161, v161, v165\n" \
"ds_read_b64_tr_b16 v[132:133], v242 offset:32768\n" \
"v_mfma_f32_16x16x32_f16 v[28:31], v[152:155], v[164:167], v[28:31]\n" \
"v_pk_add_f16 v162, v162, v166\n" \
"v_pk_add_f16 v163, v163, v167\n" \
"v_mfma_f32_16x16x32_f16 v[40:43], v[156:159], v[164:167], v[40:43]\n" \
"v_cvt_pk_f16_f32 v184, v184, v185\n" \
"ds_read_b128 v[180:183], v240 offset:49664\n" \
"ds_read_b64_tr_b16 v[134:135], v242 offset:40960\n" \
"v_mfma_f32_16x16x32_f16 v[32:35], v[120:123], v[160:163], v[32:35]\n" \
"v_cvt_pk_f16_f32 v185, v186, v187\n" \
"v_cvt_pk_f16_f32 v186, v188, v189\n" \
"buffer_load_dwordx4 v232, s[36:39], s80 offen offset:1024 sc1 lds\n" \
"v_mfma_f32_16x16x32_f16 v[44:47], v[124:127], v[160:163], v[44:47]\n" \
"v_cvt_pk_f16_f32 v187, v190, v191\n" \
"v_add_u32 v184, s91, v184\n" \
"v_mfma_f32_16x16x32_f16 v[48:51], v[144:147], v[168:171], v[48:51]\n" \
"v_add_u32 v185, s91, v185\n" \
"v_add_u32 v186, s91, v186\n" \
"ds_read_b128 v[160:163], v240 offset:40960\n" \
"v_mfma_f32_16x16x32_f16 v[60:63], v[148:151], v[168:171], v[60:63]\n" \
"s_waitcnt lgkmcnt(4)\n" \
"v_pk_add_f16 v168, v168, v172\n" \
"v_pk_add_f16 v169, v169, v173\n" \
"v_add_u32 v187, s91, v187\n" \
"v_and_b32 v184, s92, v184\n" \
"s_add_i32 s82, s49, 0x2800000\n" \
"v_mfma_f32_16x16x32_f16 v[52:55], v[152:155], v[172:175], v[52:55]\n" \
"v_pk_add_f16 v170, v170, v174\n" \
"v_pk_add_f16 v171, v171, v175\n" \
"v_and_b32 v185, s92, v185\n" \
"v_and_b32 v186, s92, v186\n" \
"v_and_b32 v187, s92, v187\n" \
"v_mfma_f32_16x16x32_f16 v[64:67], v[156:159], v[172:175], v[64:67]\n" \
"ds_read_b128 v[164:167], v240 offset:41472\n" \
"buffer_store_dwordx4 v[184:187], v234, s[36:39], s82 offen sc1\n" \
"ds_read_b64_tr_b16 v[136:137], v241 offset:33280\n" \
"v_mfma_f32_16x16x32_f16 v[56:59], v[120:123], v[168:171], v[56:59]\n" \
"buffer_load_dwordx4 v232, s[36:39], s80 offen offset:2048 sc1 lds\n" \
"ds_read_b64_tr_b16 v[138:139], v241 offset:41472\n" \
"v_mfma_f32_16x16x32_f16 v[68:71], v[124:127], v[168:171], v[68:71]\n" \
"s_setprio 0\n" \
"ds_read_b64_tr_b16 v[184:185], v238 offset:0\n" \
"ds_read_b64_tr_b16 v[188:189], v239 offset:0\n" \
"v_mfma_f32_16x16x32_f16 v[72:75], v[144:147], v[176:179], v[72:75]\n" \
"ds_read_b128 v[168:171], v240 offset:43008\n" \
"ds_read_b64_tr_b16 v[140:141], v242 offset:33280\n" \
"v_mfma_f32_16x16x32_f16 v[84:87], v[148:151], v[176:179], v[84:87]\n" \
"s_waitcnt lgkmcnt(7)\n" \
"v_pk_add_f16 v176, v176, v180\n" \
"v_pk_add_f16 v177, v177, v181\n" \
"v_mfma_f32_16x16x32_f16 v[76:79], v[152:155], v[180:183], v[76:79]\n" \
"v_pk_add_f16 v178, v178, v182\n" \
"v_pk_add_f16 v179, v179, v183\n" \
"s_waitcnt lgkmcnt(3)\n" \
"v_mfma_f32_16x16x16_bf16 v[184:187], v[184:185], v[192:193], 0\n" \
"v_mfma_f32_16x16x32_f16 v[88:91], v[156:159], v[180:183], v[88:91]\n" \
"ds_read_b128 v[172:175], v240 offset:43520\n" \
"ds_read_b64_tr_b16 v[142:143], v242 offset:41472\n" \
"s_waitcnt lgkmcnt(4)\n" \
"v_mfma_f32_16x16x16_bf16 v[188:191], v[188:189], v[192:193], 0\n" \
"v_mfma_f32_16x16x32_f16 v[80:83], v[120:123], v[176:179], v[80:83]\n" \
"buffer_load_dwordx4 v232, s[36:39], s80 offen offset:3072 sc1 lds\n" \
"v_mfma_f32_16x16x32_f16 v[92:95], v[124:127], v[176:179], v[92:95]\n" \
"v_mfma_f32_16x16x32_f16 v[96:99], v[144:147], v[160:163], v[96:99]\n" \
"v_pk_add_f16 v144, v128, v136\n" \
"v_pk_add_f16 v145, v129, v137\n" \
"v_cvt_pk_f16_f32 v184, v184, v185\n" \
"v_cvt_pk_f16_f32 v185, v186, v187\n" \
"ds_read_b128 v[176:179], v240 offset:45056\n" \
"v_mfma_f32_16x16x32_f16 v[108:111], v[148:151], v[160:163], v[108:111]\n" \
"v_pk_add_f16 v160, v160, v164\n" \
"v_pk_add_f16 v161, v161, v165\n" \
"v_cvt_pk_f16_f32 v186, v188, v189\n" \
"v_cvt_pk_f16_f32 v187, v190, v191\n" \
"ds_read_b128 v[180:183], v240 offset:45568\n" \
"v_mfma_f32_16x16x32_f16 v[100:103], v[152:155], v[164:167], v[100:103]\n" \
"v_pk_add_f16 v162, v162, v166\n" \
"v_pk_add_f16 v163, v163, v167\n" \
"v_add_u32 v184, s91, v184\n" \
"v_add_u32 v185, s91, v185\n" \
"v_add_u32 v186, s91, v186\n" \
"s_add_i32 m0, s45, 0x5000\n" \
"s_add_i32 s81, s47, 0xc00000\n" \
"v_mfma_f32_16x16x32_f16 v[112:115], v[156:159], v[164:167], v[112:115]\n" \
"v_pk_add_f16 v146, v130, v138\n" \
"v_pk_add_f16 v147, v131, v139\n" \
"v_add_u32 v187, s91, v187\n" \
"v_and_b32 v184, s92, v184\n" \
"buffer_load_dwordx4 v232, s[36:39], s81 offen sc1 lds\n" \
"v_mfma_f32_16x16x32_f16 v[104:107], v[120:123], v[160:163], v[104:107]\n" \
"s_waitcnt lgkmcnt(2)\n" \
"v_pk_add_f16 v148, v132, v140\n" \
"v_pk_add_f16 v149, v133, v141\n" \
"v_and_b32 v185, s92, v185\n" \
"v_and_b32 v186, s92, v186\n" \
"v_and_b32 v187, s92, v187\n" \
"v_mfma_f32_16x16x32_f16 v[116:119], v[124:127], v[160:163], v[116:119]\n" \
"s_setprio 1\n" \
"v_pk_add_f16 v150, v134, v142\n" \
"v_pk_add_f16 v151, v135, v143\n" \
"buffer_store_dwordx4 v[184:187], v234, s[36:39], s82 offen offset:1024 sc1\n" \
"s_waitcnt vmcnt(7) lgkmcnt(0)\n" \
"s_cmp_eq_u32 s94, 0\n" \
"s_cbranch_scc1 .Lp2_%=\n" \
".Lq2_%=:\n" \
"s_memrealtime s[86:87]\n" \
"s_waitcnt lgkmcnt(0)\n" \
"s_sub_i32 s87, s86, s75\n" \
"s_cmp_lt_i32 s87, 0\n" \
"s_cbranch_scc1 .Lq2_%=\n" \
"s_lshr_b64 s[94:95], s[94:95], 8\n" \
"s_and_b32 s87, s94, 0xff\n" \
"s_add_i32 s75, s86, s87\n" \
".Lp2_%=:\n" \
"s_barrier\n" \
"v_mfma_f32_16x16x32_f16 v[0:3], v[128:131], v[168:171], v[0:3]\n" \
"ds_read_b128 v[160:163], v240 offset:6144\n" \
"ds_read_b64_tr_b16 v[184:185], v236 offset:4096\n" \
"ds_read_b64_tr_b16 v[188:189], v237 offset:4096\n" \
"v_mfma_f32_16x16x32_f16 v[12:15], v[132:135], v[168:171], v[12:15]\n" \
"v_pk_add_f16 v168, v168, v172\n" \
"v_pk_add_f16 v169, v169, v173\n" \
"v_mfma_f32_16x16x32_f16 v[4:7], v[136:139], v[172:175], v[4:7]\n" \
"v_pk_add_f16 v170, v170, v174\n" \
"v_pk_add_f16 v171, v171, v175\n" \
"buffer_store_dwordx4 v[204:207], v235, s[36:39], s62 offen sc1\n" \
"v_mfma_f32_16x16x32_f16 v[16:19], v[140:143], v[172:175], v[16:19]\n" \
"ds_read_b128 v[164:167], v240 offset:6656\n" \
"s_add_i32 m0, s44, 0xa000\n" \
"s_add_i32 s80, s46, 0x1000000\n" \
"v_mfma_f32_16x16x32_f16 v[8:11], v[144:147], v[168:171], v[8:11]\n" \
"buffer_load_dwordx4 v232, s[36:39], s80 offen sc1 lds\n" \
"s_waitcnt lgkmcnt(2)\n" \
"v_mfma_f32_16x16x16_bf16 v[184:187], v[184:185], v[192:193], 0\n" \
"v_mfma_f32_16x16x32_f16 v[20:23], v[148:151], v[168:171], v[20:23]\n" \
"ds_read_b64_tr_b16 v[120:121], v241 offset:0\n" \
"ds_read_b64_tr_b16 v[122:123], v241 offset:8192\n" \
"s_waitcnt lgkmcnt(3)\n" \
"v_mfma_f32_16x16x16_bf16 v[188:191], v[188:189], v[192:193], 0\n" \
"v_mfma_f32_16x16x32_f16 v[24:27], v[128:131], v[176:179], v[24:27]\n" \
"ds_read_b128 v[168:171], v240 offset:8192\n" \
"v_mfma_f32_16x16x32_f16 v[36:39], v[132:135], v[176:179], v[36:39]\n" \
"v_pk_add_f16 v176, v176, v180\n" \
"v_pk_add_f16 v177, v177, v181\n" \
"ds_read_b64_tr_b16 v[124:125], v242 offset:0\n" \
"v_mfma_f32_16x16x32_f16 v[28:31], v[136:139], v[180:183], v[28:31]\n" \
"v_pk_add_f16 v178, v178, v182\n" \
"v_pk_add_f16 v179, v179, v183\n" \
"v_cvt_pk_f16_f32 v184, v184, v185\n" \
"v_mfma_f32_16x16x32_f16 v[40:43], v[140:143], v[180:183], v[40:43]\n" \
"v_cvt_pk_f16_f32 v185, v186, v187\n" \
"v_cvt_pk_f16_f32 v186, v188, v189\n" \
"ds_read_b128 v[172:175], v240 offset:8704\n" \
"v_mfma_f32_16x16x32_f16 v[32:35], v[144:147], v[176:179], v[32:35]\n" \
"v_cvt_pk_f16_f32 v187, v190, v191\n" \
"v_add_u32 v184, s91, v184\n" \
"buffer_load_dwordx4 v232, s[36:39], s80 offen offset:1024 sc1 lds\n" \
"v_mfma_f32_16x16x32_f16 v[44:47], v[148:151], v[176:179], v[44:47]\n" \
"v_add_u32 v185, s91, v185\n" \
"v_add_u32 v186, s91, v186\n" \
"ds_read_b64_tr_b16 v[126:127], v242 offset:8192\n" \
"v_mfma_f32_16x16x32_f16 v[48:51], v[128:131], v[160:163], v[48:51]\n" \
"v_add_u32 v187, s91, v187\n" \
"v_and_b32 v184, s92, v184\n" \
"ds_read_b128 v[176:179], v240 offset:0\n" \
"v_mfma_f32_16x16x32_f16 v[60:63], v[132:135], v[160:163], v[60:63]\n" \
"s_waitcnt lgkmcnt(4)\n" \
"v_pk_add_f16 v160, v160, v164\n" \
"v_pk_add_f16 v161, v161, v165\n" \
"v_and_b32 v185, s92, v185\n" \
"s_add_i32 s82, s50, 0x3000000\n" \
"v_mfma_f32_16x16x32_f16 v[52:55], v[136:139], v[164:167], v[52:55]\n" \
"v_pk_add_f16 v162, v162, v166\n" \
"v_pk_add_f16 v163, v163, v167\n" \
"v_and_b32 v186, s92, v186\n" \
"v_and_b32 v187, s92, v187\n" \
"v_mfma_f32_16x16x32_f16 v[64:67], v[140:143], v[164:167], v[64:67]\n" \
"ds_read_b128 v[180:183], v240 offset:512\n" \
"buffer_store_dwordx4 v[184:187], v234, s[36:39], s82 offen sc1\n" \
"ds_read_b64_tr_b16 v[152:153], v241 offset:512\n" \
"v_mfma_f32_16x16x32_f16 v[56:59], v[144:147], v[160:163], v[56:59]\n" \
"buffer_load_dwordx4 v232, s[36:39], s80 offen offset:2048 sc1 lds\n" \
"v_mfma_f32_16x16x32_f16 v[68:71], v[148:151], v[160:163], v[68:71]\n" \
"s_setprio 0\n" \
"ds_read_b64_tr_b16 v[154:155], v241 offset:8704\n" \
"ds_read_b64_tr_b16 v[156:157], v242 offset:512\n" \
"v_mfma_f32_16x16x32_f16 v[72:75], v[128:131], v[168:171], v[72:75]\n" \
"ds_read_b128 v[160:163], v240 offset:2048\n" \
"ds_read_b64_tr_b16 v[158:159], v242 offset:8704\n" \
"v_mfma_f32_16x16x32_f16 v[84:87], v[132:135], v[168:171], v[84:87]\n" \
"s_waitcnt lgkmcnt(6)\n" \
"v_pk_add_f16 v168, v168, v172\n" \
"v_pk_add_f16 v169, v169, v173\n" \
"v_mfma_f32_16x16x32_f16 v[76:79], v[136:139], v[172:175], v[76:79]\n" \
"v_pk_add_f16 v170, v170, v174\n" \
"v_pk_add_f16 v171, v171, v175\n" \
"v_mfma_f32_16x16x32_f16 v[88:91], v[140:143], v[172:175], v[88:91]\n" \
"ds_read_b128 v[164:167], v240 offset:2560\n" \
"v_mfma_f32_16x16x32_f16 v[80:83], v[144:147], v[168:171], v[80:83]\n" \
"buffer_load_dwordx4 v232, s[36:39], s80 offen offset:3072 sc1 lds\n" \
"v_mfma_f32_16x16x32_f16 v[92:95], v[148:151], v[168:171], v[92:95]\n" \
"v_mfma_f32_16x16x32_f16 v[96:99], v[128:131], v[176:179], v[96:99]\n" \
"s_waitcnt lgkmcnt(5)\n" \
"v_pk_add_f16 v128, v120, v152\n" \
"v_pk_add_f16 v129, v121, v153\n" \
"ds_read_b128 v[168:171], v240 offset:4096\n" \
"v_mfma_f32_16x16x32_f16 v[108:111], v[132:135], v[176:179], v[108:111]\n" \
"v_pk_add_f16 v176, v176, v180\n" \
"v_pk_add_f16 v177, v177, v181\n" \
"ds_read_b128 v[172:175], v240 offset:4608\n" \
"v_mfma_f32_16x16x32_f16 v[100:103], v[136:139], v[180:183], v[100:103]\n" \
"v_pk_add_f16 v178, v178, v182\n" \
"v_pk_add_f16 v179, v179, v183\n" \
"s_add_i32 m0, s45, 0xa000\n" \
"s_add_i32 s81, s47, 0x1000000\n" \
"v_mfma_f32_16x16x32_f16 v[112:115], v[140:143], v[180:183], v[112:115]\n" \
"s_waitcnt lgkmcnt(3)\n" \
"v_pk_add_f16 v130, v122, v154\n" \
"v_pk_add_f16 v131, v123, v155\n" \
"buffer_load_dwordx4 v232, s[36:39], s81 offen sc1 lds\n" \
"v_mfma_f32_16x16x32_f16 v[104:107], v[144:147], v[176:179], v[104:107]\n" \
"v_pk_add_f16 v132, v124, v156\n" \
"v_pk_add_f16 v133, v125, v157\n" \
"v_mfma_f32_16x16x32_f16 v[116:119], v[148:151], v[176:179], v[116:119]\n" \
"s_setprio 1\n" \
"v_pk_add_f16 v134, v126, v158\n" \
"v_pk_add_f16 v135, v127, v159\n" \
"s_waitcnt vmcnt(7) lgkmcnt(0)\n" \
"s_cmp_eq_u32 s94, 0\n" \
"s_cbranch_scc1 .Lp3_%=\n" \
".Lq3_%=:\n" \
"s_memrealtime s[86:87]\n" \
"s_waitcnt lgkmcnt(0)\n" \
"s_sub_i32 s87, s86, s75\n" \
"s_cmp_lt_i32 s87, 0\n" \
"s_cbranch_scc1 .Lq3_%=\n" \
"s_lshr_b64 s[94:95], s[94:95], 8\n" \
"s_and_b32 s87, s94, 0xff\n" \
"s_add_i32 s75, s86, s87\n" \
".Lp3_%=:\n" \
"s_barrier\n" \
"v_mfma_f32_16x16x32_f16 v[0:3], v[120:123], v[160:163], v[0:3]\n" \
"ds_read_b128 v[176:179], v240 offset:26624\n" \
"ds_read_b64_tr_b16 v[184:185], v238 offset:4096\n" \
"ds_read_b64_tr_b16 v[188:189], v239 offset:4096\n" \
"v_mfma_f32_16x16x32_f16 v[12:15], v[124:127], v[160:163], v[12:15]\n" \
"v_pk_add_f16 v160, v160, v164\n" \
"v_pk_add_f16 v161, v161, v165\n" \
"v_mfma_f32_16x16x32_f16 v[4:7], v[152:155], v[164:167], v[4:7]\n" \
"v_pk_add_f16 v162, v162, v166\n" \
"v_pk_add_f16 v163, v163, v167\n" \
"v_mfma_f32_16x16x32_f16 v[16:19], v[156:159], v[164:167], v[16:19]\n" \
"ds_read_b128 v[180:183], v240 offset:27136\n" \
"s_add_i32 m0, s44, 0x0\n" \
"s_add_i32 s80, s46, 0x1400000\n" \
"v_mfma_f32_16x16x32_f16 v[8:11], v[128:131], v[160:163], v[8:11]\n" \
"buffer_load_dwordx4 v232, s[36:39], s80 offen sc1 lds\n" \
"s_waitcnt lgkmcnt(2)\n" \
"v_mfma_f32_16x16x16_bf16 v[184:187], v[184:185], v[192:193], 0\n" \
"v_mfma_f32_16x16x32_f16 v[20:23], v[132:135], v[160:163], v[20:23]\n" \
"ds_read_b64_tr_b16 v[136:137], v241 offset:16384\n" \
"ds_read_b64_tr_b16 v[138:139], v241 offset:24576\n" \
"s_waitcnt lgkmcnt(3)\n" \
"v_mfma_f32_16x16x16_bf16 v[188:191], v[188:189], v[192:193], 0\n" \
"v_mfma_f32_16x16x32_f16 v[24:27], v[120:123], v[168:171], v[24:27]\n" \
"ds_read_b128 v[160:163], v240 offset:28672\n" \
"v_mfma_f32_16x16x32_f16 v[36:39], v[124:127], v[168:171], v[36:39]\n" \
"v_pk_add_f16 v168, v168, v172\n" \
"v_pk_add_f16 v169, v169, v173\n" \
"ds_read_b64_tr_b16 v[140:141], v242 offset:16384\n" \
"v_mfma_f32_16x16x32_f16 v[28:31], v[152:155], v[172:175], v[28:31]\n" \
"v_pk_add_f16 v170, v170, v174\n" \
"v_pk_add_f16 v171, v171, v175\n" \
"v_cvt_pk_f16_f32 v184, v184, v185\n" \
"v_mfma_f32_16x16x32_f16 v[40:43], v[156:159], v[172:175], v[40:43]\n" \
"v_cvt_pk_f16_f32 v185, v186, v187\n" \
"v_cvt_pk_f16_f32 v186, v188, v189\n" \
"ds_read_b128 v[164:167], v240 offset:29184\n" \
"v_mfma_f32_16x16x32_f16 v[32:35], v[128:131], v[168:171], v[32:35]\n" \
"v_cvt_pk_f16_f32 v187, v190, v191\n" \
"v_add_u32 v184, s91, v184\n" \
"buffer_load_dwordx4 v232, s[36:39], s80 offen offset:1024 sc1 lds\n" \
"v_mfma_f32_16x16x32_f16 v[44:47], v[132:135], v[168:171], v[44:47]\n" \
"v_add_u32 v185, s91, v185\n" \
"v_add_u32 v186, s91, v186\n" \
"ds_read_b64_tr_b16 v[142:143], v242 offset:24576\n" \
"v_mfma_f32_16x16x32_f16 v[48:51], v[120:123], v[176:179], v[48:51]\n" \
"v_add_u32 v187, s91, v187\n" \
"v_and_b32 v184, s92, v184\n" \
"ds_read_b128 v[168:171], v240 offset:20480\n" \
"v_mfma_f32_16x16x32_f16 v[60:63], v[124:127], v[176:179], v[60:63]\n" \
"s_waitcnt lgkmcnt(4)\n" \
"v_pk_add_f16 v176, v176, v180\n" \
"v_pk_add_f16 v177, v177, v181\n" \
"v_and_b32 v185, s92, v185\n" \
"v_mfma_f32_16x16x32_f16 v[52:55], v[152:155], v[180:183], v[52:55]\n" \
"v_pk_add_f16 v178, v178, v182\n" \
"v_pk_add_f16 v179, v179, v183\n" \
"v_and_b32 v186, s92, v186\n" \
"v_and_b32 v187, s92, v187\n" \
"v_mfma_f32_16x16x32_f16 v[64:67], v[156:159], v[180:183], v[64:67]\n" \
"ds_read_b128 v[172:175], v240 offset:20992\n" \
"buffer_store_dwordx4 v[184:187], v234, s[36:39], s82 offen offset:1024 sc1\n" \
"ds_read_b64_tr_b16 v[144:145], v241 offset:16896\n" \
"v_mfma_f32_16x16x32_f16 v[56:59], v[128:131], v[176:179], v[56:59]\n" \
"buffer_load_dwordx4 v232, s[36:39], s80 offen offset:2048 sc1 lds\n" \
"v_mfma_f32_16x16x32_f16 v[68:71], v[132:135], v[176:179], v[68:71]\n" \
"s_setprio 0\n" \
"ds_read_b64_tr_b16 v[146:147], v241 offset:25088\n" \
"ds_read_b64_tr_b16 v[148:149], v242 offset:16896\n" \
"v_mfma_f32_16x16x32_f16 v[72:75], v[120:123], v[160:163], v[72:75]\n" \
"ds_read_b128 v[176:179], v240 offset:22528\n" \
"ds_read_b64_tr_b16 v[150:151], v242 offset:25088\n" \
"v_mfma_f32_16x16x32_f16 v[84:87], v[124:127], v[160:163], v[84:87]\n" \
"s_waitcnt lgkmcnt(6)\n" \
"v_pk_add_f16 v160, v160, v164\n" \
"v_pk_add_f16 v161, v161, v165\n" \
"v_mfma_f32_16x16x32_f16 v[76:79], v[152:155], v[164:167], v[76:79]\n" \
"v_pk_add_f16 v162, v162, v166\n" \
"v_pk_add_f16 v163, v163, v167\n" \
"v_mfma_f32_16x16x32_f16 v[88:91], v[156:159], v[164:167], v[88:91]\n" \
"ds_read_b128 v[180:183], v240 offset:23040\n" \
"v_mfma_f32_16x16x32_f16 v[80:83], v[128:131], v[160:163], v[80:83]\n" \
"buffer_load_dwordx4 v232, s[36:39], s80 offen offset:3072 sc1 lds\n" \
"v_mfma_f32_16x16x32_f16 v[92:95], v[132:135], v[160:163], v[92:95]\n" \
"v_mfma_f32_16x16x32_f16 v[96:99], v[120:123], v[168:171], v[96:99]\n" \
"s_waitcnt lgkmcnt(5)\n" \
"v_pk_add_f16 v120, v136, v144\n" \
"v_pk_add_f16 v121, v137, v145\n" \
"ds_read_b128 v[160:163], v240 offset:24576\n" \
"v_mfma_f32_16x16x32_f16 v[108:111], v[124:127], v[168:171], v[108:111]\n" \
"v_pk_add_f16 v168, v168, v172\n" \
"v_pk_add_f16 v169, v169, v173\n" \
"ds_read_b128 v[164:167], v240 offset:25088\n" \
"v_mfma_f32_16x16x32_f16 v[100:103], v[152:155], v[172:175], v[100:103]\n" \
"v_pk_add_f16 v170, v170, v174\n" \
"v_pk_add_f16 v171, v171, v175\n" \
"s_add_i32 m0, s45, 0x0\n" \
"s_add_i32 s81, s47, 0x1400000\n" \
"v_mfma_f32_16x16x32_f16 v[112:115], v[156:159], v[172:175], v[112:115]\n" \
"s_waitcnt lgkmcnt(3)\n" \
"v_pk_add_f16 v122, v138, v146\n" \
"v_pk_add_f16 v123, v139, v147\n" \
"buffer_load_dwordx4 v232, s[36:39], s81 offen sc1 lds\n" \
"v_mfma_f32_16x16x32_f16 v[104:107], v[128:131], v[168:171], v[104:107]\n" \
"v_pk_add_f16 v124, v140, v148\n" \
"v_pk_add_f16 v125, v141, v149\n" \
"v_mfma_f32_16x16x32_f16 v[116:119], v[132:135], v[168:171], v[116:119]\n" \
"s_setprio 1\n" \
"v_pk_add_f16 v126, v142, v150\n" \
"v_pk_add_f16 v127, v143, v151\n" \
"s_waitcnt vmcnt(6) lgkmcnt(0)\n" \
"s_cmp_eq_u32 s94, 0\n" \
"s_cbranch_scc1 .Lp4_%=\n" \
".Lq4_%=:\n" \
"s_memrealtime s[86:87]\n" \
"s_waitcnt lgkmcnt(0)\n" \
"s_sub_i32 s87, s86, s75\n" \
"s_cmp_lt_i32 s87, 0\n" \
"s_cbranch_scc1 .Lq4_%=\n" \
"s_lshr_b64 s[94:95], s[94:95], 8\n" \
"s_and_b32 s87, s94, 0xff\n" \
"s_add_i32 s75, s86, s87\n" \
".Lp4_%=:\n" \
"s_barrier\n" \
"v_mfma_f32_16x16x32_f16 v[0:3], v[136:139], v[176:179], v[0:3]\n" \
"ds_read_b128 v[168:171], v240 offset:47104\n" \
"ds_read_b64_tr_b16 v[184:185], v236 offset:8192\n" \
"ds_read_b64_tr_b16 v[188:189], v237 offset:8192\n" \
"v_mfma_f32_16x16x32_f16 v[12:15], v[140:143], v[176:179], v[12:15]\n" \
"v_pk_add_f16 v176, v176, v180\n" \
"v_pk_add_f16 v177, v177, v181\n" \
"s_mov_b32 s84, 0x8004000\n" \
"v_mfma_f32_16x16x32_f16 v[4:7], v[144:147], v[180:183], v[4:7]\n" \
"v_pk_add_f16 v178, v178, v182\n" \
"v_pk_add_f16 v179, v179, v183\n" \
"buffer_load_dword v253, v252, s[36:39], s84 offen sc1\n" \
"buffer_store_dwordx4 v[208:211], v235, s[36:39], s63 offen sc1\n" \
"v_mfma_f32_16x16x32_f16 v[16:19], v[148:151], v[180:183], v[16:19]\n" \
"ds_read_b128 v[172:175], v240 offset:47616\n" \
"s_add_i32 m0, s44, 0x5000\n" \
"s_add_i32 s80, s46, 0x1800000\n" \
"v_mfma_f32_16x16x32_f16 v[8:11], v[120:123], v[176:179], v[8:11]\n" \
"buffer_load_dwordx4 v232, s[36:39], s80 offen sc1 lds\n" \
"s_waitcnt lgkmcnt(2)\n" \
"v_mfma_f32_16x16x16_bf16 v[184:187], v[184:185], v[192:193], 0\n" \
"v_mfma_f32_16x16x32_f16 v[20:23], v[124:127], v[176:179], v[20:23]\n" \
"ds_read_b64_tr_b16 v[128:129], v241 offset:32768\n" \
"ds_read_b64_tr_b16 v[130:131], v241 offset:40960\n" \
"s_waitcnt lgkmcnt(3)\n" \
"v_mfma_f32_16x16x16_bf16 v[188:191], v[188:189], v[192:193], 0\n" \
"v_mfma_f32_16x16x32_f16 v[24:27], v[136:139], v[160:163], v[24:27]\n" \
"ds_read_b128 v[176:179], v240 offset:49152\n" \
"v_mfma_f32_16x16x32_f16 v[36:39], v[140:143], v[160:163], v[36:39]\n" \
"v_pk_add_f16 v160, v160, v164\n" \
"v_pk_add_f16 v161, v161, v165\n" \
"ds_read_b64_tr_b16 v[132:133], v242 offset:32768\n" \
"v_mfma_f32_16x16x32_f16 v[28:31], v[144:147], v[164:167], v[28:31]\n" \
"v_pk_add_f16 v162, v162, v166\n" \
"v_pk_add_f16 v163, v163, v167\n" \
"v_cvt_pk_f16_f32 v184, v184, v185\n" \
"v_mfma_f32_16x16x32_f16 v[40:43], v[148:151], v[164:167], v[40:43]\n" \
"v_cvt_pk_f16_f32 v185, v186, v187\n" \
"v_cvt_pk_f16_f32 v186, v188, v189\n" \
"ds_read_b128 v[180:183], v240 offset:49664\n" \
"v_mfma_f32_16x16x32_f16 v[32:35], v[120:123], v[160:163], v[32:35]\n" \
"v_cvt_pk_f16_f32 v187, v190, v191\n" \
"v_add_u32 v184, s91, v184\n" \
"buffer_load_dwordx4 v232, s[36:39], s80 offen offset:1024 sc1 lds\n" \
"v_mfma_f32_16x16x32_f16 v[44:47], v[124:127], v[160:163], v[44:47]\n" \
"v_add_u32 v185, s91, v185\n" \
"v_add_u32 v186, s91, v186\n" \
"ds_read_b64_tr_b16 v[134:135], v242 offset:40960\n" \
"v_mfma_f32_16x16x32_f16 v[48:51], v[136:139], v[168:171], v[48:51]\n" \
"v_add_u32 v187, s91, v187\n" \
"v_and_b32 v184, s92, v184\n" \
"ds_read_b128 v[160:163], v240 offset:40960\n" \
"v_mfma_f32_16x16x32_f16 v[60:63], v[140:143], v[168:171], v[60:63]\n" \
"s_waitcnt lgkmcnt(4)\n" \
"v_pk_add_f16 v168, v168, v172\n" \
"v_pk_add_f16 v169, v169, v173\n" \
"v_and_b32 v185, s92, v185\n" \
"s_add_i32 s82, s51, 0x3800000\n" \
"v_mfma_f32_16x16x32_f16 v[52:55], v[144:147], v[172:175], v[52:55]\n" \
"v_pk_add_f16 v170, v170, v174\n" \
"v_pk_add_f16 v171, v171, v175\n" \
"v_and_b32 v186, s92, v186\n" \
"v_and_b32 v187, s92, v187\n" \
"v_mfma_f32_16x16x32_f16 v[64:67], v[148:151], v[172:175], v[64:67]\n" \
"ds_read_b128 v[164:167], v240 offset:41472\n" \
"buffer_store_dwordx4 v[184:187], v234, s[36:39], s82 offen sc1\n" \
"ds_read_b64_tr_b16 v[152:153], v241 offset:33280\n" \
"v_mfma_f32_16x16x32_f16 v[56:59], v[120:123], v[168:171], v[56:59]\n" \
"buffer_load_dwordx4 v232, s[36:39], s80 offen offset:2048 sc1 lds\n" \
"v_mfma_f32_16x16x32_f16 v[68:71], v[124:127], v[168:171], v[68:71]\n" \
"s_setprio 0\n" \
"ds_read_b64_tr_b16 v[154:155], v241 offset:41472\n" \
"ds_read_b64_tr_b16 v[156:157], v242 offset:33280\n" \
"v_mfma_f32_16x16x32_f16 v[72:75], v[136:139], v[176:179], v[72:75]\n" \
"ds_read_b128 v[168:171], v240 offset:43008\n" \
"ds_read_b64_tr_b16 v[158:159], v242 offset:41472\n" \
"v_mfma_f32_16x16x32_f16 v[84:87], v[140:143], v[176:179], v[84:87]\n" \
"s_waitcnt lgkmcnt(6)\n" \
"v_pk_add_f16 v176, v176, v180\n" \
"v_pk_add_f16 v177, v177, v181\n" \
"v_mfma_f32_16x16x32_f16 v[76:79], v[144:147], v[180:183], v[76:79]\n" \
"v_pk_add_f16 v178, v178, v182\n" \
"v_pk_add_f16 v179, v179, v183\n" \
"v_mfma_f32_16x16x32_f16 v[88:91], v[148:151], v[180:183], v[88:91]\n" \
"ds_read_b128 v[172:175], v240 offset:43520\n" \
"v_mfma_f32_16x16x32_f16 v[80:83], v[120:123], v[176:179], v[80:83]\n" \
"buffer_load_dwordx4 v232, s[36:39], s80 offen offset:3072 sc1 lds\n" \
"v_mfma_f32_16x16x32_f16 v[92:95], v[124:127], v[176:179], v[92:95]\n" \
"v_mfma_f32_16x16x32_f16 v[96:99], v[136:139], v[160:163], v[96:99]\n" \
"s_waitcnt lgkmcnt(5)\n" \
"v_pk_add_f16 v136, v128, v152\n" \
"v_pk_add_f16 v137, v129, v153\n" \
"ds_read_b128 v[176:179], v240 offset:45056\n" \
"v_mfma_f32_16x16x32_f16 v[108:111], v[140:143], v[160:163], v[108:111]\n" \
"v_pk_add_f16 v160, v160, v164\n" \
"v_pk_add_f16 v161, v161, v165\n" \
"ds_read_b128 v[180:183], v240 offset:45568\n" \
"v_mfma_f32_16x16x32_f16 v[100:103], v[144:147], v[164:167], v[100:103]\n" \
"v_pk_add_f16 v162, v162, v166\n" \
"v_pk_add_f16 v163, v163, v167\n" \
"s_waitcnt vmcnt(6)\n" \
"v_cmp_eq_u32 vcc, 0, v253\n" \
"s_cbranch_vccnz .Ls12_%=\n" \
".Lr12_%=:\n" \
"s_add_i32 m0, s45, 0x5000\n" \
"s_add_i32 s81, s47, 0x1800000\n" \
"v_mfma_f32_16x16x32_f16 v[112:115], v[148:151], v[164:167], v[112:115]\n" \
"s_waitcnt lgkmcnt(3)\n" \
"v_pk_add_f16 v138, v130, v154\n" \
"v_pk_add_f16 v139, v131, v155\n" \
"buffer_load_dwordx4 v232, s[36:39], s81 offen sc1 lds\n" \
"v_mfma_f32_16x16x32_f16 v[104:107], v[120:123], v[160:163], v[104:107]\n" \
"v_pk_add_f16 v140, v132, v156\n" \
"v_pk_add_f16 v141, v133, v157\n" \
"v_mfma_f32_16x16x32_f16 v[116:119], v[124:127], v[160:163], v[116:119]\n" \
"s_setprio 1\n" \
"v_pk_add_f16 v142, v134, v158\n" \
"v_pk_add_f16 v143, v135, v159\n" \
"s_waitcnt vmcnt(7) lgkmcnt(0)\n" \
"s_cmp_eq_u32 s94, 0\n" \
"s_cbranch_scc1 .Lp5_%=\n" \
".Lq5_%=:\n" \
"s_memrealtime s[86:87]\n" \
"s_waitcnt lgkmcnt(0)\n" \
"s_sub_i32 s87, s86, s75\n" \
"s_cmp_lt_i32 s87, 0\n" \
"s_cbranch_scc1 .Lq5_%=\n" \
"s_lshr_b64 s[94:95], s[94:95], 8\n" \
"s_and_b32 s87, s94, 0xff\n" \
"s_add_i32 s75, s86, s87\n" \
".Lp5_%=:\n" \
"s_barrier\n" \
"v_mfma_f32_16x16x32_f16 v[0:3], v[128:131], v[168:171], v[0:3]\n" \
"ds_read_b128 v[160:163], v240 offset:6144\n" \
"ds_read_b64_tr_b16 v[184:185], v238 offset:8192\n" \
"ds_read_b64_tr_b16 v[188:189], v239 offset:8192\n" \
"v_mfma_f32_16x16x32_f16 v[12:15], v[132:135], v[168:171], v[12:15]\n" \
"v_pk_add_f16 v168, v168, v172\n" \
"v_pk_add_f16 v169, v169, v173\n" \
"v_mfma_f32_16x16x32_f16 v[4:7], v[152:155], v[172:175], v[4:7]\n" \
"v_pk_add_f16 v170, v170, v174\n" \
"v_pk_add_f16 v171, v171, v175\n" \
"v_mfma_f32_16x16x32_f16 v[16:19], v[156:159], v[172:175], v[16:19]\n" \
"ds_read_b128 v[164:167], v240 offset:6656\n" \
"s_add_i32 m0, s44, 0xa000\n" \
"s_add_i32 s80, s46, 0x1c00000\n" \
"v_mfma_f32_16x16x32_f16 v[8:11], v[136:139], v[168:171], v[8:11]\n" \
"buffer_load_dwordx4 v232, s[36:39], s80 offen sc1 lds\n" \
"s_waitcnt lgkmcnt(2)\n" \
"v_mfma_f32_16x16x16_bf16 v[184:187], v[184:185], v[192:193], 0\n" \
"v_mfma_f32_16x16x32_f16 v[20:23], v[140:143], v[168:171], v[20:23]\n" \
"ds_read_b64_tr_b16 v[120:121], v241 offset:0\n" \
"ds_read_b64_tr_b16 v[122:123], v241 offset:8192\n" \
"s_waitcnt lgkmcnt(3)\n" \
"v_mfma_f32_16x16x16_bf16 v[188:191], v[188:189], v[192:193], 0\n" \
"v_mfma_f32_16x16x32_f16 v[24:27], v[128:131], v[176:179], v[24:27]\n" \
"ds_read_b128 v[168:171], v240 offset:8192\n" \
"v_mfma_f32_16x16x32_f16 v[36:39], v[132:135], v[176:179], v[36:39]\n" \
"v_pk_add_f16 v176, v176, v180\n" \
"v_pk_add_f16 v177, v177, v181\n" \
"ds_read_b64_tr_b16 v[124:125], v242 offset:0\n" \
"v_mfma_f32_16x16x32_f16 v[28:31], v[152:155], v[180:183], v[28:31]\n" \
"v_pk_add_f16 v178, v178, v182\n" \
"v_pk_add_f16 v179, v179, v183\n" \
"v_cvt_pk_f16_f32 v184, v184, v185\n" \
"v_mfma_f32_16x16x32_f16 v[40:43], v[156:159], v[180:183], v[40:43]\n" \
"v_cvt_pk_f16_f32 v185, v186, v187\n" \
"v_cvt_pk_f16_f32 v186, v188, v189\n" \
"ds_read_b128 v[172:175], v240 offset:8704\n" \
"v_mfma_f32_16x16x32_f16 v[32:35], v[136:139], v[176:179], v[32:35]\n" \
"v_cvt_pk_f16_f32 v187, v190, v191\n" \
"v_add_u32 v184, s91, v184\n" \
"buffer_load_dwordx4 v232, s[36:39], s80 offen offset:1024 sc1 lds\n" \
"v_mfma_f32_16x16x32_f16 v[44:47], v[140:143], v[176:179], v[44:47]\n" \
"v_add_u32 v185, s91, v185\n" \
"v_add_u32 v186, s91, v186\n" \
"ds_read_b64_tr_b16 v[126:127], v242 offset:8192\n" \
"v_mfma_f32_16x16x32_f16 v[48:51], v[128:131], v[160:163], v[48:51]\n" \
"v_add_u32 v187, s91, v187\n" \
"v_and_b32 v184, s92, v184\n" \
"ds_read_b128 v[176:179], v240 offset:0\n" \
"v_mfma_f32_16x16x32_f16 v[60:63], v[132:135], v[160:163], v[60:63]\n" \
"s_waitcnt lgkmcnt(4)\n" \
"v_pk_add_f16 v160, v160, v164\n" \
"v_pk_add_f16 v161, v161, v165\n" \
"v_and_b32 v185, s92, v185\n" \
"v_mfma_f32_16x16x32_f16 v[52:55], v[152:155], v[164:167], v[52:55]\n" \
"v_pk_add_f16 v162, v162, v166\n" \
"v_pk_add_f16 v163, v163, v167\n" \
"v_and_b32 v186, s92, v186\n" \
"v_and_b32 v187, s92, v187\n" \
"v_mfma_f32_16x16x32_f16 v[64:67], v[156:159], v[164:167], v[64:67]\n" \
"ds_read_b128 v[180:183], v240 offset:512\n" \
"buffer_store_dwordx4 v[184:187], v234, s[36:39], s82 offen offset:1024 sc1\n" \
"ds_read_b64_tr_b16 v[144:145], v241 offset:512\n" \
"v_mfma_f32_16x16x32_f16 v[56:59], v[136:139], v[160:163], v[56:59]\n" \
"buffer_load_dwordx4 v232, s[36:39], s80 offen offset:2048 sc1 lds\n" \
"v_mfma_f32_16x16x32_f16 v[68:71], v[140:143], v[160:163], v[68:71]\n" \
"s_setprio 0\n" \
"ds_read_b64_tr_b16 v[146:147], v241 offset:8704\n" \
"ds_read_b64_tr_b16 v[148:149], v242 offset:512\n" \
"v_mfma_f32_16x16x32_f16 v[72:75], v[128:131], v[168:171], v[72:75]\n" \
"ds_read_b128 v[160:163], v240 offset:2048\n" \
"ds_read_b64_tr_b16 v[150:151], v242 offset:8704\n" \
"v_mfma_f32_16x16x32_f16 v[84:87], v[132:135], v[168:171], v[84:87]\n" \
"s_waitcnt lgkmcnt(6)\n" \
"v_pk_add_f16 v168, v168, v172\n" \
"v_pk_add_f16 v169, v169, v173\n" \
"v_mfma_f32_16x16x32_f16 v[76:79], v[152:155], v[172:175], v[76:79]\n" \
"v_pk_add_f16 v170, v170, v174\n" \
"v_pk_add_f16 v171, v171, v175\n" \
"v_mfma_f32_16x16x32_f16 v[88:91], v[156:159], v[172:175], v[88:91]\n" \
"ds_read_b128 v[164:167], v240 offset:2560\n" \
"v_mfma_f32_16x16x32_f16 v[80:83], v[136:139], v[168:171], v[80:83]\n" \
"buffer_load_dwordx4 v232, s[36:39], s80 offen offset:3072 sc1 lds\n" \
"v_mfma_f32_16x16x32_f16 v[92:95], v[140:143], v[168:171], v[92:95]\n" \
"v_mfma_f32_16x16x32_f16 v[96:99], v[128:131], v[176:179], v[96:99]\n" \
"s_waitcnt lgkmcnt(5)\n" \
"v_pk_add_f16 v128, v120, v144\n" \
"v_pk_add_f16 v129, v121, v145\n" \
"ds_read_b128 v[168:171], v240 offset:4096\n" \
"v_mfma_f32_16x16x32_f16 v[108:111], v[132:135], v[176:179], v[108:111]\n" \
"v_pk_add_f16 v176, v176, v180\n" \
"v_pk_add_f16 v177, v177, v181\n" \
"ds_read_b128 v[172:175], v240 offset:4608\n" \
"v_mfma_f32_16x16x32_f16 v[100:103], v[152:155], v[180:183], v[100:103]\n" \
"v_pk_add_f16 v178, v178, v182\n" \
"v_pk_add_f16 v179, v179, v183\n" \
"s_add_i32 m0, s45, 0xa000\n" \
"s_add_i32 s81, s47, 0x1c00000\n" \
"v_mfma_f32_16x16x32_f16 v[112:115], v[156:159], v[180:183], v[112:115]\n" \
"s_waitcnt lgkmcnt(3)\n" \
"v_pk_add_f16 v130, v122, v146\n" \
"v_pk_add_f16 v131, v123, v147\n" \
"buffer_load_dwordx4 v232, s[36:39], s81 offen sc1 lds\n" \
"v_mfma_f32_16x16x32_f16 v[104:107], v[136:139], v[176:179], v[104:107]\n" \
"v_pk_add_f16 v132, v124, v148\n" \
"v_pk_add_f16 v133, v125, v149\n" \
"v_mfma_f32_16x16x32_f16 v[116:119], v[140:143], v[176:179], v[116:119]\n" \
"s_setprio 1\n" \
"v_pk_add_f16 v134, v126, v150\n" \
"v_pk_add_f16 v135, v127, v151\n" \
"s_waitcnt vmcnt(6) lgkmcnt(0)\n" \
"s_cmp_eq_u32 s94, 0\n" \
"s_cbranch_scc1 .Lp6_%=\n" \
".Lq6_%=:\n" \
"s_memrealtime s[86:87]\n" \
"s_waitcnt lgkmcnt(0)\n" \
"s_sub_i32 s87, s86, s75\n" \
"s_cmp_lt_i32 s87, 0\n" \
"s_cbranch_scc1 .Lq6_%=\n" \
"s_lshr_b64 s[94:95], s[94:95], 8\n" \
"s_and_b32 s87, s94, 0xff\n" \
"s_add_i32 s75, s86, s87\n" \
".Lp6_%=:\n" \
"s_barrier\n" \
"v_mfma_f32_16x16x32_f16 v[0:3], v[120:123], v[160:163], v[0:3]\n" \
"ds_read_b128 v[176:179], v240 offset:26624\n" \
"ds_read_b64_tr_b16 v[184:185], v236 offset:12288\n" \
"ds_read_b64_tr_b16 v[188:189], v237 offset:12288\n" \
"v_mfma_f32_16x16x32_f16 v[12:15], v[124:127], v[160:163], v[12:15]\n" \
"v_pk_add_f16 v160, v160, v164\n" \
"v_pk_add_f16 v161, v161, v165\n" \
"v_mfma_f32_16x16x32_f16 v[4:7], v[144:147], v[164:167], v[4:7]\n" \
"v_pk_add_f16 v162, v162, v166\n" \
"v_pk_add_f16 v163, v163, v167\n" \
"buffer_store_dwordx4 v[212:215], v235, s[36:39], s64 offen sc1\n" \
"v_mfma_f32_16x16x32_f16 v[16:19], v[148:151], v[164:167], v[16:19]\n" \
"ds_read_b128 v[180:183], v240 offset:27136\n" \
"s_add_i32 m0, s44, 0x0\n" \
"s_add_i32 s80, s46, 0x2000000\n" \
"v_mfma_f32_16x16x32_f16 v[8:11], v[128:131], v[160:163], v[8:11]\n" \
"buffer_load_dwordx4 v232, s[36:39], s80 offen sc1 lds\n" \
"s_waitcnt lgkmcnt(2)\n" \
"v_mfma_f32_16x16x16_bf16 v[184:187], v[184:185], v[192:193], 0\n" \
"v_mfma_f32_16x16x32_f16 v[20:23], v[132:135], v[160:163], v[20:23]\n" \
"ds_read_b64_tr_b16 v[136:137], v241 offset:16384\n" \
"ds_read_b64_tr_b16 v[138:139], v241 offset:24576\n" \
"s_waitcnt lgkmcnt(3)\n" \
"v_mfma_f32_16x16x16_bf16 v[188:191], v[188:189], v[192:193], 0\n" \
"v_mfma_f32_16x16x32_f16 v[24:27], v[120:123], v[168:171], v[24:27]\n" \
"ds_read_b128 v[160:163], v240 offset:28672\n" \
"v_mfma_f32_16x16x32_f16 v[36:39], v[124:127], v[168:171], v[36:39]\n" \
"v_pk_add_f16 v168, v168, v172\n" \
"v_pk_add_f16 v169, v169, v173\n" \
"ds_read_b64_tr_b16 v[140:141], v242 offset:16384\n" \
"v_mfma_f32_16x16x32_f16 v[28:31], v[144:147], v[172:175], v[28:31]\n" \
"v_pk_add_f16 v170, v170, v174\n" \
"v_pk_add_f16 v171, v171, v175\n" \
"v_cvt_pk_f16_f32 v184, v184, v185\n" \
"v_mfma_f32_16x16x32_f16 v[40:43], v[148:151], v[172:175], v[40:43]\n" \
"v_cvt_pk_f16_f32 v185, v186, v187\n" \
"v_cvt_pk_f16_f32 v186, v188, v189\n" \
"ds_read_b128 v[164:167], v240 offset:29184\n" \
"v_mfma_f32_16x16x32_f16 v[32:35], v[128:131], v[168:171], v[32:35]\n" \
"v_cvt_pk_f16_f32 v187, v190, v191\n" \
"v_add_u32 v184, s91, v184\n" \
"buffer_load_dwordx4 v232, s[36:39], s80 offen offset:1024 sc1 lds\n" \
"v_mfma_f32_16x16x32_f16 v[44:47], v[132:135], v[168:171], v[44:47]\n" \
"v_add_u32 v185, s91, v185\n" \
"v_add_u32 v186, s91, v186\n" \
"ds_read_b64_tr_b16 v[142:143], v242 offset:24576\n" \
"v_mfma_f32_16x16x32_f16 v[48:51], v[120:123], v[176:179], v[48:51]\n" \
"v_add_u32 v187, s91, v187\n" \
"v_and_b32 v184, s92, v184\n" \
"ds_read_b128 v[168:171], v240 offset:20480\n" \
"v_mfma_f32_16x16x32_f16 v[60:63], v[124:127], v[176:179], v[60:63]\n" \
"s_waitcnt lgkmcnt(4)\n" \
"v_pk_add_f16 v176, v176, v180\n" \
"v_pk_add_f16 v177, v177, v181\n" \
"v_and_b32 v185, s92, v185\n" \
"s_add_i32 s82, s48, 0x4000000\n" \
"v_mfma_f32_16x16x32_f16 v[52:55], v[144:147], v[180:183], v[52:55]\n" \
"v_pk_add_f16 v178, v178, v182\n" \
"v_pk_add_f16 v179, v179, v183\n" \
"v_and_b32 v186, s92, v186\n" \
"v_and_b32 v187, s92, v187\n" \
"v_mfma_f32_16x16x32_f16 v[64:67], v[148:151], v[180:183], v[64:67]\n" \
"ds_read_b128 v[172:175], v240 offset:20992\n" \
"buffer_store_dwordx4 v[184:187], v234, s[36:39], s82 offen sc1\n" \
"ds_read_b64_tr_b16 v[152:153], v241 offset:16896\n" \
"v_mfma_f32_16x16x32_f16 v[56:59], v[128:131], v[176:179], v[56:59]\n" \
"buffer_load_dwordx4 v232, s[36:39], s80 offen offset:2048 sc1 lds\n" \
"v_mfma_f32_16x16x32_f16 v[68:71], v[132:135], v[176:179], v[68:71]\n" \
"s_setprio 0\n" \
"ds_read_b64_tr_b16 v[154:155], v241 offset:25088\n" \
"ds_read_b64_tr_b16 v[156:157], v242 offset:16896\n" \
"v_mfma_f32_16x16x32_f16 v[72:75], v[120:123], v[160:163], v[72:75]\n" \
"ds_read_b128 v[176:179], v240 offset:22528\n" \
"ds_read_b64_tr_b16 v[158:159], v242 offset:25088\n" \
"v_mfma_f32_16x16x32_f16 v[84:87], v[124:127], v[160:163], v[84:87]\n" \
"s_waitcnt lgkmcnt(6)\n" \
"v_pk_add_f16 v160, v160, v164\n" \
"v_pk_add_f16 v161, v161, v165\n" \
"v_mfma_f32_16x16x32_f16 v[76:79], v[144:147], v[164:167], v[76:79]\n" \
"v_pk_add_f16 v162, v162, v166\n" \
"v_pk_add_f16 v163, v163, v167\n" \
"v_mfma_f32_16x16x32_f16 v[88:91], v[148:151], v[164:167], v[88:91]\n" \
"ds_read_b128 v[180:183], v240 offset:23040\n" \
"v_mfma_f32_16x16x32_f16 v[80:83], v[128:131], v[160:163], v[80:83]\n" \
"buffer_load_dwordx4 v232, s[36:39], s80 offen offset:3072 sc1 lds\n" \
"v_mfma_f32_16x16x32_f16 v[92:95], v[132:135], v[160:163], v[92:95]\n" \
"v_mfma_f32_16x16x32_f16 v[96:99], v[120:123], v[168:171], v[96:99]\n" \
"s_waitcnt lgkmcnt(5)\n" \
"v_pk_add_f16 v120, v136, v152\n" \
"v_pk_add_f16 v121, v137, v153\n" \
"ds_read_b128 v[160:163], v240 offset:24576\n" \
"v_mfma_f32_16x16x32_f16 v[108:111], v[124:127], v[168:171], v[108:111]\n" \
"v_pk_add_f16 v168, v168, v172\n" \
"v_pk_add_f16 v169, v169, v173\n" \
"ds_read_b128 v[164:167], v240 offset:25088\n" \
"v_mfma_f32_16x16x32_f16 v[100:103], v[144:147], v[172:175], v[100:103]\n" \
"v_pk_add_f16 v170, v170, v174\n" \
"v_pk_add_f16 v171, v171, v175\n" \
"s_add_i32 m0, s45, 0x0\n" \
"s_add_i32 s81, s47, 0x2000000\n" \
"v_mfma_f32_16x16x32_f16 v[112:115], v[148:151], v[172:175], v[112:115]\n" \
"s_waitcnt lgkmcnt(3)\n" \
"v_pk_add_f16 v122, v138, v154\n" \
"v_pk_add_f16 v123, v139, v155\n" \
"buffer_load_dwordx4 v232, s[36:39], s81 offen sc1 lds\n" \
"v_mfma_f32_16x16x32_f16 v[104:107], v[128:131], v[168:171], v[104:107]\n" \
"v_pk_add_f16 v124, v140, v156\n" \
"v_pk_add_f16 v125, v141, v157\n" \
"v_mfma_f32_16x16x32_f16 v[116:119], v[132:135], v[168:171], v[116:119]\n" \
"s_setprio 1\n" \
"v_pk_add_f16 v126, v142, v158\n" \
"v_pk_add_f16 v127, v143, v159\n" \
"s_waitcnt vmcnt(7) lgkmcnt(0)\n" \
"s_cmp_eq_u32 s94, 0\n" \
"s_cbranch_scc1 .Lp7_%=\n" \
".Lq7_%=:\n" \
"s_memrealtime s[86:87]\n" \
"s_waitcnt lgkmcnt(0)\n" \
"s_sub_i32 s87, s86, s75\n" \
"s_cmp_lt_i32 s87, 0\n" \
"s_cbranch_scc1 .Lq7_%=\n" \
"s_lshr_b64 s[94:95], s[94:95], 8\n" \
"s_and_b32 s87, s94, 0xff\n" \
"s_add_i32 s75, s86, s87\n" \
".Lp7_%=:\n" \
"s_barrier\n" \
"v_mfma_f32_16x16x32_f16 v[0:3], v[136:139], v[176:179], v[0:3]\n" \
"ds_read_b128 v[168:171], v240 offset:47104\n" \
"ds_read_b64_tr_b16 v[184:185], v238 offset:12288\n" \
"ds_read_b64_tr_b16 v[188:189], v239 offset:12288\n" \
"v_mfma_f32_16x16x32_f16 v[12:15], v[140:143], v[176:179], v[12:15]\n" \
"v_pk_add_f16 v176, v176, v180\n" \
"v_pk_add_f16 v177, v177, v181\n" \
"v_mfma_f32_16x16x32_f16 v[4:7], v[152:155], v[180:183], v[4:7]\n" \
"v_pk_add_f16 v178, v178, v182\n" \
"v_pk_add_f16 v179, v179, v183\n" \
"v_mfma_f32_16x16x32_f16 v[16:19], v[156:159], v[180:183], v[16:19]\n" \
"ds_read_b128 v[172:175], v240 offset:47616\n" \
"s_add_i32 m0, s44, 0x5000\n" \
"s_add_i32 s80, s46, 0x2400000\n" \
"v_mfma_f32_16x16x32_f16 v[8:11], v[120:123], v[176:179], v[8:11]\n" \
"buffer_load_dwordx4 v232, s[36:39], s80 offen sc1 lds\n" \
"s_waitcnt lgkmcnt(2)\n" \
"v_mfma_f32_16x16x16_bf16 v[184:187], v[184:185], v[192:193], 0\n" \
"v_mfma_f32_16x16x32_f16 v[20:23], v[124:127], v[176:179], v[20:23]\n" \
"ds_read_b64_tr_b16 v[128:129], v241 offset:32768\n" \
"ds_read_b64_tr_b16 v[130:131], v241 offset:40960\n" \
"s_waitcnt lgkmcnt(3)\n" \
"v_mfma_f32_16x16x16_bf16 v[188:191], v[188:189], v[192:193], 0\n" \
"v_mfma_f32_16x16x32_f16 v[24:27], v[136:139], v[160:163], v[24:27]\n" \
"ds_read_b128 v[176:179], v240 offset:49152\n" \
"v_mfma_f32_16x16x32_f16 v[36:39], v[140:143], v[160:163], v[36:39]\n" \
"v_pk_add_f16 v160, v160, v164\n" \
"v_pk_add_f16 v161, v161, v165\n" \
"ds_read_b64_tr_b16 v[132:133], v242 offset:32768\n" \
"v_mfma_f32_16x16x32_f16 v[28:31], v[152:155], v[164:167], v[28:31]\n" \
"v_pk_add_f16 v162, v162, v166\n" \
"v_pk_add_f16 v163, v163, v167\n" \
"v_cvt_pk_f16_f32 v184, v184, v185\n" \
"v_mfma_f32_16x16x32_f16 v[40:43], v[156:159], v[164:167], v[40:43]\n" \
"v_cvt_pk_f16_f32 v185, v186, v187\n" \
"v_cvt_pk_f16_f32 v186, v188, v189\n" \
"ds_read_b128 v[180:183], v240 offset:49664\n" \
"v_mfma_f32_16x16x32_f16 v[32:35], v[120:123], v[160:163], v[32:35]\n" \
"v_cvt_pk_f16_f32 v187, v190, v191\n" \
"v_add_u32 v184, s91, v184\n" \
"buffer_load_dwordx4 v232, s[36:39], s80 offen offset:1024 sc1 lds\n" \
"v_mfma_f32_16x16x32_f16 v[44:47], v[124:127], v[160:163], v[44:47]\n" \
"v_add_u32 v185, s91, v185\n" \
"v_add_u32 v186, s91, v186\n" \
"ds_read_b64_tr_b16 v[134:135], v242 offset:40960\n" \
"v_mfma_f32_16x16x32_f16 v[48:51], v[136:139], v[168:171], v[48:51]\n" \
"v_add_u32 v187, s91, v187\n" \
"v_and_b32 v184, s92, v184\n" \
"ds_read_b128 v[160:163], v240 offset:40960\n" \
"v_mfma_f32_16x16x32_f16 v[60:63], v[140:143], v[168:171], v[60:63]\n" \
"s_waitcnt lgkmcnt(4)\n" \
"v_pk_add_f16 v168, v168, v172\n" \
"v_pk_add_f16 v169, v169, v173\n" \
"v_and_b32 v185, s92, v185\n" \
"v_mfma_f32_16x16x32_f16 v[52:55], v[152:155], v[172:175], v[52:55]\n" \
"v_pk_add_f16 v170, v170, v174\n" \
"v_pk_add_f16 v171, v171, v175\n" \
"v_and_b32 v186, s92, v186\n" \
"v_and_b32 v187, s92, v187\n" \
"v_mfma_f32_16x16x32_f16 v[64:67], v[156:159], v[172:175], v[64:67]\n" \
"ds_read_b128 v[164:167], v240 offset:41472\n" \
"buffer_store_dwordx4 v[184:187], v234, s[36:39], s82 offen offset:1024 sc1\n" \
"ds_read_b64_tr_b16 v[144:145], v241 offset:33280\n" \
"v_mfma_f32_16x16x32_f16 v[56:59], v[120:123], v[168:171], v[56:59]\n" \
"buffer_load_dwordx4 v232, s[36:39], s80 offen offset:2048 sc1 lds\n" \
"v_mfma_f32_16x16x32_f16 v[68:71], v[124:127], v[168:171], v[68:71]\n" \
"s_setprio 0\n" \
"ds_read_b64_tr_b16 v[146:147], v241 offset:41472\n" \
"ds_read_b64_tr_b16 v[148:149], v242 offset:33280\n" \
"v_mfma_f32_16x16x32_f16 v[72:75], v[136:139], v[176:179], v[72:75]\n" \
"ds_read_b128 v[168:171], v240 offset:43008\n" \
"ds_read_b64_tr_b16 v[150:151], v242 offset:41472\n" \
"v_mfma_f32_16x16x32_f16 v[84:87], v[140:143], v[176:179], v[84:87]\n" \
"s_waitcnt lgkmcnt(6)\n" \
"v_pk_add_f16 v176, v176, v180\n" \
"v_pk_add_f16 v177, v177, v181\n" \
"v_mfma_f32_16x16x32_f16 v[76:79], v[152:155], v[180:183], v[76:79]\n" \
"v_pk_add_f16 v178, v178, v182\n" \
"v_pk_add_f16 v179, v179, v183\n" \
"v_mfma_f32_16x16x32_f16 v[88:91], v[156:159], v[180:183], v[88:91]\n" \
"ds_read_b128 v[172:175], v240 offset:43520\n" \
"v_mfma_f32_16x16x32_f16 v[80:83], v[120:123], v[176:179], v[80:83]\n" \
"buffer_load_dwordx4 v232, s[36:39], s80 offen offset:3072 sc1 lds\n" \
"v_mfma_f32_16x16x32_f16 v[92:95], v[124:127], v[176:179], v[92:95]\n" \
"v_mfma_f32_16x16x32_f16 v[96:99], v[136:139], v[160:163], v[96:99]\n" \
"s_waitcnt lgkmcnt(5)\n" \
"v_pk_add_f16 v136, v128, v144\n" \
"v_pk_add_f16 v137, v129, v145\n" \
"ds_read_b128 v[176:179], v240 offset:45056\n" \
"v_mfma_f32_16x16x32_f16 v[108:111], v[140:143], v[160:163], v[108:111]\n" \
"v_pk_add_f16 v160, v160, v164\n" \
"v_pk_add_f16 v161, v161, v165\n" \
"ds_read_b128 v[180:183], v240 offset:45568\n" \
"v_mfma_f32_16x16x32_f16 v[100:103], v[152:155], v[164:167], v[100:103]\n" \
"v_pk_add_f16 v162, v162, v166\n" \
"v_pk_add_f16 v163, v163, v167\n" \
"s_add_i32 m0, s45, 0x5000\n" \
"s_add_i32 s81, s47, 0x2400000\n" \
"v_mfma_f32_16x16x32_f16 v[112:115], v[156:159], v[164:167], v[112:115]\n" \
"s_waitcnt lgkmcnt(3)\n" \
"v_pk_add_f16 v138, v130, v146\n" \
"v_pk_add_f16 v139, v131, v147\n" \
"buffer_load_dwordx4 v232, s[36:39], s81 offen sc1 lds\n" \
"v_mfma_f32_16x16x32_f16 v[104:107], v[120:123], v[160:163], v[104:107]\n" \
"v_pk_add_f16 v140, v132, v148\n" \
"v_pk_add_f16 v141, v133, v149\n" \
"v_mfma_f32_16x16x32_f16 v[116:119], v[124:127], v[160:163], v[116:119]\n" \
"s_setprio 1\n" \
"v_pk_add_f16 v142, v134, v150\n" \
"v_pk_add_f16 v143, v135, v151\n" \
"s_waitcnt vmcnt(6) lgkmcnt(0)\n" \
"s_barrier\n" \
"v_mfma_f32_16x16x32_f16 v[0:3], v[128:131], v[168:171], v[0:3]\n" \
"ds_read_b128 v[160:163], v240 offset:6144\n" \
"ds_read_b64_tr_b16 v[184:185], v236 offset:16384\n" \
"ds_read_b64_tr_b16 v[188:189], v237 offset:16384\n" \
"v_mfma_f32_16x16x32_f16 v[12:15], v[132:135], v[168:171], v[12:15]\n" \
"v_pk_add_f16 v168, v168, v172\n" \
"v_pk_add_f16 v169, v169, v173\n" \
"v_mfma_f32_16x16x32_f16 v[4:7], v[144:147], v[172:175], v[4:7]\n" \
"v_pk_add_f16 v170, v170, v174\n" \
"v_pk_add_f16 v171, v171, v175\n" \
"buffer_store_dwordx4 v[216:219], v235, s[36:39], s65 offen sc1\n" \
"v_mfma_f32_16x16x32_f16 v[16:19], v[148:151], v[172:175], v[16:19]\n" \
"ds_read_b128 v[164:167], v240 offset:6656\n" \
"s_add_i32 m0, s44, 0xa000\n" \
"s_add_i32 s80, s46, 0x2800000\n" \
"v_mfma_f32_16x16x32_f16 v[8:11], v[136:139], v[168:171], v[8:11]\n" \
"buffer_load_dwordx4 v232, s[36:39], s80 offen sc1 lds\n" \
"s_waitcnt lgkmcnt(2)\n" \
"v_mfma_f32_16x16x16_bf16 v[184:187], v[184:185], v[192:193], 0\n" \
"v_mfma_f32_16x16x32_f16 v[20:23], v[140:143], v[168:171], v[20:23]\n" \
"ds_read_b64_tr_b16 v[120:121], v241 offset:0\n" \
"ds_read_b64_tr_b16 v[122:123], v241 offset:8192\n" \
"s_waitcnt lgkmcnt(3)\n" \
"v_mfma_f32_16x16x16_bf16 v[188:191], v[188:189], v[192:193], 0\n" \
"v_mfma_f32_16x16x32_f16 v[24:27], v[128:131], v[176:179], v[24:27]\n" \
"ds_read_b128 v[168:171], v240 offset:8192\n" \
"v_mfma_f32_16x16x32_f16 v[36:39], v[132:135], v[176:179], v[36:39]\n" \
"v_pk_add_f16 v176, v176, v180\n" \
"v_pk_add_f16 v177, v177, v181\n" \
"ds_read_b64_tr_b16 v[124:125], v242 offset:0\n" \
"v_mfma_f32_16x16x32_f16 v[28:31], v[144:147], v[180:183], v[28:31]\n" \
"v_pk_add_f16 v178, v178, v182\n" \
"v_pk_add_f16 v179, v179, v183\n" \
"v_cvt_pk_f16_f32 v184, v184, v185\n" \
"v_mfma_f32_16x16x32_f16 v[40:43], v[148:151], v[180:183], v[40:43]\n" \
"v_cvt_pk_f16_f32 v185, v186, v187\n" \
"v_cvt_pk_f16_f32 v186, v188, v189\n" \
"ds_read_b128 v[172:175], v240 offset:8704\n" \
"v_mfma_f32_16x16x32_f16 v[32:35], v[136:139], v[176:179], v[32:35]\n" \
"v_cvt_pk_f16_f32 v187, v190, v191\n" \
"v_add_u32 v184, s91, v184\n" \
"buffer_load_dwordx4 v232, s[36:39], s80 offen offset:1024 sc1 lds\n" \
"v_mfma_f32_16x16x32_f16 v[44:47], v[140:143], v[176:179], v[44:47]\n" \
"v_add_u32 v185, s91, v185\n" \
"v_add_u32 v186, s91, v186\n" \
"ds_read_b64_tr_b16 v[126:127], v242 offset:8192\n" \
"v_mfma_f32_16x16x32_f16 v[48:51], v[128:131], v[160:163], v[48:51]\n" \
"v_add_u32 v187, s91, v187\n" \
"v_and_b32 v184, s92, v184\n" \
"ds_read_b128 v[176:179], v240 offset:0\n" \
"v_mfma_f32_16x16x32_f16 v[60:63], v[132:135], v[160:163], v[60:63]\n" \
"s_waitcnt lgkmcnt(4)\n" \
"v_pk_add_f16 v160, v160, v164\n" \
"v_pk_add_f16 v161, v161, v165\n" \
"v_and_b32 v185, s92, v185\n" \
"s_add_i32 s82, s49, 0x4800000\n" \
"v_mfma_f32_16x16x32_f16 v[52:55], v[144:147], v[164:167], v[52:55]\n" \
"v_pk_add_f16 v162, v162, v166\n" \
"v_pk_add_f16 v163, v163, v167\n" \
"v_and_b32 v186, s92, v186\n" \
"v_and_b32 v187, s92, v187\n" \
"v_mfma_f32_16x16x32_f16 v[64:67], v[148:151], v[164:167], v[64:67]\n" \
"ds_read_b128 v[180:183], v240 offset:512\n" \
"buffer_store_dwordx4 v[184:187], v234, s[36:39], s82 offen sc1\n" \
"ds_read_b64_tr_b16 v[152:153], v241 offset:512\n" \
"v_mfma_f32_16x16x32_f16 v[56:59], v[136:139], v[160:163], v[56:59]\n" \
"buffer_load_dwordx4 v232, s[36:39], s80 offen offset:2048 sc1 lds\n" \
"v_mfma_f32_16x16x32_f16 v[68:71], v[140:143], v[160:163], v[68:71]\n" \
"s_setprio 0\n" \
"ds_read_b64_tr_b16 v[154:155], v241 offset:8704\n" \
"ds_read_b64_tr_b16 v[156:157], v242 offset:512\n" \
"v_mfma_f32_16x16x32_f16 v[72:75], v[128:131], v[168:171], v[72:75]\n" \
"ds_read_b128 v[160:163], v240 offset:2048\n" \
"ds_read_b64_tr_b16 v[158:159], v242 offset:8704\n" \
"v_mfma_f32_16x16x32_f16 v[84:87], v[132:135], v[168:171], v[84:87]\n" \
"s_waitcnt lgkmcnt(6)\n" \
"v_pk_add_f16 v168, v168, v172\n" \
"v_pk_add_f16 v169, v169, v173\n" \
"v_mfma_f32_16x16x32_f16 v[76:79], v[144:147], v[172:175], v[76:79]\n" \
"v_pk_add_f16 v170, v170, v174\n" \
"v_pk_add_f16 v171, v171, v175\n" \
"v_mfma_f32_16x16x32_f16 v[88:91], v[148:151], v[172:175], v[88:91]\n" \
"ds_read_b128 v[164:167], v240 offset:2560\n" \
"v_mfma_f32_16x16x32_f16 v[80:83], v[136:139], v[168:171], v[80:83]\n" \
"buffer_load_dwordx4 v232, s[36:39], s80 offen offset:3072 sc1 lds\n" \
"v_mfma_f32_16x16x32_f16 v[92:95], v[140:143], v[168:171], v[92:95]\n" \
"v_mfma_f32_16x16x32_f16 v[96:99], v[128:131], v[176:179], v[96:99]\n" \
"s_waitcnt lgkmcnt(5)\n" \
"v_pk_add_f16 v128, v120, v152\n" \
"v_pk_add_f16 v129, v121, v153\n" \
"ds_read_b128 v[168:171], v240 offset:4096\n" \
"v_mfma_f32_16x16x32_f16 v[108:111], v[132:135], v[176:179], v[108:111]\n" \
"v_pk_add_f16 v176, v176, v180\n" \
"v_pk_add_f16 v177, v177, v181\n" \
"ds_read_b128 v[172:175], v240 offset:4608\n" \
"v_mfma_f32_16x16x32_f16 v[100:103], v[144:147], v[180:183], v[100:103]\n" \
"v_pk_add_f16 v178, v178, v182\n" \
"v_pk_add_f16 v179, v179, v183\n" \
"s_add_i32 m0, s45, 0xa000\n" \
"s_add_i32 s81, s47, 0x2800000\n" \
"v_mfma_f32_16x16x32_f16 v[112:115], v[148:151], v[180:183], v[112:115]\n" \
"s_waitcnt lgkmcnt(3)\n" \
"v_pk_add_f16 v130, v122, v154\n" \
"v_pk_add_f16 v131, v123, v155\n" \
"buffer_load_dwordx4 v232, s[36:39], s81 offen sc1 lds\n" \
"v_mfma_f32_16x16x32_f16 v[104:107], v[136:139], v[176:179], v[104:107]\n" \
"v_pk_add_f16 v132, v124, v156\n" \
"v_pk_add_f16 v133, v125, v157\n" \
"v_mfma_f32_16x16x32_f16 v[116:119], v[140:143], v[176:179], v[116:119]\n" \
"s_setprio 1\n" \
"v_pk_add_f16 v134, v126, v158\n" \
"v_pk_add_f16 v135, v127, v159\n" \
"s_waitcnt vmcnt(7) lgkmcnt(0)\n" \
"s_barrier\n" \
"v_mfma_f32_16x16x32_f16 v[0:3], v[120:123], v[160:163], v[0:3]\n" \
"ds_read_b128 v[176:179], v240 offset:26624\n" \
"ds_read_b64_tr_b16 v[184:185], v238 offset:16384\n" \
"ds_read_b64_tr_b16 v[188:189], v239 offset:16384\n" \
"v_mfma_f32_16x16x32_f16 v[12:15], v[124:127], v[160:163], v[12:15]\n" \
"v_pk_add_f16 v160, v160, v164\n" \
"v_pk_add_f16 v161, v161, v165\n" \
"v_mfma_f32_16x16x32_f16 v[4:7], v[152:155], v[164:167], v[4:7]\n" \
"v_pk_add_f16 v162, v162, v166\n" \
"v_pk_add_f16 v163, v163, v167\n" \
"v_mfma_f32_16x16x32_f16 v[16:19], v[156:159], v[164:167], v[16:19]\n" \
"ds_read_b128 v[180:183], v240 offset:27136\n" \
"s_add_i32 m0, s44, 0x0\n" \
"s_add_i32 s80, s46, 0x2c00000\n" \
"v_mfma_f32_16x16x32_f16 v[8:11], v[128:131], v[160:163], v[8:11]\n" \
"buffer_load_dwordx4 v232, s[36:39], s80 offen sc1 lds\n" \
"s_waitcnt lgkmcnt(2)\n" \
"v_mfma_f32_16x16x16_bf16 v[184:187], v[184:185], v[192:193], 0\n" \
"v_mfma_f32_16x16x32_f16 v[20:23], v[132:135], v[160:163], v[20:23]\n" \
"ds_read_b64_tr_b16 v[136:137], v241 offset:16384\n" \
"ds_read_b64_tr_b16 v[138:139], v241 offset:24576\n" \
"s_waitcnt lgkmcnt(3)\n" \
"v_mfma_f32_16x16x16_bf16 v[188:191], v[188:189], v[192:193], 0\n" \
"v_mfma_f32_16x16x32_f16 v[24:27], v[120:123], v[168:171], v[24:27]\n" \
"ds_read_b128 v[160:163], v240 offset:28672\n" \
"v_mfma_f32_16x16x32_f16 v[36:39], v[124:127], v[168:171], v[36:39]\n" \
"v_pk_add_f16 v168, v168, v172\n" \
"v_pk_add_f16 v169, v169, v173\n" \
"ds_read_b64_tr_b16 v[140:141], v242 offset:16384\n" \
"v_mfma_f32_16x16x32_f16 v[28:31], v[152:155], v[172:175], v[28:31]\n" \
"v_pk_add_f16 v170, v170, v174\n" \
"v_pk_add_f16 v171, v171, v175\n" \
"v_cvt_pk_f16_f32 v184, v184, v185\n" \
"v_mfma_f32_16x16x32_f16 v[40:43], v[156:159], v[172:175], v[40:43]\n" \
"v_cvt_pk_f16_f32 v185, v186, v187\n" \
"v_cvt_pk_f16_f32 v186, v188, v189\n" \
"ds_read_b128 v[164:167], v240 offset:29184\n" \
"v_mfma_f32_16x16x32_f16 v[32:35], v[128:131], v[168:171], v[32:35]\n" \
"v_cvt_pk_f16_f32 v187, v190, v191\n" \
"v_add_u32 v184, s91, v184\n" \
"buffer_load_dwordx4 v232, s[36:39], s80 offen offset:1024 sc1 lds\n" \
"v_mfma_f32_16x16x32_f16 v[44:47], v[132:135], v[168:171], v[44:47]\n" \
"v_add_u32 v185, s91, v185\n" \
"v_add_u32 v186, s91, v186\n" \
"ds_read_b64_tr_b16 v[142:143], v242 offset:24576\n" \
"v_mfma_f32_16x16x32_f16 v[48:51], v[120:123], v[176:179], v[48:51]\n" \
"v_add_u32 v187, s91, v187\n" \
"v_and_b32 v184, s92, v184\n" \
"ds_read_b128 v[168:171], v240 offset:20480\n" \
"v_mfma_f32_16x16x32_f16 v[60:63], v[124:127], v[176:179], v[60:63]\n" \
"s_waitcnt lgkmcnt(4)\n" \
"v_pk_add_f16 v176, v176, v180\n" \
"v_pk_add_f16 v177, v177, v181\n" \
"v_and_b32 v185, s92, v185\n" \
"v_mfma_f32_16x16x32_f16 v[52:55], v[152:155], v[180:183], v[52:55]\n" \
"v_pk_add_f16 v178, v178, v182\n" \
"v_pk_add_f16 v179, v179, v183\n" \
"v_and_b32 v186, s92, v186\n" \
"v_and_b32 v187, s92, v187\n" \
"v_mfma_f32_16x16x32_f16 v[64:67], v[156:159], v[180:183], v[64:67]\n" \
"ds_read_b128 v[172:175], v240 offset:20992\n" \
"buffer_store_dwordx4 v[184:187], v234, s[36:39], s82 offen offset:1024 sc1\n" \
"ds_read_b64_tr_b16 v[144:145], v241 offset:16896\n" \
"v_mfma_f32_16x16x32_f16 v[56:59], v[128:131], v[176:179], v[56:59]\n" \
"buffer_load_dwordx4 v232, s[36:39], s80 offen offset:2048 sc1 lds\n" \
"v_mfma_f32_16x16x32_f16 v[68:71], v[132:135], v[176:179], v[68:71]\n" \
"s_setprio 0\n" \
"ds_read_b64_tr_b16 v[146:147], v241 offset:25088\n" \
"ds_read_b64_tr_b16 v[148:149], v242 offset:16896\n" \
"v_mfma_f32_16x16x32_f16 v[72:75], v[120:123], v[160:163], v[72:75]\n" \
"ds_read_b128 v[176:179], v240 offset:22528\n" \
"ds_read_b64_tr_b16 v[150:151], v242 offset:25088\n" \
"v_mfma_f32_16x16x32_f16 v[84:87], v[124:127], v[160:163], v[84:87]\n" \
"s_waitcnt lgkmcnt(6)\n" \
"v_pk_add_f16 v160, v160, v164\n" \
"v_pk_add_f16 v161, v161, v165\n" \
"v_mfma_f32_16x16x32_f16 v[76:79], v[152:155], v[164:167], v[76:79]\n" \
"v_pk_add_f16 v162, v162, v166\n" \
"v_pk_add_f16 v163, v163, v167\n" \
"v_mfma_f32_16x16x32_f16 v[88:91], v[156:159], v[164:167], v[88:91]\n" \
"ds_read_b128 v[180:183], v240 offset:23040\n" \
"v_mfma_f32_16x16x32_f16 v[80:83], v[128:131], v[160:163], v[80:83]\n" \
"buffer_load_dwordx4 v232, s[36:39], s80 offen offset:3072 sc1 lds\n" \
"v_mfma_f32_16x16x32_f16 v[92:95], v[132:135], v[160:163], v[92:95]\n" \
"v_mfma_f32_16x16x32_f16 v[96:99], v[120:123], v[168:171], v[96:99]\n" \
"s_waitcnt lgkmcnt(5)\n" \
"v_pk_add_f16 v120, v136, v144\n" \
"v_pk_add_f16 v121, v137, v145\n" \
"ds_read_b128 v[160:163], v240 offset:24576\n" \
"v_mfma_f32_16x16x32_f16 v[108:111], v[124:127], v[168:171], v[108:111]\n" \
"v_pk_add_f16 v168, v168, v172\n" \
"v_pk_add_f16 v169, v169, v173\n" \
"ds_read_b128 v[164:167], v240 offset:25088\n" \
"v_mfma_f32_16x16x32_f16 v[100:103], v[152:155], v[172:175], v[100:103]\n" \
"v_pk_add_f16 v170, v170, v174\n" \
"v_pk_add_f16 v171, v171, v175\n" \
"s_add_i32 m0, s45, 0x0\n" \
"s_add_i32 s81, s47, 0x2c00000\n" \
"v_mfma_f32_16x16x32_f16 v[112:115], v[156:159], v[172:175], v[112:115]\n" \
"s_waitcnt lgkmcnt(3)\n" \
"v_pk_add_f16 v122, v138, v146\n" \
"v_pk_add_f16 v123, v139, v147\n" \
"buffer_load_dwordx4 v232, s[36:39], s81 offen sc1 lds\n" \
"v_mfma_f32_16x16x32_f16 v[104:107], v[128:131], v[168:171], v[104:107]\n" \
"v_pk_add_f16 v124, v140, v148\n" \
"v_pk_add_f16 v125, v141, v149\n" \
"v_mfma_f32_16x16x32_f16 v[116:119], v[132:135], v[168:171], v[116:119]\n" \
"s_setprio 1\n" \
"v_pk_add_f16 v126, v142, v150\n" \
"v_pk_add_f16 v127, v143, v151\n" \
"s_waitcnt vmcnt(6) lgkmcnt(0)\n" \
"s_barrier\n" \
"v_mfma_f32_16x16x32_f16 v[0:3], v[136:139], v[176:179], v[0:3]\n" \
"ds_read_b128 v[168:171], v240 offset:47104\n" \
"ds_read_b64_tr_b16 v[184:185], v236 offset:20480\n" \
"ds_read_b64_tr_b16 v[188:189], v237 offset:20480\n" \
"v_mfma_f32_16x16x32_f16 v[12:15], v[140:143], v[176:179], v[12:15]\n" \
"v_pk_add_f16 v176, v176, v180\n" \
"v_pk_add_f16 v177, v177, v181\n" \
"v_mfma_f32_16x16x32_f16 v[4:7], v[144:147], v[180:183], v[4:7]\n" \
"v_pk_add_f16 v178, v178, v182\n" \
"v_pk_add_f16 v179, v179, v183\n" \
"buffer_store_dwordx4 v[220:223], v235, s[36:39], s66 offen sc1\n" \
"v_mfma_f32_16x16x32_f16 v[16:19], v[148:151], v[180:183], v[16:19]\n" \
"ds_read_b128 v[172:175], v240 offset:47616\n" \
"s_add_i32 m0, s44, 0x5000\n" \
"s_add_i32 s80, s46, 0x3000000\n" \
"v_mfma_f32_16x16x32_f16 v[8:11], v[120:123], v[176:179], v[8:11]\n" \
"buffer_load_dwordx4 v232, s[36:39], s80 offen sc1 lds\n" \
"s_waitcnt lgkmcnt(2)\n" \
"v_mfma_f32_16x16x16_bf16 v[184:187], v[184:185], v[192:193], 0\n" \
"v_mfma_f32_16x16x32_f16 v[20:23], v[124:127], v[176:179], v[20:23]\n" \
"ds_read_b64_tr_b16 v[128:129], v241 offset:32768\n" \
"ds_read_b64_tr_b16 v[130:131], v241 offset:40960\n" \
"s_waitcnt lgkmcnt(3)\n" \
"v_mfma_f32_16x16x16_bf16 v[188:191], v[188:189], v[192:193], 0\n" \
"v_mfma_f32_16x16x32_f16 v[24:27], v[136:139], v[160:163], v[24:27]\n" \
"ds_read_b128 v[176:179], v240 offset:49152\n" \
"v_mfma_f32_16x16x32_f16 v[36:39], v[140:143], v[160:163], v[36:39]\n" \
"v_pk_add_f16 v160, v160, v164\n" \
"v_pk_add_f16 v161, v161, v165\n" \
"ds_read_b64_tr_b16 v[132:133], v242 offset:32768\n" \
"v_mfma_f32_16x16x32_f16 v[28:31], v[144:147], v[164:167], v[28:31]\n" \
"v_pk_add_f16 v162, v162, v166\n" \
"v_pk_add_f16 v163, v163, v167\n" \
"v_cvt_pk_f16_f32 v184, v184, v185\n" \
"v_mfma_f32_16x16x32_f16 v[40:43], v[148:151], v[164:167], v[40:43]\n" \
"v_cvt_pk_f16_f32 v185, v186, v187\n" \
"v_cvt_pk_f16_f32 v186, v188, v189\n" \
"ds_read_b128 v[180:183], v240 offset:49664\n" \
"v_mfma_f32_16x16x32_f16 v[32:35], v[120:123], v[160:163], v[32:35]\n" \
"v_cvt_pk_f16_f32 v187, v190, v191\n" \
"v_add_u32 v184, s91, v184\n" \
"buffer_load_dwordx4 v232, s[36:39], s80 offen offset:1024 sc1 lds\n" \
"v_mfma_f32_16x16x32_f16 v[44:47], v[124:127], v[160:163], v[44:47]\n" \
"v_add_u32 v185, s91, v185\n" \
"v_add_u32 v186, s91, v186\n" \
"ds_read_b64_tr_b16 v[134:135], v242 offset:40960\n" \
"v_mfma_f32_16x16x32_f16 v[48:51], v[136:139], v[168:171], v[48:51]\n" \
"v_add_u32 v187, s91, v187\n" \
"v_and_b32 v184, s92, v184\n" \
"ds_read_b128 v[160:163], v240 offset:40960\n" \
"v_mfma_f32_16x16x32_f16 v[60:63], v[140:143], v[168:171], v[60:63]\n" \
"s_waitcnt lgkmcnt(4)\n" \
"v_pk_add_f16 v168, v168, v172\n" \
"v_pk_add_f16 v169, v169, v173\n" \
"v_and_b32 v185, s92, v185\n" \
"s_add_i32 s82, s50, 0x5000000\n" \
"v_mfma_f32_16x16x32_f16 v[52:55], v[144:147], v[172:175], v[52:55]\n" \
"v_pk_add_f16 v170, v170, v174\n" \
"v_pk_add_f16 v171, v171, v175\n" \
"v_and_b32 v186, s92, v186\n" \
"v_and_b32 v187, s92, v187\n" \
"v_mfma_f32_16x16x32_f16 v[64:67], v[148:151], v[172:175], v[64:67]\n" \
"ds_read_b128 v[164:167], v240 offset:41472\n" \
"buffer_store_dwordx4 v[184:187], v234, s[36:39], s82 offen sc1\n" \
"ds_read_b64_tr_b16 v[152:153], v241 offset:33280\n" \
"v_mfma_f32_16x16x32_f16 v[56:59], v[120:123], v[168:171], v[56:59]\n" \
"buffer_load_dwordx4 v232, s[36:39], s80 offen offset:2048 sc1 lds\n" \
"v_mfma_f32_16x16x32_f16 v[68:71], v[124:127], v[168:171], v[68:71]\n" \
"s_setprio 0\n" \
"ds_read_b64_tr_b16 v[154:155], v241 offset:41472\n" \
"ds_read_b64_tr_b16 v[156:157], v242 offset:33280\n" \
"v_mfma_f32_16x16x32_f16 v[72:75], v[136:139], v[176:179], v[72:75]\n" \
"ds_read_b128 v[168:171], v240 offset:43008\n" \
"ds_read_b64_tr_b16 v[158:159], v242 offset:41472\n" \
"v_mfma_f32_16x16x32_f16 v[84:87], v[140:143], v[176:179], v[84:87]\n" \
"s_waitcnt lgkmcnt(6)\n" \
"v_pk_add_f16 v176, v176, v180\n" \
"v_pk_add_f16 v177, v177, v181\n" \
"v_mfma_f32_16x16x32_f16 v[76:79], v[144:147], v[180:183], v[76:79]\n" \
"v_pk_add_f16 v178, v178, v182\n" \
"v_pk_add_f16 v179, v179, v183\n" \
"v_mfma_f32_16x16x32_f16 v[88:91], v[148:151], v[180:183], v[88:91]\n" \
"ds_read_b128 v[172:175], v240 offset:43520\n" \
"v_mfma_f32_16x16x32_f16 v[80:83], v[120:123], v[176:179], v[80:83]\n" \
"buffer_load_dwordx4 v232, s[36:39], s80 offen offset:3072 sc1 lds\n" \
"v_mfma_f32_16x16x32_f16 v[92:95], v[124:127], v[176:179], v[92:95]\n" \
"v_mfma_f32_16x16x32_f16 v[96:99], v[136:139], v[160:163], v[96:99]\n" \
"s_waitcnt lgkmcnt(5)\n" \
"v_pk_add_f16 v136, v128, v152\n" \
"v_pk_add_f16 v137, v129, v153\n" \
"ds_read_b128 v[176:179], v240 offset:45056\n" \
"v_mfma_f32_16x16x32_f16 v[108:111], v[140:143], v[160:163], v[108:111]\n" \
"v_pk_add_f16 v160, v160, v164\n" \
"v_pk_add_f16 v161, v161, v165\n" \
"ds_read_b128 v[180:183], v240 offset:45568\n" \
"v_mfma_f32_16x16x32_f16 v[100:103], v[144:147], v[164:167], v[100:103]\n" \
"v_pk_add_f16 v162, v162, v166\n" \
"v_pk_add_f16 v163, v163, v167\n" \
"s_add_i32 m0, s45, 0x5000\n" \
"s_add_i32 s81, s47, 0x3000000\n" \
"v_mfma_f32_16x16x32_f16 v[112:115], v[148:151], v[164:167], v[112:115]\n" \
"s_waitcnt lgkmcnt(3)\n" \
"v_pk_add_f16 v138, v130, v154\n" \
"v_pk_add_f16 v139, v131, v155\n" \
"buffer_load_dwordx4 v232, s[36:39], s81 offen sc1 lds\n" \
"v_mfma_f32_16x16x32_f16 v[104:107], v[120:123], v[160:163], v[104:107]\n" \
"v_pk_add_f16 v140, v132, v156\n" \
"v_pk_add_f16 v141, v133, v157\n" \
"v_mfma_f32_16x16x32_f16 v[116:119], v[124:127], v[160:163], v[116:119]\n" \
"s_setprio 1\n" \
"v_pk_add_f16 v142, v134, v158\n" \
"v_pk_add_f16 v143, v135, v159\n" \
"s_waitcnt vmcnt(7) lgkmcnt(0)\n" \
"s_barrier\n" \
"v_mfma_f32_16x16x32_f16 v[0:3], v[128:131], v[168:171], v[0:3]\n" \
"ds_read_b128 v[160:163], v240 offset:6144\n" \
"ds_read_b64_tr_b16 v[184:185], v238 offset:20480\n" \
"ds_read_b64_tr_b16 v[188:189], v239 offset:20480\n" \
"v_mfma_f32_16x16x32_f16 v[12:15], v[132:135], v[168:171], v[12:15]\n" \
"v_pk_add_f16 v168, v168, v172\n" \
"v_pk_add_f16 v169, v169, v173\n" \
"v_mfma_f32_16x16x32_f16 v[4:7], v[152:155], v[172:175], v[4:7]\n" \
"v_pk_add_f16 v170, v170, v174\n" \
"v_pk_add_f16 v171, v171, v175\n" \
"v_mfma_f32_16x16x32_f16 v[16:19], v[156:159], v[172:175], v[16:19]\n" \
"ds_read_b128 v[164:167], v240 offset:6656\n" \
"s_add_i32 m0, s44, 0xa000\n" \
"s_add_i32 s80, s46, 0x3400000\n" \
"v_mfma_f32_16x16x32_f16 v[8:11], v[136:139], v[168:171], v[8:11]\n" \
"buffer_load_dwordx4 v232, s[36:39], s80 offen sc1 lds\n" \
"s_waitcnt lgkmcnt(2)\n" \
"v_mfma_f32_16x16x16_bf16 v[184:187], v[184:185], v[192:193], 0\n" \
"v_mfma_f32_16x16x32_f16 v[20:23], v[140:143], v[168:171], v[20:23]\n" \
"ds_read_b64_tr_b16 v[120:121], v241 offset:0\n" \
"ds_read_b64_tr_b16 v[122:123], v241 offset:8192\n" \
"s_waitcnt lgkmcnt(3)\n" \
"v_mfma_f32_16x16x16_bf16 v[188:191], v[188:189], v[192:193], 0\n" \
"v_mfma_f32_16x16x32_f16 v[24:27], v[128:131], v[176:179], v[24:27]\n" \
"ds_read_b128 v[168:171], v240 offset:8192\n" \
"v_mfma_f32_16x16x32_f16 v[36:39], v[132:135], v[176:179], v[36:39]\n" \
"v_pk_add_f16 v176, v176, v180\n" \
"v_pk_add_f16 v177, v177, v181\n" \
"ds_read_b64_tr_b16 v[124:125], v242 offset:0\n" \
"v_mfma_f32_16x16x32_f16 v[28:31], v[152:155], v[180:183], v[28:31]\n" \
"v_pk_add_f16 v178, v178, v182\n" \
"v_pk_add_f16 v179, v179, v183\n" \
"v_cvt_pk_f16_f32 v184, v184, v185\n" \
"v_mfma_f32_16x16x32_f16 v[40:43], v[156:159], v[180:183], v[40:43]\n" \
"v_cvt_pk_f16_f32 v185, v186, v187\n" \
"v_cvt_pk_f16_f32 v186, v188, v189\n" \
"ds_read_b128 v[172:175], v240 offset:8704\n" \
"v_mfma_f32_16x16x32_f16 v[32:35], v[136:139], v[176:179], v[32:35]\n" \
"v_cvt_pk_f16_f32 v187, v190, v191\n" \
"v_add_u32 v184, s91, v184\n" \
"buffer_load_dwordx4 v232, s[36:39], s80 offen offset:1024 sc1 lds\n" \
"v_mfma_f32_16x16x32_f16 v[44:47], v[140:143], v[176:179], v[44:47]\n" \
"v_add_u32 v185, s91, v185\n" \
"v_add_u32 v186, s91, v186\n" \
"ds_read_b64_tr_b16 v[126:127], v242 offset:8192\n" \
"v_mfma_f32_16x16x32_f16 v[48:51], v[128:131], v[160:163], v[48:51]\n" \
"v_add_u32 v187, s91, v187\n" \
"v_and_b32 v184, s92, v184\n" \
"ds_read_b128 v[176:179], v240 offset:0\n" \
"v_mfma_f32_16x16x32_f16 v[60:63], v[132:135], v[160:163], v[60:63]\n" \
"s_waitcnt lgkmcnt(4)\n" \
"v_pk_add_f16 v160, v160, v164\n" \
"v_pk_add_f16 v161, v161, v165\n" \
"v_and_b32 v185, s92, v185\n" \
"v_mfma_f32_16x16x32_f16 v[52:55], v[152:155], v[164:167], v[52:55]\n" \
"v_pk_add_f16 v162, v162, v166\n" \
"v_pk_add_f16 v163, v163, v167\n" \
"v_and_b32 v186, s92, v186\n" \
"v_and_b32 v187, s92, v187\n" \
"v_mfma_f32_16x16x32_f16 v[64:67], v[156:159], v[164:167], v[64:67]\n" \
"ds_read_b128 v[180:183], v240 offset:512\n" \
"buffer_store_dwordx4 v[184:187], v234, s[36:39], s82 offen offset:1024 sc1\n" \
"ds_read_b64_tr_b16 v[144:145], v241 offset:512\n" \
"v_mfma_f32_16x16x32_f16 v[56:59], v[136:139], v[160:163], v[56:59]\n" \
"buffer_load_dwordx4 v232, s[36:39], s80 offen offset:2048 sc1 lds\n" \
"v_mfma_f32_16x16x32_f16 v[68:71], v[140:143], v[160:163], v[68:71]\n" \
"s_setprio 0\n" \
"ds_read_b64_tr_b16 v[146:147], v241 offset:8704\n" \
"ds_read_b64_tr_b16 v[148:149], v242 offset:512\n" \
"v_mfma_f32_16x16x32_f16 v[72:75], v[128:131], v[168:171], v[72:75]\n" \
"ds_read_b128 v[160:163], v240 offset:2048\n" \
"ds_read_b64_tr_b16 v[150:151], v242 offset:8704\n" \
"v_mfma_f32_16x16x32_f16 v[84:87], v[132:135], v[168:171], v[84:87]\n" \
"s_waitcnt lgkmcnt(6)\n" \
"v_pk_add_f16 v168, v168, v172\n" \
"v_pk_add_f16 v169, v169, v173\n" \
"v_mfma_f32_16x16x32_f16 v[76:79], v[152:155], v[172:175], v[76:79]\n" \
"v_pk_add_f16 v170, v170, v174\n" \
"v_pk_add_f16 v171, v171, v175\n" \
"v_mfma_f32_16x16x32_f16 v[88:91], v[156:159], v[172:175], v[88:91]\n" \
"ds_read_b128 v[164:167], v240 offset:2560\n" \
"v_mfma_f32_16x16x32_f16 v[80:83], v[136:139], v[168:171], v[80:83]\n" \
"buffer_load_dwordx4 v232, s[36:39], s80 offen offset:3072 sc1 lds\n" \
"v_mfma_f32_16x16x32_f16 v[92:95], v[140:143], v[168:171], v[92:95]\n" \
"v_mfma_f32_16x16x32_f16 v[96:99], v[128:131], v[176:179], v[96:99]\n" \
"s_waitcnt lgkmcnt(5)\n" \
"v_pk_add_f16 v128, v120, v144\n" \
"v_pk_add_f16 v129, v121, v145\n" \
"ds_read_b128 v[168:171], v240 offset:4096\n" \
"v_mfma_f32_16x16x32_f16 v[108:111], v[132:135], v[176:179], v[108:111]\n" \
"v_pk_add_f16 v176, v176, v180\n" \
"v_pk_add_f16 v177, v177, v181\n" \
"ds_read_b128 v[172:175], v240 offset:4608\n" \
"v_mfma_f32_16x16x32_f16 v[100:103], v[152:155], v[180:183], v[100:103]\n" \
"v_pk_add_f16 v178, v178, v182\n" \
"v_pk_add_f16 v179, v179, v183\n" \
"s_add_i32 m0, s45, 0xa000\n" \
"s_add_i32 s81, s47, 0x3400000\n" \
"v_mfma_f32_16x16x32_f16 v[112:115], v[156:159], v[180:183], v[112:115]\n" \
"s_waitcnt lgkmcnt(3)\n" \
"v_pk_add_f16 v130, v122, v146\n" \
"v_pk_add_f16 v131, v123, v147\n" \
"buffer_load_dwordx4 v232, s[36:39], s81 offen sc1 lds\n" \
"v_mfma_f32_16x16x32_f16 v[104:107], v[136:139], v[176:179], v[104:107]\n" \
"v_pk_add_f16 v132, v124, v148\n" \
"v_pk_add_f16 v133, v125, v149\n" \
"v_mfma_f32_16x16x32_f16 v[116:119], v[140:143], v[176:179], v[116:119]\n" \
"s_setprio 1\n" \
"v_pk_add_f16 v134, v126, v150\n" \
"v_pk_add_f16 v135, v127, v151\n" \
"s_waitcnt vmcnt(6) lgkmcnt(0)\n" \
"s_barrier\n" \
"v_mfma_f32_16x16x32_f16 v[0:3], v[120:123], v[160:163], v[0:3]\n" \
"ds_read_b128 v[176:179], v240 offset:26624\n" \
"ds_read_b64_tr_b16 v[184:185], v236 offset:24576\n" \
"ds_read_b64_tr_b16 v[188:189], v237 offset:24576\n" \
"v_mfma_f32_16x16x32_f16 v[12:15], v[124:127], v[160:163], v[12:15]\n" \
"v_pk_add_f16 v160, v160, v164\n" \
"v_pk_add_f16 v161, v161, v165\n" \
"s_mov_b32 s84, 0x800c000\n" \
"v_mfma_f32_16x16x32_f16 v[4:7], v[144:147], v[164:167], v[4:7]\n" \
"v_pk_add_f16 v162, v162, v166\n" \
"v_pk_add_f16 v163, v163, v167\n" \
"buffer_load_dword v253, v252, s[36:39], s84 offen sc1\n" \
"buffer_store_dwordx4 v[224:227], v235, s[36:39], s67 offen sc1\n" \
"v_mfma_f32_16x16x32_f16 v[16:19], v[148:151], v[164:167], v[16:19]\n" \
"ds_read_b128 v[180:183], v240 offset:27136\n" \
"s_add_i32 m0, s44, 0x0\n" \
"s_add_i32 s80, s46, 0x3800000\n" \
"v_mfma_f32_16x16x32_f16 v[8:11], v[128:131], v[160:163], v[8:11]\n" \
"buffer_load_dwordx4 v232, s[36:39], s80 offen sc1 lds\n" \
"s_waitcnt lgkmcnt(2)\n" \
"v_mfma_f32_16x16x16_bf16 v[184:187], v[184:185], v[192:193], 0\n" \
"v_mfma_f32_16x16x32_f16 v[20:23], v[132:135], v[160:163], v[20:23]\n" \
"ds_read_b64_tr_b16 v[136:137], v241 offset:16384\n" \
"ds_read_b64_tr_b16 v[138:139], v241 offset:24576\n" \
"s_waitcnt lgkmcnt(3)\n" \
"v_mfma_f32_16x16x16_bf16 v[188:191], v[188:189], v[192:193], 0\n" \
"v_mfma_f32_16x16x32_f16 v[24:27], v[120:123], v[168:171], v[24:27]\n" \
"ds_read_b128 v[160:163], v240 offset:28672\n" \
"v_mfma_f32_16x16x32_f16 v[36:39], v[124:127], v[168:171], v[36:39]\n" \
"v_pk_add_f16 v168, v168, v172\n" \
"v_pk_add_f16 v169, v169, v173\n" \
"ds_read_b64_tr_b16 v[140:141], v242 offset:16384\n" \
"v_mfma_f32_16x16x32_f16 v[28:31], v[144:147], v[172:175], v[28:31]\n" \
"v_pk_add_f16 v170, v170, v174\n" \
"v_pk_add_f16 v171, v171, v175\n" \
"v_cvt_pk_f16_f32 v184, v184, v185\n" \
"v_mfma_f32_16x16x32_f16 v[40:43], v[148:151], v[172:175], v[40:43]\n" \
"v_cvt_pk_f16_f32 v185, v186, v187\n" \
"v_cvt_pk_f16_f32 v186, v188, v189\n" \
"ds_read_b128 v[164:167], v240 offset:29184\n" \
"v_mfma_f32_16x16x32_f16 v[32:35], v[128:131], v[168:171], v[32:35]\n" \
"v_cvt_pk_f16_f32 v187, v190, v191\n" \
"v_add_u32 v184, s91, v184\n" \
"buffer_load_dwordx4 v232, s[36:39], s80 offen offset:1024 sc1 lds\n" \
"v_mfma_f32_16x16x32_f16 v[44:47], v[132:135], v[168:171], v[44:47]\n" \
"v_add_u32 v185, s91, v185\n" \
"v_add_u32 v186, s91, v186\n" \
"ds_read_b64_tr_b16 v[142:143], v242 offset:24576\n" \
"v_mfma_f32_16x16x32_f16 v[48:51], v[120:123], v[176:179], v[48:51]\n" \
"v_add_u32 v187, s91, v187\n" \
"v_and_b32 v184, s92, v184\n" \
"ds_read_b128 v[168:171], v240 offset:20480\n" \
"v_mfma_f32_16x16x32_f16 v[60:63], v[124:127], v[176:179], v[60:63]\n" \
"s_waitcnt lgkmcnt(4)\n" \
"v_pk_add_f16 v176, v176, v180\n" \
"v_pk_add_f16 v177, v177, v181\n" \
"v_and_b32 v185, s92, v185\n" \
"s_add_i32 s82, s51, 0x5800000\n" \
"v_mfma_f32_16x16x32_f16 v[52:55], v[144:147], v[180:183], v[52:55]\n" \
"v_pk_add_f16 v178, v178, v182\n" \
"v_pk_add_f16 v179, v179, v183\n" \
"v_and_b32 v186, s92, v186\n" \
"v_and_b32 v187, s92, v187\n" \
"v_mfma_f32_16x16x32_f16 v[64:67], v[148:151], v[180:183], v[64:67]\n" \
"ds_read_b128 v[172:175], v240 offset:20992\n" \
"buffer_store_dwordx4 v[184:187], v234, s[36:39], s82 offen sc1\n" \
"ds_read_b64_tr_b16 v[152:153], v241 offset:16896\n" \
"v_mfma_f32_16x16x32_f16 v[56:59], v[128:131], v[176:179], v[56:59]\n" \
"buffer_load_dwordx4 v232, s[36:39], s80 offen offset:2048 sc1 lds\n" \
"v_mfma_f32_16x16x32_f16 v[68:71], v[132:135], v[176:179], v[68:71]\n" \
"s_setprio 0\n" \
"ds_read_b64_tr_b16 v[154:155], v241 offset:25088\n" \
"ds_read_b64_tr_b16 v[156:157], v242 offset:16896\n" \
"v_mfma_f32_16x16x32_f16 v[72:75], v[120:123], v[160:163], v[72:75]\n" \
"ds_read_b128 v[176:179], v240 offset:22528\n" \
"ds_read_b64_tr_b16 v[158:159], v242 offset:25088\n" \
"v_mfma_f32_16x16x32_f16 v[84:87], v[124:127], v[160:163], v[84:87]\n" \
"s_waitcnt lgkmcnt(6)\n" \
"v_pk_add_f16 v160, v160, v164\n" \
"v_pk_add_f16 v161, v161, v165\n" \
"v_mfma_f32_16x16x32_f16 v[76:79], v[144:147], v[164:167], v[76:79]\n" \
"v_pk_add_f16 v162, v162, v166\n" \
"v_pk_add_f16 v163, v163, v167\n" \
"v_mfma_f32_16x16x32_f16 v[88:91], v[148:151], v[164:167], v[88:91]\n" \
"ds_read_b128 v[180:183], v240 offset:23040\n" \
"v_mfma_f32_16x16x32_f16 v[80:83], v[128:131], v[160:163], v[80:83]\n" \
"buffer_load_dwordx4 v232, s[36:39], s80 offen offset:3072 sc1 lds\n" \
"v_mfma_f32_16x16x32_f16 v[92:95], v[132:135], v[160:163], v[92:95]\n" \
"v_mfma_f32_16x16x32_f16 v[96:99], v[120:123], v[168:171], v[96:99]\n" \
"s_waitcnt lgkmcnt(5)\n" \
"v_pk_add_f16 v120, v136, v152\n" \
"v_pk_add_f16 v121, v137, v153\n" \
"ds_read_b128 v[160:163], v240 offset:24576\n" \
"v_mfma_f32_16x16x32_f16 v[108:111], v[124:127], v[168:171], v[108:111]\n" \
"v_pk_add_f16 v168, v168, v172\n" \
"v_pk_add_f16 v169, v169, v173\n" \
"ds_read_b128 v[164:167], v240 offset:25088\n" \
"v_mfma_f32_16x16x32_f16 v[100:103], v[144:147], v[172:175], v[100:103]\n" \
"v_pk_add_f16 v170, v170, v174\n" \
"v_pk_add_f16 v171, v171, v175\n" \
"s_waitcnt vmcnt(6)\n" \
"v_cmp_eq_u32 vcc, 0, v253\n" \
"s_cbranch_vccnz .Ls16_%=\n" \
".Lr16_%=:\n" \
"s_add_i32 m0, s45, 0x0\n" \
"s_add_i32 s81, s47, 0x3800000\n" \
"v_mfma_f32_16x16x32_f16 v[112:115], v[148:151], v[172:175], v[112:115]\n" \
"s_waitcnt lgkmcnt(3)\n" \
"v_pk_add_f16 v122, v138, v154\n" \
"v_pk_add_f16 v123, v139, v155\n" \
"buffer_load_dwordx4 v232, s[36:39], s81 offen sc1 lds\n" \
"v_mfma_f32_16x16x32_f16 v[104:107], v[128:131], v[168:171], v[104:107]\n" \
"v_pk_add_f16 v124, v140, v156\n" \
"v_pk_add_f16 v125, v141, v157\n" \
"v_mfma_f32_16x16x32_f16 v[116:119], v[132:135], v[168:171], v[116:119]\n" \
"s_setprio 1\n" \
"v_pk_add_f16 v126, v142, v158\n" \
"v_pk_add_f16 v127, v143, v159\n" \
"s_waitcnt vmcnt(7) lgkmcnt(0)\n" \
"s_barrier\n" \
"v_mfma_f32_16x16x32_f16 v[0:3], v[136:139], v[176:179], v[0:3]\n" \
"ds_read_b128 v[168:171], v240 offset:47104\n" \
"ds_read_b64_tr_b16 v[184:185], v238 offset:24576\n" \
"ds_read_b64_tr_b16 v[188:189], v239 offset:24576\n" \
"v_mfma_f32_16x16x32_f16 v[12:15], v[140:143], v[176:179], v[12:15]\n" \
"v_pk_add_f16 v176, v176, v180\n" \
"v_pk_add_f16 v177, v177, v181\n" \
"v_mfma_f32_16x16x32_f16 v[4:7], v[152:155], v[180:183], v[4:7]\n" \
"v_pk_add_f16 v178, v178, v182\n" \
"v_pk_add_f16 v179, v179, v183\n" \
"v_mfma_f32_16x16x32_f16 v[16:19], v[156:159], v[180:183], v[16:19]\n" \
"ds_read_b128 v[172:175], v240 offset:47616\n" \
"s_add_i32 m0, s44, 0x5000\n" \
"s_add_i32 s80, s46, 0x3c00000\n" \
"v_mfma_f32_16x16x32_f16 v[8:11], v[120:123], v[176:179], v[8:11]\n" \
"buffer_load_dwordx4 v232, s[36:39], s80 offen sc1 lds\n" \
"s_waitcnt lgkmcnt(2)\n" \
"v_mfma_f32_16x16x16_bf16 v[184:187], v[184:185], v[192:193], 0\n" \
"v_mfma_f32_16x16x32_f16 v[20:23], v[124:127], v[176:179], v[20:23]\n" \
"ds_read_b64_tr_b16 v[128:129], v241 offset:32768\n" \
"ds_read_b64_tr_b16 v[130:131], v241 offset:40960\n" \
"s_waitcnt lgkmcnt(3)\n" \
"v_mfma_f32_16x16x16_bf16 v[188:191], v[188:189], v[192:193], 0\n" \
"v_mfma_f32_16x16x32_f16 v[24:27], v[136:139], v[160:163], v[24:27]\n" \
"ds_read_b128 v[176:179], v240 offset:49152\n" \
"v_mfma_f32_16x16x32_f16 v[36:39], v[140:143], v[160:163], v[36:39]\n" \
"v_pk_add_f16 v160, v160, v164\n" \
"v_pk_add_f16 v161, v161, v165\n" \
"ds_read_b64_tr_b16 v[132:133], v242 offset:32768\n" \
"v_mfma_f32_16x16x32_f16 v[28:31], v[152:155], v[164:167], v[28:31]\n" \
"v_pk_add_f16 v162, v162, v166\n" \
"v_pk_add_f16 v163, v163, v167\n" \
"v_cvt_pk_f16_f32 v184, v184, v185\n" \
"v_mfma_f32_16x16x32_f16 v[40:43], v[156:159], v[164:167], v[40:43]\n" \
"v_cvt_pk_f16_f32 v185, v186, v187\n" \
"v_cvt_pk_f16_f32 v186, v188, v189\n" \
"ds_read_b128 v[180:183], v240 offset:49664\n" \
"v_mfma_f32_16x16x32_f16 v[32:35], v[120:123], v[160:163], v[32:35]\n" \
"v_cvt_pk_f16_f32 v187, v190, v191\n" \
"v_add_u32 v184, s91, v184\n" \
"buffer_load_dwordx4 v232, s[36:39], s80 offen offset:1024 sc1 lds\n" \
"v_mfma_f32_16x16x32_f16 v[44:47], v[124:127], v[160:163], v[44:47]\n" \
"v_add_u32 v185, s91, v185\n" \
"v_add_u32 v186, s91, v186\n" \
"ds_read_b64_tr_b16 v[134:135], v242 offset:40960\n" \
"v_mfma_f32_16x16x32_f16 v[48:51], v[136:139], v[168:171], v[48:51]\n" \
"v_add_u32 v187, s91, v187\n" \
"v_and_b32 v184, s92, v184\n" \
"ds_read_b128 v[160:163], v240 offset:40960\n" \
"v_mfma_f32_16x16x32_f16 v[60:63], v[140:143], v[168:171], v[60:63]\n" \
"s_waitcnt lgkmcnt(4)\n" \
"v_pk_add_f16 v168, v168, v172\n" \
"v_pk_add_f16 v169, v169, v173\n" \
"v_and_b32 v185, s92, v185\n" \
"v_mfma_f32_16x16x32_f16 v[52:55], v[152:155], v[172:175], v[52:55]\n" \
"v_pk_add_f16 v170, v170, v174\n" \
"v_pk_add_f16 v171, v171, v175\n" \
"v_and_b32 v186, s92, v186\n" \
"v_and_b32 v187, s92, v187\n" \
"v_mfma_f32_16x16x32_f16 v[64:67], v[156:159], v[172:175], v[64:67]\n" \
"ds_read_b128 v[164:167], v240 offset:41472\n" \
"buffer_store_dwordx4 v[184:187], v234, s[36:39], s82 offen offset:1024 sc1\n" \
"ds_read_b64_tr_b16 v[144:145], v241 offset:33280\n" \
"v_mfma_f32_16x16x32_f16 v[56:59], v[120:123], v[168:171], v[56:59]\n" \
"buffer_load_dwordx4 v232, s[36:39], s80 offen offset:2048 sc1 lds\n" \
"v_mfma_f32_16x16x32_f16 v[68:71], v[124:127], v[168:171], v[68:71]\n" \
"s_setprio 0\n" \
"ds_read_b64_tr_b16 v[146:147], v241 offset:41472\n" \
"ds_read_b64_tr_b16 v[148:149], v242 offset:33280\n" \
"v_mfma_f32_16x16x32_f16 v[72:75], v[136:139], v[176:179], v[72:75]\n" \
"ds_read_b128 v[168:171], v240 offset:43008\n" \
"ds_read_b64_tr_b16 v[150:151], v242 offset:41472\n" \
"v_mfma_f32_16x16x32_f16 v[84:87], v[140:143], v[176:179], v[84:87]\n" \
"s_waitcnt lgkmcnt(6)\n" \
"v_pk_add_f16 v176, v176, v180\n" \
"v_pk_add_f16 v177, v177, v181\n" \
"v_mfma_f32_16x16x32_f16 v[76:79], v[152:155], v[180:183], v[76:79]\n" \
"v_pk_add_f16 v178, v178, v182\n" \
"v_pk_add_f16 v179, v179, v183\n" \
"v_mfma_f32_16x16x32_f16 v[88:91], v[156:159], v[180:183], v[88:91]\n" \
"ds_read_b128 v[172:175], v240 offset:43520\n" \
"v_mfma_f32_16x16x32_f16 v[80:83], v[120:123], v[176:179], v[80:83]\n" \
"buffer_load_dwordx4 v232, s[36:39], s80 offen offset:3072 sc1 lds\n" \
"v_mfma_f32_16x16x32_f16 v[92:95], v[124:127], v[176:179], v[92:95]\n" \
"v_mfma_f32_16x16x32_f16 v[96:99], v[136:139], v[160:163], v[96:99]\n" \
"s_waitcnt lgkmcnt(5)\n" \
"v_pk_add_f16 v136, v128, v144\n" \
"v_pk_add_f16 v137, v129, v145\n" \
"ds_read_b128 v[176:179], v240 offset:45056\n" \
"v_mfma_f32_16x16x32_f16 v[108:111], v[140:143], v[160:163], v[108:111]\n" \
"v_pk_add_f16 v160, v160, v164\n" \
"v_pk_add_f16 v161, v161, v165\n" \
"ds_read_b128 v[180:183], v240 offset:45568\n" \
"v_mfma_f32_16x16x32_f16 v[100:103], v[152:155], v[164:167], v[100:103]\n" \
"v_pk_add_f16 v162, v162, v166\n" \
"v_pk_add_f16 v163, v163, v167\n" \
"s_add_i32 m0, s45, 0x5000\n" \
"s_add_i32 s81, s47, 0x3c00000\n" \
"v_mfma_f32_16x16x32_f16 v[112:115], v[156:159], v[164:167], v[112:115]\n" \
"s_waitcnt lgkmcnt(3)\n" \
"v_pk_add_f16 v138, v130, v146\n" \
"v_pk_add_f16 v139, v131, v147\n" \
"buffer_load_dwordx4 v232, s[36:39], s81 offen sc1 lds\n" \
"v_mfma_f32_16x16x32_f16 v[104:107], v[120:123], v[160:163], v[104:107]\n" \
"v_pk_add_f16 v140, v132, v148\n" \
"v_pk_add_f16 v141, v133, v149\n" \
"v_mfma_f32_16x16x32_f16 v[116:119], v[124:127], v[160:163], v[116:119]\n" \
"s_setprio 1\n" \
"v_pk_add_f16 v142, v134, v150\n" \
"v_pk_add_f16 v143, v135, v151\n" \
"s_waitcnt vmcnt(6) lgkmcnt(0)\n" \
"s_barrier\n" \
"v_mfma_f32_16x16x32_f16 v[0:3], v[128:131], v[168:171], v[0:3]\n" \
"ds_read_b128 v[160:163], v240 offset:6144\n" \
"ds_read_b64_tr_b16 v[184:185], v236 offset:28672\n" \
"ds_read_b64_tr_b16 v[188:189], v237 offset:28672\n" \
"s_add_i32 m0, s54, 0x0\n" \
"s_mov_b32 s83, 0x0\n" \
"v_mfma_f32_16x16x32_f16 v[12:15], v[132:135], v[168:171], v[12:15]\n" \
"v_pk_add_f16 v168, v168, v172\n" \
"v_pk_add_f16 v169, v169, v173\n" \
"buffer_load_dwordx4 v233, s[36:39], s83 offen sc1 lds\n" \
"v_mfma_f32_16x16x32_f16 v[4:7], v[144:147], v[172:175], v[4:7]\n" \
"v_pk_add_f16 v170, v170, v174\n" \
"v_pk_add_f16 v171, v171, v175\n" \
"buffer_store_dwordx4 v[228:231], v235, s[36:39], s68 offen sc1\n" \
"v_mfma_f32_16x16x32_f16 v[16:19], v[148:151], v[172:175], v[16:19]\n" \
"ds_read_b128 v[164:167], v240 offset:6656\n" \
"s_add_i32 m0, s44, 0xa000\n" \
"s_add_i32 s80, s46, 0x4000000\n" \
"v_mfma_f32_16x16x32_f16 v[8:11], v[136:139], v[168:171], v[8:11]\n" \
"buffer_load_dwordx4 v232, s[36:39], s80 offen sc1 lds\n" \
"s_waitcnt lgkmcnt(2)\n" \
"v_mfma_f32_16x16x16_bf16 v[184:187], v[184:185], v[192:193], 0\n" \
"v_mfma_f32_16x16x32_f16 v[20:23], v[140:143], v[168:171], v[20:23]\n" \
"ds_read_b64_tr_b16 v[120:121], v241 offset:0\n" \
"ds_read_b64_tr_b16 v[122:123], v241 offset:8192\n" \
"s_waitcnt lgkmcnt(3)\n" \
"v_mfma_f32_16x16x16_bf16 v[188:191], v[188:189], v[192:193], 0\n" \
"v_mfma_f32_16x16x32_f16 v[24:27], v[128:131], v[176:179], v[24:27]\n" \
"ds_read_b128 v[168:171], v240 offset:8192\n" \
"v_mfma_f32_16x16x32_f16 v[36:39], v[132:135], v[176:179], v[36:39]\n" \
"v_pk_add_f16 v176, v176, v180\n" \
"v_pk_add_f16 v177, v177, v181\n" \
"ds_read_b64_tr_b16 v[124:125], v242 offset:0\n" \
"v_mfma_f32_16x16x32_f16 v[28:31], v[144:147], v[180:183], v[28:31]\n" \
"v_pk_add_f16 v178, v178, v182\n" \
"v_pk_add_f16 v179, v179, v183\n" \
"v_cvt_pk_f16_f32 v184, v184, v185\n" \
"v_mfma_f32_16x16x32_f16 v[40:43], v[148:151], v[180:183], v[40:43]\n" \
"v_cvt_pk_f16_f32 v185, v186, v187\n" \
"v_cvt_pk_f16_f32 v186, v188, v189\n" \
"ds_read_b128 v[172:175], v240 offset:8704\n" \
"v_mfma_f32_16x16x32_f16 v[32:35], v[136:139], v[176:179], v[32:35]\n" \
"v_cvt_pk_f16_f32 v187, v190, v191\n" \
"v_add_u32 v184, s91, v184\n" \
"buffer_load_dwordx4 v232, s[36:39], s80 offen offset:1024 sc1 lds\n" \
"v_mfma_f32_16x16x32_f16 v[44:47], v[140:143], v[176:179], v[44:47]\n" \
"v_add_u32 v185, s91, v185\n" \
"v_add_u32 v186, s91, v186\n" \
"ds_read_b64_tr_b16 v[126:127], v242 offset:8192\n" \
"v_mfma_f32_16x16x32_f16 v[48:51], v[128:131], v[160:163], v[48:51]\n" \
"v_add_u32 v187, s91, v187\n" \
"v_and_b32 v184, s92, v184\n" \
"ds_read_b128 v[176:179], v240 offset:0\n" \
"v_mfma_f32_16x16x32_f16 v[60:63], v[132:135], v[160:163], v[60:63]\n" \
"s_waitcnt lgkmcnt(4)\n" \
"v_pk_add_f16 v160, v160, v164\n" \
"v_pk_add_f16 v161, v161, v165\n" \
"v_and_b32 v185, s92, v185\n" \
"s_add_i32 s82, s48, 0x6000000\n" \
"v_mfma_f32_16x16x32_f16 v[52:55], v[144:147], v[164:167], v[52:55]\n" \
"v_pk_add_f16 v162, v162, v166\n" \
"v_pk_add_f16 v163, v163, v167\n" \
"v_and_b32 v186, s92, v186\n" \
"v_and_b32 v187, s92, v187\n" \
"v_mfma_f32_16x16x32_f16 v[64:67], v[148:151], v[164:167], v[64:67]\n" \
"ds_read_b128 v[180:183], v240 offset:512\n" \
"buffer_store_dwordx4 v[184:187], v234, s[36:39], s82 offen sc1\n" \
"ds_read_b64_tr_b16 v[152:153], v241 offset:512\n" \
"v_mfma_f32_16x16x32_f16 v[56:59], v[136:139], v[160:163], v[56:59]\n" \
"buffer_load_dwordx4 v232, s[36:39], s80 offen offset:2048 sc1 lds\n" \
"v_mfma_f32_16x16x32_f16 v[68:71], v[140:143], v[160:163], v[68:71]\n" \
"s_setprio 0\n" \
"ds_read_b64_tr_b16 v[154:155], v241 offset:8704\n" \
"ds_read_b64_tr_b16 v[156:157], v242 offset:512\n" \
"v_mfma_f32_16x16x32_f16 v[72:75], v[128:131], v[168:171], v[72:75]\n" \
"ds_read_b128 v[160:163], v240 offset:2048\n" \
"ds_read_b64_tr_b16 v[158:159], v242 offset:8704\n" \
"v_mfma_f32_16x16x32_f16 v[84:87], v[132:135], v[168:171], v[84:87]\n" \
"s_waitcnt lgkmcnt(6)\n" \
"v_pk_add_f16 v168, v168, v172\n" \
"v_pk_add_f16 v169, v169, v173\n" \
"v_mfma_f32_16x16x32_f16 v[76:79], v[144:147], v[172:175], v[76:79]\n" \
"v_pk_add_f16 v170, v170, v174\n" \
"v_pk_add_f16 v171, v171, v175\n" \
"v_mfma_f32_16x16x32_f16 v[88:91], v[148:151], v[172:175], v[88:91]\n" \
"ds_read_b128 v[164:167], v240 offset:2560\n" \
"v_mfma_f32_16x16x32_f16 v[80:83], v[136:139], v[168:171], v[80:83]\n" \
"buffer_load_dwordx4 v232, s[36:39], s80 offen offset:3072 sc1 lds\n" \
"v_mfma_f32_16x16x32_f16 v[92:95], v[140:143], v[168:171], v[92:95]\n" \
"v_mfma_f32_16x16x32_f16 v[96:99], v[128:131], v[176:179], v[96:99]\n" \
"s_waitcnt lgkmcnt(5)\n" \
"v_pk_add_f16 v128, v120, v152\n" \
"v_pk_add_f16 v129, v121, v153\n" \
"ds_read_b128 v[168:171], v240 offset:4096\n" \
"v_mfma_f32_16x16x32_f16 v[108:111], v[132:135], v[176:179], v[108:111]\n" \
"v_pk_add_f16 v176, v176, v180\n" \
"v_pk_add_f16 v177, v177, v181\n" \
"ds_read_b128 v[172:175], v240 offset:4608\n" \
"v_mfma_f32_16x16x32_f16 v[100:103], v[144:147], v[180:183], v[100:103]\n" \
"v_pk_add_f16 v178, v178, v182\n" \
"v_pk_add_f16 v179, v179, v183\n" \
"s_add_i32 m0, s45, 0xa000\n" \
"s_add_i32 s81, s47, 0x4000000\n" \
"v_mfma_f32_16x16x32_f16 v[112:115], v[148:151], v[180:183], v[112:115]\n" \
"s_waitcnt lgkmcnt(3)\n" \
"v_pk_add_f16 v130, v122, v154\n" \
"v_pk_add_f16 v131, v123, v155\n" \
"buffer_load_dwordx4 v232, s[36:39], s81 offen sc1 lds\n" \
"v_mfma_f32_16x16x32_f16 v[104:107], v[136:139], v[176:179], v[104:107]\n" \
"v_pk_add_f16 v132, v124, v156\n" \
"v_pk_add_f16 v133, v125, v157\n" \
"v_mfma_f32_16x16x32_f16 v[116:119], v[140:143], v[176:179], v[116:119]\n" \
"s_setprio 1\n" \
"v_pk_add_f16 v134, v126, v158\n" \
"v_pk_add_f16 v135, v127, v159\n" \
"s_waitcnt vmcnt(8) lgkmcnt(0)\n" \
"s_barrier\n" \
"v_mfma_f32_16x16x32_f16 v[0:3], v[120:123], v[160:163], v[0:3]\n" \
"ds_read_b128 v[176:179], v240 offset:26624\n" \
"ds_read_b64_tr_b16 v[184:185], v238 offset:28672\n" \
"ds_read_b64_tr_b16 v[188:189], v239 offset:28672\n" \
"v_mfma_f32_16x16x32_f16 v[12:15], v[124:127], v[160:163], v[12:15]\n" \
"v_pk_add_f16 v160, v160, v164\n" \
"v_pk_add_f16 v161, v161, v165\n" \
"v_mfma_f32_16x16x32_f16 v[4:7], v[152:155], v[164:167], v[4:7]\n" \
"v_pk_add_f16 v162, v162, v166\n" \
"v_pk_add_f16 v163, v163, v167\n" \
"v_mfma_f32_16x16x32_f16 v[16:19], v[156:159], v[164:167], v[16:19]\n" \
"ds_read_b128 v[180:183], v240 offset:27136\n" \
"s_add_i32 m0, s44, 0x0\n" \
"s_add_i32 s80, s46, 0x4400000\n" \
"v_mfma_f32_16x16x32_f16 v[8:11], v[128:131], v[160:163], v[8:11]\n" \
"buffer_load_dwordx4 v232, s[36:39], s80 offen sc1 lds\n" \
"s_waitcnt lgkmcnt(2)\n" \
"v_mfma_f32_16x16x16_bf16 v[184:187], v[184:185], v[192:193], 0\n" \
"v_mfma_f32_16x16x32_f16 v[20:23], v[132:135], v[160:163], v[20:23]\n" \
"ds_read_b64_tr_b16 v[136:137], v241 offset:16384\n" \
"ds_read_b64_tr_b16 v[138:139], v241 offset:24576\n" \
"s_waitcnt lgkmcnt(3)\n" \
"v_mfma_f32_16x16x16_bf16 v[188:191], v[188:189], v[192:193], 0\n" \
"v_mfma_f32_16x16x32_f16 v[24:27], v[120:123], v[168:171], v[24:27]\n" \
"ds_read_b128 v[160:163], v240 offset:28672\n" \
"v_mfma_f32_16x16x32_f16 v[36:39], v[124:127], v[168:171], v[36:39]\n" \
"v_pk_add_f16 v168, v168, v172\n" \
"v_pk_add_f16 v169, v169, v173\n" \
"ds_read_b64_tr_b16 v[140:141], v242 offset:16384\n" \
"v_mfma_f32_16x16x32_f16 v[28:31], v[152:155], v[172:175], v[28:31]\n" \
"v_pk_add_f16 v170, v170, v174\n" \
"v_pk_add_f16 v171, v171, v175\n" \
"v_cvt_pk_f16_f32 v184, v184, v185\n" \
"v_mfma_f32_16x16x32_f16 v[40:43], v[156:159], v[172:175], v[40:43]\n" \
"v_cvt_pk_f16_f32 v185, v186, v187\n" \
"v_cvt_pk_f16_f32 v186, v188, v189\n" \
"ds_read_b128 v[164:167], v240 offset:29184\n" \
"v_mfma_f32_16x16x32_f16 v[32:35], v[128:131], v[168:171], v[32:35]\n" \
"v_cvt_pk_f16_f32 v187, v190, v191\n" \
"v_add_u32 v184, s91, v184\n" \
"buffer_load_dwordx4 v232, s[36:39], s80 offen offset:1024 sc1 lds\n" \
"v_mfma_f32_16x16x32_f16 v[44:47], v[132:135], v[168:171], v[44:47]\n" \
"v_add_u32 v185, s91, v185\n" \
"v_add_u32 v186, s91, v186\n" \
"ds_read_b64_tr_b16 v[142:143], v242 offset:24576\n" \
"v_mfma_f32_16x16x32_f16 v[48:51], v[120:123], v[176:179], v[48:51]\n" \
"v_add_u32 v187, s91, v187\n" \
"v_and_b32 v184, s92, v184\n" \
"ds_read_b128 v[168:171], v240 offset:20480\n" \
"v_mfma_f32_16x16x32_f16 v[60:63], v[124:127], v[176:179], v[60:63]\n" \
"s_waitcnt lgkmcnt(4)\n" \
"v_pk_add_f16 v176, v176, v180\n" \
"v_pk_add_f16 v177, v177, v181\n" \
"v_and_b32 v185, s92, v185\n" \
"v_mfma_f32_16x16x32_f16 v[52:55], v[152:155], v[180:183], v[52:55]\n" \
"v_pk_add_f16 v178, v178, v182\n" \
"v_pk_add_f16 v179, v179, v183\n" \
"v_and_b32 v186, s92, v186\n" \
"v_and_b32 v187, s92, v187\n" \
"v_mfma_f32_16x16x32_f16 v[64:67], v[156:159], v[180:183], v[64:67]\n" \
"ds_read_b128 v[172:175], v240 offset:20992\n" \
"buffer_store_dwordx4 v[184:187], v234, s[36:39], s82 offen offset:1024 sc1\n" \
"ds_read_b64_tr_b16 v[144:145], v241 offset:16896\n" \
"v_mfma_f32_16x16x32_f16 v[56:59], v[128:131], v[176:179], v[56:59]\n" \
"buffer_load_dwordx4 v232, s[36:39], s80 offen offset:2048 sc1 lds\n" \
"v_mfma_f32_16x16x32_f16 v[68:71], v[132:135], v[176:179], v[68:71]\n" \
"s_setprio 0\n" \
"ds_read_b64_tr_b16 v[146:147], v241 offset:25088\n" \
"ds_read_b64_tr_b16 v[148:149], v242 offset:16896\n" \
"v_mfma_f32_16x16x32_f16 v[72:75], v[120:123], v[160:163], v[72:75]\n" \
"ds_read_b128 v[176:179], v240 offset:22528\n" \
"ds_read_b64_tr_b16 v[150:151], v242 offset:25088\n" \
"v_mfma_f32_16x16x32_f16 v[84:87], v[124:127], v[160:163], v[84:87]\n" \
"s_waitcnt lgkmcnt(6)\n" \
"v_pk_add_f16 v160, v160, v164\n" \
"v_pk_add_f16 v161, v161, v165\n" \
"v_mfma_f32_16x16x32_f16 v[76:79], v[152:155], v[164:167], v[76:79]\n" \
"v_pk_add_f16 v162, v162, v166\n" \
"v_pk_add_f16 v163, v163, v167\n" \
"v_mfma_f32_16x16x32_f16 v[88:91], v[156:159], v[164:167], v[88:91]\n" \
"ds_read_b128 v[180:183], v240 offset:23040\n" \
"v_mfma_f32_16x16x32_f16 v[80:83], v[128:131], v[160:163], v[80:83]\n" \
"buffer_load_dwordx4 v232, s[36:39], s80 offen offset:3072 sc1 lds\n" \
"v_mfma_f32_16x16x32_f16 v[92:95], v[132:135], v[160:163], v[92:95]\n" \
"v_mfma_f32_16x16x32_f16 v[96:99], v[120:123], v[168:171], v[96:99]\n" \
"s_waitcnt lgkmcnt(5)\n" \
"v_pk_add_f16 v120, v136, v144\n" \
"v_pk_add_f16 v121, v137, v145\n" \
"ds_read_b128 v[160:163], v240 offset:24576\n" \
"v_mfma_f32_16x16x32_f16 v[108:111], v[124:127], v[168:171], v[108:111]\n" \
"v_pk_add_f16 v168, v168, v172\n" \
"v_pk_add_f16 v169, v169, v173\n" \
"ds_read_b128 v[164:167], v240 offset:25088\n" \
"v_mfma_f32_16x16x32_f16 v[100:103], v[152:155], v[172:175], v[100:103]\n" \
"v_pk_add_f16 v170, v170, v174\n" \
"v_pk_add_f16 v171, v171, v175\n" \
"s_add_i32 m0, s45, 0x0\n" \
"s_add_i32 s81, s47, 0x4400000\n" \
"v_mfma_f32_16x16x32_f16 v[112:115], v[156:159], v[172:175], v[112:115]\n" \
"s_waitcnt lgkmcnt(3)\n" \
"v_pk_add_f16 v122, v138, v146\n" \
"v_pk_add_f16 v123, v139, v147\n" \
"buffer_load_dwordx4 v232, s[36:39], s81 offen sc1 lds\n" \
"v_mfma_f32_16x16x32_f16 v[104:107], v[128:131], v[168:171], v[104:107]\n" \
"v_pk_add_f16 v124, v140, v148\n" \
"v_pk_add_f16 v125, v141, v149\n" \
"v_mfma_f32_16x16x32_f16 v[116:119], v[132:135], v[168:171], v[116:119]\n" \
"s_setprio 1\n" \
"v_pk_add_f16 v126, v142, v150\n" \
"v_pk_add_f16 v127, v143, v151\n" \
"s_waitcnt vmcnt(6) lgkmcnt(0)\n" \
"s_barrier\n" \
"v_mfma_f32_16x16x32_f16 v[0:3], v[136:139], v[176:179], v[0:3]\n" \
"ds_read_b128 v[168:171], v240 offset:47104\n" \
"ds_read_b64_tr_b16 v[184:185], v236 offset:32768\n" \
"ds_read_b64_tr_b16 v[188:189], v237 offset:32768\n" \
"s_add_i32 m0, s54, 0x1000\n" \
"s_mov_b32 s83, 0x100\n" \
"v_mfma_f32_16x16x32_f16 v[12:15], v[140:143], v[176:179], v[12:15]\n" \
"v_pk_add_f16 v176, v176, v180\n" \
"v_pk_add_f16 v177, v177, v181\n" \
"buffer_load_dwordx4 v233, s[36:39], s83 offen sc1 lds\n" \
"v_mfma_f32_16x16x32_f16 v[4:7], v[144:147], v[180:183], v[4:7]\n" \
"v_pk_add_f16 v178, v178, v182\n" \
"v_pk_add_f16 v179, v179, v183\n" \
"v_mfma_f32_16x16x32_f16 v[16:19], v[148:151], v[180:183], v[16:19]\n" \
"ds_read_b128 v[172:175], v240 offset:47616\n" \
"s_add_i32 m0, s44, 0x5000\n" \
"s_add_i32 s80, s46, 0x4800000\n" \
"v_mfma_f32_16x16x32_f16 v[8:11], v[120:123], v[176:179], v[8:11]\n" \
"buffer_load_dwordx4 v232, s[36:39], s80 offen sc1 lds\n" \
"s_waitcnt lgkmcnt(2)\n" \
"v_mfma_f32_16x16x16_bf16 v[184:187], v[184:185], v[192:193], 0\n" \
"v_mfma_f32_16x16x32_f16 v[20:23], v[124:127], v[176:179], v[20:23]\n" \
"ds_read_b64_tr_b16 v[128:129], v241 offset:32768\n" \
"ds_read_b64_tr_b16 v[130:131], v241 offset:40960\n" \
"s_waitcnt lgkmcnt(3)\n" \
"v_mfma_f32_16x16x16_bf16 v[188:191], v[188:189], v[192:193], 0\n" \
"v_mfma_f32_16x16x32_f16 v[24:27], v[136:139], v[160:163], v[24:27]\n" \
"ds_read_b128 v[176:179], v240 offset:49152\n" \
"v_mfma_f32_16x16x32_f16 v[36:39], v[140:143], v[160:163], v[36:39]\n" \
"v_pk_add_f16 v160, v160, v164\n" \
"v_pk_add_f16 v161, v161, v165\n" \
"ds_read_b64_tr_b16 v[132:133], v242 offset:32768\n" \
"v_mfma_f32_16x16x32_f16 v[28:31], v[144:147], v[164:167], v[28:31]\n" \
"v_pk_add_f16 v162, v162, v166\n" \
"v_pk_add_f16 v163, v163, v167\n" \
"v_cvt_pk_f16_f32 v184, v184, v185\n" \
"v_mfma_f32_16x16x32_f16 v[40:43], v[148:151], v[164:167], v[40:43]\n" \
"v_cvt_pk_f16_f32 v185, v186, v187\n" \
"v_cvt_pk_f16_f32 v186, v188, v189\n" \
"ds_read_b128 v[180:183], v240 offset:49664\n" \
"v_mfma_f32_16x16x32_f16 v[32:35], v[120:123], v[160:163], v[32:35]\n" \
"v_cvt_pk_f16_f32 v187, v190, v191\n" \
"v_add_u32 v184, s91, v184\n" \
"buffer_load_dwordx4 v232, s[36:39], s80 offen offset:1024 sc1 lds\n" \
"v_mfma_f32_16x16x32_f16 v[44:47], v[124:127], v[160:163], v[44:47]\n" \
"v_add_u32 v185, s91, v185\n" \
"v_add_u32 v186, s91, v186\n" \
"ds_read_b64_tr_b16 v[134:135], v242 offset:40960\n" \
"v_mfma_f32_16x16x32_f16 v[48:51], v[136:139], v[168:171], v[48:51]\n" \
"v_add_u32 v187, s91, v187\n" \
"v_and_b32 v184, s92, v184\n" \
"ds_read_b128 v[160:163], v240 offset:40960\n" \
"v_mfma_f32_16x16x32_f16 v[60:63], v[140:143], v[168:171], v[60:63]\n" \
"s_waitcnt lgkmcnt(4)\n" \
"v_pk_add_f16 v168, v168, v172\n" \
"v_pk_add_f16 v169, v169, v173\n" \
"v_and_b32 v185, s92, v185\n" \
"s_add_i32 s82, s49, 0x6800000\n" \
"v_mfma_f32_16x16x32_f16 v[52:55], v[144:147], v[172:175], v[52:55]\n" \
"v_pk_add_f16 v170, v170, v174\n" \
"v_pk_add_f16 v171, v171, v175\n" \
"v_and_b32 v186, s92, v186\n" \
"v_and_b32 v187, s92, v187\n" \
"v_mfma_f32_16x16x32_f16 v[64:67], v[148:151], v[172:175], v[64:67]\n" \
"ds_read_b128 v[164:167], v240 offset:41472\n" \
"buffer_store_dwordx4 v[184:187], v234, s[36:39], s82 offen sc1\n" \
"ds_read_b64_tr_b16 v[152:153], v241 offset:33280\n" \
"v_mfma_f32_16x16x32_f16 v[56:59], v[120:123], v[168:171], v[56:59]\n" \
"buffer_load_dwordx4 v232, s[36:39], s80 offen offset:2048 sc1 lds\n" \
"v_mfma_f32_16x16x32_f16 v[68:71], v[124:127], v[168:171], v[68:71]\n" \
"s_setprio 0\n" \
"ds_read_b64_tr_b16 v[154:155], v241 offset:41472\n" \
"ds_read_b64_tr_b16 v[156:157], v242 offset:33280\n" \
"v_mfma_f32_16x16x32_f16 v[72:75], v[136:139], v[176:179], v[72:75]\n" \
"ds_read_b128 v[168:171], v240 offset:43008\n" \
"ds_read_b64_tr_b16 v[158:159], v242 offset:41472\n" \
"v_mfma_f32_16x16x32_f16 v[84:87], v[140:143], v[176:179], v[84:87]\n" \
"s_waitcnt lgkmcnt(6)\n" \
"v_pk_add_f16 v176, v176, v180\n" \
"v_pk_add_f16 v177, v177, v181\n" \
"v_mfma_f32_16x16x32_f16 v[76:79], v[144:147], v[180:183], v[76:79]\n" \
"v_pk_add_f16 v178, v178, v182\n" \
"v_pk_add_f16 v179, v179, v183\n" \
"v_mfma_f32_16x16x32_f16 v[88:91], v[148:151], v[180:183], v[88:91]\n" \
"ds_read_b128 v[172:175], v240 offset:43520\n" \
"v_mfma_f32_16x16x32_f16 v[80:83], v[120:123], v[176:179], v[80:83]\n" \
"buffer_load_dwordx4 v232, s[36:39], s80 offen offset:3072 sc1 lds\n" \
"v_mfma_f32_16x16x32_f16 v[92:95], v[124:127], v[176:179], v[92:95]\n" \
"v_mfma_f32_16x16x32_f16 v[96:99], v[136:139], v[160:163], v[96:99]\n" \
"s_waitcnt lgkmcnt(5)\n" \
"v_pk_add_f16 v136, v128, v152\n" \
"v_pk_add_f16 v137, v129, v153\n" \
"ds_read_b128 v[176:179], v240 offset:45056\n" \
"v_mfma_f32_16x16x32_f16 v[108:111], v[140:143], v[160:163], v[108:111]\n" \
"v_pk_add_f16 v160, v160, v164\n" \
"v_pk_add_f16 v161, v161, v165\n" \
"ds_read_b128 v[180:183], v240 offset:45568\n" \
"v_mfma_f32_16x16x32_f16 v[100:103], v[144:147], v[164:167], v[100:103]\n" \
"v_pk_add_f16 v162, v162, v166\n" \
"v_pk_add_f16 v163, v163, v167\n" \
"s_add_i32 m0, s45, 0x5000\n" \
"s_add_i32 s81, s47, 0x4800000\n" \
"v_mfma_f32_16x16x32_f16 v[112:115], v[148:151], v[164:167], v[112:115]\n" \
"s_waitcnt lgkmcnt(3)\n" \
"v_pk_add_f16 v138, v130, v154\n" \
"v_pk_add_f16 v139, v131, v155\n" \
"buffer_load_dwordx4 v232, s[36:39], s81 offen sc1 lds\n" \
"v_mfma_f32_16x16x32_f16 v[104:107], v[120:123], v[160:163], v[104:107]\n" \
"v_pk_add_f16 v140, v132, v156\n" \
"v_pk_add_f16 v141, v133, v157\n" \
"v_mfma_f32_16x16x32_f16 v[116:119], v[124:127], v[160:163], v[116:119]\n" \
"s_setprio 1\n" \
"v_pk_add_f16 v142, v134, v158\n" \
"v_pk_add_f16 v143, v135, v159\n" \
"s_waitcnt vmcnt(7) lgkmcnt(0)\n" \
"s_barrier\n" \
"v_mfma_f32_16x16x32_f16 v[0:3], v[128:131], v[168:171], v[0:3]\n" \
"ds_read_b128 v[160:163], v240 offset:6144\n" \
"ds_read_b64_tr_b16 v[184:185], v238 offset:32768\n" \
"ds_read_b64_tr_b16 v[188:189], v239 offset:32768\n" \
"v_mfma_f32_16x16x32_f16 v[12:15], v[132:135], v[168:171], v[12:15]\n" \
"v_pk_add_f16 v168, v168, v172\n" \
"v_pk_add_f16 v169, v169, v173\n" \
"v_mfma_f32_16x16x32_f16 v[4:7], v[152:155], v[172:175], v[4:7]\n" \
"v_pk_add_f16 v170, v170, v174\n" \
"v_pk_add_f16 v171, v171, v175\n" \
"v_mfma_f32_16x16x32_f16 v[16:19], v[156:159], v[172:175], v[16:19]\n" \
"ds_read_b128 v[164:167], v240 offset:6656\n" \
"s_add_i32 m0, s44, 0xa000\n" \
"s_add_i32 s80, s46, 0x4c00000\n" \
"v_mfma_f32_16x16x32_f16 v[8:11], v[136:139], v[168:171], v[8:11]\n" \
"buffer_load_dwordx4 v232, s[36:39], s80 offen sc1 lds\n" \
"s_waitcnt lgkmcnt(2)\n" \
"v_mfma_f32_16x16x16_bf16 v[184:187], v[184:185], v[192:193], 0\n" \
"v_mfma_f32_16x16x32_f16 v[20:23], v[140:143], v[168:171], v[20:23]\n" \
"ds_read_b64_tr_b16 v[120:121], v241 offset:0\n" \
"ds_read_b64_tr_b16 v[122:123], v241 offset:8192\n" \
"s_waitcnt lgkmcnt(3)\n" \
"v_mfma_f32_16x16x16_bf16 v[188:191], v[188:189], v[192:193], 0\n" \
"v_mfma_f32_16x16x32_f16 v[24:27], v[128:131], v[176:179], v[24:27]\n" \
"ds_read_b128 v[168:171], v240 offset:8192\n" \
"v_mfma_f32_16x16x32_f16 v[36:39], v[132:135], v[176:179], v[36:39]\n" \
"v_pk_add_f16 v176, v176, v180\n" \
"v_pk_add_f16 v177, v177, v181\n" \
"ds_read_b64_tr_b16 v[124:125], v242 offset:0\n" \
"v_mfma_f32_16x16x32_f16 v[28:31], v[152:155], v[180:183], v[28:31]\n" \
"v_pk_add_f16 v178, v178, v182\n" \
"v_pk_add_f16 v179, v179, v183\n" \
"v_cvt_pk_f16_f32 v184, v184, v185\n" \
"v_mfma_f32_16x16x32_f16 v[40:43], v[156:159], v[180:183], v[40:43]\n" \
"v_cvt_pk_f16_f32 v185, v186, v187\n" \
"v_cvt_pk_f16_f32 v186, v188, v189\n" \
"ds_read_b128 v[172:175], v240 offset:8704\n" \
"v_mfma_f32_16x16x32_f16 v[32:35], v[136:139], v[176:179], v[32:35]\n" \
"v_cvt_pk_f16_f32 v187, v190, v191\n" \
"v_add_u32 v184, s91, v184\n" \
"buffer_load_dwordx4 v232, s[36:39], s80 offen offset:1024 sc1 lds\n" \
"v_mfma_f32_16x16x32_f16 v[44:47], v[140:143], v[176:179], v[44:47]\n" \
"v_add_u32 v185, s91, v185\n" \
"v_add_u32 v186, s91, v186\n" \
"ds_read_b64_tr_b16 v[126:127], v242 offset:8192\n" \
"v_mfma_f32_16x16x32_f16 v[48:51], v[128:131], v[160:163], v[48:51]\n" \
"v_add_u32 v187, s91, v187\n" \
"v_and_b32 v184, s92, v184\n" \
"ds_read_b128 v[176:179], v240 offset:0\n" \
"v_mfma_f32_16x16x32_f16 v[60:63], v[132:135], v[160:163], v[60:63]\n" \
"s_waitcnt lgkmcnt(4)\n" \
"v_pk_add_f16 v160, v160, v164\n" \
"v_pk_add_f16 v161, v161, v165\n" \
"v_and_b32 v185, s92, v185\n" \
"v_mfma_f32_16x16x32_f16 v[52:55], v[152:155], v[164:167], v[52:55]\n" \
"v_pk_add_f16 v162, v162, v166\n" \
"v_pk_add_f16 v163, v163, v167\n" \
"v_and_b32 v186, s92, v186\n" \
"v_and_b32 v187, s92, v187\n" \
"v_mfma_f32_16x16x32_f16 v[64:67], v[156:159], v[164:167], v[64:67]\n" \
"ds_read_b128 v[180:183], v240 offset:512\n" \
"buffer_store_dwordx4 v[184:187], v234, s[36:39], s82 offen offset:1024 sc1\n" \
"ds_read_b64_tr_b16 v[144:145], v241 offset:512\n" \
"v_mfma_f32_16x16x32_f16 v[56:59], v[136:139], v[160:163], v[56:59]\n" \
"buffer_load_dwordx4 v232, s[36:39], s80 offen offset:2048 sc1 lds\n" \
"v_mfma_f32_16x16x32_f16 v[68:71], v[140:143], v[160:163], v[68:71]\n" \
"s_setprio 0\n" \
"ds_read_b64_tr_b16 v[146:147], v241 offset:8704\n" \
"ds_read_b64_tr_b16 v[148:149], v242 offset:512\n" \
"v_mfma_f32_16x16x32_f16 v[72:75], v[128:131], v[168:171], v[72:75]\n" \
"ds_read_b128 v[160:163], v240 offset:2048\n" \
"ds_read_b64_tr_b16 v[150:151], v242 offset:8704\n" \
"v_mfma_f32_16x16x32_f16 v[84:87], v[132:135], v[168:171], v[84:87]\n" \
"s_waitcnt lgkmcnt(6)\n" \
"v_pk_add_f16 v168, v168, v172\n" \
"v_pk_add_f16 v169, v169, v173\n" \
"v_mfma_f32_16x16x32_f16 v[76:79], v[152:155], v[172:175], v[76:79]\n" \
"v_pk_add_f16 v170, v170, v174\n" \
"v_pk_add_f16 v171, v171, v175\n" \
"v_mfma_f32_16x16x32_f16 v[88:91], v[156:159], v[172:175], v[88:91]\n" \
"ds_read_b128 v[164:167], v240 offset:2560\n" \
"v_mfma_f32_16x16x32_f16 v[80:83], v[136:139], v[168:171], v[80:83]\n" \
"buffer_load_dwordx4 v232, s[36:39], s80 offen offset:3072 sc1 lds\n" \
"v_mfma_f32_16x16x32_f16 v[92:95], v[140:143], v[168:171], v[92:95]\n" \
"v_mfma_f32_16x16x32_f16 v[96:99], v[128:131], v[176:179], v[96:99]\n" \
"s_waitcnt lgkmcnt(5)\n" \
"v_pk_add_f16 v128, v120, v144\n" \
"v_pk_add_f16 v129, v121, v145\n" \
"ds_read_b128 v[168:171], v240 offset:4096\n" \
"v_mfma_f32_16x16x32_f16 v[108:111], v[132:135], v[176:179], v[108:111]\n" \
"v_pk_add_f16 v176, v176, v180\n" \
"v_pk_add_f16 v177, v177, v181\n" \
"ds_read_b128 v[172:175], v240 offset:4608\n" \
"v_mfma_f32_16x16x32_f16 v[100:103], v[152:155], v[180:183], v[100:103]\n" \
"v_pk_add_f16 v178, v178, v182\n" \
"v_pk_add_f16 v179, v179, v183\n" \
"s_add_i32 m0, s45, 0xa000\n" \
"s_add_i32 s81, s47, 0x4c00000\n" \
"v_mfma_f32_16x16x32_f16 v[112:115], v[156:159], v[180:183], v[112:115]\n" \
"s_waitcnt lgkmcnt(3)\n" \
"v_pk_add_f16 v130, v122, v146\n" \
"v_pk_add_f16 v131, v123, v147\n" \
"buffer_load_dwordx4 v232, s[36:39], s81 offen sc1 lds\n" \
"v_mfma_f32_16x16x32_f16 v[104:107], v[136:139], v[176:179], v[104:107]\n" \
"v_pk_add_f16 v132, v124, v148\n" \
"v_pk_add_f16 v133, v125, v149\n" \
"v_mfma_f32_16x16x32_f16 v[116:119], v[140:143], v[176:179], v[116:119]\n" \
"s_setprio 1\n" \
"v_pk_add_f16 v134, v126, v150\n" \
"v_pk_add_f16 v135, v127, v151\n" \
"s_waitcnt vmcnt(6) lgkmcnt(0)\n" \
"s_barrier\n" \
"v_mfma_f32_16x16x32_f16 v[0:3], v[120:123], v[160:163], v[0:3]\n" \
"ds_read_b128 v[176:179], v240 offset:26624\n" \
"ds_read_b64_tr_b16 v[184:185], v236 offset:36864\n" \
"ds_read_b64_tr_b16 v[188:189], v237 offset:36864\n" \
"s_add_i32 m0, s54, 0x0\n" \
"s_mov_b32 s83, 0x200\n" \
"v_mfma_f32_16x16x32_f16 v[12:15], v[124:127], v[160:163], v[12:15]\n" \
"v_pk_add_f16 v160, v160, v164\n" \
"v_pk_add_f16 v161, v161, v165\n" \
"buffer_load_dwordx4 v233, s[36:39], s83 offen sc1 lds\n" \
"v_mfma_f32_16x16x32_f16 v[4:7], v[144:147], v[164:167], v[4:7]\n" \
"v_pk_add_f16 v162, v162, v166\n" \
"v_pk_add_f16 v163, v163, v167\n" \
"v_mfma_f32_16x16x32_f16 v[16:19], v[148:151], v[164:167], v[16:19]\n" \
"ds_read_b128 v[180:183], v240 offset:27136\n" \
"s_add_i32 m0, s44, 0x0\n" \
"s_add_i32 s80, s46, 0x5000000\n" \
"v_mfma_f32_16x16x32_f16 v[8:11], v[128:131], v[160:163], v[8:11]\n" \
"buffer_load_dwordx4 v232, s[36:39], s80 offen sc1 lds\n" \
"s_waitcnt lgkmcnt(2)\n" \
"v_mfma_f32_16x16x16_bf16 v[184:187], v[184:185], v[192:193], 0\n" \
"v_mfma_f32_16x16x32_f16 v[20:23], v[132:135], v[160:163], v[20:23]\n" \
"ds_read_b64_tr_b16 v[136:137], v241 offset:16384\n" \
"ds_read_b64_tr_b16 v[138:139], v241 offset:24576\n" \
"s_waitcnt lgkmcnt(3)\n" \
"v_mfma_f32_16x16x16_bf16 v[188:191], v[188:189], v[192:193], 0\n" \
"v_mfma_f32_16x16x32_f16 v[24:27], v[120:123], v[168:171], v[24:27]\n" \
"ds_read_b128 v[160:163], v240 offset:28672\n" \
"v_mfma_f32_16x16x32_f16 v[36:39], v[124:127], v[168:171], v[36:39]\n" \
"v_pk_add_f16 v168, v168, v172\n" \
"v_pk_add_f16 v169, v169, v173\n" \
"ds_read_b64_tr_b16 v[140:141], v242 offset:16384\n" \
"v_mfma_f32_16x16x32_f16 v[28:31], v[144:147], v[172:175], v[28:31]\n" \
"v_pk_add_f16 v170, v170, v174\n" \
"v_pk_add_f16 v171, v171, v175\n" \
"v_cvt_pk_f16_f32 v184, v184, v185\n" \
"v_mfma_f32_16x16x32_f16 v[40:43], v[148:151], v[172:175], v[40:43]\n" \
"v_cvt_pk_f16_f32 v185, v186, v187\n" \
"v_cvt_pk_f16_f32 v186, v188, v189\n" \
"ds_read_b128 v[164:167], v240 offset:29184\n" \
"v_mfma_f32_16x16x32_f16 v[32:35], v[128:131], v[168:171], v[32:35]\n" \
"v_cvt_pk_f16_f32 v187, v190, v191\n" \
"v_add_u32 v184, s91, v184\n" \
"buffer_load_dwordx4 v232, s[36:39], s80 offen offset:1024 sc1 lds\n" \
"v_mfma_f32_16x16x32_f16 v[44:47], v[132:135], v[168:171], v[44:47]\n" \
"v_add_u32 v185, s91, v185\n" \
"v_add_u32 v186, s91, v186\n" \
"ds_read_b64_tr_b16 v[142:143], v242 offset:24576\n" \
"v_mfma_f32_16x16x32_f16 v[48:51], v[120:123], v[176:179], v[48:51]\n" \
"v_add_u32 v187, s91, v187\n" \
"v_and_b32 v184, s92, v184\n" \
"ds_read_b128 v[168:171], v240 offset:20480\n" \
"v_mfma_f32_16x16x32_f16 v[60:63], v[124:127], v[176:179], v[60:63]\n" \
"s_waitcnt lgkmcnt(4)\n" \
"v_pk_add_f16 v176, v176, v180\n" \
"v_pk_add_f16 v177, v177, v181\n" \
"v_and_b32 v185, s92, v185\n" \
"s_add_i32 s82, s50, 0x7000000\n" \
"v_mfma_f32_16x16x32_f16 v[52:55], v[144:147], v[180:183], v[52:55]\n" \
"v_pk_add_f16 v178, v178, v182\n" \
"v_pk_add_f16 v179, v179, v183\n" \
"v_and_b32 v186, s92, v186\n" \
"v_and_b32 v187, s92, v187\n" \
"v_mfma_f32_16x16x32_f16 v[64:67], v[148:151], v[180:183], v[64:67]\n" \
"ds_read_b128 v[172:175], v240 offset:20992\n" \
"buffer_store_dwordx4 v[184:187], v234, s[36:39], s82 offen sc1\n" \
"ds_read_b64_tr_b16 v[152:153], v241 offset:16896\n" \
"v_mfma_f32_16x16x32_f16 v[56:59], v[128:131], v[176:179], v[56:59]\n" \
"buffer_load_dwordx4 v232, s[36:39], s80 offen offset:2048 sc1 lds\n" \
"v_mfma_f32_16x16x32_f16 v[68:71], v[132:135], v[176:179], v[68:71]\n" \
"s_setprio 0\n" \
"ds_read_b64_tr_b16 v[154:155], v241 offset:25088\n" \
"ds_read_b64_tr_b16 v[156:157], v242 offset:16896\n" \
"v_mfma_f32_16x16x32_f16 v[72:75], v[120:123], v[160:163], v[72:75]\n" \
"ds_read_b128 v[176:179], v240 offset:22528\n" \
"ds_read_b64_tr_b16 v[158:159], v242 offset:25088\n" \
"v_mfma_f32_16x16x32_f16 v[84:87], v[124:127], v[160:163], v[84:87]\n" \
"s_waitcnt lgkmcnt(6)\n" \
"v_pk_add_f16 v160, v160, v164\n" \
"v_pk_add_f16 v161, v161, v165\n" \
"v_mfma_f32_16x16x32_f16 v[76:79], v[144:147], v[164:167], v[76:79]\n" \
"v_pk_add_f16 v162, v162, v166\n" \
"v_pk_add_f16 v163, v163, v167\n" \
"v_mfma_f32_16x16x32_f16 v[88:91], v[148:151], v[164:167], v[88:91]\n" \
"ds_read_b128 v[180:183], v240 offset:23040\n" \
"v_mfma_f32_16x16x32_f16 v[80:83], v[128:131], v[160:163], v[80:83]\n" \
"buffer_load_dwordx4 v232, s[36:39], s80 offen offset:3072 sc1 lds\n" \
"v_mfma_f32_16x16x32_f16 v[92:95], v[132:135], v[160:163], v[92:95]\n" \
"v_mfma_f32_16x16x32_f16 v[96:99], v[120:123], v[168:171], v[96:99]\n" \
"s_waitcnt lgkmcnt(5)\n" \
"v_pk_add_f16 v120, v136, v152\n" \
"v_pk_add_f16 v121, v137, v153\n" \
"ds_read_b128 v[160:163], v240 offset:24576\n" \
"v_mfma_f32_16x16x32_f16 v[108:111], v[124:127], v[168:171], v[108:111]\n" \
"v_pk_add_f16 v168, v168, v172\n" \
"v_pk_add_f16 v169, v169, v173\n" \
"ds_read_b128 v[164:167], v240 offset:25088\n" \
"v_mfma_f32_16x16x32_f16 v[100:103], v[144:147], v[172:175], v[100:103]\n" \
"v_pk_add_f16 v170, v170, v174\n" \
"v_pk_add_f16 v171, v171, v175\n" \
"s_add_i32 m0, s45, 0x0\n" \
"s_add_i32 s81, s47, 0x5000000\n" \
"v_mfma_f32_16x16x32_f16 v[112:115], v[148:151], v[172:175], v[112:115]\n" \
"s_waitcnt lgkmcnt(3)\n" \
"v_pk_add_f16 v122, v138, v154\n" \
"v_pk_add_f16 v123, v139, v155\n" \
"buffer_load_dwordx4 v232, s[36:39], s81 offen sc1 lds\n" \
"v_mfma_f32_16x16x32_f16 v[104:107], v[128:131], v[168:171], v[104:107]\n" \
"v_pk_add_f16 v124, v140, v156\n" \
"v_pk_add_f16 v125, v141, v157\n" \
"v_mfma_f32_16x16x32_f16 v[116:119], v[132:135], v[168:171], v[116:119]\n" \
"s_setprio 1\n" \
"v_pk_add_f16 v126, v142, v158\n" \
"v_pk_add_f16 v127, v143, v159\n" \
"s_waitcnt vmcnt(7) lgkmcnt(0)\n" \
"s_barrier\n" \
"v_mfma_f32_16x16x32_f16 v[0:3], v[136:139], v[176:179], v[0:3]\n" \
"ds_read_b128 v[168:171], v240 offset:47104\n" \
"ds_read_b64_tr_b16 v[184:185], v238 offset:36864\n" \
"ds_read_b64_tr_b16 v[188:189], v239 offset:36864\n" \
"v_mfma_f32_16x16x32_f16 v[12:15], v[140:143], v[176:179], v[12:15]\n" \
"v_pk_add_f16 v176, v176, v180\n" \
"v_pk_add_f16 v177, v177, v181\n" \
"v_mfma_f32_16x16x32_f16 v[4:7], v[152:155], v[180:183], v[4:7]\n" \
"v_pk_add_f16 v178, v178, v182\n" \
"v_pk_add_f16 v179, v179, v183\n" \
"v_mfma_f32_16x16x32_f16 v[16:19], v[156:159], v[180:183], v[16:19]\n" \
"ds_read_b128 v[172:175], v240 offset:47616\n" \
"s_add_i32 m0, s44, 0x5000\n" \
"s_add_i32 s80, s46, 0x5400000\n" \
"v_mfma_f32_16x16x32_f16 v[8:11], v[120:123], v[176:179], v[8:11]\n" \
"buffer_load_dwordx4 v232, s[36:39], s80 offen sc1 lds\n" \
"s_waitcnt lgkmcnt(2)\n" \
"v_mfma_f32_16x16x16_bf16 v[184:187], v[184:185], v[192:193], 0\n" \
"v_mfma_f32_16x16x32_f16 v[20:23], v[124:127], v[176:179], v[20:23]\n" \
"ds_read_b64_tr_b16 v[128:129], v241 offset:32768\n" \
"ds_read_b64_tr_b16 v[130:131], v241 offset:40960\n" \
"s_waitcnt lgkmcnt(3)\n" \
"v_mfma_f32_16x16x16_bf16 v[188:191], v[188:189], v[192:193], 0\n" \
"v_mfma_f32_16x16x32_f16 v[24:27], v[136:139], v[160:163], v[24:27]\n" \
"ds_read_b128 v[176:179], v240 offset:49152\n" \
"v_mfma_f32_16x16x32_f16 v[36:39], v[140:143], v[160:163], v[36:39]\n" \
"v_pk_add_f16 v160, v160, v164\n" \
"v_pk_add_f16 v161, v161, v165\n" \
"ds_read_b64_tr_b16 v[132:133], v242 offset:32768\n" \
"v_mfma_f32_16x16x32_f16 v[28:31], v[152:155], v[164:167], v[28:31]\n" \
"v_pk_add_f16 v162, v162, v166\n" \
"v_pk_add_f16 v163, v163, v167\n" \
"v_cvt_pk_f16_f32 v184, v184, v185\n" \
"v_mfma_f32_16x16x32_f16 v[40:43], v[156:159], v[164:167], v[40:43]\n" \
"v_cvt_pk_f16_f32 v185, v186, v187\n" \
"v_cvt_pk_f16_f32 v186, v188, v189\n" \
"ds_read_b128 v[180:183], v240 offset:49664\n" \
"v_mfma_f32_16x16x32_f16 v[32:35], v[120:123], v[160:163], v[32:35]\n" \
"v_cvt_pk_f16_f32 v187, v190, v191\n" \
"v_add_u32 v184, s91, v184\n" \
"buffer_load_dwordx4 v232, s[36:39], s80 offen offset:1024 sc1 lds\n" \
"v_mfma_f32_16x16x32_f16 v[44:47], v[124:127], v[160:163], v[44:47]\n" \
"v_add_u32 v185, s91, v185\n" \
"v_add_u32 v186, s91, v186\n" \
"ds_read_b64_tr_b16 v[134:135], v242 offset:40960\n" \
"v_mfma_f32_16x16x32_f16 v[48:51], v[136:139], v[168:171], v[48:51]\n" \
"v_add_u32 v187, s91, v187\n" \
"v_and_b32 v184, s92, v184\n" \
"ds_read_b128 v[160:163], v240 offset:40960\n" \
"v_mfma_f32_16x16x32_f16 v[60:63], v[140:143], v[168:171], v[60:63]\n" \
"s_waitcnt lgkmcnt(4)\n" \
"v_pk_add_f16 v168, v168, v172\n" \
"v_pk_add_f16 v169, v169, v173\n" \
"v_and_b32 v185, s92, v185\n" \
"v_mfma_f32_16x16x32_f16 v[52:55], v[152:155], v[172:175], v[52:55]\n" \
"v_pk_add_f16 v170, v170, v174\n" \
"v_pk_add_f16 v171, v171, v175\n" \
"v_and_b32 v186, s92, v186\n" \
"v_and_b32 v187, s92, v187\n" \
"v_mfma_f32_16x16x32_f16 v[64:67], v[156:159], v[172:175], v[64:67]\n" \
"ds_read_b128 v[164:167], v240 offset:41472\n" \
"buffer_store_dwordx4 v[184:187], v234, s[36:39], s82 offen offset:1024 sc1\n" \
"ds_read_b64_tr_b16 v[144:145], v241 offset:33280\n" \
"v_mfma_f32_16x16x32_f16 v[56:59], v[120:123], v[168:171], v[56:59]\n" \
"buffer_load_dwordx4 v232, s[36:39], s80 offen offset:2048 sc1 lds\n" \
"v_mfma_f32_16x16x32_f16 v[68:71], v[124:127], v[168:171], v[68:71]\n" \
"s_setprio 0\n" \
"ds_read_b64_tr_b16 v[146:147], v241 offset:41472\n" \
"ds_read_b64_tr_b16 v[148:149], v242 offset:33280\n" \
"v_mfma_f32_16x16x32_f16 v[72:75], v[136:139], v[176:179], v[72:75]\n" \
"ds_read_b128 v[168:171], v240 offset:43008\n" \
"ds_read_b64_tr_b16 v[150:151], v242 offset:41472\n" \
"v_mfma_f32_16x16x32_f16 v[84:87], v[140:143], v[176:179], v[84:87]\n" \
"s_waitcnt lgkmcnt(6)\n" \
"v_pk_add_f16 v176, v176, v180\n" \
"v_pk_add_f16 v177, v177, v181\n" \
"v_mfma_f32_16x16x32_f16 v[76:79], v[152:155], v[180:183], v[76:79]\n" \
"v_pk_add_f16 v178, v178, v182\n" \
"v_pk_add_f16 v179, v179, v183\n" \
"v_mfma_f32_16x16x32_f16 v[88:91], v[156:159], v[180:183], v[88:91]\n" \
"ds_read_b128 v[172:175], v240 offset:43520\n" \
"v_mfma_f32_16x16x32_f16 v[80:83], v[120:123], v[176:179], v[80:83]\n" \
"buffer_load_dwordx4 v232, s[36:39], s80 offen offset:3072 sc1 lds\n" \
"v_mfma_f32_16x16x32_f16 v[92:95], v[124:127], v[176:179], v[92:95]\n" \
"v_mfma_f32_16x16x32_f16 v[96:99], v[136:139], v[160:163], v[96:99]\n" \
"s_waitcnt lgkmcnt(5)\n" \
"v_pk_add_f16 v136, v128, v144\n" \
"v_pk_add_f16 v137, v129, v145\n" \
"ds_read_b128 v[176:179], v240 offset:45056\n" \
"v_mfma_f32_16x16x32_f16 v[108:111], v[140:143], v[160:163], v[108:111]\n" \
"v_pk_add_f16 v160, v160, v164\n" \
"v_pk_add_f16 v161, v161, v165\n" \
"ds_read_b128 v[180:183], v240 offset:45568\n" \
"v_mfma_f32_16x16x32_f16 v[100:103], v[152:155], v[164:167], v[100:103]\n" \
"v_pk_add_f16 v162, v162, v166\n" \
"v_pk_add_f16 v163, v163, v167\n" \
"s_add_i32 m0, s45, 0x5000\n" \
"s_add_i32 s81, s47, 0x5400000\n" \
"v_mfma_f32_16x16x32_f16 v[112:115], v[156:159], v[164:167], v[112:115]\n" \
"s_waitcnt lgkmcnt(3)\n" \
"v_pk_add_f16 v138, v130, v146\n" \
"v_pk_add_f16 v139, v131, v147\n" \
"buffer_load_dwordx4 v232, s[36:39], s81 offen sc1 lds\n" \
"v_mfma_f32_16x16x32_f16 v[104:107], v[120:123], v[160:163], v[104:107]\n" \
"v_pk_add_f16 v140, v132, v148\n" \
"v_pk_add_f16 v141, v133, v149\n" \
"v_mfma_f32_16x16x32_f16 v[116:119], v[124:127], v[160:163], v[116:119]\n" \
"s_setprio 1\n" \
"v_pk_add_f16 v142, v134, v150\n" \
"v_pk_add_f16 v143, v135, v151\n" \
"s_waitcnt vmcnt(6) lgkmcnt(0)\n" \
"s_barrier\n" \
"v_mfma_f32_16x16x32_f16 v[0:3], v[128:131], v[168:171], v[0:3]\n" \
"ds_read_b128 v[160:163], v240 offset:6144\n" \
"ds_read_b64_tr_b16 v[184:185], v236 offset:40960\n" \
"s_add_i32 m0, s54, 0x1000\n" \
"s_mov_b32 s83, 0x300\n" \
"v_mfma_f32_16x16x32_f16 v[12:15], v[132:135], v[168:171], v[12:15]\n" \
"v_pk_add_f16 v168, v168, v172\n" \
"v_pk_add_f16 v169, v169, v173\n" \
"s_mov_b32 s84, 0x8014000\n" \
"buffer_load_dwordx4 v233, s[36:39], s83 offen sc1 lds\n" \
"v_mfma_f32_16x16x32_f16 v[4:7], v[144:147], v[172:175], v[4:7]\n" \
"v_pk_add_f16 v170, v170, v174\n" \
"v_pk_add_f16 v171, v171, v175\n" \
"buffer_load_dword v253, v252, s[36:39], s84 offen sc1\n" \
"v_mfma_f32_16x16x32_f16 v[16:19], v[148:151], v[172:175], v[16:19]\n" \
"ds_read_b128 v[164:167], v240 offset:6656\n" \
"s_add_i32 m0, s44, 0xa000\n" \
"s_add_i32 s80, s46, 0x5800000\n" \
"ds_read_b64_tr_b16 v[188:189], v237 offset:40960\n" \
"v_mfma_f32_16x16x32_f16 v[8:11], v[136:139], v[168:171], v[8:11]\n" \
"buffer_load_dwordx4 v232, s[36:39], s80 offen sc1 lds\n" \
"v_mfma_f32_16x16x32_f16 v[20:23], v[140:143], v[168:171], v[20:23]\n" \
"ds_read_b64_tr_b16 v[120:121], v241 offset:0\n" \
"ds_read_b64_tr_b16 v[122:123], v241 offset:8192\n" \
"s_waitcnt lgkmcnt(4)\n" \
"v_mfma_f32_16x16x16_bf16 v[184:187], v[184:185], v[192:193], 0\n" \
"v_mfma_f32_16x16x32_f16 v[24:27], v[128:131], v[176:179], v[24:27]\n" \
"ds_read_b128 v[168:171], v240 offset:8192\n" \
"s_waitcnt lgkmcnt(3)\n" \
"v_mfma_f32_16x16x16_bf16 v[188:191], v[188:189], v[192:193], 0\n" \
"v_mfma_f32_16x16x32_f16 v[36:39], v[132:135], v[176:179], v[36:39]\n" \
"v_pk_add_f16 v176, v176, v180\n" \
"v_pk_add_f16 v177, v177, v181\n" \
"ds_read_b64_tr_b16 v[124:125], v242 offset:0\n" \
"v_mfma_f32_16x16x32_f16 v[28:31], v[144:147], v[180:183], v[28:31]\n" \
"v_pk_add_f16 v178, v178, v182\n" \
"v_pk_add_f16 v179, v179, v183\n" \
"v_mfma_f32_16x16x32_f16 v[40:43], v[148:151], v[180:183], v[40:43]\n" \
"v_cvt_pk_f16_f32 v184, v184, v185\n" \
"ds_read_b128 v[172:175], v240 offset:8704\n" \
"ds_read_b64_tr_b16 v[126:127], v242 offset:8192\n" \
"v_mfma_f32_16x16x32_f16 v[32:35], v[136:139], v[176:179], v[32:35]\n" \
"v_cvt_pk_f16_f32 v185, v186, v187\n" \
"v_cvt_pk_f16_f32 v186, v188, v189\n" \
"buffer_load_dwordx4 v232, s[36:39], s80 offen offset:1024 sc1 lds\n" \
"v_mfma_f32_16x16x32_f16 v[44:47], v[140:143], v[176:179], v[44:47]\n" \
"v_cvt_pk_f16_f32 v187, v190, v191\n" \
"v_add_u32 v184, s91, v184\n" \
"v_mfma_f32_16x16x32_f16 v[48:51], v[128:131], v[160:163], v[48:51]\n" \
"v_add_u32 v185, s91, v185\n" \
"v_add_u32 v186, s91, v186\n" \
"ds_read_b128 v[176:179], v240 offset:0\n" \
"v_mfma_f32_16x16x32_f16 v[60:63], v[132:135], v[160:163], v[60:63]\n" \
"v_pk_add_f16 v160, v160, v164\n" \
"v_pk_add_f16 v161, v161, v165\n" \
"v_add_u32 v187, s91, v187\n" \
"v_and_b32 v184, s92, v184\n" \
"s_add_i32 s82, s51, 0x7800000\n" \
"v_mfma_f32_16x16x32_f16 v[52:55], v[144:147], v[164:167], v[52:55]\n" \
"v_pk_add_f16 v162, v162, v166\n" \
"v_pk_add_f16 v163, v163, v167\n" \
"v_and_b32 v185, s92, v185\n" \
"v_and_b32 v186, s92, v186\n" \
"v_and_b32 v187, s92, v187\n" \
"v_mfma_f32_16x16x32_f16 v[64:67], v[148:151], v[164:167], v[64:67]\n" \
"ds_read_b128 v[180:183], v240 offset:512\n" \
"buffer_store_dwordx4 v[184:187], v234, s[36:39], s82 offen sc1\n" \
"ds_read_b64_tr_b16 v[152:153], v241 offset:512\n" \
"v_mfma_f32_16x16x32_f16 v[56:59], v[136:139], v[160:163], v[56:59]\n" \
"buffer_load_dwordx4 v232, s[36:39], s80 offen offset:2048 sc1 lds\n" \
"ds_read_b64_tr_b16 v[154:155], v241 offset:8704\n" \
"v_mfma_f32_16x16x32_f16 v[68:71], v[140:143], v[160:163], v[68:71]\n" \
"s_setprio 0\n" \
"ds_read_b64_tr_b16 v[184:185], v238 offset:40960\n" \
"ds_read_b64_tr_b16 v[188:189], v239 offset:40960\n" \
"s_waitcnt lgkmcnt(5)\n" \
"v_mfma_f32_16x16x32_f16 v[72:75], v[128:131], v[168:171], v[72:75]\n" \
"ds_read_b128 v[160:163], v240 offset:2048\n" \
"ds_read_b64_tr_b16 v[156:157], v242 offset:512\n" \
"v_mfma_f32_16x16x32_f16 v[84:87], v[132:135], v[168:171], v[84:87]\n" \
"v_pk_add_f16 v168, v168, v172\n" \
"v_pk_add_f16 v169, v169, v173\n" \
"v_mfma_f32_16x16x32_f16 v[76:79], v[144:147], v[172:175], v[76:79]\n" \
"v_pk_add_f16 v170, v170, v174\n" \
"v_pk_add_f16 v171, v171, v175\n" \
"s_waitcnt lgkmcnt(3)\n" \
"v_mfma_f32_16x16x16_bf16 v[184:187], v[184:185], v[192:193], 0\n" \
"v_mfma_f32_16x16x32_f16 v[88:91], v[148:151], v[172:175], v[88:91]\n" \
"ds_read_b128 v[164:167], v240 offset:2560\n" \
"ds_read_b64_tr_b16 v[158:159], v242 offset:8704\n" \
"s_waitcnt lgkmcnt(4)\n" \
"v_mfma_f32_16x16x16_bf16 v[188:191], v[188:189], v[192:193], 0\n" \
"v_mfma_f32_16x16x32_f16 v[80:83], v[136:139], v[168:171], v[80:83]\n" \
"buffer_load_dwordx4 v232, s[36:39], s80 offen offset:3072 sc1 lds\n" \
"v_mfma_f32_16x16x32_f16 v[92:95], v[140:143], v[168:171], v[92:95]\n" \
"v_mfma_f32_16x16x32_f16 v[96:99], v[128:131], v[176:179], v[96:99]\n" \
"v_pk_add_f16 v128, v120, v152\n" \
"v_pk_add_f16 v129, v121, v153\n" \
"v_cvt_pk_f16_f32 v184, v184, v185\n" \
"v_cvt_pk_f16_f32 v185, v186, v187\n" \
"ds_read_b128 v[168:171], v240 offset:4096\n" \
"v_mfma_f32_16x16x32_f16 v[108:111], v[132:135], v[176:179], v[108:111]\n" \
"v_pk_add_f16 v176, v176, v180\n" \
"v_pk_add_f16 v177, v177, v181\n" \
"v_cvt_pk_f16_f32 v186, v188, v189\n" \
"v_cvt_pk_f16_f32 v187, v190, v191\n" \
"v_add_u32 v184, s91, v184\n" \
"v_add_u32 v185, s91, v185\n" \
"v_add_u32 v186, s91, v186\n" \
"ds_read_b128 v[172:175], v240 offset:4608\n" \
"v_mfma_f32_16x16x32_f16 v[100:103], v[144:147], v[180:183], v[100:103]\n" \
"v_pk_add_f16 v178, v178, v182\n" \
"v_pk_add_f16 v179, v179, v183\n" \
"s_waitcnt vmcnt(5)\n" \
"v_cmp_eq_u32 vcc, 0, v253\n" \
"s_cbranch_vccnz .Ls110_%=\n" \
".Lr110_%=:\n" \
"s_add_i32 m0, s45, 0xa000\n" \
"s_add_i32 s81, s47, 0x5800000\n" \
"v_mfma_f32_16x16x32_f16 v[112:115], v[148:151], v[180:183], v[112:115]\n" \
"v_pk_add_f16 v130, v122, v154\n" \
"v_pk_add_f16 v131, v123, v155\n" \
"v_add_u32 v187, s91, v187\n" \
"v_and_b32 v184, s92, v184\n" \
"buffer_load_dwordx4 v232, s[36:39], s81 offen sc1 lds\n" \
"v_mfma_f32_16x16x32_f16 v[104:107], v[136:139], v[176:179], v[104:107]\n" \
"s_waitcnt lgkmcnt(1)\n" \
"v_pk_add_f16 v132, v124, v156\n" \
"v_pk_add_f16 v133, v125, v157\n" \
"v_and_b32 v185, s92, v185\n" \
"v_and_b32 v186, s92, v186\n" \
"v_and_b32 v187, s92, v187\n" \
"v_mfma_f32_16x16x32_f16 v[116:119], v[140:143], v[176:179], v[116:119]\n" \
"s_setprio 1\n" \
"v_pk_add_f16 v134, v126, v158\n" \
"v_pk_add_f16 v135, v127, v159\n" \
"buffer_store_dwordx4 v[184:187], v234, s[36:39], s82 offen offset:1024 sc1\n" \
"s_waitcnt vmcnt(7) lgkmcnt(0)\n" \
"s_barrier\n" \
"v_mfma_f32_16x16x32_f16 v[0:3], v[120:123], v[160:163], v[0:3]\n" \
"ds_read_b128 v[176:179], v240 offset:26624\n" \
"v_mfma_f32_16x16x32_f16 v[12:15], v[124:127], v[160:163], v[12:15]\n" \
"v_pk_add_f16 v160, v160, v164\n" \
"v_pk_add_f16 v161, v161, v165\n" \
"v_mfma_f32_16x16x32_f16 v[4:7], v[152:155], v[164:167], v[4:7]\n" \
"v_pk_add_f16 v162, v162, v166\n" \
"v_pk_add_f16 v163, v163, v167\n" \
"v_mfma_f32_16x16x32_f16 v[16:19], v[156:159], v[164:167], v[16:19]\n" \
"ds_read_b128 v[180:183], v240 offset:27136\n" \
"s_add_i32 m0, s44, 0x0\n" \
"s_add_i32 s80, s46, 0x5c00000\n" \
"v_mfma_f32_16x16x32_f16 v[8:11], v[128:131], v[160:163], v[8:11]\n" \
"buffer_load_dwordx4 v232, s[36:39], s80 offen sc1 lds\n" \
"v_mfma_f32_16x16x32_f16 v[20:23], v[132:135], v[160:163], v[20:23]\n" \
"ds_read_b64_tr_b16 v[136:137], v241 offset:16384\n" \
"ds_read_b64_tr_b16 v[138:139], v241 offset:24576\n" \
"v_mfma_f32_16x16x32_f16 v[24:27], v[120:123], v[168:171], v[24:27]\n" \
"ds_read_b128 v[160:163], v240 offset:28672\n" \
"v_mfma_f32_16x16x32_f16 v[36:39], v[124:127], v[168:171], v[36:39]\n" \
"v_pk_add_f16 v168, v168, v172\n" \
"v_pk_add_f16 v169, v169, v173\n" \
"v_mfma_f32_16x16x32_f16 v[28:31], v[152:155], v[172:175], v[28:31]\n" \
"v_pk_add_f16 v170, v170, v174\n" \
"v_pk_add_f16 v171, v171, v175\n" \
"v_mfma_f32_16x16x32_f16 v[40:43], v[156:159], v[172:175], v[40:43]\n" \
"ds_read_b128 v[164:167], v240 offset:29184\n" \
"ds_read_b64_tr_b16 v[140:141], v242 offset:16384\n" \
"v_mfma_f32_16x16x32_f16 v[32:35], v[128:131], v[168:171], v[32:35]\n" \
"buffer_load_dwordx4 v232, s[36:39], s80 offen offset:1024 sc1 lds\n" \
"v_mfma_f32_16x16x32_f16 v[44:47], v[132:135], v[168:171], v[44:47]\n" \
"ds_read_b64_tr_b16 v[142:143], v242 offset:24576\n" \
"s_waitcnt lgkmcnt(7)\n" \
"v_mfma_f32_16x16x32_f16 v[48:51], v[120:123], v[176:179], v[48:51]\n" \
"ds_read_b128 v[168:171], v240 offset:20480\n" \
"ds_read_b64_tr_b16 v[144:145], v241 offset:16896\n" \
"v_mfma_f32_16x16x32_f16 v[60:63], v[124:127], v[176:179], v[60:63]\n" \
"s_waitcnt lgkmcnt(8)\n" \
"v_pk_add_f16 v176, v176, v180\n" \
"v_pk_add_f16 v177, v177, v181\n" \
"v_mfma_f32_16x16x32_f16 v[52:55], v[152:155], v[180:183], v[52:55]\n" \
"v_pk_add_f16 v178, v178, v182\n" \
"v_pk_add_f16 v179, v179, v183\n" \
"v_mfma_f32_16x16x32_f16 v[64:67], v[156:159], v[180:183], v[64:67]\n" \
"ds_read_b128 v[172:175], v240 offset:20992\n" \
"v_mfma_f32_16x16x32_f16 v[56:59], v[128:131], v[176:179], v[56:59]\n" \
"buffer_load_dwordx4 v232, s[36:39], s80 offen offset:2048 sc1 lds\n" \
"v_mfma_f32_16x16x32_f16 v[68:71], v[132:135], v[176:179], v[68:71]\n" \
"s_setprio 0\n" \
"ds_read_b64_tr_b16 v[146:147], v241 offset:25088\n" \
"ds_read_b64_tr_b16 v[148:149], v242 offset:16896\n" \
"s_waitcnt lgkmcnt(6)\n" \
"v_mfma_f32_16x16x32_f16 v[72:75], v[120:123], v[160:163], v[72:75]\n" \
"ds_read_b128 v[176:179], v240 offset:22528\n" \
"ds_read_b64_tr_b16 v[150:151], v242 offset:25088\n" \
"v_mfma_f32_16x16x32_f16 v[84:87], v[124:127], v[160:163], v[84:87]\n" \
"v_pk_add_f16 v160, v160, v164\n" \
"v_pk_add_f16 v161, v161, v165\n" \
"v_mfma_f32_16x16x32_f16 v[76:79], v[152:155], v[164:167], v[76:79]\n" \
"v_pk_add_f16 v162, v162, v166\n" \
"v_pk_add_f16 v163, v163, v167\n" \
"v_mfma_f32_16x16x32_f16 v[88:91], v[156:159], v[164:167], v[88:91]\n" \
"ds_read_b128 v[180:183], v240 offset:23040\n" \
"v_mfma_f32_16x16x32_f16 v[80:83], v[128:131], v[160:163], v[80:83]\n" \
"buffer_load_dwordx4 v232, s[36:39], s80 offen offset:3072 sc1 lds\n" \
"v_mfma_f32_16x16x32_f16 v[92:95], v[132:135], v[160:163], v[92:95]\n" \
"s_waitcnt lgkmcnt(6)\n" \
"v_mfma_f32_16x16x32_f16 v[96:99], v[120:123], v[168:171], v[96:99]\n" \
"v_pk_add_f16 v120, v136, v144\n" \
"v_pk_add_f16 v121, v137, v145\n" \
"ds_read_b128 v[160:163], v240 offset:24576\n" \
"v_mfma_f32_16x16x32_f16 v[108:111], v[124:127], v[168:171], v[108:111]\n" \
"s_waitcnt lgkmcnt(6)\n" \
"v_pk_add_f16 v168, v168, v172\n" \
"v_pk_add_f16 v169, v169, v173\n" \
"ds_read_b128 v[164:167], v240 offset:25088\n" \
"v_mfma_f32_16x16x32_f16 v[100:103], v[152:155], v[172:175], v[100:103]\n" \
"v_pk_add_f16 v170, v170, v174\n" \
"v_pk_add_f16 v171, v171, v175\n" \
"s_add_i32 m0, s45, 0x0\n" \
"s_add_i32 s81, s47, 0x5c00000\n" \
"v_mfma_f32_16x16x32_f16 v[112:115], v[156:159], v[172:175], v[112:115]\n" \
"s_waitcnt lgkmcnt(3)\n" \
"v_pk_add_f16 v122, v138, v146\n" \
"v_pk_add_f16 v123, v139, v147\n" \
"buffer_load_dwordx4 v232, s[36:39], s81 offen sc1 lds\n" \
"v_mfma_f32_16x16x32_f16 v[104:107], v[128:131], v[168:171], v[104:107]\n" \
"v_pk_add_f16 v124, v140, v148\n" \
"v_pk_add_f16 v125, v141, v149\n" \
"v_mfma_f32_16x16x32_f16 v[116:119], v[132:135], v[168:171], v[116:119]\n" \
"s_setprio 1\n" \
"v_pk_add_f16 v126, v142, v150\n" \
"v_pk_add_f16 v127, v143, v151\n" \
"s_waitcnt vmcnt(5) lgkmcnt(0)\n" \
"s_barrier\n" \
"v_mfma_f32_16x16x32_f16 v[0:3], v[136:139], v[176:179], v[0:3]\n" \
"ds_read_b128 v[168:171], v240 offset:47104\n" \
"v_mfma_f32_16x16x32_f16 v[12:15], v[140:143], v[176:179], v[12:15]\n" \
"v_pk_add_f16 v176, v176, v180\n" \
"v_pk_add_f16 v177, v177, v181\n" \
"v_mfma_f32_16x16x32_f16 v[4:7], v[144:147], v[180:183], v[4:7]\n" \
"v_pk_add_f16 v178, v178, v182\n" \
"v_pk_add_f16 v179, v179, v183\n" \
"v_mfma_f32_16x16x32_f16 v[16:19], v[148:151], v[180:183], v[16:19]\n" \
"ds_read_b128 v[172:175], v240 offset:47616\n" \
"s_add_i32 m0, s44, 0x5000\n" \
"s_add_i32 s80, s46, 0x6000000\n" \
"v_mfma_f32_16x16x32_f16 v[8:11], v[120:123], v[176:179], v[8:11]\n" \
"buffer_load_dwordx4 v232, s[36:39], s80 offen sc1 lds\n" \
"v_mfma_f32_16x16x32_f16 v[20:23], v[124:127], v[176:179], v[20:23]\n" \
"ds_read_b64_tr_b16 v[128:129], v241 offset:32768\n" \
"ds_read_b64_tr_b16 v[130:131], v241 offset:40960\n" \
"v_mfma_f32_16x16x32_f16 v[24:27], v[136:139], v[160:163], v[24:27]\n" \
"ds_read_b128 v[176:179], v240 offset:49152\n" \
"v_mfma_f32_16x16x32_f16 v[36:39], v[140:143], v[160:163], v[36:39]\n" \
"v_pk_add_f16 v160, v160, v164\n" \
"v_pk_add_f16 v161, v161, v165\n" \
"v_mfma_f32_16x16x32_f16 v[28:31], v[144:147], v[164:167], v[28:31]\n" \
"v_pk_add_f16 v162, v162, v166\n" \
"v_pk_add_f16 v163, v163, v167\n" \
"v_mfma_f32_16x16x32_f16 v[40:43], v[148:151], v[164:167], v[40:43]\n" \
"ds_read_b128 v[180:183], v240 offset:49664\n" \
"ds_read_b64_tr_b16 v[132:133], v242 offset:32768\n" \
"v_mfma_f32_16x16x32_f16 v[32:35], v[120:123], v[160:163], v[32:35]\n" \
"buffer_load_dwordx4 v232, s[36:39], s80 offen offset:1024 sc1 lds\n" \
"v_mfma_f32_16x16x32_f16 v[44:47], v[124:127], v[160:163], v[44:47]\n" \
"ds_read_b64_tr_b16 v[134:135], v242 offset:40960\n" \
"s_waitcnt lgkmcnt(7)\n" \
"v_mfma_f32_16x16x32_f16 v[48:51], v[136:139], v[168:171], v[48:51]\n" \
"ds_read_b128 v[160:163], v240 offset:40960\n" \
"ds_read_b64_tr_b16 v[152:153], v241 offset:33280\n" \
"v_mfma_f32_16x16x32_f16 v[60:63], v[140:143], v[168:171], v[60:63]\n" \
"s_waitcnt lgkmcnt(8)\n" \
"v_pk_add_f16 v168, v168, v172\n" \
"v_pk_add_f16 v169, v169, v173\n" \
"v_mfma_f32_16x16x32_f16 v[52:55], v[144:147], v[172:175], v[52:55]\n" \
"v_pk_add_f16 v170, v170, v174\n" \
"v_pk_add_f16 v171, v171, v175\n" \
"v_mfma_f32_16x16x32_f16 v[64:67], v[148:151], v[172:175], v[64:67]\n" \
"ds_read_b128 v[164:167], v240 offset:41472\n" \
"v_mfma_f32_16x16x32_f16 v[56:59], v[120:123], v[168:171], v[56:59]\n" \
"buffer_load_dwordx4 v232, s[36:39], s80 offen offset:2048 sc1 lds\n" \
"v_mfma_f32_16x16x32_f16 v[68:71], v[124:127], v[168:171], v[68:71]\n" \
"s_setprio 0\n" \
"ds_read_b64_tr_b16 v[154:155], v241 offset:41472\n" \
"ds_read_b64_tr_b16 v[156:157], v242 offset:33280\n" \
"s_waitcnt lgkmcnt(6)\n" \
"v_mfma_f32_16x16x32_f16 v[72:75], v[136:139], v[176:179], v[72:75]\n" \
"ds_read_b128 v[168:171], v240 offset:43008\n" \
"ds_read_b64_tr_b16 v[158:159], v242 offset:41472\n" \
"v_mfma_f32_16x16x32_f16 v[84:87], v[140:143], v[176:179], v[84:87]\n" \
"v_pk_add_f16 v176, v176, v180\n" \
"v_pk_add_f16 v177, v177, v181\n" \
"v_mfma_f32_16x16x32_f16 v[76:79], v[144:147], v[180:183], v[76:79]\n" \
"v_pk_add_f16 v178, v178, v182\n" \
"v_pk_add_f16 v179, v179, v183\n" \
"v_mfma_f32_16x16x32_f16 v[88:91], v[148:151], v[180:183], v[88:91]\n" \
"ds_read_b128 v[172:175], v240 offset:43520\n" \
"v_mfma_f32_16x16x32_f16 v[80:83], v[120:123], v[176:179], v[80:83]\n" \
"buffer_load_dwordx4 v232, s[36:39], s80 offen offset:3072 sc1 lds\n" \
"v_mfma_f32_16x16x32_f16 v[92:95], v[124:127], v[176:179], v[92:95]\n" \
"s_waitcnt lgkmcnt(6)\n" \
"v_mfma_f32_16x16x32_f16 v[96:99], v[136:139], v[160:163], v[96:99]\n" \
"v_pk_add_f16 v136, v128, v152\n" \
"v_pk_add_f16 v137, v129, v153\n" \
"ds_read_b128 v[176:179], v240 offset:45056\n" \
"v_mfma_f32_16x16x32_f16 v[108:111], v[140:143], v[160:163], v[108:111]\n" \
"s_waitcnt lgkmcnt(6)\n" \
"v_pk_add_f16 v160, v160, v164\n" \
"v_pk_add_f16 v161, v161, v165\n" \
"ds_read_b128 v[180:183], v240 offset:45568\n" \
"v_mfma_f32_16x16x32_f16 v[100:103], v[144:147], v[164:167], v[100:103]\n" \
"v_pk_add_f16 v162, v162, v166\n" \
"v_pk_add_f16 v163, v163, v167\n" \
"s_add_i32 m0, s45, 0x5000\n" \
"s_add_i32 s81, s47, 0x6000000\n" \
"v_mfma_f32_16x16x32_f16 v[112:115], v[148:151], v[164:167], v[112:115]\n" \
"s_waitcnt lgkmcnt(3)\n" \
"v_pk_add_f16 v138, v130, v154\n" \
"v_pk_add_f16 v139, v131, v155\n" \
"buffer_load_dwordx4 v232, s[36:39], s81 offen sc1 lds\n" \
"v_mfma_f32_16x16x32_f16 v[104:107], v[120:123], v[160:163], v[104:107]\n" \
"v_pk_add_f16 v140, v132, v156\n" \
"v_pk_add_f16 v141, v133, v157\n" \
"v_mfma_f32_16x16x32_f16 v[116:119], v[124:127], v[160:163], v[116:119]\n" \
"s_setprio 1\n" \
"v_pk_add_f16 v142, v134, v158\n" \
"v_pk_add_f16 v143, v135, v159\n" \
"s_waitcnt vmcnt(5) lgkmcnt(0)\n" \
"s_barrier\n" \
"v_mfma_f32_16x16x32_f16 v[0:3], v[128:131], v[168:171], v[0:3]\n" \
"ds_read_b128 v[160:163], v240 offset:6144\n" \
"v_mfma_f32_16x16x32_f16 v[12:15], v[132:135], v[168:171], v[12:15]\n" \
"v_pk_add_f16 v168, v168, v172\n" \
"v_pk_add_f16 v169, v169, v173\n" \
"v_mfma_f32_16x16x32_f16 v[4:7], v[152:155], v[172:175], v[4:7]\n" \
"v_pk_add_f16 v170, v170, v174\n" \
"v_pk_add_f16 v171, v171, v175\n" \
"v_mfma_f32_16x16x32_f16 v[16:19], v[156:159], v[172:175], v[16:19]\n" \
"ds_read_b128 v[164:167], v240 offset:6656\n" \
"s_add_i32 m0, s44, 0xa000\n" \
"s_add_i32 s80, s46, 0x6400000\n" \
"v_mfma_f32_16x16x32_f16 v[8:11], v[136:139], v[168:171], v[8:11]\n" \
"buffer_load_dwordx4 v232, s[36:39], s80 offen sc1 lds\n" \
"v_mfma_f32_16x16x32_f16 v[20:23], v[140:143], v[168:171], v[20:23]\n" \
"ds_read_b64_tr_b16 v[120:121], v241 offset:0\n" \
"ds_read_b64_tr_b16 v[122:123], v241 offset:8192\n" \
"v_mfma_f32_16x16x32_f16 v[24:27], v[128:131], v[176:179], v[24:27]\n" \
"ds_read_b128 v[168:171], v240 offset:8192\n" \
"v_mfma_f32_16x16x32_f16 v[36:39], v[132:135], v[176:179], v[36:39]\n" \
"v_pk_add_f16 v176, v176, v180\n" \
"v_pk_add_f16 v177, v177, v181\n" \
"v_mfma_f32_16x16x32_f16 v[28:31], v[152:155], v[180:183], v[28:31]\n" \
"v_pk_add_f16 v178, v178, v182\n" \
"v_pk_add_f16 v179, v179, v183\n" \
"v_mfma_f32_16x16x32_f16 v[40:43], v[156:159], v[180:183], v[40:43]\n" \
"ds_read_b128 v[172:175], v240 offset:8704\n" \
"ds_read_b64_tr_b16 v[124:125], v242 offset:0\n" \
"v_mfma_f32_16x16x32_f16 v[32:35], v[136:139], v[176:179], v[32:35]\n" \
"buffer_load_dwordx4 v232, s[36:39], s80 offen offset:1024 sc1 lds\n" \
"v_mfma_f32_16x16x32_f16 v[44:47], v[140:143], v[176:179], v[44:47]\n" \
"ds_read_b64_tr_b16 v[126:127], v242 offset:8192\n" \
"s_waitcnt lgkmcnt(7)\n" \
"v_mfma_f32_16x16x32_f16 v[48:51], v[128:131], v[160:163], v[48:51]\n" \
"ds_read_b128 v[176:179], v240 offset:0\n" \
"ds_read_b64_tr_b16 v[144:145], v241 offset:512\n" \
"v_mfma_f32_16x16x32_f16 v[60:63], v[132:135], v[160:163], v[60:63]\n" \
"s_waitcnt lgkmcnt(8)\n" \
"v_pk_add_f16 v160, v160, v164\n" \
"v_pk_add_f16 v161, v161, v165\n" \
"v_mfma_f32_16x16x32_f16 v[52:55], v[152:155], v[164:167], v[52:55]\n" \
"v_pk_add_f16 v162, v162, v166\n" \
"v_pk_add_f16 v163, v163, v167\n" \
"v_mfma_f32_16x16x32_f16 v[64:67], v[156:159], v[164:167], v[64:67]\n" \
"ds_read_b128 v[180:183], v240 offset:512\n" \
"v_mfma_f32_16x16x32_f16 v[56:59], v[136:139], v[160:163], v[56:59]\n" \
"buffer_load_dwordx4 v232, s[36:39], s80 offen offset:2048 sc1 lds\n" \
"v_mfma_f32_16x16x32_f16 v[68:71], v[140:143], v[160:163], v[68:71]\n" \
"s_setprio 0\n" \
"ds_read_b64_tr_b16 v[146:147], v241 offset:8704\n" \
"ds_read_b64_tr_b16 v[148:149], v242 offset:512\n" \
"s_waitcnt lgkmcnt(6)\n" \
"v_mfma_f32_16x16x32_f16 v[72:75], v[128:131], v[168:171], v[72:75]\n" \
"ds_read_b128 v[160:163], v240 offset:2048\n" \
"ds_read_b64_tr_b16 v[150:151], v242 offset:8704\n" \
"v_mfma_f32_16x16x32_f16 v[84:87], v[132:135], v[168:171], v[84:87]\n" \
"v_pk_add_f16 v168, v168, v172\n" \
"v_pk_add_f16 v169, v169, v173\n" \
"v_mfma_f32_16x16x32_f16 v[76:79], v[152:155], v[172:175], v[76:79]\n" \
"v_pk_add_f16 v170, v170, v174\n" \
"v_pk_add_f16 v171, v171, v175\n" \
"v_mfma_f32_16x16x32_f16 v[88:91], v[156:159], v[172:175], v[88:91]\n" \
"ds_read_b128 v[164:167], v240 offset:2560\n" \
"v_mfma_f32_16x16x32_f16 v[80:83], v[136:139], v[168:171], v[80:83]\n" \
"buffer_load_dwordx4 v232, s[36:39], s80 offen offset:3072 sc1 lds\n" \
"v_mfma_f32_16x16x32_f16 v[92:95], v[140:143], v[168:171], v[92:95]\n" \
"s_waitcnt lgkmcnt(6)\n" \
"v_mfma_f32_16x16x32_f16 v[96:99], v[128:131], v[176:179], v[96:99]\n" \
"v_pk_add_f16 v128, v120, v144\n" \
"v_pk_add_f16 v129, v121, v145\n" \
"ds_read_b128 v[168:171], v240 offset:4096\n" \
"v_mfma_f32_16x16x32_f16 v[108:111], v[132:135], v[176:179], v[108:111]\n" \
"s_waitcnt lgkmcnt(6)\n" \
"v_pk_add_f16 v176, v176, v180\n" \
"v_pk_add_f16 v177, v177, v181\n" \
"ds_read_b128 v[172:175], v240 offset:4608\n" \
"v_mfma_f32_16x16x32_f16 v[100:103], v[152:155], v[180:183], v[100:103]\n" \
"v_pk_add_f16 v178, v178, v182\n" \
"v_pk_add_f16 v179, v179, v183\n" \
"s_add_i32 m0, s45, 0xa000\n" \
"s_add_i32 s81, s47, 0x6400000\n" \
"v_mfma_f32_16x16x32_f16 v[112:115], v[156:159], v[180:183], v[112:115]\n" \
"s_waitcnt lgkmcnt(3)\n" \
"v_pk_add_f16 v130, v122, v146\n" \
"v_pk_add_f16 v131, v123, v147\n" \
"buffer_load_dwordx4 v232, s[36:39], s81 offen sc1 lds\n" \
"v_mfma_f32_16x16x32_f16 v[104:107], v[136:139], v[176:179], v[104:107]\n" \
"v_pk_add_f16 v132, v124, v148\n" \
"v_pk_add_f16 v133, v125, v149\n" \
"v_mfma_f32_16x16x32_f16 v[116:119], v[140:143], v[176:179], v[116:119]\n" \
"s_setprio 1\n" \
"v_pk_add_f16 v134, v126, v150\n" \
"v_pk_add_f16 v135, v127, v151\n" \
"s_waitcnt vmcnt(5) lgkmcnt(0)\n" \
"s_barrier\n" \
"v_mfma_f32_16x16x32_f16 v[0:3], v[120:123], v[160:163], v[0:3]\n" \
"ds_read_b128 v[176:179], v240 offset:26624\n" \
"v_mfma_f32_16x16x32_f16 v[12:15], v[124:127], v[160:163], v[12:15]\n" \
"v_pk_add_f16 v160, v160, v164\n" \
"v_pk_add_f16 v161, v161, v165\n" \
"v_mfma_f32_16x16x32_f16 v[4:7], v[144:147], v[164:167], v[4:7]\n" \
"v_pk_add_f16 v162, v162, v166\n" \
"v_pk_add_f16 v163, v163, v167\n" \
"v_mfma_f32_16x16x32_f16 v[16:19], v[148:151], v[164:167], v[16:19]\n" \
"ds_read_b128 v[180:183], v240 offset:27136\n" \
"s_add_i32 m0, s44, 0x0\n" \
"s_add_i32 s80, s46, 0x6800000\n" \
"v_mfma_f32_16x16x32_f16 v[8:11], v[128:131], v[160:163], v[8:11]\n" \
"buffer_load_dwordx4 v232, s[36:39], s80 offen sc1 lds\n" \
"v_mfma_f32_16x16x32_f16 v[20:23], v[132:135], v[160:163], v[20:23]\n" \
"ds_read_b64_tr_b16 v[136:137], v241 offset:16384\n" \
"ds_read_b64_tr_b16 v[138:139], v241 offset:24576\n" \
"v_mfma_f32_16x16x32_f16 v[24:27], v[120:123], v[168:171], v[24:27]\n" \
"ds_read_b128 v[160:163], v240 offset:28672\n" \
"v_mfma_f32_16x16x32_f16 v[36:39], v[124:127], v[168:171], v[36:39]\n" \
"v_pk_add_f16 v168, v168, v172\n" \
"v_pk_add_f16 v169, v169, v173\n" \
"v_mfma_f32_16x16x32_f16 v[28:31], v[144:147], v[172:175], v[28:31]\n" \
"v_pk_add_f16 v170, v170, v174\n" \
"v_pk_add_f16 v171, v171, v175\n" \
"v_mfma_f32_16x16x32_f16 v[40:43], v[148:151], v[172:175], v[40:43]\n" \
"ds_read_b128 v[164:167], v240 offset:29184\n" \
"ds_read_b64_tr_b16 v[140:141], v242 offset:16384\n" \
"v_mfma_f32_16x16x32_f16 v[32:35], v[128:131], v[168:171], v[32:35]\n" \
"buffer_load_dwordx4 v232, s[36:39], s80 offen offset:1024 sc1 lds\n" \
"v_mfma_f32_16x16x32_f16 v[44:47], v[132:135], v[168:171], v[44:47]\n" \
"ds_read_b64_tr_b16 v[142:143], v242 offset:24576\n" \
"s_waitcnt lgkmcnt(7)\n" \
"v_mfma_f32_16x16x32_f16 v[48:51], v[120:123], v[176:179], v[48:51]\n" \
"ds_read_b128 v[168:171], v240 offset:20480\n" \
"ds_read_b64_tr_b16 v[152:153], v241 offset:16896\n" \
"v_mfma_f32_16x16x32_f16 v[60:63], v[124:127], v[176:179], v[60:63]\n" \
"s_waitcnt lgkmcnt(8)\n" \
"v_pk_add_f16 v176, v176, v180\n" \
"v_pk_add_f16 v177, v177, v181\n" \
"v_mfma_f32_16x16x32_f16 v[52:55], v[144:147], v[180:183], v[52:55]\n" \
"v_pk_add_f16 v178, v178, v182\n" \
"v_pk_add_f16 v179, v179, v183\n" \
"v_mfma_f32_16x16x32_f16 v[64:67], v[148:151], v[180:183], v[64:67]\n" \
"ds_read_b128 v[172:175], v240 offset:20992\n" \
"v_mfma_f32_16x16x32_f16 v[56:59], v[128:131], v[176:179], v[56:59]\n" \
"buffer_load_dwordx4 v232, s[36:39], s80 offen offset:2048 sc1 lds\n" \
"v_mfma_f32_16x16x32_f16 v[68:71], v[132:135], v[176:179], v[68:71]\n" \
"s_setprio 0\n" \
"ds_read_b64_tr_b16 v[154:155], v241 offset:25088\n" \
"ds_read_b64_tr_b16 v[156:157], v242 offset:16896\n" \
"s_waitcnt lgkmcnt(6)\n" \
"v_mfma_f32_16x16x32_f16 v[72:75], v[120:123], v[160:163], v[72:75]\n" \
"ds_read_b128 v[176:179], v240 offset:22528\n" \
"ds_read_b64_tr_b16 v[158:159], v242 offset:25088\n" \
"v_mfma_f32_16x16x32_f16 v[84:87], v[124:127], v[160:163], v[84:87]\n" \
"v_pk_add_f16 v160, v160, v164\n" \
"v_pk_add_f16 v161, v161, v165\n" \
"v_mfma_f32_16x16x32_f16 v[76:79], v[144:147], v[164:167], v[76:79]\n" \
"v_pk_add_f16 v162, v162, v166\n" \
"v_pk_add_f16 v163, v163, v167\n" \
"v_mfma_f32_16x16x32_f16 v[88:91], v[148:151], v[164:167], v[88:91]\n" \
"ds_read_b128 v[180:183], v240 offset:23040\n" \
"v_mfma_f32_16x16x32_f16 v[80:83], v[128:131], v[160:163], v[80:83]\n" \
"buffer_load_dwordx4 v232, s[36:39], s80 offen offset:3072 sc1 lds\n" \
"v_mfma_f32_16x16x32_f16 v[92:95], v[132:135], v[160:163], v[92:95]\n" \
"s_waitcnt lgkmcnt(6)\n" \
"v_mfma_f32_16x16x32_f16 v[96:99], v[120:123], v[168:171], v[96:99]\n" \
"v_pk_add_f16 v120, v136, v152\n" \
"v_pk_add_f16 v121, v137, v153\n" \
"ds_read_b128 v[160:163], v240 offset:24576\n" \
"v_mfma_f32_16x16x32_f16 v[108:111], v[124:127], v[168:171], v[108:111]\n" \
"s_waitcnt lgkmcnt(6)\n" \
"v_pk_add_f16 v168, v168, v172\n" \
"v_pk_add_f16 v169, v169, v173\n" \
"ds_read_b128 v[164:167], v240 offset:25088\n" \
"v_mfma_f32_16x16x32_f16 v[100:103], v[144:147], v[172:175], v[100:103]\n" \
"v_pk_add_f16 v170, v170, v174\n" \
"v_pk_add_f16 v171, v171, v175\n" \
"s_add_i32 m0, s45, 0x0\n" \
"s_add_i32 s81, s47, 0x6800000\n" \
"v_mfma_f32_16x16x32_f16 v[112:115], v[148:151], v[172:175], v[112:115]\n" \
"s_waitcnt lgkmcnt(3)\n" \
"v_pk_add_f16 v122, v138, v154\n" \
"v_pk_add_f16 v123, v139, v155\n" \
"buffer_load_dwordx4 v232, s[36:39], s81 offen sc1 lds\n" \
"v_mfma_f32_16x16x32_f16 v[104:107], v[128:131], v[168:171], v[104:107]\n" \
"v_pk_add_f16 v124, v140, v156\n" \
"v_pk_add_f16 v125, v141, v157\n" \
"v_mfma_f32_16x16x32_f16 v[116:119], v[132:135], v[168:171], v[116:119]\n" \
"s_setprio 1\n" \
"v_pk_add_f16 v126, v142, v158\n" \
"v_pk_add_f16 v127, v143, v159\n" \
"s_waitcnt vmcnt(5) lgkmcnt(0)\n" \
"s_barrier\n" \
"v_mfma_f32_16x16x32_f16 v[0:3], v[136:139], v[176:179], v[0:3]\n" \
"ds_read_b128 v[168:171], v240 offset:47104\n" \
"v_mfma_f32_16x16x32_f16 v[12:15], v[140:143], v[176:179], v[12:15]\n" \
"v_pk_add_f16 v176, v176, v180\n" \
"v_pk_add_f16 v177, v177, v181\n" \
"v_mfma_f32_16x16x32_f16 v[4:7], v[152:155], v[180:183], v[4:7]\n" \
"v_pk_add_f16 v178, v178, v182\n" \
"v_pk_add_f16 v179, v179, v183\n" \
"s_add_i32 m0, s44, 0x5000\n" \
"s_add_i32 s80, s46, 0x6c00000\n" \
"v_mfma_f32_16x16x32_f16 v[16:19], v[156:159], v[180:183], v[16:19]\n" \
"ds_read_b128 v[172:175], v240 offset:47616\n" \
"buffer_load_dwordx4 v232, s[36:39], s80 offen sc1 lds\n" \
"v_mfma_f32_16x16x32_f16 v[8:11], v[120:123], v[176:179], v[8:11]\n" \
"ds_read_b64_tr_b16 v[128:129], v241 offset:32768\n" \
"v_mfma_f32_16x16x32_f16 v[20:23], v[124:127], v[176:179], v[20:23]\n" \
"ds_read_b64_tr_b16 v[130:131], v241 offset:40960\n" \
"v_mfma_f32_16x16x32_f16 v[24:27], v[136:139], v[160:163], v[24:27]\n" \
"ds_read_b128 v[176:179], v240 offset:49152\n" \
"buffer_load_dwordx4 v232, s[36:39], s80 offen offset:1024 sc1 lds\n" \
"v_mfma_f32_16x16x32_f16 v[36:39], v[140:143], v[160:163], v[36:39]\n" \
"v_pk_add_f16 v160, v160, v164\n" \
"v_pk_add_f16 v161, v161, v165\n" \
"v_mfma_f32_16x16x32_f16 v[28:31], v[152:155], v[164:167], v[28:31]\n" \
"v_pk_add_f16 v162, v162, v166\n" \
"v_pk_add_f16 v163, v163, v167\n" \
"v_mfma_f32_16x16x32_f16 v[40:43], v[156:159], v[164:167], v[40:43]\n" \
"ds_read_b128 v[180:183], v240 offset:49664\n" \
"buffer_load_dwordx4 v232, s[36:39], s80 offen offset:2048 sc1 lds\n" \
"v_mfma_f32_16x16x32_f16 v[32:35], v[120:123], v[160:163], v[32:35]\n" \
"ds_read_b64_tr_b16 v[132:133], v242 offset:32768\n" \
"v_mfma_f32_16x16x32_f16 v[44:47], v[124:127], v[160:163], v[44:47]\n" \
"ds_read_b64_tr_b16 v[134:135], v242 offset:40960\n" \
"s_waitcnt lgkmcnt(7)\n" \
"v_mfma_f32_16x16x32_f16 v[48:51], v[136:139], v[168:171], v[48:51]\n" \
"ds_read_b128 v[160:163], v240 offset:40960\n" \
"buffer_load_dwordx4 v232, s[36:39], s80 offen offset:3072 sc1 lds\n" \
"v_mfma_f32_16x16x32_f16 v[60:63], v[140:143], v[168:171], v[60:63]\n" \
"s_waitcnt lgkmcnt(6)\n" \
"v_pk_add_f16 v168, v168, v172\n" \
"v_pk_add_f16 v169, v169, v173\n" \
"ds_read_b64_tr_b16 v[144:145], v241 offset:33280\n" \
"v_mfma_f32_16x16x32_f16 v[52:55], v[152:155], v[172:175], v[52:55]\n" \
"v_pk_add_f16 v170, v170, v174\n" \
"v_pk_add_f16 v171, v171, v175\n" \
"s_add_i32 m0, s45, 0x5000\n" \
"s_add_i32 s81, s47, 0x6c00000\n" \
"v_mfma_f32_16x16x32_f16 v[64:67], v[156:159], v[172:175], v[64:67]\n" \
"ds_read_b128 v[164:167], v240 offset:41472\n" \
"buffer_load_dwordx4 v232, s[36:39], s81 offen sc1 lds\n" \
"v_mfma_f32_16x16x32_f16 v[56:59], v[120:123], v[168:171], v[56:59]\n" \
"ds_read_b64_tr_b16 v[146:147], v241 offset:41472\n" \
"ds_read_b64_tr_b16 v[148:149], v242 offset:33280\n" \
"v_mfma_f32_16x16x32_f16 v[68:71], v[124:127], v[168:171], v[68:71]\n" \
"s_setprio 0\n" \
"s_add_i32 m0, s52, 0x0\n" \
"s_add_i32 s80, s55, 0x0\n" \
"s_waitcnt lgkmcnt(5)\n" \
"v_mfma_f32_16x16x32_f16 v[72:75], v[136:139], v[176:179], v[72:75]\n" \
"ds_read_b128 v[168:171], v240 offset:43008\n" \
"buffer_load_dwordx4 v232, s[36:39], s80 offen sc1 lds\n" \
"v_mfma_f32_16x16x32_f16 v[84:87], v[140:143], v[176:179], v[84:87]\n" \
"v_pk_add_f16 v176, v176, v180\n" \
"v_pk_add_f16 v177, v177, v181\n" \
"ds_read_b64_tr_b16 v[150:151], v242 offset:41472\n" \
"v_mfma_f32_16x16x32_f16 v[76:79], v[152:155], v[180:183], v[76:79]\n" \
"v_pk_add_f16 v178, v178, v182\n" \
"v_pk_add_f16 v179, v179, v183\n" \
"v_mfma_f32_16x16x32_f16 v[88:91], v[156:159], v[180:183], v[88:91]\n" \
"ds_read_b128 v[172:175], v240 offset:43520\n" \
"buffer_load_dwordx4 v232, s[36:39], s80 offen offset:1024 sc1 lds\n" \
"v_mfma_f32_16x16x32_f16 v[80:83], v[120:123], v[176:179], v[80:83]\n" \
"v_mfma_f32_16x16x32_f16 v[92:95], v[124:127], v[176:179], v[92:95]\n" \
"s_waitcnt lgkmcnt(5)\n" \
"v_mfma_f32_16x16x32_f16 v[96:99], v[136:139], v[160:163], v[96:99]\n" \
"v_pk_add_f16 v136, v128, v144\n" \
"v_pk_add_f16 v137, v129, v145\n" \
"ds_read_b128 v[176:179], v240 offset:45056\n" \
"buffer_load_dwordx4 v232, s[36:39], s80 offen offset:2048 sc1 lds\n" \
"v_mfma_f32_16x16x32_f16 v[108:111], v[140:143], v[160:163], v[108:111]\n" \
"v_pk_add_f16 v160, v160, v164\n" \
"v_pk_add_f16 v161, v161, v165\n" \
"ds_read_b128 v[180:183], v240 offset:45568\n" \
"v_mfma_f32_16x16x32_f16 v[100:103], v[152:155], v[164:167], v[100:103]\n" \
"v_pk_add_f16 v162, v162, v166\n" \
"v_pk_add_f16 v163, v163, v167\n" \
"v_mfma_f32_16x16x32_f16 v[112:115], v[156:159], v[164:167], v[112:115]\n" \
"s_waitcnt lgkmcnt(4)\n" \
"v_pk_add_f16 v138, v130, v146\n" \
"v_pk_add_f16 v139, v131, v147\n" \
"buffer_load_dwordx4 v232, s[36:39], s80 offen offset:3072 sc1 lds\n" \
"v_mfma_f32_16x16x32_f16 v[104:107], v[120:123], v[160:163], v[104:107]\n" \
"v_pk_add_f16 v140, v132, v148\n" \
"v_pk_add_f16 v141, v133, v149\n" \
"v_mfma_f32_16x16x32_f16 v[116:119], v[124:127], v[160:163], v[116:119]\n" \
"s_setprio 1\n" \
"s_waitcnt lgkmcnt(2)\n" \
"v_pk_add_f16 v142, v134, v150\n" \
"v_pk_add_f16 v143, v135, v151\n" \
"s_waitcnt vmcnt(9) lgkmcnt(0)\n" \
"s_barrier\n" \
"v_mfma_f32_16x16x32_f16 v[0:3], v[128:131], v[168:171], v[0:3]\n" \
"ds_read_b128 v[160:163], v240 offset:6144\n" \
"v_mfma_f32_16x16x32_f16 v[12:15], v[132:135], v[168:171], v[12:15]\n" \
"v_pk_add_f16 v168, v168, v172\n" \
"v_pk_add_f16 v169, v169, v173\n" \
"v_mfma_f32_16x16x32_f16 v[4:7], v[144:147], v[172:175], v[4:7]\n" \
"v_pk_add_f16 v170, v170, v174\n" \
"v_pk_add_f16 v171, v171, v175\n" \
"v_mfma_f32_16x16x32_f16 v[16:19], v[148:151], v[172:175], v[16:19]\n" \
"ds_read_b128 v[164:167], v240 offset:6656\n" \
"s_add_i32 m0, s44, 0xa000\n" \
"s_add_i32 s80, s46, 0x7000000\n" \
"v_mfma_f32_16x16x32_f16 v[8:11], v[136:139], v[168:171], v[8:11]\n" \
"buffer_load_dwordx4 v232, s[36:39], s80 offen sc1 lds\n" \
"v_mfma_f32_16x16x32_f16 v[20:23], v[140:143], v[168:171], v[20:23]\n" \
"ds_read_b64_tr_b16 v[120:121], v241 offset:0\n" \
"ds_read_b64_tr_b16 v[122:123], v241 offset:8192\n" \
"v_mfma_f32_16x16x32_f16 v[24:27], v[128:131], v[176:179], v[24:27]\n" \
"ds_read_b128 v[168:171], v240 offset:8192\n" \
"v_mfma_f32_16x16x32_f16 v[36:39], v[132:135], v[176:179], v[36:39]\n" \
"v_pk_add_f16 v176, v176, v180\n" \
"v_pk_add_f16 v177, v177, v181\n" \
"v_mfma_f32_16x16x32_f16 v[28:31], v[144:147], v[180:183], v[28:31]\n" \
"v_pk_add_f16 v178, v178, v182\n" \
"v_pk_add_f16 v179, v179, v183\n" \
"v_mfma_f32_16x16x32_f16 v[40:43], v[148:151], v[180:183], v[40:43]\n" \
"ds_read_b128 v[172:175], v240 offset:8704\n" \
"ds_read_b64_tr_b16 v[124:125], v242 offset:0\n" \
"v_mfma_f32_16x16x32_f16 v[32:35], v[136:139], v[176:179], v[32:35]\n" \
"buffer_load_dwordx4 v232, s[36:39], s80 offen offset:1024 sc1 lds\n" \
"v_mfma_f32_16x16x32_f16 v[44:47], v[140:143], v[176:179], v[44:47]\n" \
"ds_read_b64_tr_b16 v[126:127], v242 offset:8192\n" \
"s_waitcnt lgkmcnt(7)\n" \
"v_mfma_f32_16x16x32_f16 v[48:51], v[128:131], v[160:163], v[48:51]\n" \
"ds_read_b128 v[176:179], v240 offset:0\n" \
"ds_read_b64_tr_b16 v[152:153], v241 offset:512\n" \
"v_mfma_f32_16x16x32_f16 v[60:63], v[132:135], v[160:163], v[60:63]\n" \
"s_waitcnt lgkmcnt(8)\n" \
"v_pk_add_f16 v160, v160, v164\n" \
"v_pk_add_f16 v161, v161, v165\n" \
"v_mfma_f32_16x16x32_f16 v[52:55], v[144:147], v[164:167], v[52:55]\n" \
"v_pk_add_f16 v162, v162, v166\n" \
"v_pk_add_f16 v163, v163, v167\n" \
"v_mfma_f32_16x16x32_f16 v[64:67], v[148:151], v[164:167], v[64:67]\n" \
"ds_read_b128 v[180:183], v240 offset:512\n" \
"v_mfma_f32_16x16x32_f16 v[56:59], v[136:139], v[160:163], v[56:59]\n" \
"buffer_load_dwordx4 v232, s[36:39], s80 offen offset:2048 sc1 lds\n" \
"v_mfma_f32_16x16x32_f16 v[68:71], v[140:143], v[160:163], v[68:71]\n" \
"s_setprio 0\n" \
"ds_read_b64_tr_b16 v[154:155], v241 offset:8704\n" \
"ds_read_b64_tr_b16 v[156:157], v242 offset:512\n" \
"s_waitcnt lgkmcnt(6)\n" \
"v_mfma_f32_16x16x32_f16 v[72:75], v[128:131], v[168:171], v[72:75]\n" \
"ds_read_b128 v[160:163], v240 offset:2048\n" \
"ds_read_b64_tr_b16 v[158:159], v242 offset:8704\n" \
"v_mfma_f32_16x16x32_f16 v[84:87], v[132:135], v[168:171], v[84:87]\n" \
"v_pk_add_f16 v168, v168, v172\n" \
"v_pk_add_f16 v169, v169, v173\n" \
"v_mfma_f32_16x16x32_f16 v[76:79], v[144:147], v[172:175], v[76:79]\n" \
"v_pk_add_f16 v170, v170, v174\n" \
"v_pk_add_f16 v171, v171, v175\n" \
"v_mfma_f32_16x16x32_f16 v[88:91], v[148:151], v[172:175], v[88:91]\n" \
"ds_read_b128 v[164:167], v240 offset:2560\n" \
"v_mfma_f32_16x16x32_f16 v[80:83], v[136:139], v[168:171], v[80:83]\n" \
"buffer_load_dwordx4 v232, s[36:39], s80 offen offset:3072 sc1 lds\n" \
"v_mfma_f32_16x16x32_f16 v[92:95], v[140:143], v[168:171], v[92:95]\n" \
"s_waitcnt lgkmcnt(6)\n" \
"v_mfma_f32_16x16x32_f16 v[96:99], v[128:131], v[176:179], v[96:99]\n" \
"v_pk_add_f16 v128, v120, v152\n" \
"v_pk_add_f16 v129, v121, v153\n" \
"ds_read_b128 v[168:171], v240 offset:4096\n" \
"v_mfma_f32_16x16x32_f16 v[108:111], v[132:135], v[176:179], v[108:111]\n" \
"s_waitcnt lgkmcnt(6)\n" \
"v_pk_add_f16 v176, v176, v180\n" \
"v_pk_add_f16 v177, v177, v181\n" \
"ds_read_b128 v[172:175], v240 offset:4608\n" \
"v_mfma_f32_16x16x32_f16 v[100:103], v[144:147], v[180:183], v[100:103]\n" \
"v_pk_add_f16 v178, v178, v182\n" \
"v_pk_add_f16 v179, v179, v183\n" \
"s_add_i32 m0, s45, 0xa000\n" \
"s_add_i32 s81, s47, 0x7000000\n" \
"v_mfma_f32_16x16x32_f16 v[112:115], v[148:151], v[180:183], v[112:115]\n" \
"s_waitcnt lgkmcnt(3)\n" \
"v_pk_add_f16 v130, v122, v154\n" \
"v_pk_add_f16 v131, v123, v155\n" \
"buffer_load_dwordx4 v232, s[36:39], s81 offen sc1 lds\n" \
"v_mfma_f32_16x16x32_f16 v[104:107], v[136:139], v[176:179], v[104:107]\n" \
"v_pk_add_f16 v132, v124, v156\n" \
"v_pk_add_f16 v133, v125, v157\n" \
"v_mfma_f32_16x16x32_f16 v[116:119], v[140:143], v[176:179], v[116:119]\n" \
"s_setprio 1\n" \
"v_pk_add_f16 v134, v126, v158\n" \
"v_pk_add_f16 v135, v127, v159\n" \
"s_waitcnt vmcnt(9) lgkmcnt(0)\n" \
"s_barrier\n" \
"v_mfma_f32_16x16x32_f16 v[0:3], v[120:123], v[160:163], v[0:3]\n" \
"ds_read_b128 v[176:179], v240 offset:26624\n" \
"v_mfma_f32_16x16x32_f16 v[12:15], v[124:127], v[160:163], v[12:15]\n" \
"v_pk_add_f16 v160, v160, v164\n" \
"v_pk_add_f16 v161, v161, v165\n" \
"v_mfma_f32_16x16x32_f16 v[4:7], v[152:155], v[164:167], v[4:7]\n" \
"v_pk_add_f16 v162, v162, v166\n" \
"v_pk_add_f16 v163, v163, v167\n" \
"v_mfma_f32_16x16x32_f16 v[16:19], v[156:159], v[164:167], v[16:19]\n" \
"ds_read_b128 v[180:183], v240 offset:27136\n" \
"s_add_i32 m0, s44, 0x0\n" \
"s_add_i32 s80, s46, 0x7400000\n" \
"s_bitcmp1_b32 s46, 22\n" \
"s_cselect_b32 s80, 0x80000000, s80\n" \
"v_mfma_f32_16x16x32_f16 v[8:11], v[128:131], v[160:163], v[8:11]\n" \
"buffer_load_dwordx4 v232, s[36:39], s80 offen sc1 lds\n" \
"v_mfma_f32_16x16x32_f16 v[20:23], v[132:135], v[160:163], v[20:23]\n" \
"ds_read_b64_tr_b16 v[136:137], v241 offset:16384\n" \
"ds_read_b64_tr_b16 v[138:139], v241 offset:24576\n" \
"v_mfma_f32_16x16x32_f16 v[24:27], v[120:123], v[168:171], v[24:27]\n" \
"ds_read_b128 v[160:163], v240 offset:28672\n" \
"v_mfma_f32_16x16x32_f16 v[36:39], v[124:127], v[168:171], v[36:39]\n" \
"v_pk_add_f16 v168, v168, v172\n" \
"v_pk_add_f16 v169, v169, v173\n" \
"v_mfma_f32_16x16x32_f16 v[28:31], v[152:155], v[172:175], v[28:31]\n" \
"v_pk_add_f16 v170, v170, v174\n" \
"v_pk_add_f16 v171, v171, v175\n" \
"v_mfma_f32_16x16x32_f16 v[40:43], v[156:159], v[172:175], v[40:43]\n" \
"ds_read_b128 v[164:167], v240 offset:29184\n" \
"ds_read_b64_tr_b16 v[140:141], v242 offset:16384\n" \
"v_mfma_f32_16x16x32_f16 v[32:35], v[128:131], v[168:171], v[32:35]\n" \
"buffer_load_dwordx4 v232, s[36:39], s80 offen offset:1024 sc1 lds\n" \
"v_mfma_f32_16x16x32_f16 v[44:47], v[132:135], v[168:171], v[44:47]\n" \
"ds_read_b64_tr_b16 v[142:143], v242 offset:24576\n" \
"s_waitcnt lgkmcnt(6)\n" \
"v_mfma_f32_16x16x32_f16 v[48:51], v[120:123], v[176:179], v[48:51]\n" \
"ds_read_b128 v[168:171], v240 offset:20480\n" \
"ds_read_b64_tr_b16 v[144:145], v241 offset:16896\n" \
"v_mfma_f32_16x16x32_f16 v[60:63], v[124:127], v[176:179], v[60:63]\n" \
"v_pk_add_f16 v176, v176, v180\n" \
"v_pk_add_f16 v177, v177, v181\n" \
"v_mfma_f32_16x16x32_f16 v[52:55], v[152:155], v[180:183], v[52:55]\n" \
"v_pk_add_f16 v178, v178, v182\n" \
"v_pk_add_f16 v179, v179, v183\n" \
"v_mfma_f32_16x16x32_f16 v[64:67], v[156:159], v[180:183], v[64:67]\n" \
"ds_read_b128 v[172:175], v240 offset:20992\n" \
"v_mfma_f32_16x16x32_f16 v[56:59], v[128:131], v[176:179], v[56:59]\n" \
"buffer_load_dwordx4 v232, s[36:39], s80 offen offset:2048 sc1 lds\n" \
"v_mfma_f32_16x16x32_f16 v[68:71], v[132:135], v[176:179], v[68:71]\n" \
"s_setprio 0\n" \
"ds_read_b64_tr_b16 v[146:147], v241 offset:25088\n" \
"ds_read_b64_tr_b16 v[148:149], v242 offset:16896\n" \
"s_waitcnt lgkmcnt(7)\n" \
"v_mfma_f32_16x16x32_f16 v[72:75], v[120:123], v[160:163], v[72:75]\n" \
"ds_read_b128 v[176:179], v240 offset:22528\n" \
"ds_read_b64_tr_b16 v[150:151], v242 offset:25088\n" \
"v_mfma_f32_16x16x32_f16 v[84:87], v[124:127], v[160:163], v[84:87]\n" \
"v_pk_add_f16 v160, v160, v164\n" \
"v_pk_add_f16 v161, v161, v165\n" \
"v_mfma_f32_16x16x32_f16 v[76:79], v[152:155], v[164:167], v[76:79]\n" \
"v_pk_add_f16 v162, v162, v166\n" \
"v_pk_add_f16 v163, v163, v167\n" \
"v_add_u32 v243, 0x1b000, v241\n" \
"v_mfma_f32_16x16x32_f16 v[88:91], v[156:159], v[164:167], v[88:91]\n" \
"v_xor_b32 v244, 32, v243\n" \
"ds_read_b128 v[180:183], v240 offset:23040\n" \
"v_mfma_f32_16x16x32_f16 v[80:83], v[128:131], v[160:163], v[80:83]\n" \
"buffer_load_dwordx4 v232, s[36:39], s80 offen offset:3072 sc1 lds\n" \
"v_mfma_f32_16x16x32_f16 v[92:95], v[132:135], v[160:163], v[92:95]\n" \
"v_add_u32 v246, 0x15000, v240\n" \
"s_waitcnt lgkmcnt(5)\n" \
"v_mfma_f32_16x16x32_f16 v[96:99], v[120:123], v[168:171], v[96:99]\n" \
"v_pk_add_f16 v120, v136, v144\n" \
"v_pk_add_f16 v121, v137, v145\n" \
"ds_read_b128 v[160:163], v240 offset:24576\n" \
"v_mfma_f32_16x16x32_f16 v[108:111], v[124:127], v[168:171], v[108:111]\n" \
"v_pk_add_f16 v168, v168, v172\n" \
"v_pk_add_f16 v169, v169, v173\n" \
"ds_read_b128 v[164:167], v240 offset:25088\n" \
"v_mfma_f32_16x16x32_f16 v[100:103], v[152:155], v[172:175], v[100:103]\n" \
"v_pk_add_f16 v170, v170, v174\n" \
"v_pk_add_f16 v171, v171, v175\n" \
"s_add_i32 m0, s45, 0x0\n" \
"s_add_i32 s81, s47, 0x7400000\n" \
"v_mfma_f32_16x16x32_f16 v[112:115], v[156:159], v[172:175], v[112:115]\n" \
"s_waitcnt lgkmcnt(3)\n" \
"v_pk_add_f16 v122, v138, v146\n" \
"v_pk_add_f16 v123, v139, v147\n" \
"buffer_load_dwordx4 v232, s[36:39], s81 offen sc1 lds\n" \
"v_mfma_f32_16x16x32_f16 v[104:107], v[128:131], v[168:171], v[104:107]\n" \
"v_pk_add_f16 v124, v140, v148\n" \
"v_pk_add_f16 v125, v141, v149\n" \
"s_mov_b32 m0, 0x27c00\n" \
"v_mfma_f32_16x16x32_f16 v[116:119], v[132:135], v[168:171], v[116:119]\n" \
"v_pk_add_f16 v126, v142, v150\n" \
"v_pk_add_f16 v127, v143, v151\n" \
"global_load_lds_dword v[248:249], off\n" \
"s_waitcnt vmcnt(6) lgkmcnt(0)\n" \
"s_barrier\n" \
"v_mfma_f32_16x16x32_f16 v[0:3], v[136:139], v[176:179], v[0:3]\n" \
"ds_read_b128 v[168:171], v240 offset:43008\n" \
"s_add_i32 m0, s44, 0x5000\n" \
"s_add_i32 s80, s46, 0x7800000\n" \
"s_bitcmp1_b32 s46, 22\n" \
"s_cselect_b32 s80, 0x80000000, s80\n" \
"ds_read_b64_tr_b16 v[128:129], v241 offset:32768\n" \
"v_mfma_f32_16x16x32_f16 v[12:15], v[140:143], v[176:179], v[12:15]\n" \
"v_pk_add_f16 v176, v176, v180\n" \
"v_pk_add_f16 v177, v177, v181\n" \
"buffer_load_dwordx4 v232, s[36:39], s80 offen sc1 lds\n" \
"ds_read_b64_tr_b16 v[130:131], v241 offset:40960\n" \
"v_mfma_f32_16x16x32_f16 v[4:7], v[144:147], v[180:183], v[4:7]\n" \
"v_pk_add_f16 v178, v178, v182\n" \
"v_pk_add_f16 v179, v179, v183\n" \
"buffer_load_dwordx4 v232, s[36:39], s80 offen offset:1024 sc1 lds\n" \
"ds_read_b64_tr_b16 v[132:133], v242 offset:32768\n" \
"v_mfma_f32_16x16x32_f16 v[16:19], v[148:151], v[180:183], v[16:19]\n" \
"ds_read_b128 v[172:175], v240 offset:43520\n" \
"buffer_load_dwordx4 v232, s[36:39], s80 offen offset:2048 sc1 lds\n" \
"ds_read_b64_tr_b16 v[134:135], v242 offset:40960\n" \
"ds_read_b64_tr_b16 v[152:153], v241 offset:33280\n" \
"v_mfma_f32_16x16x32_f16 v[8:11], v[120:123], v[176:179], v[8:11]\n" \
"buffer_load_dwordx4 v232, s[36:39], s80 offen offset:3072 sc1 lds\n" \
"ds_read_b64_tr_b16 v[154:155], v241 offset:41472\n" \
"v_mfma_f32_16x16x32_f16 v[20:23], v[124:127], v[176:179], v[20:23]\n" \
"s_add_i32 m0, s45, 0x5000\n" \
"s_add_i32 s81, s47, 0x7800000\n" \
"ds_read_b64_tr_b16 v[156:157], v242 offset:33280\n" \
"ds_read_b64_tr_b16 v[158:159], v242 offset:41472\n" \
"v_mfma_f32_16x16x32_f16 v[24:27], v[136:139], v[160:163], v[24:27]\n" \
"ds_read_b128 v[176:179], v246 offset:2048\n" \
"buffer_load_dwordx4 v232, s[36:39], s81 offen sc1 lds\n" \
"ds_read_b64_tr_b16 v[184:185], v243 offset:0\n" \
"v_mfma_f32_16x16x32_f16 v[36:39], v[140:143], v[160:163], v[36:39]\n" \
"v_pk_add_f16 v160, v160, v164\n" \
"v_pk_add_f16 v161, v161, v165\n" \
"ds_read_b64_tr_b16 v[186:187], v243 offset:8192\n" \
"ds_read_b64_tr_b16 v[188:189], v244 offset:0\n" \
"v_mfma_f32_16x16x32_f16 v[28:31], v[144:147], v[164:167], v[28:31]\n" \
"v_pk_add_f16 v162, v162, v166\n" \
"v_pk_add_f16 v163, v163, v167\n" \
"s_add_i32 m0, s44, 0xffff7000\n" \
"s_add_i32 s80, s46, 0x7c00000\n" \
"s_bitcmp0_b32 s46, 22\n" \
"s_cselect_b64 exec, -1, 0\n" \
"buffer_load_dwordx4 v232, s[36:39], s80 offen sc1 lds\n" \
"buffer_load_dwordx4 v232, s[36:39], s80 offen offset:1024 sc1 lds\n" \
"buffer_load_dwordx4 v232, s[36:39], s80 offen offset:2048 sc1 lds\n" \
"buffer_load_dwordx4 v232, s[36:39], s80 offen offset:3072 sc1 lds\n" \
"s_mov_b64 exec, -1\n" \
"v_mfma_f32_16x16x32_f16 v[40:43], v[148:151], v[164:167], v[40:43]\n" \
"ds_read_b128 v[180:183], v246 offset:2560\n" \
"s_add_i32 m0, s45, 0xffff7000\n" \
"s_add_i32 s81, s47, 0x7c00000\n" \
"ds_read_b64_tr_b16 v[190:191], v244 offset:8192\n" \
"ds_read_b64_tr_b16 v[192:193], v243 offset:512\n" \
"v_mfma_f32_16x16x32_f16 v[32:35], v[120:123], v[160:163], v[32:35]\n" \
"buffer_load_dwordx4 v232, s[36:39], s81 offen sc1 lds\n" \
"ds_read_b64_tr_b16 v[194:195], v243 offset:8704\n" \
"v_mfma_f32_16x16x32_f16 v[44:47], v[124:127], v[160:163], v[44:47]\n" \
"s_waitcnt lgkmcnt(5)\n" \
"v_pk_add_f16 v232, v128, v152\n" \
"v_pk_add_f16 v233, v129, v153\n" \
"ds_read_b64_tr_b16 v[196:197], v244 offset:512\n" \
"ds_read_b64_tr_b16 v[198:199], v244 offset:8704\n" \
"v_mfma_f32_16x16x32_f16 v[0:3], v[128:131], v[168:171], v[0:3]\n" \
"v_pk_add_f16 v234, v130, v154\n" \
"ds_read_b128 v[160:163], v246 offset:10240\n" \
"ds_read_b64_tr_b16 v[208:209], v243 offset:16384\n" \
"ds_read_b64_tr_b16 v[210:211], v243 offset:24576\n" \
"v_mfma_f32_16x16x32_f16 v[12:15], v[132:135], v[168:171], v[12:15]\n" \
"v_pk_add_f16 v168, v168, v172\n" \
"v_pk_add_f16 v169, v169, v173\n" \
"v_pk_add_f16 v235, v131, v155\n" \
"v_pk_add_f16 v236, v132, v156\n" \
"ds_read_b64_tr_b16 v[212:213], v244 offset:16384\n" \
"v_mfma_f32_16x16x32_f16 v[4:7], v[152:155], v[172:175], v[4:7]\n" \
"v_pk_add_f16 v170, v170, v174\n" \
"v_pk_add_f16 v171, v171, v175\n" \
"v_pk_add_f16 v237, v133, v157\n" \
"v_pk_add_f16 v238, v134, v158\n" \
"v_pk_add_f16 v239, v135, v159\n" \
"v_mfma_f32_16x16x32_f16 v[16:19], v[156:159], v[172:175], v[16:19]\n" \
"s_waitcnt lgkmcnt(6)\n" \
"v_pk_add_f16 v200, v184, v192\n" \
"ds_read_b128 v[164:167], v246 offset:10752\n" \
"ds_read_b64_tr_b16 v[214:215], v244 offset:24576\n" \
"ds_read_b64_tr_b16 v[216:217], v243 offset:16896\n" \
"v_mfma_f32_16x16x32_f16 v[8:11], v[232:235], v[168:171], v[8:11]\n" \
"v_pk_add_f16 v201, v185, v193\n" \
"v_pk_add_f16 v202, v186, v194\n" \
"ds_read_b64_tr_b16 v[218:219], v243 offset:25088\n" \
"v_mfma_f32_16x16x32_f16 v[20:23], v[236:239], v[168:171], v[20:23]\n" \
"ds_read_b64_tr_b16 v[220:221], v244 offset:16896\n" \
"ds_read_b64_tr_b16 v[222:223], v244 offset:25088\n" \
"v_mfma_f32_16x16x32_f16 v[0:3], v[184:187], v[176:179], v[0:3]\n" \
"v_pk_add_f16 v203, v187, v195\n" \
"s_waitcnt lgkmcnt(7)\n" \
"v_pk_add_f16 v204, v188, v196\n" \
"ds_read_b128 v[168:171], v240 offset:45056\n" \
"v_mfma_f32_16x16x32_f16 v[12:15], v[188:191], v[176:179], v[12:15]\n" \
"v_pk_add_f16 v176, v176, v180\n" \
"v_pk_add_f16 v177, v177, v181\n" \
"v_pk_add_f16 v205, v189, v197\n" \
"v_mfma_f32_16x16x32_f16 v[4:7], v[192:195], v[180:183], v[4:7]\n" \
"v_pk_add_f16 v178, v178, v182\n" \
"v_pk_add_f16 v179, v179, v183\n" \
"v_pk_add_f16 v206, v190, v198\n" \
"v_pk_add_f16 v207, v191, v199\n" \
"v_mfma_f32_16x16x32_f16 v[16:19], v[196:199], v[180:183], v[16:19]\n" \
"s_waitcnt lgkmcnt(4)\n" \
"v_pk_add_f16 v224, v208, v216\n" \
"ds_read_b128 v[172:175], v240 offset:45568\n" \
"v_mfma_f32_16x16x32_f16 v[8:11], v[200:203], v[176:179], v[8:11]\n" \
"v_pk_add_f16 v225, v209, v217\n" \
"v_mfma_f32_16x16x32_f16 v[20:23], v[204:207], v[176:179], v[20:23]\n" \
"s_waitcnt lgkmcnt(4)\n" \
"v_pk_add_f16 v226, v210, v218\n" \
"v_pk_add_f16 v227, v211, v219\n" \
"v_mfma_f32_16x16x32_f16 v[0:3], v[208:211], v[160:163], v[0:3]\n" \
"s_waitcnt lgkmcnt(2)\n" \
"v_pk_add_f16 v228, v212, v220\n" \
"v_pk_add_f16 v229, v213, v221\n" \
"ds_read_b128 v[176:179], v246 offset:4096\n" \
"v_mfma_f32_16x16x32_f16 v[12:15], v[212:215], v[160:163], v[12:15]\n" \
"v_pk_add_f16 v160, v160, v164\n" \
"v_pk_add_f16 v161, v161, v165\n" \
"v_pk_add_f16 v230, v214, v222\n" \
"v_mfma_f32_16x16x32_f16 v[4:7], v[216:219], v[164:167], v[4:7]\n" \
"v_pk_add_f16 v162, v162, v166\n" \
"v_pk_add_f16 v163, v163, v167\n" \
"v_pk_add_f16 v231, v215, v223\n" \
"v_mfma_f32_16x16x32_f16 v[16:19], v[220:223], v[164:167], v[16:19]\n" \
"ds_read_b128 v[180:183], v246 offset:4608\n" \
"s_mov_b32 vcc_lo, 0xaaaaaaaa\n" \
"v_mfma_f32_16x16x32_f16 v[8:11], v[224:227], v[160:163], v[8:11]\n" \
"s_mov_b32 vcc_hi, 0xaaaaaaaa\n" \
"v_mfma_f32_16x16x32_f16 v[20:23], v[228:231], v[160:163], v[20:23]\n" \
"v_add_u32 v241, 0xffff7000, v240\n" \
"s_waitcnt lgkmcnt(2)\n" \
"v_mfma_f32_16x16x32_f16 v[24:27], v[128:131], v[168:171], v[24:27]\n" \
"ds_read_b128 v[160:163], v246 offset:12288\n" \
"v_mfma_f32_16x16x32_f16 v[36:39], v[132:135], v[168:171], v[36:39]\n" \
"v_pk_add_f16 v168, v168, v172\n" \
"v_pk_add_f16 v169, v169, v173\n" \
"v_mfma_f32_16x16x32_f16 v[28:31], v[152:155], v[172:175], v[28:31]\n" \
"v_pk_add_f16 v170, v170, v174\n" \
"v_pk_add_f16 v171, v171, v175\n" \
"v_add_f32 v252, v4, v8\n" \
"v_add_f32 v253, v5, v9\n" \
"v_add_f32 v254, v6, v10\n" \
"v_add_f32 v255, v7, v11\n" \
"v_mfma_f32_16x16x32_f16 v[40:43], v[156:159], v[172:175], v[40:43]\n" \
"ds_read_b128 v[164:167], v246 offset:12800\n" \
"v_fmac_f32 v4, s56, v8\n" \
"v_fmac_f32 v5, s56, v9\n" \
"v_fmac_f32 v6, s56, v10\n" \
"v_fmac_f32 v7, s56, v11\n" \
"v_mfma_f32_16x16x32_f16 v[32:35], v[232:235], v[168:171], v[32:35]\n" \
"v_fmac_f32 v0, s57, v252\n" \
"v_fmac_f32 v1, s57, v253\n" \
"v_fmac_f32 v2, s57, v254\n" \
"v_fmac_f32 v3, s57, v255\n" \
"v_add_f32 v252, v16, v20\n" \
"v_mfma_f32_16x16x32_f16 v[44:47], v[236:239], v[168:171], v[44:47]\n" \
"v_add_f32 v253, v17, v21\n" \
"v_add_f32 v254, v18, v22\n" \
"v_add_f32 v255, v19, v23\n" \
"v_fmac_f32 v16, s56, v20\n" \
"s_waitcnt lgkmcnt(1)\n" \
"v_mfma_f32_16x16x32_f16 v[24:27], v[184:187], v[176:179], v[24:27]\n" \
"ds_read_b128 v[168:171], v240 offset:47104\n" \
"v_fmac_f32 v17, s56, v21\n" \
"v_fmac_f32 v18, s56, v22\n" \
"v_fmac_f32 v19, s56, v23\n" \
"v_fmac_f32 v12, s57, v252\n" \
"v_fmac_f32 v13, s57, v253\n" \
"v_mfma_f32_16x16x32_f16 v[36:39], v[188:191], v[176:179], v[36:39]\n" \
"v_pk_add_f16 v176, v176, v180\n" \
"v_pk_add_f16 v177, v177, v181\n" \
"v_fmac_f32 v14, s57, v254\n" \
"v_fmac_f32 v15, s57, v255\n" \
"v_cvt_pk_bf16_f32 v8, v0, v1\n" \
"v_cvt_pk_bf16_f32 v20, v4, v5\n" \
"v_mfma_f32_16x16x32_f16 v[28:31], v[192:195], v[180:183], v[28:31]\n" \
"v_pk_add_f16 v178, v178, v182\n" \
"v_pk_add_f16 v179, v179, v183\n" \
"v_cvt_pk_bf16_f32 v9, v2, v3\n" \
"v_cvt_pk_bf16_f32 v21, v6, v7\n" \
"v_cvt_pk_bf16_f32 v10, v12, v13\n" \
"v_cvt_pk_bf16_f32 v22, v16, v17\n" \
"v_cvt_pk_bf16_f32 v11, v14, v15\n" \
"v_mfma_f32_16x16x32_f16 v[40:43], v[196:199], v[180:183], v[40:43]\n" \
"ds_read_b128 v[172:175], v240 offset:47616\n" \
"v_cvt_pk_bf16_f32 v23, v18, v19\n" \
"s_not_b64 vcc, vcc\n" \
"v_cndmask_b32_dpp v0, v20, v8, vcc quad_perm:[1,0,3,2] row_mask:0xf bank_mask:0xf\n" \
"v_cndmask_b32_dpp v1, v21, v9, vcc quad_perm:[1,0,3,2] row_mask:0xf bank_mask:0xf\n" \
"v_mfma_f32_16x16x32_f16 v[32:35], v[200:203], v[176:179], v[32:35]\n" \
"s_waitcnt vmcnt(0)\n" \
"s_barrier\n" \
"v_cndmask_b32_dpp v2, v22, v10, vcc quad_perm:[1,0,3,2] row_mask:0xf bank_mask:0xf\n" \
"v_cndmask_b32_dpp v3, v23, v11, vcc quad_perm:[1,0,3,2] row_mask:0xf bank_mask:0xf\n" \
"buffer_store_dwordx4 v[0:3], v245, s[40:43], s69 offen sc1\n" \
"s_not_b64 vcc, vcc\n" \
"s_add_i32 s85, s69, 0x10000\n" \
"v_mfma_f32_16x16x32_f16 v[44:47], v[204:207], v[176:179], v[44:47]\n" \
"v_cndmask_b32_dpp v12, v8, v20, vcc quad_perm:[1,0,3,2] row_mask:0xf bank_mask:0xf\n" \
"v_cndmask_b32_dpp v13, v9, v21, vcc quad_perm:[1,0,3,2] row_mask:0xf bank_mask:0xf\n" \
"v_cndmask_b32_dpp v14, v10, v22, vcc quad_perm:[1,0,3,2] row_mask:0xf bank_mask:0xf\n" \
"v_cndmask_b32_dpp v15, v11, v23, vcc quad_perm:[1,0,3,2] row_mask:0xf bank_mask:0xf\n" \
"buffer_store_dwordx4 v[12:15], v245, s[40:43], s85 offen sc1\n" \
"v_mfma_f32_16x16x32_f16 v[24:27], v[208:211], v[160:163], v[24:27]\n" \
"ds_read_b128 v[176:179], v240 offset:6144\n" \
"v_mfma_f32_16x16x32_f16 v[36:39], v[212:215], v[160:163], v[36:39]\n" \
"s_waitcnt lgkmcnt(2)\n" \
"v_pk_add_f16 v160, v160, v164\n" \
"v_pk_add_f16 v161, v161, v165\n" \
"v_mfma_f32_16x16x32_f16 v[28:31], v[216:219], v[164:167], v[28:31]\n" \
"v_pk_add_f16 v162, v162, v166\n" \
"v_pk_add_f16 v163, v163, v167\n" \
"v_mfma_f32_16x16x32_f16 v[40:43], v[220:223], v[164:167], v[40:43]\n" \
"ds_read_b128 v[180:183], v240 offset:6656\n" \
"v_mfma_f32_16x16x32_f16 v[32:35], v[224:227], v[160:163], v[32:35]\n" \
"v_mfma_f32_16x16x32_f16 v[44:47], v[228:231], v[160:163], v[44:47]\n" \
"v_mfma_f32_16x16x32_f16 v[48:51], v[136:139], v[168:171], v[48:51]\n" \
"ds_read_b128 v[160:163], v240 offset:26624\n" \
"v_mfma_f32_16x16x32_f16 v[60:63], v[140:143], v[168:171], v[60:63]\n" \
"s_waitcnt lgkmcnt(3)\n" \
"v_pk_add_f16 v168, v168, v172\n" \
"v_pk_add_f16 v169, v169, v173\n" \
"v_mfma_f32_16x16x32_f16 v[52:55], v[144:147], v[172:175], v[52:55]\n" \
"v_pk_add_f16 v170, v170, v174\n" \
"v_pk_add_f16 v171, v171, v175\n" \
"v_add_f32 v252, v28, v32\n" \
"v_add_f32 v253, v29, v33\n" \
"v_mfma_f32_16x16x32_f16 v[64:67], v[148:151], v[172:175], v[64:67]\n" \
"ds_read_b128 v[164:167], v240 offset:27136\n" \
"v_add_f32 v254, v30, v34\n" \
"v_add_f32 v255, v31, v35\n" \
"v_fmac_f32 v28, s56, v32\n" \
"v_mfma_f32_16x16x32_f16 v[56:59], v[120:123], v[168:171], v[56:59]\n" \
"v_fmac_f32 v29, s56, v33\n" \
"v_fmac_f32 v30, s56, v34\n" \
"v_fmac_f32 v31, s56, v35\n" \
"v_mfma_f32_16x16x32_f16 v[68:71], v[124:127], v[168:171], v[68:71]\n" \
"v_fmac_f32 v24, s57, v252\n" \
"v_fmac_f32 v25, s57, v253\n" \
"v_fmac_f32 v26, s57, v254\n" \
"s_waitcnt lgkmcnt(2)\n" \
"v_mfma_f32_16x16x32_f16 v[48:51], v[128:131], v[176:179], v[48:51]\n" \
"ds_read_b128 v[168:171], v241 offset:6144\n" \
"v_fmac_f32 v27, s57, v255\n" \
"v_add_f32 v252, v40, v44\n" \
"v_add_f32 v253, v41, v45\n" \
"v_mfma_f32_16x16x32_f16 v[60:63], v[132:135], v[176:179], v[60:63]\n" \
"v_pk_add_f16 v176, v176, v180\n" \
"v_pk_add_f16 v177, v177, v181\n" \
"v_add_f32 v254, v42, v46\n" \
"v_add_f32 v255, v43, v47\n" \
"v_mfma_f32_16x16x32_f16 v[52:55], v[152:155], v[180:183], v[52:55]\n" \
"v_pk_add_f16 v178, v178, v182\n" \
"v_pk_add_f16 v179, v179, v183\n" \
"v_fmac_f32 v40, s56, v44\n" \
"v_fmac_f32 v41, s56, v45\n" \
"v_fmac_f32 v42, s56, v46\n" \
"v_mfma_f32_16x16x32_f16 v[64:67], v[156:159], v[180:183], v[64:67]\n" \
"ds_read_b128 v[172:175], v241 offset:6656\n" \
"v_fmac_f32 v43, s56, v47\n" \
"v_fmac_f32 v36, s57, v252\n" \
"v_fmac_f32 v37, s57, v253\n" \
"v_mfma_f32_16x16x32_f16 v[56:59], v[232:235], v[176:179], v[56:59]\n" \
"v_fmac_f32 v38, s57, v254\n" \
"v_fmac_f32 v39, s57, v255\n" \
"v_cvt_pk_bf16_f32 v32, v24, v25\n" \
"v_mfma_f32_16x16x32_f16 v[68:71], v[236:239], v[176:179], v[68:71]\n" \
"v_cvt_pk_bf16_f32 v44, v28, v29\n" \
"v_cvt_pk_bf16_f32 v33, v26, v27\n" \
"v_cvt_pk_bf16_f32 v45, v30, v31\n" \
"s_waitcnt lgkmcnt(1)\n" \
"v_mfma_f32_16x16x32_f16 v[48:51], v[184:187], v[160:163], v[48:51]\n" \
"ds_read_b128 v[176:179], v240 offset:49152\n" \
"v_cvt_pk_bf16_f32 v34, v36, v37\n" \
"v_cvt_pk_bf16_f32 v46, v40, v41\n" \
"v_mfma_f32_16x16x32_f16 v[60:63], v[188:191], v[160:163], v[60:63]\n" \
"v_pk_add_f16 v160, v160, v164\n" \
"v_pk_add_f16 v161, v161, v165\n" \
"v_cvt_pk_bf16_f32 v35, v38, v39\n" \
"v_cvt_pk_bf16_f32 v47, v42, v43\n" \
"s_not_b64 vcc, vcc\n" \
"v_mfma_f32_16x16x32_f16 v[52:55], v[192:195], v[164:167], v[52:55]\n" \
"v_pk_add_f16 v162, v162, v166\n" \
"v_pk_add_f16 v163, v163, v167\n" \
"v_cndmask_b32_dpp v24, v44, v32, vcc quad_perm:[1,0,3,2] row_mask:0xf bank_mask:0xf\n" \
"v_cndmask_b32_dpp v25, v45, v33, vcc quad_perm:[1,0,3,2] row_mask:0xf bank_mask:0xf\n" \
"v_cndmask_b32_dpp v26, v46, v34, vcc quad_perm:[1,0,3,2] row_mask:0xf bank_mask:0xf\n" \
"v_mfma_f32_16x16x32_f16 v[64:67], v[196:199], v[164:167], v[64:67]\n" \
"ds_read_b128 v[180:183], v240 offset:49664\n" \
"v_cndmask_b32_dpp v27, v47, v35, vcc quad_perm:[1,0,3,2] row_mask:0xf bank_mask:0xf\n" \
"buffer_store_dwordx4 v[24:27], v245, s[40:43], s70 offen sc1\n" \
"s_not_b64 vcc, vcc\n" \
"v_mfma_f32_16x16x32_f16 v[56:59], v[200:203], v[160:163], v[56:59]\n" \
"s_add_i32 s85, s70, 0x10000\n" \
"v_cndmask_b32_dpp v36, v32, v44, vcc quad_perm:[1,0,3,2] row_mask:0xf bank_mask:0xf\n" \
"v_cndmask_b32_dpp v37, v33, v45, vcc quad_perm:[1,0,3,2] row_mask:0xf bank_mask:0xf\n" \
"v_mfma_f32_16x16x32_f16 v[68:71], v[204:207], v[160:163], v[68:71]\n" \
"v_cndmask_b32_dpp v38, v34, v46, vcc quad_perm:[1,0,3,2] row_mask:0xf bank_mask:0xf\n" \
"v_cndmask_b32_dpp v39, v35, v47, vcc quad_perm:[1,0,3,2] row_mask:0xf bank_mask:0xf\n" \
"buffer_store_dwordx4 v[36:39], v245, s[40:43], s85 offen sc1\n" \
"v_mfma_f32_16x16x32_f16 v[48:51], v[208:211], v[168:171], v[48:51]\n" \
"ds_read_b128 v[160:163], v240 offset:8192\n" \
"v_mfma_f32_16x16x32_f16 v[60:63], v[212:215], v[168:171], v[60:63]\n" \
"s_waitcnt lgkmcnt(2)\n" \
"v_pk_add_f16 v168, v168, v172\n" \
"v_pk_add_f16 v169, v169, v173\n" \
"v_mfma_f32_16x16x32_f16 v[52:55], v[216:219], v[172:175], v[52:55]\n" \
"v_pk_add_f16 v170, v170, v174\n" \
"v_pk_add_f16 v171, v171, v175\n" \
"v_mfma_f32_16x16x32_f16 v[64:67], v[220:223], v[172:175], v[64:67]\n" \
"ds_read_b128 v[164:167], v240 offset:8704\n" \
"v_mfma_f32_16x16x32_f16 v[56:59], v[224:227], v[168:171], v[56:59]\n" \
"v_mfma_f32_16x16x32_f16 v[68:71], v[228:231], v[168:171], v[68:71]\n" \
"v_mfma_f32_16x16x32_f16 v[72:75], v[136:139], v[176:179], v[72:75]\n" \
"ds_read_b128 v[168:171], v240 offset:28672\n" \
"v_mfma_f32_16x16x32_f16 v[84:87], v[140:143], v[176:179], v[84:87]\n" \
"s_waitcnt lgkmcnt(3)\n" \
"v_pk_add_f16 v176, v176, v180\n" \
"v_pk_add_f16 v177, v177, v181\n" \
"v_mfma_f32_16x16x32_f16 v[76:79], v[144:147], v[180:183], v[76:79]\n" \
"v_pk_add_f16 v178, v178, v182\n" \
"v_pk_add_f16 v179, v179, v183\n" \
"v_add_f32 v252, v52, v56\n" \
"v_add_f32 v253, v53, v57\n" \
"v_mfma_f32_16x16x32_f16 v[88:91], v[148:151], v[180:183], v[88:91]\n" \
"ds_read_b128 v[172:175], v240 offset:29184\n" \
"v_add_f32 v254, v54, v58\n" \
"v_add_f32 v255, v55, v59\n" \
"v_mfma_f32_16x16x32_f16 v[80:83], v[120:123], v[176:179], v[80:83]\n" \
"v_fmac_f32 v52, s56, v56\n" \
"v_fmac_f32 v53, s56, v57\n" \
"v_mfma_f32_16x16x32_f16 v[92:95], v[124:127], v[176:179], v[92:95]\n" \
"v_fmac_f32 v54, s56, v58\n" \
"v_fmac_f32 v55, s56, v59\n" \
"s_waitcnt lgkmcnt(2)\n" \
"v_mfma_f32_16x16x32_f16 v[72:75], v[128:131], v[160:163], v[72:75]\n" \
"ds_read_b128 v[176:179], v241 offset:8192\n" \
"v_fmac_f32 v48, s57, v252\n" \
"v_fmac_f32 v49, s57, v253\n" \
"v_mfma_f32_16x16x32_f16 v[84:87], v[132:135], v[160:163], v[84:87]\n" \
"v_pk_add_f16 v160, v160, v164\n" \
"v_pk_add_f16 v161, v161, v165\n" \
"v_fmac_f32 v50, s57, v254\n" \
"v_fmac_f32 v51, s57, v255\n" \
"v_mfma_f32_16x16x32_f16 v[76:79], v[152:155], v[164:167], v[76:79]\n" \
"v_pk_add_f16 v162, v162, v166\n" \
"v_pk_add_f16 v163, v163, v167\n" \
"v_add_f32 v252, v64, v68\n" \
"v_add_f32 v253, v65, v69\n" \
"v_mfma_f32_16x16x32_f16 v[88:91], v[156:159], v[164:167], v[88:91]\n" \
"ds_read_b128 v[180:183], v241 offset:8704\n" \
"v_add_f32 v254, v66, v70\n" \
"v_add_f32 v255, v67, v71\n" \
"v_mfma_f32_16x16x32_f16 v[80:83], v[232:235], v[160:163], v[80:83]\n" \
"v_fmac_f32 v64, s56, v68\n" \
"v_fmac_f32 v65, s56, v69\n" \
"v_mfma_f32_16x16x32_f16 v[92:95], v[236:239], v[160:163], v[92:95]\n" \
"v_fmac_f32 v66, s56, v70\n" \
"v_fmac_f32 v67, s56, v71\n" \
"s_waitcnt lgkmcnt(2)\n" \
"v_mfma_f32_16x16x32_f16 v[72:75], v[184:187], v[168:171], v[72:75]\n" \
"ds_read_b128 v[160:163], v240 offset:40960\n" \
"v_fmac_f32 v60, s57, v252\n" \
"v_fmac_f32 v61, s57, v253\n" \
"v_mfma_f32_16x16x32_f16 v[84:87], v[188:191], v[168:171], v[84:87]\n" \
"v_pk_add_f16 v168, v168, v172\n" \
"v_pk_add_f16 v169, v169, v173\n" \
"v_fmac_f32 v62, s57, v254\n" \
"v_fmac_f32 v63, s57, v255\n" \
"v_mfma_f32_16x16x32_f16 v[76:79], v[192:195], v[172:175], v[76:79]\n" \
"v_pk_add_f16 v170, v170, v174\n" \
"v_pk_add_f16 v171, v171, v175\n" \
"v_cvt_pk_bf16_f32 v56, v48, v49\n" \
"v_cvt_pk_bf16_f32 v68, v52, v53\n" \
"v_mfma_f32_16x16x32_f16 v[88:91], v[196:199], v[172:175], v[88:91]\n" \
"ds_read_b128 v[164:167], v240 offset:41472\n" \
"v_cvt_pk_bf16_f32 v57, v50, v51\n" \
"v_cvt_pk_bf16_f32 v69, v54, v55\n" \
"v_mfma_f32_16x16x32_f16 v[80:83], v[200:203], v[168:171], v[80:83]\n" \
"v_cvt_pk_bf16_f32 v58, v60, v61\n" \
"v_cvt_pk_bf16_f32 v70, v64, v65\n" \
"v_mfma_f32_16x16x32_f16 v[92:95], v[204:207], v[168:171], v[92:95]\n" \
"v_cvt_pk_bf16_f32 v59, v62, v63\n" \
"v_cvt_pk_bf16_f32 v71, v66, v67\n" \
"s_waitcnt lgkmcnt(2)\n" \
"v_mfma_f32_16x16x32_f16 v[72:75], v[208:211], v[176:179], v[72:75]\n" \
"ds_read_b128 v[168:171], v240 offset:0\n" \
"s_not_b64 vcc, vcc\n" \
"v_cndmask_b32_dpp v48, v68, v56, vcc quad_perm:[1,0,3,2] row_mask:0xf bank_mask:0xf\n" \
"v_mfma_f32_16x16x32_f16 v[84:87], v[212:215], v[176:179], v[84:87]\n" \
"v_pk_add_f16 v176, v176, v180\n" \
"v_pk_add_f16 v177, v177, v181\n" \
"v_cndmask_b32_dpp v49, v69, v57, vcc quad_perm:[1,0,3,2] row_mask:0xf bank_mask:0xf\n" \
"v_cndmask_b32_dpp v50, v70, v58, vcc quad_perm:[1,0,3,2] row_mask:0xf bank_mask:0xf\n" \
"v_mfma_f32_16x16x32_f16 v[76:79], v[216:219], v[180:183], v[76:79]\n" \
"v_pk_add_f16 v178, v178, v182\n" \
"v_pk_add_f16 v179, v179, v183\n" \
"v_cndmask_b32_dpp v51, v71, v59, vcc quad_perm:[1,0,3,2] row_mask:0xf bank_mask:0xf\n" \
"buffer_store_dwordx4 v[48:51], v245, s[40:43], s71 offen sc1\n" \
"v_mfma_f32_16x16x32_f16 v[88:91], v[220:223], v[180:183], v[88:91]\n" \
"ds_read_b128 v[172:175], v240 offset:512\n" \
"s_not_b64 vcc, vcc\n" \
"s_add_i32 s85, s71, 0x10000\n" \
"v_mfma_f32_16x16x32_f16 v[80:83], v[224:227], v[176:179], v[80:83]\n" \
"v_cndmask_b32_dpp v60, v56, v68, vcc quad_perm:[1,0,3,2] row_mask:0xf bank_mask:0xf\n" \
"v_cndmask_b32_dpp v61, v57, v69, vcc quad_perm:[1,0,3,2] row_mask:0xf bank_mask:0xf\n" \
"v_mfma_f32_16x16x32_f16 v[92:95], v[228:231], v[176:179], v[92:95]\n" \
"v_cndmask_b32_dpp v62, v58, v70, vcc quad_perm:[1,0,3,2] row_mask:0xf bank_mask:0xf\n" \
"v_cndmask_b32_dpp v63, v59, v71, vcc quad_perm:[1,0,3,2] row_mask:0xf bank_mask:0xf\n" \
"buffer_store_dwordx4 v[60:63], v245, s[40:43], s85 offen sc1\n" \
"s_waitcnt lgkmcnt(1)\n" \
"v_mfma_f32_16x16x32_f16 v[96:99], v[136:139], v[160:163], v[96:99]\n" \
"ds_read_b128 v[176:179], v240 offset:20480\n" \
"v_mfma_f32_16x16x32_f16 v[108:111], v[140:143], v[160:163], v[108:111]\n" \
"v_pk_add_f16 v160, v160, v164\n" \
"v_pk_add_f16 v161, v161, v165\n" \
"v_mfma_f32_16x16x32_f16 v[100:103], v[144:147], v[164:167], v[100:103]\n" \
"v_pk_add_f16 v162, v162, v166\n" \
"v_pk_add_f16 v163, v163, v167\n" \
"v_add_f32 v252, v76, v80\n" \
"v_add_f32 v253, v77, v81\n" \
"v_mfma_f32_16x16x32_f16 v[112:115], v[148:151], v[164:167], v[112:115]\n" \
"ds_read_b128 v[180:183], v240 offset:20992\n" \
"v_add_f32 v254, v78, v82\n" \
"v_add_f32 v255, v79, v83\n" \
"v_mfma_f32_16x16x32_f16 v[104:107], v[120:123], v[160:163], v[104:107]\n" \
"v_fmac_f32 v76, s56, v80\n" \
"v_fmac_f32 v77, s56, v81\n" \
"v_mfma_f32_16x16x32_f16 v[116:119], v[124:127], v[160:163], v[116:119]\n" \
"v_fmac_f32 v78, s56, v82\n" \
"v_fmac_f32 v79, s56, v83\n" \
"v_mfma_f32_16x16x32_f16 v[96:99], v[128:131], v[168:171], v[96:99]\n" \
"ds_read_b128 v[160:163], v241 offset:0\n" \
"v_fmac_f32 v72, s57, v252\n" \
"v_fmac_f32 v73, s57, v253\n" \
"v_mfma_f32_16x16x32_f16 v[108:111], v[132:135], v[168:171], v[108:111]\n" \
"s_waitcnt lgkmcnt(2)\n" \
"v_pk_add_f16 v168, v168, v172\n" \
"v_pk_add_f16 v169, v169, v173\n" \
"v_fmac_f32 v74, s57, v254\n" \
"v_fmac_f32 v75, s57, v255\n" \
"v_mfma_f32_16x16x32_f16 v[100:103], v[152:155], v[172:175], v[100:103]\n" \
"v_pk_add_f16 v170, v170, v174\n" \
"v_pk_add_f16 v171, v171, v175\n" \
"v_add_f32 v252, v88, v92\n" \
"v_add_f32 v253, v89, v93\n" \
"v_mfma_f32_16x16x32_f16 v[112:115], v[156:159], v[172:175], v[112:115]\n" \
"ds_read_b128 v[164:167], v241 offset:512\n" \
"v_add_f32 v254, v90, v94\n" \
"v_add_f32 v255, v91, v95\n" \
"v_mfma_f32_16x16x32_f16 v[104:107], v[232:235], v[168:171], v[104:107]\n" \
"v_fmac_f32 v88, s56, v92\n" \
"v_fmac_f32 v89, s56, v93\n" \
"v_mfma_f32_16x16x32_f16 v[116:119], v[236:239], v[168:171], v[116:119]\n" \
"v_fmac_f32 v90, s56, v94\n" \
"v_fmac_f32 v91, s56, v95\n" \
"v_mfma_f32_16x16x32_f16 v[96:99], v[184:187], v[176:179], v[96:99]\n" \
"v_fmac_f32 v84, s57, v252\n" \
"v_fmac_f32 v85, s57, v253\n" \
"v_fmac_f32 v86, s57, v254\n" \
"v_mfma_f32_16x16x32_f16 v[108:111], v[188:191], v[176:179], v[108:111]\n" \
"s_waitcnt lgkmcnt(1)\n" \
"v_pk_add_f16 v176, v176, v180\n" \
"v_pk_add_f16 v177, v177, v181\n" \
"v_fmac_f32 v87, s57, v255\n" \
"v_mfma_f32_16x16x32_f16 v[100:103], v[192:195], v[180:183], v[100:103]\n" \
"v_pk_add_f16 v178, v178, v182\n" \
"v_pk_add_f16 v179, v179, v183\n" \
"v_cvt_pk_bf16_f32 v80, v72, v73\n" \
"v_cvt_pk_bf16_f32 v92, v76, v77\n" \
"v_mfma_f32_16x16x32_f16 v[112:115], v[196:199], v[180:183], v[112:115]\n" \
"v_cvt_pk_bf16_f32 v81, v74, v75\n" \
"v_cvt_pk_bf16_f32 v93, v78, v79\n" \
"v_mfma_f32_16x16x32_f16 v[104:107], v[200:203], v[176:179], v[104:107]\n" \
"v_cvt_pk_bf16_f32 v82, v84, v85\n" \
"v_cvt_pk_bf16_f32 v94, v88, v89\n" \
"v_mfma_f32_16x16x32_f16 v[116:119], v[204:207], v[176:179], v[116:119]\n" \
"v_cvt_pk_bf16_f32 v83, v86, v87\n" \
"v_cvt_pk_bf16_f32 v95, v90, v91\n" \
"v_mfma_f32_16x16x32_f16 v[96:99], v[208:211], v[160:163], v[96:99]\n" \
"s_not_b64 vcc, vcc\n" \
"v_cndmask_b32_dpp v72, v92, v80, vcc quad_perm:[1,0,3,2] row_mask:0xf bank_mask:0xf\n" \
"v_cndmask_b32_dpp v73, v93, v81, vcc quad_perm:[1,0,3,2] row_mask:0xf bank_mask:0xf\n" \
"v_mfma_f32_16x16x32_f16 v[108:111], v[212:215], v[160:163], v[108:111]\n" \
"s_waitcnt lgkmcnt(0)\n" \
"v_pk_add_f16 v160, v160, v164\n" \
"v_pk_add_f16 v161, v161, v165\n" \
"v_cndmask_b32_dpp v74, v94, v82, vcc quad_perm:[1,0,3,2] row_mask:0xf bank_mask:0xf\n" \
"v_mfma_f32_16x16x32_f16 v[100:103], v[216:219], v[164:167], v[100:103]\n" \
"v_pk_add_f16 v162, v162, v166\n" \
"v_pk_add_f16 v163, v163, v167\n" \
"v_cndmask_b32_dpp v75, v95, v83, vcc quad_perm:[1,0,3,2] row_mask:0xf bank_mask:0xf\n" \
"buffer_store_dwordx4 v[72:75], v245, s[40:43], s72 offen sc1\n" \
"v_mfma_f32_16x16x32_f16 v[112:115], v[220:223], v[164:167], v[112:115]\n" \
"s_not_b64 vcc, vcc\n" \
"s_add_i32 s85, s72, 0x10000\n" \
"v_mfma_f32_16x16x32_f16 v[104:107], v[224:227], v[160:163], v[104:107]\n" \
"v_cndmask_b32_dpp v84, v80, v92, vcc quad_perm:[1,0,3,2] row_mask:0xf bank_mask:0xf\n" \
"v_cndmask_b32_dpp v85, v81, v93, vcc quad_perm:[1,0,3,2] row_mask:0xf bank_mask:0xf\n" \
"v_mfma_f32_16x16x32_f16 v[116:119], v[228:231], v[160:163], v[116:119]\n" \
"v_cndmask_b32_dpp v86, v82, v94, vcc quad_perm:[1,0,3,2] row_mask:0xf bank_mask:0xf\n" \
"v_cndmask_b32_dpp v87, v83, v95, vcc quad_perm:[1,0,3,2] row_mask:0xf bank_mask:0xf\n" \
"buffer_store_dwordx4 v[84:87], v245, s[40:43], s85 offen sc1\n" \
"s_nop 7\n" \
"v_add_f32 v252, v100, v104\n" \
"v_add_f32 v253, v101, v105\n" \
"v_add_f32 v254, v102, v106\n" \
"v_add_f32 v255, v103, v107\n" \
"v_fmac_f32 v100, s56, v104\n" \
"v_fmac_f32 v101, s56, v105\n" \
"v_fmac_f32 v102, s56, v106\n" \
"v_fmac_f32 v103, s56, v107\n" \
"v_fmac_f32 v96, s57, v252\n" \
"v_fmac_f32 v97, s57, v253\n" \
"v_fmac_f32 v98, s57, v254\n" \
"v_fmac_f32 v99, s57, v255\n" \
"v_add_f32 v252, v112, v116\n" \
"v_add_f32 v253, v113, v117\n" \
"v_add_f32 v254, v114, v118\n" \
"v_add_f32 v255, v115, v119\n" \
"v_fmac_f32 v112, s56, v116\n" \
"v_fmac_f32 v113, s56, v117\n" \
"v_fmac_f32 v114, s56, v118\n" \
"v_fmac_f32 v115, s56, v119\n" \
"v_fmac_f32 v108, s57, v252\n" \
"v_fmac_f32 v109, s57, v253\n" \
"v_fmac_f32 v110, s57, v254\n" \
"v_fmac_f32 v111, s57, v255\n" \
"v_cvt_pk_bf16_f32 v104, v96, v97\n" \
"v_cvt_pk_bf16_f32 v116, v100, v101\n" \
"v_cvt_pk_bf16_f32 v105, v98, v99\n" \
"v_cvt_pk_bf16_f32 v117, v102, v103\n" \
"v_cvt_pk_bf16_f32 v106, v108, v109\n" \
"v_cvt_pk_bf16_f32 v118, v112, v113\n" \
"v_cvt_pk_bf16_f32 v107, v110, v111\n" \
"v_cvt_pk_bf16_f32 v119, v114, v115\n" \
"s_not_b64 vcc, vcc\n" \
"v_cndmask_b32_dpp v96, v116, v104, vcc quad_perm:[1,0,3,2] row_mask:0xf bank_mask:0xf\n" \
"v_cndmask_b32_dpp v97, v117, v105, vcc quad_perm:[1,0,3,2] row_mask:0xf bank_mask:0xf\n" \
"v_cndmask_b32_dpp v98, v118, v106, vcc quad_perm:[1,0,3,2] row_mask:0xf bank_mask:0xf\n" \
"v_cndmask_b32_dpp v99, v119, v107, vcc quad_perm:[1,0,3,2] row_mask:0xf bank_mask:0xf\n" \
"buffer_store_dwordx4 v[96:99], v245, s[40:43], s73 offen sc1\n" \
"s_not_b64 vcc, vcc\n" \
"s_add_i32 s85, s73, 0x10000\n" \
"v_cndmask_b32_dpp v108, v104, v116, vcc quad_perm:[1,0,3,2] row_mask:0xf bank_mask:0xf\n" \
"v_cndmask_b32_dpp v109, v105, v117, vcc quad_perm:[1,0,3,2] row_mask:0xf bank_mask:0xf\n" \
"v_cndmask_b32_dpp v110, v106, v118, vcc quad_perm:[1,0,3,2] row_mask:0xf bank_mask:0xf\n" \
"v_cndmask_b32_dpp v111, v107, v119, vcc quad_perm:[1,0,3,2] row_mask:0xf bank_mask:0xf\n" \
"buffer_store_dwordx4 v[108:111], v245, s[40:43], s85 offen sc1\n" \
"s_waitcnt vmcnt(0)\n" \
"s_barrier\n" \
"s_branch .Lcf_e\n" \
".Ls12_%=:\n" \
"s_mov_b32 s87, 0x200000\n" \
"s_mov_b32 s86, 0x8004000\n" \
".Lt12_%=:\n" \
"s_sleep 3\n" \
"buffer_load_dword v253, v252, s[36:39], s86 offen sc1\n" \
"s_waitcnt vmcnt(0)\n" \
"s_sub_u32 s87, s87, 1\n" \
"s_cbranch_scc1 .Lr12_%=\n" \
"v_cmp_eq_u32 vcc, 0, v253\n" \
"s_cbranch_vccnz .Lt12_%=\n" \
"s_branch .Lr12_%=\n" \
".Ls16_%=:\n" \
"s_mov_b32 s87, 0x200000\n" \
"s_mov_b32 s86, 0x800c000\n" \
".Lt16_%=:\n" \
"s_sleep 3\n" \
"buffer_load_dword v253, v252, s[36:39], s86 offen sc1\n" \
"s_waitcnt vmcnt(0)\n" \
"s_sub_u32 s87, s87, 1\n" \
"s_cbranch_scc1 .Lr16_%=\n" \
"v_cmp_eq_u32 vcc, 0, v253\n" \
"s_cbranch_vccnz .Lt16_%=\n" \
"s_branch .Lr16_%=\n" \
".Ls110_%=:\n" \
"s_mov_b32 s87, 0x200000\n" \
"s_mov_b32 s86, 0x8014000\n" \
".Lt110_%=:\n" \
"s_sleep 3\n" \
"buffer_load_dword v253, v252, s[36:39], s86 offen sc1\n" \
"s_waitcnt vmcnt(0)\n" \
"s_sub_u32 s87, s87, 1\n" \
"s_cbranch_scc1 .Lr110_%=\n" \
"v_cmp_eq_u32 vcc, 0, v253\n" \
"s_cbranch_vccnz .Lt110_%=\n" \
"s_branch .Lr110_%=\n" \
""
#define CLOBBERS "v0", "v1", "v2", "v3", "v4", "v5", "v6", "v7", "v8", "v9", "v10", "v11", "v12", "v13", "v14", "v15", "v16", "v17", "v18", "v19", "v20", "v21", "v22", "v23", "v24", "v25", "v26", "v27", "v28", "v29", "v30", "v31", "v32", "v33", "v34", "v35", "v36", "v37", "v38", "v39", "v40", "v41", "v42", "v43", "v44", "v45", "v46", "v47", "v48", "v49", "v50", "v51", "v52", "v53", "v54", "v55", "v56", "v57", "v58", "v59", "v60", "v61", "v62", "v63", "v64", "v65", "v66", "v67", "v68", "v69", "v70", "v71", "v84", "v85", "v86", "v87", "v88", "v89", "v90", "v91", "v92", "v93", "v94", "v95", "v120", "v121", "v122", "v123", "v124", "v125", "v126", "v127", "v128", "v129", "v130", "v131", "v132", "v133", "v134", "v135", "v136", "v137", "v138", "v139", "v140", "v141", "v142", "v143", "v144", "v145", "v146", "v147", "v148", "v149", "v150", "v151", "v152", "v153", "v154", "v155", "v156", "v157", "v158", "v159", "v160", "v161", "v162", "v163", "v164", "v165", "v166", "v167", "v168", "v169", "v170", "v171", "v172", "v173", "v174", "v175", "v176", "v177", "v178", "v179", "v180", "v181", "v182", "v183", "v184", "v185", "v186", "v187", "v188", "v189", "v190", "v191", "v76", "v77", "v78", "v79", "v80", "v81", "v82", "v83", "v253", "v254", "v255", "s80", "s81", "s82", "s83", "s84", "s85", "s86", "s87", "s88", "s89", "s90", "s91", "s92", "s75", "s94", "s95"
template <int NM>
__device__ __forceinline__ void run1(Ctx& cx, const unsigned short* __restrict__ x, const unsigned short* __restrict__ w, RQ& rq) {
  unsigned xs;
  phase1<NM>(cx, x, w, rq, xs, rq.vb, [&]() __attribute__((always_inline)) {
  static_assert(NS - st_first(PG) == 3 && NH == 10 && NQW == 11 && RAWL == 110592, "constants of the generated code");
  const unsigned bid = cx.vb, idx = bid >> 3, slot = (bid & 7) * 4 + (idx >> 3), lane = cx.lane4 >> 2, h = (idx >> 2) & 1, wv = cx.wv;
  const unsigned lds0 = (unsigned)(uintptr_t)(lds_void*)cx.smem;
  unsigned own = (((wv ^ idx) & 3) | (bid & 0)) - 1u;
  asm volatile("s_ashr_i32 %0, %0, 31" : "+s"(own) :: "scc");
  unsigned* sa = rq.s, *sb = rq.s + 16;
  sa[0] = cx.ldg; sa[1] = cx.ldf; sa[2] = cx.sg; sa[3] = cx.sf;
#pragma unroll
  for (int j = 0; j < 4; ++j) {
    sa[4 + j] = ((bid + j) & 3) * 16384;
    sa[8 + j] = ((bid + j) & 3) * 256;
  }
  if (NM == 5) {
    sa[8] = lds0 + RAWL + 32768 + (wv & 1) * 8192 + (wv & 2) * 2048;
    sa[11] = (30 + (wv & 1)) * SBLK + (wv & 2) * 10240;
    const unsigned long long pace = wv == 7 ? (bid & 1 ? bytes(105, 100, 90, 88, 86, 84, 82) : bytes(150, 140, 120, 100, 90, 88, 86)) : 0ull;
    sa[9] = (unsigned)pace;
    sb[14] = (unsigned)(pace >> 32);
    sa[10] = lds0 + CORD + (wv & 3) * 1024;
  }
  sa[12] = min(slot, 1u) * 0xbf800000u;
  sa[13] = min(slot, 1u) * 0xbf000000u;
  sa[14] = sa[15] = own;
#pragma unroll
  for (int n = 0; n < NH; ++n) {
    const unsigned blk = (xs & 255u) + n % 5;
    rq.s[n ? 15 + n : 32] = (xs & ~255u) + (n / 5) * (8 * SBLK) + (blk + (blk & 192u)) * 128u;
  }
#pragma unroll
  for (int m = 0; m < 5; ++m) {
    const unsigned t = NM == 5 ? (m + 1) % 5 : m;
    sb[9 + m] = t * (16 * 65536) + ((h * 10 + (wv >> 2) * 5 + (wv & 2) + t) & 2) * 4096;
  }
#pragma unroll
  for (int i = 0; i < 33; ++i) if (i != 31) rq.s[i] = __builtin_amdgcn_readfirstlane(rq.s[i]);
  const unsigned prd = cx.prd + (RAWL - RAWB);
  const unsigned yo = (h * NBLK + (wv >> 2) * 80 + (lane & 14)) * 65536 + ((idx & 3) * 4 + (wv & 1)) * 4096 + (lane >> 4) * 16 + (2 * slot + (lane & 1)) * 64;
  unsigned* v = rq.v;
  v[0] = cx.voff; v[1] = cx.voff5; v[2] = cx.poff; v[3] = cx.xo; v[4] = prd; v[5] = prd ^ 32u; v[6] = prd ^ 64u; v[7] = prd ^ 96u;
  v[8] = cx.xbase; v[9] = cx.wbase; v[10] = cx.wbase ^ 32u; v[11] = cx.fvo; v[12] = lane * 16; v[13] = yo; v[14] = cx.pwr + (RAWL - RAWB);
  if (NM == 6) {
    v[15] = cx.crd;
    v[16] = CBUF + min(lane & 15, 10u) * 272 + wv * 64 + (cx.lane4 >> 6) * 16;
    v[17] = CORD + lane * 16;
    v[18] = (bid & 7) * 1024 + (idx & 3) * 256 + lane * 4;
    const unsigned fg = WS_GRP - WS_S + (idx & 3) * 256 + h * 128;
    v[19] = wv == 1 && lane < 8 ? fg + (lane + (lane & 4)) * 1024 + slot * 4 : 0x80000000u;
    v[20] = wv == 0 ? fg + ((bid & 7) + (bid & 4)) * 1024 + ((lane & 31) ^ ((lane & 31) == slot)) * 4 : 0x80000000u;
  } else {
    const unsigned co = (((bid >> 3) & 3) * 2 + ((bid >> 2) & 1)) * 128u + (cx.lane4 & 127u);
    const unsigned long long p = (unsigned long long)((const char*)(cx.lane4 < 128 ? cx.gamma : cx.beta) + co);
    v[15] = 0u;
    v[16] = (unsigned)p;
    v[17] = (unsigned)(p >> 32);
    v[18] = cx.fvo;
  }
  rq.s[31] = __builtin_amdgcn_readfirstlane(wv & 2 ? cx.pft + (wv & 1) * 8192 : cx.pfo - 8192 - (wv & 1) * 8192);
#pragma unroll
  for (int i = 0; i < 33; ++i) asm volatile("" : "+s"(rq.s[i]));
#pragma unroll
  for (int i = 0; i < (NM == 6 ? 21 : 19); ++i) asm volatile("" : "+v"(rq.v[i]));
  SB0;
  });
}

template <int CTRL>
__device__ __forceinline__ float bfly(float a, float b, bool hi) {
  const float give = hi ? a : b, keep = hi ? b : a;
  return keep + __builtin_bit_cast(float, __builtin_amdgcn_update_dpp(0, __builtin_bit_cast(int, give), CTRL, 0xf, 0xf, false));
}
__device__ __forceinline__ float halves_sum(float v) {
  const auto r = __builtin_amdgcn_permlane32_swap(__float_as_uint(v), __float_as_uint(v), false, false);
  return __uint_as_float(r[0]) + __uint_as_float(r[1]);
}

extern "C" __global__ __launch_bounds__(512, 2) void conv_fft(char* ws, const unsigned short* __restrict__ x,
                                                   const unsigned short* __restrict__ w,
                                                   const unsigned short* __restrict__ gamma,
                                                   const unsigned short* __restrict__ beta,
                                                   unsigned short* __restrict__ out) {
  __shared__ __attribute__((aligned(16))) char smem[160 * 1024];
  const unsigned lds0 = (unsigned)(uintptr_t)(lds_void*)smem;
  const int wv = __builtin_amdgcn_readfirstlane(threadIdx.x >> 6);
  Ctx cx;
  {
    unsigned len;
    asm volatile("s_getpc_b64 %0\n.Lcf_a:\n\ts_mov_b32 %1, .Lcf_b-.Lcf_a\n\ts_mov_b32 %2, .Lcf_t-.Lcf_a" : "=s"(cx.pc), "=s"(len), "=s"(cx.pft));
    cx.wv = wv;
    cx.pfo = len;
    unsigned xcc;
    asm volatile("s_getreg_b32 %0, hwreg(HW_REG_XCC_ID, 0, 3)" : "=s"(xcc));
    unsigned id = blockIdx.x;
    asm volatile("" : "+s"(id));
    cx.id = id;
    cx.vb = (int)((id & 248u) | (id & 256u ? (id >> 10) & 7u : xcc));
    unsigned xa = x_addr(cx.vb, 0, wv), xo = ((cx.vb >> 2) & 1) * (T_ * D_ * 2), l1 = lds0 + wv * 1024;
    asm volatile("" : "+v"(xa));
    asm volatile("" : "+s"(xo), "+s"(l1));
    SB0;
    const char* pb = ws + WS_PUB;
    u32x4 sx, sw;
    if (((id | wv) == 0)) {
      const unsigned l = threadIdx.x;
      const u32x4 z = {0u, 0u, 0u, 0u};
      {
#pragma unroll
        for (int k = 0; k < 2; ++k)
          asm volatile("global_store_dwordx4 %0, %1, %2 sc1" :: "v"(32768 + (l & 7) * 16 + (l & 8) * 512 + (l & 16) * 1024 + (l & 32) * 32 + k * 8192), "v"(z), "s"(pb) : "memory");
      }
      {
        const unsigned so = l * (31u * 1024u + 16u) + (4u << 20);
        asm volatile("global_store_dwordx4 %2, %3, %4 sc1\n\tglobal_load_dwordx4 %0, %5, %6\n\tglobal_load_dwordx4 %1, %7, %8"
                     : "=&v"(sx), "=&v"(sw) : "v"(l * 16 + 1024), "v"(z), "s"(pb), "v"(so + (l & 3u) * (8u << 20)), "s"(x), "v"(so + (l & 1u) * (6u << 20)), "s"(w) : "memory");
      }
    }
    const u32x4 rx = make_rsrc((const char*)x + xo, T_ * D_ * 2);
    issue_x(0, 0, xa, rx, l1, 0, 2);
    if (id) code_touch(cx, 0, 1 & 1 ? 1 : 2);
    SB0;
    issue_x(0, wv, xa, rx, lds0, 2, 9);
    SB0;
    if (((id | wv) == 0)) {
      wait_vm<9>();
      asm volatile("" : "+v"(sx), "+v"(sw));
      unsigned m = expo(amax(amax(amax(amax(0, sx[0]), sx[1]), sx[2]), sx[3])) | expo(amax(amax(amax(amax(0, sw[0]), sw[1]), sw[2]), sw[3])) << 16;
      m = amax(m, (unsigned)__builtin_amdgcn_update_dpp(0, (int)m, 0x128, 0xf, 0xf, false));
      m = amax(m, (unsigned)__builtin_amdgcn_update_dpp(0, (int)m, 0x124, 0xf, 0xf, false));
      m = amax(m, (unsigned)__builtin_amdgcn_update_dpp(0, (int)m, 0x122, 0xf, 0xf, false));
      m = amax(m, (unsigned)__builtin_amdgcn_update_dpp(0, (int)m, 0x121, 0xf, 0xf, false));
      unsigned ex = 28, ew = 28;
#pragma unroll
      for (int l = 0; l < 64; l += 16) {
        const unsigned v = (unsigned)__builtin_amdgcn_readlane((int)m, l);
        ex = max(ex + 2, v & 255u) - 2;
        ew = max(ew + 3, v >> 16) - 3;
      }
      const unsigned long long tg = __builtin_amdgcn_s_memrealtime();
      const unsigned lo = (unsigned)tg << 4 | xcc << 1, hi = ((unsigned)(tg >> 28) & 0xffffu) | min(ex, 220u) << 16 | min(ew, 220u) << 24;
      asm volatile("global_store_dwordx4 %0, %1, %2 sc1\n\ts_nop 1" :: "v"(threadIdx.x * 16), "v"(u32x4{lo, hi, ~lo, ~hi}), "s"(pb) : "memory");
      cx.tag[0] = lo;
      cx.tag[1] = hi;
    }
  }
  cx.smem = smem;
  cx.ws = ws;
  cx.out = (const char*)out;
  cx.gamma = gamma;
  cx.beta = beta;
  bf16x8 gb[8];
  unsigned fv;
  {
    RQ rq;
    if (wv < 4) run1<6>(cx, x, w, rq); else run1<5>(cx, x, w, rq);
    const unsigned long long tpc = cx.pc + rq.s[31];
    __builtin_amdgcn_s_barrier();
    asm volatile("s_cmp_gt_u32 %[wv], 3\n\ts_cbranch_scc1 .Lcf_1\n" LOOP0 ".Lcf_1:\n" LOOP1 ".Lcf_e:"
      :
        "+{v[72:75]}"(rq.h[0]), "+{v232}"(rq.v[0]), "+{v233}"(rq.v[1]), "+{v234}"(rq.v[2]),
        "+{v235}"(rq.v[3]), "+{v236}"(rq.v[4]), "+{v237}"(rq.v[5]), "+{v238}"(rq.v[6]), "+{v239}"(rq.v[7]), "+{v[196:199]}"(rq.h[1]),
        "+{v[200:203]}"(rq.h[2]), "+{v[204:207]}"(rq.h[3]), "+{v[96:97]}"(rq.raw[0]), "+{v[98:99]}"(rq.raw[1]), "+{v[100:101]}"(rq.raw[2]),
        "+{v[102:103]}"(rq.raw[3]), "+{v[104:105]}"(rq.raw[4]), "+{v[106:107]}"(rq.raw[5]), "+{v[108:109]}"(rq.raw[6]), "+{v[110:111]}"(rq.raw[7]),
        "+{v[112:113]}"(rq.raw[8]), "+{v[114:115]}"(rq.raw[9]), "+{v[116:117]}"(rq.raw[10]), "+{v241}"(rq.v[9]), "+{v243}"(rq.v[11]), "+{v244}"(rq.v[12]), "+{v252}"(rq.v[18]), "+{v[192:193]}"(rq.vb),
        "+{v[208:211]}"(rq.h[4]), "+{v[212:215]}"(rq.h[5]), "+{v[216:219]}"(rq.h[6]), "+{v[220:223]}"(rq.h[7]), "+{v[224:227]}"(rq.h[8]), "+{v[228:231]}"(rq.h[9]),
        "+{v194}"(rq.v[19]), "+{v195}"(rq.v[20]), "+{v248}"(rq.v[16]), "+{v249}"(rq.v[17]), "+{v246}"(rq.v[14]), "+{v247}"(rq.v[15])
      :
        "{v240}"(rq.v[8]), "{v242}"(rq.v[10]), "{v245}"(rq.v[13]),

            "{s44}"(rq.s[0]), "{s45}"(rq.s[1]),
        "{s46}"(rq.s[2]), "{s47}"(rq.s[3]), "{s48}"(rq.s[4]), "{s49}"(rq.s[5]), "{s50}"(rq.s[6]), "{s51}"(rq.s[7]), "{s52}"(rq.s[8]), "{s53}"(rq.s[9]),
        "{s54}"(rq.s[10]), "{s55}"(rq.s[11]), "{s56}"(rq.s[12]), "{s57}"(rq.s[13]), "{s58}"(rq.s[14]), "{s59}"(rq.s[15]), "{s60}"(rq.s[16]),
        "{s61}"(rq.s[17]), "{s62}"(rq.s[18]), "{s63}"(rq.s[19]), "{s64}"(rq.s[20]), "{s65}"(rq.s[21]), "{s66}"(rq.s[22]), "{s67}"(rq.s[23]),
        "{s68}"(rq.s[24]), "{s69}"(rq.s[25]), "{s70}"(rq.s[26]), "{s71}"(rq.s[27]), "{s72}"(rq.s[28]), "{s73}"(rq.s[29]), "{s74}"(rq.s[30]), "{s93}"(rq.s[32]),
        "{s[36:39]}"(cx.rs), "{s[40:43]}"(make_rsrc(cx.ws + WS_Y, ROWS * 65536)), "{s[76:77]}"(&TAB.G[0][0][0][0]), "{s[78:79]}"(tpc), [wv] "s"(wv)
      : "memory", "vcc", "scc", "v118", "v119", CLOBBERS);
    fv = rq.v[17];
  }
  ws = cx.ws;
  const int bid = cx.vb, idx = bid >> 3;
  const int slot = (bid & 7) * 4 + (idx >> 3), b = (idx >> 2) & 1, nt = idx & 3;
  const int lane = (int)__builtin_amdgcn_mbcnt_hi(~0u, __builtin_amdgcn_mbcnt_lo(~0u, 0u));
  const int c64i = nt * 2 + ((bid >> 2) & 1), rg = (bid & 1) * 8 + (slot & 1) * 4 + (bid & 2) + (idx >> 4);
  const int hw = wv >> 2, wp = hw ? 7 - wv : wv, cg = c64i * 2 + hw;
  const int bs = 10 * rg;
  const int ntk = wp < 2 ? 3 : 2;
  const int g = lane >> 4, qq = (lane >> 2) & 3, pp = lane & 3, fr = lane & 15;
  const int ch0 = cg * 32 + 8 * g;
  const unsigned cnt = lds0 + 8 * 12288 + 3072 + hw * 8;
  if (wv == 2) asm volatile("ds_write_b32 %0, %1\n\ts_waitcnt lgkmcnt(0)" :: "v"(cnt + (lane & 3) * 4), "v"(0u) : "memory");
#pragma unroll
  for (int i = 0; i < 8; ++i) gb[i] = *reinterpret_cast<const bf16x8*>(smem + i * 1024 + lane * 16);
  const int l0 = HOP * (bs + wp) + fr, le = (fr >> 1) < ntk ? HOP * (bs + wp + 4 * (fr >> 1)) + 48 + (fr & 1) : 1 << 20;
  const int lend = min(HOP * (bs + 10), L_);
  f32x4 nk, rk;
  float rn = 1.0f / (float)(lend - HOP * bs);
#pragma unroll
  for (int k = 0; k < 4; ++k) {
    nk[k] = (float)(2 * HOP + min(max(lend - HOP * (bs + k + 8), 0), HOP));
    rk[k] = 1.0f / nk[k];
  }
  asm volatile("" : "+v"(nk), "+v"(rk), "+v"(rn));
  f32x4 cin[2];
#pragma unroll
    for (int s = 0; s < 2; ++s) {
      cin[s] = *reinterpret_cast<const f32x4*>(smem + CBUF + min(wp + 4 * (fr >> 1), 10) * 272 + hw * 128 + g * 32 + s * 16);
#pragma unroll
      for (int r = 0; r < 4; ++r) cin[s][r] = fr & 1 ? __builtin_amdgcn_ldexpf(cin[s][r], cx.es) : 0.f;
    }
    asm volatile("" : "+v"(cin[0]), "+v"(cin[1]));
#pragma unroll
  for (int i = 0; i < 8; i += 4) asm volatile("" : "+v"(gb[i]), "+v"(gb[i + 1]), "+v"(gb[i + 2]), "+v"(gb[i + 3]));
  SB0;
  unsigned so[3], vo = (unsigned)((lane >> 2) * 64 + (((lane & 3) ^ ((lane >> 3) & 2)) << 4));
#pragma unroll
  for (int i = 0; i < 3; ++i) {
    const unsigned yr = (unsigned)(b * NBLK + bs + wp + 4 * i);
    so[i] = i < ntk ? (yr * 16 + (cg ^ ((yr >> 4) & 2))) * 4096 : 0xf0000000u;
  }
  asm volatile("s_waitcnt vmcnt(0)" : "+v"(fv), "+v"(vo), "+s"(so[0]), "+s"(so[1]), "+s"(so[2]) :: "memory");
  if (wv == 0 && !__all(fv != 0u)) spin_flags(ws + WS_GRP + nt * 256 + b * 128 + ((bid & 7) + (bid & 4)) * 1024 + ((lane & 31) ^ ((lane & 31) == slot)) * 4);
  __builtin_amdgcn_s_barrier();
  char* my = smem + wv * 12288;
  if (1) { if (1 & 4 && !hw) __builtin_amdgcn_s_setprio(3); else __builtin_amdgcn_s_setprio(2); }
  if (24 && hw) __builtin_amdgcn_s_sleep(24);
  if (12 && (nt & 1)) __builtin_amdgcn_s_sleep(12);
  if (0 && b) __builtin_amdgcn_s_sleep(0);
  {
    const u32x4 rs = make_rsrc(ws + WS_Y, ROWS * 65536);
#pragma unroll
    for (int i = 0; i < 3; ++i)
      asm volatile("s_mov_b32 m0, %0\n\ts_nop 0\n\tbuffer_load_dwordx4 %1, %2, %3 offen sc1 lds\n\tbuffer_load_dwordx4 %1, %2, %3 offen offset:1024 sc1 lds\n\t"
                   "buffer_load_dwordx4 %1, %2, %3 offen offset:2048 sc1 lds\n\tbuffer_load_dwordx4 %1, %2, %3 offen offset:3072 sc1 lds"
                   :: "s"(lds0 + wv * 12288 + i * 4096), "v"(vo), "s"(rs), "s"(so[i]) : "memory");
  }
  f32x4 y[24], npv[2];
  float s1[8], s2[8];
  {
    bf16x8 a[12];
    const char* src = my + (4 * g + qq) * 64 + ((g & 1) << 5) + pp * 8;
    auto rd = [&](int q) __attribute__((always_inline)) {
      const char* p = reinterpret_cast<const char*>(smem + (((unsigned)(src - smem)) ^ (unsigned)((q & 2) << 4)) + (q >> 2) * 4096 + (q & 1) * 2048);
      a[q] = cat(tr_read(p), tr_read(p + 1024));
    };
    auto sums = [&](int cp, int q) __attribute__((always_inline)) {
      const int e = (cp & 4) + q;
      if ((cp & 3) == 3) {
        if (cp >= 8) y[cp & 7][q] = (fr >> 1) == (cp >> 3) ? y[cp][q] : y[cp & 7][q];
        return;
      }
      float d = y[cp][q];
      if (cp >= 16 && l0 + 8 * HOP + 16 * (cp & 3) >= lend) d = 0.f;
      if (cp == 0 || cp == 4) { s1[e] = d; s2[e] = d * d; }
      else { s1[e] += d; s2[e] = __builtin_fmaf(d, d, s2[e]); }
      asm volatile("" : "+v"(s1[e]), "+v"(s2[e]));
    };
    wait_vm<8>();
    SB0;
    u32x2 pv[2];
#pragma unroll
    for (int s = 0; s < 2; ++s) pv[s] = *reinterpret_cast<const u32x2*>(my + s * 32 + g * 8);
    SB0;
#pragma unroll
    for (int q = 0; q < 4; ++q) { rd(q); SB0; }
#pragma unroll
    for (int s = 0; s < 2; ++s) {
      npv[s] = f32x4{bf_lo(pv[s][0]), bf_hi(pv[s][0]), bf_lo(pv[s][1]), bf_hi(pv[s][1])} * (-1.0f / 64.0f);
      cin[s] += npv[s];
    }
    SB0;
    auto chain = [&](auto cc) __attribute__((always_inline)) {
      constexpr int c = cc.v, is = c >> 2, tt = c & 3;
      if (c == 7) wait_vm<4>();
      if (c == 15) wait_vm<0>();
#pragma unroll
      for (int q = 0; q < 4; ++q) {
        if (c < 24 && !(q & 1)) y[c % 24] = MFMA(a[(2 * is + (q >> 1)) % 12], gb[2 * tt + (q >> 1)], q ? y[c % 24] : tt == 3 ? cin[is & 1] : npv[is & 1]);
        SB0;
        if (c == 7 || c == 15) rd((c == 7 ? 4 : 8) + q);
        if (c >= 2) sums(c - 2, q);
        SB0;
      }
    };
    sfor<0, 16>(chain);
    if (ntk == 3) {
      sfor<16, 26>(chain);
    } else {
#pragma unroll
      for (int q = 0; q < 8; ++q) sums(14 + (q >> 2), q & 3);
    }
#pragma unroll
    for (int e = 0; e < 8; ++e) {
      const float d = le < lend ? y[3 + (e & 4)][e & 3] : 0.f;
      s1[e] += d;
      s2[e] = __builtin_fmaf(d, d, s2[e]);
    }
  }
  float* red = reinterpret_cast<float*>(smem + 8 * 12288);
  {
    float v[8];
#pragma unroll
    for (int j = 0; j < 8; ++j) v[j] = bfly<0x140>(s1[j], s2[j], fr & 8);
#pragma unroll
    for (int j = 0; j < 4; ++j) v[j] = bfly<0x141>(v[j], v[j + 4], fr & 4);
#pragma unroll
    for (int j = 0; j < 2; ++j) v[j] = bfly<0x4E>(v[j], v[j + 2], fr & 2);
    red[((fr >> 3) * 8 + 2 * wp + hw) * 32 + 8 * g + (fr & 7)] = bfly<0xB1>(v[0], v[1], fr & 1);
    if (fr < 2) *reinterpret_cast<f32x4*>(red + (16 + 2 * wp + hw) * 32 + 8 * g + 4 * fr) = fr ? npv[1] : npv[0];
  }
  unsigned arr = 1u;
  if (lane == 0) asm volatile("ds_add_rtn_u32 %0, %1, %0\n\ts_waitcnt lgkmcnt(0)" : "+v"(arr) : "v"(cnt) : "memory");
  unsigned long long* stats = reinterpret_cast<unsigned long long*>(ws + WS_STATS);
  if (__builtin_amdgcn_readfirstlane(arr) == 3u && lane < 32) {
    const int tid = hw * 32 + lane;
    float mk[4], sm = 0.f, m2 = 0.f;
#pragma unroll
    for (int k = 0; k < 4; ++k) {
      const float* e = red + 64 * k + tid;
      const float dm = e[0] * rk[k];
      mk[k] = dm - e[512];
      m2 += fmaxf(e[256] - e[0] * dm, 0.f);
      sm += nk[k] * mk[k];
    }
    const float mean = sm * rn;
#pragma unroll
    for (int k = 0; k < 4; ++k) m2 += nk[k] * (mk[k] - mean) * (mk[k] - mean);
    unsigned long long pk = (unsigned long long)__float_as_uint(mean) | ((unsigned long long)__float_as_uint(m2) << 32);
    if (pk == ~0ull) pk = ~1ull;
    __hip_atomic_store((gu64*)(stats + (b * 16 + rg + (rg & 12) + (c64i & 1) * 4 + b * 16) * 256 + nt * 64 + tid), pk, RLX_AGENT);
  }
  constexpr float GS = -0.4686686930532063f, GA = -2.0f * 0.7978845608028654f * 1.4426950408889634f / GS;
  const unsigned short* gp = reinterpret_cast<const unsigned short*>(smem + GAM + hw * 64) + (lane & 31);
  float gs = bf_lo(gp[0]) * GS, bt = bf_lo(gp[64]) * GS, eps = fmaxf(__builtin_amdgcn_ldexpf(1e-5f, 2 * cx.es), 1e-30f);
  float cl = lane < 32 ? 0.f : -15.f / 500.f;
  asm volatile("" : "+v"(gs), "+v"(bt), "+v"(eps), "+v"(cl));
  unsigned long long* gl = reinterpret_cast<unsigned long long*>(smem + 8 * 12288 + 4096);
  {
    unsigned long long pk[2];
    const int row = 4 * wp + (lane >> 5), col = hw * 32 + (lane & 31);
    for (unsigned spins = 0;; ++spins) {
#pragma unroll
      for (int r = 0; r < 2; ++r)
        pk[r] = __hip_atomic_load((gu64*)(stats + (b * 32 + row + 2 * r + 4 * wp + (c64i & 1) * 4) * 256 + nt * 64 + col), RLX_AGENT);
      if (__all(pk[0] != ~0ull && pk[1] != ~0ull) || spins > (1u << 20)) break;
      if (1) __builtin_amdgcn_s_sleep(1);
    }
    gl[row * 66 + col] = pk[0];
    gl[(row + 2) * 66 + col] = pk[1];
    if (lane == 0) asm volatile("ds_add_u32 %0, %1" :: "v"(cnt + 4), "v"(1u) : "memory");
    unsigned c;
    do asm volatile("ds_read_b32 %0, %1\n\ts_waitcnt lgkmcnt(0)" : "=v"(c) : "v"(cnt + 4) : "memory");
    while (__builtin_amdgcn_readfirstlane(c) < 4u);
  }
  f32x4 ga[2], be[2];
  {
    const f32x2* gr = reinterpret_cast<const f32x2*>(gl + hw * 32 + (lane & 31));
    const f32x2 p = {gr[0][0], 0.f};
    f32x2 d, sm = {0.f, 0.f};
    float q = 0.f;
#pragma unroll
    for (int k = 0; k < 8; ++k) {
      d = gr[((lane >> 5) * 8 + k) * 66] - p;
      sm += d;
      q = __builtin_fmaf(d[0], d[0], q);
    }
    const float dl = cl * d[0];
    const float dm = halves_sum(sm[0] + dl) * (500.f / L_), var = halves_sum(sm[1] + 500.f * __builtin_fmaf(dl, d[0], q)) * (1.f / L_) - dm * dm;
    gs *= __builtin_amdgcn_rsqf(fmaxf(var, 0.f) + eps);
    float* am = reinterpret_cast<float*>(my);
    am[lane] = lane < 32 ? gs : bt - (p[0] + dm) * gs;
#pragma unroll
    for (int s = 0; s < 2; ++s) {
      ga[s] = *reinterpret_cast<const f32x4*>(am + 8 * g + 4 * s);
      be[s] = *reinterpret_cast<const f32x4*>(am + 32 + 8 * g + 4 * s) - npv[s] * ga[s];
    }
  }
  {
    if (1) __builtin_amdgcn_s_setprio(0);
    f32x4 k1 = {GA, GA, GA, GA}, k2 = {GS, GS, GS, GS};
    asm volatile("" : "+v"(k1), "+v"(k2));
    const u32x4 ro = make_rsrc(out + (size_t)b * L_ * D_, lend * (D_ * 2));
    const unsigned oo = (unsigned)((l0 * D_ + ch0) * 2);
#pragma unroll
    for (int i = 0; i < 3; ++i)
      if (i < 2 || ntk == 3)
#pragma unroll
      for (int tt = 0; tt < 4; ++tt) {
        if (tt == 3 && i) break;
        f32x4 o[2];
#pragma unroll
        for (int s = 0; s < 2; ++s) {
          const f32x4 z = y[8 * i + 4 * s + tt] * ga[s] + be[s];
          const f32x4 a = z * (z * z + k1);
          f32x4 e;
#pragma unroll
          for (int r = 0; r < 4; ++r) e[r] = __builtin_amdgcn_exp2f(a[r]);
          e = e * k2 + k2;
#pragma unroll
          for (int r = 0; r < 4; ++r) e[r] = __builtin_amdgcn_rcpf(e[r]);
          o[s] = z * e;
        }
        st16(pack8(o[0], o[1]), ro, tt < 3 ? oo : (unsigned)((le * D_ + ch0) * 2), tt < 3 ? (4 * HOP * i + 16 * tt) * (D_ * 2) : 0);
      }
  }
  asm volatile(".Lcf_b:\n\ts_nop 0" ::: "memory");
}

extern "C" void kernel_launch(void* const* d_in, const int* in_sizes, int n_in,
                              void* d_out, int out_size, void* d_ws, size_t ws_size,
                              hipStream_t stream) {
  const unsigned short* x = (const unsigned short*)d_in[0];
  const unsigned short* w = (const unsigned short*)d_in[1];
  const unsigned short* gamma = (const unsigned short*)d_in[2];
  const unsigned short* beta = (const unsigned short*)d_in[3];
  char* ws = (char*)(((uintptr_t)d_ws + (2u << 20) - 1) & ~(uintptr_t)((2u << 20) - 1));
  conv_fft<<<256, 512, 0, stream>>>(ws, x, w, gamma, beta, (unsigned short*)d_out);
}
```

```cpp
#include <hip/hip_runtime.h>
#include <stdint.h>

typedef __attribute__((ext_vector_type(8))) short bf16x8;
typedef __attribute__((ext_vector_type(4))) short s16x4;
typedef __attribute__((ext_vector_type(4))) float f32x4;
typedef __attribute__((ext_vector_type(2))) float f32x2;
typedef __attribute__((ext_vector_type(2))) __bf16 bf16x2v;
typedef __attribute__((ext_vector_type(4))) unsigned int u32x4;
typedef __attribute__((ext_vector_type(2))) unsigned int u32x2;
constexpr int NH = 10;
typedef __attribute__((ext_vector_type(8))) unsigned int u32x8;
typedef __attribute__((ext_vector_type(16))) unsigned int u32x16;
typedef __attribute__((ext_vector_type(8))) _Float16 f16x8;
typedef __attribute__((ext_vector_type(2))) _Float16 f16x2;
typedef __attribute__((ext_vector_type(2))) unsigned short u16x2;
typedef __attribute__((address_space(1))) unsigned long long gu64;
typedef __attribute__((address_space(1))) unsigned gu32;
typedef __attribute__((address_space(3))) void lds_void;
typedef __attribute__((address_space(3))) s16x4 lds_s16x4;

constexpr unsigned long long bytes(unsigned a, unsigned b = 0, unsigned c = 0, unsigned d = 0, unsigned e = 0, unsigned f = 0, unsigned g = 0, unsigned h = 0) {
  return a | b << 8 | c << 16 | d << 24 | (unsigned long long)(e | f << 8 | g << 16 | h << 24) << 32;
}
constexpr int T_ = 16000, D_ = 512, L_ = 7985;
constexpr int HOP = 50, NBLK = 160, ROWS = 2 * NBLK;

constexpr unsigned WS_S = 0;
constexpr unsigned SBLK = 4u << 20;
constexpr unsigned sbase(unsigned sl) { return sl * 131072 + (sl >> 4) * 8192; }
constexpr unsigned FLG = 32u * SBLK;
constexpr unsigned WS_GRP = WS_S + FLG + 16 * 8192;
constexpr unsigned WS_D = WS_S + FLG + 20 * 8192;
constexpr unsigned WS_L = WS_D + ROWS * 1024;
constexpr unsigned WS_Y = 40u << 20;
constexpr unsigned WS_STATS = 129u << 20;
constexpr unsigned WS_FA = WS_STATS + (1u << 17);
constexpr unsigned WS_PUB = WS_STATS + (1u << 19);
static_assert(WS_L + D_ * D_ * 2 <= WS_STATS && FLG % (16 * 8192) == 0, "workspace layout");

#define RLX_AGENT __ATOMIC_RELAXED, __HIP_MEMORY_SCOPE_AGENT

constexpr double COS64[17] = {1.0, 0.99518472667219688624, 0.98078528040323044913, 0.95694033573220886494,
    0.92387953251128675613, 0.88192126434835502971, 0.83146961230254523708, 0.77301045336273696081,
    0.70710678118654752440, 0.63439328416364549822, 0.55557023301960222474, 0.47139673682599764856,
    0.38268343236508977173, 0.29028467725446236764, 0.19509032201612826785, 0.09801714032956060199, 0.0};
constexpr double c64(int k) { k &= 63; if (k > 32) k = 64 - k; return k <= 16 ? COS64[k] : -COS64[32 - k]; }
constexpr double s64(int k) { return c64(k + 48); }
constexpr unsigned short bf_rne(double v) {
  const unsigned u = __builtin_bit_cast(unsigned, (float)v);
  return (unsigned short)((u + 0x7fffu + ((u >> 16) & 1u)) >> 16);
}
constexpr double R3 = 0.86602540378443864676;
constexpr double dft(int c, int t, double sgn) {
  return (c >> 1) == 0 ? ((c & t) & 1 ? -1.0 : 1.0)
                       : ((c & 1) ? -0.5 * c64((c >> 1) * t) - R3 * sgn * s64((c >> 1) * t) : c64((c >> 1) * t));
}
constexpr double idft(int c, int t) {
  return (c >> 1) == 0 ? ((c & t) & 1 ? -1.0 : 1.0) / 64.0
                       : ((c & 1) ? -s64((c >> 1) * t) / R3 - c64((c >> 1) * t) * (2.0 / 3.0) : c64((c >> 1) * t) * (4.0 / 3.0)) / 32.0;
}
constexpr int kidx(int g, int j) { return j < 4 ? 4 * g + j : 12 + 4 * g + j; }
struct Tables {
  unsigned short G[4][2][64][8];
  unsigned short F[4][2][64][8];
  unsigned short V[4][64][4];
};
constexpr Tables make_tables() {
  Tables tb{};
  for (int l = 0; l < 64; ++l)
    for (int j = 0; j < 8; ++j)
      for (int t = 0; t < 4; ++t) {
        const int g = l >> 4, n = l & 15;
        for (int ks = 0; ks < 2; ++ks) {
          tb.F[t][ks][l][j] = bf_rne(dft(16 * t + n, 32 * ks + kidx(g, j), -1.0));
          tb.G[t][ks][l][j] = bf_rne(idft(32 * ks + kidx(g, j), 16 * t + (t < 3 ? n : n & 1)));
        }
        tb.V[t][l][j & 3] = bf_rne(dft(16 * t + n, 4 * g + (j & 3), 1.0));
      }
  return tb;
}
__device__ const __attribute__((aligned(16))) Tables TAB = make_tables();

__device__ __forceinline__ unsigned pk_bf16(float lo, float hi) {
  return __builtin_bit_cast(unsigned, __builtin_convertvector(f32x2{lo, hi}, bf16x2v));
}
__device__ __forceinline__ float bf_lo(unsigned v) { return __uint_as_float(v << 16); }
__device__ __forceinline__ float bf_hi(unsigned v) { return __uint_as_float(v & 0xffff0000u); }
__device__ __forceinline__ u32x4 pack8(f32x4 a, f32x4 b) {
  return u32x4{pk_bf16(a[0], a[1]), pk_bf16(a[2], a[3]), pk_bf16(b[0], b[1]), pk_bf16(b[2], b[3])};
}
__device__ __forceinline__ unsigned pk_f16(float lo, float hi) {
  return (__builtin_bit_cast(unsigned, __builtin_convertvector(f32x2{lo, hi}, f16x2)) + 0x40004u) & 0xfff8fff8u;
}
__device__ __forceinline__ u32x4 pack8h(f32x4 a, f32x4 b) {
  return u32x4{pk_f16(a[0], a[1]), pk_f16(a[2], a[3]), pk_f16(b[0], b[1]), pk_f16(b[2], b[3])};
}
__device__ __forceinline__ unsigned add_h(const bf16x8& a, const bf16x8& b, int d) {
  const unsigned x = __builtin_bit_cast(u32x4, a)[d], y = __builtin_bit_cast(u32x4, b)[d];
  unsigned r = __builtin_bit_cast(unsigned, __builtin_bit_cast(f16x2, x) + __builtin_bit_cast(f16x2, y));
  return r;
}
__device__ __forceinline__ unsigned amax(unsigned m, unsigned v) {
  return __builtin_bit_cast(unsigned, __builtin_elementwise_max(__builtin_bit_cast(u16x2, m), __builtin_bit_cast(u16x2, v & 0x7fff7fffu)));
}
struct Rng { f16x2 hi = {0, 0}, lo = {0, 0}; };
__device__ __forceinline__ void widen(Rng& r, unsigned a, unsigned b) {
  const f16x2 x = __builtin_bit_cast(f16x2, a), y = __builtin_bit_cast(f16x2, b);
  r.hi = __builtin_elementwise_maximum(__builtin_elementwise_maximum(r.hi, x), y);
  r.lo = __builtin_elementwise_minimum(__builtin_elementwise_minimum(r.lo, x), y);
}
__device__ __forceinline__ unsigned amax(const Rng& r) { return amax(amax(0, __builtin_bit_cast(unsigned, r.hi)), __builtin_bit_cast(unsigned, r.lo)); }
__device__ __forceinline__ unsigned expo(unsigned m) { return max(m & 0xffffu, m >> 16) >> 7; }
__device__ __forceinline__ unsigned wave_max(unsigned v) {
  unsigned m = 0;
#pragma unroll 1
  for (int l = 0; l < 64; ++l) m = max(m, (unsigned)__builtin_amdgcn_readlane((int)v, l));
  return m;
}
__device__ __forceinline__ unsigned scale2(unsigned u, int e) {
  return __builtin_amdgcn_perm(__float_as_uint(__builtin_amdgcn_ldexpf(bf_hi(u), e)), __float_as_uint(__builtin_amdgcn_ldexpf(bf_lo(u), e)), 0x07060302u);
}
__device__ __forceinline__ void restart(unsigned id) {
  asm volatile("s_waitcnt vmcnt(0)\n\ts_barrier\n\ts_branch conv_fft" :: "{s[0:1]}"(__builtin_amdgcn_kernarg_segment_ptr()), "{s2}"(id), "{v0}"(threadIdx.x) : "memory");
}
__device__ __forceinline__ s16x4 tr_read(const char* p) {
  return __builtin_amdgcn_ds_read_tr16_b64_v4i16((lds_s16x4*)p);
}
__device__ __forceinline__ bf16x8 cat(s16x4 a, s16x4 b) {
  const u32x2 x = __builtin_bit_cast(u32x2, a), y = __builtin_bit_cast(u32x2, b);
  return __builtin_bit_cast(bf16x8, u32x4{x[0], x[1], y[0], y[1]});
}
#define MFMA(a, b, c) __builtin_amdgcn_mfma_f32_16x16x32_bf16(a, b, c, 0, 0, 0)
#define MFMS(a, b) __builtin_amdgcn_mfma_f32_16x16x16bf16_1k(a, __builtin_bit_cast(s16x4, b), ZERO4, 0, 0, 0)
#define SB0 __builtin_amdgcn_sched_barrier(0)
#define ZERO4 (f32x4{0.f, 0.f, 0.f, 0.f})
#define DMA16(POL, rs, lds, voff, soff) \
  asm volatile("s_mov_b32 m0, %0\n\ts_nop 0\n\tbuffer_load_dwordx4 %1, %2, %3 offen " POL " lds" \
               :: "s"(lds), "v"(voff), "s"(rs), "s"(soff) : "memory")
__device__ __forceinline__ void st16(const u32x4& v, const u32x4& rs, unsigned voff, unsigned soff) {
  asm volatile("buffer_store_dwordx4 %0, %1, %2, %3 offen sc1\n\ts_nop 1" :: "v"(v), "v"(voff), "s"(rs), "s"(soff) : "memory");
}
template <int N> __device__ __forceinline__ void wait_vm() { asm volatile("s_waitcnt vmcnt(%0)" :: "i"(N) : "memory"); }
__device__ __forceinline__ u32x4 make_rsrc(const void* p, unsigned bytes) {
  const unsigned long long a = (unsigned long long)p;
  return u32x4{(unsigned)a, (unsigned)(a >> 32) & 0xffffu, bytes, 0x00020000u};
}
__device__ __forceinline__ void spin_flags(const char* p) {
  for (unsigned spins = 0; !__all(__hip_atomic_load((const gu32*)p, RLX_AGENT) != 0u) && spins < (1u << 21); ++spins)
    __builtin_amdgcn_s_sleep(3);
}

constexpr int GW = 10;
constexpr int NQ1 = GW / 2, NQW = 16 - NQ1;
constexpr int WSL = 16384, XSL = 20480, XRING = 3 * WSL;
constexpr int RAWB = 67 * 1024, XT1 = RAWB + NQ1 * 4096, SINK = XT1 + RAWB;
constexpr int RAWL = XRING + 3 * XSL;
constexpr int CORR = RAWL + 4096, CORD = RAWL + NQW * 4096;
constexpr int GAM = RAWL + 52224, CBUF = RAWL + 49152;

struct Ctx {
  char* smem;
  char* ws;
  const char* out;
  const unsigned short *gamma, *beta;
  u32x4 rs;
  unsigned ldg, ldf, sg, sf;
  unsigned voff, voff5, crd;
  unsigned xbase, wbase;
  unsigned pwr, prd, poff;
  unsigned xo;
  unsigned lane4, fvo;
  unsigned id;
  unsigned tag[2];
  int es;
  int wv, vb;
  unsigned long long pc;
  unsigned pfo;
};
__device__ __forceinline__ void code_touch(const Ctx& cx, int h0 = 0, int h1 = 2) {
  if (((cx.wv ^ (cx.vb >> 3)) & 3) | (cx.vb & (h0 || h1 == 1 ? 0 : 0))) return;
#pragma unroll
  for (int h = h0; h < h1; ++h)
    asm volatile("s_mov_b32 m0, %0\n\ts_nop 4\n\tglobal_load_lds_dword %1, %2" :: "s"(SINK + cx.wv * 256), "v"(min(__builtin_amdgcn_mbcnt_hi(~0u, __builtin_amdgcn_mbcnt_lo(~0u, cx.wv * 64u + h * 512u)) * 128u, cx.pfo)), "s"(cx.pc) : "memory");
}
__device__ __forceinline__ void x_store(const Ctx& cx, const u32x4& v, unsigned kb, unsigned blk) {
  st16(v, cx.rs, cx.xo, kb + (blk + (blk & 192u)) * 128u);
}
template <int NM>
__device__ __forceinline__ void stage_issue(const Ctx& cx, int kc) {
  const unsigned slot = (unsigned)kc * (NM == 6 ? WSL : XSL);
  asm volatile("s_mov_b32 m0, %0\n\ts_nop 0\n\tbuffer_load_dwordx4 %1, %2, %3 offen sc1 lds\n\tbuffer_load_dwordx4 %1, %2, %3 offen offset:1024 sc1 lds\n\t"
                 "buffer_load_dwordx4 %1, %2, %3 offen offset:2048 sc1 lds\n\tbuffer_load_dwordx4 %1, %2, %3 offen offset:3072 sc1 lds"
                 :: "s"(cx.ldg + slot), "v"(cx.voff), "s"(cx.rs), "s"(cx.sg + kc * SBLK) : "memory");
  if (NM == 5) DMA16("sc1", cx.rs, cx.ldf + slot, cx.voff, cx.sf + kc * SBLK);
}
template <int V> struct IC { static constexpr int v = V; constexpr operator int() const { return V; } };
template <int A, int B, class F> __device__ __forceinline__ void sfor(F f) {
  if constexpr (A < B) { f(IC<A>{}); sfor<A + 1, B>(f); }
}
struct RQ { u32x4 h[NH]; u32x2 raw[NQW]; u32x2 vb; unsigned s[32], v[19]; };

__device__ __forceinline__ int run_bs(int vb, int t) { return 5 * ((vb >> 4) + 16 * ((vb ^ t) & 1)); }
__device__ __forceinline__ int run_q(int vb, int t) { return (((vb >> 1) & 1) + ((vb >> 2) & 2) + 2 * t) & 3; }
__device__ __forceinline__ unsigned x_addr(int vb, int t, int wv) {
  const int lane = threadIdx.x & 63, q = run_q(vb, t);
  const unsigned c = (unsigned)((lane & 15) ^ (((4 * wv + (lane >> 4)) & 7) << 1));
  return (unsigned)(2 * (HOP * run_bs(vb, t) + 4 * wv + (lane >> 4)) + ((t ^ q ^ (q >> 1)) & 1)) * 1024u + c * 16 + q * 256;
}
__device__ __forceinline__ u32x4 x_rsrc(const unsigned short* x, int vb) { return make_rsrc(x + (size_t)((vb >> 2) & 1) * T_ * D_, T_ * D_ * 2); }
__device__ __forceinline__ void issue_x(int t, int wv, unsigned a, const u32x4& rx, unsigned lds0, int j0 = 0, int j1 = 9) {
#pragma unroll
  for (int jj = 0; jj < 9; ++jj)
    if (jj >= j0 && jj < j1 && (jj < 8 || wv < 3)) DMA16("nt", rx, lds0 + t * XT1 + wv * 1024 + jj * 8192, a, jj * 65536u);
}
struct XR { s16x4 t[8]; };
constexpr int P0 = 3, P1 = 23;
constexpr int PG = 20, NFA = 1;
constexpr int TE = 4;
constexpr int NIT = 23, NS = 30 - NH, NA = 7, NB = 13;
constexpr int ord(int p) { return p < 0 || p >= NIT ? -1 : p == 0 ? 0 : p < 6 ? p + 2 : p < 8 ? p - 5 : p; }
constexpr int st_w(int j) { return j < 2 ? j : j < 7 ? -1 : j < 15 ? j - 5 : -1; }
constexpr int st_x(int j) { return j < 7 ? j - 2 : j - 10; }
constexpr int st_pos(int j) { return st_w(j) >= 0 ? (j < 9 ? 0 : j < 13 ? 6 : 7) : j < 7 ? j - 1 : j - 7; }
constexpr int st_first(int p) {
  int j = p <= P0 ? 0 : p >= P1 ? NS : (p - P0) * NS / (P1 - P0);
  while (j > 0 && p <= NIT && st_pos(j - 1) + 2 > p - 1) --j;
  return j;
}
template <int NM, class F>
__device__ __forceinline__ void phase1(Ctx& cx, const unsigned short* __restrict__ x, const unsigned short* __restrict__ w,
                                       RQ& rqv, unsigned& xs, u32x2& vt, F setup) {
  u32x2 rq[NQW];
  static_assert(NQ1 == 5, "phase-1 wait counts assume five quanta");
  char* smem = cx.smem;
  const unsigned lds0 = (unsigned)(uintptr_t)(lds_void*)smem;
  const int idx = cx.vb >> 3, wv = cx.wv, lane = threadIdx.x & 63;
  const u32x4 rw = make_rsrc(w, 31 * D_ * D_ * 2);
  const int tap = threadIdx.x >> 5, pc = threadIdx.x & 31;
  const unsigned roff = (unsigned)(tap * (2 * D_ * D_ * 2) + pc * 8);
  const int ng = wv & 3, mg = wv >> 2;
  const int g = lane >> 4, qq = (lane >> 2) & 3, pp = lane & 3;
  u32x2 r1[NQ1];
  u32x4 fb[2];
  unsigned long long tag;
  const char* rec = cx.ws + WS_PUB + ((cx.id & 7) * 8 + wv) * 16;
  u32x4 pr, wl;
  {
    const int vb = cx.vb;
    const int kr = (vb >> 7) * 32 + (vb & 4) * 4 + (idx & 15);
    auto raw_load = [&](u32x2& r, int q) {
      const unsigned so = (unsigned)(kr + 64 * (2 * (q & 3) + ((q >> 2) & 1)) + 512 * (((q >> 3) ^ q ^ (q >> 1)) & 1)) * 1024u + (unsigned)((vb + q) & 3) * 256u;
      asm volatile("buffer_load_dwordx2 %0, %1, %2, %3 offen nt" : "=v"(r) : "v"(roff), "s"(rw), "s"(so) : "memory");
    };
#pragma unroll
    for (int k = 0; k < NQ1; ++k) raw_load(r1[k], k);
    const unsigned xa1 = x_addr(vb, 1, wv);
    issue_x(1, wv, xa1, x_rsrc(x, vb), lds0, 0, TE);
    if (NM == 6 && (cx.id | wv) == 0) {
      unsigned g[2] = {~0u, ~0u};
#pragma unroll 1
      for (int k = -1; k < 16 && ((k & 1) || min(g[0], g[1]) > 440u); ++k) {
        unsigned long long t0, t1;
        unsigned d;
        asm volatile("s_memtime %0\n\ts_waitcnt lgkmcnt(0)\n\ts_load_dword %2, %3, 0x0\n\ts_waitcnt lgkmcnt(0)\n\ts_memtime %1\n\ts_waitcnt lgkmcnt(0)"
                     : "=&s"(t0), "=&s"(t1), "=&s"(d) : "s"(rec + 32768 + (k < 0 ? 128 : (k & 1) * 8192 + (k & 2) * 2048 + (k & 4) * 4096 + (k & 8) * 128)) : "memory");
        if (k >= 0) { if (k & 1) g[1] = min(g[1], (unsigned)(t1 - t0)); else g[0] = min(g[0], (unsigned)(t1 - t0)); }
      }
      const unsigned lo = cx.tag[0] | (((g[0] < g[1] ? 0u : 1u) ^ (cx.vb >> 2)) & 1u), hi = cx.tag[1];
      asm volatile("global_store_dwordx4 %0, %1, %2 sc1\n\ts_nop 1" :: "v"(lane * 16 + 2048), "v"(u32x4{lo, hi, lo, hi}), "s"(rec) : "memory");
    }
    {
#pragma unroll
      for (int i = 0; i < 2; ++i) asm volatile("global_load_dwordx4 %0, %1, off" : "=v"(fb[i]) : "v"(TAB.F[ng][i][lane]) : "memory");
      asm volatile("global_load_dwordx2 %0, %1, off" : "=v"(vt) : "v"(TAB.V[ng][lane]) : "memory");
      asm volatile("global_load_dwordx4 %0, %1, %2 sc1" : "=v"(pr) : "v"(0u), "s"(rec) : "memory");
    }
    if (NM == 5 && wv >= 6)
      asm volatile("buffer_load_dwordx4 %0, %1, %2, %3 offen nt" : "=v"(wl) : "v"((threadIdx.x & 127) * 16), "s"(rw), "s"(30 * D_ * D_ * 2 + vb * 2048) : "memory");
    if (cx.id == 0 && wv >= TE - 1)
      DMA16("nt", x_rsrc(x, vb), lds0 + XT1 + (wv + 1) * 8192, x_addr(vb, 1, 0), (wv + 1) * 65536u);
    if (NM == 5 || (cx.id | wv)) issue_x(1, wv, xa1, x_rsrc(x, vb), lds0, TE, 9);
#pragma unroll
    for (int k = 0; k < NQW; ++k) raw_load(rq[k], NQ1 + k);
    if (1 == 1 && cx.id) code_touch(cx, 1, 2);
    if (1 && NM == 5 && wv == 5) {
      const char* p = lane < 2 ? cx.ws + (idx + 32 * lane) * (2u << 20) + 65536 : cx.out + idx * 511040;
      asm volatile("s_mov_b32 m0, %0\n\ts_mov_b64 exec, 7\n\tglobal_load_lds_dword %1, off nt\n\ts_mov_b64 exec, -1" :: "s"(SINK + 5 * 256), "v"(p + (vb & 7) * 128) : "memory");
    }
    {
      const int bid = vb, xb = (bid >> 2) & 1, krow = kr;
      const int slot = (bid & 7) * 4 + (idx >> 3), b = (idx >> 2) & 1, nt = idx & 3;
      const int rt = 4 * g + qq;
      cx.lane4 = lane * 4;
      cx.fvo = NM == 5 ? (unsigned)((bid & 7) * 1024 + ng * 256 + lane * 4) : wv == 0 && lane < 8 ? (unsigned)(lane * 1024 + (bid >> 2) * 4) : 0x80000000u;
      cx.pwr = (unsigned)(RAWB + tap * 256 + (((pc >> 2) ^ (tap & 7)) << 5) + (pc & 3) * 8);
      cx.prd = (unsigned)(RAWB + rt * 256 + (((4 * mg) ^ (rt & 7)) << 5) + pp * 8);
      cx.poff = (unsigned)(sbase(8 * ng + ((lane >> 1) & 7)) + (krow >> 5) * SBLK + (krow & 24) * 512 + (krow & 7) * 128 +
                           mg * 2048 + (lane & 1) * 64 + g * 16);
      cx.xo = (unsigned)(sbase(8 * ng + ((lane >> 1) & 7)) + mg * SBLK + 65536 + xb * 8192 + (lane & 1) * 64 + g * 16);
      cx.xbase = (unsigned)(XRING + mg * 5 * 2048 + (lane & 8) * 128 + (lane & 7) * 64 + ((g ^ ((lane >> 1) & 2)) << 4));
      cx.wbase = (unsigned)((rt >> 3) * 4096 + ng * 1024 + (rt & 7) * 64 + (rt & 4) * 8 + pp * 8);
      const int pat = ((lane >> 2) & 7) * 128 + (lane >> 5) * 64 + (((lane & 3) ^ ((lane >> 3) & 2)) << 4);
      if (NM == 6) {
        cx.voff = (unsigned)(sbase(slot) + nt * 16384 + ng * 4096 + pat);
        cx.ldg = lds0 + ng * 4096;
        cx.ldf = lds0 + CORR + ng * 1024;
        cx.sg = 0;
        cx.sf = 0;
        const int kr8 = lane >> 3;
        cx.voff5 = (unsigned)(WS_L - WS_S + (8 * ng + kr8) * 1024 + (nt * 2 + xb) * 128 + (((lane & 7) ^ ((kr8 & 2) | (ng & 1) << 2)) << 4));
        cx.crd = (unsigned)(CORR + (8 * g + qq) * 128 + ((ng ^ ((qq >> 1) | (g & 1) << 1)) << 5) + pp * 8);
      } else {
        const int rg = (bid & 1) * 8 + (slot & 1) * 4 + (bid & 2) + (idx >> 4);
        cx.voff = (unsigned)(sbase(slot) + 65536 + b * 8192 + pat);
        cx.ldg = lds0 + XRING + (ng + (ng >> 1)) * 4096;
        cx.ldf = lds0 + XRING + 8192 + ng * 1024;
        cx.sg = (ng + (ng >> 1) * 3 + ((ng + 1) >> 2) * 2) * 4096;
        cx.sf = 16384 + ng * 1024;
        cx.voff5 = (unsigned)(WS_D - WS_S + (b * NBLK + 10 * rg + min(lane & 15, 9)) * 1024 + ng * 64 + g * 16);
      }
    }
    asm volatile("" : "+v"(cx.fvo), "+v"(cx.poff), "+v"(cx.xo), "+v"(cx.voff), "+v"(cx.voff5), "+v"(cx.lane4));
  }
  Rng gx, gw;
  auto scan = [&](int t) __attribute__((always_inline)) {
    u32x4 v[9];
#pragma unroll
    for (int j = 0; j < 9; ++j)
      if (j < 8 || NM == 6) v[j] = *reinterpret_cast<const u32x4*>(smem + t * XT1 + (j < 8 || wv < 3 ? j : 7) * 8192 + threadIdx.x * 16);
    SB0;
#pragma unroll
    for (int j = 0; j < 9; ++j)
      if (j < 8 || NM == 6) {
        widen(gx, v[j][0], v[j][1]);
        widen(gx, v[j][2], v[j][3]);
      }
    asm volatile("" : "+v"(gx.hi), "+v"(gx.lo));
    SB0;
  };
  if (NM == 6 && (cx.id | wv) == 0) wait_vm<NQW>();
  wait_vm<8 + 4 + NQW>();
  unsigned near;
  scan(0);
#pragma unroll
  for (int q = 0; q < NQ1; ++q) {
    asm volatile("" : "+v"(r1[q]));
    widen(gw, r1[q][0], r1[q][1]);
  }
  {
    wait_vm<NQW + 8 - TE>();
    unsigned d0, d1;
    for (unsigned spins = 0;; ++spins) {
      asm volatile("" : "+v"(pr));
      d0 = __builtin_amdgcn_readfirstlane(pr[0]);
      d1 = __builtin_amdgcn_readfirstlane(pr[1]);
      const unsigned d2 = __builtin_amdgcn_readfirstlane(pr[2]), d3 = __builtin_amdgcn_readfirstlane(pr[3]);
      if (__builtin_expect((d0 == ~d2 && d1 == ~d3) || spins > (1u << 21), 1)) break;
      __builtin_amdgcn_s_sleep(2);
      asm volatile("global_load_dwordx4 %0, %1, %2 sc1\n\ts_waitcnt vmcnt(0)" : "=v"(pr) : "v"(0u), "s"(rec) : "memory");
    }
    tag = d0 | (unsigned long long)d1 << 32;
    const unsigned vn = (cx.id + (d0 >> 1)) & 7u;
    if (__builtin_expect(vn != (cx.vb & 7u), 0)) restart(cx.id | 256u | vn << 10);
  }
  const unsigned ee = (cx.id & 512u ? cx.id : (unsigned)(tag >> 32)) >> 16, ex = ee & 255u, ew = ee >> 8;
  cx.es = 256 - (int)(ex + ew);
  char* ws;
  const int bid = cx.vb;
  u32x4 f0, f1;
  char* fab;
  const u32x4* fa;
  u32x2 ov;
  unsigned pollv;
  const int rb[2] = {run_bs(bid, 0), run_bs(bid, 1)};
  unsigned xb0[4];
#pragma unroll
  for (int k = 0; k < 4; ++k) xb0[k] = (unsigned)((4 * g + qq) * 256 + (((2 * mg) ^ ((4 * g + qq + 2 * k) & 7)) << 5) + pp * 8);
#pragma unroll
  for (int q = 0; q < NQ1; ++q) *reinterpret_cast<u32x2*>(smem + cx.pwr + q * 4096) = r1[q];
  asm volatile("" : "+v"(fb[0]), "+v"(fb[1]), "+v"(vt));
#pragma unroll
  for (int i = 0; i < 2; ++i) vt[i] = scale2(vt[i], 128 - (int)ew);
#pragma unroll
  for (int i = 0; i < 8; ++i) fb[i >> 2][i & 3] = scale2(fb[i >> 2][i & 3], 128 - (int)ex);
  asm volatile("" : "+v"(fb[0]), "+v"(fb[1]), "+v"(vt));
  asm volatile("s_waitcnt lgkmcnt(0)" : "+v"(xb0[0]), "+v"(xb0[1]), "+v"(xb0[2]), "+v"(xb0[3]) :: "memory");
  wait_vm<NQW>();
  asm volatile("global_load_dwordx2 %0, %1, %2 sc1" : "=v"(ov) : "v"(2048u), "s"(rec) : "memory");
  __builtin_amdgcn_s_barrier();
  SB0;
  u32x4 hw[6][2], hx[20];
  f32x4 pend[2];
  auto rd = [&](int it, int i) __attribute__((always_inline)) {
    if (it < 3) return tr_read(smem + ((cx.prd ^ (unsigned)((i & 3) << 5)) + (2 * it + (i >> 2)) * 4096));
    const int n = it - 3;
    const unsigned base = (xb0[n % 5 & 3] ^ (unsigned)((4 * ((n / 5) & 1) + (i & 1)) << 5)) + n / 10 * XT1;
    return tr_read(smem + (base + n % 5 * (HOP * 256) + (i >> 1) * (16 * 256)));
  };
  auto dst = [&](int it) -> u32x4& { return it < 3 ? hw[it == 2 ? 4 : 2 * it + 1][1] : hx[it - 3]; };
  auto item = [&](int it, int nx, int pv, XR& xr, auto side) __attribute__((always_inline)) {
    const XR c = xr;
    const int nm = it < 2 ? 8 : 4, nr = nx == 2 ? 4 : 8;
    f32x4 r[8];
#pragma unroll
    for (int i = 0; i < 8; ++i) {
      if (i >= nm) break;
      const int s2 = i & 1, ks = (i >> 1) & 1;
      if (it < 3) r[i] = MFMS(c.t[i], vt);
      else r[i] = MFMA(cat(c.t[4 * ks + s2], c.t[4 * ks + 2 + s2]), __builtin_bit_cast(bf16x8, fb[ks]), i < 2 ? ZERO4 : r[i - 2]);
      SB0;
      if (nx >= 0) {
#pragma unroll
        for (int j = 0; j < 8; ++j) if (j < nr && j * nm / nr == i) xr.t[j] = rd(nx, j);
      }
      if (pv >= 0 && (nm == 4 ? i >= 2 : i >= 1 && i <= 4)) {
        u32x4& d = dst(pv);
#pragma unroll
        for (int e = 0; e < 4; ++e)
          if ((nm == 4 ? 2 + (e >> 1) : 1 + e) == i) d[e] = pk_f16(pend[e >> 1][(e & 1) * 2], pend[e >> 1][(e & 1) * 2 + 1]);
        if (i == (nm == 4 ? 3 : 4)) asm volatile("" : "+v"(d));
      }
      if (it < 3 && i >= 3 && (i & 1)) {
        u32x4& d = hw[2 * it + ((i - 3) >> 2)][((i - 3) >> 1) & 1];
        d = pack8h(r[i - 3], r[i - 2]);
        asm volatile("" : "+v"(d));
      }
      side(i * (8 / nm));
      if (nm == 4) side(2 * i + 1);
      SB0;
    }
    pend[0] = r[nm - 2];
    pend[1] = r[nm - 1];
  };
  const unsigned so0 = (unsigned)(2 * run_q(bid, 0)) * SBLK, so1 = (unsigned)(16 + 2 * run_q(bid, 1)) * SBLK;
  auto store = [&](int j) __attribute__((always_inline)) {
    if (j == 0) {
      wait_vm<0>();
      for (unsigned spins = 0;; ++spins) {
        asm volatile("" : "+v"(ov));
        near = __builtin_amdgcn_readfirstlane(ov[0]) ^ (unsigned)tag;
        if (__builtin_expect((near < 2u && __builtin_amdgcn_readfirstlane(ov[1]) == (unsigned)(tag >> 32)) || spins > (1u << 21), 1)) break;
        __builtin_amdgcn_s_sleep(2);
        asm volatile("global_load_dwordx2 %0, %1, %2 sc1\n\ts_waitcnt vmcnt(0)" : "=v"(ov) : "v"(2048u), "s"(rec) : "memory");
      }
      ws = cx.ws += (near & 1u) * 8192;
      cx.rs = make_rsrc(ws + WS_S, WS_L - WS_S + D_ * D_ * 2);
      fab = ws + WS_FA + (cx.id & 512u) * 32;
      fa = reinterpret_cast<const u32x4*>(fab + (bid & 7) * 2048) + lane;
      if (NM == 6 && wv == 1) {
        const unsigned o = (lane < 32 ? WS_STATS : WS_S + FLG + 16384 - 512) + bid * 512 + lane * 16;
        const unsigned v = lane < 32 ? ~0u : 0u;
        asm volatile("global_store_dwordx4 %0, %1, %2 sc1\n\ts_nop 1" :: "v"(o), "v"(u32x4{v, v, v, v}), "s"(ws) : "memory");
        if (lane < 8) __hip_atomic_store((gu32*)(ws + WS_S + FLG + lane * 1024 + ((bid + 2) & 3) * 256 + (bid >> 2) * 4), 0u, RLX_AGENT);
      }
    }
    if (j == NB + 1 && NM == 6) {
      if (((bid >> 1) ^ (bid >> 3) ^ 1) & 1 && threadIdx.x < 160) {
        const int t = threadIdx.x >= 80, blk = (threadIdx.x >> 4) - 5 * t;
        const unsigned o = (unsigned)(t * XT1 + blk * (HOP * 256) + (((lane & 15) ^ ((blk & 3) << 2)) << 4));
        const u32x4 lo = *reinterpret_cast<const u32x4*>(smem + o), hi = *reinterpret_cast<const u32x4*>(smem + o + 64 * 256);
        u32x4 d;
#pragma unroll
        for (int e = 0; e < 4; ++e) d[e] = pk_bf16(bf_lo(hi[e]) - bf_lo(lo[e]), bf_hi(hi[e]) - bf_hi(lo[e]));
        st16(d, cx.rs, (unsigned)((((bid >> 2) & 1) * NBLK + (t ? rb[1] : rb[0]) + blk) * 1024 + run_q(bid, t) * 256 + (lane & 15) * 16), WS_D - WS_S);
      }
    }
    if (j == NB + 1 && NM == 5) {
      if (wv >= 6) {
        asm volatile("" : "+v"(wl));
        st16(wl, cx.rs, (threadIdx.x & 127) * 16, WS_L - WS_S + bid * 2048);
      }
    }
    const int wq = st_w(j), xn = st_x(j);
    if (wq >= 0) st16(hw[wq >> 1][wq & 1], cx.rs, cx.poff, (unsigned)(wq >> 1) * (2 * SBLK) + (unsigned)((bid + (wq >> 1)) & 3) * 16384u + (wq & 1) * 1024u);
    else x_store(cx, hx[xn], (xn < 10 ? so0 : so1) + ((xn / 5) & 1) * (8 * SBLK), (unsigned)(rb[xn / 10] + xn % 5));
    if (j == NA + NFA - 1) {
      scan(1);
#pragma unroll
      for (int k = 0; k < NQW; ++k) {
        asm volatile("" : "+v"(rq[k]));
        widen(gw, rq[k][0], rq[k][1]);
      }
      const unsigned mx = amax(gx), mw = amax(gw);
      if (__builtin_expect(__any(expo(mx) > ex + 7 || expo(mw) > ew + 9) && !(cx.id & 512u), 0)) {
        const u32x2 m = {wave_max(expo(mx)), wave_max(expo(mw))};
        asm volatile("global_atomic_umax %0, %1, %3 sc1\n\tglobal_atomic_umax %0, %2, %3 offset:4 sc1\n\ts_waitcnt vmcnt(0)"
                     :: "v"(lane * 16 + 1024), "v"(m[0]), "v"(m[1]), "s"(rec - ((cx.id & 7) * 8 + wv) * 16) : "memory");
      }
    }
    if (j == NA + NFA - 1 || j == NB + NFA - 1) {
      wait_vm<NFA>();
      __builtin_amdgcn_s_barrier();
      if (wv == 0 && lane < 8) {
        if (j < NB) __hip_atomic_store((gu64*)(fab + lane * 2048) + bid, tag, RLX_AGENT);
        else __hip_atomic_store((gu32*)(ws + WS_S + FLG + lane * 1024 + ((bid + 2) & 3) * 256 + (bid >> 2) * 4), 1u, RLX_AGENT);
      }
    }
    SB0;
  };
  XR xr;
#pragma unroll
  for (int j = 0; j < 8; ++j) xr.t[j] = rd(0, j);
  SB0;
  static_assert(st_first(NIT) == NS && PG >= 14 && st_first(PG - 2) >= NB + 2 && NA + NFA <= NB && NFA >= 1, "order of the hand-off steps");
  sfor<0, NIT>([&](auto p) __attribute__((always_inline)) {
    constexpr int j0 = st_first(p.v), j1 = st_first(p.v + 1);
    if (p >= 12 && p < PG) { if (!(bid & 1) && cx.id) __builtin_amdgcn_s_sleep(3); }
    if (p == PG - 2) {
      if (!0) code_touch(cx);
      if (NM == 6 && wv == 0)
        asm volatile("global_load_dwordx4 %0, %2, off sc1\n\tglobal_load_dwordx4 %1, %2, off offset:1024 sc1" : "=&v"(f0), "=&v"(f1) : "v"(fa) : "memory");
    }
    if (p == PG) {
      if (NM == 6 && wv == 0) {
        wait_vm<st_first(PG) - st_first(PG - 2)>();
        for (unsigned spins = 0; spins < (1u << 21); ++spins) {
          asm volatile("" : "+v"(f0), "+v"(f1));
          const unsigned t0 = (unsigned)tag, t1 = (unsigned)(tag >> 32);
          if (__all(f0[0] == t0 && f0[1] == t1 && f0[2] == t0 && f0[3] == t1 && f1[0] == t0 && f1[1] == t1 && f1[2] == t0 && f1[3] == t1)) break;
          __builtin_amdgcn_s_sleep(2);
          asm volatile("global_load_dwordx4 %0, %2, off sc1\n\tglobal_load_dwordx4 %1, %2, off offset:1024 sc1\n\ts_waitcnt vmcnt(0)"
                       : "=&v"(f0), "=&v"(f1) : "v"(fa) : "memory");
        }
      }
      __builtin_amdgcn_s_barrier();
      stage_issue<NM>(cx, 0);
      asm volatile("global_load_dwordx2 %0, %1, %2 sc1" : "=v"(ov) : "v"(1024u), "s"(rec) : "memory");
      stage_issue<NM>(cx, 1);
      SB0;
    }
    if (p == NIT - 1 && NM == 5) {
      asm volatile("buffer_load_dword %0, %1, %2, %3 offen sc1" : "=v"(pollv) : "v"(cx.fvo), "s"(cx.rs), "s"(FLG) : "memory");
    }
    item(ord(p), ord(p + 1), ord(p - 1), xr, [&](int i) __attribute__((always_inline)) {
      if ((i * (j1 - j0) + 7) / 8 < ((i + 1) * (j1 - j0) + 7) / 8) store(j0 + (i * (j1 - j0) + 7) / 8);
    });
  });
  hx[19] = pack8h(pend[0], pend[1]);
  xs = so1 + (unsigned)rb[1];
#pragma unroll
  for (int e = 0; e < NH; ++e) rqv.h[e] = hx[20 - NH + e];
  asm volatile("" : "+v"(rqv.h[NH - 1]));
  setup();
  wait_vm<NM == 5 ? NS - st_first(NIT - 1) : 10 - NM + NS - st_first(PG)>();
  {
    asm volatile("" : "+v"(ov));
    const unsigned ox = __builtin_amdgcn_readfirstlane(ov[0]), ow = __builtin_amdgcn_readfirstlane(ov[1]);
    if (__builtin_expect((ox | ow) && !(cx.id & 512u), 0))
      restart((cx.id & 255u) | 768u | (bid & 7u) << 10 | min(max(ex + 7, ox) - 7, 220u) << 16 | min(max(ew + 9, ow) - 9, 220u) << 24);
    if (NM == 6 && (cx.id & 255u) == 0 && wv == 0)
      asm volatile("global_store_dwordx4 %0, %1, %2 sc1\n\ts_nop 1" :: "v"(lane * 16), "v"(u32x4{0u, 0u, 0u, 0u}), "s"(rec) : "memory");
  }
  if (NM == 5) {
    asm volatile("" : "+v"(pollv));
    if (!__all(pollv != 0u)) spin_flags(ws + WS_S + FLG + cx.fvo);
  }
#pragma unroll
  for (int k = 0; k < NQW; ++k) rqv.raw[k] = rq[k];
}

#define LOOP0 \
"s_mov_b32 s91, 0x40004\n" \
"s_mov_b32 s92, 0xfff8fff8\n" \
"ds_read_b64_tr_b16 v[120:121], v241 offset:0\n" \
"ds_read_b64_tr_b16 v[122:123], v241 offset:8192\n" \
"ds_read_b128 v[144:147], v240 offset:0\n" \
"ds_read_b64_tr_b16 v[124:125], v242 offset:0\n" \
"ds_read_b64_tr_b16 v[126:127], v242 offset:8192\n" \
"ds_read_b64_tr_b16 v[128:129], v241 offset:512\n" \
"ds_read_b64_tr_b16 v[130:131], v241 offset:8704\n" \
"ds_read_b64_tr_b16 v[132:133], v242 offset:512\n" \
"ds_read_b64_tr_b16 v[134:135], v242 offset:8704\n" \
"ds_read_b128 v[164:167], v240 offset:512\n" \
"ds_read_b128 v[168:171], v240 offset:2560\n" \
"ds_read_b128 v[148:151], v240 offset:2048\n" \
"ds_read_b128 v[172:175], v240 offset:4608\n" \
"ds_read_b128 v[152:155], v240 offset:4096\n" \
"ds_read_b128 v[176:179], v240 offset:6656\n" \
"ds_read_b128 v[156:159], v240 offset:6144\n" \
"ds_read_b128 v[180:183], v240 offset:8704\n" \
"ds_read_b128 v[160:163], v240 offset:8192\n" \
"s_waitcnt lgkmcnt(15)\n" \
"v_mfma_f32_16x16x32_f16 v[0:3], v[120:123], v[144:147], 0\n" \
"s_waitcnt lgkmcnt(12)\n" \
"v_pk_add_f16 v136, v120, v128\n" \
"v_pk_add_f16 v137, v121, v129\n" \
"s_add_i32 m0, s44, 0x8000\n" \
"s_mov_b32 s80, 0x800000\n" \
"ds_write_b64 v246, v[96:97] offset:0\n" \
"v_mfma_f32_16x16x32_f16 v[12:15], v[124:127], v[144:147], 0\n" \
"s_waitcnt lgkmcnt(9)\n" \
"v_pk_add_f16 v144, v144, v164\n" \
"v_pk_add_f16 v138, v122, v130\n" \
"buffer_load_dwordx4 v232, s[36:39], s80 offen sc1 lds\n" \
"ds_write_b64 v246, v[98:99] offset:4096\n" \
"s_waitcnt lgkmcnt(8)\n" \
"v_mfma_f32_16x16x32_f16 v[36:39], v[124:127], v[148:151], 0\n" \
"v_pk_add_f16 v145, v145, v165\n" \
"v_pk_add_f16 v146, v146, v166\n" \
"v_pk_add_f16 v147, v147, v167\n" \
"ds_write_b64 v246, v[100:101] offset:8192\n" \
"ds_write_b64 v246, v[102:103] offset:12288\n" \
"v_mfma_f32_16x16x32_f16 v[24:27], v[120:123], v[148:151], 0\n" \
"v_pk_add_f16 v148, v148, v168\n" \
"v_pk_add_f16 v139, v123, v131\n" \
"buffer_load_dwordx4 v232, s[36:39], s80 offen offset:1024 sc1 lds\n" \
"ds_write_b64 v246, v[104:105] offset:16384\n" \
"s_waitcnt lgkmcnt(5)\n" \
"v_mfma_f32_16x16x32_f16 v[48:51], v[120:123], v[152:155], 0\n" \
"v_pk_add_f16 v149, v149, v169\n" \
"v_pk_add_f16 v140, v124, v132\n" \
"buffer_store_dwordx4 v[72:75], v235, s[36:39], s93 offen sc1\n" \
"ds_write_b64 v246, v[106:107] offset:20480\n" \
"v_mfma_f32_16x16x32_f16 v[60:63], v[124:127], v[152:155], 0\n" \
"v_pk_add_f16 v150, v150, v170\n" \
"v_pk_add_f16 v151, v151, v171\n" \
"buffer_load_dwordx4 v232, s[36:39], s80 offen offset:2048 sc1 lds\n" \
"ds_write_b64 v246, v[108:109] offset:24576\n" \
"ds_write_b64 v246, v[110:111] offset:28672\n" \
"v_mfma_f32_16x16x32_f16 v[84:87], v[124:127], v[156:159], 0\n" \
"v_pk_add_f16 v152, v152, v172\n" \
"v_pk_add_f16 v141, v125, v133\n" \
"ds_write_b64 v246, v[112:113] offset:32768\n" \
"ds_write_b64 v246, v[114:115] offset:36864\n" \
"v_mfma_f32_16x16x32_f16 v[72:75], v[120:123], v[156:159], 0\n" \
"v_pk_add_f16 v153, v153, v173\n" \
"v_pk_add_f16 v142, v126, v134\n" \
"buffer_load_dwordx4 v232, s[36:39], s80 offen offset:3072 sc1 lds\n" \
"ds_write_b64 v246, v[116:117] offset:40960\n" \
"v_mfma_f32_16x16x32_f16 v[96:99], v[120:123], v[160:163], 0\n" \
"v_pk_add_f16 v154, v154, v174\n" \
"v_pk_add_f16 v155, v155, v175\n" \
"v_mfma_f32_16x16x32_f16 v[108:111], v[124:127], v[160:163], 0\n" \
"v_pk_add_f16 v156, v156, v176\n" \
"v_pk_add_f16 v143, v127, v135\n" \
"s_waitcnt vmcnt(8)\n" \
"s_barrier\n" \
"v_mfma_f32_16x16x32_f16 v[4:7], v[128:131], v[164:167], 0\n" \
"v_pk_add_f16 v157, v157, v177\n" \
"ds_read_b64_tr_b16 v[120:121], v241 offset:16384\n" \
"v_mfma_f32_16x16x32_f16 v[16:19], v[132:135], v[164:167], 0\n" \
"v_pk_add_f16 v158, v158, v178\n" \
"ds_read_b128 v[184:187], v240 offset:20992\n" \
"v_mfma_f32_16x16x32_f16 v[40:43], v[132:135], v[168:171], 0\n" \
"v_pk_add_f16 v159, v159, v179\n" \
"ds_read_b128 v[164:167], v240 offset:20480\n" \
"buffer_store_dwordx4 v[196:199], v235, s[36:39], s60 offen sc1\n" \
"v_mfma_f32_16x16x32_f16 v[28:31], v[128:131], v[168:171], 0\n" \
"v_pk_add_f16 v160, v160, v180\n" \
"ds_read_b64_tr_b16 v[122:123], v241 offset:24576\n" \
"s_add_i32 m0, s44, 0x0\n" \
"s_mov_b32 s80, 0xc00000\n" \
"v_mfma_f32_16x16x32_f16 v[52:55], v[128:131], v[172:175], 0\n" \
"v_pk_add_f16 v161, v161, v181\n" \
"ds_read_b128 v[168:171], v240 offset:22528\n" \
"buffer_load_dwordx4 v232, s[36:39], s80 offen sc1 lds\n" \
"v_mfma_f32_16x16x32_f16 v[64:67], v[132:135], v[172:175], 0\n" \
"v_pk_add_f16 v162, v162, v182\n" \
"ds_read_b64_tr_b16 v[124:125], v242 offset:16384\n" \
"v_mfma_f32_16x16x32_f16 v[88:91], v[132:135], v[176:179], 0\n" \
"v_pk_add_f16 v163, v163, v183\n" \
"ds_read_b128 v[172:175], v240 offset:24576\n" \
"v_mfma_f32_16x16x32_f16 v[76:79], v[128:131], v[176:179], 0\n" \
"ds_read_b64_tr_b16 v[126:127], v242 offset:24576\n" \
"buffer_load_dwordx4 v232, s[36:39], s80 offen offset:1024 sc1 lds\n" \
"v_mfma_f32_16x16x32_f16 v[100:103], v[128:131], v[180:183], 0\n" \
"ds_read_b128 v[176:179], v240 offset:26624\n" \
"buffer_store_dwordx4 v[200:203], v235, s[36:39], s61 offen sc1\n" \
"v_mfma_f32_16x16x32_f16 v[112:115], v[132:135], v[180:183], 0\n" \
"ds_read_b64_tr_b16 v[128:129], v241 offset:16896\n" \
"v_mfma_f32_16x16x32_f16 v[8:11], v[136:139], v[144:147], 0\n" \
"ds_read_b128 v[180:183], v240 offset:28672\n" \
"buffer_load_dwordx4 v232, s[36:39], s80 offen offset:2048 sc1 lds\n" \
"v_mfma_f32_16x16x32_f16 v[20:23], v[140:143], v[144:147], 0\n" \
"ds_read_b128 v[144:147], v240 offset:23040\n" \
"v_mfma_f32_16x16x32_f16 v[44:47], v[140:143], v[148:151], 0\n" \
"ds_read_b64_tr_b16 v[130:131], v241 offset:25088\n" \
"ds_read_b64_tr_b16 v[132:133], v242 offset:16896\n" \
"v_mfma_f32_16x16x32_f16 v[32:35], v[136:139], v[148:151], 0\n" \
"ds_read_b128 v[148:151], v240 offset:25088\n" \
"buffer_load_dwordx4 v232, s[36:39], s80 offen offset:3072 sc1 lds\n" \
"v_mfma_f32_16x16x32_f16 v[56:59], v[136:139], v[152:155], 0\n" \
"ds_read_b64_tr_b16 v[134:135], v242 offset:25088\n" \
"v_mfma_f32_16x16x32_f16 v[68:71], v[140:143], v[152:155], 0\n" \
"ds_read_b128 v[152:155], v240 offset:27136\n" \
"v_mfma_f32_16x16x32_f16 v[92:95], v[140:143], v[156:159], 0\n" \
"v_mfma_f32_16x16x32_f16 v[80:83], v[136:139], v[156:159], 0\n" \
"ds_read_b128 v[156:159], v240 offset:29184\n" \
"v_mfma_f32_16x16x32_f16 v[104:107], v[136:139], v[160:163], 0\n" \
"v_mfma_f32_16x16x32_f16 v[116:119], v[140:143], v[160:163], 0\n" \
"s_waitcnt vmcnt(6) lgkmcnt(0)\n" \
"s_barrier\n" \
"v_mfma_f32_16x16x32_f16 v[0:3], v[120:123], v[164:167], v[0:3]\n" \
"v_pk_add_f16 v136, v120, v128\n" \
"v_pk_add_f16 v137, v121, v129\n" \
"ds_read_b64_tr_b16 v[188:189], v236 offset:0\n" \
"v_mfma_f32_16x16x32_f16 v[12:15], v[124:127], v[164:167], v[12:15]\n" \
"v_pk_add_f16 v164, v164, v184\n" \
"v_pk_add_f16 v138, v122, v130\n" \
"ds_read_b64_tr_b16 v[192:193], v237 offset:0\n" \
"v_mfma_f32_16x16x32_f16 v[36:39], v[124:127], v[168:171], v[36:39]\n" \
"v_pk_add_f16 v165, v165, v185\n" \
"v_pk_add_f16 v166, v166, v186\n" \
"v_pk_add_f16 v167, v167, v187\n" \
"v_mfma_f32_16x16x32_f16 v[24:27], v[120:123], v[168:171], v[24:27]\n" \
"v_pk_add_f16 v168, v168, v144\n" \
"v_pk_add_f16 v139, v123, v131\n" \
"s_add_i32 m0, s44, 0x4000\n" \
"s_mov_b32 s80, 0x1000000\n" \
"v_mfma_f32_16x16x32_f16 v[48:51], v[120:123], v[172:175], v[48:51]\n" \
"v_pk_add_f16 v169, v169, v145\n" \
"v_pk_add_f16 v140, v124, v132\n" \
"buffer_load_dwordx4 v232, s[36:39], s80 offen sc1 lds\n" \
"v_mfma_f32_16x16x32_f16 v[60:63], v[124:127], v[172:175], v[60:63]\n" \
"v_pk_add_f16 v170, v170, v146\n" \
"v_pk_add_f16 v171, v171, v147\n" \
"s_waitcnt lgkmcnt(1)\n" \
"v_mfma_f32_16x16x16_bf16 v[188:191], v[188:189], v[254:255], 0\n" \
"v_mfma_f32_16x16x32_f16 v[84:87], v[124:127], v[176:179], v[84:87]\n" \
"v_pk_add_f16 v172, v172, v148\n" \
"v_pk_add_f16 v141, v125, v133\n" \
"s_add_i32 s84, s52, 0x8004000\n" \
"s_waitcnt lgkmcnt(0)\n" \
"v_mfma_f32_16x16x16_bf16 v[192:195], v[192:193], v[254:255], 0\n" \
"v_mfma_f32_16x16x32_f16 v[72:75], v[120:123], v[176:179], v[72:75]\n" \
"v_pk_add_f16 v173, v173, v149\n" \
"v_pk_add_f16 v142, v126, v134\n" \
"buffer_store_dword v240, v243, s[36:39], s84 offen sc1\n" \
"v_mfma_f32_16x16x32_f16 v[96:99], v[120:123], v[180:183], v[96:99]\n" \
"v_pk_add_f16 v174, v174, v150\n" \
"v_pk_add_f16 v175, v175, v151\n" \
"v_mfma_f32_16x16x32_f16 v[108:111], v[124:127], v[180:183], v[108:111]\n" \
"v_pk_add_f16 v176, v176, v152\n" \
"v_pk_add_f16 v143, v127, v135\n" \
"v_cvt_pk_f16_f32 v188, v188, v189\n" \
"v_cvt_pk_f16_f32 v189, v190, v191\n" \
"v_mfma_f32_16x16x32_f16 v[4:7], v[128:131], v[184:187], v[4:7]\n" \
"v_pk_add_f16 v177, v177, v153\n" \
"v_cvt_pk_f16_f32 v190, v192, v193\n" \
"ds_read_b64_tr_b16 v[120:121], v241 offset:32768\n" \
"buffer_load_dwordx4 v232, s[36:39], s80 offen offset:1024 sc1 lds\n" \
"v_mfma_f32_16x16x32_f16 v[16:19], v[132:135], v[184:187], v[16:19]\n" \
"v_pk_add_f16 v178, v178, v154\n" \
"v_cvt_pk_f16_f32 v191, v194, v195\n" \
"v_add_u32 v188, s91, v188\n" \
"ds_read_b128 v[160:163], v240 offset:41472\n" \
"v_mfma_f32_16x16x32_f16 v[40:43], v[132:135], v[144:147], v[40:43]\n" \
"v_pk_add_f16 v179, v179, v155\n" \
"v_add_u32 v189, s91, v189\n" \
"v_add_u32 v190, s91, v190\n" \
"ds_read_b128 v[184:187], v240 offset:40960\n" \
"v_mfma_f32_16x16x32_f16 v[28:31], v[128:131], v[144:147], v[28:31]\n" \
"v_pk_add_f16 v180, v180, v156\n" \
"v_add_u32 v191, s91, v191\n" \
"v_and_b32 v188, s92, v188\n" \
"ds_read_b64_tr_b16 v[122:123], v241 offset:40960\n" \
"s_add_i32 s82, s49, 0x2800000\n" \
"v_mfma_f32_16x16x32_f16 v[52:55], v[128:131], v[148:151], v[52:55]\n" \
"v_pk_add_f16 v181, v181, v157\n" \
"v_and_b32 v189, s92, v189\n" \
"v_and_b32 v190, s92, v190\n" \
"v_and_b32 v191, s92, v191\n" \
"ds_read_b128 v[144:147], v240 offset:43008\n" \
"v_mfma_f32_16x16x32_f16 v[64:67], v[132:135], v[148:151], v[64:67]\n" \
"v_pk_add_f16 v182, v182, v158\n" \
"ds_read_b64_tr_b16 v[124:125], v242 offset:32768\n" \
"buffer_store_dwordx4 v[188:191], v234, s[36:39], s82 offen sc1\n" \
"v_mfma_f32_16x16x32_f16 v[88:91], v[132:135], v[152:155], v[88:91]\n" \
"v_pk_add_f16 v183, v183, v159\n" \
"ds_read_b128 v[148:151], v240 offset:45056\n" \
"buffer_load_dwordx4 v232, s[36:39], s80 offen offset:2048 sc1 lds\n" \
"v_mfma_f32_16x16x32_f16 v[76:79], v[128:131], v[152:155], v[76:79]\n" \
"ds_read_b64_tr_b16 v[126:127], v242 offset:40960\n" \
"ds_read_b64_tr_b16 v[188:189], v238 offset:0\n" \
"v_mfma_f32_16x16x32_f16 v[100:103], v[128:131], v[156:159], v[100:103]\n" \
"ds_read_b128 v[152:155], v240 offset:47104\n" \
"ds_read_b64_tr_b16 v[192:193], v239 offset:0\n" \
"v_mfma_f32_16x16x32_f16 v[112:115], v[132:135], v[156:159], v[112:115]\n" \
"ds_read_b64_tr_b16 v[128:129], v241 offset:33280\n" \
"v_mfma_f32_16x16x32_f16 v[8:11], v[136:139], v[164:167], v[8:11]\n" \
"ds_read_b128 v[156:159], v240 offset:49152\n" \
"s_waitcnt lgkmcnt(4)\n" \
"v_mfma_f32_16x16x16_bf16 v[188:191], v[188:189], v[254:255], 0\n" \
"v_mfma_f32_16x16x32_f16 v[20:23], v[140:143], v[164:167], v[20:23]\n" \
"ds_read_b128 v[164:167], v240 offset:43520\n" \
"s_waitcnt lgkmcnt(3)\n" \
"v_mfma_f32_16x16x16_bf16 v[192:195], v[192:193], v[254:255], 0\n" \
"v_mfma_f32_16x16x32_f16 v[44:47], v[140:143], v[168:171], v[44:47]\n" \
"ds_read_b64_tr_b16 v[130:131], v241 offset:41472\n" \
"ds_read_b64_tr_b16 v[132:133], v242 offset:33280\n" \
"buffer_load_dwordx4 v232, s[36:39], s80 offen offset:3072 sc1 lds\n" \
"v_mfma_f32_16x16x32_f16 v[32:35], v[136:139], v[168:171], v[32:35]\n" \
"ds_read_b128 v[168:171], v240 offset:45568\n" \
"v_mfma_f32_16x16x32_f16 v[56:59], v[136:139], v[172:175], v[56:59]\n" \
"v_cvt_pk_f16_f32 v188, v188, v189\n" \
"v_cvt_pk_f16_f32 v189, v190, v191\n" \
"ds_read_b64_tr_b16 v[134:135], v242 offset:41472\n" \
"v_mfma_f32_16x16x32_f16 v[68:71], v[140:143], v[172:175], v[68:71]\n" \
"v_cvt_pk_f16_f32 v190, v192, v193\n" \
"v_cvt_pk_f16_f32 v191, v194, v195\n" \
"ds_read_b128 v[172:175], v240 offset:47616\n" \
"v_mfma_f32_16x16x32_f16 v[92:95], v[140:143], v[176:179], v[92:95]\n" \
"v_add_u32 v188, s91, v188\n" \
"v_add_u32 v189, s91, v189\n" \
"v_add_u32 v190, s91, v190\n" \
"v_mfma_f32_16x16x32_f16 v[80:83], v[136:139], v[176:179], v[80:83]\n" \
"v_add_u32 v191, s91, v191\n" \
"v_and_b32 v188, s92, v188\n" \
"ds_read_b128 v[176:179], v240 offset:49664\n" \
"v_mfma_f32_16x16x32_f16 v[104:107], v[136:139], v[180:183], v[104:107]\n" \
"v_and_b32 v189, s92, v189\n" \
"v_and_b32 v190, s92, v190\n" \
"v_and_b32 v191, s92, v191\n" \
"v_mfma_f32_16x16x32_f16 v[116:119], v[140:143], v[180:183], v[116:119]\n" \
"buffer_store_dwordx4 v[188:191], v234, s[36:39], s82 offen offset:1024 sc1\n" \
"s_waitcnt vmcnt(7) lgkmcnt(0)\n" \
"s_barrier\n" \
"v_mfma_f32_16x16x32_f16 v[0:3], v[120:123], v[184:187], v[0:3]\n" \
"v_pk_add_f16 v136, v120, v128\n" \
"v_pk_add_f16 v137, v121, v129\n" \
"v_mfma_f32_16x16x32_f16 v[12:15], v[124:127], v[184:187], v[12:15]\n" \
"v_pk_add_f16 v184, v184, v160\n" \
"v_pk_add_f16 v138, v122, v130\n" \
"v_mfma_f32_16x16x32_f16 v[36:39], v[124:127], v[144:147], v[36:39]\n" \
"v_pk_add_f16 v185, v185, v161\n" \
"v_pk_add_f16 v186, v186, v162\n" \
"v_pk_add_f16 v187, v187, v163\n" \
"buffer_store_dwordx4 v[204:207], v235, s[36:39], s62 offen sc1\n" \
"v_mfma_f32_16x16x32_f16 v[24:27], v[120:123], v[144:147], v[24:27]\n" \
"v_pk_add_f16 v144, v144, v164\n" \
"v_pk_add_f16 v139, v123, v131\n" \
"s_add_i32 m0, s44, 0x8000\n" \
"s_mov_b32 s80, 0x1400000\n" \
"v_mfma_f32_16x16x32_f16 v[48:51], v[120:123], v[148:151], v[48:51]\n" \
"v_pk_add_f16 v145, v145, v165\n" \
"v_pk_add_f16 v140, v124, v132\n" \
"buffer_load_dwordx4 v232, s[36:39], s80 offen sc1 lds\n" \
"v_mfma_f32_16x16x32_f16 v[60:63], v[124:127], v[148:151], v[60:63]\n" \
"v_pk_add_f16 v146, v146, v166\n" \
"v_pk_add_f16 v147, v147, v167\n" \
"v_mfma_f32_16x16x32_f16 v[84:87], v[124:127], v[152:155], v[84:87]\n" \
"v_pk_add_f16 v148, v148, v168\n" \
"v_pk_add_f16 v141, v125, v133\n" \
"v_mfma_f32_16x16x32_f16 v[72:75], v[120:123], v[152:155], v[72:75]\n" \
"v_pk_add_f16 v149, v149, v169\n" \
"v_pk_add_f16 v142, v126, v134\n" \
"v_mfma_f32_16x16x32_f16 v[96:99], v[120:123], v[156:159], v[96:99]\n" \
"v_pk_add_f16 v150, v150, v170\n" \
"v_pk_add_f16 v151, v151, v171\n" \
"ds_read_b64_tr_b16 v[188:189], v236 offset:4096\n" \
"v_mfma_f32_16x16x32_f16 v[108:111], v[124:127], v[156:159], v[108:111]\n" \
"v_pk_add_f16 v152, v152, v172\n" \
"v_pk_add_f16 v143, v127, v135\n" \
"ds_read_b64_tr_b16 v[192:193], v237 offset:4096\n" \
"v_mfma_f32_16x16x32_f16 v[4:7], v[128:131], v[160:163], v[4:7]\n" \
"v_pk_add_f16 v153, v153, v173\n" \
"ds_read_b64_tr_b16 v[120:121], v241 offset:0\n" \
"buffer_load_dwordx4 v232, s[36:39], s80 offen offset:1024 sc1 lds\n" \
"v_mfma_f32_16x16x32_f16 v[16:19], v[132:135], v[160:163], v[16:19]\n" \
"v_pk_add_f16 v154, v154, v174\n" \
"ds_read_b128 v[180:183], v240 offset:512\n" \
"v_mfma_f32_16x16x32_f16 v[40:43], v[132:135], v[164:167], v[40:43]\n" \
"v_pk_add_f16 v155, v155, v175\n" \
"ds_read_b128 v[160:163], v240 offset:0\n" \
"v_mfma_f32_16x16x32_f16 v[28:31], v[128:131], v[164:167], v[28:31]\n" \
"v_pk_add_f16 v156, v156, v176\n" \
"ds_read_b64_tr_b16 v[122:123], v241 offset:8192\n" \
"v_mfma_f32_16x16x32_f16 v[52:55], v[128:131], v[168:171], v[52:55]\n" \
"v_pk_add_f16 v157, v157, v177\n" \
"ds_read_b128 v[164:167], v240 offset:2048\n" \
"s_waitcnt lgkmcnt(6)\n" \
"v_mfma_f32_16x16x16_bf16 v[188:191], v[188:189], v[254:255], 0\n" \
"v_mfma_f32_16x16x32_f16 v[64:67], v[132:135], v[168:171], v[64:67]\n" \
"v_pk_add_f16 v158, v158, v178\n" \
"ds_read_b64_tr_b16 v[124:125], v242 offset:0\n" \
"s_waitcnt lgkmcnt(6)\n" \
"v_mfma_f32_16x16x16_bf16 v[192:195], v[192:193], v[254:255], 0\n" \
"v_mfma_f32_16x16x32_f16 v[88:91], v[132:135], v[172:175], v[88:91]\n" \
"v_pk_add_f16 v159, v159, v179\n" \
"ds_read_b128 v[168:171], v240 offset:4096\n" \
"buffer_load_dwordx4 v232, s[36:39], s80 offen offset:2048 sc1 lds\n" \
"v_mfma_f32_16x16x32_f16 v[76:79], v[128:131], v[172:175], v[76:79]\n" \
"ds_read_b64_tr_b16 v[126:127], v242 offset:8192\n" \
"v_mfma_f32_16x16x32_f16 v[100:103], v[128:131], v[176:179], v[100:103]\n" \
"v_cvt_pk_f16_f32 v188, v188, v189\n" \
"ds_read_b128 v[172:175], v240 offset:6144\n" \
"v_mfma_f32_16x16x32_f16 v[112:115], v[132:135], v[176:179], v[112:115]\n" \
"v_cvt_pk_f16_f32 v189, v190, v191\n" \
"ds_read_b64_tr_b16 v[128:129], v241 offset:512\n" \
"v_mfma_f32_16x16x32_f16 v[8:11], v[136:139], v[184:187], v[8:11]\n" \
"v_cvt_pk_f16_f32 v190, v192, v193\n" \
"ds_read_b128 v[176:179], v240 offset:8192\n" \
"v_mfma_f32_16x16x32_f16 v[20:23], v[140:143], v[184:187], v[20:23]\n" \
"v_cvt_pk_f16_f32 v191, v194, v195\n" \
"v_add_u32 v188, s91, v188\n" \
"ds_read_b128 v[184:187], v240 offset:2560\n" \
"v_mfma_f32_16x16x32_f16 v[44:47], v[140:143], v[144:147], v[44:47]\n" \
"ds_read_b64_tr_b16 v[130:131], v241 offset:8704\n" \
"ds_read_b64_tr_b16 v[132:133], v242 offset:512\n" \
"buffer_load_dwordx4 v232, s[36:39], s80 offen offset:3072 sc1 lds\n" \
"v_mfma_f32_16x16x32_f16 v[32:35], v[136:139], v[144:147], v[32:35]\n" \
"v_add_u32 v189, s91, v189\n" \
"v_add_u32 v190, s91, v190\n" \
"ds_read_b128 v[144:147], v240 offset:4608\n" \
"v_mfma_f32_16x16x32_f16 v[56:59], v[136:139], v[148:151], v[56:59]\n" \
"v_add_u32 v191, s91, v191\n" \
"ds_read_b64_tr_b16 v[134:135], v242 offset:8704\n" \
"v_mfma_f32_16x16x32_f16 v[68:71], v[140:143], v[148:151], v[68:71]\n" \
"v_and_b32 v188, s92, v188\n" \
"ds_read_b128 v[148:151], v240 offset:6656\n" \
"v_mfma_f32_16x16x32_f16 v[92:95], v[140:143], v[152:155], v[92:95]\n" \
"v_and_b32 v189, s92, v189\n" \
"v_and_b32 v190, s92, v190\n" \
"s_add_i32 s82, s50, 0x3000000\n" \
"v_mfma_f32_16x16x32_f16 v[80:83], v[136:139], v[152:155], v[80:83]\n" \
"v_and_b32 v191, s92, v191\n" \
"ds_read_b128 v[152:155], v240 offset:8704\n" \
"v_mfma_f32_16x16x32_f16 v[104:107], v[136:139], v[156:159], v[104:107]\n" \
"buffer_store_dwordx4 v[188:191], v234, s[36:39], s82 offen sc1\n" \
"v_mfma_f32_16x16x32_f16 v[116:119], v[140:143], v[156:159], v[116:119]\n" \
"s_waitcnt vmcnt(6) lgkmcnt(0)\n" \
"s_barrier\n" \
"v_mfma_f32_16x16x32_f16 v[0:3], v[120:123], v[160:163], v[0:3]\n" \
"v_pk_add_f16 v136, v120, v128\n" \
"v_pk_add_f16 v137, v121, v129\n" \
"v_mfma_f32_16x16x32_f16 v[12:15], v[124:127], v[160:163], v[12:15]\n" \
"v_pk_add_f16 v160, v160, v180\n" \
"v_pk_add_f16 v138, v122, v130\n" \
"v_mfma_f32_16x16x32_f16 v[36:39], v[124:127], v[164:167], v[36:39]\n" \
"v_pk_add_f16 v161, v161, v181\n" \
"v_pk_add_f16 v162, v162, v182\n" \
"v_pk_add_f16 v163, v163, v183\n" \
"v_mfma_f32_16x16x32_f16 v[24:27], v[120:123], v[164:167], v[24:27]\n" \
"v_pk_add_f16 v164, v164, v184\n" \
"v_pk_add_f16 v139, v123, v131\n" \
"s_add_i32 m0, s44, 0x0\n" \
"s_mov_b32 s80, 0x1800000\n" \
"v_mfma_f32_16x16x32_f16 v[48:51], v[120:123], v[168:171], v[48:51]\n" \
"v_pk_add_f16 v165, v165, v185\n" \
"v_pk_add_f16 v140, v124, v132\n" \
"buffer_load_dwordx4 v232, s[36:39], s80 offen sc1 lds\n" \
"v_mfma_f32_16x16x32_f16 v[60:63], v[124:127], v[168:171], v[60:63]\n" \
"v_pk_add_f16 v166, v166, v186\n" \
"v_pk_add_f16 v167, v167, v187\n" \
"v_mfma_f32_16x16x32_f16 v[84:87], v[124:127], v[172:175], v[84:87]\n" \
"v_pk_add_f16 v168, v168, v144\n" \
"v_pk_add_f16 v141, v125, v133\n" \
"s_add_i32 s84, s53, 0x8006000\n" \
"v_mfma_f32_16x16x32_f16 v[72:75], v[120:123], v[172:175], v[72:75]\n" \
"v_pk_add_f16 v169, v169, v145\n" \
"v_pk_add_f16 v142, v126, v134\n" \
"buffer_store_dword v240, v243, s[36:39], s84 offen sc1\n" \
"v_mfma_f32_16x16x32_f16 v[96:99], v[120:123], v[176:179], v[96:99]\n" \
"v_pk_add_f16 v170, v170, v146\n" \
"v_pk_add_f16 v171, v171, v147\n" \
"ds_read_b64_tr_b16 v[188:189], v238 offset:4096\n" \
"v_mfma_f32_16x16x32_f16 v[108:111], v[124:127], v[176:179], v[108:111]\n" \
"v_pk_add_f16 v172, v172, v148\n" \
"v_pk_add_f16 v143, v127, v135\n" \
"ds_read_b64_tr_b16 v[192:193], v239 offset:4096\n" \
"v_mfma_f32_16x16x32_f16 v[4:7], v[128:131], v[180:183], v[4:7]\n" \
"v_pk_add_f16 v173, v173, v149\n" \
"ds_read_b64_tr_b16 v[120:121], v241 offset:16384\n" \
"buffer_load_dwordx4 v232, s[36:39], s80 offen offset:1024 sc1 lds\n" \
"v_mfma_f32_16x16x32_f16 v[16:19], v[132:135], v[180:183], v[16:19]\n" \
"v_pk_add_f16 v174, v174, v150\n" \
"ds_read_b128 v[156:159], v240 offset:20992\n" \
"v_mfma_f32_16x16x32_f16 v[40:43], v[132:135], v[184:187], v[40:43]\n" \
"v_pk_add_f16 v175, v175, v151\n" \
"ds_read_b128 v[180:183], v240 offset:20480\n" \
"v_mfma_f32_16x16x32_f16 v[28:31], v[128:131], v[184:187], v[28:31]\n" \
"v_pk_add_f16 v176, v176, v152\n" \
"ds_read_b64_tr_b16 v[122:123], v241 offset:24576\n" \
"v_mfma_f32_16x16x32_f16 v[52:55], v[128:131], v[144:147], v[52:55]\n" \
"v_pk_add_f16 v177, v177, v153\n" \
"ds_read_b128 v[184:187], v240 offset:22528\n" \
"s_waitcnt lgkmcnt(6)\n" \
"v_mfma_f32_16x16x16_bf16 v[188:191], v[188:189], v[254:255], 0\n" \
"v_mfma_f32_16x16x32_f16 v[64:67], v[132:135], v[144:147], v[64:67]\n" \
"v_pk_add_f16 v178, v178, v154\n" \
"ds_read_b64_tr_b16 v[124:125], v242 offset:16384\n" \
"s_waitcnt lgkmcnt(6)\n" \
"v_mfma_f32_16x16x16_bf16 v[192:195], v[192:193], v[254:255], 0\n" \
"v_mfma_f32_16x16x32_f16 v[88:91], v[132:135], v[148:151], v[88:91]\n" \
"v_pk_add_f16 v179, v179, v155\n" \
"ds_read_b128 v[144:147], v240 offset:24576\n" \
"buffer_load_dwordx4 v232, s[36:39], s80 offen offset:2048 sc1 lds\n" \
"v_mfma_f32_16x16x32_f16 v[76:79], v[128:131], v[148:151], v[76:79]\n" \
"ds_read_b64_tr_b16 v[126:127], v242 offset:24576\n" \
"v_mfma_f32_16x16x32_f16 v[100:103], v[128:131], v[152:155], v[100:103]\n" \
"v_cvt_pk_f16_f32 v188, v188, v189\n" \
"ds_read_b128 v[148:151], v240 offset:26624\n" \
"v_mfma_f32_16x16x32_f16 v[112:115], v[132:135], v[152:155], v[112:115]\n" \
"v_cvt_pk_f16_f32 v189, v190, v191\n" \
"ds_read_b64_tr_b16 v[128:129], v241 offset:16896\n" \
"v_mfma_f32_16x16x32_f16 v[8:11], v[136:139], v[160:163], v[8:11]\n" \
"v_cvt_pk_f16_f32 v190, v192, v193\n" \
"ds_read_b128 v[152:155], v240 offset:28672\n" \
"v_mfma_f32_16x16x32_f16 v[20:23], v[140:143], v[160:163], v[20:23]\n" \
"v_cvt_pk_f16_f32 v191, v194, v195\n" \
"v_add_u32 v188, s91, v188\n" \
"ds_read_b128 v[160:163], v240 offset:23040\n" \
"v_mfma_f32_16x16x32_f16 v[44:47], v[140:143], v[164:167], v[44:47]\n" \
"ds_read_b64_tr_b16 v[130:131], v241 offset:25088\n" \
"ds_read_b64_tr_b16 v[132:133], v242 offset:16896\n" \
"buffer_load_dwordx4 v232, s[36:39], s80 offen offset:3072 sc1 lds\n" \
"v_mfma_f32_16x16x32_f16 v[32:35], v[136:139], v[164:167], v[32:35]\n" \
"v_add_u32 v189, s91, v189\n" \
"v_add_u32 v190, s91, v190\n" \
"ds_read_b128 v[164:167], v240 offset:25088\n" \
"v_mfma_f32_16x16x32_f16 v[56:59], v[136:139], v[168:171], v[56:59]\n" \
"v_add_u32 v191, s91, v191\n" \
"ds_read_b64_tr_b16 v[134:135], v242 offset:25088\n" \
"v_mfma_f32_16x16x32_f16 v[68:71], v[140:143], v[168:171], v[68:71]\n" \
"v_and_b32 v188, s92, v188\n" \
"ds_read_b128 v[168:171], v240 offset:27136\n" \
"v_mfma_f32_16x16x32_f16 v[92:95], v[140:143], v[172:175], v[92:95]\n" \
"v_and_b32 v189, s92, v189\n" \
"v_and_b32 v190, s92, v190\n" \
"v_mfma_f32_16x16x32_f16 v[80:83], v[136:139], v[172:175], v[80:83]\n" \
"v_and_b32 v191, s92, v191\n" \
"ds_read_b128 v[172:175], v240 offset:29184\n" \
"v_mfma_f32_16x16x32_f16 v[104:107], v[136:139], v[176:179], v[104:107]\n" \
"buffer_store_dwordx4 v[188:191], v234, s[36:39], s82 offen offset:1024 sc1\n" \
"v_mfma_f32_16x16x32_f16 v[116:119], v[140:143], v[176:179], v[116:119]\n" \
"s_waitcnt vmcnt(7) lgkmcnt(0)\n" \
"s_barrier\n" \
"v_mfma_f32_16x16x32_f16 v[0:3], v[120:123], v[180:183], v[0:3]\n" \
"v_pk_add_f16 v136, v120, v128\n" \
"v_pk_add_f16 v137, v121, v129\n" \
"v_mfma_f32_16x16x32_f16 v[12:15], v[124:127], v[180:183], v[12:15]\n" \
"v_pk_add_f16 v180, v180, v156\n" \
"v_pk_add_f16 v138, v122, v130\n" \
"v_mfma_f32_16x16x32_f16 v[36:39], v[124:127], v[184:187], v[36:39]\n" \
"v_pk_add_f16 v181, v181, v157\n" \
"v_pk_add_f16 v182, v182, v158\n" \
"v_pk_add_f16 v183, v183, v159\n" \
"buffer_store_dwordx4 v[208:211], v235, s[36:39], s63 offen sc1\n" \
"v_mfma_f32_16x16x32_f16 v[24:27], v[120:123], v[184:187], v[24:27]\n" \
"v_pk_add_f16 v184, v184, v160\n" \
"v_pk_add_f16 v139, v123, v131\n" \
"s_add_i32 m0, s44, 0x4000\n" \
"s_mov_b32 s80, 0x1c00000\n" \
"v_mfma_f32_16x16x32_f16 v[48:51], v[120:123], v[144:147], v[48:51]\n" \
"v_pk_add_f16 v185, v185, v161\n" \
"v_pk_add_f16 v140, v124, v132\n" \
"buffer_load_dwordx4 v232, s[36:39], s80 offen sc1 lds\n" \
"v_mfma_f32_16x16x32_f16 v[60:63], v[124:127], v[144:147], v[60:63]\n" \
"v_pk_add_f16 v186, v186, v162\n" \
"v_pk_add_f16 v187, v187, v163\n" \
"v_mfma_f32_16x16x32_f16 v[84:87], v[124:127], v[148:151], v[84:87]\n" \
"v_pk_add_f16 v144, v144, v164\n" \
"v_pk_add_f16 v141, v125, v133\n" \
"v_mfma_f32_16x16x32_f16 v[72:75], v[120:123], v[148:151], v[72:75]\n" \
"v_pk_add_f16 v145, v145, v165\n" \
"v_pk_add_f16 v142, v126, v134\n" \
"v_mfma_f32_16x16x32_f16 v[96:99], v[120:123], v[152:155], v[96:99]\n" \
"v_pk_add_f16 v146, v146, v166\n" \
"v_pk_add_f16 v147, v147, v167\n" \
"ds_read_b64_tr_b16 v[188:189], v236 offset:8192\n" \
"v_mfma_f32_16x16x32_f16 v[108:111], v[124:127], v[152:155], v[108:111]\n" \
"v_pk_add_f16 v148, v148, v168\n" \
"v_pk_add_f16 v143, v127, v135\n" \
"ds_read_b64_tr_b16 v[192:193], v237 offset:8192\n" \
"v_mfma_f32_16x16x32_f16 v[4:7], v[128:131], v[156:159], v[4:7]\n" \
"v_pk_add_f16 v149, v149, v169\n" \
"ds_read_b64_tr_b16 v[120:121], v241 offset:32768\n" \
"buffer_load_dwordx4 v232, s[36:39], s80 offen offset:1024 sc1 lds\n" \
"v_mfma_f32_16x16x32_f16 v[16:19], v[132:135], v[156:159], v[16:19]\n" \
"v_pk_add_f16 v150, v150, v170\n" \
"ds_read_b128 v[176:179], v240 offset:41472\n" \
"v_mfma_f32_16x16x32_f16 v[40:43], v[132:135], v[160:163], v[40:43]\n" \
"v_pk_add_f16 v151, v151, v171\n" \
"ds_read_b128 v[156:159], v240 offset:40960\n" \
"v_mfma_f32_16x16x32_f16 v[28:31], v[128:131], v[160:163], v[28:31]\n" \
"v_pk_add_f16 v152, v152, v172\n" \
"ds_read_b64_tr_b16 v[122:123], v241 offset:40960\n" \
"v_mfma_f32_16x16x32_f16 v[52:55], v[128:131], v[164:167], v[52:55]\n" \
"v_pk_add_f16 v153, v153, v173\n" \
"ds_read_b128 v[160:163], v240 offset:43008\n" \
"s_waitcnt lgkmcnt(6)\n" \
"v_mfma_f32_16x16x16_bf16 v[188:191], v[188:189], v[254:255], 0\n" \
"v_mfma_f32_16x16x32_f16 v[64:67], v[132:135], v[164:167], v[64:67]\n" \
"v_pk_add_f16 v154, v154, v174\n" \
"ds_read_b64_tr_b16 v[124:125], v242 offset:32768\n" \
"s_waitcnt lgkmcnt(6)\n" \
"v_mfma_f32_16x16x16_bf16 v[192:195], v[192:193], v[254:255], 0\n" \
"v_mfma_f32_16x16x32_f16 v[88:91], v[132:135], v[168:171], v[88:91]\n" \
"v_pk_add_f16 v155, v155, v175\n" \
"ds_read_b128 v[164:167], v240 offset:45056\n" \
"buffer_load_dwordx4 v232, s[36:39], s80 offen offset:2048 sc1 lds\n" \
"v_mfma_f32_16x16x32_f16 v[76:79], v[128:131], v[168:171], v[76:79]\n" \
"ds_read_b64_tr_b16 v[126:127], v242 offset:40960\n" \
"v_mfma_f32_16x16x32_f16 v[100:103], v[128:131], v[172:175], v[100:103]\n" \
"v_cvt_pk_f16_f32 v188, v188, v189\n" \
"ds_read_b128 v[168:171], v240 offset:47104\n" \
"v_mfma_f32_16x16x32_f16 v[112:115], v[132:135], v[172:175], v[112:115]\n" \
"v_cvt_pk_f16_f32 v189, v190, v191\n" \
"ds_read_b64_tr_b16 v[128:129], v241 offset:33280\n" \
"v_mfma_f32_16x16x32_f16 v[8:11], v[136:139], v[180:183], v[8:11]\n" \
"v_cvt_pk_f16_f32 v190, v192, v193\n" \
"ds_read_b128 v[172:175], v240 offset:49152\n" \
"v_mfma_f32_16x16x32_f16 v[20:23], v[140:143], v[180:183], v[20:23]\n" \
"v_cvt_pk_f16_f32 v191, v194, v195\n" \
"v_add_u32 v188, s91, v188\n" \
"ds_read_b128 v[180:183], v240 offset:43520\n" \
"v_mfma_f32_16x16x32_f16 v[44:47], v[140:143], v[184:187], v[44:47]\n" \
"ds_read_b64_tr_b16 v[130:131], v241 offset:41472\n" \
"ds_read_b64_tr_b16 v[132:133], v242 offset:33280\n" \
"buffer_load_dwordx4 v232, s[36:39], s80 offen offset:3072 sc1 lds\n" \
"v_mfma_f32_16x16x32_f16 v[32:35], v[136:139], v[184:187], v[32:35]\n" \
"v_add_u32 v189, s91, v189\n" \
"v_add_u32 v190, s91, v190\n" \
"ds_read_b128 v[184:187], v240 offset:45568\n" \
"v_mfma_f32_16x16x32_f16 v[56:59], v[136:139], v[144:147], v[56:59]\n" \
"v_add_u32 v191, s91, v191\n" \
"ds_read_b64_tr_b16 v[134:135], v242 offset:41472\n" \
"v_mfma_f32_16x16x32_f16 v[68:71], v[140:143], v[144:147], v[68:71]\n" \
"v_and_b32 v188, s92, v188\n" \
"ds_read_b128 v[144:147], v240 offset:47616\n" \
"v_mfma_f32_16x16x32_f16 v[92:95], v[140:143], v[148:151], v[92:95]\n" \
"v_and_b32 v189, s92, v189\n" \
"v_and_b32 v190, s92, v190\n" \
"s_add_i32 s82, s51, 0x3800000\n" \
"v_mfma_f32_16x16x32_f16 v[80:83], v[136:139], v[148:151], v[80:83]\n" \
"v_and_b32 v191, s92, v191\n" \
"ds_read_b128 v[148:151], v240 offset:49664\n" \
"v_mfma_f32_16x16x32_f16 v[104:107], v[136:139], v[152:155], v[104:107]\n" \
"buffer_store_dwordx4 v[188:191], v234, s[36:39], s82 offen sc1\n" \
"v_mfma_f32_16x16x32_f16 v[116:119], v[140:143], v[152:155], v[116:119]\n" \
"s_waitcnt vmcnt(6) lgkmcnt(0)\n" \
"s_barrier\n" \
"v_mfma_f32_16x16x32_f16 v[0:3], v[120:123], v[156:159], v[0:3]\n" \
"v_pk_add_f16 v136, v120, v128\n" \
"v_pk_add_f16 v137, v121, v129\n" \
"v_mfma_f32_16x16x32_f16 v[12:15], v[124:127], v[156:159], v[12:15]\n" \
"v_pk_add_f16 v156, v156, v176\n" \
"v_pk_add_f16 v138, v122, v130\n" \
"v_mfma_f32_16x16x32_f16 v[36:39], v[124:127], v[160:163], v[36:39]\n" \
"v_pk_add_f16 v157, v157, v177\n" \
"v_pk_add_f16 v158, v158, v178\n" \
"v_pk_add_f16 v159, v159, v179\n" \
"v_mfma_f32_16x16x32_f16 v[24:27], v[120:123], v[160:163], v[24:27]\n" \
"v_pk_add_f16 v160, v160, v180\n" \
"v_pk_add_f16 v139, v123, v131\n" \
"s_add_i32 m0, s44, 0x8000\n" \
"s_mov_b32 s80, 0x2000000\n" \
"v_mfma_f32_16x16x32_f16 v[48:51], v[120:123], v[164:167], v[48:51]\n" \
"v_pk_add_f16 v161, v161, v181\n" \
"v_pk_add_f16 v140, v124, v132\n" \
"buffer_load_dwordx4 v232, s[36:39], s80 offen sc1 lds\n" \
"v_mfma_f32_16x16x32_f16 v[60:63], v[124:127], v[164:167], v[60:63]\n" \
"v_pk_add_f16 v162, v162, v182\n" \
"v_pk_add_f16 v163, v163, v183\n" \
"ds_read_b128 v[208:211], v240 offset:2560\n" \
"v_mfma_f32_16x16x32_f16 v[84:87], v[124:127], v[168:171], v[84:87]\n" \
"v_pk_add_f16 v164, v164, v184\n" \
"v_pk_add_f16 v141, v125, v133\n" \
"s_add_i32 s84, s54, 0x8008000\n" \
"v_mfma_f32_16x16x32_f16 v[72:75], v[120:123], v[168:171], v[72:75]\n" \
"v_pk_add_f16 v165, v165, v185\n" \
"v_pk_add_f16 v142, v126, v134\n" \
"buffer_store_dword v240, v243, s[36:39], s84 offen sc1\n" \
"v_mfma_f32_16x16x32_f16 v[96:99], v[120:123], v[172:175], v[96:99]\n" \
"v_pk_add_f16 v166, v166, v186\n" \
"v_pk_add_f16 v167, v167, v187\n" \
"ds_read_b64_tr_b16 v[188:189], v238 offset:8192\n" \
"v_mfma_f32_16x16x32_f16 v[108:111], v[124:127], v[172:175], v[108:111]\n" \
"v_pk_add_f16 v168, v168, v144\n" \
"v_pk_add_f16 v143, v127, v135\n" \
"ds_read_b64_tr_b16 v[192:193], v239 offset:8192\n" \
"v_mfma_f32_16x16x32_f16 v[4:7], v[128:131], v[176:179], v[4:7]\n" \
"v_pk_add_f16 v169, v169, v145\n" \
"ds_read_b64_tr_b16 v[120:121], v241 offset:0\n" \
"buffer_load_dwordx4 v232, s[36:39], s80 offen offset:1024 sc1 lds\n" \
"v_mfma_f32_16x16x32_f16 v[16:19], v[132:135], v[176:179], v[16:19]\n" \
"v_pk_add_f16 v170, v170, v146\n" \
"ds_read_b128 v[152:155], v240 offset:512\n" \
"v_mfma_f32_16x16x32_f16 v[40:43], v[132:135], v[180:183], v[40:43]\n" \
"v_pk_add_f16 v171, v171, v147\n" \
"ds_read_b128 v[176:179], v240 offset:0\n" \
"v_mfma_f32_16x16x32_f16 v[28:31], v[128:131], v[180:183], v[28:31]\n" \
"v_pk_add_f16 v172, v172, v148\n" \
"ds_read_b64_tr_b16 v[122:123], v241 offset:8192\n" \
"v_mfma_f32_16x16x32_f16 v[52:55], v[128:131], v[184:187], v[52:55]\n" \
"v_pk_add_f16 v173, v173, v149\n" \
"ds_read_b128 v[180:183], v240 offset:2048\n" \
"s_waitcnt lgkmcnt(6)\n" \
"v_mfma_f32_16x16x16_bf16 v[188:191], v[188:189], v[254:255], 0\n" \
"v_mfma_f32_16x16x32_f16 v[64:67], v[132:135], v[184:187], v[64:67]\n" \
"v_pk_add_f16 v174, v174, v150\n" \
"ds_read_b64_tr_b16 v[124:125], v242 offset:0\n" \
"s_waitcnt lgkmcnt(6)\n" \
"v_mfma_f32_16x16x16_bf16 v[192:195], v[192:193], v[254:255], 0\n" \
"v_mfma_f32_16x16x32_f16 v[88:91], v[132:135], v[144:147], v[88:91]\n" \
"v_pk_add_f16 v175, v175, v151\n" \
"ds_read_b128 v[184:187], v240 offset:4096\n" \
"buffer_load_dwordx4 v232, s[36:39], s80 offen offset:2048 sc1 lds\n" \
"v_mfma_f32_16x16x32_f16 v[76:79], v[128:131], v[144:147], v[76:79]\n" \
"ds_read_b64_tr_b16 v[126:127], v242 offset:8192\n" \
"v_mfma_f32_16x16x32_f16 v[100:103], v[128:131], v[148:151], v[100:103]\n" \
"v_cvt_pk_f16_f32 v188, v188, v189\n" \
"ds_read_b128 v[144:147], v240 offset:6144\n" \
"v_mfma_f32_16x16x32_f16 v[112:115], v[132:135], v[148:151], v[112:115]\n" \
"v_cvt_pk_f16_f32 v189, v190, v191\n" \
"ds_read_b64_tr_b16 v[128:129], v241 offset:512\n" \
"v_mfma_f32_16x16x32_f16 v[8:11], v[136:139], v[156:159], v[8:11]\n" \
"v_cvt_pk_f16_f32 v190, v192, v193\n" \
"ds_read_b128 v[148:151], v240 offset:8192\n" \
"v_mfma_f32_16x16x32_f16 v[20:23], v[140:143], v[156:159], v[20:23]\n" \
"v_cvt_pk_f16_f32 v191, v194, v195\n" \
"v_add_u32 v188, s91, v188\n" \
"ds_read_b128 v[156:159], v240 offset:4608\n" \
"v_mfma_f32_16x16x32_f16 v[44:47], v[140:143], v[160:163], v[44:47]\n" \
"ds_read_b64_tr_b16 v[130:131], v241 offset:8704\n" \
"ds_read_b64_tr_b16 v[132:133], v242 offset:512\n" \
"buffer_load_dwordx4 v232, s[36:39], s80 offen offset:3072 sc1 lds\n" \
"v_mfma_f32_16x16x32_f16 v[32:35], v[136:139], v[160:163], v[32:35]\n" \
"v_add_u32 v189, s91, v189\n" \
"v_add_u32 v190, s91, v190\n" \
"ds_read_b128 v[160:163], v240 offset:6656\n" \
"v_mfma_f32_16x16x32_f16 v[56:59], v[136:139], v[164:167], v[56:59]\n" \
"v_add_u32 v191, s91, v191\n" \
"ds_read_b64_tr_b16 v[134:135], v242 offset:8704\n" \
"v_mfma_f32_16x16x32_f16 v[68:71], v[140:143], v[164:167], v[68:71]\n" \
"v_and_b32 v188, s92, v188\n" \
"ds_read_b128 v[164:167], v240 offset:8704\n" \
"v_mfma_f32_16x16x32_f16 v[92:95], v[140:143], v[168:171], v[92:95]\n" \
"v_and_b32 v189, s92, v189\n" \
"v_mfma_f32_16x16x32_f16 v[80:83], v[136:139], v[168:171], v[80:83]\n" \
"v_and_b32 v190, s92, v190\n" \
"v_and_b32 v191, s92, v191\n" \
"v_mfma_f32_16x16x32_f16 v[104:107], v[136:139], v[172:175], v[104:107]\n" \
"buffer_store_dwordx4 v[188:191], v234, s[36:39], s82 offen offset:1024 sc1\n" \
"v_mfma_f32_16x16x32_f16 v[116:119], v[140:143], v[172:175], v[116:119]\n" \
"s_waitcnt vmcnt(7) lgkmcnt(0)\n" \
"s_barrier\n" \
"v_mfma_f32_16x16x32_f16 v[0:3], v[120:123], v[176:179], v[0:3]\n" \
"v_pk_add_f16 v136, v120, v128\n" \
"v_pk_add_f16 v137, v121, v129\n" \
"s_mov_b32 s81, 0x8006000\n" \
"v_mfma_f32_16x16x32_f16 v[12:15], v[124:127], v[176:179], v[12:15]\n" \
"v_pk_add_f16 v176, v176, v152\n" \
"v_pk_add_f16 v138, v122, v130\n" \
"buffer_load_dword v253, v252, s[36:39], s81 offen sc1\n" \
"v_mfma_f32_16x16x32_f16 v[36:39], v[124:127], v[180:183], v[36:39]\n" \
"v_pk_add_f16 v177, v177, v153\n" \
"v_pk_add_f16 v178, v178, v154\n" \
"v_pk_add_f16 v179, v179, v155\n" \
"buffer_store_dwordx4 v[212:215], v235, s[36:39], s64 offen sc1\n" \
"v_mfma_f32_16x16x32_f16 v[24:27], v[120:123], v[180:183], v[24:27]\n" \
"v_pk_add_f16 v180, v180, v208\n" \
"v_pk_add_f16 v139, v123, v131\n" \
"s_add_i32 m0, s44, 0x0\n" \
"s_mov_b32 s80, 0x2400000\n" \
"v_mfma_f32_16x16x32_f16 v[48:51], v[120:123], v[184:187], v[48:51]\n" \
"v_pk_add_f16 v181, v181, v209\n" \
"v_pk_add_f16 v140, v124, v132\n" \
"buffer_load_dwordx4 v232, s[36:39], s80 offen sc1 lds\n" \
"v_mfma_f32_16x16x32_f16 v[60:63], v[124:127], v[184:187], v[60:63]\n" \
"v_pk_add_f16 v182, v182, v210\n" \
"v_pk_add_f16 v183, v183, v211\n" \
"ds_read_b128 v[172:175], v240 offset:23040\n" \
"v_mfma_f32_16x16x32_f16 v[84:87], v[124:127], v[144:147], v[84:87]\n" \
"v_pk_add_f16 v184, v184, v156\n" \
"v_pk_add_f16 v141, v125, v133\n" \
"v_mfma_f32_16x16x32_f16 v[72:75], v[120:123], v[144:147], v[72:75]\n" \
"v_pk_add_f16 v185, v185, v157\n" \
"v_pk_add_f16 v142, v126, v134\n" \
"v_mfma_f32_16x16x32_f16 v[96:99], v[120:123], v[148:151], v[96:99]\n" \
"v_pk_add_f16 v186, v186, v158\n" \
"v_pk_add_f16 v187, v187, v159\n" \
"ds_read_b64_tr_b16 v[188:189], v236 offset:12288\n" \
"v_mfma_f32_16x16x32_f16 v[108:111], v[124:127], v[148:151], v[108:111]\n" \
"v_pk_add_f16 v144, v144, v160\n" \
"v_pk_add_f16 v143, v127, v135\n" \
"ds_read_b64_tr_b16 v[192:193], v237 offset:12288\n" \
"v_mfma_f32_16x16x32_f16 v[4:7], v[128:131], v[152:155], v[4:7]\n" \
"v_pk_add_f16 v145, v145, v161\n" \
"ds_read_b64_tr_b16 v[120:121], v241 offset:16384\n" \
"buffer_load_dwordx4 v232, s[36:39], s80 offen offset:1024 sc1 lds\n" \
"v_mfma_f32_16x16x32_f16 v[16:19], v[132:135], v[152:155], v[16:19]\n" \
"v_pk_add_f16 v146, v146, v162\n" \
"ds_read_b128 v[168:171], v240 offset:20992\n" \
"v_mfma_f32_16x16x32_f16 v[40:43], v[132:135], v[208:211], v[40:43]\n" \
"v_pk_add_f16 v147, v147, v163\n" \
"ds_read_b128 v[152:155], v240 offset:20480\n" \
"v_mfma_f32_16x16x32_f16 v[28:31], v[128:131], v[208:211], v[28:31]\n" \
"v_pk_add_f16 v148, v148, v164\n" \
"ds_read_b64_tr_b16 v[122:123], v241 offset:24576\n" \
"v_mfma_f32_16x16x32_f16 v[52:55], v[128:131], v[156:159], v[52:55]\n" \
"v_pk_add_f16 v149, v149, v165\n" \
"ds_read_b128 v[208:211], v240 offset:22528\n" \
"s_waitcnt lgkmcnt(6)\n" \
"v_mfma_f32_16x16x16_bf16 v[188:191], v[188:189], v[254:255], 0\n" \
"v_mfma_f32_16x16x32_f16 v[64:67], v[132:135], v[156:159], v[64:67]\n" \
"v_pk_add_f16 v150, v150, v166\n" \
"ds_read_b64_tr_b16 v[124:125], v242 offset:16384\n" \
"s_waitcnt lgkmcnt(6)\n" \
"v_mfma_f32_16x16x16_bf16 v[192:195], v[192:193], v[254:255], 0\n" \
"v_mfma_f32_16x16x32_f16 v[88:91], v[132:135], v[160:163], v[88:91]\n" \
"v_pk_add_f16 v151, v151, v167\n" \
"ds_read_b128 v[156:159], v240 offset:24576\n" \
"buffer_load_dwordx4 v232, s[36:39], s80 offen offset:2048 sc1 lds\n" \
"v_mfma_f32_16x16x32_f16 v[76:79], v[128:131], v[160:163], v[76:79]\n" \
"ds_read_b64_tr_b16 v[126:127], v242 offset:24576\n" \
"v_mfma_f32_16x16x32_f16 v[100:103], v[128:131], v[164:167], v[100:103]\n" \
"v_cvt_pk_f16_f32 v188, v188, v189\n" \
"ds_read_b128 v[160:163], v240 offset:26624\n" \
"v_mfma_f32_16x16x32_f16 v[112:115], v[132:135], v[164:167], v[112:115]\n" \
"v_cvt_pk_f16_f32 v189, v190, v191\n" \
"ds_read_b64_tr_b16 v[128:129], v241 offset:16896\n" \
"v_mfma_f32_16x16x32_f16 v[8:11], v[136:139], v[176:179], v[8:11]\n" \
"v_cvt_pk_f16_f32 v190, v192, v193\n" \
"ds_read_b128 v[164:167], v240 offset:28672\n" \
"v_mfma_f32_16x16x32_f16 v[20:23], v[140:143], v[176:179], v[20:23]\n" \
"v_cvt_pk_f16_f32 v191, v194, v195\n" \
"v_add_u32 v188, s91, v188\n" \
"ds_read_b128 v[176:179], v240 offset:25088\n" \
"v_mfma_f32_16x16x32_f16 v[44:47], v[140:143], v[180:183], v[44:47]\n" \
"ds_read_b64_tr_b16 v[130:131], v241 offset:25088\n" \
"ds_read_b64_tr_b16 v[132:133], v242 offset:16896\n" \
"buffer_load_dwordx4 v232, s[36:39], s80 offen offset:3072 sc1 lds\n" \
"v_mfma_f32_16x16x32_f16 v[32:35], v[136:139], v[180:183], v[32:35]\n" \
"v_add_u32 v189, s91, v189\n" \
"v_add_u32 v190, s91, v190\n" \
"ds_read_b128 v[180:183], v240 offset:27136\n" \
"v_mfma_f32_16x16x32_f16 v[56:59], v[136:139], v[184:187], v[56:59]\n" \
"v_add_u32 v191, s91, v191\n" \
"ds_read_b64_tr_b16 v[134:135], v242 offset:25088\n" \
"v_mfma_f32_16x16x32_f16 v[68:71], v[140:143], v[184:187], v[68:71]\n" \
"v_and_b32 v188, s92, v188\n" \
"ds_read_b128 v[184:187], v240 offset:29184\n" \
"v_mfma_f32_16x16x32_f16 v[92:95], v[140:143], v[144:147], v[92:95]\n" \
"v_and_b32 v189, s92, v189\n" \
"s_add_i32 s82, s48, 0x4000000\n" \
"v_mfma_f32_16x16x32_f16 v[80:83], v[136:139], v[144:147], v[80:83]\n" \
"v_and_b32 v190, s92, v190\n" \
"v_and_b32 v191, s92, v191\n" \
"v_mfma_f32_16x16x32_f16 v[104:107], v[136:139], v[148:151], v[104:107]\n" \
"buffer_store_dwordx4 v[188:191], v234, s[36:39], s82 offen sc1\n" \
"v_mfma_f32_16x16x32_f16 v[116:119], v[140:143], v[148:151], v[116:119]\n" \
"s_waitcnt vmcnt(6) lgkmcnt(0)\n" \
"s_barrier\n" \
"v_mfma_f32_16x16x32_f16 v[0:3], v[120:123], v[152:155], v[0:3]\n" \
"v_pk_add_f16 v136, v120, v128\n" \
"v_pk_add_f16 v137, v121, v129\n" \
"v_mfma_f32_16x16x32_f16 v[12:15], v[124:127], v[152:155], v[12:15]\n" \
"v_pk_add_f16 v152, v152, v168\n" \
"v_pk_add_f16 v138, v122, v130\n" \
"v_mfma_f32_16x16x32_f16 v[36:39], v[124:127], v[208:211], v[36:39]\n" \
"v_pk_add_f16 v153, v153, v169\n" \
"v_pk_add_f16 v154, v154, v170\n" \
"v_pk_add_f16 v155, v155, v171\n" \
"v_cmp_eq_u32 vcc, 0, v253\n" \
"s_cbranch_vccnz .Ls03_%=\n" \
".Lr03_%=:\n" \
"v_mfma_f32_16x16x32_f16 v[24:27], v[120:123], v[208:211], v[24:27]\n" \
"v_pk_add_f16 v208, v208, v172\n" \
"v_pk_add_f16 v139, v123, v131\n" \
"s_add_i32 m0, s44, 0x4000\n" \
"s_mov_b32 s80, 0x2800000\n" \
"v_mfma_f32_16x16x32_f16 v[48:51], v[120:123], v[156:159], v[48:51]\n" \
"v_pk_add_f16 v209, v209, v173\n" \
"v_pk_add_f16 v140, v124, v132\n" \
"buffer_load_dwordx4 v232, s[36:39], s80 offen sc1 lds\n" \
"v_mfma_f32_16x16x32_f16 v[60:63], v[124:127], v[156:159], v[60:63]\n" \
"v_pk_add_f16 v210, v210, v174\n" \
"v_pk_add_f16 v211, v211, v175\n" \
"ds_read_b128 v[148:151], v240 offset:43520\n" \
"v_mfma_f32_16x16x32_f16 v[84:87], v[124:127], v[160:163], v[84:87]\n" \
"v_pk_add_f16 v156, v156, v176\n" \
"v_pk_add_f16 v141, v125, v133\n" \
"s_add_i32 s84, s55, 0x800a000\n" \
"v_mfma_f32_16x16x32_f16 v[72:75], v[120:123], v[160:163], v[72:75]\n" \
"v_pk_add_f16 v157, v157, v177\n" \
"v_pk_add_f16 v142, v126, v134\n" \
"buffer_store_dword v240, v243, s[36:39], s84 offen sc1\n" \
"v_mfma_f32_16x16x32_f16 v[96:99], v[120:123], v[164:167], v[96:99]\n" \
"v_pk_add_f16 v158, v158, v178\n" \
"v_pk_add_f16 v159, v159, v179\n" \
"ds_read_b64_tr_b16 v[188:189], v238 offset:12288\n" \
"v_mfma_f32_16x16x32_f16 v[108:111], v[124:127], v[164:167], v[108:111]\n" \
"v_pk_add_f16 v160, v160, v180\n" \
"v_pk_add_f16 v143, v127, v135\n" \
"ds_read_b64_tr_b16 v[192:193], v239 offset:12288\n" \
"v_mfma_f32_16x16x32_f16 v[4:7], v[128:131], v[168:171], v[4:7]\n" \
"v_pk_add_f16 v161, v161, v181\n" \
"ds_read_b64_tr_b16 v[120:121], v241 offset:32768\n" \
"buffer_load_dwordx4 v232, s[36:39], s80 offen offset:1024 sc1 lds\n" \
"v_mfma_f32_16x16x32_f16 v[16:19], v[132:135], v[168:171], v[16:19]\n" \
"v_pk_add_f16 v162, v162, v182\n" \
"ds_read_b128 v[144:147], v240 offset:41472\n" \
"v_mfma_f32_16x16x32_f16 v[40:43], v[132:135], v[172:175], v[40:43]\n" \
"v_pk_add_f16 v163, v163, v183\n" \
"ds_read_b128 v[168:171], v240 offset:40960\n" \
"v_mfma_f32_16x16x32_f16 v[28:31], v[128:131], v[172:175], v[28:31]\n" \
"v_pk_add_f16 v164, v164, v184\n" \
"ds_read_b64_tr_b16 v[122:123], v241 offset:40960\n" \
"v_mfma_f32_16x16x32_f16 v[52:55], v[128:131], v[176:179], v[52:55]\n" \
"v_pk_add_f16 v165, v165, v185\n" \
"ds_read_b128 v[172:175], v240 offset:43008\n" \
"s_waitcnt lgkmcnt(6)\n" \
"v_mfma_f32_16x16x16_bf16 v[188:191], v[188:189], v[254:255], 0\n" \
"v_mfma_f32_16x16x32_f16 v[64:67], v[132:135], v[176:179], v[64:67]\n" \
"v_pk_add_f16 v166, v166, v186\n" \
"ds_read_b64_tr_b16 v[124:125], v242 offset:32768\n" \
"s_waitcnt lgkmcnt(6)\n" \
"v_mfma_f32_16x16x16_bf16 v[192:195], v[192:193], v[254:255], 0\n" \
"v_mfma_f32_16x16x32_f16 v[88:91], v[132:135], v[180:183], v[88:91]\n" \
"v_pk_add_f16 v167, v167, v187\n" \
"ds_read_b128 v[176:179], v240 offset:45056\n" \
"buffer_load_dwordx4 v232, s[36:39], s80 offen offset:2048 sc1 lds\n" \
"v_mfma_f32_16x16x32_f16 v[76:79], v[128:131], v[180:183], v[76:79]\n" \
"ds_read_b64_tr_b16 v[126:127], v242 offset:40960\n" \
"v_mfma_f32_16x16x32_f16 v[100:103], v[128:131], v[184:187], v[100:103]\n" \
"v_cvt_pk_f16_f32 v188, v188, v189\n" \
"ds_read_b128 v[180:183], v240 offset:47104\n" \
"v_mfma_f32_16x16x32_f16 v[112:115], v[132:135], v[184:187], v[112:115]\n" \
"v_cvt_pk_f16_f32 v189, v190, v191\n" \
"ds_read_b64_tr_b16 v[128:129], v241 offset:33280\n" \
"v_mfma_f32_16x16x32_f16 v[8:11], v[136:139], v[152:155], v[8:11]\n" \
"v_cvt_pk_f16_f32 v190, v192, v193\n" \
"ds_read_b128 v[184:187], v240 offset:49152\n" \
"v_mfma_f32_16x16x32_f16 v[20:23], v[140:143], v[152:155], v[20:23]\n" \
"v_cvt_pk_f16_f32 v191, v194, v195\n" \
"v_add_u32 v188, s91, v188\n" \
"ds_read_b128 v[152:155], v240 offset:45568\n" \
"v_mfma_f32_16x16x32_f16 v[44:47], v[140:143], v[208:211], v[44:47]\n" \
"ds_read_b64_tr_b16 v[130:131], v241 offset:41472\n" \
"ds_read_b64_tr_b16 v[132:133], v242 offset:33280\n" \
"buffer_load_dwordx4 v232, s[36:39], s80 offen offset:3072 sc1 lds\n" \
"v_mfma_f32_16x16x32_f16 v[32:35], v[136:139], v[208:211], v[32:35]\n" \
"v_add_u32 v189, s91, v189\n" \
"v_add_u32 v190, s91, v190\n" \
"ds_read_b128 v[208:211], v240 offset:47616\n" \
"v_mfma_f32_16x16x32_f16 v[56:59], v[136:139], v[156:159], v[56:59]\n" \
"v_add_u32 v191, s91, v191\n" \
"ds_read_b64_tr_b16 v[134:135], v242 offset:41472\n" \
"v_mfma_f32_16x16x32_f16 v[68:71], v[140:143], v[156:159], v[68:71]\n" \
"v_and_b32 v188, s92, v188\n" \
"ds_read_b128 v[156:159], v240 offset:49664\n" \
"v_mfma_f32_16x16x32_f16 v[92:95], v[140:143], v[160:163], v[92:95]\n" \
"v_and_b32 v189, s92, v189\n" \
"v_mfma_f32_16x16x32_f16 v[80:83], v[136:139], v[160:163], v[80:83]\n" \
"v_and_b32 v190, s92, v190\n" \
"v_and_b32 v191, s92, v191\n" \
"v_mfma_f32_16x16x32_f16 v[104:107], v[136:139], v[164:167], v[104:107]\n" \
"buffer_store_dwordx4 v[188:191], v234, s[36:39], s82 offen offset:1024 sc1\n" \
"v_mfma_f32_16x16x32_f16 v[116:119], v[140:143], v[164:167], v[116:119]\n" \
"s_waitcnt vmcnt(7) lgkmcnt(0)\n" \
"s_barrier\n" \
"v_mfma_f32_16x16x32_f16 v[0:3], v[120:123], v[168:171], v[0:3]\n" \
"v_pk_add_f16 v136, v120, v128\n" \
"v_pk_add_f16 v137, v121, v129\n" \
"s_mov_b32 s81, 0x8008000\n" \
"v_mfma_f32_16x16x32_f16 v[12:15], v[124:127], v[168:171], v[12:15]\n" \
"v_pk_add_f16 v168, v168, v144\n" \
"v_pk_add_f16 v138, v122, v130\n" \
"buffer_load_dword v253, v252, s[36:39], s81 offen sc1\n" \
"v_mfma_f32_16x16x32_f16 v[36:39], v[124:127], v[172:175], v[36:39]\n" \
"v_pk_add_f16 v169, v169, v145\n" \
"v_pk_add_f16 v170, v170, v146\n" \
"v_pk_add_f16 v171, v171, v147\n" \
"buffer_store_dwordx4 v[216:219], v235, s[36:39], s65 offen sc1\n" \
"v_mfma_f32_16x16x32_f16 v[24:27], v[120:123], v[172:175], v[24:27]\n" \
"v_pk_add_f16 v172, v172, v148\n" \
"v_pk_add_f16 v139, v123, v131\n" \
"s_add_i32 m0, s44, 0x8000\n" \
"s_mov_b32 s80, 0x2c00000\n" \
"v_mfma_f32_16x16x32_f16 v[48:51], v[120:123], v[176:179], v[48:51]\n" \
"v_pk_add_f16 v173, v173, v149\n" \
"v_pk_add_f16 v140, v124, v132\n" \
"buffer_load_dwordx4 v232, s[36:39], s80 offen sc1 lds\n" \
"v_mfma_f32_16x16x32_f16 v[60:63], v[124:127], v[176:179], v[60:63]\n" \
"v_pk_add_f16 v174, v174, v150\n" \
"v_pk_add_f16 v175, v175, v151\n" \
"ds_read_b128 v[164:167], v240 offset:2560\n" \
"v_mfma_f32_16x16x32_f16 v[84:87], v[124:127], v[180:183], v[84:87]\n" \
"v_pk_add_f16 v176, v176, v152\n" \
"v_pk_add_f16 v141, v125, v133\n" \
"v_mfma_f32_16x16x32_f16 v[72:75], v[120:123], v[180:183], v[72:75]\n" \
"v_pk_add_f16 v177, v177, v153\n" \
"v_pk_add_f16 v142, v126, v134\n" \
"v_mfma_f32_16x16x32_f16 v[96:99], v[120:123], v[184:187], v[96:99]\n" \
"v_pk_add_f16 v178, v178, v154\n" \
"v_pk_add_f16 v179, v179, v155\n" \
"ds_read_b64_tr_b16 v[188:189], v236 offset:16384\n" \
"v_mfma_f32_16x16x32_f16 v[108:111], v[124:127], v[184:187], v[108:111]\n" \
"v_pk_add_f16 v180, v180, v208\n" \
"v_pk_add_f16 v143, v127, v135\n" \
"ds_read_b64_tr_b16 v[192:193], v237 offset:16384\n" \
"v_mfma_f32_16x16x32_f16 v[4:7], v[128:131], v[144:147], v[4:7]\n" \
"v_pk_add_f16 v181, v181, v209\n" \
"ds_read_b64_tr_b16 v[120:121], v241 offset:0\n" \
"buffer_load_dwordx4 v232, s[36:39], s80 offen offset:1024 sc1 lds\n" \
"v_mfma_f32_16x16x32_f16 v[16:19], v[132:135], v[144:147], v[16:19]\n" \
"v_pk_add_f16 v182, v182, v210\n" \
"ds_read_b128 v[160:163], v240 offset:512\n" \
"v_mfma_f32_16x16x32_f16 v[40:43], v[132:135], v[148:151], v[40:43]\n" \
"v_pk_add_f16 v183, v183, v211\n" \
"ds_read_b128 v[144:147], v240 offset:0\n" \
"v_mfma_f32_16x16x32_f16 v[28:31], v[128:131], v[148:151], v[28:31]\n" \
"v_pk_add_f16 v184, v184, v156\n" \
"ds_read_b64_tr_b16 v[122:123], v241 offset:8192\n" \
"v_mfma_f32_16x16x32_f16 v[52:55], v[128:131], v[152:155], v[52:55]\n" \
"v_pk_add_f16 v185, v185, v157\n" \
"ds_read_b128 v[148:151], v240 offset:2048\n" \
"s_waitcnt lgkmcnt(6)\n" \
"v_mfma_f32_16x16x16_bf16 v[188:191], v[188:189], v[254:255], 0\n" \
"v_mfma_f32_16x16x32_f16 v[64:67], v[132:135], v[152:155], v[64:67]\n" \
"v_pk_add_f16 v186, v186, v158\n" \
"ds_read_b64_tr_b16 v[124:125], v242 offset:0\n" \
"s_waitcnt lgkmcnt(6)\n" \
"v_mfma_f32_16x16x16_bf16 v[192:195], v[192:193], v[254:255], 0\n" \
"v_mfma_f32_16x16x32_f16 v[88:91], v[132:135], v[208:211], v[88:91]\n" \
"v_pk_add_f16 v187, v187, v159\n" \
"ds_read_b128 v[152:155], v240 offset:4096\n" \
"buffer_load_dwordx4 v232, s[36:39], s80 offen offset:2048 sc1 lds\n" \
"v_mfma_f32_16x16x32_f16 v[76:79], v[128:131], v[208:211], v[76:79]\n" \
"ds_read_b64_tr_b16 v[126:127], v242 offset:8192\n" \
"v_mfma_f32_16x16x32_f16 v[100:103], v[128:131], v[156:159], v[100:103]\n" \
"v_cvt_pk_f16_f32 v188, v188, v189\n" \
"ds_read_b128 v[208:211], v240 offset:6144\n" \
"v_mfma_f32_16x16x32_f16 v[112:115], v[132:135], v[156:159], v[112:115]\n" \
"v_cvt_pk_f16_f32 v189, v190, v191\n" \
"ds_read_b64_tr_b16 v[128:129], v241 offset:512\n" \
"v_mfma_f32_16x16x32_f16 v[8:11], v[136:139], v[168:171], v[8:11]\n" \
"v_cvt_pk_f16_f32 v190, v192, v193\n" \
"ds_read_b128 v[156:159], v240 offset:8192\n" \
"v_mfma_f32_16x16x32_f16 v[20:23], v[140:143], v[168:171], v[20:23]\n" \
"v_cvt_pk_f16_f32 v191, v194, v195\n" \
"v_add_u32 v188, s91, v188\n" \
"ds_read_b128 v[168:171], v240 offset:4608\n" \
"v_mfma_f32_16x16x32_f16 v[44:47], v[140:143], v[172:175], v[44:47]\n" \
"ds_read_b64_tr_b16 v[130:131], v241 offset:8704\n" \
"ds_read_b64_tr_b16 v[132:133], v242 offset:512\n" \
"buffer_load_dwordx4 v232, s[36:39], s80 offen offset:3072 sc1 lds\n" \
"v_mfma_f32_16x16x32_f16 v[32:35], v[136:139], v[172:175], v[32:35]\n" \
"v_add_u32 v189, s91, v189\n" \
"v_add_u32 v190, s91, v190\n" \
"ds_read_b128 v[172:175], v240 offset:6656\n" \
"v_mfma_f32_16x16x32_f16 v[56:59], v[136:139], v[176:179], v[56:59]\n" \
"v_add_u32 v191, s91, v191\n" \
"ds_read_b64_tr_b16 v[134:135], v242 offset:8704\n" \
"v_mfma_f32_16x16x32_f16 v[68:71], v[140:143], v[176:179], v[68:71]\n" \
"v_and_b32 v188, s92, v188\n" \
"ds_read_b128 v[176:179], v240 offset:8704\n" \
"v_mfma_f32_16x16x32_f16 v[92:95], v[140:143], v[180:183], v[92:95]\n" \
"v_and_b32 v189, s92, v189\n" \
"s_add_i32 s82, s49, 0x4800000\n" \
"v_mfma_f32_16x16x32_f16 v[80:83], v[136:139], v[180:183], v[80:83]\n" \
"v_and_b32 v190, s92, v190\n" \
"v_and_b32 v191, s92, v191\n" \
"v_mfma_f32_16x16x32_f16 v[104:107], v[136:139], v[184:187], v[104:107]\n" \
"buffer_store_dwordx4 v[188:191], v234, s[36:39], s82 offen sc1\n" \
"v_mfma_f32_16x16x32_f16 v[116:119], v[140:143], v[184:187], v[116:119]\n" \
"s_waitcnt vmcnt(6) lgkmcnt(0)\n" \
"s_barrier\n" \
"v_mfma_f32_16x16x32_f16 v[0:3], v[120:123], v[144:147], v[0:3]\n" \
"v_pk_add_f16 v136, v120, v128\n" \
"v_pk_add_f16 v137, v121, v129\n" \
"v_mfma_f32_16x16x32_f16 v[12:15], v[124:127], v[144:147], v[12:15]\n" \
"v_pk_add_f16 v144, v144, v160\n" \
"v_pk_add_f16 v138, v122, v130\n" \
"v_mfma_f32_16x16x32_f16 v[36:39], v[124:127], v[148:151], v[36:39]\n" \
"v_pk_add_f16 v145, v145, v161\n" \
"v_pk_add_f16 v146, v146, v162\n" \
"v_pk_add_f16 v147, v147, v163\n" \
"v_cmp_eq_u32 vcc, 0, v253\n" \
"s_cbranch_vccnz .Ls04_%=\n" \
".Lr04_%=:\n" \
"v_mfma_f32_16x16x32_f16 v[24:27], v[120:123], v[148:151], v[24:27]\n" \
"v_pk_add_f16 v148, v148, v164\n" \
"v_pk_add_f16 v139, v123, v131\n" \
"s_add_i32 m0, s44, 0x0\n" \
"s_mov_b32 s80, 0x3000000\n" \
"v_mfma_f32_16x16x32_f16 v[48:51], v[120:123], v[152:155], v[48:51]\n" \
"v_pk_add_f16 v149, v149, v165\n" \
"v_pk_add_f16 v140, v124, v132\n" \
"buffer_load_dwordx4 v232, s[36:39], s80 offen sc1 lds\n" \
"v_mfma_f32_16x16x32_f16 v[60:63], v[124:127], v[152:155], v[60:63]\n" \
"v_pk_add_f16 v150, v150, v166\n" \
"v_pk_add_f16 v151, v151, v167\n" \
"ds_read_b128 v[184:187], v240 offset:23040\n" \
"v_mfma_f32_16x16x32_f16 v[84:87], v[124:127], v[208:211], v[84:87]\n" \
"v_pk_add_f16 v152, v152, v168\n" \
"v_pk_add_f16 v141, v125, v133\n" \
"s_add_i32 s84, s52, 0x800c000\n" \
"v_mfma_f32_16x16x32_f16 v[72:75], v[120:123], v[208:211], v[72:75]\n" \
"v_pk_add_f16 v153, v153, v169\n" \
"v_pk_add_f16 v142, v126, v134\n" \
"buffer_store_dword v240, v243, s[36:39], s84 offen sc1\n" \
"v_mfma_f32_16x16x32_f16 v[96:99], v[120:123], v[156:159], v[96:99]\n" \
"v_pk_add_f16 v154, v154, v170\n" \
"v_pk_add_f16 v155, v155, v171\n" \
"ds_read_b64_tr_b16 v[188:189], v238 offset:16384\n" \
"v_mfma_f32_16x16x32_f16 v[108:111], v[124:127], v[156:159], v[108:111]\n" \
"v_pk_add_f16 v208, v208, v172\n" \
"v_pk_add_f16 v143, v127, v135\n" \
"ds_read_b64_tr_b16 v[192:193], v239 offset:16384\n" \
"v_mfma_f32_16x16x32_f16 v[4:7], v[128:131], v[160:163], v[4:7]\n" \
"v_pk_add_f16 v209, v209, v173\n" \
"ds_read_b64_tr_b16 v[120:121], v241 offset:16384\n" \
"buffer_load_dwordx4 v232, s[36:39], s80 offen offset:1024 sc1 lds\n" \
"v_mfma_f32_16x16x32_f16 v[16:19], v[132:135], v[160:163], v[16:19]\n" \
"v_pk_add_f16 v210, v210, v174\n" \
"ds_read_b128 v[180:183], v240 offset:20992\n" \
"v_mfma_f32_16x16x32_f16 v[40:43], v[132:135], v[164:167], v[40:43]\n" \
"v_pk_add_f16 v211, v211, v175\n" \
"ds_read_b128 v[160:163], v240 offset:20480\n" \
"v_mfma_f32_16x16x32_f16 v[28:31], v[128:131], v[164:167], v[28:31]\n" \
"v_pk_add_f16 v156, v156, v176\n" \
"ds_read_b64_tr_b16 v[122:123], v241 offset:24576\n" \
"v_mfma_f32_16x16x32_f16 v[52:55], v[128:131], v[168:171], v[52:55]\n" \
"v_pk_add_f16 v157, v157, v177\n" \
"ds_read_b128 v[164:167], v240 offset:22528\n" \
"s_waitcnt lgkmcnt(6)\n" \
"v_mfma_f32_16x16x16_bf16 v[188:191], v[188:189], v[254:255], 0\n" \
"v_mfma_f32_16x16x32_f16 v[64:67], v[132:135], v[168:171], v[64:67]\n" \
"v_pk_add_f16 v158, v158, v178\n" \
"ds_read_b64_tr_b16 v[124:125], v242 offset:16384\n" \
"s_waitcnt lgkmcnt(6)\n" \
"v_mfma_f32_16x16x16_bf16 v[192:195], v[192:193], v[254:255], 0\n" \
"v_mfma_f32_16x16x32_f16 v[88:91], v[132:135], v[172:175], v[88:91]\n" \
"v_pk_add_f16 v159, v159, v179\n" \
"ds_read_b128 v[168:171], v240 offset:24576\n" \
"buffer_load_dwordx4 v232, s[36:39], s80 offen offset:2048 sc1 lds\n" \
"v_mfma_f32_16x16x32_f16 v[76:79], v[128:131], v[172:175], v[76:79]\n" \
"ds_read_b64_tr_b16 v[126:127], v242 offset:24576\n" \
"v_mfma_f32_16x16x32_f16 v[100:103], v[128:131], v[176:179], v[100:103]\n" \
"v_cvt_pk_f16_f32 v188, v188, v189\n" \
"ds_read_b128 v[172:175], v240 offset:26624\n" \
"v_mfma_f32_16x16x32_f16 v[112:115], v[132:135], v[176:179], v[112:115]\n" \
"v_cvt_pk_f16_f32 v189, v190, v191\n" \
"ds_read_b64_tr_b16 v[128:129], v241 offset:16896\n" \
"v_mfma_f32_16x16x32_f16 v[8:11], v[136:139], v[144:147], v[8:11]\n" \
"v_cvt_pk_f16_f32 v190, v192, v193\n" \
"ds_read_b128 v[176:179], v240 offset:28672\n" \
"v_mfma_f32_16x16x32_f16 v[20:23], v[140:143], v[144:147], v[20:23]\n" \
"v_cvt_pk_f16_f32 v191, v194, v195\n" \
"v_add_u32 v188, s91, v188\n" \
"ds_read_b128 v[144:147], v240 offset:25088\n" \
"v_mfma_f32_16x16x32_f16 v[44:47], v[140:143], v[148:151], v[44:47]\n" \
"ds_read_b64_tr_b16 v[130:131], v241 offset:25088\n" \
"ds_read_b64_tr_b16 v[132:133], v242 offset:16896\n" \
"buffer_load_dwordx4 v232, s[36:39], s80 offen offset:3072 sc1 lds\n" \
"v_mfma_f32_16x16x32_f16 v[32:35], v[136:139], v[148:151], v[32:35]\n" \
"v_add_u32 v189, s91, v189\n" \
"v_add_u32 v190, s91, v190\n" \
"ds_read_b128 v[148:151], v240 offset:27136\n" \
"v_mfma_f32_16x16x32_f16 v[56:59], v[136:139], v[152:155], v[56:59]\n" \
"v_add_u32 v191, s91, v191\n" \
"ds_read_b64_tr_b16 v[134:135], v242 offset:25088\n" \
"v_mfma_f32_16x16x32_f16 v[68:71], v[140:143], v[152:155], v[68:71]\n" \
"v_and_b32 v188, s92, v188\n" \
"ds_read_b128 v[152:155], v240 offset:29184\n" \
"v_mfma_f32_16x16x32_f16 v[92:95], v[140:143], v[208:211], v[92:95]\n" \
"v_and_b32 v189, s92, v189\n" \
"v_mfma_f32_16x16x32_f16 v[80:83], v[136:139], v[208:211], v[80:83]\n" \
"v_and_b32 v190, s92, v190\n" \
"v_and_b32 v191, s92, v191\n" \
"v_mfma_f32_16x16x32_f16 v[104:107], v[136:139], v[156:159], v[104:107]\n" \
"buffer_store_dwordx4 v[188:191], v234, s[36:39], s82 offen offset:1024 sc1\n" \
"v_mfma_f32_16x16x32_f16 v[116:119], v[140:143], v[156:159], v[116:119]\n" \
"s_waitcnt vmcnt(7) lgkmcnt(0)\n" \
"s_barrier\n" \
"v_mfma_f32_16x16x32_f16 v[0:3], v[120:123], v[160:163], v[0:3]\n" \
"v_pk_add_f16 v136, v120, v128\n" \
"v_pk_add_f16 v137, v121, v129\n" \
"s_mov_b32 s81, 0x800a000\n" \
"v_mfma_f32_16x16x32_f16 v[12:15], v[124:127], v[160:163], v[12:15]\n" \
"v_pk_add_f16 v160, v160, v180\n" \
"v_pk_add_f16 v138, v122, v130\n" \
"buffer_load_dword v253, v252, s[36:39], s81 offen sc1\n" \
"v_mfma_f32_16x16x32_f16 v[36:39], v[124:127], v[164:167], v[36:39]\n" \
"v_pk_add_f16 v161, v161, v181\n" \
"v_pk_add_f16 v162, v162, v182\n" \
"v_pk_add_f16 v163, v163, v183\n" \
"buffer_store_dwordx4 v[220:223], v235, s[36:39], s66 offen sc1\n" \
"v_mfma_f32_16x16x32_f16 v[24:27], v[120:123], v[164:167], v[24:27]\n" \
"v_pk_add_f16 v164, v164, v184\n" \
"v_pk_add_f16 v139, v123, v131\n" \
"s_add_i32 m0, s44, 0x4000\n" \
"s_mov_b32 s80, 0x3400000\n" \
"v_mfma_f32_16x16x32_f16 v[48:51], v[120:123], v[168:171], v[48:51]\n" \
"v_pk_add_f16 v165, v165, v185\n" \
"v_pk_add_f16 v140, v124, v132\n" \
"buffer_load_dwordx4 v232, s[36:39], s80 offen sc1 lds\n" \
"v_mfma_f32_16x16x32_f16 v[60:63], v[124:127], v[168:171], v[60:63]\n" \
"v_pk_add_f16 v166, v166, v186\n" \
"v_pk_add_f16 v167, v167, v187\n" \
"ds_read_b128 v[156:159], v240 offset:43520\n" \
"v_mfma_f32_16x16x32_f16 v[84:87], v[124:127], v[172:175], v[84:87]\n" \
"v_pk_add_f16 v168, v168, v144\n" \
"v_pk_add_f16 v141, v125, v133\n" \
"v_mfma_f32_16x16x32_f16 v[72:75], v[120:123], v[172:175], v[72:75]\n" \
"v_pk_add_f16 v169, v169, v145\n" \
"v_pk_add_f16 v142, v126, v134\n" \
"v_mfma_f32_16x16x32_f16 v[96:99], v[120:123], v[176:179], v[96:99]\n" \
"v_pk_add_f16 v170, v170, v146\n" \
"v_pk_add_f16 v171, v171, v147\n" \
"ds_read_b64_tr_b16 v[188:189], v236 offset:20480\n" \
"v_mfma_f32_16x16x32_f16 v[108:111], v[124:127], v[176:179], v[108:111]\n" \
"v_pk_add_f16 v172, v172, v148\n" \
"v_pk_add_f16 v143, v127, v135\n" \
"ds_read_b64_tr_b16 v[192:193], v237 offset:20480\n" \
"v_mfma_f32_16x16x32_f16 v[4:7], v[128:131], v[180:183], v[4:7]\n" \
"v_pk_add_f16 v173, v173, v149\n" \
"ds_read_b64_tr_b16 v[120:121], v241 offset:32768\n" \
"buffer_load_dwordx4 v232, s[36:39], s80 offen offset:1024 sc1 lds\n" \
"v_mfma_f32_16x16x32_f16 v[16:19], v[132:135], v[180:183], v[16:19]\n" \
"v_pk_add_f16 v174, v174, v150\n" \
"ds_read_b128 v[208:211], v240 offset:41472\n" \
"v_mfma_f32_16x16x32_f16 v[40:43], v[132:135], v[184:187], v[40:43]\n" \
"v_pk_add_f16 v175, v175, v151\n" \
"ds_read_b128 v[180:183], v240 offset:40960\n" \
"v_mfma_f32_16x16x32_f16 v[28:31], v[128:131], v[184:187], v[28:31]\n" \
"v_pk_add_f16 v176, v176, v152\n" \
"ds_read_b64_tr_b16 v[122:123], v241 offset:40960\n" \
"v_mfma_f32_16x16x32_f16 v[52:55], v[128:131], v[144:147], v[52:55]\n" \
"v_pk_add_f16 v177, v177, v153\n" \
"ds_read_b128 v[184:187], v240 offset:43008\n" \
"s_waitcnt lgkmcnt(6)\n" \
"v_mfma_f32_16x16x16_bf16 v[188:191], v[188:189], v[254:255], 0\n" \
"v_mfma_f32_16x16x32_f16 v[64:67], v[132:135], v[144:147], v[64:67]\n" \
"v_pk_add_f16 v178, v178, v154\n" \
"ds_read_b64_tr_b16 v[124:125], v242 offset:32768\n" \
"s_waitcnt lgkmcnt(6)\n" \
"v_mfma_f32_16x16x16_bf16 v[192:195], v[192:193], v[254:255], 0\n" \
"v_mfma_f32_16x16x32_f16 v[88:91], v[132:135], v[148:151], v[88:91]\n" \
"v_pk_add_f16 v179, v179, v155\n" \
"ds_read_b128 v[144:147], v240 offset:45056\n" \
"buffer_load_dwordx4 v232, s[36:39], s80 offen offset:2048 sc1 lds\n" \
"v_mfma_f32_16x16x32_f16 v[76:79], v[128:131], v[148:151], v[76:79]\n" \
"ds_read_b64_tr_b16 v[126:127], v242 offset:40960\n" \
"v_mfma_f32_16x16x32_f16 v[100:103], v[128:131], v[152:155], v[100:103]\n" \
"v_cvt_pk_f16_f32 v188, v188, v189\n" \
"ds_read_b128 v[148:151], v240 offset:47104\n" \
"v_mfma_f32_16x16x32_f16 v[112:115], v[132:135], v[152:155], v[112:115]\n" \
"v_cvt_pk_f16_f32 v189, v190, v191\n" \
"ds_read_b64_tr_b16 v[128:129], v241 offset:33280\n" \
"v_mfma_f32_16x16x32_f16 v[8:11], v[136:139], v[160:163], v[8:11]\n" \
"v_cvt_pk_f16_f32 v190, v192, v193\n" \
"ds_read_b128 v[152:155], v240 offset:49152\n" \
"v_mfma_f32_16x16x32_f16 v[20:23], v[140:143], v[160:163], v[20:23]\n" \
"v_cvt_pk_f16_f32 v191, v194, v195\n" \
"v_add_u32 v188, s91, v188\n" \
"ds_read_b128 v[160:163], v240 offset:45568\n" \
"v_mfma_f32_16x16x32_f16 v[44:47], v[140:143], v[164:167], v[44:47]\n" \
"ds_read_b64_tr_b16 v[130:131], v241 offset:41472\n" \
"ds_read_b64_tr_b16 v[132:133], v242 offset:33280\n" \
"buffer_load_dwordx4 v232, s[36:39], s80 offen offset:3072 sc1 lds\n" \
"v_mfma_f32_16x16x32_f16 v[32:35], v[136:139], v[164:167], v[32:35]\n" \
"v_add_u32 v189, s91, v189\n" \
"v_add_u32 v190, s91, v190\n" \
"ds_read_b128 v[164:167], v240 offset:47616\n" \
"v_mfma_f32_16x16x32_f16 v[56:59], v[136:139], v[168:171], v[56:59]\n" \
"v_add_u32 v191, s91, v191\n" \
"ds_read_b64_tr_b16 v[134:135], v242 offset:41472\n" \
"v_mfma_f32_16x16x32_f16 v[68:71], v[140:143], v[168:171], v[68:71]\n" \
"v_and_b32 v188, s92, v188\n" \
"ds_read_b128 v[168:171], v240 offset:49664\n" \
"v_mfma_f32_16x16x32_f16 v[92:95], v[140:143], v[172:175], v[92:95]\n" \
"v_and_b32 v189, s92, v189\n" \
"s_add_i32 s82, s50, 0x5000000\n" \
"v_mfma_f32_16x16x32_f16 v[80:83], v[136:139], v[172:175], v[80:83]\n" \
"v_and_b32 v190, s92, v190\n" \
"v_and_b32 v191, s92, v191\n" \
"v_mfma_f32_16x16x32_f16 v[104:107], v[136:139], v[176:179], v[104:107]\n" \
"buffer_store_dwordx4 v[188:191], v234, s[36:39], s82 offen sc1\n" \
"v_mfma_f32_16x16x32_f16 v[116:119], v[140:143], v[176:179], v[116:119]\n" \
"s_waitcnt vmcnt(6) lgkmcnt(0)\n" \
"s_barrier\n" \
"v_mfma_f32_16x16x32_f16 v[0:3], v[120:123], v[180:183], v[0:3]\n" \
"v_pk_add_f16 v136, v120, v128\n" \
"v_pk_add_f16 v137, v121, v129\n" \
"v_mfma_f32_16x16x32_f16 v[12:15], v[124:127], v[180:183], v[12:15]\n" \
"v_pk_add_f16 v180, v180, v208\n" \
"v_pk_add_f16 v138, v122, v130\n" \
"v_mfma_f32_16x16x32_f16 v[36:39], v[124:127], v[184:187], v[36:39]\n" \
"v_pk_add_f16 v181, v181, v209\n" \
"v_pk_add_f16 v182, v182, v210\n" \
"v_pk_add_f16 v183, v183, v211\n" \
"v_cmp_eq_u32 vcc, 0, v253\n" \
"s_cbranch_vccnz .Ls05_%=\n" \
".Lr05_%=:\n" \
"v_mfma_f32_16x16x32_f16 v[24:27], v[120:123], v[184:187], v[24:27]\n" \
"v_pk_add_f16 v184, v184, v156\n" \
"v_pk_add_f16 v139, v123, v131\n" \
"s_add_i32 m0, s44, 0x8000\n" \
"s_mov_b32 s80, 0x3800000\n" \
"v_mfma_f32_16x16x32_f16 v[48:51], v[120:123], v[144:147], v[48:51]\n" \
"v_pk_add_f16 v185, v185, v157\n" \
"v_pk_add_f16 v140, v124, v132\n" \
"buffer_load_dwordx4 v232, s[36:39], s80 offen sc1 lds\n" \
"v_mfma_f32_16x16x32_f16 v[60:63], v[124:127], v[144:147], v[60:63]\n" \
"v_pk_add_f16 v186, v186, v158\n" \
"v_pk_add_f16 v187, v187, v159\n" \
"ds_read_b128 v[176:179], v240 offset:2560\n" \
"v_mfma_f32_16x16x32_f16 v[84:87], v[124:127], v[148:151], v[84:87]\n" \
"v_pk_add_f16 v144, v144, v160\n" \
"v_pk_add_f16 v141, v125, v133\n" \
"s_add_i32 s84, s53, 0x800e000\n" \
"v_mfma_f32_16x16x32_f16 v[72:75], v[120:123], v[148:151], v[72:75]\n" \
"v_pk_add_f16 v145, v145, v161\n" \
"v_pk_add_f16 v142, v126, v134\n" \
"buffer_store_dword v240, v243, s[36:39], s84 offen sc1\n" \
"v_mfma_f32_16x16x32_f16 v[96:99], v[120:123], v[152:155], v[96:99]\n" \
"v_pk_add_f16 v146, v146, v162\n" \
"v_pk_add_f16 v147, v147, v163\n" \
"ds_read_b64_tr_b16 v[188:189], v238 offset:20480\n" \
"v_mfma_f32_16x16x32_f16 v[108:111], v[124:127], v[152:155], v[108:111]\n" \
"v_pk_add_f16 v148, v148, v164\n" \
"v_pk_add_f16 v143, v127, v135\n" \
"ds_read_b64_tr_b16 v[192:193], v239 offset:20480\n" \
"v_mfma_f32_16x16x32_f16 v[4:7], v[128:131], v[208:211], v[4:7]\n" \
"v_pk_add_f16 v149, v149, v165\n" \
"ds_read_b64_tr_b16 v[120:121], v241 offset:0\n" \
"buffer_load_dwordx4 v232, s[36:39], s80 offen offset:1024 sc1 lds\n" \
"v_mfma_f32_16x16x32_f16 v[16:19], v[132:135], v[208:211], v[16:19]\n" \
"v_pk_add_f16 v150, v150, v166\n" \
"ds_read_b128 v[172:175], v240 offset:512\n" \
"v_mfma_f32_16x16x32_f16 v[40:43], v[132:135], v[156:159], v[40:43]\n" \
"v_pk_add_f16 v151, v151, v167\n" \
"ds_read_b128 v[208:211], v240 offset:0\n" \
"v_mfma_f32_16x16x32_f16 v[28:31], v[128:131], v[156:159], v[28:31]\n" \
"v_pk_add_f16 v152, v152, v168\n" \
"ds_read_b64_tr_b16 v[122:123], v241 offset:8192\n" \
"v_mfma_f32_16x16x32_f16 v[52:55], v[128:131], v[160:163], v[52:55]\n" \
"v_pk_add_f16 v153, v153, v169\n" \
"ds_read_b128 v[156:159], v240 offset:2048\n" \
"s_waitcnt lgkmcnt(6)\n" \
"v_mfma_f32_16x16x16_bf16 v[188:191], v[188:189], v[254:255], 0\n" \
"v_mfma_f32_16x16x32_f16 v[64:67], v[132:135], v[160:163], v[64:67]\n" \
"v_pk_add_f16 v154, v154, v170\n" \
"ds_read_b64_tr_b16 v[124:125], v242 offset:0\n" \
"s_waitcnt lgkmcnt(6)\n" \
"v_mfma_f32_16x16x16_bf16 v[192:195], v[192:193], v[254:255], 0\n" \
"v_mfma_f32_16x16x32_f16 v[88:91], v[132:135], v[164:167], v[88:91]\n" \
"v_pk_add_f16 v155, v155, v171\n" \
"ds_read_b128 v[160:163], v240 offset:4096\n" \
"buffer_load_dwordx4 v232, s[36:39], s80 offen offset:2048 sc1 lds\n" \
"v_mfma_f32_16x16x32_f16 v[76:79], v[128:131], v[164:167], v[76:79]\n" \
"ds_read_b64_tr_b16 v[126:127], v242 offset:8192\n" \
"v_mfma_f32_16x16x32_f16 v[100:103], v[128:131], v[168:171], v[100:103]\n" \
"v_cvt_pk_f16_f32 v188, v188, v189\n" \
"ds_read_b128 v[164:167], v240 offset:6144\n" \
"v_mfma_f32_16x16x32_f16 v[112:115], v[132:135], v[168:171], v[112:115]\n" \
"v_cvt_pk_f16_f32 v189, v190, v191\n" \
"ds_read_b64_tr_b16 v[128:129], v241 offset:512\n" \
"v_mfma_f32_16x16x32_f16 v[8:11], v[136:139], v[180:183], v[8:11]\n" \
"v_cvt_pk_f16_f32 v190, v192, v193\n" \
"ds_read_b128 v[168:171], v240 offset:8192\n" \
"v_mfma_f32_16x16x32_f16 v[20:23], v[140:143], v[180:183], v[20:23]\n" \
"v_cvt_pk_f16_f32 v191, v194, v195\n" \
"v_add_u32 v188, s91, v188\n" \
"ds_read_b128 v[180:183], v240 offset:4608\n" \
"v_mfma_f32_16x16x32_f16 v[44:47], v[140:143], v[184:187], v[44:47]\n" \
"ds_read_b64_tr_b16 v[130:131], v241 offset:8704\n" \
"ds_read_b64_tr_b16 v[132:133], v242 offset:512\n" \
"buffer_load_dwordx4 v232, s[36:39], s80 offen offset:3072 sc1 lds\n" \
"v_mfma_f32_16x16x32_f16 v[32:35], v[136:139], v[184:187], v[32:35]\n" \
"v_add_u32 v189, s91, v189\n" \
"v_add_u32 v190, s91, v190\n" \
"ds_read_b128 v[184:187], v240 offset:6656\n" \
"v_mfma_f32_16x16x32_f16 v[56:59], v[136:139], v[144:147], v[56:59]\n" \
"v_add_u32 v191, s91, v191\n" \
"ds_read_b64_tr_b16 v[134:135], v242 offset:8704\n" \
"v_mfma_f32_16x16x32_f16 v[68:71], v[140:143], v[144:147], v[68:71]\n" \
"v_and_b32 v188, s92, v188\n" \
"ds_read_b128 v[144:147], v240 offset:8704\n" \
"v_mfma_f32_16x16x32_f16 v[92:95], v[140:143], v[148:151], v[92:95]\n" \
"v_and_b32 v189, s92, v189\n" \
"v_mfma_f32_16x16x32_f16 v[80:83], v[136:139], v[148:151], v[80:83]\n" \
"v_and_b32 v190, s92, v190\n" \
"v_and_b32 v191, s92, v191\n" \
"v_mfma_f32_16x16x32_f16 v[104:107], v[136:139], v[152:155], v[104:107]\n" \
"buffer_store_dwordx4 v[188:191], v234, s[36:39], s82 offen offset:1024 sc1\n" \
"v_mfma_f32_16x16x32_f16 v[116:119], v[140:143], v[152:155], v[116:119]\n" \
"s_waitcnt vmcnt(7) lgkmcnt(0)\n" \
"s_barrier\n" \
"v_mfma_f32_16x16x32_f16 v[0:3], v[120:123], v[208:211], v[0:3]\n" \
"v_pk_add_f16 v136, v120, v128\n" \
"v_pk_add_f16 v137, v121, v129\n" \
"v_mfma_f32_16x16x32_f16 v[12:15], v[124:127], v[208:211], v[12:15]\n" \
"v_pk_add_f16 v208, v208, v172\n" \
"v_pk_add_f16 v138, v122, v130\n" \
"v_mfma_f32_16x16x32_f16 v[36:39], v[124:127], v[156:159], v[36:39]\n" \
"v_pk_add_f16 v209, v209, v173\n" \
"v_pk_add_f16 v210, v210, v174\n" \
"v_pk_add_f16 v211, v211, v175\n" \
"buffer_store_dwordx4 v[224:227], v235, s[36:39], s67 offen sc1\n" \
"v_mfma_f32_16x16x32_f16 v[24:27], v[120:123], v[156:159], v[24:27]\n" \
"v_pk_add_f16 v156, v156, v176\n" \
"v_pk_add_f16 v139, v123, v131\n" \
"s_add_i32 m0, s44, 0x0\n" \
"s_mov_b32 s80, 0x3c00000\n" \
"v_mfma_f32_16x16x32_f16 v[48:51], v[120:123], v[160:163], v[48:51]\n" \
"v_pk_add_f16 v157, v157, v177\n" \
"v_pk_add_f16 v140, v124, v132\n" \
"buffer_load_dwordx4 v232, s[36:39], s80 offen sc1 lds\n" \
"v_mfma_f32_16x16x32_f16 v[60:63], v[124:127], v[160:163], v[60:63]\n" \
"v_pk_add_f16 v158, v158, v178\n" \
"v_pk_add_f16 v159, v159, v179\n" \
"ds_read_b128 v[152:155], v240 offset:23040\n" \
"v_mfma_f32_16x16x32_f16 v[84:87], v[124:127], v[164:167], v[84:87]\n" \
"v_pk_add_f16 v160, v160, v180\n" \
"v_pk_add_f16 v141, v125, v133\n" \
"v_mfma_f32_16x16x32_f16 v[72:75], v[120:123], v[164:167], v[72:75]\n" \
"v_pk_add_f16 v161, v161, v181\n" \
"v_pk_add_f16 v142, v126, v134\n" \
"v_mfma_f32_16x16x32_f16 v[96:99], v[120:123], v[168:171], v[96:99]\n" \
"v_pk_add_f16 v162, v162, v182\n" \
"v_pk_add_f16 v163, v163, v183\n" \
"ds_read_b64_tr_b16 v[188:189], v236 offset:24576\n" \
"v_mfma_f32_16x16x32_f16 v[108:111], v[124:127], v[168:171], v[108:111]\n" \
"v_pk_add_f16 v164, v164, v184\n" \
"v_pk_add_f16 v143, v127, v135\n" \
"ds_read_b64_tr_b16 v[192:193], v237 offset:24576\n" \
"v_mfma_f32_16x16x32_f16 v[4:7], v[128:131], v[172:175], v[4:7]\n" \
"v_pk_add_f16 v165, v165, v185\n" \
"ds_read_b64_tr_b16 v[120:121], v241 offset:16384\n" \
"buffer_load_dwordx4 v232, s[36:39], s80 offen offset:1024 sc1 lds\n" \
"v_mfma_f32_16x16x32_f16 v[16:19], v[132:135], v[172:175], v[16:19]\n" \
"v_pk_add_f16 v166, v166, v186\n" \
"ds_read_b128 v[148:151], v240 offset:20992\n" \
"v_mfma_f32_16x16x32_f16 v[40:43], v[132:135], v[176:179], v[40:43]\n" \
"v_pk_add_f16 v167, v167, v187\n" \
"ds_read_b128 v[172:175], v240 offset:20480\n" \
"v_mfma_f32_16x16x32_f16 v[28:31], v[128:131], v[176:179], v[28:31]\n" \
"v_pk_add_f16 v168, v168, v144\n" \
"ds_read_b64_tr_b16 v[122:123], v241 offset:24576\n" \
"v_mfma_f32_16x16x32_f16 v[52:55], v[128:131], v[180:183], v[52:55]\n" \
"v_pk_add_f16 v169, v169, v145\n" \
"ds_read_b128 v[176:179], v240 offset:22528\n" \
"s_waitcnt lgkmcnt(6)\n" \
"v_mfma_f32_16x16x16_bf16 v[188:191], v[188:189], v[254:255], 0\n" \
"v_mfma_f32_16x16x32_f16 v[64:67], v[132:135], v[180:183], v[64:67]\n" \
"v_pk_add_f16 v170, v170, v146\n" \
"ds_read_b64_tr_b16 v[124:125], v242 offset:16384\n" \
"s_waitcnt lgkmcnt(6)\n" \
"v_mfma_f32_16x16x16_bf16 v[192:195], v[192:193], v[254:255], 0\n" \
"v_mfma_f32_16x16x32_f16 v[88:91], v[132:135], v[184:187], v[88:91]\n" \
"v_pk_add_f16 v171, v171, v147\n" \
"ds_read_b128 v[180:183], v240 offset:24576\n" \
"buffer_load_dwordx4 v232, s[36:39], s80 offen offset:2048 sc1 lds\n" \
"v_mfma_f32_16x16x32_f16 v[76:79], v[128:131], v[184:187], v[76:79]\n" \
"ds_read_b64_tr_b16 v[126:127], v242 offset:24576\n" \
"v_mfma_f32_16x16x32_f16 v[100:103], v[128:131], v[144:147], v[100:103]\n" \
"v_cvt_pk_f16_f32 v188, v188, v189\n" \
"ds_read_b128 v[184:187], v240 offset:26624\n" \
"v_mfma_f32_16x16x32_f16 v[112:115], v[132:135], v[144:147], v[112:115]\n" \
"v_cvt_pk_f16_f32 v189, v190, v191\n" \
"ds_read_b64_tr_b16 v[128:129], v241 offset:16896\n" \
"v_mfma_f32_16x16x32_f16 v[8:11], v[136:139], v[208:211], v[8:11]\n" \
"v_cvt_pk_f16_f32 v190, v192, v193\n" \
"ds_read_b128 v[144:147], v240 offset:28672\n" \
"v_mfma_f32_16x16x32_f16 v[20:23], v[140:143], v[208:211], v[20:23]\n" \
"v_cvt_pk_f16_f32 v191, v194, v195\n" \
"v_add_u32 v188, s91, v188\n" \
"ds_read_b128 v[208:211], v240 offset:25088\n" \
"v_mfma_f32_16x16x32_f16 v[44:47], v[140:143], v[156:159], v[44:47]\n" \
"ds_read_b64_tr_b16 v[130:131], v241 offset:25088\n" \
"ds_read_b64_tr_b16 v[132:133], v242 offset:16896\n" \
"buffer_load_dwordx4 v232, s[36:39], s80 offen offset:3072 sc1 lds\n" \
"v_mfma_f32_16x16x32_f16 v[32:35], v[136:139], v[156:159], v[32:35]\n" \
"v_add_u32 v189, s91, v189\n" \
"v_add_u32 v190, s91, v190\n" \
"ds_read_b128 v[156:159], v240 offset:27136\n" \
"v_mfma_f32_16x16x32_f16 v[56:59], v[136:139], v[160:163], v[56:59]\n" \
"v_add_u32 v191, s91, v191\n" \
"ds_read_b64_tr_b16 v[134:135], v242 offset:25088\n" \
"v_mfma_f32_16x16x32_f16 v[68:71], v[140:143], v[160:163], v[68:71]\n" \
"v_and_b32 v188, s92, v188\n" \
"ds_read_b128 v[160:163], v240 offset:29184\n" \
"v_mfma_f32_16x16x32_f16 v[92:95], v[140:143], v[164:167], v[92:95]\n" \
"v_and_b32 v189, s92, v189\n" \
"s_add_i32 s82, s51, 0x5800000\n" \
"v_mfma_f32_16x16x32_f16 v[80:83], v[136:139], v[164:167], v[80:83]\n" \
"v_and_b32 v190, s92, v190\n" \
"v_and_b32 v191, s92, v191\n" \
"v_mfma_f32_16x16x32_f16 v[104:107], v[136:139], v[168:171], v[104:107]\n" \
"buffer_store_dwordx4 v[188:191], v234, s[36:39], s82 offen sc1\n" \
"v_mfma_f32_16x16x32_f16 v[116:119], v[140:143], v[168:171], v[116:119]\n" \
"s_waitcnt vmcnt(6) lgkmcnt(0)\n" \
"s_barrier\n" \
"v_mfma_f32_16x16x32_f16 v[0:3], v[120:123], v[172:175], v[0:3]\n" \
"v_pk_add_f16 v136, v120, v128\n" \
"v_pk_add_f16 v137, v121, v129\n" \
"v_mfma_f32_16x16x32_f16 v[12:15], v[124:127], v[172:175], v[12:15]\n" \
"v_pk_add_f16 v172, v172, v148\n" \
"v_pk_add_f16 v138, v122, v130\n" \
"v_mfma_f32_16x16x32_f16 v[36:39], v[124:127], v[176:179], v[36:39]\n" \
"v_pk_add_f16 v173, v173, v149\n" \
"v_pk_add_f16 v174, v174, v150\n" \
"v_pk_add_f16 v175, v175, v151\n" \
"v_mfma_f32_16x16x32_f16 v[24:27], v[120:123], v[176:179], v[24:27]\n" \
"v_pk_add_f16 v176, v176, v152\n" \
"v_pk_add_f16 v139, v123, v131\n" \
"s_add_i32 m0, s44, 0x4000\n" \
"s_mov_b32 s80, 0x4000000\n" \
"v_mfma_f32_16x16x32_f16 v[48:51], v[120:123], v[180:183], v[48:51]\n" \
"v_pk_add_f16 v177, v177, v153\n" \
"v_pk_add_f16 v140, v124, v132\n" \
"buffer_load_dwordx4 v232, s[36:39], s80 offen sc1 lds\n" \
"v_mfma_f32_16x16x32_f16 v[60:63], v[124:127], v[180:183], v[60:63]\n" \
"v_pk_add_f16 v178, v178, v154\n" \
"v_pk_add_f16 v179, v179, v155\n" \
"ds_read_b128 v[168:171], v240 offset:43520\n" \
"v_mfma_f32_16x16x32_f16 v[84:87], v[124:127], v[184:187], v[84:87]\n" \
"v_pk_add_f16 v180, v180, v208\n" \
"v_pk_add_f16 v141, v125, v133\n" \
"s_add_i32 s84, s54, 0x8010000\n" \
"v_mfma_f32_16x16x32_f16 v[72:75], v[120:123], v[184:187], v[72:75]\n" \
"v_pk_add_f16 v181, v181, v209\n" \
"v_pk_add_f16 v142, v126, v134\n" \
"buffer_store_dword v240, v243, s[36:39], s84 offen sc1\n" \
"v_mfma_f32_16x16x32_f16 v[96:99], v[120:123], v[144:147], v[96:99]\n" \
"v_pk_add_f16 v182, v182, v210\n" \
"v_pk_add_f16 v183, v183, v211\n" \
"ds_read_b64_tr_b16 v[188:189], v238 offset:24576\n" \
"v_mfma_f32_16x16x32_f16 v[108:111], v[124:127], v[144:147], v[108:111]\n" \
"v_pk_add_f16 v184, v184, v156\n" \
"v_pk_add_f16 v143, v127, v135\n" \
"ds_read_b64_tr_b16 v[192:193], v239 offset:24576\n" \
"v_mfma_f32_16x16x32_f16 v[4:7], v[128:131], v[148:151], v[4:7]\n" \
"v_pk_add_f16 v185, v185, v157\n" \
"ds_read_b64_tr_b16 v[120:121], v241 offset:32768\n" \
"buffer_load_dwordx4 v232, s[36:39], s80 offen offset:1024 sc1 lds\n" \
"v_mfma_f32_16x16x32_f16 v[16:19], v[132:135], v[148:151], v[16:19]\n" \
"v_pk_add_f16 v186, v186, v158\n" \
"ds_read_b128 v[164:167], v240 offset:41472\n" \
"v_mfma_f32_16x16x32_f16 v[40:43], v[132:135], v[152:155], v[40:43]\n" \
"v_pk_add_f16 v187, v187, v159\n" \
"ds_read_b128 v[148:151], v240 offset:40960\n" \
"v_mfma_f32_16x16x32_f16 v[28:31], v[128:131], v[152:155], v[28:31]\n" \
"v_pk_add_f16 v144, v144, v160\n" \
"ds_read_b64_tr_b16 v[122:123], v241 offset:40960\n" \
"v_mfma_f32_16x16x32_f16 v[52:55], v[128:131], v[208:211], v[52:55]\n" \
"v_pk_add_f16 v145, v145, v161\n" \
"ds_read_b128 v[152:155], v240 offset:43008\n" \
"s_waitcnt lgkmcnt(6)\n" \
"v_mfma_f32_16x16x16_bf16 v[188:191], v[188:189], v[254:255], 0\n" \
"v_mfma_f32_16x16x32_f16 v[64:67], v[132:135], v[208:211], v[64:67]\n" \
"v_pk_add_f16 v146, v146, v162\n" \
"ds_read_b64_tr_b16 v[124:125], v242 offset:32768\n" \
"s_waitcnt lgkmcnt(6)\n" \
"v_mfma_f32_16x16x16_bf16 v[192:195], v[192:193], v[254:255], 0\n" \
"v_mfma_f32_16x16x32_f16 v[88:91], v[132:135], v[156:159], v[88:91]\n" \
"v_pk_add_f16 v147, v147, v163\n" \
"ds_read_b128 v[208:211], v240 offset:45056\n" \
"buffer_load_dwordx4 v232, s[36:39], s80 offen offset:2048 sc1 lds\n" \
"v_mfma_f32_16x16x32_f16 v[76:79], v[128:131], v[156:159], v[76:79]\n" \
"ds_read_b64_tr_b16 v[126:127], v242 offset:40960\n" \
"v_mfma_f32_16x16x32_f16 v[100:103], v[128:131], v[160:163], v[100:103]\n" \
"v_cvt_pk_f16_f32 v188, v188, v189\n" \
"ds_read_b128 v[156:159], v240 offset:47104\n" \
"v_mfma_f32_16x16x32_f16 v[112:115], v[132:135], v[160:163], v[112:115]\n" \
"v_cvt_pk_f16_f32 v189, v190, v191\n" \
"ds_read_b64_tr_b16 v[128:129], v241 offset:33280\n" \
"v_mfma_f32_16x16x32_f16 v[8:11], v[136:139], v[172:175], v[8:11]\n" \
"v_cvt_pk_f16_f32 v190, v192, v193\n" \
"ds_read_b128 v[160:163], v240 offset:49152\n" \
"v_mfma_f32_16x16x32_f16 v[20:23], v[140:143], v[172:175], v[20:23]\n" \
"v_cvt_pk_f16_f32 v191, v194, v195\n" \
"v_add_u32 v188, s91, v188\n" \
"ds_read_b128 v[172:175], v240 offset:45568\n" \
"v_mfma_f32_16x16x32_f16 v[44:47], v[140:143], v[176:179], v[44:47]\n" \
"ds_read_b64_tr_b16 v[130:131], v241 offset:41472\n" \
"ds_read_b64_tr_b16 v[132:133], v242 offset:33280\n" \
"buffer_load_dwordx4 v232, s[36:39], s80 offen offset:3072 sc1 lds\n" \
"v_mfma_f32_16x16x32_f16 v[32:35], v[136:139], v[176:179], v[32:35]\n" \
"v_add_u32 v189, s91, v189\n" \
"v_add_u32 v190, s91, v190\n" \
"ds_read_b128 v[176:179], v240 offset:47616\n" \
"v_mfma_f32_16x16x32_f16 v[56:59], v[136:139], v[180:183], v[56:59]\n" \
"v_add_u32 v191, s91, v191\n" \
"ds_read_b64_tr_b16 v[134:135], v242 offset:41472\n" \
"v_mfma_f32_16x16x32_f16 v[68:71], v[140:143], v[180:183], v[68:71]\n" \
"v_and_b32 v188, s92, v188\n" \
"ds_read_b128 v[180:183], v240 offset:49664\n" \
"v_mfma_f32_16x16x32_f16 v[92:95], v[140:143], v[184:187], v[92:95]\n" \
"v_and_b32 v189, s92, v189\n" \
"v_mfma_f32_16x16x32_f16 v[80:83], v[136:139], v[184:187], v[80:83]\n" \
"v_and_b32 v190, s92, v190\n" \
"v_and_b32 v191, s92, v191\n" \
"v_mfma_f32_16x16x32_f16 v[104:107], v[136:139], v[144:147], v[104:107]\n" \
"buffer_store_dwordx4 v[188:191], v234, s[36:39], s82 offen offset:1024 sc1\n" \
"v_mfma_f32_16x16x32_f16 v[116:119], v[140:143], v[144:147], v[116:119]\n" \
"s_waitcnt vmcnt(7) lgkmcnt(0)\n" \
"s_barrier\n" \
"v_mfma_f32_16x16x32_f16 v[0:3], v[120:123], v[148:151], v[0:3]\n" \
"v_pk_add_f16 v136, v120, v128\n" \
"v_pk_add_f16 v137, v121, v129\n" \
"s_mov_b32 s81, 0x800e000\n" \
"v_mfma_f32_16x16x32_f16 v[12:15], v[124:127], v[148:151], v[12:15]\n" \
"v_pk_add_f16 v148, v148, v164\n" \
"v_pk_add_f16 v138, v122, v130\n" \
"buffer_load_dword v253, v252, s[36:39], s81 offen sc1\n" \
"v_mfma_f32_16x16x32_f16 v[36:39], v[124:127], v[152:155], v[36:39]\n" \
"v_pk_add_f16 v149, v149, v165\n" \
"v_pk_add_f16 v150, v150, v166\n" \
"v_pk_add_f16 v151, v151, v167\n" \
"buffer_store_dwordx4 v[228:231], v235, s[36:39], s68 offen sc1\n" \
"v_mfma_f32_16x16x32_f16 v[24:27], v[120:123], v[152:155], v[24:27]\n" \
"v_pk_add_f16 v152, v152, v168\n" \
"v_pk_add_f16 v139, v123, v131\n" \
"s_add_i32 m0, s44, 0x8000\n" \
"s_mov_b32 s80, 0x4400000\n" \
"v_mfma_f32_16x16x32_f16 v[48:51], v[120:123], v[208:211], v[48:51]\n" \
"v_pk_add_f16 v153, v153, v169\n" \
"v_pk_add_f16 v140, v124, v132\n" \
"buffer_load_dwordx4 v232, s[36:39], s80 offen sc1 lds\n" \
"v_mfma_f32_16x16x32_f16 v[60:63], v[124:127], v[208:211], v[60:63]\n" \
"v_pk_add_f16 v154, v154, v170\n" \
"v_pk_add_f16 v155, v155, v171\n" \
"ds_read_b128 v[144:147], v240 offset:2560\n" \
"v_mfma_f32_16x16x32_f16 v[84:87], v[124:127], v[156:159], v[84:87]\n" \
"v_pk_add_f16 v208, v208, v172\n" \
"v_pk_add_f16 v141, v125, v133\n" \
"v_mfma_f32_16x16x32_f16 v[72:75], v[120:123], v[156:159], v[72:75]\n" \
"v_pk_add_f16 v209, v209, v173\n" \
"v_pk_add_f16 v142, v126, v134\n" \
"v_mfma_f32_16x16x32_f16 v[96:99], v[120:123], v[160:163], v[96:99]\n" \
"v_pk_add_f16 v210, v210, v174\n" \
"v_pk_add_f16 v211, v211, v175\n" \
"ds_read_b64_tr_b16 v[188:189], v236 offset:28672\n" \
"v_mfma_f32_16x16x32_f16 v[108:111], v[124:127], v[160:163], v[108:111]\n" \
"v_pk_add_f16 v156, v156, v176\n" \
"v_pk_add_f16 v143, v127, v135\n" \
"ds_read_b64_tr_b16 v[192:193], v237 offset:28672\n" \
"v_mfma_f32_16x16x32_f16 v[4:7], v[128:131], v[164:167], v[4:7]\n" \
"v_pk_add_f16 v157, v157, v177\n" \
"ds_read_b64_tr_b16 v[120:121], v241 offset:0\n" \
"buffer_load_dwordx4 v232, s[36:39], s80 offen offset:1024 sc1 lds\n" \
"v_mfma_f32_16x16x32_f16 v[16:19], v[132:135], v[164:167], v[16:19]\n" \
"v_pk_add_f16 v158, v158, v178\n" \
"ds_read_b128 v[184:187], v240 offset:512\n" \
"v_mfma_f32_16x16x32_f16 v[40:43], v[132:135], v[168:171], v[40:43]\n" \
"v_pk_add_f16 v159, v159, v179\n" \
"ds_read_b128 v[164:167], v240 offset:0\n" \
"v_mfma_f32_16x16x32_f16 v[28:31], v[128:131], v[168:171], v[28:31]\n" \
"v_pk_add_f16 v160, v160, v180\n" \
"ds_read_b64_tr_b16 v[122:123], v241 offset:8192\n" \
"v_mfma_f32_16x16x32_f16 v[52:55], v[128:131], v[172:175], v[52:55]\n" \
"v_pk_add_f16 v161, v161, v181\n" \
"ds_read_b128 v[168:171], v240 offset:2048\n" \
"s_waitcnt lgkmcnt(6)\n" \
"v_mfma_f32_16x16x16_bf16 v[188:191], v[188:189], v[254:255], 0\n" \
"v_mfma_f32_16x16x32_f16 v[64:67], v[132:135], v[172:175], v[64:67]\n" \
"v_pk_add_f16 v162, v162, v182\n" \
"ds_read_b64_tr_b16 v[124:125], v242 offset:0\n" \
"s_waitcnt lgkmcnt(6)\n" \
"v_mfma_f32_16x16x16_bf16 v[192:195], v[192:193], v[254:255], 0\n" \
"v_mfma_f32_16x16x32_f16 v[88:91], v[132:135], v[176:179], v[88:91]\n" \
"v_pk_add_f16 v163, v163, v183\n" \
"ds_read_b128 v[172:175], v240 offset:4096\n" \
"buffer_load_dwordx4 v232, s[36:39], s80 offen offset:2048 sc1 lds\n" \
"v_mfma_f32_16x16x32_f16 v[76:79], v[128:131], v[176:179], v[76:79]\n" \
"ds_read_b64_tr_b16 v[126:127], v242 offset:8192\n" \
"v_mfma_f32_16x16x32_f16 v[100:103], v[128:131], v[180:183], v[100:103]\n" \
"v_cvt_pk_f16_f32 v188, v188, v189\n" \
"ds_read_b128 v[176:179], v240 offset:6144\n" \
"v_mfma_f32_16x16x32_f16 v[112:115], v[132:135], v[180:183], v[112:115]\n" \
"v_cvt_pk_f16_f32 v189, v190, v191\n" \
"ds_read_b64_tr_b16 v[128:129], v241 offset:512\n" \
"v_mfma_f32_16x16x32_f16 v[8:11], v[136:139], v[148:151], v[8:11]\n" \
"v_cvt_pk_f16_f32 v190, v192, v193\n" \
"ds_read_b128 v[180:183], v240 offset:8192\n" \
"v_mfma_f32_16x16x32_f16 v[20:23], v[140:143], v[148:151], v[20:23]\n" \
"v_cvt_pk_f16_f32 v191, v194, v195\n" \
"v_add_u32 v188, s91, v188\n" \
"ds_read_b128 v[148:151], v240 offset:4608\n" \
"v_mfma_f32_16x16x32_f16 v[44:47], v[140:143], v[152:155], v[44:47]\n" \
"ds_read_b64_tr_b16 v[130:131], v241 offset:8704\n" \
"ds_read_b64_tr_b16 v[132:133], v242 offset:512\n" \
"buffer_load_dwordx4 v232, s[36:39], s80 offen offset:3072 sc1 lds\n" \
"v_mfma_f32_16x16x32_f16 v[32:35], v[136:139], v[152:155], v[32:35]\n" \
"v_add_u32 v189, s91, v189\n" \
"v_add_u32 v190, s91, v190\n" \
"ds_read_b128 v[152:155], v240 offset:6656\n" \
"v_mfma_f32_16x16x32_f16 v[56:59], v[136:139], v[208:211], v[56:59]\n" \
"v_add_u32 v191, s91, v191\n" \
"ds_read_b64_tr_b16 v[134:135], v242 offset:8704\n" \
"v_mfma_f32_16x16x32_f16 v[68:71], v[140:143], v[208:211], v[68:71]\n" \
"v_and_b32 v188, s92, v188\n" \
"ds_read_b128 v[208:211], v240 offset:8704\n" \
"v_mfma_f32_16x16x32_f16 v[92:95], v[140:143], v[156:159], v[92:95]\n" \
"v_and_b32 v189, s92, v189\n" \
"s_add_i32 s82, s48, 0x6000000\n" \
"v_mfma_f32_16x16x32_f16 v[80:83], v[136:139], v[156:159], v[80:83]\n" \
"v_and_b32 v190, s92, v190\n" \
"v_and_b32 v191, s92, v191\n" \
"v_mfma_f32_16x16x32_f16 v[104:107], v[136:139], v[160:163], v[104:107]\n" \
"buffer_store_dwordx4 v[188:191], v234, s[36:39], s82 offen sc1\n" \
"v_mfma_f32_16x16x32_f16 v[116:119], v[140:143], v[160:163], v[116:119]\n" \
"s_waitcnt vmcnt(6) lgkmcnt(0)\n" \
"s_barrier\n" \
"v_mfma_f32_16x16x32_f16 v[0:3], v[120:123], v[164:167], v[0:3]\n" \
"v_pk_add_f16 v136, v120, v128\n" \
"v_pk_add_f16 v137, v121, v129\n" \
"v_mfma_f32_16x16x32_f16 v[12:15], v[124:127], v[164:167], v[12:15]\n" \
"v_pk_add_f16 v164, v164, v184\n" \
"v_pk_add_f16 v138, v122, v130\n" \
"v_mfma_f32_16x16x32_f16 v[36:39], v[124:127], v[168:171], v[36:39]\n" \
"v_pk_add_f16 v165, v165, v185\n" \
"v_pk_add_f16 v166, v166, v186\n" \
"v_pk_add_f16 v167, v167, v187\n" \
"v_cmp_eq_u32 vcc, 0, v253\n" \
"s_cbranch_vccnz .Ls07_%=\n" \
".Lr07_%=:\n" \
"v_mfma_f32_16x16x32_f16 v[24:27], v[120:123], v[168:171], v[24:27]\n" \
"v_pk_add_f16 v168, v168, v144\n" \
"v_pk_add_f16 v139, v123, v131\n" \
"s_add_i32 m0, s44, 0x0\n" \
"s_mov_b32 s80, 0x4800000\n" \
"v_mfma_f32_16x16x32_f16 v[48:51], v[120:123], v[172:175], v[48:51]\n" \
"v_pk_add_f16 v169, v169, v145\n" \
"v_pk_add_f16 v140, v124, v132\n" \
"buffer_load_dwordx4 v232, s[36:39], s80 offen sc1 lds\n" \
"v_mfma_f32_16x16x32_f16 v[60:63], v[124:127], v[172:175], v[60:63]\n" \
"v_pk_add_f16 v170, v170, v146\n" \
"v_pk_add_f16 v171, v171, v147\n" \
"ds_read_b128 v[160:163], v240 offset:23040\n" \
"v_mfma_f32_16x16x32_f16 v[84:87], v[124:127], v[176:179], v[84:87]\n" \
"v_pk_add_f16 v172, v172, v148\n" \
"v_pk_add_f16 v141, v125, v133\n" \
"s_add_i32 s84, s55, 0x8012000\n" \
"v_mfma_f32_16x16x32_f16 v[72:75], v[120:123], v[176:179], v[72:75]\n" \
"v_pk_add_f16 v173, v173, v149\n" \
"v_pk_add_f16 v142, v126, v134\n" \
"buffer_store_dword v240, v243, s[36:39], s84 offen sc1\n" \
"v_mfma_f32_16x16x32_f16 v[96:99], v[120:123], v[180:183], v[96:99]\n" \
"v_pk_add_f16 v174, v174, v150\n" \
"v_pk_add_f16 v175, v175, v151\n" \
"ds_read_b64_tr_b16 v[188:189], v238 offset:28672\n" \
"v_mfma_f32_16x16x32_f16 v[108:111], v[124:127], v[180:183], v[108:111]\n" \
"v_pk_add_f16 v176, v176, v152\n" \
"v_pk_add_f16 v143, v127, v135\n" \
"ds_read_b64_tr_b16 v[192:193], v239 offset:28672\n" \
"v_mfma_f32_16x16x32_f16 v[4:7], v[128:131], v[184:187], v[4:7]\n" \
"v_pk_add_f16 v177, v177, v153\n" \
"ds_read_b64_tr_b16 v[120:121], v241 offset:16384\n" \
"buffer_load_dwordx4 v232, s[36:39], s80 offen offset:1024 sc1 lds\n" \
"v_mfma_f32_16x16x32_f16 v[16:19], v[132:135], v[184:187], v[16:19]\n" \
"v_pk_add_f16 v178, v178, v154\n" \
"ds_read_b128 v[156:159], v240 offset:20992\n" \
"v_mfma_f32_16x16x32_f16 v[40:43], v[132:135], v[144:147], v[40:43]\n" \
"v_pk_add_f16 v179, v179, v155\n" \
"ds_read_b128 v[184:187], v240 offset:20480\n" \
"v_mfma_f32_16x16x32_f16 v[28:31], v[128:131], v[144:147], v[28:31]\n" \
"v_pk_add_f16 v180, v180, v208\n" \
"ds_read_b64_tr_b16 v[122:123], v241 offset:24576\n" \
"v_mfma_f32_16x16x32_f16 v[52:55], v[128:131], v[148:151], v[52:55]\n" \
"v_pk_add_f16 v181, v181, v209\n" \
"ds_read_b128 v[144:147], v240 offset:22528\n" \
"s_waitcnt lgkmcnt(6)\n" \
"v_mfma_f32_16x16x16_bf16 v[188:191], v[188:189], v[254:255], 0\n" \
"v_mfma_f32_16x16x32_f16 v[64:67], v[132:135], v[148:151], v[64:67]\n" \
"v_pk_add_f16 v182, v182, v210\n" \
"ds_read_b64_tr_b16 v[124:125], v242 offset:16384\n" \
"s_waitcnt lgkmcnt(6)\n" \
"v_mfma_f32_16x16x16_bf16 v[192:195], v[192:193], v[254:255], 0\n" \
"v_mfma_f32_16x16x32_f16 v[88:91], v[132:135], v[152:155], v[88:91]\n" \
"v_pk_add_f16 v183, v183, v211\n" \
"ds_read_b128 v[148:151], v240 offset:24576\n" \
"buffer_load_dwordx4 v232, s[36:39], s80 offen offset:2048 sc1 lds\n" \
"v_mfma_f32_16x16x32_f16 v[76:79], v[128:131], v[152:155], v[76:79]\n" \
"ds_read_b64_tr_b16 v[126:127], v242 offset:24576\n" \
"v_mfma_f32_16x16x32_f16 v[100:103], v[128:131], v[208:211], v[100:103]\n" \
"v_cvt_pk_f16_f32 v188, v188, v189\n" \
"ds_read_b128 v[152:155], v240 offset:26624\n" \
"v_mfma_f32_16x16x32_f16 v[112:115], v[132:135], v[208:211], v[112:115]\n" \
"v_cvt_pk_f16_f32 v189, v190, v191\n" \
"ds_read_b64_tr_b16 v[128:129], v241 offset:16896\n" \
"v_mfma_f32_16x16x32_f16 v[8:11], v[136:139], v[164:167], v[8:11]\n" \
"v_cvt_pk_f16_f32 v190, v192, v193\n" \
"ds_read_b128 v[208:211], v240 offset:28672\n" \
"v_mfma_f32_16x16x32_f16 v[20:23], v[140:143], v[164:167], v[20:23]\n" \
"v_cvt_pk_f16_f32 v191, v194, v195\n" \
"v_add_u32 v188, s91, v188\n" \
"ds_read_b128 v[164:167], v240 offset:25088\n" \
"v_mfma_f32_16x16x32_f16 v[44:47], v[140:143], v[168:171], v[44:47]\n" \
"ds_read_b64_tr_b16 v[130:131], v241 offset:25088\n" \
"ds_read_b64_tr_b16 v[132:133], v242 offset:16896\n" \
"buffer_load_dwordx4 v232, s[36:39], s80 offen offset:3072 sc1 lds\n" \
"v_mfma_f32_16x16x32_f16 v[32:35], v[136:139], v[168:171], v[32:35]\n" \
"v_add_u32 v189, s91, v189\n" \
"v_add_u32 v190, s91, v190\n" \
"ds_read_b128 v[168:171], v240 offset:27136\n" \
"v_mfma_f32_16x16x32_f16 v[56:59], v[136:139], v[172:175], v[56:59]\n" \
"v_add_u32 v191, s91, v191\n" \
"ds_read_b64_tr_b16 v[134:135], v242 offset:25088\n" \
"v_mfma_f32_16x16x32_f16 v[68:71], v[140:143], v[172:175], v[68:71]\n" \
"v_and_b32 v188, s92, v188\n" \
"ds_read_b128 v[172:175], v240 offset:29184\n" \
"v_mfma_f32_16x16x32_f16 v[92:95], v[140:143], v[176:179], v[92:95]\n" \
"v_and_b32 v189, s92, v189\n" \
"v_mfma_f32_16x16x32_f16 v[80:83], v[136:139], v[176:179], v[80:83]\n" \
"v_and_b32 v190, s92, v190\n" \
"v_and_b32 v191, s92, v191\n" \
"v_mfma_f32_16x16x32_f16 v[104:107], v[136:139], v[180:183], v[104:107]\n" \
"buffer_store_dwordx4 v[188:191], v234, s[36:39], s82 offen offset:1024 sc1\n" \
"v_mfma_f32_16x16x32_f16 v[116:119], v[140:143], v[180:183], v[116:119]\n" \
"s_waitcnt vmcnt(7) lgkmcnt(0)\n" \
"s_barrier\n" \
"v_mfma_f32_16x16x32_f16 v[0:3], v[120:123], v[184:187], v[0:3]\n" \
"v_pk_add_f16 v136, v120, v128\n" \
"v_pk_add_f16 v137, v121, v129\n" \
"s_mov_b32 s81, 0x8010000\n" \
"s_add_i32 m0, s45, 0x0\n" \
"s_mov_b32 s83, 0x0\n" \
"v_mfma_f32_16x16x32_f16 v[12:15], v[124:127], v[184:187], v[12:15]\n" \
"v_pk_add_f16 v184, v184, v156\n" \
"v_pk_add_f16 v138, v122, v130\n" \
"buffer_load_dword v253, v252, s[36:39], s81 offen sc1\n" \
"buffer_load_dwordx4 v233, s[36:39], s83 offen sc1 lds\n" \
"v_mfma_f32_16x16x32_f16 v[36:39], v[124:127], v[144:147], v[36:39]\n" \
"v_pk_add_f16 v185, v185, v157\n" \
"v_pk_add_f16 v186, v186, v158\n" \
"v_pk_add_f16 v187, v187, v159\n" \
"v_mfma_f32_16x16x32_f16 v[24:27], v[120:123], v[144:147], v[24:27]\n" \
"v_pk_add_f16 v144, v144, v160\n" \
"v_pk_add_f16 v139, v123, v131\n" \
"s_add_i32 m0, s44, 0x4000\n" \
"s_mov_b32 s80, 0x4c00000\n" \
"v_mfma_f32_16x16x32_f16 v[48:51], v[120:123], v[148:151], v[48:51]\n" \
"v_pk_add_f16 v145, v145, v161\n" \
"v_pk_add_f16 v140, v124, v132\n" \
"buffer_load_dwordx4 v232, s[36:39], s80 offen sc1 lds\n" \
"v_mfma_f32_16x16x32_f16 v[60:63], v[124:127], v[148:151], v[60:63]\n" \
"v_pk_add_f16 v146, v146, v162\n" \
"v_pk_add_f16 v147, v147, v163\n" \
"ds_read_b128 v[180:183], v240 offset:43520\n" \
"v_mfma_f32_16x16x32_f16 v[84:87], v[124:127], v[152:155], v[84:87]\n" \
"v_pk_add_f16 v148, v148, v164\n" \
"v_pk_add_f16 v141, v125, v133\n" \
"v_mfma_f32_16x16x32_f16 v[72:75], v[120:123], v[152:155], v[72:75]\n" \
"v_pk_add_f16 v149, v149, v165\n" \
"v_pk_add_f16 v142, v126, v134\n" \
"v_mfma_f32_16x16x32_f16 v[96:99], v[120:123], v[208:211], v[96:99]\n" \
"v_pk_add_f16 v150, v150, v166\n" \
"v_pk_add_f16 v151, v151, v167\n" \
"ds_read_b64_tr_b16 v[188:189], v236 offset:32768\n" \
"v_mfma_f32_16x16x32_f16 v[108:111], v[124:127], v[208:211], v[108:111]\n" \
"v_pk_add_f16 v152, v152, v168\n" \
"v_pk_add_f16 v143, v127, v135\n" \
"ds_read_b64_tr_b16 v[192:193], v237 offset:32768\n" \
"v_mfma_f32_16x16x32_f16 v[4:7], v[128:131], v[156:159], v[4:7]\n" \
"v_pk_add_f16 v153, v153, v169\n" \
"ds_read_b64_tr_b16 v[120:121], v241 offset:32768\n" \
"buffer_load_dwordx4 v232, s[36:39], s80 offen offset:1024 sc1 lds\n" \
"v_mfma_f32_16x16x32_f16 v[16:19], v[132:135], v[156:159], v[16:19]\n" \
"v_pk_add_f16 v154, v154, v170\n" \
"ds_read_b128 v[176:179], v240 offset:41472\n" \
"v_mfma_f32_16x16x32_f16 v[40:43], v[132:135], v[160:163], v[40:43]\n" \
"v_pk_add_f16 v155, v155, v171\n" \
"ds_read_b128 v[156:159], v240 offset:40960\n" \
"v_mfma_f32_16x16x32_f16 v[28:31], v[128:131], v[160:163], v[28:31]\n" \
"v_pk_add_f16 v208, v208, v172\n" \
"ds_read_b64_tr_b16 v[122:123], v241 offset:40960\n" \
"v_mfma_f32_16x16x32_f16 v[52:55], v[128:131], v[164:167], v[52:55]\n" \
"v_pk_add_f16 v209, v209, v173\n" \
"ds_read_b128 v[160:163], v240 offset:43008\n" \
"s_waitcnt lgkmcnt(6)\n" \
"v_mfma_f32_16x16x16_bf16 v[188:191], v[188:189], v[254:255], 0\n" \
"v_mfma_f32_16x16x32_f16 v[64:67], v[132:135], v[164:167], v[64:67]\n" \
"v_pk_add_f16 v210, v210, v174\n" \
"ds_read_b64_tr_b16 v[124:125], v242 offset:32768\n" \
"s_waitcnt lgkmcnt(6)\n" \
"v_mfma_f32_16x16x16_bf16 v[192:195], v[192:193], v[254:255], 0\n" \
"v_mfma_f32_16x16x32_f16 v[88:91], v[132:135], v[168:171], v[88:91]\n" \
"v_pk_add_f16 v211, v211, v175\n" \
"ds_read_b128 v[164:167], v240 offset:45056\n" \
"buffer_load_dwordx4 v232, s[36:39], s80 offen offset:2048 sc1 lds\n" \
"v_mfma_f32_16x16x32_f16 v[76:79], v[128:131], v[168:171], v[76:79]\n" \
"ds_read_b64_tr_b16 v[126:127], v242 offset:40960\n" \
"v_mfma_f32_16x16x32_f16 v[100:103], v[128:131], v[172:175], v[100:103]\n" \
"v_cvt_pk_f16_f32 v188, v188, v189\n" \
"ds_read_b128 v[168:171], v240 offset:47104\n" \
"v_mfma_f32_16x16x32_f16 v[112:115], v[132:135], v[172:175], v[112:115]\n" \
"v_cvt_pk_f16_f32 v189, v190, v191\n" \
"ds_read_b64_tr_b16 v[128:129], v241 offset:33280\n" \
"v_mfma_f32_16x16x32_f16 v[8:11], v[136:139], v[184:187], v[8:11]\n" \
"v_cvt_pk_f16_f32 v190, v192, v193\n" \
"ds_read_b128 v[172:175], v240 offset:49152\n" \
"v_mfma_f32_16x16x32_f16 v[20:23], v[140:143], v[184:187], v[20:23]\n" \
"v_cvt_pk_f16_f32 v191, v194, v195\n" \
"v_add_u32 v188, s91, v188\n" \
"ds_read_b128 v[184:187], v240 offset:45568\n" \
"v_mfma_f32_16x16x32_f16 v[44:47], v[140:143], v[144:147], v[44:47]\n" \
"ds_read_b64_tr_b16 v[130:131], v241 offset:41472\n" \
"ds_read_b64_tr_b16 v[132:133], v242 offset:33280\n" \
"buffer_load_dwordx4 v232, s[36:39], s80 offen offset:3072 sc1 lds\n" \
"v_mfma_f32_16x16x32_f16 v[32:35], v[136:139], v[144:147], v[32:35]\n" \
"v_add_u32 v189, s91, v189\n" \
"v_add_u32 v190, s91, v190\n" \
"ds_read_b128 v[144:147], v240 offset:47616\n" \
"v_mfma_f32_16x16x32_f16 v[56:59], v[136:139], v[148:151], v[56:59]\n" \
"v_add_u32 v191, s91, v191\n" \
"ds_read_b64_tr_b16 v[134:135], v242 offset:41472\n" \
"s_add_i32 m0, s45, 0x1000\n" \
"s_mov_b32 s83, 0x8000\n" \
"v_mfma_f32_16x16x32_f16 v[68:71], v[140:143], v[148:151], v[68:71]\n" \
"v_and_b32 v188, s92, v188\n" \
"ds_read_b128 v[148:151], v240 offset:49664\n" \
"buffer_load_dwordx4 v233, s[36:39], s83 offen sc1 lds\n" \
"v_mfma_f32_16x16x32_f16 v[92:95], v[140:143], v[152:155], v[92:95]\n" \
"v_and_b32 v189, s92, v189\n" \
"s_add_i32 s82, s49, 0x6800000\n" \
"v_mfma_f32_16x16x32_f16 v[80:83], v[136:139], v[152:155], v[80:83]\n" \
"v_and_b32 v190, s92, v190\n" \
"v_and_b32 v191, s92, v191\n" \
"v_mfma_f32_16x16x32_f16 v[104:107], v[136:139], v[208:211], v[104:107]\n" \
"buffer_store_dwordx4 v[188:191], v234, s[36:39], s82 offen sc1\n" \
"v_mfma_f32_16x16x32_f16 v[116:119], v[140:143], v[208:211], v[116:119]\n" \
"s_waitcnt vmcnt(7) lgkmcnt(0)\n" \
"s_barrier\n" \
"v_mfma_f32_16x16x32_f16 v[0:3], v[120:123], v[156:159], v[0:3]\n" \
"v_pk_add_f16 v136, v120, v128\n" \
"v_pk_add_f16 v137, v121, v129\n" \
"s_add_i32 m0, s45, 0x2000\n" \
"s_mov_b32 s83, 0x10000\n" \
"v_mfma_f32_16x16x32_f16 v[12:15], v[124:127], v[156:159], v[12:15]\n" \
"v_pk_add_f16 v156, v156, v176\n" \
"v_pk_add_f16 v138, v122, v130\n" \
"buffer_load_dwordx4 v233, s[36:39], s83 offen sc1 lds\n" \
"v_mfma_f32_16x16x32_f16 v[36:39], v[124:127], v[160:163], v[36:39]\n" \
"v_pk_add_f16 v157, v157, v177\n" \
"v_pk_add_f16 v158, v158, v178\n" \
"v_pk_add_f16 v159, v159, v179\n" \
"v_cmp_eq_u32 vcc, 0, v253\n" \
"s_cbranch_vccnz .Ls08_%=\n" \
".Lr08_%=:\n" \
"v_mfma_f32_16x16x32_f16 v[24:27], v[120:123], v[160:163], v[24:27]\n" \
"v_pk_add_f16 v160, v160, v180\n" \
"v_pk_add_f16 v139, v123, v131\n" \
"s_add_i32 m0, s44, 0x8000\n" \
"s_mov_b32 s80, 0x5000000\n" \
"v_mfma_f32_16x16x32_f16 v[48:51], v[120:123], v[164:167], v[48:51]\n" \
"v_pk_add_f16 v161, v161, v181\n" \
"v_pk_add_f16 v140, v124, v132\n" \
"buffer_load_dwordx4 v232, s[36:39], s80 offen sc1 lds\n" \
"v_mfma_f32_16x16x32_f16 v[60:63], v[124:127], v[164:167], v[60:63]\n" \
"v_pk_add_f16 v162, v162, v182\n" \
"v_pk_add_f16 v163, v163, v183\n" \
"ds_read_b128 v[208:211], v240 offset:2560\n" \
"v_mfma_f32_16x16x32_f16 v[84:87], v[124:127], v[168:171], v[84:87]\n" \
"v_pk_add_f16 v164, v164, v184\n" \
"v_pk_add_f16 v141, v125, v133\n" \
"s_add_i32 s84, s52, 0x8014000\n" \
"v_mfma_f32_16x16x32_f16 v[72:75], v[120:123], v[168:171], v[72:75]\n" \
"v_pk_add_f16 v165, v165, v185\n" \
"v_pk_add_f16 v142, v126, v134\n" \
"buffer_store_dword v240, v243, s[36:39], s84 offen sc1\n" \
"v_mfma_f32_16x16x32_f16 v[96:99], v[120:123], v[172:175], v[96:99]\n" \
"v_pk_add_f16 v166, v166, v186\n" \
"v_pk_add_f16 v167, v167, v187\n" \
"ds_read_b64_tr_b16 v[188:189], v238 offset:32768\n" \
"v_mfma_f32_16x16x32_f16 v[108:111], v[124:127], v[172:175], v[108:111]\n" \
"v_pk_add_f16 v168, v168, v144\n" \
"v_pk_add_f16 v143, v127, v135\n" \
"ds_read_b64_tr_b16 v[192:193], v239 offset:32768\n" \
"v_mfma_f32_16x16x32_f16 v[4:7], v[128:131], v[176:179], v[4:7]\n" \
"v_pk_add_f16 v169, v169, v145\n" \
"ds_read_b64_tr_b16 v[120:121], v241 offset:0\n" \
"buffer_load_dwordx4 v232, s[36:39], s80 offen offset:1024 sc1 lds\n" \
"v_mfma_f32_16x16x32_f16 v[16:19], v[132:135], v[176:179], v[16:19]\n" \
"v_pk_add_f16 v170, v170, v146\n" \
"ds_read_b128 v[152:155], v240 offset:512\n" \
"v_mfma_f32_16x16x32_f16 v[40:43], v[132:135], v[180:183], v[40:43]\n" \
"v_pk_add_f16 v171, v171, v147\n" \
"ds_read_b128 v[176:179], v240 offset:0\n" \
"v_mfma_f32_16x16x32_f16 v[28:31], v[128:131], v[180:183], v[28:31]\n" \
"v_pk_add_f16 v172, v172, v148\n" \
"ds_read_b64_tr_b16 v[122:123], v241 offset:8192\n" \
"v_mfma_f32_16x16x32_f16 v[52:55], v[128:131], v[184:187], v[52:55]\n" \
"v_pk_add_f16 v173, v173, v149\n" \
"ds_read_b128 v[180:183], v240 offset:2048\n" \
"s_waitcnt lgkmcnt(6)\n" \
"v_mfma_f32_16x16x16_bf16 v[188:191], v[188:189], v[254:255], 0\n" \
"v_mfma_f32_16x16x32_f16 v[64:67], v[132:135], v[184:187], v[64:67]\n" \
"v_pk_add_f16 v174, v174, v150\n" \
"ds_read_b64_tr_b16 v[124:125], v242 offset:0\n" \
"s_waitcnt lgkmcnt(6)\n" \
"v_mfma_f32_16x16x16_bf16 v[192:195], v[192:193], v[254:255], 0\n" \
"v_mfma_f32_16x16x32_f16 v[88:91], v[132:135], v[144:147], v[88:91]\n" \
"v_pk_add_f16 v175, v175, v151\n" \
"ds_read_b128 v[184:187], v240 offset:4096\n" \
"buffer_load_dwordx4 v232, s[36:39], s80 offen offset:2048 sc1 lds\n" \
"v_mfma_f32_16x16x32_f16 v[76:79], v[128:131], v[144:147], v[76:79]\n" \
"ds_read_b64_tr_b16 v[126:127], v242 offset:8192\n" \
"v_mfma_f32_16x16x32_f16 v[100:103], v[128:131], v[148:151], v[100:103]\n" \
"v_cvt_pk_f16_f32 v188, v188, v189\n" \
"ds_read_b128 v[144:147], v240 offset:6144\n" \
"v_mfma_f32_16x16x32_f16 v[112:115], v[132:135], v[148:151], v[112:115]\n" \
"v_cvt_pk_f16_f32 v189, v190, v191\n" \
"ds_read_b64_tr_b16 v[128:129], v241 offset:512\n" \
"v_mfma_f32_16x16x32_f16 v[8:11], v[136:139], v[156:159], v[8:11]\n" \
"v_cvt_pk_f16_f32 v190, v192, v193\n" \
"ds_read_b128 v[148:151], v240 offset:8192\n" \
"v_mfma_f32_16x16x32_f16 v[20:23], v[140:143], v[156:159], v[20:23]\n" \
"v_cvt_pk_f16_f32 v191, v194, v195\n" \
"v_add_u32 v188, s91, v188\n" \
"ds_read_b128 v[156:159], v240 offset:4608\n" \
"v_mfma_f32_16x16x32_f16 v[44:47], v[140:143], v[160:163], v[44:47]\n" \
"ds_read_b64_tr_b16 v[130:131], v241 offset:8704\n" \
"ds_read_b64_tr_b16 v[132:133], v242 offset:512\n" \
"buffer_load_dwordx4 v232, s[36:39], s80 offen offset:3072 sc1 lds\n" \
"v_mfma_f32_16x16x32_f16 v[32:35], v[136:139], v[160:163], v[32:35]\n" \
"v_add_u32 v189, s91, v189\n" \
"v_add_u32 v190, s91, v190\n" \
"ds_read_b128 v[160:163], v240 offset:6656\n" \
"v_mfma_f32_16x16x32_f16 v[56:59], v[136:139], v[164:167], v[56:59]\n" \
"v_add_u32 v191, s91, v191\n" \
"ds_read_b64_tr_b16 v[134:135], v242 offset:8704\n" \
"s_add_i32 m0, s45, 0x3000\n" \
"s_mov_b32 s83, 0x18000\n" \
"v_mfma_f32_16x16x32_f16 v[68:71], v[140:143], v[164:167], v[68:71]\n" \
"v_and_b32 v188, s92, v188\n" \
"ds_read_b128 v[164:167], v240 offset:8704\n" \
"buffer_load_dwordx4 v233, s[36:39], s83 offen sc1 lds\n" \
"v_mfma_f32_16x16x32_f16 v[92:95], v[140:143], v[168:171], v[92:95]\n" \
"v_and_b32 v189, s92, v189\n" \
"v_mfma_f32_16x16x32_f16 v[80:83], v[136:139], v[168:171], v[80:83]\n" \
"v_and_b32 v190, s92, v190\n" \
"v_and_b32 v191, s92, v191\n" \
"v_mfma_f32_16x16x32_f16 v[104:107], v[136:139], v[172:175], v[104:107]\n" \
"buffer_store_dwordx4 v[188:191], v234, s[36:39], s82 offen offset:1024 sc1\n" \
"v_mfma_f32_16x16x32_f16 v[116:119], v[140:143], v[172:175], v[116:119]\n" \
"s_waitcnt vmcnt(9) lgkmcnt(0)\n" \
"s_barrier\n" \
"v_mfma_f32_16x16x32_f16 v[0:3], v[120:123], v[176:179], v[0:3]\n" \
"v_pk_add_f16 v136, v120, v128\n" \
"v_pk_add_f16 v137, v121, v129\n" \
"s_mov_b32 s81, 0x8012000\n" \
"s_add_i32 m0, s45, 0x4000\n" \
"s_mov_b32 s83, 0x20000\n" \
"v_mfma_f32_16x16x32_f16 v[12:15], v[124:127], v[176:179], v[12:15]\n" \
"v_pk_add_f16 v176, v176, v152\n" \
"v_pk_add_f16 v138, v122, v130\n" \
"buffer_load_dword v253, v252, s[36:39], s81 offen sc1\n" \
"buffer_load_dwordx4 v233, s[36:39], s83 offen sc1 lds\n" \
"v_mfma_f32_16x16x32_f16 v[36:39], v[124:127], v[180:183], v[36:39]\n" \
"v_pk_add_f16 v177, v177, v153\n" \
"v_pk_add_f16 v178, v178, v154\n" \
"v_pk_add_f16 v179, v179, v155\n" \
"ds_read_b128 v[196:199], v249 offset:0\n" \
"v_mfma_f32_16x16x32_f16 v[24:27], v[120:123], v[180:183], v[24:27]\n" \
"v_pk_add_f16 v180, v180, v208\n" \
"v_pk_add_f16 v139, v123, v131\n" \
"s_add_i32 m0, s44, 0x0\n" \
"s_mov_b32 s80, 0x5400000\n" \
"v_mfma_f32_16x16x32_f16 v[48:51], v[120:123], v[184:187], v[48:51]\n" \
"v_pk_add_f16 v181, v181, v209\n" \
"v_pk_add_f16 v140, v124, v132\n" \
"buffer_load_dwordx4 v232, s[36:39], s80 offen sc1 lds\n" \
"v_mfma_f32_16x16x32_f16 v[60:63], v[124:127], v[184:187], v[60:63]\n" \
"v_pk_add_f16 v182, v182, v210\n" \
"v_pk_add_f16 v183, v183, v211\n" \
"ds_read_b128 v[172:175], v240 offset:23040\n" \
"ds_read_b64_tr_b16 v[200:201], v247 offset:0\n" \
"v_mfma_f32_16x16x32_f16 v[84:87], v[124:127], v[144:147], v[84:87]\n" \
"v_pk_add_f16 v184, v184, v156\n" \
"v_pk_add_f16 v141, v125, v133\n" \
"ds_read_b64_tr_b16 v[202:203], v247 offset:512\n" \
"v_mfma_f32_16x16x32_f16 v[72:75], v[120:123], v[144:147], v[72:75]\n" \
"v_pk_add_f16 v185, v185, v157\n" \
"v_pk_add_f16 v142, v126, v134\n" \
"v_mfma_f32_16x16x32_f16 v[96:99], v[120:123], v[148:151], v[96:99]\n" \
"v_pk_add_f16 v186, v186, v158\n" \
"v_pk_add_f16 v187, v187, v159\n" \
"ds_read_b64_tr_b16 v[188:189], v236 offset:36864\n" \
"v_mfma_f32_16x16x32_f16 v[108:111], v[124:127], v[148:151], v[108:111]\n" \
"v_pk_add_f16 v144, v144, v160\n" \
"v_pk_add_f16 v143, v127, v135\n" \
"ds_read_b64_tr_b16 v[192:193], v237 offset:36864\n" \
"v_mfma_f32_16x16x32_f16 v[4:7], v[128:131], v[152:155], v[4:7]\n" \
"v_pk_add_f16 v145, v145, v161\n" \
"ds_read_b64_tr_b16 v[120:121], v241 offset:16384\n" \
"buffer_load_dwordx4 v232, s[36:39], s80 offen offset:1024 sc1 lds\n" \
"s_waitcnt lgkmcnt(3)\n" \
"v_mfma_f32_16x16x32_bf16 v[204:207], v[200:203], v[196:199], 0\n" \
"v_mfma_f32_16x16x32_f16 v[16:19], v[132:135], v[152:155], v[16:19]\n" \
"v_pk_add_f16 v146, v146, v162\n" \
"ds_read_b128 v[168:171], v240 offset:20992\n" \
"ds_read_b128 v[196:199], v249 offset:1024\n" \
"v_mfma_f32_16x16x32_f16 v[40:43], v[132:135], v[208:211], v[40:43]\n" \
"v_pk_add_f16 v147, v147, v163\n" \
"ds_read_b128 v[152:155], v240 offset:20480\n" \
"ds_read_b64_tr_b16 v[200:201], v247 offset:4096\n" \
"v_mfma_f32_16x16x32_f16 v[28:31], v[128:131], v[208:211], v[28:31]\n" \
"v_pk_add_f16 v148, v148, v164\n" \
"ds_read_b64_tr_b16 v[122:123], v241 offset:24576\n" \
"v_mfma_f32_16x16x32_f16 v[52:55], v[128:131], v[156:159], v[52:55]\n" \
"v_pk_add_f16 v149, v149, v165\n" \
"ds_read_b128 v[208:211], v240 offset:22528\n" \
"s_waitcnt lgkmcnt(8)\n" \
"v_mfma_f32_16x16x16_bf16 v[188:191], v[188:189], v[254:255], 0\n" \
"v_mfma_f32_16x16x32_f16 v[64:67], v[132:135], v[156:159], v[64:67]\n" \
"v_pk_add_f16 v150, v150, v166\n" \
"ds_read_b64_tr_b16 v[124:125], v242 offset:16384\n" \
"s_waitcnt lgkmcnt(8)\n" \
"v_mfma_f32_16x16x16_bf16 v[192:195], v[192:193], v[254:255], 0\n" \
"v_mfma_f32_16x16x32_f16 v[88:91], v[132:135], v[160:163], v[88:91]\n" \
"v_pk_add_f16 v151, v151, v167\n" \
"ds_read_b128 v[156:159], v240 offset:24576\n" \
"buffer_load_dwordx4 v232, s[36:39], s80 offen offset:2048 sc1 lds\n" \
"v_mfma_f32_16x16x32_f16 v[76:79], v[128:131], v[160:163], v[76:79]\n" \
"ds_read_b64_tr_b16 v[126:127], v242 offset:24576\n" \
"ds_read_b64_tr_b16 v[202:203], v247 offset:4608\n" \
"v_mfma_f32_16x16x32_f16 v[100:103], v[128:131], v[164:167], v[100:103]\n" \
"v_cvt_pk_f16_f32 v188, v188, v189\n" \
"ds_read_b128 v[160:163], v240 offset:26624\n" \
"v_mfma_f32_16x16x32_f16 v[112:115], v[132:135], v[164:167], v[112:115]\n" \
"v_cvt_pk_f16_f32 v189, v190, v191\n" \
"ds_read_b64_tr_b16 v[128:129], v241 offset:16896\n" \
"v_mfma_f32_16x16x32_f16 v[8:11], v[136:139], v[176:179], v[8:11]\n" \
"v_cvt_pk_f16_f32 v190, v192, v193\n" \
"ds_read_b128 v[164:167], v240 offset:28672\n" \
"v_mfma_f32_16x16x32_f16 v[20:23], v[140:143], v[176:179], v[20:23]\n" \
"v_cvt_pk_f16_f32 v191, v194, v195\n" \
"v_add_u32 v188, s91, v188\n" \
"ds_read_b128 v[176:179], v240 offset:25088\n" \
"v_mfma_f32_16x16x32_f16 v[44:47], v[140:143], v[180:183], v[44:47]\n" \
"ds_read_b64_tr_b16 v[130:131], v241 offset:25088\n" \
"ds_read_b64_tr_b16 v[132:133], v242 offset:16896\n" \
"buffer_load_dwordx4 v232, s[36:39], s80 offen offset:3072 sc1 lds\n" \
"v_mfma_f32_16x16x32_f16 v[32:35], v[136:139], v[180:183], v[32:35]\n" \
"v_add_u32 v189, s91, v189\n" \
"v_add_u32 v190, s91, v190\n" \
"ds_read_b128 v[180:183], v240 offset:27136\n" \
"v_mfma_f32_16x16x32_f16 v[56:59], v[136:139], v[184:187], v[56:59]\n" \
"v_add_u32 v191, s91, v191\n" \
"ds_read_b64_tr_b16 v[134:135], v242 offset:25088\n" \
"s_add_i32 m0, s45, 0x5000\n" \
"s_mov_b32 s83, 0x28000\n" \
"s_waitcnt lgkmcnt(7)\n" \
"v_mfma_f32_16x16x32_bf16 v[204:207], v[200:203], v[196:199], v[204:207]\n" \
"v_mfma_f32_16x16x32_f16 v[68:71], v[140:143], v[184:187], v[68:71]\n" \
"v_and_b32 v188, s92, v188\n" \
"ds_read_b128 v[184:187], v240 offset:29184\n" \
"buffer_load_dwordx4 v233, s[36:39], s83 offen sc1 lds\n" \
"v_mfma_f32_16x16x32_f16 v[92:95], v[140:143], v[144:147], v[92:95]\n" \
"v_and_b32 v189, s92, v189\n" \
"s_add_i32 s82, s50, 0x7000000\n" \
"v_mfma_f32_16x16x32_f16 v[80:83], v[136:139], v[144:147], v[80:83]\n" \
"v_and_b32 v190, s92, v190\n" \
"v_and_b32 v191, s92, v191\n" \
"v_mfma_f32_16x16x32_f16 v[104:107], v[136:139], v[148:151], v[104:107]\n" \
"buffer_store_dwordx4 v[188:191], v234, s[36:39], s82 offen sc1\n" \
"v_mfma_f32_16x16x32_f16 v[116:119], v[140:143], v[148:151], v[116:119]\n" \
"s_waitcnt vmcnt(7) lgkmcnt(0)\n" \
"s_barrier\n" \
"v_mfma_f32_16x16x32_f16 v[0:3], v[120:123], v[152:155], v[0:3]\n" \
"v_pk_add_f16 v136, v120, v128\n" \
"v_pk_add_f16 v137, v121, v129\n" \
"s_add_i32 m0, s45, 0x0\n" \
"s_mov_b32 s83, 0x30000\n" \
"v_mfma_f32_16x16x32_f16 v[12:15], v[124:127], v[152:155], v[12:15]\n" \
"v_pk_add_f16 v152, v152, v168\n" \
"v_pk_add_f16 v138, v122, v130\n" \
"buffer_load_dwordx4 v233, s[36:39], s83 offen sc1 lds\n" \
"v_mfma_f32_16x16x32_f16 v[36:39], v[124:127], v[208:211], v[36:39]\n" \
"v_pk_add_f16 v153, v153, v169\n" \
"v_pk_add_f16 v154, v154, v170\n" \
"v_pk_add_f16 v155, v155, v171\n" \
"v_cmp_eq_u32 vcc, 0, v253\n" \
"s_cbranch_vccnz .Ls09_%=\n" \
".Lr09_%=:\n" \
"v_mfma_f32_16x16x32_f16 v[24:27], v[120:123], v[208:211], v[24:27]\n" \
"v_pk_add_f16 v208, v208, v172\n" \
"v_pk_add_f16 v139, v123, v131\n" \
"s_add_i32 m0, s44, 0x4000\n" \
"s_mov_b32 s80, 0x5800000\n" \
"ds_read_b128 v[196:199], v249 offset:2048\n" \
"v_mfma_f32_16x16x32_f16 v[48:51], v[120:123], v[156:159], v[48:51]\n" \
"v_pk_add_f16 v209, v209, v173\n" \
"v_pk_add_f16 v140, v124, v132\n" \
"buffer_load_dwordx4 v232, s[36:39], s80 offen sc1 lds\n" \
"v_mfma_f32_16x16x32_f16 v[60:63], v[124:127], v[156:159], v[60:63]\n" \
"v_pk_add_f16 v210, v210, v174\n" \
"v_pk_add_f16 v211, v211, v175\n" \
"ds_read_b128 v[148:151], v240 offset:43520\n" \
"ds_read_b64_tr_b16 v[200:201], v247 offset:8192\n" \
"v_mfma_f32_16x16x32_f16 v[84:87], v[124:127], v[160:163], v[84:87]\n" \
"v_pk_add_f16 v156, v156, v176\n" \
"v_pk_add_f16 v141, v125, v133\n" \
"s_add_i32 s84, s53, 0x8016000\n" \
"ds_read_b64_tr_b16 v[202:203], v247 offset:8704\n" \
"v_mfma_f32_16x16x32_f16 v[72:75], v[120:123], v[160:163], v[72:75]\n" \
"v_pk_add_f16 v157, v157, v177\n" \
"v_pk_add_f16 v142, v126, v134\n" \
"buffer_store_dword v240, v243, s[36:39], s84 offen sc1\n" \
"v_mfma_f32_16x16x32_f16 v[96:99], v[120:123], v[164:167], v[96:99]\n" \
"v_pk_add_f16 v158, v158, v178\n" \
"v_pk_add_f16 v159, v159, v179\n" \
"ds_read_b64_tr_b16 v[188:189], v238 offset:36864\n" \
"v_mfma_f32_16x16x32_f16 v[108:111], v[124:127], v[164:167], v[108:111]\n" \
"v_pk_add_f16 v160, v160, v180\n" \
"v_pk_add_f16 v143, v127, v135\n" \
"ds_read_b64_tr_b16 v[192:193], v239 offset:36864\n" \
"v_mfma_f32_16x16x32_f16 v[4:7], v[128:131], v[168:171], v[4:7]\n" \
"v_pk_add_f16 v161, v161, v181\n" \
"ds_read_b64_tr_b16 v[120:121], v241 offset:32768\n" \
"buffer_load_dwordx4 v232, s[36:39], s80 offen offset:1024 sc1 lds\n" \
"s_waitcnt lgkmcnt(3)\n" \
"v_mfma_f32_16x16x32_bf16 v[204:207], v[200:203], v[196:199], v[204:207]\n" \
"v_mfma_f32_16x16x32_f16 v[16:19], v[132:135], v[168:171], v[16:19]\n" \
"v_pk_add_f16 v162, v162, v182\n" \
"ds_read_b128 v[144:147], v240 offset:41472\n" \
"ds_read_b128 v[196:199], v249 offset:3072\n" \
"v_mfma_f32_16x16x32_f16 v[40:43], v[132:135], v[172:175], v[40:43]\n" \
"v_pk_add_f16 v163, v163, v183\n" \
"ds_read_b128 v[168:171], v240 offset:40960\n" \
"ds_read_b64_tr_b16 v[200:201], v247 offset:12288\n" \
"v_mfma_f32_16x16x32_f16 v[28:31], v[128:131], v[172:175], v[28:31]\n" \
"v_pk_add_f16 v164, v164, v184\n" \
"ds_read_b64_tr_b16 v[122:123], v241 offset:40960\n" \
"v_mfma_f32_16x16x32_f16 v[52:55], v[128:131], v[176:179], v[52:55]\n" \
"v_pk_add_f16 v165, v165, v185\n" \
"ds_read_b128 v[172:175], v240 offset:43008\n" \
"s_waitcnt lgkmcnt(8)\n" \
"v_mfma_f32_16x16x16_bf16 v[188:191], v[188:189], v[254:255], 0\n" \
"v_mfma_f32_16x16x32_f16 v[64:67], v[132:135], v[176:179], v[64:67]\n" \
"v_pk_add_f16 v166, v166, v186\n" \
"ds_read_b64_tr_b16 v[124:125], v242 offset:32768\n" \
"s_waitcnt lgkmcnt(8)\n" \
"v_mfma_f32_16x16x16_bf16 v[192:195], v[192:193], v[254:255], 0\n" \
"v_mfma_f32_16x16x32_f16 v[88:91], v[132:135], v[180:183], v[88:91]\n" \
"v_pk_add_f16 v167, v167, v187\n" \
"ds_read_b128 v[176:179], v240 offset:45056\n" \
"buffer_load_dwordx4 v232, s[36:39], s80 offen offset:2048 sc1 lds\n" \
"v_mfma_f32_16x16x32_f16 v[76:79], v[128:131], v[180:183], v[76:79]\n" \
"ds_read_b64_tr_b16 v[126:127], v242 offset:40960\n" \
"ds_read_b64_tr_b16 v[202:203], v247 offset:12800\n" \
"v_mfma_f32_16x16x32_f16 v[100:103], v[128:131], v[184:187], v[100:103]\n" \
"v_cvt_pk_f16_f32 v188, v188, v189\n" \
"ds_read_b128 v[180:183], v240 offset:47104\n" \
"v_mfma_f32_16x16x32_f16 v[112:115], v[132:135], v[184:187], v[112:115]\n" \
"v_cvt_pk_f16_f32 v189, v190, v191\n" \
"ds_read_b64_tr_b16 v[128:129], v241 offset:33280\n" \
"v_mfma_f32_16x16x32_f16 v[8:11], v[136:139], v[152:155], v[8:11]\n" \
"v_cvt_pk_f16_f32 v190, v192, v193\n" \
"ds_read_b128 v[184:187], v240 offset:49152\n" \
"v_mfma_f32_16x16x32_f16 v[20:23], v[140:143], v[152:155], v[20:23]\n" \
"v_cvt_pk_f16_f32 v191, v194, v195\n" \
"v_add_u32 v188, s91, v188\n" \
"ds_read_b128 v[152:155], v240 offset:45568\n" \
"v_mfma_f32_16x16x32_f16 v[44:47], v[140:143], v[208:211], v[44:47]\n" \
"ds_read_b64_tr_b16 v[130:131], v241 offset:41472\n" \
"ds_read_b64_tr_b16 v[132:133], v242 offset:33280\n" \
"buffer_load_dwordx4 v232, s[36:39], s80 offen offset:3072 sc1 lds\n" \
"v_mfma_f32_16x16x32_f16 v[32:35], v[136:139], v[208:211], v[32:35]\n" \
"v_add_u32 v189, s91, v189\n" \
"v_add_u32 v190, s91, v190\n" \
"ds_read_b128 v[208:211], v240 offset:47616\n" \
"v_mfma_f32_16x16x32_f16 v[56:59], v[136:139], v[156:159], v[56:59]\n" \
"v_add_u32 v191, s91, v191\n" \
"ds_read_b64_tr_b16 v[134:135], v242 offset:41472\n" \
"s_add_i32 m0, s45, 0x1000\n" \
"s_mov_b32 s83, 0x38000\n" \
"s_waitcnt lgkmcnt(7)\n" \
"v_mfma_f32_16x16x32_bf16 v[204:207], v[200:203], v[196:199], v[204:207]\n" \
"v_mfma_f32_16x16x32_f16 v[68:71], v[140:143], v[156:159], v[68:71]\n" \
"v_and_b32 v188, s92, v188\n" \
"ds_read_b128 v[156:159], v240 offset:49664\n" \
"buffer_load_dwordx4 v233, s[36:39], s83 offen sc1 lds\n" \
"v_mfma_f32_16x16x32_f16 v[92:95], v[140:143], v[160:163], v[92:95]\n" \
"v_and_b32 v189, s92, v189\n" \
"v_mfma_f32_16x16x32_f16 v[80:83], v[136:139], v[160:163], v[80:83]\n" \
"v_and_b32 v190, s92, v190\n" \
"v_and_b32 v191, s92, v191\n" \
"v_mfma_f32_16x16x32_f16 v[104:107], v[136:139], v[164:167], v[104:107]\n" \
"buffer_store_dwordx4 v[188:191], v234, s[36:39], s82 offen offset:1024 sc1\n" \
"v_mfma_f32_16x16x32_f16 v[116:119], v[140:143], v[164:167], v[116:119]\n" \
"s_waitcnt vmcnt(9) lgkmcnt(0)\n" \
"s_barrier\n" \
"v_mfma_f32_16x16x32_f16 v[0:3], v[120:123], v[168:171], v[0:3]\n" \
"v_pk_add_f16 v136, v120, v128\n" \
"v_pk_add_f16 v137, v121, v129\n" \
"ds_read_b64_tr_b16 v[188:189], v236 offset:40960\n" \
"s_add_i32 m0, s45, 0x2000\n" \
"s_mov_b32 s83, 0x40000\n" \
"v_mfma_f32_16x16x32_f16 v[12:15], v[124:127], v[168:171], v[12:15]\n" \
"v_pk_add_f16 v168, v168, v144\n" \
"v_pk_add_f16 v138, v122, v130\n" \
"ds_read_b64_tr_b16 v[192:193], v237 offset:40960\n" \
"buffer_load_dwordx4 v233, s[36:39], s83 offen sc1 lds\n" \
"v_mfma_f32_16x16x32_f16 v[36:39], v[124:127], v[172:175], v[36:39]\n" \
"v_pk_add_f16 v169, v169, v145\n" \
"v_pk_add_f16 v170, v170, v146\n" \
"v_pk_add_f16 v171, v171, v147\n" \
"ds_read_b128 v[196:199], v249 offset:4096\n" \
"v_mfma_f32_16x16x32_f16 v[24:27], v[120:123], v[172:175], v[24:27]\n" \
"v_pk_add_f16 v172, v172, v148\n" \
"v_pk_add_f16 v139, v123, v131\n" \
"s_add_i32 m0, s44, 0x8000\n" \
"s_mov_b32 s80, 0x5c00000\n" \
"v_mfma_f32_16x16x32_f16 v[48:51], v[120:123], v[176:179], v[48:51]\n" \
"v_pk_add_f16 v173, v173, v149\n" \
"v_pk_add_f16 v140, v124, v132\n" \
"buffer_load_dwordx4 v232, s[36:39], s80 offen sc1 lds\n" \
"v_mfma_f32_16x16x32_f16 v[60:63], v[124:127], v[176:179], v[60:63]\n" \
"v_pk_add_f16 v174, v174, v150\n" \
"v_pk_add_f16 v175, v175, v151\n" \
"ds_read_b128 v[164:167], v240 offset:2560\n" \
"ds_read_b64_tr_b16 v[200:201], v247 offset:16384\n" \
"s_waitcnt lgkmcnt(4)\n" \
"v_mfma_f32_16x16x16_bf16 v[188:191], v[188:189], v[254:255], 0\n" \
"v_mfma_f32_16x16x32_f16 v[84:87], v[124:127], v[180:183], v[84:87]\n" \
"v_pk_add_f16 v176, v176, v152\n" \
"v_pk_add_f16 v141, v125, v133\n" \
"ds_read_b64_tr_b16 v[202:203], v247 offset:16896\n" \
"s_waitcnt lgkmcnt(4)\n" \
"v_mfma_f32_16x16x16_bf16 v[192:195], v[192:193], v[254:255], 0\n" \
"v_mfma_f32_16x16x32_f16 v[72:75], v[120:123], v[180:183], v[72:75]\n" \
"v_pk_add_f16 v177, v177, v153\n" \
"v_pk_add_f16 v142, v126, v134\n" \
"v_mfma_f32_16x16x32_f16 v[96:99], v[120:123], v[184:187], v[96:99]\n" \
"v_pk_add_f16 v178, v178, v154\n" \
"v_pk_add_f16 v179, v179, v155\n" \
"v_mfma_f32_16x16x32_f16 v[108:111], v[124:127], v[184:187], v[108:111]\n" \
"v_pk_add_f16 v180, v180, v208\n" \
"v_pk_add_f16 v143, v127, v135\n" \
"v_cvt_pk_f16_f32 v188, v188, v189\n" \
"v_cvt_pk_f16_f32 v189, v190, v191\n" \
"v_mfma_f32_16x16x32_f16 v[4:7], v[128:131], v[144:147], v[4:7]\n" \
"v_pk_add_f16 v181, v181, v209\n" \
"v_cvt_pk_f16_f32 v190, v192, v193\n" \
"ds_read_b64_tr_b16 v[120:121], v241 offset:0\n" \
"buffer_load_dwordx4 v232, s[36:39], s80 offen offset:1024 sc1 lds\n" \
"s_waitcnt lgkmcnt(1)\n" \
"v_mfma_f32_16x16x32_bf16 v[204:207], v[200:203], v[196:199], v[204:207]\n" \
"v_mfma_f32_16x16x32_f16 v[16:19], v[132:135], v[144:147], v[16:19]\n" \
"v_pk_add_f16 v182, v182, v210\n" \
"v_cvt_pk_f16_f32 v191, v194, v195\n" \
"v_add_u32 v188, s91, v188\n" \
"ds_read_b128 v[160:163], v240 offset:512\n" \
"ds_read_b128 v[196:199], v249 offset:5120\n" \
"v_mfma_f32_16x16x32_f16 v[40:43], v[132:135], v[148:151], v[40:43]\n" \
"v_pk_add_f16 v183, v183, v211\n" \
"v_add_u32 v189, s91, v189\n" \
"v_add_u32 v190, s91, v190\n" \
"ds_read_b128 v[144:147], v240 offset:0\n" \
"v_mfma_f32_16x16x32_f16 v[28:31], v[128:131], v[148:151], v[28:31]\n" \
"v_pk_add_f16 v184, v184, v156\n" \
"v_add_u32 v191, s91, v191\n" \
"v_and_b32 v188, s92, v188\n" \
"ds_read_b64_tr_b16 v[122:123], v241 offset:8192\n" \
"s_add_i32 s82, s51, 0x7800000\n" \
"v_mfma_f32_16x16x32_f16 v[52:55], v[128:131], v[152:155], v[52:55]\n" \
"v_pk_add_f16 v185, v185, v157\n" \
"v_and_b32 v189, s92, v189\n" \
"v_and_b32 v190, s92, v190\n" \
"v_and_b32 v191, s92, v191\n" \
"ds_read_b128 v[148:151], v240 offset:2048\n" \
"v_mfma_f32_16x16x32_f16 v[64:67], v[132:135], v[152:155], v[64:67]\n" \
"v_pk_add_f16 v186, v186, v158\n" \
"ds_read_b64_tr_b16 v[124:125], v242 offset:0\n" \
"buffer_store_dwordx4 v[188:191], v234, s[36:39], s82 offen sc1\n" \
"ds_read_b64_tr_b16 v[200:201], v247 offset:20480\n" \
"v_mfma_f32_16x16x32_f16 v[88:91], v[132:135], v[208:211], v[88:91]\n" \
"v_pk_add_f16 v187, v187, v159\n" \
"ds_read_b128 v[152:155], v240 offset:4096\n" \
"buffer_load_dwordx4 v232, s[36:39], s80 offen offset:2048 sc1 lds\n" \
"v_mfma_f32_16x16x32_f16 v[76:79], v[128:131], v[208:211], v[76:79]\n" \
"ds_read_b64_tr_b16 v[126:127], v242 offset:8192\n" \
"ds_read_b64_tr_b16 v[188:189], v238 offset:40960\n" \
"ds_read_b64_tr_b16 v[202:203], v247 offset:20992\n" \
"v_mfma_f32_16x16x32_f16 v[100:103], v[128:131], v[156:159], v[100:103]\n" \
"ds_read_b128 v[208:211], v240 offset:6144\n" \
"ds_read_b64_tr_b16 v[192:193], v239 offset:40960\n" \
"v_mfma_f32_16x16x32_f16 v[112:115], v[132:135], v[156:159], v[112:115]\n" \
"ds_read_b64_tr_b16 v[128:129], v241 offset:512\n" \
"v_mfma_f32_16x16x32_f16 v[8:11], v[136:139], v[168:171], v[8:11]\n" \
"ds_read_b128 v[156:159], v240 offset:8192\n" \
"s_waitcnt lgkmcnt(5)\n" \
"v_mfma_f32_16x16x16_bf16 v[188:191], v[188:189], v[254:255], 0\n" \
"v_mfma_f32_16x16x32_f16 v[20:23], v[140:143], v[168:171], v[20:23]\n" \
"ds_read_b128 v[168:171], v240 offset:4608\n" \
"s_waitcnt lgkmcnt(3)\n" \
"v_mfma_f32_16x16x16_bf16 v[192:195], v[192:193], v[254:255], 0\n" \
"v_mfma_f32_16x16x32_f16 v[44:47], v[140:143], v[172:175], v[44:47]\n" \
"ds_read_b64_tr_b16 v[130:131], v241 offset:8704\n" \
"ds_read_b64_tr_b16 v[132:133], v242 offset:512\n" \
"buffer_load_dwordx4 v232, s[36:39], s80 offen offset:3072 sc1 lds\n" \
"v_mfma_f32_16x16x32_f16 v[32:35], v[136:139], v[172:175], v[32:35]\n" \
"ds_read_b128 v[172:175], v240 offset:6656\n" \
"v_mfma_f32_16x16x32_f16 v[56:59], v[136:139], v[176:179], v[56:59]\n" \
"v_cvt_pk_f16_f32 v188, v188, v189\n" \
"v_cvt_pk_f16_f32 v189, v190, v191\n" \
"ds_read_b64_tr_b16 v[134:135], v242 offset:8704\n" \
"s_add_i32 m0, s45, 0x3000\n" \
"s_mov_b32 s83, 0x48000\n" \
"v_mfma_f32_16x16x32_bf16 v[204:207], v[200:203], v[196:199], v[204:207]\n" \
"v_mfma_f32_16x16x32_f16 v[68:71], v[140:143], v[176:179], v[68:71]\n" \
"v_cvt_pk_f16_f32 v190, v192, v193\n" \
"v_cvt_pk_f16_f32 v191, v194, v195\n" \
"ds_read_b128 v[176:179], v240 offset:8704\n" \
"buffer_load_dwordx4 v233, s[36:39], s83 offen sc1 lds\n" \
"v_mfma_f32_16x16x32_f16 v[92:95], v[140:143], v[180:183], v[92:95]\n" \
"v_add_u32 v188, s91, v188\n" \
"v_add_u32 v189, s91, v189\n" \
"v_add_u32 v190, s91, v190\n" \
"v_mfma_f32_16x16x32_f16 v[80:83], v[136:139], v[180:183], v[80:83]\n" \
"v_add_u32 v191, s91, v191\n" \
"v_and_b32 v188, s92, v188\n" \
"v_mfma_f32_16x16x32_f16 v[104:107], v[136:139], v[184:187], v[104:107]\n" \
"v_and_b32 v189, s92, v189\n" \
"v_and_b32 v190, s92, v190\n" \
"v_and_b32 v191, s92, v191\n" \
"v_mfma_f32_16x16x32_f16 v[116:119], v[140:143], v[184:187], v[116:119]\n" \
"buffer_store_dwordx4 v[188:191], v234, s[36:39], s82 offen offset:1024 sc1\n" \
"s_waitcnt vmcnt(8) lgkmcnt(0)\n" \
"s_barrier\n" \
"v_mfma_f32_16x16x32_f16 v[0:3], v[120:123], v[144:147], v[0:3]\n" \
"v_pk_add_f16 v136, v120, v128\n" \
"v_pk_add_f16 v137, v121, v129\n" \
"s_add_i32 m0, s45, 0x4000\n" \
"s_mov_b32 s83, 0x50000\n" \
"v_mfma_f32_16x16x32_f16 v[12:15], v[124:127], v[144:147], v[12:15]\n" \
"v_pk_add_f16 v144, v144, v160\n" \
"v_pk_add_f16 v138, v122, v130\n" \
"buffer_load_dwordx4 v233, s[36:39], s83 offen sc1 lds\n" \
"v_mfma_f32_16x16x32_f16 v[36:39], v[124:127], v[148:151], v[36:39]\n" \
"v_pk_add_f16 v145, v145, v161\n" \
"v_pk_add_f16 v146, v146, v162\n" \
"v_pk_add_f16 v147, v147, v163\n" \
"ds_read_b128 v[196:199], v249 offset:6144\n" \
"v_mfma_f32_16x16x32_f16 v[24:27], v[120:123], v[148:151], v[24:27]\n" \
"v_pk_add_f16 v148, v148, v164\n" \
"v_pk_add_f16 v139, v123, v131\n" \
"s_add_i32 m0, s44, 0x0\n" \
"s_mov_b32 s80, 0x6000000\n" \
"v_mfma_f32_16x16x32_f16 v[48:51], v[120:123], v[152:155], v[48:51]\n" \
"v_pk_add_f16 v149, v149, v165\n" \
"v_pk_add_f16 v140, v124, v132\n" \
"buffer_load_dwordx4 v232, s[36:39], s80 offen sc1 lds\n" \
"v_mfma_f32_16x16x32_f16 v[60:63], v[124:127], v[152:155], v[60:63]\n" \
"v_pk_add_f16 v150, v150, v166\n" \
"v_pk_add_f16 v151, v151, v167\n" \
"ds_read_b128 v[184:187], v240 offset:23040\n" \
"ds_read_b64_tr_b16 v[200:201], v247 offset:0\n" \
"v_mfma_f32_16x16x32_f16 v[84:87], v[124:127], v[208:211], v[84:87]\n" \
"v_pk_add_f16 v152, v152, v168\n" \
"v_pk_add_f16 v141, v125, v133\n" \
"s_add_i32 s84, s54, 0x8018000\n" \
"ds_read_b64_tr_b16 v[202:203], v247 offset:512\n" \
"v_mfma_f32_16x16x32_f16 v[72:75], v[120:123], v[208:211], v[72:75]\n" \
"v_pk_add_f16 v153, v153, v169\n" \
"v_pk_add_f16 v142, v126, v134\n" \
"buffer_store_dword v240, v243, s[36:39], s84 offen sc1\n" \
"v_mfma_f32_16x16x32_f16 v[96:99], v[120:123], v[156:159], v[96:99]\n" \
"v_pk_add_f16 v154, v154, v170\n" \
"v_pk_add_f16 v155, v155, v171\n" \
"v_mfma_f32_16x16x32_f16 v[108:111], v[124:127], v[156:159], v[108:111]\n" \
"v_pk_add_f16 v208, v208, v172\n" \
"v_pk_add_f16 v143, v127, v135\n" \
"v_mfma_f32_16x16x32_f16 v[4:7], v[128:131], v[160:163], v[4:7]\n" \
"v_pk_add_f16 v209, v209, v173\n" \
"ds_read_b64_tr_b16 v[120:121], v241 offset:16384\n" \
"buffer_load_dwordx4 v232, s[36:39], s80 offen offset:1024 sc1 lds\n" \
"s_waitcnt lgkmcnt(1)\n" \
"v_mfma_f32_16x16x32_bf16 v[204:207], v[200:203], v[196:199], v[204:207]\n" \
"v_mfma_f32_16x16x32_f16 v[16:19], v[132:135], v[160:163], v[16:19]\n" \
"v_pk_add_f16 v210, v210, v174\n" \
"ds_read_b128 v[180:183], v240 offset:20992\n" \
"ds_read_b128 v[196:199], v249 offset:7168\n" \
"v_mfma_f32_16x16x32_f16 v[40:43], v[132:135], v[164:167], v[40:43]\n" \
"v_pk_add_f16 v211, v211, v175\n" \
"ds_read_b128 v[160:163], v240 offset:20480\n" \
"ds_read_b64_tr_b16 v[200:201], v247 offset:4096\n" \
"v_mfma_f32_16x16x32_f16 v[28:31], v[128:131], v[164:167], v[28:31]\n" \
"v_pk_add_f16 v156, v156, v176\n" \
"ds_read_b64_tr_b16 v[122:123], v241 offset:24576\n" \
"v_mfma_f32_16x16x32_f16 v[52:55], v[128:131], v[168:171], v[52:55]\n" \
"v_pk_add_f16 v157, v157, v177\n" \
"ds_read_b128 v[164:167], v240 offset:22528\n" \
"v_mfma_f32_16x16x32_f16 v[64:67], v[132:135], v[168:171], v[64:67]\n" \
"v_pk_add_f16 v158, v158, v178\n" \
"ds_read_b64_tr_b16 v[124:125], v242 offset:16384\n" \
"v_mfma_f32_16x16x32_f16 v[88:91], v[132:135], v[172:175], v[88:91]\n" \
"v_pk_add_f16 v159, v159, v179\n" \
"ds_read_b128 v[168:171], v240 offset:24576\n" \
"buffer_load_dwordx4 v232, s[36:39], s80 offen offset:2048 sc1 lds\n" \
"v_mfma_f32_16x16x32_f16 v[76:79], v[128:131], v[172:175], v[76:79]\n" \
"ds_read_b64_tr_b16 v[126:127], v242 offset:24576\n" \
"ds_read_b64_tr_b16 v[202:203], v247 offset:4608\n" \
"v_mfma_f32_16x16x32_f16 v[100:103], v[128:131], v[176:179], v[100:103]\n" \
"ds_read_b128 v[172:175], v240 offset:26624\n" \
"v_mfma_f32_16x16x32_f16 v[112:115], v[132:135], v[176:179], v[112:115]\n" \
"ds_read_b64_tr_b16 v[128:129], v241 offset:16896\n" \
"v_mfma_f32_16x16x32_f16 v[8:11], v[136:139], v[144:147], v[8:11]\n" \
"ds_read_b128 v[176:179], v240 offset:28672\n" \
"v_mfma_f32_16x16x32_f16 v[20:23], v[140:143], v[144:147], v[20:23]\n" \
"ds_read_b128 v[144:147], v240 offset:25088\n" \
"v_mfma_f32_16x16x32_f16 v[44:47], v[140:143], v[148:151], v[44:47]\n" \
"ds_read_b64_tr_b16 v[130:131], v241 offset:25088\n" \
"ds_read_b64_tr_b16 v[132:133], v242 offset:16896\n" \
"buffer_load_dwordx4 v232, s[36:39], s80 offen offset:3072 sc1 lds\n" \
"v_mfma_f32_16x16x32_f16 v[32:35], v[136:139], v[148:151], v[32:35]\n" \
"ds_read_b128 v[148:151], v240 offset:27136\n" \
"v_mfma_f32_16x16x32_f16 v[56:59], v[136:139], v[152:155], v[56:59]\n" \
"ds_read_b64_tr_b16 v[134:135], v242 offset:25088\n" \
"s_add_i32 m0, s45, 0x5000\n" \
"s_mov_b32 s83, 0x58000\n" \
"s_waitcnt lgkmcnt(8)\n" \
"v_mfma_f32_16x16x32_bf16 v[204:207], v[200:203], v[196:199], v[204:207]\n" \
"v_mfma_f32_16x16x32_f16 v[68:71], v[140:143], v[152:155], v[68:71]\n" \
"ds_read_b128 v[152:155], v240 offset:29184\n" \
"buffer_load_dwordx4 v233, s[36:39], s83 offen sc1 lds\n" \
"v_mfma_f32_16x16x32_f16 v[92:95], v[140:143], v[208:211], v[92:95]\n" \
"v_mfma_f32_16x16x32_f16 v[80:83], v[136:139], v[208:211], v[80:83]\n" \
"v_mfma_f32_16x16x32_f16 v[104:107], v[136:139], v[156:159], v[104:107]\n" \
"v_mfma_f32_16x16x32_f16 v[116:119], v[140:143], v[156:159], v[116:119]\n" \
"s_waitcnt vmcnt(7) lgkmcnt(0)\n" \
"s_barrier\n" \
"v_mfma_f32_16x16x32_f16 v[0:3], v[120:123], v[160:163], v[0:3]\n" \
"v_pk_add_f16 v136, v120, v128\n" \
"v_pk_add_f16 v137, v121, v129\n" \
"s_mov_b32 s81, 0x8016000\n" \
"s_add_i32 m0, s45, 0x0\n" \
"s_mov_b32 s83, 0x60000\n" \
"v_mfma_f32_16x16x32_f16 v[12:15], v[124:127], v[160:163], v[12:15]\n" \
"v_pk_add_f16 v160, v160, v180\n" \
"v_pk_add_f16 v138, v122, v130\n" \
"buffer_load_dword v253, v252, s[36:39], s81 offen sc1\n" \
"buffer_load_dwordx4 v233, s[36:39], s83 offen sc1 lds\n" \
"v_mfma_f32_16x16x32_f16 v[36:39], v[124:127], v[164:167], v[36:39]\n" \
"v_pk_add_f16 v161, v161, v181\n" \
"v_pk_add_f16 v162, v162, v182\n" \
"v_pk_add_f16 v163, v163, v183\n" \
"ds_read_b128 v[196:199], v249 offset:0\n" \
"v_mfma_f32_16x16x32_f16 v[24:27], v[120:123], v[164:167], v[24:27]\n" \
"v_pk_add_f16 v164, v164, v184\n" \
"v_pk_add_f16 v139, v123, v131\n" \
"s_add_i32 m0, s44, 0x4000\n" \
"s_mov_b32 s80, 0x6400000\n" \
"v_mfma_f32_16x16x32_f16 v[48:51], v[120:123], v[168:171], v[48:51]\n" \
"v_pk_add_f16 v165, v165, v185\n" \
"v_pk_add_f16 v140, v124, v132\n" \
"buffer_load_dwordx4 v232, s[36:39], s80 offen sc1 lds\n" \
"v_mfma_f32_16x16x32_f16 v[60:63], v[124:127], v[168:171], v[60:63]\n" \
"v_pk_add_f16 v166, v166, v186\n" \
"v_pk_add_f16 v167, v167, v187\n" \
"ds_read_b128 v[156:159], v240 offset:43520\n" \
"ds_read_b64_tr_b16 v[200:201], v247 offset:8192\n" \
"v_mfma_f32_16x16x32_f16 v[84:87], v[124:127], v[172:175], v[84:87]\n" \
"v_pk_add_f16 v168, v168, v144\n" \
"v_pk_add_f16 v141, v125, v133\n" \
"s_add_i32 s84, s55, 0x801a000\n" \
"ds_read_b64_tr_b16 v[202:203], v247 offset:8704\n" \
"v_mfma_f32_16x16x32_f16 v[72:75], v[120:123], v[172:175], v[72:75]\n" \
"v_pk_add_f16 v169, v169, v145\n" \
"v_pk_add_f16 v142, v126, v134\n" \
"buffer_store_dword v240, v243, s[36:39], s84 offen sc1\n" \
"v_mfma_f32_16x16x32_f16 v[96:99], v[120:123], v[176:179], v[96:99]\n" \
"v_pk_add_f16 v170, v170, v146\n" \
"v_pk_add_f16 v171, v171, v147\n" \
"v_mfma_f32_16x16x32_f16 v[108:111], v[124:127], v[176:179], v[108:111]\n" \
"v_pk_add_f16 v172, v172, v148\n" \
"v_pk_add_f16 v143, v127, v135\n" \
"v_mfma_f32_16x16x32_f16 v[4:7], v[128:131], v[180:183], v[4:7]\n" \
"v_pk_add_f16 v173, v173, v149\n" \
"ds_read_b64_tr_b16 v[120:121], v241 offset:32768\n" \
"buffer_load_dwordx4 v232, s[36:39], s80 offen offset:1024 sc1 lds\n" \
"s_waitcnt lgkmcnt(1)\n" \
"v_mfma_f32_16x16x32_bf16 v[204:207], v[200:203], v[196:199], v[204:207]\n" \
"v_mfma_f32_16x16x32_f16 v[16:19], v[132:135], v[180:183], v[16:19]\n" \
"v_pk_add_f16 v174, v174, v150\n" \
"ds_read_b128 v[208:211], v240 offset:41472\n" \
"ds_read_b128 v[196:199], v249 offset:1024\n" \
"v_mfma_f32_16x16x32_f16 v[40:43], v[132:135], v[184:187], v[40:43]\n" \
"v_pk_add_f16 v175, v175, v151\n" \
"ds_read_b128 v[180:183], v240 offset:40960\n" \
"ds_read_b64_tr_b16 v[200:201], v247 offset:12288\n" \
"v_mfma_f32_16x16x32_f16 v[28:31], v[128:131], v[184:187], v[28:31]\n" \
"v_pk_add_f16 v176, v176, v152\n" \
"ds_read_b64_tr_b16 v[122:123], v241 offset:40960\n" \
"v_mfma_f32_16x16x32_f16 v[52:55], v[128:131], v[144:147], v[52:55]\n" \
"v_pk_add_f16 v177, v177, v153\n" \
"ds_read_b128 v[184:187], v240 offset:43008\n" \
"v_mfma_f32_16x16x32_f16 v[64:67], v[132:135], v[144:147], v[64:67]\n" \
"v_pk_add_f16 v178, v178, v154\n" \
"ds_read_b64_tr_b16 v[124:125], v242 offset:32768\n" \
"v_mfma_f32_16x16x32_f16 v[88:91], v[132:135], v[148:151], v[88:91]\n" \
"v_pk_add_f16 v179, v179, v155\n" \
"ds_read_b128 v[144:147], v240 offset:45056\n" \
"buffer_load_dwordx4 v232, s[36:39], s80 offen offset:2048 sc1 lds\n" \
"v_mfma_f32_16x16x32_f16 v[76:79], v[128:131], v[148:151], v[76:79]\n" \
"ds_read_b64_tr_b16 v[126:127], v242 offset:40960\n" \
"ds_read_b64_tr_b16 v[202:203], v247 offset:12800\n" \
"v_mfma_f32_16x16x32_f16 v[100:103], v[128:131], v[152:155], v[100:103]\n" \
"ds_read_b128 v[148:151], v240 offset:47104\n" \
"v_mfma_f32_16x16x32_f16 v[112:115], v[132:135], v[152:155], v[112:115]\n" \
"ds_read_b64_tr_b16 v[128:129], v241 offset:33280\n" \
"v_mfma_f32_16x16x32_f16 v[8:11], v[136:139], v[160:163], v[8:11]\n" \
"ds_read_b128 v[152:155], v240 offset:49152\n" \
"v_mfma_f32_16x16x32_f16 v[20:23], v[140:143], v[160:163], v[20:23]\n" \
"ds_read_b128 v[160:163], v240 offset:45568\n" \
"v_mfma_f32_16x16x32_f16 v[44:47], v[140:143], v[164:167], v[44:47]\n" \
"ds_read_b64_tr_b16 v[130:131], v241 offset:41472\n" \
"ds_read_b64_tr_b16 v[132:133], v242 offset:33280\n" \
"buffer_load_dwordx4 v232, s[36:39], s80 offen offset:3072 sc1 lds\n" \
"v_mfma_f32_16x16x32_f16 v[32:35], v[136:139], v[164:167], v[32:35]\n" \
"ds_read_b128 v[164:167], v240 offset:47616\n" \
"v_mfma_f32_16x16x32_f16 v[56:59], v[136:139], v[168:171], v[56:59]\n" \
"ds_read_b64_tr_b16 v[134:135], v242 offset:41472\n" \
"s_add_i32 m0, s45, 0x1000\n" \
"s_mov_b32 s83, 0x68000\n" \
"s_waitcnt lgkmcnt(8)\n" \
"v_mfma_f32_16x16x32_bf16 v[204:207], v[200:203], v[196:199], v[204:207]\n" \
"v_mfma_f32_16x16x32_f16 v[68:71], v[140:143], v[168:171], v[68:71]\n" \
"ds_read_b128 v[168:171], v240 offset:49664\n" \
"buffer_load_dwordx4 v233, s[36:39], s83 offen sc1 lds\n" \
"v_mfma_f32_16x16x32_f16 v[92:95], v[140:143], v[172:175], v[92:95]\n" \
"v_mfma_f32_16x16x32_f16 v[80:83], v[136:139], v[172:175], v[80:83]\n" \
"v_mfma_f32_16x16x32_f16 v[104:107], v[136:139], v[176:179], v[104:107]\n" \
"v_mfma_f32_16x16x32_f16 v[116:119], v[140:143], v[176:179], v[116:119]\n" \
"s_waitcnt vmcnt(7) lgkmcnt(0)\n" \
"s_barrier\n" \
"v_mfma_f32_16x16x32_f16 v[0:3], v[120:123], v[180:183], v[0:3]\n" \
"v_pk_add_f16 v136, v120, v128\n" \
"v_pk_add_f16 v137, v121, v129\n" \
"s_add_i32 m0, s45, 0x2000\n" \
"s_mov_b32 s83, 0x70000\n" \
"v_mfma_f32_16x16x32_f16 v[12:15], v[124:127], v[180:183], v[12:15]\n" \
"v_pk_add_f16 v180, v180, v208\n" \
"v_pk_add_f16 v138, v122, v130\n" \
"buffer_load_dwordx4 v233, s[36:39], s83 offen sc1 lds\n" \
"v_mfma_f32_16x16x32_f16 v[36:39], v[124:127], v[184:187], v[36:39]\n" \
"v_pk_add_f16 v181, v181, v209\n" \
"v_pk_add_f16 v182, v182, v210\n" \
"v_pk_add_f16 v183, v183, v211\n" \
"v_cmp_eq_u32 vcc, 0, v253\n" \
"s_cbranch_vccnz .Ls011_%=\n" \
".Lr011_%=:\n" \
"v_mfma_f32_16x16x32_f16 v[24:27], v[120:123], v[184:187], v[24:27]\n" \
"v_pk_add_f16 v184, v184, v156\n" \
"v_pk_add_f16 v139, v123, v131\n" \
"s_add_i32 m0, s44, 0x8000\n" \
"s_mov_b32 s80, 0x6800000\n" \
"ds_read_b128 v[196:199], v249 offset:2048\n" \
"v_mfma_f32_16x16x32_f16 v[48:51], v[120:123], v[144:147], v[48:51]\n" \
"v_pk_add_f16 v185, v185, v157\n" \
"v_pk_add_f16 v140, v124, v132\n" \
"buffer_load_dwordx4 v232, s[36:39], s80 offen sc1 lds\n" \
"v_mfma_f32_16x16x32_f16 v[60:63], v[124:127], v[144:147], v[60:63]\n" \
"v_pk_add_f16 v186, v186, v158\n" \
"v_pk_add_f16 v187, v187, v159\n" \
"ds_read_b128 v[176:179], v240 offset:2560\n" \
"ds_read_b64_tr_b16 v[200:201], v247 offset:16384\n" \
"v_mfma_f32_16x16x32_f16 v[84:87], v[124:127], v[148:151], v[84:87]\n" \
"v_pk_add_f16 v144, v144, v160\n" \
"v_pk_add_f16 v141, v125, v133\n" \
"ds_read_b64_tr_b16 v[202:203], v247 offset:16896\n" \
"v_mfma_f32_16x16x32_f16 v[72:75], v[120:123], v[148:151], v[72:75]\n" \
"v_pk_add_f16 v145, v145, v161\n" \
"v_pk_add_f16 v142, v126, v134\n" \
"v_mfma_f32_16x16x32_f16 v[96:99], v[120:123], v[152:155], v[96:99]\n" \
"v_pk_add_f16 v146, v146, v162\n" \
"v_pk_add_f16 v147, v147, v163\n" \
"v_mfma_f32_16x16x32_f16 v[108:111], v[124:127], v[152:155], v[108:111]\n" \
"v_pk_add_f16 v148, v148, v164\n" \
"v_pk_add_f16 v143, v127, v135\n" \
"v_mfma_f32_16x16x32_f16 v[4:7], v[128:131], v[208:211], v[4:7]\n" \
"v_pk_add_f16 v149, v149, v165\n" \
"ds_read_b64_tr_b16 v[120:121], v241 offset:0\n" \
"buffer_load_dwordx4 v232, s[36:39], s80 offen offset:1024 sc1 lds\n" \
"s_waitcnt lgkmcnt(1)\n" \
"v_mfma_f32_16x16x32_bf16 v[204:207], v[200:203], v[196:199], v[204:207]\n" \
"v_mfma_f32_16x16x32_f16 v[16:19], v[132:135], v[208:211], v[16:19]\n" \
"v_pk_add_f16 v150, v150, v166\n" \
"ds_read_b128 v[172:175], v240 offset:512\n" \
"ds_read_b128 v[196:199], v249 offset:3072\n" \
"v_mfma_f32_16x16x32_f16 v[40:43], v[132:135], v[156:159], v[40:43]\n" \
"v_pk_add_f16 v151, v151, v167\n" \
"ds_read_b128 v[208:211], v240 offset:0\n" \
"ds_read_b64_tr_b16 v[200:201], v247 offset:20480\n" \
"v_mfma_f32_16x16x32_f16 v[28:31], v[128:131], v[156:159], v[28:31]\n" \
"v_pk_add_f16 v152, v152, v168\n" \
"ds_read_b64_tr_b16 v[122:123], v241 offset:8192\n" \
"v_mfma_f32_16x16x32_f16 v[52:55], v[128:131], v[160:163], v[52:55]\n" \
"v_pk_add_f16 v153, v153, v169\n" \
"ds_read_b128 v[156:159], v240 offset:2048\n" \
"v_mfma_f32_16x16x32_f16 v[64:67], v[132:135], v[160:163], v[64:67]\n" \
"v_pk_add_f16 v154, v154, v170\n" \
"ds_read_b64_tr_b16 v[124:125], v242 offset:0\n" \
"v_mfma_f32_16x16x32_f16 v[88:91], v[132:135], v[164:167], v[88:91]\n" \
"v_pk_add_f16 v155, v155, v171\n" \
"ds_read_b128 v[160:163], v240 offset:4096\n" \
"buffer_load_dwordx4 v232, s[36:39], s80 offen offset:2048 sc1 lds\n" \
"v_mfma_f32_16x16x32_f16 v[76:79], v[128:131], v[164:167], v[76:79]\n" \
"ds_read_b64_tr_b16 v[126:127], v242 offset:8192\n" \
"ds_read_b64_tr_b16 v[202:203], v247 offset:20992\n" \
"v_mfma_f32_16x16x32_f16 v[100:103], v[128:131], v[168:171], v[100:103]\n" \
"ds_read_b128 v[164:167], v240 offset:6144\n" \
"v_mfma_f32_16x16x32_f16 v[112:115], v[132:135], v[168:171], v[112:115]\n" \
"ds_read_b64_tr_b16 v[128:129], v241 offset:512\n" \
"v_mfma_f32_16x16x32_f16 v[8:11], v[136:139], v[180:183], v[8:11]\n" \
"ds_read_b128 v[168:171], v240 offset:8192\n" \
"v_mfma_f32_16x16x32_f16 v[20:23], v[140:143], v[180:183], v[20:23]\n" \
"ds_read_b128 v[180:183], v240 offset:4608\n" \
"v_mfma_f32_16x16x32_f16 v[44:47], v[140:143], v[184:187], v[44:47]\n" \
"ds_read_b64_tr_b16 v[130:131], v241 offset:8704\n" \
"ds_read_b64_tr_b16 v[132:133], v242 offset:512\n" \
"buffer_load_dwordx4 v232, s[36:39], s80 offen offset:3072 sc1 lds\n" \
"v_mfma_f32_16x16x32_f16 v[32:35], v[136:139], v[184:187], v[32:35]\n" \
"ds_read_b128 v[184:187], v240 offset:6656\n" \
"v_mfma_f32_16x16x32_f16 v[56:59], v[136:139], v[144:147], v[56:59]\n" \
"ds_read_b64_tr_b16 v[134:135], v242 offset:8704\n" \
"s_add_i32 m0, s45, 0x3000\n" \
"s_mov_b32 s83, 0x78000\n" \
"s_waitcnt lgkmcnt(8)\n" \
"v_mfma_f32_16x16x32_bf16 v[204:207], v[200:203], v[196:199], v[204:207]\n" \
"v_mfma_f32_16x16x32_f16 v[68:71], v[140:143], v[144:147], v[68:71]\n" \
"ds_read_b128 v[144:147], v240 offset:8704\n" \
"buffer_load_dwordx4 v233, s[36:39], s83 offen sc1 lds\n" \
"v_mfma_f32_16x16x32_f16 v[92:95], v[140:143], v[148:151], v[92:95]\n" \
"v_mfma_f32_16x16x32_f16 v[80:83], v[136:139], v[148:151], v[80:83]\n" \
"v_mfma_f32_16x16x32_f16 v[104:107], v[136:139], v[152:155], v[104:107]\n" \
"v_mfma_f32_16x16x32_f16 v[116:119], v[140:143], v[152:155], v[116:119]\n" \
"s_waitcnt vmcnt(6) lgkmcnt(0)\n" \
"s_barrier\n" \
"v_mfma_f32_16x16x32_f16 v[0:3], v[120:123], v[208:211], v[0:3]\n" \
"v_pk_add_f16 v136, v120, v128\n" \
"v_pk_add_f16 v137, v121, v129\n" \
"s_mov_b32 s81, 0x8018000\n" \
"v_mfma_f32_16x16x32_f16 v[12:15], v[124:127], v[208:211], v[12:15]\n" \
"v_pk_add_f16 v208, v208, v172\n" \
"v_pk_add_f16 v138, v122, v130\n" \
"buffer_load_dword v253, v252, s[36:39], s81 offen sc1\n" \
"v_mfma_f32_16x16x32_f16 v[36:39], v[124:127], v[156:159], v[36:39]\n" \
"v_pk_add_f16 v209, v209, v173\n" \
"v_pk_add_f16 v210, v210, v174\n" \
"v_pk_add_f16 v211, v211, v175\n" \
"ds_read_b128 v[196:199], v249 offset:4096\n" \
"v_mfma_f32_16x16x32_f16 v[24:27], v[120:123], v[156:159], v[24:27]\n" \
"v_pk_add_f16 v156, v156, v176\n" \
"v_pk_add_f16 v139, v123, v131\n" \
"s_add_i32 m0, s44, 0x0\n" \
"s_mov_b32 s80, 0x6c00000\n" \
"v_mfma_f32_16x16x32_f16 v[48:51], v[120:123], v[160:163], v[48:51]\n" \
"v_pk_add_f16 v157, v157, v177\n" \
"v_pk_add_f16 v140, v124, v132\n" \
"buffer_load_dwordx4 v232, s[36:39], s80 offen sc1 lds\n" \
"v_mfma_f32_16x16x32_f16 v[60:63], v[124:127], v[160:163], v[60:63]\n" \
"v_pk_add_f16 v158, v158, v178\n" \
"v_pk_add_f16 v159, v159, v179\n" \
"ds_read_b128 v[152:155], v240 offset:23040\n" \
"ds_read_b64_tr_b16 v[200:201], v247 offset:0\n" \
"v_mfma_f32_16x16x32_f16 v[84:87], v[124:127], v[164:167], v[84:87]\n" \
"v_pk_add_f16 v160, v160, v180\n" \
"v_pk_add_f16 v141, v125, v133\n" \
"ds_read_b64_tr_b16 v[202:203], v247 offset:512\n" \
"v_mfma_f32_16x16x32_f16 v[72:75], v[120:123], v[164:167], v[72:75]\n" \
"v_pk_add_f16 v161, v161, v181\n" \
"v_pk_add_f16 v142, v126, v134\n" \
"v_mfma_f32_16x16x32_f16 v[96:99], v[120:123], v[168:171], v[96:99]\n" \
"v_pk_add_f16 v162, v162, v182\n" \
"v_pk_add_f16 v163, v163, v183\n" \
"v_mfma_f32_16x16x32_f16 v[108:111], v[124:127], v[168:171], v[108:111]\n" \
"v_pk_add_f16 v164, v164, v184\n" \
"v_pk_add_f16 v143, v127, v135\n" \
"v_mfma_f32_16x16x32_f16 v[4:7], v[128:131], v[172:175], v[4:7]\n" \
"v_pk_add_f16 v165, v165, v185\n" \
"ds_read_b64_tr_b16 v[120:121], v241 offset:16384\n" \
"buffer_load_dwordx4 v232, s[36:39], s80 offen offset:1024 sc1 lds\n" \
"s_waitcnt lgkmcnt(1)\n" \
"v_mfma_f32_16x16x32_bf16 v[204:207], v[200:203], v[196:199], v[204:207]\n" \
"v_mfma_f32_16x16x32_f16 v[16:19], v[132:135], v[172:175], v[16:19]\n" \
"v_pk_add_f16 v166, v166, v186\n" \
"ds_read_b128 v[148:151], v240 offset:20992\n" \
"ds_read_b128 v[196:199], v249 offset:5120\n" \
"v_mfma_f32_16x16x32_f16 v[40:43], v[132:135], v[176:179], v[40:43]\n" \
"v_pk_add_f16 v167, v167, v187\n" \
"ds_read_b128 v[172:175], v240 offset:20480\n" \
"ds_read_b64_tr_b16 v[200:201], v247 offset:4096\n" \
"v_mfma_f32_16x16x32_f16 v[28:31], v[128:131], v[176:179], v[28:31]\n" \
"v_pk_add_f16 v168, v168, v144\n" \
"ds_read_b64_tr_b16 v[122:123], v241 offset:24576\n" \
"v_mfma_f32_16x16x32_f16 v[52:55], v[128:131], v[180:183], v[52:55]\n" \
"v_pk_add_f16 v169, v169, v145\n" \
"ds_read_b128 v[176:179], v240 offset:22528\n" \
"v_mfma_f32_16x16x32_f16 v[64:67], v[132:135], v[180:183], v[64:67]\n" \
"v_pk_add_f16 v170, v170, v146\n" \
"ds_read_b64_tr_b16 v[124:125], v242 offset:16384\n" \
"v_mfma_f32_16x16x32_f16 v[88:91], v[132:135], v[184:187], v[88:91]\n" \
"v_pk_add_f16 v171, v171, v147\n" \
"ds_read_b128 v[180:183], v240 offset:24576\n" \
"buffer_load_dwordx4 v232, s[36:39], s80 offen offset:2048 sc1 lds\n" \
"v_mfma_f32_16x16x32_f16 v[76:79], v[128:131], v[184:187], v[76:79]\n" \
"ds_read_b64_tr_b16 v[126:127], v242 offset:24576\n" \
"ds_read_b64_tr_b16 v[202:203], v247 offset:4608\n" \
"v_mfma_f32_16x16x32_f16 v[100:103], v[128:131], v[144:147], v[100:103]\n" \
"ds_read_b128 v[184:187], v240 offset:26624\n" \
"v_mfma_f32_16x16x32_f16 v[112:115], v[132:135], v[144:147], v[112:115]\n" \
"ds_read_b64_tr_b16 v[128:129], v241 offset:16896\n" \
"v_mfma_f32_16x16x32_f16 v[8:11], v[136:139], v[208:211], v[8:11]\n" \
"ds_read_b128 v[144:147], v240 offset:28672\n" \
"v_mfma_f32_16x16x32_f16 v[20:23], v[140:143], v[208:211], v[20:23]\n" \
"ds_read_b128 v[208:211], v240 offset:25088\n" \
"v_mfma_f32_16x16x32_f16 v[44:47], v[140:143], v[156:159], v[44:47]\n" \
"ds_read_b64_tr_b16 v[130:131], v241 offset:25088\n" \
"ds_read_b64_tr_b16 v[132:133], v242 offset:16896\n" \
"buffer_load_dwordx4 v232, s[36:39], s80 offen offset:3072 sc1 lds\n" \
"v_mfma_f32_16x16x32_f16 v[32:35], v[136:139], v[156:159], v[32:35]\n" \
"ds_read_b128 v[156:159], v240 offset:27136\n" \
"v_mfma_f32_16x16x32_f16 v[56:59], v[136:139], v[160:163], v[56:59]\n" \
"ds_read_b64_tr_b16 v[134:135], v242 offset:25088\n" \
"s_waitcnt lgkmcnt(8)\n" \
"v_mfma_f32_16x16x32_bf16 v[204:207], v[200:203], v[196:199], v[204:207]\n" \
"v_mfma_f32_16x16x32_f16 v[68:71], v[140:143], v[160:163], v[68:71]\n" \
"ds_read_b128 v[160:163], v240 offset:29184\n" \
"v_mfma_f32_16x16x32_f16 v[92:95], v[140:143], v[164:167], v[92:95]\n" \
"v_mfma_f32_16x16x32_f16 v[80:83], v[136:139], v[164:167], v[80:83]\n" \
"v_mfma_f32_16x16x32_f16 v[104:107], v[136:139], v[168:171], v[104:107]\n" \
"v_mfma_f32_16x16x32_f16 v[116:119], v[140:143], v[168:171], v[116:119]\n" \
"s_waitcnt vmcnt(4) lgkmcnt(0)\n" \
"s_barrier\n" \
"v_mfma_f32_16x16x32_f16 v[0:3], v[120:123], v[172:175], v[0:3]\n" \
"v_pk_add_f16 v136, v120, v128\n" \
"v_pk_add_f16 v137, v121, v129\n" \
"v_mfma_f32_16x16x32_f16 v[12:15], v[124:127], v[172:175], v[12:15]\n" \
"v_pk_add_f16 v172, v172, v148\n" \
"v_pk_add_f16 v138, v122, v130\n" \
"v_mfma_f32_16x16x32_f16 v[36:39], v[124:127], v[176:179], v[36:39]\n" \
"v_pk_add_f16 v173, v173, v149\n" \
"v_pk_add_f16 v174, v174, v150\n" \
"v_pk_add_f16 v175, v175, v151\n" \
"v_cmp_eq_u32 vcc, 0, v253\n" \
"s_cbranch_vccnz .Ls012_%=\n" \
".Lr012_%=:\n" \
"s_mov_b32 s81, 0x801a000\n" \
"v_mfma_f32_16x16x32_f16 v[24:27], v[120:123], v[176:179], v[24:27]\n" \
"v_pk_add_f16 v176, v176, v152\n" \
"v_pk_add_f16 v139, v123, v131\n" \
"buffer_load_dword v253, v252, s[36:39], s81 offen sc1\n" \
"s_add_i32 m0, s44, 0x4000\n" \
"s_mov_b32 s80, 0x7000000\n" \
"ds_read_b128 v[196:199], v249 offset:6144\n" \
"v_mfma_f32_16x16x32_f16 v[48:51], v[120:123], v[180:183], v[48:51]\n" \
"v_pk_add_f16 v177, v177, v153\n" \
"v_pk_add_f16 v140, v124, v132\n" \
"buffer_load_dwordx4 v232, s[36:39], s80 offen sc1 lds\n" \
"v_mfma_f32_16x16x32_f16 v[60:63], v[124:127], v[180:183], v[60:63]\n" \
"v_pk_add_f16 v178, v178, v154\n" \
"v_pk_add_f16 v179, v179, v155\n" \
"ds_read_b128 v[168:171], v240 offset:43520\n" \
"ds_read_b64_tr_b16 v[200:201], v247 offset:8192\n" \
"v_mfma_f32_16x16x32_f16 v[84:87], v[124:127], v[184:187], v[84:87]\n" \
"v_pk_add_f16 v180, v180, v208\n" \
"v_pk_add_f16 v141, v125, v133\n" \
"ds_read_b64_tr_b16 v[202:203], v247 offset:8704\n" \
"v_mfma_f32_16x16x32_f16 v[72:75], v[120:123], v[184:187], v[72:75]\n" \
"v_pk_add_f16 v181, v181, v209\n" \
"v_pk_add_f16 v142, v126, v134\n" \
"v_mfma_f32_16x16x32_f16 v[96:99], v[120:123], v[144:147], v[96:99]\n" \
"v_pk_add_f16 v182, v182, v210\n" \
"v_pk_add_f16 v183, v183, v211\n" \
"v_mfma_f32_16x16x32_f16 v[108:111], v[124:127], v[144:147], v[108:111]\n" \
"v_pk_add_f16 v184, v184, v156\n" \
"v_pk_add_f16 v143, v127, v135\n" \
"v_mfma_f32_16x16x32_f16 v[4:7], v[128:131], v[148:151], v[4:7]\n" \
"v_pk_add_f16 v185, v185, v157\n" \
"ds_read_b64_tr_b16 v[120:121], v241 offset:32768\n" \
"buffer_load_dwordx4 v232, s[36:39], s80 offen offset:1024 sc1 lds\n" \
"s_waitcnt lgkmcnt(1)\n" \
"v_mfma_f32_16x16x32_bf16 v[204:207], v[200:203], v[196:199], v[204:207]\n" \
"v_mfma_f32_16x16x32_f16 v[16:19], v[132:135], v[148:151], v[16:19]\n" \
"v_pk_add_f16 v186, v186, v158\n" \
"ds_read_b128 v[164:167], v240 offset:41472\n" \
"ds_read_b128 v[196:199], v249 offset:7168\n" \
"v_mfma_f32_16x16x32_f16 v[40:43], v[132:135], v[152:155], v[40:43]\n" \
"v_pk_add_f16 v187, v187, v159\n" \
"ds_read_b128 v[148:151], v240 offset:40960\n" \
"ds_read_b64_tr_b16 v[200:201], v247 offset:12288\n" \
"v_mfma_f32_16x16x32_f16 v[28:31], v[128:131], v[152:155], v[28:31]\n" \
"v_pk_add_f16 v144, v144, v160\n" \
"ds_read_b64_tr_b16 v[122:123], v241 offset:40960\n" \
"v_mfma_f32_16x16x32_f16 v[52:55], v[128:131], v[208:211], v[52:55]\n" \
"v_pk_add_f16 v145, v145, v161\n" \
"ds_read_b128 v[152:155], v240 offset:43008\n" \
"v_mfma_f32_16x16x32_f16 v[64:67], v[132:135], v[208:211], v[64:67]\n" \
"v_pk_add_f16 v146, v146, v162\n" \
"ds_read_b64_tr_b16 v[124:125], v242 offset:32768\n" \
"v_mfma_f32_16x16x32_f16 v[88:91], v[132:135], v[156:159], v[88:91]\n" \
"v_pk_add_f16 v147, v147, v163\n" \
"ds_read_b128 v[208:211], v240 offset:45056\n" \
"buffer_load_dwordx4 v232, s[36:39], s80 offen offset:2048 sc1 lds\n" \
"v_mfma_f32_16x16x32_f16 v[76:79], v[128:131], v[156:159], v[76:79]\n" \
"ds_read_b64_tr_b16 v[126:127], v242 offset:40960\n" \
"ds_read_b64_tr_b16 v[202:203], v247 offset:12800\n" \
"v_mfma_f32_16x16x32_f16 v[100:103], v[128:131], v[160:163], v[100:103]\n" \
"ds_read_b128 v[156:159], v240 offset:47104\n" \
"v_mfma_f32_16x16x32_f16 v[112:115], v[132:135], v[160:163], v[112:115]\n" \
"ds_read_b64_tr_b16 v[128:129], v241 offset:33280\n" \
"v_mfma_f32_16x16x32_f16 v[8:11], v[136:139], v[172:175], v[8:11]\n" \
"ds_read_b128 v[160:163], v240 offset:49152\n" \
"v_mfma_f32_16x16x32_f16 v[20:23], v[140:143], v[172:175], v[20:23]\n" \
"ds_read_b128 v[172:175], v240 offset:45568\n" \
"v_mfma_f32_16x16x32_f16 v[44:47], v[140:143], v[176:179], v[44:47]\n" \
"ds_read_b64_tr_b16 v[130:131], v241 offset:41472\n" \
"ds_read_b64_tr_b16 v[132:133], v242 offset:33280\n" \
"buffer_load_dwordx4 v232, s[36:39], s80 offen offset:3072 sc1 lds\n" \
"v_mfma_f32_16x16x32_f16 v[32:35], v[136:139], v[176:179], v[32:35]\n" \
"ds_read_b128 v[176:179], v240 offset:47616\n" \
"v_mfma_f32_16x16x32_f16 v[56:59], v[136:139], v[180:183], v[56:59]\n" \
"ds_read_b64_tr_b16 v[134:135], v242 offset:41472\n" \
"s_waitcnt lgkmcnt(8)\n" \
"v_mfma_f32_16x16x32_bf16 v[204:207], v[200:203], v[196:199], v[204:207]\n" \
"v_mfma_f32_16x16x32_f16 v[68:71], v[140:143], v[180:183], v[68:71]\n" \
"ds_read_b128 v[180:183], v240 offset:49664\n" \
"v_mfma_f32_16x16x32_f16 v[92:95], v[140:143], v[184:187], v[92:95]\n" \
"v_mfma_f32_16x16x32_f16 v[80:83], v[136:139], v[184:187], v[80:83]\n" \
"v_mfma_f32_16x16x32_f16 v[104:107], v[136:139], v[144:147], v[104:107]\n" \
"v_mfma_f32_16x16x32_f16 v[116:119], v[140:143], v[144:147], v[116:119]\n" \
"s_waitcnt vmcnt(4) lgkmcnt(0)\n" \
"s_barrier\n" \
"v_mfma_f32_16x16x32_f16 v[0:3], v[120:123], v[148:151], v[0:3]\n" \
"v_pk_add_f16 v136, v120, v128\n" \
"v_pk_add_f16 v137, v121, v129\n" \
"v_mfma_f32_16x16x32_f16 v[12:15], v[124:127], v[148:151], v[12:15]\n" \
"v_pk_add_f16 v148, v148, v164\n" \
"v_pk_add_f16 v138, v122, v130\n" \
"s_add_i32 m0, s44, 0x8000\n" \
"s_mov_b32 s80, 0x7400000\n" \
"v_mfma_f32_16x16x32_f16 v[36:39], v[124:127], v[152:155], v[36:39]\n" \
"v_pk_add_f16 v149, v149, v165\n" \
"v_pk_add_f16 v150, v150, v166\n" \
"v_pk_add_f16 v151, v151, v167\n" \
"buffer_load_dwordx4 v232, s[36:39], s80 offen sc1 lds\n" \
"v_mfma_f32_16x16x32_f16 v[24:27], v[120:123], v[152:155], v[24:27]\n" \
"v_pk_add_f16 v152, v152, v168\n" \
"v_pk_add_f16 v139, v123, v131\n" \
"v_mfma_f32_16x16x32_f16 v[48:51], v[120:123], v[208:211], v[48:51]\n" \
"v_pk_add_f16 v153, v153, v169\n" \
"v_pk_add_f16 v140, v124, v132\n" \
"buffer_load_dwordx4 v232, s[36:39], s80 offen offset:1024 sc1 lds\n" \
"v_mfma_f32_16x16x32_f16 v[60:63], v[124:127], v[208:211], v[60:63]\n" \
"v_pk_add_f16 v154, v154, v170\n" \
"v_pk_add_f16 v155, v155, v171\n" \
"ds_read_b128 v[144:147], v240 offset:2560\n" \
"ds_write_b128 v248, v[204:207]\n" \
"v_mfma_f32_16x16x32_f16 v[84:87], v[124:127], v[156:159], v[84:87]\n" \
"v_pk_add_f16 v208, v208, v172\n" \
"v_pk_add_f16 v141, v125, v133\n" \
"buffer_load_dwordx4 v232, s[36:39], s80 offen offset:2048 sc1 lds\n" \
"v_mfma_f32_16x16x32_f16 v[72:75], v[120:123], v[156:159], v[72:75]\n" \
"v_pk_add_f16 v209, v209, v173\n" \
"v_pk_add_f16 v142, v126, v134\n" \
"v_mfma_f32_16x16x32_f16 v[96:99], v[120:123], v[160:163], v[96:99]\n" \
"v_pk_add_f16 v210, v210, v174\n" \
"v_pk_add_f16 v211, v211, v175\n" \
"v_mfma_f32_16x16x32_f16 v[108:111], v[124:127], v[160:163], v[108:111]\n" \
"v_pk_add_f16 v156, v156, v176\n" \
"v_pk_add_f16 v143, v127, v135\n" \
"buffer_load_dwordx4 v232, s[36:39], s80 offen offset:3072 sc1 lds\n" \
"v_mfma_f32_16x16x32_f16 v[4:7], v[128:131], v[164:167], v[4:7]\n" \
"v_pk_add_f16 v157, v157, v177\n" \
"ds_read_b64_tr_b16 v[120:121], v241 offset:0\n" \
"v_cmp_eq_u32 vcc, 0, v253\n" \
"s_cbranch_vccnz .Ls013_%=\n" \
".Lr013_%=:\n" \
"v_mfma_f32_16x16x32_f16 v[16:19], v[132:135], v[164:167], v[16:19]\n" \
"v_pk_add_f16 v158, v158, v178\n" \
"ds_read_b128 v[184:187], v240 offset:512\n" \
"s_add_i32 m0, s44, 0x1b000\n" \
"s_mov_b32 s80, 0x7800000\n" \
"v_mfma_f32_16x16x32_f16 v[40:43], v[132:135], v[168:171], v[40:43]\n" \
"v_pk_add_f16 v159, v159, v179\n" \
"ds_read_b128 v[164:167], v240 offset:0\n" \
"buffer_load_dwordx4 v232, s[36:39], s80 offen sc1 lds\n" \
"v_mfma_f32_16x16x32_f16 v[28:31], v[128:131], v[168:171], v[28:31]\n" \
"v_pk_add_f16 v160, v160, v180\n" \
"ds_read_b64_tr_b16 v[122:123], v241 offset:8192\n" \
"v_mfma_f32_16x16x32_f16 v[52:55], v[128:131], v[172:175], v[52:55]\n" \
"v_pk_add_f16 v161, v161, v181\n" \
"ds_read_b128 v[168:171], v240 offset:2048\n" \
"buffer_load_dwordx4 v232, s[36:39], s80 offen offset:1024 sc1 lds\n" \
"v_mfma_f32_16x16x32_f16 v[64:67], v[132:135], v[172:175], v[64:67]\n" \
"v_pk_add_f16 v162, v162, v182\n" \
"ds_read_b64_tr_b16 v[124:125], v242 offset:0\n" \
"v_mfma_f32_16x16x32_f16 v[88:91], v[132:135], v[176:179], v[88:91]\n" \
"v_pk_add_f16 v163, v163, v183\n" \
"ds_read_b128 v[172:175], v240 offset:4096\n" \
"buffer_load_dwordx4 v232, s[36:39], s80 offen offset:2048 sc1 lds\n" \
"v_mfma_f32_16x16x32_f16 v[76:79], v[128:131], v[176:179], v[76:79]\n" \
"ds_read_b64_tr_b16 v[126:127], v242 offset:8192\n" \
"v_mfma_f32_16x16x32_f16 v[100:103], v[128:131], v[180:183], v[100:103]\n" \
"ds_read_b128 v[176:179], v240 offset:6144\n" \
"buffer_load_dwordx4 v232, s[36:39], s80 offen offset:3072 sc1 lds\n" \
"v_mfma_f32_16x16x32_f16 v[112:115], v[132:135], v[180:183], v[112:115]\n" \
"ds_read_b64_tr_b16 v[128:129], v241 offset:512\n" \
"v_mfma_f32_16x16x32_f16 v[8:11], v[136:139], v[148:151], v[8:11]\n" \
"ds_read_b128 v[180:183], v240 offset:8192\n" \
"s_add_i32 m0, s44, 0x1f000\n" \
"s_mov_b32 s80, 0x7c00000\n" \
"v_mfma_f32_16x16x32_f16 v[20:23], v[140:143], v[148:151], v[20:23]\n" \
"ds_read_b128 v[148:151], v240 offset:4608\n" \
"buffer_load_dwordx4 v232, s[36:39], s80 offen sc1 lds\n" \
"v_mfma_f32_16x16x32_f16 v[44:47], v[140:143], v[152:155], v[44:47]\n" \
"ds_read_b64_tr_b16 v[130:131], v241 offset:8704\n" \
"ds_read_b64_tr_b16 v[132:133], v242 offset:512\n" \
"v_mfma_f32_16x16x32_f16 v[32:35], v[136:139], v[152:155], v[32:35]\n" \
"ds_read_b128 v[152:155], v240 offset:6656\n" \
"buffer_load_dwordx4 v232, s[36:39], s80 offen offset:1024 sc1 lds\n" \
"v_mfma_f32_16x16x32_f16 v[56:59], v[136:139], v[208:211], v[56:59]\n" \
"ds_read_b64_tr_b16 v[134:135], v242 offset:8704\n" \
"v_mfma_f32_16x16x32_f16 v[68:71], v[140:143], v[208:211], v[68:71]\n" \
"ds_read_b128 v[208:211], v240 offset:8704\n" \
"buffer_load_dwordx4 v232, s[36:39], s80 offen offset:2048 sc1 lds\n" \
"v_mfma_f32_16x16x32_f16 v[92:95], v[140:143], v[156:159], v[92:95]\n" \
"v_mfma_f32_16x16x32_f16 v[80:83], v[136:139], v[156:159], v[80:83]\n" \
"buffer_load_dwordx4 v232, s[36:39], s80 offen offset:3072 sc1 lds\n" \
"v_mfma_f32_16x16x32_f16 v[104:107], v[136:139], v[160:163], v[104:107]\n" \
"v_mfma_f32_16x16x32_f16 v[116:119], v[140:143], v[160:163], v[116:119]\n" \
"s_waitcnt vmcnt(12) lgkmcnt(0)\n" \
"s_barrier\n" \
"v_mfma_f32_16x16x32_f16 v[0:3], v[120:123], v[164:167], v[0:3]\n" \
"v_pk_add_f16 v136, v120, v128\n" \
"v_pk_add_f16 v137, v121, v129\n" \
"v_mfma_f32_16x16x32_f16 v[12:15], v[124:127], v[164:167], v[12:15]\n" \
"v_pk_add_f16 v164, v164, v184\n" \
"v_pk_add_f16 v138, v122, v130\n" \
"s_lshr_b32 m0, s44, 1\n" \
"v_mfma_f32_16x16x32_f16 v[36:39], v[124:127], v[168:171], v[36:39]\n" \
"v_pk_add_f16 v165, v165, v185\n" \
"v_pk_add_f16 v166, v166, v186\n" \
"v_pk_add_f16 v167, v167, v187\n" \
"v_add_u32 v252, m0, v244\n" \
"v_mfma_f32_16x16x32_f16 v[24:27], v[120:123], v[168:171], v[24:27]\n" \
"v_pk_add_f16 v168, v168, v144\n" \
"v_pk_add_f16 v139, v123, v131\n" \
"v_mfma_f32_16x16x32_f16 v[48:51], v[120:123], v[172:175], v[48:51]\n" \
"v_pk_add_f16 v169, v169, v145\n" \
"v_pk_add_f16 v140, v124, v132\n" \
"global_load_lds_dwordx4 v252, s[76:77]\n" \
"v_mfma_f32_16x16x32_f16 v[60:63], v[124:127], v[172:175], v[60:63]\n" \
"v_pk_add_f16 v170, v170, v146\n" \
"v_pk_add_f16 v171, v171, v147\n" \
"v_mfma_f32_16x16x32_f16 v[84:87], v[124:127], v[176:179], v[84:87]\n" \
"v_pk_add_f16 v172, v172, v148\n" \
"v_pk_add_f16 v141, v125, v133\n" \
"v_mfma_f32_16x16x32_f16 v[72:75], v[120:123], v[176:179], v[72:75]\n" \
"v_pk_add_f16 v173, v173, v149\n" \
"v_pk_add_f16 v142, v126, v134\n" \
"v_mfma_f32_16x16x32_f16 v[96:99], v[120:123], v[180:183], v[96:99]\n" \
"v_pk_add_f16 v174, v174, v150\n" \
"v_pk_add_f16 v175, v175, v151\n" \
"v_mfma_f32_16x16x32_f16 v[108:111], v[124:127], v[180:183], v[108:111]\n" \
"v_pk_add_f16 v176, v176, v152\n" \
"v_pk_add_f16 v143, v127, v135\n" \
"v_mfma_f32_16x16x32_f16 v[4:7], v[128:131], v[184:187], v[4:7]\n" \
"v_pk_add_f16 v177, v177, v153\n" \
"ds_read_b64_tr_b16 v[120:121], v241 offset:16384\n" \
"global_load_lds_dwordx4 v252, s[76:77] offset:1024\n" \
"v_mfma_f32_16x16x32_f16 v[16:19], v[132:135], v[184:187], v[16:19]\n" \
"v_pk_add_f16 v178, v178, v154\n" \
"v_and_b32 v248, 64, v244\n" \
"ds_read_b64_tr_b16 v[122:123], v241 offset:24576\n" \
"v_mfma_f32_16x16x32_f16 v[40:43], v[132:135], v[144:147], v[40:43]\n" \
"v_pk_add_f16 v179, v179, v155\n" \
"v_add_lshl_u32 v248, v248, v244, 6\n" \
"ds_read_b128 v[184:187], v240 offset:20480\n" \
"v_mfma_f32_16x16x32_f16 v[28:31], v[128:131], v[144:147], v[28:31]\n" \
"v_pk_add_f16 v180, v180, v208\n" \
"ds_read_b64_tr_b16 v[124:125], v242 offset:16384\n" \
"v_mfma_f32_16x16x32_f16 v[52:55], v[128:131], v[148:151], v[52:55]\n" \
"v_pk_add_f16 v181, v181, v209\n" \
"ds_read_b128 v[144:147], v240 offset:20992\n" \
"v_mfma_f32_16x16x32_f16 v[64:67], v[132:135], v[148:151], v[64:67]\n" \
"v_pk_add_f16 v182, v182, v210\n" \
"v_bfe_u32 v249, v244, 2, 7\n" \
"v_mfma_f32_16x16x32_f16 v[88:91], v[132:135], v[152:155], v[88:91]\n" \
"v_pk_add_f16 v183, v183, v211\n" \
"ds_read_b128 v[148:151], v240 offset:22528\n" \
"v_mfma_f32_16x16x32_f16 v[76:79], v[128:131], v[152:155], v[76:79]\n" \
"v_cmp_eq_u32 vcc, s74, v249\n" \
"ds_read_b64_tr_b16 v[126:127], v242 offset:24576\n" \
"v_mfma_f32_16x16x32_f16 v[100:103], v[128:131], v[208:211], v[100:103]\n" \
"v_xor_b32 v253, 4, v249\n" \
"ds_read_b128 v[152:155], v240 offset:23040\n" \
"v_mfma_f32_16x16x32_f16 v[112:115], v[132:135], v[208:211], v[112:115]\n" \
"v_cndmask_b32 v249, v249, v253, vcc\n" \
"ds_read_b64_tr_b16 v[128:129], v241 offset:16896\n" \
"v_mfma_f32_16x16x32_f16 v[8:11], v[136:139], v[164:167], v[8:11]\n" \
"ds_read_b64_tr_b16 v[130:131], v241 offset:25088\n" \
"v_mfma_f32_16x16x32_f16 v[20:23], v[140:143], v[164:167], v[20:23]\n" \
"ds_read_b64_tr_b16 v[132:133], v242 offset:16896\n" \
"v_mfma_f32_16x16x32_f16 v[44:47], v[140:143], v[168:171], v[44:47]\n" \
"v_add_u32 v243, 0x1b000, v241\n" \
"ds_read_b64_tr_b16 v[134:135], v242 offset:25088\n" \
"v_mfma_f32_16x16x32_f16 v[32:35], v[136:139], v[168:171], v[32:35]\n" \
"v_xor_b32 v244, 32, v243\n" \
"v_mfma_f32_16x16x32_f16 v[56:59], v[136:139], v[172:175], v[56:59]\n" \
"v_mfma_f32_16x16x32_f16 v[68:71], v[140:143], v[172:175], v[68:71]\n" \
"v_add_u32 v246, 0x17000, v240\n" \
"v_mfma_f32_16x16x32_f16 v[92:95], v[140:143], v[176:179], v[92:95]\n" \
"v_mfma_f32_16x16x32_f16 v[80:83], v[136:139], v[176:179], v[80:83]\n" \
"v_mfma_f32_16x16x32_f16 v[104:107], v[136:139], v[180:183], v[104:107]\n" \
"v_mfma_f32_16x16x32_f16 v[116:119], v[140:143], v[180:183], v[116:119]\n" \
"s_waitcnt vmcnt(2) lgkmcnt(0)\n" \
"s_barrier\n" \
"v_mfma_f32_16x16x32_f16 v[0:3], v[120:123], v[184:187], v[0:3]\n" \
"v_pk_add_f16 v136, v120, v128\n" \
"v_pk_add_f16 v137, v121, v129\n" \
"v_pk_add_f16 v138, v122, v130\n" \
"v_pk_add_f16 v139, v123, v131\n" \
"ds_read_b128 v[156:159], v240 offset:40960\n" \
"ds_read_b64_tr_b16 v[164:165], v241 offset:32768\n" \
"v_mfma_f32_16x16x32_f16 v[12:15], v[124:127], v[184:187], v[12:15]\n" \
"v_pk_add_f16 v184, v184, v144\n" \
"v_pk_add_f16 v185, v185, v145\n" \
"v_pk_add_f16 v140, v124, v132\n" \
"ds_read_b64_tr_b16 v[166:167], v241 offset:40960\n" \
"v_mfma_f32_16x16x32_f16 v[4:7], v[128:131], v[144:147], v[4:7]\n" \
"v_pk_add_f16 v186, v186, v146\n" \
"v_pk_add_f16 v187, v187, v147\n" \
"v_pk_add_f16 v141, v125, v133\n" \
"ds_read_b64_tr_b16 v[168:169], v242 offset:32768\n" \
"v_mfma_f32_16x16x32_f16 v[16:19], v[132:135], v[144:147], v[16:19]\n" \
"v_pk_add_f16 v142, v126, v134\n" \
"v_pk_add_f16 v143, v127, v135\n" \
"ds_read_b128 v[160:163], v240 offset:41472\n" \
"ds_read_b64_tr_b16 v[170:171], v242 offset:40960\n" \
"ds_read_b64_tr_b16 v[172:173], v241 offset:33280\n" \
"v_mfma_f32_16x16x32_f16 v[8:11], v[136:139], v[184:187], v[8:11]\n" \
"ds_read_b64_tr_b16 v[174:175], v241 offset:41472\n" \
"v_mfma_f32_16x16x32_f16 v[20:23], v[140:143], v[184:187], v[20:23]\n" \
"ds_read_b64_tr_b16 v[176:177], v242 offset:33280\n" \
"ds_read_b64_tr_b16 v[178:179], v242 offset:41472\n" \
"v_mfma_f32_16x16x32_f16 v[24:27], v[120:123], v[148:151], v[24:27]\n" \
"ds_read_b128 v[184:187], v246 offset:0\n" \
"ds_read_b64_tr_b16 v[188:189], v243 offset:0\n" \
"ds_read_b64_tr_b16 v[190:191], v243 offset:8192\n" \
"v_mfma_f32_16x16x32_f16 v[36:39], v[124:127], v[148:151], v[36:39]\n" \
"v_pk_add_f16 v148, v148, v152\n" \
"v_pk_add_f16 v149, v149, v153\n" \
"ds_read_b64_tr_b16 v[192:193], v244 offset:0\n" \
"v_mfma_f32_16x16x32_f16 v[28:31], v[128:131], v[152:155], v[28:31]\n" \
"v_pk_add_f16 v150, v150, v154\n" \
"v_pk_add_f16 v151, v151, v155\n" \
"v_mfma_f32_16x16x32_f16 v[40:43], v[132:135], v[152:155], v[40:43]\n" \
"s_waitcnt lgkmcnt(7)\n" \
"v_pk_add_f16 v180, v164, v172\n" \
"ds_read_b128 v[144:147], v246 offset:512\n" \
"ds_read_b64_tr_b16 v[194:195], v244 offset:8192\n" \
"ds_read_b64_tr_b16 v[196:197], v243 offset:512\n" \
"v_mfma_f32_16x16x32_f16 v[32:35], v[136:139], v[148:151], v[32:35]\n" \
"v_pk_add_f16 v181, v165, v173\n" \
"s_waitcnt lgkmcnt(9)\n" \
"v_pk_add_f16 v182, v166, v174\n" \
"ds_read_b64_tr_b16 v[198:199], v243 offset:8704\n" \
"v_mfma_f32_16x16x32_f16 v[44:47], v[140:143], v[148:151], v[44:47]\n" \
"v_pk_add_f16 v183, v167, v175\n" \
"s_waitcnt lgkmcnt(8)\n" \
"v_pk_add_f16 v236, v168, v176\n" \
"ds_read_b64_tr_b16 v[200:201], v244 offset:512\n" \
"ds_read_b64_tr_b16 v[202:203], v244 offset:8704\n" \
"v_mfma_f32_16x16x32_f16 v[0:3], v[164:167], v[156:159], v[0:3]\n" \
"v_pk_add_f16 v237, v169, v177\n" \
"ds_read_b128 v[148:151], v246 offset:8192\n" \
"ds_read_b64_tr_b16 v[212:213], v243 offset:16384\n" \
"ds_read_b64_tr_b16 v[214:215], v243 offset:24576\n" \
"v_mfma_f32_16x16x32_f16 v[12:15], v[168:171], v[156:159], v[12:15]\n" \
"v_pk_add_f16 v156, v156, v160\n" \
"v_pk_add_f16 v157, v157, v161\n" \
"ds_read_b64_tr_b16 v[216:217], v244 offset:16384\n" \
"v_mfma_f32_16x16x32_f16 v[4:7], v[172:175], v[160:163], v[4:7]\n" \
"v_pk_add_f16 v158, v158, v162\n" \
"v_pk_add_f16 v159, v159, v163\n" \
"v_pk_add_f16 v238, v170, v178\n" \
"v_pk_add_f16 v239, v171, v179\n" \
"v_mfma_f32_16x16x32_f16 v[16:19], v[176:179], v[160:163], v[16:19]\n" \
"s_waitcnt lgkmcnt(7)\n" \
"v_pk_add_f16 v204, v188, v196\n" \
"ds_read_b128 v[152:155], v246 offset:8704\n" \
"ds_read_b64_tr_b16 v[218:219], v244 offset:24576\n" \
"ds_read_b64_tr_b16 v[220:221], v243 offset:16896\n" \
"v_mfma_f32_16x16x32_f16 v[8:11], v[180:183], v[156:159], v[8:11]\n" \
"v_pk_add_f16 v205, v189, v197\n" \
"s_waitcnt lgkmcnt(8)\n" \
"v_pk_add_f16 v206, v190, v198\n" \
"ds_read_b64_tr_b16 v[222:223], v243 offset:25088\n" \
"v_mfma_f32_16x16x32_f16 v[20:23], v[236:239], v[156:159], v[20:23]\n" \
"ds_read_b64_tr_b16 v[224:225], v244 offset:16896\n" \
"ds_read_b64_tr_b16 v[226:227], v244 offset:25088\n" \
"v_mfma_f32_16x16x32_f16 v[0:3], v[188:191], v[184:187], v[0:3]\n" \
"v_pk_add_f16 v207, v191, v199\n" \
"v_pk_add_f16 v208, v192, v200\n" \
"ds_read_b128 v[156:159], v240 offset:43008\n" \
"v_mfma_f32_16x16x32_f16 v[12:15], v[192:195], v[184:187], v[12:15]\n" \
"v_pk_add_f16 v184, v184, v144\n" \
"v_pk_add_f16 v185, v185, v145\n" \
"v_pk_add_f16 v209, v193, v201\n" \
"v_mfma_f32_16x16x32_f16 v[4:7], v[196:199], v[144:147], v[4:7]\n" \
"v_pk_add_f16 v186, v186, v146\n" \
"v_pk_add_f16 v187, v187, v147\n" \
"s_waitcnt lgkmcnt(7)\n" \
"v_pk_add_f16 v210, v194, v202\n" \
"v_pk_add_f16 v211, v195, v203\n" \
"v_mfma_f32_16x16x32_f16 v[16:19], v[200:203], v[144:147], v[16:19]\n" \
"s_waitcnt lgkmcnt(4)\n" \
"v_pk_add_f16 v228, v212, v220\n" \
"ds_read_b128 v[160:163], v240 offset:43520\n" \
"v_mfma_f32_16x16x32_f16 v[8:11], v[204:207], v[184:187], v[8:11]\n" \
"v_pk_add_f16 v229, v213, v221\n" \
"v_mfma_f32_16x16x32_f16 v[20:23], v[208:211], v[184:187], v[20:23]\n" \
"s_waitcnt lgkmcnt(4)\n" \
"v_pk_add_f16 v230, v214, v222\n" \
"v_pk_add_f16 v231, v215, v223\n" \
"v_mfma_f32_16x16x32_f16 v[0:3], v[212:215], v[148:151], v[0:3]\n" \
"s_waitcnt lgkmcnt(2)\n" \
"v_pk_add_f16 v232, v216, v224\n" \
"v_pk_add_f16 v233, v217, v225\n" \
"ds_read_b128 v[184:187], v246 offset:2048\n" \
"v_mfma_f32_16x16x32_f16 v[12:15], v[216:219], v[148:151], v[12:15]\n" \
"v_pk_add_f16 v148, v148, v152\n" \
"v_pk_add_f16 v149, v149, v153\n" \
"v_pk_add_f16 v234, v218, v226\n" \
"v_mfma_f32_16x16x32_f16 v[4:7], v[220:223], v[152:155], v[4:7]\n" \
"v_pk_add_f16 v150, v150, v154\n" \
"v_pk_add_f16 v151, v151, v155\n" \
"v_pk_add_f16 v235, v219, v227\n" \
"v_mfma_f32_16x16x32_f16 v[16:19], v[224:227], v[152:155], v[16:19]\n" \
"ds_read_b128 v[144:147], v246 offset:2560\n" \
"v_mfma_f32_16x16x32_f16 v[8:11], v[228:231], v[148:151], v[8:11]\n" \
"v_mfma_f32_16x16x32_f16 v[20:23], v[232:235], v[148:151], v[20:23]\n" \
"v_add_u32 v241, 0xffff7000, v240\n" \
"s_waitcnt lgkmcnt(2)\n" \
"v_mfma_f32_16x16x32_f16 v[24:27], v[164:167], v[156:159], v[24:27]\n" \
"ds_read_b128 v[148:151], v246 offset:10240\n" \
"v_mfma_f32_16x16x32_f16 v[36:39], v[168:171], v[156:159], v[36:39]\n" \
"v_pk_add_f16 v156, v156, v160\n" \
"v_pk_add_f16 v157, v157, v161\n" \
"v_mfma_f32_16x16x32_f16 v[28:31], v[172:175], v[160:163], v[28:31]\n" \
"v_pk_add_f16 v158, v158, v162\n" \
"v_pk_add_f16 v159, v159, v163\n" \
"v_mfma_f32_16x16x32_f16 v[40:43], v[176:179], v[160:163], v[40:43]\n" \
"ds_read_b128 v[152:155], v246 offset:10752\n" \
"v_fmac_f32 v0, s56, v8\n" \
"v_fmac_f32 v1, s56, v9\n" \
"v_fmac_f32 v2, s56, v10\n" \
"v_fmac_f32 v3, s56, v11\n" \
"v_mfma_f32_16x16x32_f16 v[32:35], v[180:183], v[156:159], v[32:35]\n" \
"v_fmac_f32 v12, s56, v20\n" \
"v_fmac_f32 v13, s56, v21\n" \
"v_fmac_f32 v14, s56, v22\n" \
"v_fmac_f32 v15, s56, v23\n" \
"v_mfma_f32_16x16x32_f16 v[44:47], v[236:239], v[156:159], v[44:47]\n" \
"v_cvt_pk_bf16_f32 v0, v0, v1\n" \
"v_cvt_pk_bf16_f32 v1, v2, v3\n" \
"v_cvt_pk_bf16_f32 v2, v12, v13\n" \
"v_cvt_pk_bf16_f32 v3, v14, v15\n" \
"s_waitcnt lgkmcnt(2)\n" \
"v_mfma_f32_16x16x32_f16 v[24:27], v[188:191], v[184:187], v[24:27]\n" \
"ds_read_b128 v[156:159], v240 offset:24576\n" \
"buffer_store_dwordx4 v[0:3], v245, s[40:43], s69 offen sc1\n" \
"v_fmac_f32 v4, s56, v8\n" \
"v_fmac_f32 v5, s56, v9\n" \
"v_fmac_f32 v6, s56, v10\n" \
"v_fmac_f32 v7, s56, v11\n" \
"v_mfma_f32_16x16x32_f16 v[36:39], v[192:195], v[184:187], v[36:39]\n" \
"v_pk_add_f16 v184, v184, v144\n" \
"v_pk_add_f16 v185, v185, v145\n" \
"v_fmac_f32 v16, s56, v20\n" \
"v_fmac_f32 v17, s56, v21\n" \
"v_fmac_f32 v18, s56, v22\n" \
"v_fmac_f32 v19, s56, v23\n" \
"v_mfma_f32_16x16x32_f16 v[28:31], v[196:199], v[144:147], v[28:31]\n" \
"v_pk_add_f16 v186, v186, v146\n" \
"v_pk_add_f16 v187, v187, v147\n" \
"v_cvt_pk_bf16_f32 v4, v4, v5\n" \
"v_cvt_pk_bf16_f32 v5, v6, v7\n" \
"v_cvt_pk_bf16_f32 v6, v16, v17\n" \
"v_cvt_pk_bf16_f32 v7, v18, v19\n" \
"buffer_store_dwordx4 v[4:7], v245, s[40:43], s69 offen offset:128 sc1\n" \
"v_mfma_f32_16x16x32_f16 v[40:43], v[200:203], v[144:147], v[40:43]\n" \
"ds_read_b128 v[160:163], v240 offset:25088\n" \
"v_mfma_f32_16x16x32_f16 v[32:35], v[204:207], v[184:187], v[32:35]\n" \
"v_mfma_f32_16x16x32_f16 v[44:47], v[208:211], v[184:187], v[44:47]\n" \
"s_waitcnt lgkmcnt(1)\n" \
"v_mfma_f32_16x16x32_f16 v[24:27], v[212:215], v[148:151], v[24:27]\n" \
"ds_read_b128 v[184:187], v240 offset:45056\n" \
"v_mfma_f32_16x16x32_f16 v[36:39], v[216:219], v[148:151], v[36:39]\n" \
"v_pk_add_f16 v148, v148, v152\n" \
"v_pk_add_f16 v149, v149, v153\n" \
"v_mfma_f32_16x16x32_f16 v[28:31], v[220:223], v[152:155], v[28:31]\n" \
"v_pk_add_f16 v150, v150, v154\n" \
"v_pk_add_f16 v151, v151, v155\n" \
"v_mfma_f32_16x16x32_f16 v[40:43], v[224:227], v[152:155], v[40:43]\n" \
"ds_read_b128 v[144:147], v240 offset:45568\n" \
"v_mfma_f32_16x16x32_f16 v[32:35], v[228:231], v[148:151], v[32:35]\n" \
"s_waitcnt vmcnt(2)\n" \
"s_barrier\n" \
"v_mfma_f32_16x16x32_f16 v[44:47], v[232:235], v[148:151], v[44:47]\n" \
"v_mfma_f32_16x16x32_f16 v[48:51], v[120:123], v[156:159], v[48:51]\n" \
"ds_read_b128 v[148:151], v240 offset:4096\n" \
"v_mfma_f32_16x16x32_f16 v[60:63], v[124:127], v[156:159], v[60:63]\n" \
"s_waitcnt lgkmcnt(3)\n" \
"v_pk_add_f16 v156, v156, v160\n" \
"v_pk_add_f16 v157, v157, v161\n" \
"v_mfma_f32_16x16x32_f16 v[52:55], v[128:131], v[160:163], v[52:55]\n" \
"v_pk_add_f16 v158, v158, v162\n" \
"v_pk_add_f16 v159, v159, v163\n" \
"v_mfma_f32_16x16x32_f16 v[64:67], v[132:135], v[160:163], v[64:67]\n" \
"ds_read_b128 v[152:155], v240 offset:4608\n" \
"v_fmac_f32 v24, s56, v32\n" \
"v_fmac_f32 v25, s56, v33\n" \
"v_fmac_f32 v26, s56, v34\n" \
"v_mfma_f32_16x16x32_f16 v[56:59], v[136:139], v[156:159], v[56:59]\n" \
"v_fmac_f32 v27, s56, v35\n" \
"v_fmac_f32 v36, s56, v44\n" \
"v_fmac_f32 v37, s56, v45\n" \
"v_mfma_f32_16x16x32_f16 v[68:71], v[140:143], v[156:159], v[68:71]\n" \
"v_fmac_f32 v38, s56, v46\n" \
"v_fmac_f32 v39, s56, v47\n" \
"v_cvt_pk_bf16_f32 v24, v24, v25\n" \
"s_waitcnt lgkmcnt(2)\n" \
"v_mfma_f32_16x16x32_f16 v[48:51], v[164:167], v[184:187], v[48:51]\n" \
"ds_read_b128 v[156:159], v241 offset:4096\n" \
"v_cvt_pk_bf16_f32 v25, v26, v27\n" \
"v_cvt_pk_bf16_f32 v26, v36, v37\n" \
"v_cvt_pk_bf16_f32 v27, v38, v39\n" \
"v_mfma_f32_16x16x32_f16 v[60:63], v[168:171], v[184:187], v[60:63]\n" \
"v_pk_add_f16 v184, v184, v144\n" \
"v_pk_add_f16 v185, v185, v145\n" \
"buffer_store_dwordx4 v[24:27], v245, s[40:43], s70 offen sc1\n" \
"v_fmac_f32 v28, s56, v32\n" \
"v_fmac_f32 v29, s56, v33\n" \
"v_fmac_f32 v30, s56, v34\n" \
"v_mfma_f32_16x16x32_f16 v[52:55], v[172:175], v[144:147], v[52:55]\n" \
"v_pk_add_f16 v186, v186, v146\n" \
"v_pk_add_f16 v187, v187, v147\n" \
"v_fmac_f32 v31, s56, v35\n" \
"v_fmac_f32 v40, s56, v44\n" \
"v_fmac_f32 v41, s56, v45\n" \
"v_mfma_f32_16x16x32_f16 v[64:67], v[176:179], v[144:147], v[64:67]\n" \
"ds_read_b128 v[160:163], v241 offset:4608\n" \
"v_fmac_f32 v42, s56, v46\n" \
"v_fmac_f32 v43, s56, v47\n" \
"v_cvt_pk_bf16_f32 v28, v28, v29\n" \
"v_mfma_f32_16x16x32_f16 v[56:59], v[180:183], v[184:187], v[56:59]\n" \
"v_cvt_pk_bf16_f32 v29, v30, v31\n" \
"v_cvt_pk_bf16_f32 v30, v40, v41\n" \
"v_cvt_pk_bf16_f32 v31, v42, v43\n" \
"buffer_store_dwordx4 v[28:31], v245, s[40:43], s70 offen offset:128 sc1\n" \
"v_mfma_f32_16x16x32_f16 v[68:71], v[236:239], v[184:187], v[68:71]\n" \
"s_waitcnt lgkmcnt(1)\n" \
"v_mfma_f32_16x16x32_f16 v[48:51], v[188:191], v[148:151], v[48:51]\n" \
"ds_read_b128 v[184:187], v240 offset:26624\n" \
"v_mfma_f32_16x16x32_f16 v[60:63], v[192:195], v[148:151], v[60:63]\n" \
"v_pk_add_f16 v148, v148, v152\n" \
"v_pk_add_f16 v149, v149, v153\n" \
"v_mfma_f32_16x16x32_f16 v[52:55], v[196:199], v[152:155], v[52:55]\n" \
"v_pk_add_f16 v150, v150, v154\n" \
"v_pk_add_f16 v151, v151, v155\n" \
"v_mfma_f32_16x16x32_f16 v[64:67], v[200:203], v[152:155], v[64:67]\n" \
"ds_read_b128 v[144:147], v240 offset:27136\n" \
"v_mfma_f32_16x16x32_f16 v[56:59], v[204:207], v[148:151], v[56:59]\n" \
"v_mfma_f32_16x16x32_f16 v[68:71], v[208:211], v[148:151], v[68:71]\n" \
"v_mfma_f32_16x16x32_f16 v[48:51], v[212:215], v[156:159], v[48:51]\n" \
"ds_read_b128 v[148:151], v240 offset:47104\n" \
"v_mfma_f32_16x16x32_f16 v[60:63], v[216:219], v[156:159], v[60:63]\n" \
"s_waitcnt lgkmcnt(3)\n" \
"v_pk_add_f16 v156, v156, v160\n" \
"v_pk_add_f16 v157, v157, v161\n" \
"v_mfma_f32_16x16x32_f16 v[52:55], v[220:223], v[160:163], v[52:55]\n" \
"v_pk_add_f16 v158, v158, v162\n" \
"v_pk_add_f16 v159, v159, v163\n" \
"v_mfma_f32_16x16x32_f16 v[64:67], v[224:227], v[160:163], v[64:67]\n" \
"ds_read_b128 v[152:155], v240 offset:47616\n" \
"v_mfma_f32_16x16x32_f16 v[56:59], v[228:231], v[156:159], v[56:59]\n" \
"v_mfma_f32_16x16x32_f16 v[68:71], v[232:235], v[156:159], v[68:71]\n" \
"s_waitcnt lgkmcnt(3)\n" \
"v_mfma_f32_16x16x32_f16 v[72:75], v[120:123], v[184:187], v[72:75]\n" \
"ds_read_b128 v[156:159], v240 offset:6144\n" \
"v_mfma_f32_16x16x32_f16 v[84:87], v[124:127], v[184:187], v[84:87]\n" \
"s_waitcnt lgkmcnt(3)\n" \
"v_pk_add_f16 v184, v184, v144\n" \
"v_pk_add_f16 v185, v185, v145\n" \
"v_mfma_f32_16x16x32_f16 v[76:79], v[128:131], v[144:147], v[76:79]\n" \
"v_pk_add_f16 v186, v186, v146\n" \
"v_pk_add_f16 v187, v187, v147\n" \
"v_mfma_f32_16x16x32_f16 v[88:91], v[132:135], v[144:147], v[88:91]\n" \
"ds_read_b128 v[160:163], v240 offset:6656\n" \
"v_fmac_f32 v48, s56, v56\n" \
"v_fmac_f32 v49, s56, v57\n" \
"v_mfma_f32_16x16x32_f16 v[80:83], v[136:139], v[184:187], v[80:83]\n" \
"v_fmac_f32 v50, s56, v58\n" \
"v_fmac_f32 v51, s56, v59\n" \
"v_fmac_f32 v60, s56, v68\n" \
"v_mfma_f32_16x16x32_f16 v[92:95], v[140:143], v[184:187], v[92:95]\n" \
"v_fmac_f32 v61, s56, v69\n" \
"v_fmac_f32 v62, s56, v70\n" \
"s_waitcnt lgkmcnt(2)\n" \
"v_mfma_f32_16x16x32_f16 v[72:75], v[164:167], v[148:151], v[72:75]\n" \
"ds_read_b128 v[184:187], v241 offset:6144\n" \
"v_fmac_f32 v63, s56, v71\n" \
"v_cvt_pk_bf16_f32 v48, v48, v49\n" \
"v_cvt_pk_bf16_f32 v49, v50, v51\n" \
"v_mfma_f32_16x16x32_f16 v[84:87], v[168:171], v[148:151], v[84:87]\n" \
"v_pk_add_f16 v148, v148, v152\n" \
"v_pk_add_f16 v149, v149, v153\n" \
"v_cvt_pk_bf16_f32 v50, v60, v61\n" \
"v_cvt_pk_bf16_f32 v51, v62, v63\n" \
"v_mfma_f32_16x16x32_f16 v[76:79], v[172:175], v[152:155], v[76:79]\n" \
"v_pk_add_f16 v150, v150, v154\n" \
"v_pk_add_f16 v151, v151, v155\n" \
"buffer_store_dwordx4 v[48:51], v245, s[40:43], s71 offen sc1\n" \
"v_fmac_f32 v52, s56, v56\n" \
"v_fmac_f32 v53, s56, v57\n" \
"v_mfma_f32_16x16x32_f16 v[88:91], v[176:179], v[152:155], v[88:91]\n" \
"ds_read_b128 v[144:147], v241 offset:6656\n" \
"v_fmac_f32 v54, s56, v58\n" \
"v_fmac_f32 v55, s56, v59\n" \
"v_fmac_f32 v64, s56, v68\n" \
"v_mfma_f32_16x16x32_f16 v[80:83], v[180:183], v[148:151], v[80:83]\n" \
"v_fmac_f32 v65, s56, v69\n" \
"v_fmac_f32 v66, s56, v70\n" \
"v_mfma_f32_16x16x32_f16 v[92:95], v[236:239], v[148:151], v[92:95]\n" \
"v_fmac_f32 v67, s56, v71\n" \
"v_cvt_pk_bf16_f32 v52, v52, v53\n" \
"v_cvt_pk_bf16_f32 v53, v54, v55\n" \
"s_waitcnt lgkmcnt(1)\n" \
"v_mfma_f32_16x16x32_f16 v[72:75], v[188:191], v[156:159], v[72:75]\n" \
"ds_read_b128 v[148:151], v240 offset:28672\n" \
"v_cvt_pk_bf16_f32 v54, v64, v65\n" \
"v_cvt_pk_bf16_f32 v55, v66, v67\n" \
"buffer_store_dwordx4 v[52:55], v245, s[40:43], s71 offen offset:128 sc1\n" \
"v_mfma_f32_16x16x32_f16 v[84:87], v[192:195], v[156:159], v[84:87]\n" \
"v_pk_add_f16 v156, v156, v160\n" \
"v_pk_add_f16 v157, v157, v161\n" \
"v_mfma_f32_16x16x32_f16 v[76:79], v[196:199], v[160:163], v[76:79]\n" \
"v_pk_add_f16 v158, v158, v162\n" \
"v_pk_add_f16 v159, v159, v163\n" \
"v_mfma_f32_16x16x32_f16 v[88:91], v[200:203], v[160:163], v[88:91]\n" \
"ds_read_b128 v[152:155], v240 offset:29184\n" \
"v_mfma_f32_16x16x32_f16 v[80:83], v[204:207], v[156:159], v[80:83]\n" \
"v_mfma_f32_16x16x32_f16 v[92:95], v[208:211], v[156:159], v[92:95]\n" \
"v_mfma_f32_16x16x32_f16 v[72:75], v[212:215], v[184:187], v[72:75]\n" \
"ds_read_b128 v[156:159], v240 offset:49152\n" \
"v_mfma_f32_16x16x32_f16 v[84:87], v[216:219], v[184:187], v[84:87]\n" \
"s_waitcnt lgkmcnt(3)\n" \
"v_pk_add_f16 v184, v184, v144\n" \
"v_pk_add_f16 v185, v185, v145\n" \
"v_mfma_f32_16x16x32_f16 v[76:79], v[220:223], v[144:147], v[76:79]\n" \
"v_pk_add_f16 v186, v186, v146\n" \
"v_pk_add_f16 v187, v187, v147\n" \
"v_mfma_f32_16x16x32_f16 v[88:91], v[224:227], v[144:147], v[88:91]\n" \
"ds_read_b128 v[160:163], v240 offset:49664\n" \
"v_mfma_f32_16x16x32_f16 v[80:83], v[228:231], v[184:187], v[80:83]\n" \
"v_mfma_f32_16x16x32_f16 v[92:95], v[232:235], v[184:187], v[92:95]\n" \
"s_waitcnt lgkmcnt(3)\n" \
"v_mfma_f32_16x16x32_f16 v[96:99], v[120:123], v[148:151], v[96:99]\n" \
"ds_read_b128 v[184:187], v240 offset:8192\n" \
"v_mfma_f32_16x16x32_f16 v[108:111], v[124:127], v[148:151], v[108:111]\n" \
"s_waitcnt lgkmcnt(3)\n" \
"v_pk_add_f16 v148, v148, v152\n" \
"v_pk_add_f16 v149, v149, v153\n" \
"v_mfma_f32_16x16x32_f16 v[100:103], v[128:131], v[152:155], v[100:103]\n" \
"v_pk_add_f16 v150, v150, v154\n" \
"v_pk_add_f16 v151, v151, v155\n" \
"v_mfma_f32_16x16x32_f16 v[112:115], v[132:135], v[152:155], v[112:115]\n" \
"ds_read_b128 v[144:147], v240 offset:8704\n" \
"v_fmac_f32 v72, s56, v80\n" \
"v_fmac_f32 v73, s56, v81\n" \
"v_mfma_f32_16x16x32_f16 v[104:107], v[136:139], v[148:151], v[104:107]\n" \
"v_fmac_f32 v74, s56, v82\n" \
"v_fmac_f32 v75, s56, v83\n" \
"v_fmac_f32 v84, s56, v92\n" \
"v_mfma_f32_16x16x32_f16 v[116:119], v[140:143], v[148:151], v[116:119]\n" \
"v_fmac_f32 v85, s56, v93\n" \
"v_fmac_f32 v86, s56, v94\n" \
"s_waitcnt lgkmcnt(2)\n" \
"v_mfma_f32_16x16x32_f16 v[96:99], v[164:167], v[156:159], v[96:99]\n" \
"ds_read_b128 v[148:151], v241 offset:8192\n" \
"v_fmac_f32 v87, s56, v95\n" \
"v_cvt_pk_bf16_f32 v72, v72, v73\n" \
"v_cvt_pk_bf16_f32 v73, v74, v75\n" \
"v_mfma_f32_16x16x32_f16 v[108:111], v[168:171], v[156:159], v[108:111]\n" \
"v_pk_add_f16 v156, v156, v160\n" \
"v_pk_add_f16 v157, v157, v161\n" \
"v_cvt_pk_bf16_f32 v74, v84, v85\n" \
"v_cvt_pk_bf16_f32 v75, v86, v87\n" \
"v_mfma_f32_16x16x32_f16 v[100:103], v[172:175], v[160:163], v[100:103]\n" \
"v_pk_add_f16 v158, v158, v162\n" \
"v_pk_add_f16 v159, v159, v163\n" \
"buffer_store_dwordx4 v[72:75], v245, s[40:43], s72 offen sc1\n" \
"v_fmac_f32 v76, s56, v80\n" \
"v_fmac_f32 v77, s56, v81\n" \
"v_mfma_f32_16x16x32_f16 v[112:115], v[176:179], v[160:163], v[112:115]\n" \
"ds_read_b128 v[152:155], v241 offset:8704\n" \
"v_fmac_f32 v78, s56, v82\n" \
"v_fmac_f32 v79, s56, v83\n" \
"v_fmac_f32 v88, s56, v92\n" \
"v_mfma_f32_16x16x32_f16 v[104:107], v[180:183], v[156:159], v[104:107]\n" \
"v_fmac_f32 v89, s56, v93\n" \
"v_fmac_f32 v90, s56, v94\n" \
"v_mfma_f32_16x16x32_f16 v[116:119], v[236:239], v[156:159], v[116:119]\n" \
"v_fmac_f32 v91, s56, v95\n" \
"v_cvt_pk_bf16_f32 v76, v76, v77\n" \
"v_cvt_pk_bf16_f32 v77, v78, v79\n" \
"s_waitcnt lgkmcnt(1)\n" \
"v_mfma_f32_16x16x32_f16 v[96:99], v[188:191], v[184:187], v[96:99]\n" \
"v_cvt_pk_bf16_f32 v78, v88, v89\n" \
"v_cvt_pk_bf16_f32 v79, v90, v91\n" \
"buffer_store_dwordx4 v[76:79], v245, s[40:43], s72 offen offset:128 sc1\n" \
"v_mfma_f32_16x16x32_f16 v[108:111], v[192:195], v[184:187], v[108:111]\n" \
"v_pk_add_f16 v184, v184, v144\n" \
"v_pk_add_f16 v185, v185, v145\n" \
"v_mfma_f32_16x16x32_f16 v[100:103], v[196:199], v[144:147], v[100:103]\n" \
"v_pk_add_f16 v186, v186, v146\n" \
"v_pk_add_f16 v187, v187, v147\n" \
"v_mfma_f32_16x16x32_f16 v[112:115], v[200:203], v[144:147], v[112:115]\n" \
"s_nop 0\n" \
"v_mfma_f32_16x16x32_f16 v[104:107], v[204:207], v[184:187], v[104:107]\n" \
"v_mfma_f32_16x16x32_f16 v[116:119], v[208:211], v[184:187], v[116:119]\n" \
"v_mfma_f32_16x16x32_f16 v[96:99], v[212:215], v[148:151], v[96:99]\n" \
"v_mfma_f32_16x16x32_f16 v[108:111], v[216:219], v[148:151], v[108:111]\n" \
"s_waitcnt lgkmcnt(0)\n" \
"v_pk_add_f16 v148, v148, v152\n" \
"v_pk_add_f16 v149, v149, v153\n" \
"v_mfma_f32_16x16x32_f16 v[100:103], v[220:223], v[152:155], v[100:103]\n" \
"v_pk_add_f16 v150, v150, v154\n" \
"v_pk_add_f16 v151, v151, v155\n" \
"v_mfma_f32_16x16x32_f16 v[112:115], v[224:227], v[152:155], v[112:115]\n" \
"s_nop 0\n" \
"v_mfma_f32_16x16x32_f16 v[104:107], v[228:231], v[148:151], v[104:107]\n" \
"v_mfma_f32_16x16x32_f16 v[116:119], v[232:235], v[148:151], v[116:119]\n" \
"s_nop 7\n" \
"v_fmac_f32 v96, s56, v104\n" \
"v_fmac_f32 v97, s56, v105\n" \
"v_fmac_f32 v98, s56, v106\n" \
"v_fmac_f32 v99, s56, v107\n" \
"v_fmac_f32 v108, s56, v116\n" \
"v_fmac_f32 v109, s56, v117\n" \
"v_fmac_f32 v110, s56, v118\n" \
"v_fmac_f32 v111, s56, v119\n" \
"v_cvt_pk_bf16_f32 v96, v96, v97\n" \
"v_cvt_pk_bf16_f32 v97, v98, v99\n" \
"v_cvt_pk_bf16_f32 v98, v108, v109\n" \
"v_cvt_pk_bf16_f32 v99, v110, v111\n" \
"buffer_store_dwordx4 v[96:99], v245, s[40:43], s73 offen sc1\n" \
"v_fmac_f32 v100, s56, v104\n" \
"v_fmac_f32 v101, s56, v105\n" \
"v_fmac_f32 v102, s56, v106\n" \
"v_fmac_f32 v103, s56, v107\n" \
"v_fmac_f32 v112, s56, v116\n" \
"v_fmac_f32 v113, s56, v117\n" \
"v_fmac_f32 v114, s56, v118\n" \
"v_fmac_f32 v115, s56, v119\n" \
"v_cvt_pk_bf16_f32 v100, v100, v101\n" \
"v_cvt_pk_bf16_f32 v101, v102, v103\n" \
"v_cvt_pk_bf16_f32 v102, v112, v113\n" \
"v_cvt_pk_bf16_f32 v103, v114, v115\n" \
"buffer_store_dwordx4 v[100:103], v245, s[40:43], s73 offen offset:128 sc1\n" \
"s_waitcnt vmcnt(0)\n" \
"s_barrier\n" \
"s_mov_b32 exec_lo, s57\n" \
"s_mov_b32 exec_hi, 0\n" \
"buffer_store_dword v240, v248, s[36:39], s58 offen sc1\n" \
"s_mov_b64 exec, -1\n" \
"buffer_load_dword v249, v249, s[36:39], s59 offen sc1\n" \
"s_branch .Lcf_e\n" \
".Ls03_%=:\n" \
"s_mov_b32 s87, 0x200000\n" \
"s_mov_b32 s86, 0x8006000\n" \
".Lt03_%=:\n" \
"s_sleep 3\n" \
"buffer_load_dword v253, v252, s[36:39], s86 offen sc1\n" \
"s_waitcnt vmcnt(0)\n" \
"s_sub_u32 s87, s87, 1\n" \
"s_cbranch_scc1 .Lr03_%=\n" \
"v_cmp_eq_u32 vcc, 0, v253\n" \
"s_cbranch_vccnz .Lt03_%=\n" \
"s_branch .Lr03_%=\n" \
".Ls04_%=:\n" \
"s_mov_b32 s87, 0x200000\n" \
"s_mov_b32 s86, 0x8008000\n" \
".Lt04_%=:\n" \
"s_sleep 3\n" \
"buffer_load_dword v253, v252, s[36:39], s86 offen sc1\n" \
"s_waitcnt vmcnt(0)\n" \
"s_sub_u32 s87, s87, 1\n" \
"s_cbranch_scc1 .Lr04_%=\n" \
"v_cmp_eq_u32 vcc, 0, v253\n" \
"s_cbranch_vccnz .Lt04_%=\n" \
"s_branch .Lr04_%=\n" \
".Ls05_%=:\n" \
"s_mov_b32 s87, 0x200000\n" \
"s_mov_b32 s86, 0x800a000\n" \
".Lt05_%=:\n" \
"s_sleep 3\n" \
"buffer_load_dword v253, v252, s[36:39], s86 offen sc1\n" \
"s_waitcnt vmcnt(0)\n" \
"s_sub_u32 s87, s87, 1\n" \
"s_cbranch_scc1 .Lr05_%=\n" \
"v_cmp_eq_u32 vcc, 0, v253\n" \
"s_cbranch_vccnz .Lt05_%=\n" \
"s_branch .Lr05_%=\n" \
".Ls07_%=:\n" \
"s_mov_b32 s87, 0x200000\n" \
"s_mov_b32 s86, 0x800e000\n" \
".Lt07_%=:\n" \
"s_sleep 3\n" \
"buffer_load_dword v253, v252, s[36:39], s86 offen sc1\n" \
"s_waitcnt vmcnt(0)\n" \
"s_sub_u32 s87, s87, 1\n" \
"s_cbranch_scc1 .Lr07_%=\n" \
"v_cmp_eq_u32 vcc, 0, v253\n" \
"s_cbranch_vccnz .Lt07_%=\n" \
"s_branch .Lr07_%=\n" \
".Ls08_%=:\n" \
"s_mov_b32 s87, 0x200000\n" \
"s_mov_b32 s86, 0x8010000\n" \
".Lt08_%=:\n" \
"s_sleep 3\n" \
"buffer_load_dword v253, v252, s[36:39], s86 offen sc1\n" \
"s_waitcnt vmcnt(0)\n" \
"s_sub_u32 s87, s87, 1\n" \
"s_cbranch_scc1 .Lr08_%=\n" \
"v_cmp_eq_u32 vcc, 0, v253\n" \
"s_cbranch_vccnz .Lt08_%=\n" \
"s_branch .Lr08_%=\n" \
".Ls09_%=:\n" \
"s_mov_b32 s87, 0x200000\n" \
"s_mov_b32 s86, 0x8012000\n" \
".Lt09_%=:\n" \
"s_sleep 3\n" \
"buffer_load_dword v253, v252, s[36:39], s86 offen sc1\n" \
"s_waitcnt vmcnt(0)\n" \
"s_sub_u32 s87, s87, 1\n" \
"s_cbranch_scc1 .Lr09_%=\n" \
"v_cmp_eq_u32 vcc, 0, v253\n" \
"s_cbranch_vccnz .Lt09_%=\n" \
"s_branch .Lr09_%=\n" \
".Ls011_%=:\n" \
"s_mov_b32 s87, 0x200000\n" \
"s_mov_b32 s86, 0x8016000\n" \
".Lt011_%=:\n" \
"s_sleep 3\n" \
"buffer_load_dword v253, v252, s[36:39], s86 offen sc1\n" \
"s_waitcnt vmcnt(0)\n" \
"s_sub_u32 s87, s87, 1\n" \
"s_cbranch_scc1 .Lr011_%=\n" \
"v_cmp_eq_u32 vcc, 0, v253\n" \
"s_cbranch_vccnz .Lt011_%=\n" \
"s_branch .Lr011_%=\n" \
".Ls012_%=:\n" \
"s_mov_b32 s87, 0x200000\n" \
"s_mov_b32 s86, 0x8018000\n" \
".Lt012_%=:\n" \
"s_sleep 3\n" \
"buffer_load_dword v253, v252, s[36:39], s86 offen sc1\n" \
"s_waitcnt vmcnt(0)\n" \
"s_sub_u32 s87, s87, 1\n" \
"s_cbranch_scc1 .Lr012_%=\n" \
"v_cmp_eq_u32 vcc, 0, v253\n" \
"s_cbranch_vccnz .Lt012_%=\n" \
"s_branch .Lr012_%=\n" \
".Ls013_%=:\n" \
"s_mov_b32 s87, 0x200000\n" \
"s_mov_b32 s86, 0x801a000\n" \
".Lt013_%=:\n" \
"s_sleep 3\n" \
"buffer_load_dword v253, v252, s[36:39], s86 offen sc1\n" \
"s_waitcnt vmcnt(0)\n" \
"s_sub_u32 s87, s87, 1\n" \
"s_cbranch_scc1 .Lr013_%=\n" \
"v_cmp_eq_u32 vcc, 0, v253\n" \
"s_cbranch_vccnz .Lt013_%=\n" \
"s_branch .Lr013_%=\n" \
""
#define LOOP1 \
"s_mov_b32 s91, 0x40004\n" \
"s_mov_b32 s92, 0xfff8fff8\n" \
"s_mov_b32 s94, s53\n" \
"s_mov_b32 s95, s57\n" \
"s_setprio 1\n" \
"s_cmp_eq_u32 s94, 0\n" \
"s_cbranch_scc1 .Lp0_%=\n" \
".Lq0_%=:\n" \
"s_memrealtime s[86:87]\n" \
"s_waitcnt lgkmcnt(0)\n" \
"s_and_b32 s87, s94, 0xff\n" \
"s_add_i32 s75, s86, s87\n" \
".Lp0_%=:\n" \
"ds_read_b64_tr_b16 v[120:121], v241 offset:0\n" \
"ds_read_b64_tr_b16 v[122:123], v241 offset:8192\n" \
"ds_read_b128 v[144:147], v240 offset:2048\n" \
"ds_read_b64_tr_b16 v[124:125], v242 offset:0\n" \
"ds_read_b64_tr_b16 v[126:127], v242 offset:8192\n" \
"ds_read_b64_tr_b16 v[128:129], v241 offset:512\n" \
"ds_read_b64_tr_b16 v[130:131], v241 offset:8704\n" \
"ds_read_b64_tr_b16 v[132:133], v242 offset:512\n" \
"ds_read_b64_tr_b16 v[134:135], v242 offset:8704\n" \
"ds_read_b128 v[164:167], v240 offset:2560\n" \
"ds_read_b128 v[168:171], v240 offset:4608\n" \
"ds_read_b128 v[148:151], v240 offset:4096\n" \
"ds_read_b128 v[172:175], v240 offset:6656\n" \
"ds_read_b128 v[152:155], v240 offset:6144\n" \
"ds_read_b128 v[176:179], v240 offset:8704\n" \
"ds_read_b128 v[156:159], v240 offset:8192\n" \
"ds_read_b128 v[180:183], v240 offset:512\n" \
"ds_read_b128 v[160:163], v240 offset:0\n" \
"s_waitcnt lgkmcnt(15)\n" \
"v_mfma_f32_16x16x32_f16 v[0:3], v[120:123], v[144:147], 0\n" \
"s_waitcnt lgkmcnt(12)\n" \
"v_pk_add_f16 v136, v120, v128\n" \
"v_pk_add_f16 v137, v121, v129\n" \
"s_add_i32 m0, s44, 0xa000\n" \
"s_add_i32 s80, s46, 0x800000\n" \
"ds_write_b64 v246, v[96:97] offset:0\n" \
"v_mfma_f32_16x16x32_f16 v[12:15], v[124:127], v[144:147], 0\n" \
"s_waitcnt lgkmcnt(9)\n" \
"v_pk_add_f16 v144, v144, v164\n" \
"v_pk_add_f16 v138, v122, v130\n" \
"buffer_load_dwordx4 v232, s[36:39], s80 offen sc1 lds\n" \
"ds_write_b64 v246, v[98:99] offset:4096\n" \
"s_waitcnt lgkmcnt(8)\n" \
"v_mfma_f32_16x16x32_f16 v[36:39], v[124:127], v[148:151], 0\n" \
"v_pk_add_f16 v145, v145, v165\n" \
"v_pk_add_f16 v146, v146, v166\n" \
"v_pk_add_f16 v147, v147, v167\n" \
"ds_write_b64 v246, v[100:101] offset:8192\n" \
"ds_write_b64 v246, v[102:103] offset:12288\n" \
"v_mfma_f32_16x16x32_f16 v[24:27], v[120:123], v[148:151], 0\n" \
"v_pk_add_f16 v148, v148, v168\n" \
"v_pk_add_f16 v139, v123, v131\n" \
"buffer_load_dwordx4 v232, s[36:39], s80 offen offset:1024 sc1 lds\n" \
"ds_write_b64 v246, v[104:105] offset:16384\n" \
"s_waitcnt lgkmcnt(5)\n" \
"v_mfma_f32_16x16x32_f16 v[48:51], v[120:123], v[152:155], 0\n" \
"v_pk_add_f16 v149, v149, v169\n" \
"v_pk_add_f16 v140, v124, v132\n" \
"buffer_store_dwordx4 v[72:75], v235, s[36:39], s93 offen sc1\n" \
"ds_write_b64 v246, v[106:107] offset:20480\n" \
"v_mfma_f32_16x16x32_f16 v[60:63], v[124:127], v[152:155], 0\n" \
"v_pk_add_f16 v150, v150, v170\n" \
"v_pk_add_f16 v151, v151, v171\n" \
"buffer_load_dwordx4 v232, s[36:39], s80 offen offset:2048 sc1 lds\n" \
"ds_write_b64 v246, v[108:109] offset:24576\n" \
"ds_write_b64 v246, v[110:111] offset:28672\n" \
"v_mfma_f32_16x16x32_f16 v[84:87], v[124:127], v[156:159], 0\n" \
"v_pk_add_f16 v152, v152, v172\n" \
"v_pk_add_f16 v141, v125, v133\n" \
"ds_write_b64 v246, v[112:113] offset:32768\n" \
"ds_write_b64 v246, v[114:115] offset:36864\n" \
"v_mfma_f32_16x16x32_f16 v[72:75], v[120:123], v[156:159], 0\n" \
"v_pk_add_f16 v153, v153, v173\n" \
"v_pk_add_f16 v142, v126, v134\n" \
"buffer_load_dwordx4 v232, s[36:39], s80 offen offset:3072 sc1 lds\n" \
"ds_write_b64 v246, v[116:117] offset:40960\n" \
"v_mfma_f32_16x16x32_f16 v[96:99], v[120:123], v[160:163], 0\n" \
"v_pk_add_f16 v154, v154, v174\n" \
"v_pk_add_f16 v155, v155, v175\n" \
"v_mfma_f32_16x16x32_f16 v[108:111], v[124:127], v[160:163], 0\n" \
"v_pk_add_f16 v156, v156, v176\n" \
"v_pk_add_f16 v143, v127, v135\n" \
"s_waitcnt vmcnt(8)\n" \
"s_barrier\n" \
"v_mfma_f32_16x16x32_f16 v[4:7], v[128:131], v[164:167], 0\n" \
"v_pk_add_f16 v157, v157, v177\n" \
"ds_read_b64_tr_b16 v[120:121], v241 offset:16384\n" \
"s_add_i32 m0, s45, 0xa000\n" \
"s_add_i32 s81, s47, 0x800000\n" \
"v_mfma_f32_16x16x32_f16 v[16:19], v[132:135], v[164:167], 0\n" \
"v_pk_add_f16 v158, v158, v178\n" \
"ds_read_b128 v[184:187], v240 offset:23040\n" \
"buffer_load_dwordx4 v232, s[36:39], s81 offen sc1 lds\n" \
"v_mfma_f32_16x16x32_f16 v[40:43], v[132:135], v[168:171], 0\n" \
"v_pk_add_f16 v159, v159, v179\n" \
"ds_read_b128 v[164:167], v240 offset:22528\n" \
"buffer_store_dwordx4 v[196:199], v235, s[36:39], s60 offen sc1\n" \
"v_mfma_f32_16x16x32_f16 v[28:31], v[128:131], v[168:171], 0\n" \
"v_pk_add_f16 v160, v160, v180\n" \
"ds_read_b64_tr_b16 v[122:123], v241 offset:24576\n" \
"s_add_i32 m0, s44, 0x0\n" \
"s_add_i32 s80, s46, 0xc00000\n" \
"v_mfma_f32_16x16x32_f16 v[52:55], v[128:131], v[172:175], 0\n" \
"v_pk_add_f16 v161, v161, v181\n" \
"ds_read_b128 v[168:171], v240 offset:24576\n" \
"buffer_load_dwordx4 v232, s[36:39], s80 offen sc1 lds\n" \
"v_mfma_f32_16x16x32_f16 v[64:67], v[132:135], v[172:175], 0\n" \
"v_pk_add_f16 v162, v162, v182\n" \
"ds_read_b64_tr_b16 v[124:125], v242 offset:16384\n" \
"v_mfma_f32_16x16x32_f16 v[88:91], v[132:135], v[176:179], 0\n" \
"v_pk_add_f16 v163, v163, v183\n" \
"ds_read_b128 v[172:175], v240 offset:26624\n" \
"v_mfma_f32_16x16x32_f16 v[76:79], v[128:131], v[176:179], 0\n" \
"s_setprio 0\n" \
"ds_read_b64_tr_b16 v[126:127], v242 offset:24576\n" \
"buffer_load_dwordx4 v232, s[36:39], s80 offen offset:1024 sc1 lds\n" \
"v_mfma_f32_16x16x32_f16 v[100:103], v[128:131], v[180:183], 0\n" \
"ds_read_b128 v[176:179], v240 offset:28672\n" \
"buffer_store_dwordx4 v[200:203], v235, s[36:39], s61 offen sc1\n" \
"v_mfma_f32_16x16x32_f16 v[112:115], v[132:135], v[180:183], 0\n" \
"ds_read_b64_tr_b16 v[128:129], v241 offset:16896\n" \
"v_mfma_f32_16x16x32_f16 v[8:11], v[136:139], v[144:147], 0\n" \
"ds_read_b128 v[180:183], v240 offset:20480\n" \
"buffer_load_dwordx4 v232, s[36:39], s80 offen offset:2048 sc1 lds\n" \
"v_mfma_f32_16x16x32_f16 v[20:23], v[140:143], v[144:147], 0\n" \
"ds_read_b128 v[144:147], v240 offset:25088\n" \
"v_mfma_f32_16x16x32_f16 v[44:47], v[140:143], v[148:151], 0\n" \
"ds_read_b64_tr_b16 v[130:131], v241 offset:25088\n" \
"ds_read_b64_tr_b16 v[132:133], v242 offset:16896\n" \
"v_mfma_f32_16x16x32_f16 v[32:35], v[136:139], v[148:151], 0\n" \
"ds_read_b128 v[148:151], v240 offset:27136\n" \
"buffer_load_dwordx4 v232, s[36:39], s80 offen offset:3072 sc1 lds\n" \
"v_mfma_f32_16x16x32_f16 v[56:59], v[136:139], v[152:155], 0\n" \
"ds_read_b64_tr_b16 v[134:135], v242 offset:25088\n" \
"v_mfma_f32_16x16x32_f16 v[68:71], v[140:143], v[152:155], 0\n" \
"ds_read_b128 v[152:155], v240 offset:29184\n" \
"v_mfma_f32_16x16x32_f16 v[92:95], v[140:143], v[156:159], 0\n" \
"s_add_i32 m0, s45, 0x0\n" \
"s_add_i32 s81, s47, 0xc00000\n" \
"v_mfma_f32_16x16x32_f16 v[80:83], v[136:139], v[156:159], 0\n" \
"ds_read_b128 v[156:159], v240 offset:20992\n" \
"buffer_load_dwordx4 v232, s[36:39], s81 offen sc1 lds\n" \
"v_mfma_f32_16x16x32_f16 v[104:107], v[136:139], v[160:163], 0\n" \
"v_mfma_f32_16x16x32_f16 v[116:119], v[140:143], v[160:163], 0\n" \
"s_setprio 1\n" \
"s_waitcnt vmcnt(7) lgkmcnt(0)\n" \
"s_cmp_eq_u32 s94, 0\n" \
"s_cbranch_scc1 .Lp1_%=\n" \
".Lq1_%=:\n" \
"s_memrealtime s[86:87]\n" \
"s_waitcnt lgkmcnt(0)\n" \
"s_sub_i32 s87, s86, s75\n" \
"s_cmp_lt_i32 s87, 0\n" \
"s_cbranch_scc1 .Lq1_%=\n" \
"s_lshr_b64 s[94:95], s[94:95], 8\n" \
"s_and_b32 s87, s94, 0xff\n" \
"s_add_i32 s75, s86, s87\n" \
".Lp1_%=:\n" \
"s_barrier\n" \
"v_mfma_f32_16x16x32_f16 v[0:3], v[120:123], v[164:167], v[0:3]\n" \
"v_pk_add_f16 v136, v120, v128\n" \
"v_pk_add_f16 v137, v121, v129\n" \
"ds_read_b64_tr_b16 v[188:189], v236 offset:0\n" \
"v_mfma_f32_16x16x32_f16 v[12:15], v[124:127], v[164:167], v[12:15]\n" \
"v_pk_add_f16 v164, v164, v184\n" \
"v_pk_add_f16 v138, v122, v130\n" \
"ds_read_b64_tr_b16 v[192:193], v237 offset:0\n" \
"v_mfma_f32_16x16x32_f16 v[36:39], v[124:127], v[168:171], v[36:39]\n" \
"v_pk_add_f16 v165, v165, v185\n" \
"v_pk_add_f16 v166, v166, v186\n" \
"v_pk_add_f16 v167, v167, v187\n" \
"v_mfma_f32_16x16x32_f16 v[24:27], v[120:123], v[168:171], v[24:27]\n" \
"v_pk_add_f16 v168, v168, v144\n" \
"v_pk_add_f16 v139, v123, v131\n" \
"s_add_i32 m0, s44, 0x5000\n" \
"s_add_i32 s80, s46, 0x1000000\n" \
"v_mfma_f32_16x16x32_f16 v[48:51], v[120:123], v[172:175], v[48:51]\n" \
"v_pk_add_f16 v169, v169, v145\n" \
"v_pk_add_f16 v140, v124, v132\n" \
"buffer_load_dwordx4 v232, s[36:39], s80 offen sc1 lds\n" \
"v_mfma_f32_16x16x32_f16 v[60:63], v[124:127], v[172:175], v[60:63]\n" \
"v_pk_add_f16 v170, v170, v146\n" \
"v_pk_add_f16 v171, v171, v147\n" \
"s_waitcnt lgkmcnt(1)\n" \
"v_mfma_f32_16x16x16_bf16 v[188:191], v[188:189], v[254:255], 0\n" \
"v_mfma_f32_16x16x32_f16 v[84:87], v[124:127], v[176:179], v[84:87]\n" \
"v_pk_add_f16 v172, v172, v148\n" \
"v_pk_add_f16 v141, v125, v133\n" \
"s_waitcnt lgkmcnt(0)\n" \
"v_mfma_f32_16x16x16_bf16 v[192:195], v[192:193], v[254:255], 0\n" \
"v_mfma_f32_16x16x32_f16 v[72:75], v[120:123], v[176:179], v[72:75]\n" \
"v_pk_add_f16 v173, v173, v149\n" \
"v_pk_add_f16 v142, v126, v134\n" \
"v_mfma_f32_16x16x32_f16 v[96:99], v[120:123], v[180:183], v[96:99]\n" \
"v_pk_add_f16 v174, v174, v150\n" \
"v_pk_add_f16 v175, v175, v151\n" \
"v_mfma_f32_16x16x32_f16 v[108:111], v[124:127], v[180:183], v[108:111]\n" \
"v_pk_add_f16 v176, v176, v152\n" \
"v_pk_add_f16 v143, v127, v135\n" \
"v_cvt_pk_f16_f32 v188, v188, v189\n" \
"v_cvt_pk_f16_f32 v189, v190, v191\n" \
"v_mfma_f32_16x16x32_f16 v[4:7], v[128:131], v[184:187], v[4:7]\n" \
"v_pk_add_f16 v177, v177, v153\n" \
"v_cvt_pk_f16_f32 v190, v192, v193\n" \
"ds_read_b64_tr_b16 v[120:121], v241 offset:32768\n" \
"buffer_load_dwordx4 v232, s[36:39], s80 offen offset:1024 sc1 lds\n" \
"v_mfma_f32_16x16x32_f16 v[16:19], v[132:135], v[184:187], v[16:19]\n" \
"v_pk_add_f16 v178, v178, v154\n" \
"v_cvt_pk_f16_f32 v191, v194, v195\n" \
"v_add_u32 v188, s91, v188\n" \
"ds_read_b128 v[160:163], v240 offset:43520\n" \
"v_mfma_f32_16x16x32_f16 v[40:43], v[132:135], v[144:147], v[40:43]\n" \
"v_pk_add_f16 v179, v179, v155\n" \
"v_add_u32 v189, s91, v189\n" \
"v_add_u32 v190, s91, v190\n" \
"ds_read_b128 v[184:187], v240 offset:43008\n" \
"v_mfma_f32_16x16x32_f16 v[28:31], v[128:131], v[144:147], v[28:31]\n" \
"v_pk_add_f16 v180, v180, v156\n" \
"v_add_u32 v191, s91, v191\n" \
"v_and_b32 v188, s92, v188\n" \
"ds_read_b64_tr_b16 v[122:123], v241 offset:40960\n" \
"s_add_i32 s82, s49, 0x2800000\n" \
"v_mfma_f32_16x16x32_f16 v[52:55], v[128:131], v[148:151], v[52:55]\n" \
"v_pk_add_f16 v181, v181, v157\n" \
"v_and_b32 v189, s92, v189\n" \
"v_and_b32 v190, s92, v190\n" \
"v_and_b32 v191, s92, v191\n" \
"ds_read_b128 v[144:147], v240 offset:45056\n" \
"v_mfma_f32_16x16x32_f16 v[64:67], v[132:135], v[148:151], v[64:67]\n" \
"v_pk_add_f16 v182, v182, v158\n" \
"ds_read_b64_tr_b16 v[124:125], v242 offset:32768\n" \
"buffer_store_dwordx4 v[188:191], v234, s[36:39], s82 offen sc1\n" \
"v_mfma_f32_16x16x32_f16 v[88:91], v[132:135], v[152:155], v[88:91]\n" \
"v_pk_add_f16 v183, v183, v159\n" \
"ds_read_b128 v[148:151], v240 offset:47104\n" \
"buffer_load_dwordx4 v232, s[36:39], s80 offen offset:2048 sc1 lds\n" \
"v_mfma_f32_16x16x32_f16 v[76:79], v[128:131], v[152:155], v[76:79]\n" \
"s_setprio 0\n" \
"ds_read_b64_tr_b16 v[126:127], v242 offset:40960\n" \
"ds_read_b64_tr_b16 v[188:189], v238 offset:0\n" \
"v_mfma_f32_16x16x32_f16 v[100:103], v[128:131], v[156:159], v[100:103]\n" \
"ds_read_b128 v[152:155], v240 offset:49152\n" \
"ds_read_b64_tr_b16 v[192:193], v239 offset:0\n" \
"v_mfma_f32_16x16x32_f16 v[112:115], v[132:135], v[156:159], v[112:115]\n" \
"ds_read_b64_tr_b16 v[128:129], v241 offset:33280\n" \
"v_mfma_f32_16x16x32_f16 v[8:11], v[136:139], v[164:167], v[8:11]\n" \
"ds_read_b128 v[156:159], v240 offset:40960\n" \
"s_waitcnt lgkmcnt(4)\n" \
"v_mfma_f32_16x16x16_bf16 v[188:191], v[188:189], v[254:255], 0\n" \
"v_mfma_f32_16x16x32_f16 v[20:23], v[140:143], v[164:167], v[20:23]\n" \
"ds_read_b128 v[164:167], v240 offset:45568\n" \
"s_waitcnt lgkmcnt(3)\n" \
"v_mfma_f32_16x16x16_bf16 v[192:195], v[192:193], v[254:255], 0\n" \
"v_mfma_f32_16x16x32_f16 v[44:47], v[140:143], v[168:171], v[44:47]\n" \
"ds_read_b64_tr_b16 v[130:131], v241 offset:41472\n" \
"ds_read_b64_tr_b16 v[132:133], v242 offset:33280\n" \
"buffer_load_dwordx4 v232, s[36:39], s80 offen offset:3072 sc1 lds\n" \
"v_mfma_f32_16x16x32_f16 v[32:35], v[136:139], v[168:171], v[32:35]\n" \
"ds_read_b128 v[168:171], v240 offset:47616\n" \
"v_mfma_f32_16x16x32_f16 v[56:59], v[136:139], v[172:175], v[56:59]\n" \
"v_cvt_pk_f16_f32 v188, v188, v189\n" \
"v_cvt_pk_f16_f32 v189, v190, v191\n" \
"ds_read_b64_tr_b16 v[134:135], v242 offset:41472\n" \
"v_mfma_f32_16x16x32_f16 v[68:71], v[140:143], v[172:175], v[68:71]\n" \
"v_cvt_pk_f16_f32 v190, v192, v193\n" \
"v_cvt_pk_f16_f32 v191, v194, v195\n" \
"ds_read_b128 v[172:175], v240 offset:49664\n" \
"v_mfma_f32_16x16x32_f16 v[92:95], v[140:143], v[176:179], v[92:95]\n" \
"v_add_u32 v188, s91, v188\n" \
"v_add_u32 v189, s91, v189\n" \
"v_add_u32 v190, s91, v190\n" \
"s_add_i32 m0, s45, 0x5000\n" \
"s_add_i32 s81, s47, 0x1000000\n" \
"v_mfma_f32_16x16x32_f16 v[80:83], v[136:139], v[176:179], v[80:83]\n" \
"v_add_u32 v191, s91, v191\n" \
"v_and_b32 v188, s92, v188\n" \
"ds_read_b128 v[176:179], v240 offset:41472\n" \
"buffer_load_dwordx4 v232, s[36:39], s81 offen sc1 lds\n" \
"v_mfma_f32_16x16x32_f16 v[104:107], v[136:139], v[180:183], v[104:107]\n" \
"v_and_b32 v189, s92, v189\n" \
"v_and_b32 v190, s92, v190\n" \
"v_and_b32 v191, s92, v191\n" \
"v_mfma_f32_16x16x32_f16 v[116:119], v[140:143], v[180:183], v[116:119]\n" \
"s_setprio 1\n" \
"buffer_store_dwordx4 v[188:191], v234, s[36:39], s82 offen offset:1024 sc1\n" \
"s_waitcnt vmcnt(7) lgkmcnt(0)\n" \
"s_cmp_eq_u32 s94, 0\n" \
"s_cbranch_scc1 .Lp2_%=\n" \
".Lq2_%=:\n" \
"s_memrealtime s[86:87]\n" \
"s_waitcnt lgkmcnt(0)\n" \
"s_sub_i32 s87, s86, s75\n" \
"s_cmp_lt_i32 s87, 0\n" \
"s_cbranch_scc1 .Lq2_%=\n" \
"s_lshr_b64 s[94:95], s[94:95], 8\n" \
"s_and_b32 s87, s94, 0xff\n" \
"s_add_i32 s75, s86, s87\n" \
".Lp2_%=:\n" \
"s_barrier\n" \
"v_mfma_f32_16x16x32_f16 v[0:3], v[120:123], v[184:187], v[0:3]\n" \
"v_pk_add_f16 v136, v120, v128\n" \
"v_pk_add_f16 v137, v121, v129\n" \
"v_mfma_f32_16x16x32_f16 v[12:15], v[124:127], v[184:187], v[12:15]\n" \
"v_pk_add_f16 v184, v184, v160\n" \
"v_pk_add_f16 v138, v122, v130\n" \
"v_mfma_f32_16x16x32_f16 v[36:39], v[124:127], v[144:147], v[36:39]\n" \
"v_pk_add_f16 v185, v185, v161\n" \
"v_pk_add_f16 v186, v186, v162\n" \
"v_pk_add_f16 v187, v187, v163\n" \
"buffer_store_dwordx4 v[204:207], v235, s[36:39], s62 offen sc1\n" \
"v_mfma_f32_16x16x32_f16 v[24:27], v[120:123], v[144:147], v[24:27]\n" \
"v_pk_add_f16 v144, v144, v164\n" \
"v_pk_add_f16 v139, v123, v131\n" \
"s_add_i32 m0, s44, 0xa000\n" \
"s_add_i32 s80, s46, 0x1400000\n" \
"v_mfma_f32_16x16x32_f16 v[48:51], v[120:123], v[148:151], v[48:51]\n" \
"v_pk_add_f16 v145, v145, v165\n" \
"v_pk_add_f16 v140, v124, v132\n" \
"buffer_load_dwordx4 v232, s[36:39], s80 offen sc1 lds\n" \
"v_mfma_f32_16x16x32_f16 v[60:63], v[124:127], v[148:151], v[60:63]\n" \
"v_pk_add_f16 v146, v146, v166\n" \
"v_pk_add_f16 v147, v147, v167\n" \
"v_mfma_f32_16x16x32_f16 v[84:87], v[124:127], v[152:155], v[84:87]\n" \
"v_pk_add_f16 v148, v148, v168\n" \
"v_pk_add_f16 v141, v125, v133\n" \
"v_mfma_f32_16x16x32_f16 v[72:75], v[120:123], v[152:155], v[72:75]\n" \
"v_pk_add_f16 v149, v149, v169\n" \
"v_pk_add_f16 v142, v126, v134\n" \
"v_mfma_f32_16x16x32_f16 v[96:99], v[120:123], v[156:159], v[96:99]\n" \
"v_pk_add_f16 v150, v150, v170\n" \
"v_pk_add_f16 v151, v151, v171\n" \
"ds_read_b64_tr_b16 v[188:189], v236 offset:4096\n" \
"v_mfma_f32_16x16x32_f16 v[108:111], v[124:127], v[156:159], v[108:111]\n" \
"v_pk_add_f16 v152, v152, v172\n" \
"v_pk_add_f16 v143, v127, v135\n" \
"ds_read_b64_tr_b16 v[192:193], v237 offset:4096\n" \
"v_mfma_f32_16x16x32_f16 v[4:7], v[128:131], v[160:163], v[4:7]\n" \
"v_pk_add_f16 v153, v153, v173\n" \
"ds_read_b64_tr_b16 v[120:121], v241 offset:0\n" \
"buffer_load_dwordx4 v232, s[36:39], s80 offen offset:1024 sc1 lds\n" \
"v_mfma_f32_16x16x32_f16 v[16:19], v[132:135], v[160:163], v[16:19]\n" \
"v_pk_add_f16 v154, v154, v174\n" \
"ds_read_b128 v[180:183], v240 offset:2560\n" \
"v_mfma_f32_16x16x32_f16 v[40:43], v[132:135], v[164:167], v[40:43]\n" \
"v_pk_add_f16 v155, v155, v175\n" \
"ds_read_b128 v[160:163], v240 offset:2048\n" \
"v_mfma_f32_16x16x32_f16 v[28:31], v[128:131], v[164:167], v[28:31]\n" \
"v_pk_add_f16 v156, v156, v176\n" \
"ds_read_b64_tr_b16 v[122:123], v241 offset:8192\n" \
"v_mfma_f32_16x16x32_f16 v[52:55], v[128:131], v[168:171], v[52:55]\n" \
"v_pk_add_f16 v157, v157, v177\n" \
"ds_read_b128 v[164:167], v240 offset:4096\n" \
"s_waitcnt lgkmcnt(6)\n" \
"v_mfma_f32_16x16x16_bf16 v[188:191], v[188:189], v[254:255], 0\n" \
"v_mfma_f32_16x16x32_f16 v[64:67], v[132:135], v[168:171], v[64:67]\n" \
"v_pk_add_f16 v158, v158, v178\n" \
"ds_read_b64_tr_b16 v[124:125], v242 offset:0\n" \
"s_waitcnt lgkmcnt(6)\n" \
"v_mfma_f32_16x16x16_bf16 v[192:195], v[192:193], v[254:255], 0\n" \
"v_mfma_f32_16x16x32_f16 v[88:91], v[132:135], v[172:175], v[88:91]\n" \
"v_pk_add_f16 v159, v159, v179\n" \
"ds_read_b128 v[168:171], v240 offset:6144\n" \
"buffer_load_dwordx4 v232, s[36:39], s80 offen offset:2048 sc1 lds\n" \
"v_mfma_f32_16x16x32_f16 v[76:79], v[128:131], v[172:175], v[76:79]\n" \
"s_setprio 0\n" \
"ds_read_b64_tr_b16 v[126:127], v242 offset:8192\n" \
"v_mfma_f32_16x16x32_f16 v[100:103], v[128:131], v[176:179], v[100:103]\n" \
"v_cvt_pk_f16_f32 v188, v188, v189\n" \
"ds_read_b128 v[172:175], v240 offset:8192\n" \
"v_mfma_f32_16x16x32_f16 v[112:115], v[132:135], v[176:179], v[112:115]\n" \
"v_cvt_pk_f16_f32 v189, v190, v191\n" \
"ds_read_b64_tr_b16 v[128:129], v241 offset:512\n" \
"v_mfma_f32_16x16x32_f16 v[8:11], v[136:139], v[184:187], v[8:11]\n" \
"v_cvt_pk_f16_f32 v190, v192, v193\n" \
"ds_read_b128 v[176:179], v240 offset:0\n" \
"v_mfma_f32_16x16x32_f16 v[20:23], v[140:143], v[184:187], v[20:23]\n" \
"v_cvt_pk_f16_f32 v191, v194, v195\n" \
"v_add_u32 v188, s91, v188\n" \
"ds_read_b128 v[184:187], v240 offset:4608\n" \
"v_mfma_f32_16x16x32_f16 v[44:47], v[140:143], v[144:147], v[44:47]\n" \
"ds_read_b64_tr_b16 v[130:131], v241 offset:8704\n" \
"ds_read_b64_tr_b16 v[132:133], v242 offset:512\n" \
"buffer_load_dwordx4 v232, s[36:39], s80 offen offset:3072 sc1 lds\n" \
"v_mfma_f32_16x16x32_f16 v[32:35], v[136:139], v[144:147], v[32:35]\n" \
"v_add_u32 v189, s91, v189\n" \
"v_add_u32 v190, s91, v190\n" \
"ds_read_b128 v[144:147], v240 offset:6656\n" \
"v_mfma_f32_16x16x32_f16 v[56:59], v[136:139], v[148:151], v[56:59]\n" \
"v_add_u32 v191, s91, v191\n" \
"ds_read_b64_tr_b16 v[134:135], v242 offset:8704\n" \
"v_mfma_f32_16x16x32_f16 v[68:71], v[140:143], v[148:151], v[68:71]\n" \
"v_and_b32 v188, s92, v188\n" \
"ds_read_b128 v[148:151], v240 offset:8704\n" \
"v_mfma_f32_16x16x32_f16 v[92:95], v[140:143], v[152:155], v[92:95]\n" \
"v_and_b32 v189, s92, v189\n" \
"s_add_i32 m0, s45, 0xa000\n" \
"s_add_i32 s81, s47, 0x1400000\n" \
"s_add_i32 s82, s50, 0x3000000\n" \
"v_mfma_f32_16x16x32_f16 v[80:83], v[136:139], v[152:155], v[80:83]\n" \
"v_and_b32 v190, s92, v190\n" \
"v_and_b32 v191, s92, v191\n" \
"ds_read_b128 v[152:155], v240 offset:512\n" \
"buffer_load_dwordx4 v232, s[36:39], s81 offen sc1 lds\n" \
"v_mfma_f32_16x16x32_f16 v[104:107], v[136:139], v[156:159], v[104:107]\n" \
"buffer_store_dwordx4 v[188:191], v234, s[36:39], s82 offen sc1\n" \
"v_mfma_f32_16x16x32_f16 v[116:119], v[140:143], v[156:159], v[116:119]\n" \
"s_setprio 1\n" \
"s_waitcnt vmcnt(7) lgkmcnt(0)\n" \
"s_cmp_eq_u32 s94, 0\n" \
"s_cbranch_scc1 .Lp3_%=\n" \
".Lq3_%=:\n" \
"s_memrealtime s[86:87]\n" \
"s_waitcnt lgkmcnt(0)\n" \
"s_sub_i32 s87, s86, s75\n" \
"s_cmp_lt_i32 s87, 0\n" \
"s_cbranch_scc1 .Lq3_%=\n" \
"s_lshr_b64 s[94:95], s[94:95], 8\n" \
"s_and_b32 s87, s94, 0xff\n" \
"s_add_i32 s75, s86, s87\n" \
".Lp3_%=:\n" \
"s_barrier\n" \
"v_mfma_f32_16x16x32_f16 v[0:3], v[120:123], v[160:163], v[0:3]\n" \
"v_pk_add_f16 v136, v120, v128\n" \
"v_pk_add_f16 v137, v121, v129\n" \
"v_mfma_f32_16x16x32_f16 v[12:15], v[124:127], v[160:163], v[12:15]\n" \
"v_pk_add_f16 v160, v160, v180\n" \
"v_pk_add_f16 v138, v122, v130\n" \
"v_mfma_f32_16x16x32_f16 v[36:39], v[124:127], v[164:167], v[36:39]\n" \
"v_pk_add_f16 v161, v161, v181\n" \
"v_pk_add_f16 v162, v162, v182\n" \
"v_pk_add_f16 v163, v163, v183\n" \
"v_mfma_f32_16x16x32_f16 v[24:27], v[120:123], v[164:167], v[24:27]\n" \
"v_pk_add_f16 v164, v164, v184\n" \
"v_pk_add_f16 v139, v123, v131\n" \
"s_add_i32 m0, s44, 0x0\n" \
"s_add_i32 s80, s46, 0x1800000\n" \
"v_mfma_f32_16x16x32_f16 v[48:51], v[120:123], v[168:171], v[48:51]\n" \
"v_pk_add_f16 v165, v165, v185\n" \
"v_pk_add_f16 v140, v124, v132\n" \
"buffer_load_dwordx4 v232, s[36:39], s80 offen sc1 lds\n" \
"v_mfma_f32_16x16x32_f16 v[60:63], v[124:127], v[168:171], v[60:63]\n" \
"v_pk_add_f16 v166, v166, v186\n" \
"v_pk_add_f16 v167, v167, v187\n" \
"v_mfma_f32_16x16x32_f16 v[84:87], v[124:127], v[172:175], v[84:87]\n" \
"v_pk_add_f16 v168, v168, v144\n" \
"v_pk_add_f16 v141, v125, v133\n" \
"v_mfma_f32_16x16x32_f16 v[72:75], v[120:123], v[172:175], v[72:75]\n" \
"v_pk_add_f16 v169, v169, v145\n" \
"v_pk_add_f16 v142, v126, v134\n" \
"v_mfma_f32_16x16x32_f16 v[96:99], v[120:123], v[176:179], v[96:99]\n" \
"v_pk_add_f16 v170, v170, v146\n" \
"v_pk_add_f16 v171, v171, v147\n" \
"ds_read_b64_tr_b16 v[188:189], v238 offset:4096\n" \
"v_mfma_f32_16x16x32_f16 v[108:111], v[124:127], v[176:179], v[108:111]\n" \
"v_pk_add_f16 v172, v172, v148\n" \
"v_pk_add_f16 v143, v127, v135\n" \
"ds_read_b64_tr_b16 v[192:193], v239 offset:4096\n" \
"v_mfma_f32_16x16x32_f16 v[4:7], v[128:131], v[180:183], v[4:7]\n" \
"v_pk_add_f16 v173, v173, v149\n" \
"ds_read_b64_tr_b16 v[120:121], v241 offset:16384\n" \
"buffer_load_dwordx4 v232, s[36:39], s80 offen offset:1024 sc1 lds\n" \
"v_mfma_f32_16x16x32_f16 v[16:19], v[132:135], v[180:183], v[16:19]\n" \
"v_pk_add_f16 v174, v174, v150\n" \
"ds_read_b128 v[156:159], v240 offset:23040\n" \
"v_mfma_f32_16x16x32_f16 v[40:43], v[132:135], v[184:187], v[40:43]\n" \
"v_pk_add_f16 v175, v175, v151\n" \
"ds_read_b128 v[180:183], v240 offset:22528\n" \
"v_mfma_f32_16x16x32_f16 v[28:31], v[128:131], v[184:187], v[28:31]\n" \
"v_pk_add_f16 v176, v176, v152\n" \
"ds_read_b64_tr_b16 v[122:123], v241 offset:24576\n" \
"v_mfma_f32_16x16x32_f16 v[52:55], v[128:131], v[144:147], v[52:55]\n" \
"v_pk_add_f16 v177, v177, v153\n" \
"ds_read_b128 v[184:187], v240 offset:24576\n" \
"s_waitcnt lgkmcnt(6)\n" \
"v_mfma_f32_16x16x16_bf16 v[188:191], v[188:189], v[254:255], 0\n" \
"v_mfma_f32_16x16x32_f16 v[64:67], v[132:135], v[144:147], v[64:67]\n" \
"v_pk_add_f16 v178, v178, v154\n" \
"ds_read_b64_tr_b16 v[124:125], v242 offset:16384\n" \
"s_waitcnt lgkmcnt(6)\n" \
"v_mfma_f32_16x16x16_bf16 v[192:195], v[192:193], v[254:255], 0\n" \
"v_mfma_f32_16x16x32_f16 v[88:91], v[132:135], v[148:151], v[88:91]\n" \
"v_pk_add_f16 v179, v179, v155\n" \
"ds_read_b128 v[144:147], v240 offset:26624\n" \
"buffer_load_dwordx4 v232, s[36:39], s80 offen offset:2048 sc1 lds\n" \
"v_mfma_f32_16x16x32_f16 v[76:79], v[128:131], v[148:151], v[76:79]\n" \
"s_setprio 0\n" \
"ds_read_b64_tr_b16 v[126:127], v242 offset:24576\n" \
"v_mfma_f32_16x16x32_f16 v[100:103], v[128:131], v[152:155], v[100:103]\n" \
"v_cvt_pk_f16_f32 v188, v188, v189\n" \
"ds_read_b128 v[148:151], v240 offset:28672\n" \
"v_mfma_f32_16x16x32_f16 v[112:115], v[132:135], v[152:155], v[112:115]\n" \
"v_cvt_pk_f16_f32 v189, v190, v191\n" \
"ds_read_b64_tr_b16 v[128:129], v241 offset:16896\n" \
"v_mfma_f32_16x16x32_f16 v[8:11], v[136:139], v[160:163], v[8:11]\n" \
"v_cvt_pk_f16_f32 v190, v192, v193\n" \
"ds_read_b128 v[152:155], v240 offset:20480\n" \
"v_mfma_f32_16x16x32_f16 v[20:23], v[140:143], v[160:163], v[20:23]\n" \
"v_cvt_pk_f16_f32 v191, v194, v195\n" \
"v_add_u32 v188, s91, v188\n" \
"ds_read_b128 v[160:163], v240 offset:25088\n" \
"v_mfma_f32_16x16x32_f16 v[44:47], v[140:143], v[164:167], v[44:47]\n" \
"ds_read_b64_tr_b16 v[130:131], v241 offset:25088\n" \
"ds_read_b64_tr_b16 v[132:133], v242 offset:16896\n" \
"buffer_load_dwordx4 v232, s[36:39], s80 offen offset:3072 sc1 lds\n" \
"v_mfma_f32_16x16x32_f16 v[32:35], v[136:139], v[164:167], v[32:35]\n" \
"v_add_u32 v189, s91, v189\n" \
"v_add_u32 v190, s91, v190\n" \
"ds_read_b128 v[164:167], v240 offset:27136\n" \
"v_mfma_f32_16x16x32_f16 v[56:59], v[136:139], v[168:171], v[56:59]\n" \
"v_add_u32 v191, s91, v191\n" \
"ds_read_b64_tr_b16 v[134:135], v242 offset:25088\n" \
"v_mfma_f32_16x16x32_f16 v[68:71], v[140:143], v[168:171], v[68:71]\n" \
"v_and_b32 v188, s92, v188\n" \
"ds_read_b128 v[168:171], v240 offset:29184\n" \
"v_mfma_f32_16x16x32_f16 v[92:95], v[140:143], v[172:175], v[92:95]\n" \
"v_and_b32 v189, s92, v189\n" \
"s_add_i32 m0, s45, 0x0\n" \
"s_add_i32 s81, s47, 0x1800000\n" \
"v_mfma_f32_16x16x32_f16 v[80:83], v[136:139], v[172:175], v[80:83]\n" \
"v_and_b32 v190, s92, v190\n" \
"v_and_b32 v191, s92, v191\n" \
"ds_read_b128 v[172:175], v240 offset:20992\n" \
"buffer_load_dwordx4 v232, s[36:39], s81 offen sc1 lds\n" \
"v_mfma_f32_16x16x32_f16 v[104:107], v[136:139], v[176:179], v[104:107]\n" \
"buffer_store_dwordx4 v[188:191], v234, s[36:39], s82 offen offset:1024 sc1\n" \
"v_mfma_f32_16x16x32_f16 v[116:119], v[140:143], v[176:179], v[116:119]\n" \
"s_setprio 1\n" \
"s_waitcnt vmcnt(7) lgkmcnt(0)\n" \
"s_cmp_eq_u32 s94, 0\n" \
"s_cbranch_scc1 .Lp4_%=\n" \
".Lq4_%=:\n" \
"s_memrealtime s[86:87]\n" \
"s_waitcnt lgkmcnt(0)\n" \
"s_sub_i32 s87, s86, s75\n" \
"s_cmp_lt_i32 s87, 0\n" \
"s_cbranch_scc1 .Lq4_%=\n" \
"s_lshr_b64 s[94:95], s[94:95], 8\n" \
"s_and_b32 s87, s94, 0xff\n" \
"s_add_i32 s75, s86, s87\n" \
".Lp4_%=:\n" \
"s_barrier\n" \
"v_mfma_f32_16x16x32_f16 v[0:3], v[120:123], v[180:183], v[0:3]\n" \
"v_pk_add_f16 v136, v120, v128\n" \
"v_pk_add_f16 v137, v121, v129\n" \
"v_mfma_f32_16x16x32_f16 v[12:15], v[124:127], v[180:183], v[12:15]\n" \
"v_pk_add_f16 v180, v180, v156\n" \
"v_pk_add_f16 v138, v122, v130\n" \
"s_mov_b32 s84, 0x8004000\n" \
"v_mfma_f32_16x16x32_f16 v[36:39], v[124:127], v[184:187], v[36:39]\n" \
"v_pk_add_f16 v181, v181, v157\n" \
"v_pk_add_f16 v182, v182, v158\n" \
"v_pk_add_f16 v183, v183, v159\n" \
"buffer_load_dword v253, v252, s[36:39], s84 offen sc1\n" \
"buffer_store_dwordx4 v[208:211], v235, s[36:39], s63 offen sc1\n" \
"v_mfma_f32_16x16x32_f16 v[24:27], v[120:123], v[184:187], v[24:27]\n" \
"v_pk_add_f16 v184, v184, v160\n" \
"v_pk_add_f16 v139, v123, v131\n" \
"s_add_i32 m0, s44, 0x5000\n" \
"s_add_i32 s80, s46, 0x1c00000\n" \
"v_mfma_f32_16x16x32_f16 v[48:51], v[120:123], v[144:147], v[48:51]\n" \
"v_pk_add_f16 v185, v185, v161\n" \
"v_pk_add_f16 v140, v124, v132\n" \
"buffer_load_dwordx4 v232, s[36:39], s80 offen sc1 lds\n" \
"v_mfma_f32_16x16x32_f16 v[60:63], v[124:127], v[144:147], v[60:63]\n" \
"v_pk_add_f16 v186, v186, v162\n" \
"v_pk_add_f16 v187, v187, v163\n" \
"v_mfma_f32_16x16x32_f16 v[84:87], v[124:127], v[148:151], v[84:87]\n" \
"v_pk_add_f16 v144, v144, v164\n" \
"v_pk_add_f16 v141, v125, v133\n" \
"v_mfma_f32_16x16x32_f16 v[72:75], v[120:123], v[148:151], v[72:75]\n" \
"v_pk_add_f16 v145, v145, v165\n" \
"v_pk_add_f16 v142, v126, v134\n" \
"v_mfma_f32_16x16x32_f16 v[96:99], v[120:123], v[152:155], v[96:99]\n" \
"v_pk_add_f16 v146, v146, v166\n" \
"v_pk_add_f16 v147, v147, v167\n" \
"ds_read_b64_tr_b16 v[188:189], v236 offset:8192\n" \
"v_mfma_f32_16x16x32_f16 v[108:111], v[124:127], v[152:155], v[108:111]\n" \
"v_pk_add_f16 v148, v148, v168\n" \
"v_pk_add_f16 v143, v127, v135\n" \
"ds_read_b64_tr_b16 v[192:193], v237 offset:8192\n" \
"v_mfma_f32_16x16x32_f16 v[4:7], v[128:131], v[156:159], v[4:7]\n" \
"v_pk_add_f16 v149, v149, v169\n" \
"ds_read_b64_tr_b16 v[120:121], v241 offset:32768\n" \
"buffer_load_dwordx4 v232, s[36:39], s80 offen offset:1024 sc1 lds\n" \
"v_mfma_f32_16x16x32_f16 v[16:19], v[132:135], v[156:159], v[16:19]\n" \
"v_pk_add_f16 v150, v150, v170\n" \
"ds_read_b128 v[176:179], v240 offset:43520\n" \
"v_mfma_f32_16x16x32_f16 v[40:43], v[132:135], v[160:163], v[40:43]\n" \
"v_pk_add_f16 v151, v151, v171\n" \
"ds_read_b128 v[156:159], v240 offset:43008\n" \
"v_mfma_f32_16x16x32_f16 v[28:31], v[128:131], v[160:163], v[28:31]\n" \
"v_pk_add_f16 v152, v152, v172\n" \
"ds_read_b64_tr_b16 v[122:123], v241 offset:40960\n" \
"v_mfma_f32_16x16x32_f16 v[52:55], v[128:131], v[164:167], v[52:55]\n" \
"v_pk_add_f16 v153, v153, v173\n" \
"ds_read_b128 v[160:163], v240 offset:45056\n" \
"s_waitcnt lgkmcnt(6)\n" \
"v_mfma_f32_16x16x16_bf16 v[188:191], v[188:189], v[254:255], 0\n" \
"v_mfma_f32_16x16x32_f16 v[64:67], v[132:135], v[164:167], v[64:67]\n" \
"v_pk_add_f16 v154, v154, v174\n" \
"ds_read_b64_tr_b16 v[124:125], v242 offset:32768\n" \
"s_waitcnt lgkmcnt(6)\n" \
"v_mfma_f32_16x16x16_bf16 v[192:195], v[192:193], v[254:255], 0\n" \
"v_mfma_f32_16x16x32_f16 v[88:91], v[132:135], v[168:171], v[88:91]\n" \
"v_pk_add_f16 v155, v155, v175\n" \
"ds_read_b128 v[164:167], v240 offset:47104\n" \
"buffer_load_dwordx4 v232, s[36:39], s80 offen offset:2048 sc1 lds\n" \
"v_mfma_f32_16x16x32_f16 v[76:79], v[128:131], v[168:171], v[76:79]\n" \
"s_setprio 0\n" \
"ds_read_b64_tr_b16 v[126:127], v242 offset:40960\n" \
"v_mfma_f32_16x16x32_f16 v[100:103], v[128:131], v[172:175], v[100:103]\n" \
"v_cvt_pk_f16_f32 v188, v188, v189\n" \
"ds_read_b128 v[168:171], v240 offset:49152\n" \
"v_mfma_f32_16x16x32_f16 v[112:115], v[132:135], v[172:175], v[112:115]\n" \
"v_cvt_pk_f16_f32 v189, v190, v191\n" \
"ds_read_b64_tr_b16 v[128:129], v241 offset:33280\n" \
"v_mfma_f32_16x16x32_f16 v[8:11], v[136:139], v[180:183], v[8:11]\n" \
"v_cvt_pk_f16_f32 v190, v192, v193\n" \
"ds_read_b128 v[172:175], v240 offset:40960\n" \
"v_mfma_f32_16x16x32_f16 v[20:23], v[140:143], v[180:183], v[20:23]\n" \
"v_cvt_pk_f16_f32 v191, v194, v195\n" \
"v_add_u32 v188, s91, v188\n" \
"ds_read_b128 v[180:183], v240 offset:45568\n" \
"v_mfma_f32_16x16x32_f16 v[44:47], v[140:143], v[184:187], v[44:47]\n" \
"ds_read_b64_tr_b16 v[130:131], v241 offset:41472\n" \
"ds_read_b64_tr_b16 v[132:133], v242 offset:33280\n" \
"buffer_load_dwordx4 v232, s[36:39], s80 offen offset:3072 sc1 lds\n" \
"v_mfma_f32_16x16x32_f16 v[32:35], v[136:139], v[184:187], v[32:35]\n" \
"v_add_u32 v189, s91, v189\n" \
"v_add_u32 v190, s91, v190\n" \
"ds_read_b128 v[184:187], v240 offset:47616\n" \
"v_mfma_f32_16x16x32_f16 v[56:59], v[136:139], v[144:147], v[56:59]\n" \
"v_add_u32 v191, s91, v191\n" \
"ds_read_b64_tr_b16 v[134:135], v242 offset:41472\n" \
"v_mfma_f32_16x16x32_f16 v[68:71], v[140:143], v[144:147], v[68:71]\n" \
"v_and_b32 v188, s92, v188\n" \
"v_and_b32 v189, s92, v189\n" \
"ds_read_b128 v[144:147], v240 offset:49664\n" \
"v_mfma_f32_16x16x32_f16 v[92:95], v[140:143], v[148:151], v[92:95]\n" \
"s_waitcnt vmcnt(5)\n" \
"v_cmp_eq_u32 vcc, 0, v253\n" \
"s_cbranch_vccnz .Ls12_%=\n" \
".Lr12_%=:\n" \
"s_add_i32 m0, s45, 0x5000\n" \
"s_add_i32 s81, s47, 0x1c00000\n" \
"s_add_i32 s82, s51, 0x3800000\n" \
"v_mfma_f32_16x16x32_f16 v[80:83], v[136:139], v[148:151], v[80:83]\n" \
"v_and_b32 v190, s92, v190\n" \
"v_and_b32 v191, s92, v191\n" \
"ds_read_b128 v[148:151], v240 offset:41472\n" \
"buffer_load_dwordx4 v232, s[36:39], s81 offen sc1 lds\n" \
"v_mfma_f32_16x16x32_f16 v[104:107], v[136:139], v[152:155], v[104:107]\n" \
"buffer_store_dwordx4 v[188:191], v234, s[36:39], s82 offen sc1\n" \
"v_mfma_f32_16x16x32_f16 v[116:119], v[140:143], v[152:155], v[116:119]\n" \
"s_setprio 1\n" \
"s_waitcnt vmcnt(7) lgkmcnt(0)\n" \
"s_cmp_eq_u32 s94, 0\n" \
"s_cbranch_scc1 .Lp5_%=\n" \
".Lq5_%=:\n" \
"s_memrealtime s[86:87]\n" \
"s_waitcnt lgkmcnt(0)\n" \
"s_sub_i32 s87, s86, s75\n" \
"s_cmp_lt_i32 s87, 0\n" \
"s_cbranch_scc1 .Lq5_%=\n" \
"s_lshr_b64 s[94:95], s[94:95], 8\n" \
"s_and_b32 s87, s94, 0xff\n" \
"s_add_i32 s75, s86, s87\n" \
".Lp5_%=:\n" \
"s_barrier\n" \
"v_mfma_f32_16x16x32_f16 v[0:3], v[120:123], v[156:159], v[0:3]\n" \
"v_pk_add_f16 v136, v120, v128\n" \
"v_pk_add_f16 v137, v121, v129\n" \
"v_mfma_f32_16x16x32_f16 v[12:15], v[124:127], v[156:159], v[12:15]\n" \
"v_pk_add_f16 v156, v156, v176\n" \
"v_pk_add_f16 v138, v122, v130\n" \
"v_mfma_f32_16x16x32_f16 v[36:39], v[124:127], v[160:163], v[36:39]\n" \
"v_pk_add_f16 v157, v157, v177\n" \
"v_pk_add_f16 v158, v158, v178\n" \
"v_pk_add_f16 v159, v159, v179\n" \
"v_mfma_f32_16x16x32_f16 v[24:27], v[120:123], v[160:163], v[24:27]\n" \
"v_pk_add_f16 v160, v160, v180\n" \
"v_pk_add_f16 v139, v123, v131\n" \
"s_add_i32 m0, s44, 0xa000\n" \
"s_add_i32 s80, s46, 0x2000000\n" \
"v_mfma_f32_16x16x32_f16 v[48:51], v[120:123], v[164:167], v[48:51]\n" \
"v_pk_add_f16 v161, v161, v181\n" \
"v_pk_add_f16 v140, v124, v132\n" \
"buffer_load_dwordx4 v232, s[36:39], s80 offen sc1 lds\n" \
"v_mfma_f32_16x16x32_f16 v[60:63], v[124:127], v[164:167], v[60:63]\n" \
"v_pk_add_f16 v162, v162, v182\n" \
"v_pk_add_f16 v163, v163, v183\n" \
"ds_read_b128 v[208:211], v240 offset:4608\n" \
"v_mfma_f32_16x16x32_f16 v[84:87], v[124:127], v[168:171], v[84:87]\n" \
"v_pk_add_f16 v164, v164, v184\n" \
"v_pk_add_f16 v141, v125, v133\n" \
"v_mfma_f32_16x16x32_f16 v[72:75], v[120:123], v[168:171], v[72:75]\n" \
"v_pk_add_f16 v165, v165, v185\n" \
"v_pk_add_f16 v142, v126, v134\n" \
"v_mfma_f32_16x16x32_f16 v[96:99], v[120:123], v[172:175], v[96:99]\n" \
"v_pk_add_f16 v166, v166, v186\n" \
"v_pk_add_f16 v167, v167, v187\n" \
"ds_read_b64_tr_b16 v[188:189], v238 offset:8192\n" \
"v_mfma_f32_16x16x32_f16 v[108:111], v[124:127], v[172:175], v[108:111]\n" \
"v_pk_add_f16 v168, v168, v144\n" \
"v_pk_add_f16 v143, v127, v135\n" \
"ds_read_b64_tr_b16 v[192:193], v239 offset:8192\n" \
"v_mfma_f32_16x16x32_f16 v[4:7], v[128:131], v[176:179], v[4:7]\n" \
"v_pk_add_f16 v169, v169, v145\n" \
"ds_read_b64_tr_b16 v[120:121], v241 offset:0\n" \
"buffer_load_dwordx4 v232, s[36:39], s80 offen offset:1024 sc1 lds\n" \
"v_mfma_f32_16x16x32_f16 v[16:19], v[132:135], v[176:179], v[16:19]\n" \
"v_pk_add_f16 v170, v170, v146\n" \
"ds_read_b128 v[152:155], v240 offset:2560\n" \
"v_mfma_f32_16x16x32_f16 v[40:43], v[132:135], v[180:183], v[40:43]\n" \
"v_pk_add_f16 v171, v171, v147\n" \
"ds_read_b128 v[176:179], v240 offset:2048\n" \
"v_mfma_f32_16x16x32_f16 v[28:31], v[128:131], v[180:183], v[28:31]\n" \
"v_pk_add_f16 v172, v172, v148\n" \
"ds_read_b64_tr_b16 v[122:123], v241 offset:8192\n" \
"v_mfma_f32_16x16x32_f16 v[52:55], v[128:131], v[184:187], v[52:55]\n" \
"v_pk_add_f16 v173, v173, v149\n" \
"ds_read_b128 v[180:183], v240 offset:4096\n" \
"s_waitcnt lgkmcnt(6)\n" \
"v_mfma_f32_16x16x16_bf16 v[188:191], v[188:189], v[254:255], 0\n" \
"v_mfma_f32_16x16x32_f16 v[64:67], v[132:135], v[184:187], v[64:67]\n" \
"v_pk_add_f16 v174, v174, v150\n" \
"ds_read_b64_tr_b16 v[124:125], v242 offset:0\n" \
"s_waitcnt lgkmcnt(6)\n" \
"v_mfma_f32_16x16x16_bf16 v[192:195], v[192:193], v[254:255], 0\n" \
"v_mfma_f32_16x16x32_f16 v[88:91], v[132:135], v[144:147], v[88:91]\n" \
"v_pk_add_f16 v175, v175, v151\n" \
"ds_read_b128 v[184:187], v240 offset:6144\n" \
"buffer_load_dwordx4 v232, s[36:39], s80 offen offset:2048 sc1 lds\n" \
"v_mfma_f32_16x16x32_f16 v[76:79], v[128:131], v[144:147], v[76:79]\n" \
"s_setprio 0\n" \
"ds_read_b64_tr_b16 v[126:127], v242 offset:8192\n" \
"v_mfma_f32_16x16x32_f16 v[100:103], v[128:131], v[148:151], v[100:103]\n" \
"v_cvt_pk_f16_f32 v188, v188, v189\n" \
"ds_read_b128 v[144:147], v240 offset:8192\n" \
"v_mfma_f32_16x16x32_f16 v[112:115], v[132:135], v[148:151], v[112:115]\n" \
"v_cvt_pk_f16_f32 v189, v190, v191\n" \
"ds_read_b64_tr_b16 v[128:129], v241 offset:512\n" \
"v_mfma_f32_16x16x32_f16 v[8:11], v[136:139], v[156:159], v[8:11]\n" \
"v_cvt_pk_f16_f32 v190, v192, v193\n" \
"ds_read_b128 v[148:151], v240 offset:0\n" \
"v_mfma_f32_16x16x32_f16 v[20:23], v[140:143], v[156:159], v[20:23]\n" \
"v_cvt_pk_f16_f32 v191, v194, v195\n" \
"v_add_u32 v188, s91, v188\n" \
"ds_read_b128 v[156:159], v240 offset:6656\n" \
"v_mfma_f32_16x16x32_f16 v[44:47], v[140:143], v[160:163], v[44:47]\n" \
"ds_read_b64_tr_b16 v[130:131], v241 offset:8704\n" \
"ds_read_b64_tr_b16 v[132:133], v242 offset:512\n" \
"buffer_load_dwordx4 v232, s[36:39], s80 offen offset:3072 sc1 lds\n" \
"v_mfma_f32_16x16x32_f16 v[32:35], v[136:139], v[160:163], v[32:35]\n" \
"v_add_u32 v189, s91, v189\n" \
"v_add_u32 v190, s91, v190\n" \
"ds_read_b128 v[160:163], v240 offset:8704\n" \
"v_mfma_f32_16x16x32_f16 v[56:59], v[136:139], v[164:167], v[56:59]\n" \
"v_add_u32 v191, s91, v191\n" \
"ds_read_b64_tr_b16 v[134:135], v242 offset:8704\n" \
"v_mfma_f32_16x16x32_f16 v[68:71], v[140:143], v[164:167], v[68:71]\n" \
"v_and_b32 v188, s92, v188\n" \
"ds_read_b128 v[164:167], v240 offset:512\n" \
"v_mfma_f32_16x16x32_f16 v[92:95], v[140:143], v[168:171], v[92:95]\n" \
"v_and_b32 v189, s92, v189\n" \
"s_add_i32 m0, s45, 0xa000\n" \
"s_add_i32 s81, s47, 0x2000000\n" \
"v_mfma_f32_16x16x32_f16 v[80:83], v[136:139], v[168:171], v[80:83]\n" \
"v_and_b32 v190, s92, v190\n" \
"v_and_b32 v191, s92, v191\n" \
"buffer_load_dwordx4 v232, s[36:39], s81 offen sc1 lds\n" \
"v_mfma_f32_16x16x32_f16 v[104:107], v[136:139], v[172:175], v[104:107]\n" \
"buffer_store_dwordx4 v[188:191], v234, s[36:39], s82 offen offset:1024 sc1\n" \
"v_mfma_f32_16x16x32_f16 v[116:119], v[140:143], v[172:175], v[116:119]\n" \
"s_setprio 1\n" \
"s_waitcnt vmcnt(7) lgkmcnt(0)\n" \
"s_cmp_eq_u32 s94, 0\n" \
"s_cbranch_scc1 .Lp6_%=\n" \
".Lq6_%=:\n" \
"s_memrealtime s[86:87]\n" \
"s_waitcnt lgkmcnt(0)\n" \
"s_sub_i32 s87, s86, s75\n" \
"s_cmp_lt_i32 s87, 0\n" \
"s_cbranch_scc1 .Lq6_%=\n" \
"s_lshr_b64 s[94:95], s[94:95], 8\n" \
"s_and_b32 s87, s94, 0xff\n" \
"s_add_i32 s75, s86, s87\n" \
".Lp6_%=:\n" \
"s_barrier\n" \
"v_mfma_f32_16x16x32_f16 v[0:3], v[120:123], v[176:179], v[0:3]\n" \
"v_pk_add_f16 v136, v120, v128\n" \
"v_pk_add_f16 v137, v121, v129\n" \
"v_mfma_f32_16x16x32_f16 v[12:15], v[124:127], v[176:179], v[12:15]\n" \
"v_pk_add_f16 v176, v176, v152\n" \
"v_pk_add_f16 v138, v122, v130\n" \
"v_mfma_f32_16x16x32_f16 v[36:39], v[124:127], v[180:183], v[36:39]\n" \
"v_pk_add_f16 v177, v177, v153\n" \
"v_pk_add_f16 v178, v178, v154\n" \
"v_pk_add_f16 v179, v179, v155\n" \
"buffer_store_dwordx4 v[212:215], v235, s[36:39], s64 offen sc1\n" \
"v_mfma_f32_16x16x32_f16 v[24:27], v[120:123], v[180:183], v[24:27]\n" \
"v_pk_add_f16 v180, v180, v208\n" \
"v_pk_add_f16 v139, v123, v131\n" \
"s_add_i32 m0, s44, 0x0\n" \
"s_add_i32 s80, s46, 0x2400000\n" \
"v_mfma_f32_16x16x32_f16 v[48:51], v[120:123], v[184:187], v[48:51]\n" \
"v_pk_add_f16 v181, v181, v209\n" \
"v_pk_add_f16 v140, v124, v132\n" \
"buffer_load_dwordx4 v232, s[36:39], s80 offen sc1 lds\n" \
"v_mfma_f32_16x16x32_f16 v[60:63], v[124:127], v[184:187], v[60:63]\n" \
"v_pk_add_f16 v182, v182, v210\n" \
"v_pk_add_f16 v183, v183, v211\n" \
"ds_read_b128 v[172:175], v240 offset:25088\n" \
"v_mfma_f32_16x16x32_f16 v[84:87], v[124:127], v[144:147], v[84:87]\n" \
"v_pk_add_f16 v184, v184, v156\n" \
"v_pk_add_f16 v141, v125, v133\n" \
"v_mfma_f32_16x16x32_f16 v[72:75], v[120:123], v[144:147], v[72:75]\n" \
"v_pk_add_f16 v185, v185, v157\n" \
"v_pk_add_f16 v142, v126, v134\n" \
"v_mfma_f32_16x16x32_f16 v[96:99], v[120:123], v[148:151], v[96:99]\n" \
"v_pk_add_f16 v186, v186, v158\n" \
"v_pk_add_f16 v187, v187, v159\n" \
"ds_read_b64_tr_b16 v[188:189], v236 offset:12288\n" \
"v_mfma_f32_16x16x32_f16 v[108:111], v[124:127], v[148:151], v[108:111]\n" \
"v_pk_add_f16 v144, v144, v160\n" \
"v_pk_add_f16 v143, v127, v135\n" \
"ds_read_b64_tr_b16 v[192:193], v237 offset:12288\n" \
"v_mfma_f32_16x16x32_f16 v[4:7], v[128:131], v[152:155], v[4:7]\n" \
"v_pk_add_f16 v145, v145, v161\n" \
"ds_read_b64_tr_b16 v[120:121], v241 offset:16384\n" \
"buffer_load_dwordx4 v232, s[36:39], s80 offen offset:1024 sc1 lds\n" \
"v_mfma_f32_16x16x32_f16 v[16:19], v[132:135], v[152:155], v[16:19]\n" \
"v_pk_add_f16 v146, v146, v162\n" \
"ds_read_b128 v[168:171], v240 offset:23040\n" \
"v_mfma_f32_16x16x32_f16 v[40:43], v[132:135], v[208:211], v[40:43]\n" \
"v_pk_add_f16 v147, v147, v163\n" \
"ds_read_b128 v[152:155], v240 offset:22528\n" \
"v_mfma_f32_16x16x32_f16 v[28:31], v[128:131], v[208:211], v[28:31]\n" \
"v_pk_add_f16 v148, v148, v164\n" \
"ds_read_b64_tr_b16 v[122:123], v241 offset:24576\n" \
"v_mfma_f32_16x16x32_f16 v[52:55], v[128:131], v[156:159], v[52:55]\n" \
"v_pk_add_f16 v149, v149, v165\n" \
"ds_read_b128 v[208:211], v240 offset:24576\n" \
"s_waitcnt lgkmcnt(6)\n" \
"v_mfma_f32_16x16x16_bf16 v[188:191], v[188:189], v[254:255], 0\n" \
"v_mfma_f32_16x16x32_f16 v[64:67], v[132:135], v[156:159], v[64:67]\n" \
"v_pk_add_f16 v150, v150, v166\n" \
"ds_read_b64_tr_b16 v[124:125], v242 offset:16384\n" \
"s_waitcnt lgkmcnt(6)\n" \
"v_mfma_f32_16x16x16_bf16 v[192:195], v[192:193], v[254:255], 0\n" \
"v_mfma_f32_16x16x32_f16 v[88:91], v[132:135], v[160:163], v[88:91]\n" \
"v_pk_add_f16 v151, v151, v167\n" \
"ds_read_b128 v[156:159], v240 offset:26624\n" \
"buffer_load_dwordx4 v232, s[36:39], s80 offen offset:2048 sc1 lds\n" \
"v_mfma_f32_16x16x32_f16 v[76:79], v[128:131], v[160:163], v[76:79]\n" \
"s_setprio 0\n" \
"ds_read_b64_tr_b16 v[126:127], v242 offset:24576\n" \
"v_mfma_f32_16x16x32_f16 v[100:103], v[128:131], v[164:167], v[100:103]\n" \
"v_cvt_pk_f16_f32 v188, v188, v189\n" \
"ds_read_b128 v[160:163], v240 offset:28672\n" \
"v_mfma_f32_16x16x32_f16 v[112:115], v[132:135], v[164:167], v[112:115]\n" \
"v_cvt_pk_f16_f32 v189, v190, v191\n" \
"ds_read_b64_tr_b16 v[128:129], v241 offset:16896\n" \
"v_mfma_f32_16x16x32_f16 v[8:11], v[136:139], v[176:179], v[8:11]\n" \
"v_cvt_pk_f16_f32 v190, v192, v193\n" \
"ds_read_b128 v[164:167], v240 offset:20480\n" \
"v_mfma_f32_16x16x32_f16 v[20:23], v[140:143], v[176:179], v[20:23]\n" \
"v_cvt_pk_f16_f32 v191, v194, v195\n" \
"v_add_u32 v188, s91, v188\n" \
"ds_read_b128 v[176:179], v240 offset:27136\n" \
"v_mfma_f32_16x16x32_f16 v[44:47], v[140:143], v[180:183], v[44:47]\n" \
"ds_read_b64_tr_b16 v[130:131], v241 offset:25088\n" \
"ds_read_b64_tr_b16 v[132:133], v242 offset:16896\n" \
"buffer_load_dwordx4 v232, s[36:39], s80 offen offset:3072 sc1 lds\n" \
"v_mfma_f32_16x16x32_f16 v[32:35], v[136:139], v[180:183], v[32:35]\n" \
"v_add_u32 v189, s91, v189\n" \
"v_add_u32 v190, s91, v190\n" \
"ds_read_b128 v[180:183], v240 offset:29184\n" \
"v_mfma_f32_16x16x32_f16 v[56:59], v[136:139], v[184:187], v[56:59]\n" \
"v_add_u32 v191, s91, v191\n" \
"ds_read_b64_tr_b16 v[134:135], v242 offset:25088\n" \
"v_mfma_f32_16x16x32_f16 v[68:71], v[140:143], v[184:187], v[68:71]\n" \
"v_and_b32 v188, s92, v188\n" \
"ds_read_b128 v[184:187], v240 offset:20992\n" \
"v_mfma_f32_16x16x32_f16 v[92:95], v[140:143], v[144:147], v[92:95]\n" \
"v_and_b32 v189, s92, v189\n" \
"s_add_i32 m0, s45, 0x0\n" \
"s_add_i32 s81, s47, 0x2400000\n" \
"s_add_i32 s82, s48, 0x4000000\n" \
"v_mfma_f32_16x16x32_f16 v[80:83], v[136:139], v[144:147], v[80:83]\n" \
"v_and_b32 v190, s92, v190\n" \
"v_and_b32 v191, s92, v191\n" \
"buffer_load_dwordx4 v232, s[36:39], s81 offen sc1 lds\n" \
"v_mfma_f32_16x16x32_f16 v[104:107], v[136:139], v[148:151], v[104:107]\n" \
"buffer_store_dwordx4 v[188:191], v234, s[36:39], s82 offen sc1\n" \
"v_mfma_f32_16x16x32_f16 v[116:119], v[140:143], v[148:151], v[116:119]\n" \
"s_setprio 1\n" \
"s_waitcnt vmcnt(7) lgkmcnt(0)\n" \
"s_cmp_eq_u32 s94, 0\n" \
"s_cbranch_scc1 .Lp7_%=\n" \
".Lq7_%=:\n" \
"s_memrealtime s[86:87]\n" \
"s_waitcnt lgkmcnt(0)\n" \
"s_sub_i32 s87, s86, s75\n" \
"s_cmp_lt_i32 s87, 0\n" \
"s_cbranch_scc1 .Lq7_%=\n" \
"s_lshr_b64 s[94:95], s[94:95], 8\n" \
"s_and_b32 s87, s94, 0xff\n" \
"s_add_i32 s75, s86, s87\n" \
".Lp7_%=:\n" \
"s_barrier\n" \
"v_mfma_f32_16x16x32_f16 v[0:3], v[120:123], v[152:155], v[0:3]\n" \
"v_pk_add_f16 v136, v120, v128\n" \
"v_pk_add_f16 v137, v121, v129\n" \
"v_mfma_f32_16x16x32_f16 v[12:15], v[124:127], v[152:155], v[12:15]\n" \
"v_pk_add_f16 v152, v152, v168\n" \
"v_pk_add_f16 v138, v122, v130\n" \
"v_mfma_f32_16x16x32_f16 v[36:39], v[124:127], v[208:211], v[36:39]\n" \
"v_pk_add_f16 v153, v153, v169\n" \
"v_pk_add_f16 v154, v154, v170\n" \
"v_pk_add_f16 v155, v155, v171\n" \
"v_mfma_f32_16x16x32_f16 v[24:27], v[120:123], v[208:211], v[24:27]\n" \
"v_pk_add_f16 v208, v208, v172\n" \
"v_pk_add_f16 v139, v123, v131\n" \
"s_add_i32 m0, s44, 0x5000\n" \
"s_add_i32 s80, s46, 0x2800000\n" \
"v_mfma_f32_16x16x32_f16 v[48:51], v[120:123], v[156:159], v[48:51]\n" \
"v_pk_add_f16 v209, v209, v173\n" \
"v_pk_add_f16 v140, v124, v132\n" \
"buffer_load_dwordx4 v232, s[36:39], s80 offen sc1 lds\n" \
"v_mfma_f32_16x16x32_f16 v[60:63], v[124:127], v[156:159], v[60:63]\n" \
"v_pk_add_f16 v210, v210, v174\n" \
"v_pk_add_f16 v211, v211, v175\n" \
"ds_read_b128 v[148:151], v240 offset:45568\n" \
"v_mfma_f32_16x16x32_f16 v[84:87], v[124:127], v[160:163], v[84:87]\n" \
"v_pk_add_f16 v156, v156, v176\n" \
"v_pk_add_f16 v141, v125, v133\n" \
"v_mfma_f32_16x16x32_f16 v[72:75], v[120:123], v[160:163], v[72:75]\n" \
"v_pk_add_f16 v157, v157, v177\n" \
"v_pk_add_f16 v142, v126, v134\n" \
"v_mfma_f32_16x16x32_f16 v[96:99], v[120:123], v[164:167], v[96:99]\n" \
"v_pk_add_f16 v158, v158, v178\n" \
"v_pk_add_f16 v159, v159, v179\n" \
"ds_read_b64_tr_b16 v[188:189], v238 offset:12288\n" \
"v_mfma_f32_16x16x32_f16 v[108:111], v[124:127], v[164:167], v[108:111]\n" \
"v_pk_add_f16 v160, v160, v180\n" \
"v_pk_add_f16 v143, v127, v135\n" \
"ds_read_b64_tr_b16 v[192:193], v239 offset:12288\n" \
"v_mfma_f32_16x16x32_f16 v[4:7], v[128:131], v[168:171], v[4:7]\n" \
"v_pk_add_f16 v161, v161, v181\n" \
"ds_read_b64_tr_b16 v[120:121], v241 offset:32768\n" \
"buffer_load_dwordx4 v232, s[36:39], s80 offen offset:1024 sc1 lds\n" \
"v_mfma_f32_16x16x32_f16 v[16:19], v[132:135], v[168:171], v[16:19]\n" \
"v_pk_add_f16 v162, v162, v182\n" \
"ds_read_b128 v[144:147], v240 offset:43520\n" \
"v_mfma_f32_16x16x32_f16 v[40:43], v[132:135], v[172:175], v[40:43]\n" \
"v_pk_add_f16 v163, v163, v183\n" \
"ds_read_b128 v[168:171], v240 offset:43008\n" \
"v_mfma_f32_16x16x32_f16 v[28:31], v[128:131], v[172:175], v[28:31]\n" \
"v_pk_add_f16 v164, v164, v184\n" \
"ds_read_b64_tr_b16 v[122:123], v241 offset:40960\n" \
"v_mfma_f32_16x16x32_f16 v[52:55], v[128:131], v[176:179], v[52:55]\n" \
"v_pk_add_f16 v165, v165, v185\n" \
"ds_read_b128 v[172:175], v240 offset:45056\n" \
"s_waitcnt lgkmcnt(6)\n" \
"v_mfma_f32_16x16x16_bf16 v[188:191], v[188:189], v[254:255], 0\n" \
"v_mfma_f32_16x16x32_f16 v[64:67], v[132:135], v[176:179], v[64:67]\n" \
"v_pk_add_f16 v166, v166, v186\n" \
"ds_read_b64_tr_b16 v[124:125], v242 offset:32768\n" \
"s_waitcnt lgkmcnt(6)\n" \
"v_mfma_f32_16x16x16_bf16 v[192:195], v[192:193], v[254:255], 0\n" \
"v_mfma_f32_16x16x32_f16 v[88:91], v[132:135], v[180:183], v[88:91]\n" \
"v_pk_add_f16 v167, v167, v187\n" \
"ds_read_b128 v[176:179], v240 offset:47104\n" \
"buffer_load_dwordx4 v232, s[36:39], s80 offen offset:2048 sc1 lds\n" \
"v_mfma_f32_16x16x32_f16 v[76:79], v[128:131], v[180:183], v[76:79]\n" \
"s_setprio 0\n" \
"ds_read_b64_tr_b16 v[126:127], v242 offset:40960\n" \
"v_mfma_f32_16x16x32_f16 v[100:103], v[128:131], v[184:187], v[100:103]\n" \
"v_cvt_pk_f16_f32 v188, v188, v189\n" \
"ds_read_b128 v[180:183], v240 offset:49152\n" \
"v_mfma_f32_16x16x32_f16 v[112:115], v[132:135], v[184:187], v[112:115]\n" \
"v_cvt_pk_f16_f32 v189, v190, v191\n" \
"ds_read_b64_tr_b16 v[128:129], v241 offset:33280\n" \
"v_mfma_f32_16x16x32_f16 v[8:11], v[136:139], v[152:155], v[8:11]\n" \
"v_cvt_pk_f16_f32 v190, v192, v193\n" \
"ds_read_b128 v[184:187], v240 offset:40960\n" \
"v_mfma_f32_16x16x32_f16 v[20:23], v[140:143], v[152:155], v[20:23]\n" \
"v_cvt_pk_f16_f32 v191, v194, v195\n" \
"v_add_u32 v188, s91, v188\n" \
"ds_read_b128 v[152:155], v240 offset:47616\n" \
"v_mfma_f32_16x16x32_f16 v[44:47], v[140:143], v[208:211], v[44:47]\n" \
"ds_read_b64_tr_b16 v[130:131], v241 offset:41472\n" \
"ds_read_b64_tr_b16 v[132:133], v242 offset:33280\n" \
"buffer_load_dwordx4 v232, s[36:39], s80 offen offset:3072 sc1 lds\n" \
"v_mfma_f32_16x16x32_f16 v[32:35], v[136:139], v[208:211], v[32:35]\n" \
"v_add_u32 v189, s91, v189\n" \
"v_add_u32 v190, s91, v190\n" \
"ds_read_b128 v[208:211], v240 offset:49664\n" \
"v_mfma_f32_16x16x32_f16 v[56:59], v[136:139], v[156:159], v[56:59]\n" \
"v_add_u32 v191, s91, v191\n" \
"ds_read_b64_tr_b16 v[134:135], v242 offset:41472\n" \
"v_mfma_f32_16x16x32_f16 v[68:71], v[140:143], v[156:159], v[68:71]\n" \
"v_and_b32 v188, s92, v188\n" \
"ds_read_b128 v[156:159], v240 offset:41472\n" \
"v_mfma_f32_16x16x32_f16 v[92:95], v[140:143], v[160:163], v[92:95]\n" \
"v_and_b32 v189, s92, v189\n" \
"s_add_i32 m0, s45, 0x5000\n" \
"s_add_i32 s81, s47, 0x2800000\n" \
"v_mfma_f32_16x16x32_f16 v[80:83], v[136:139], v[160:163], v[80:83]\n" \
"v_and_b32 v190, s92, v190\n" \
"v_and_b32 v191, s92, v191\n" \
"buffer_load_dwordx4 v232, s[36:39], s81 offen sc1 lds\n" \
"v_mfma_f32_16x16x32_f16 v[104:107], v[136:139], v[164:167], v[104:107]\n" \
"buffer_store_dwordx4 v[188:191], v234, s[36:39], s82 offen offset:1024 sc1\n" \
"v_mfma_f32_16x16x32_f16 v[116:119], v[140:143], v[164:167], v[116:119]\n" \
"s_setprio 1\n" \
"s_waitcnt vmcnt(7) lgkmcnt(0)\n" \
"s_barrier\n" \
"v_mfma_f32_16x16x32_f16 v[0:3], v[120:123], v[168:171], v[0:3]\n" \
"v_pk_add_f16 v136, v120, v128\n" \
"v_pk_add_f16 v137, v121, v129\n" \
"v_mfma_f32_16x16x32_f16 v[12:15], v[124:127], v[168:171], v[12:15]\n" \
"v_pk_add_f16 v168, v168, v144\n" \
"v_pk_add_f16 v138, v122, v130\n" \
"v_mfma_f32_16x16x32_f16 v[36:39], v[124:127], v[172:175], v[36:39]\n" \
"v_pk_add_f16 v169, v169, v145\n" \
"v_pk_add_f16 v170, v170, v146\n" \
"v_pk_add_f16 v171, v171, v147\n" \
"buffer_store_dwordx4 v[216:219], v235, s[36:39], s65 offen sc1\n" \
"v_mfma_f32_16x16x32_f16 v[24:27], v[120:123], v[172:175], v[24:27]\n" \
"v_pk_add_f16 v172, v172, v148\n" \
"v_pk_add_f16 v139, v123, v131\n" \
"s_add_i32 m0, s44, 0xa000\n" \
"s_add_i32 s80, s46, 0x2c00000\n" \
"v_mfma_f32_16x16x32_f16 v[48:51], v[120:123], v[176:179], v[48:51]\n" \
"v_pk_add_f16 v173, v173, v149\n" \
"v_pk_add_f16 v140, v124, v132\n" \
"buffer_load_dwordx4 v232, s[36:39], s80 offen sc1 lds\n" \
"v_mfma_f32_16x16x32_f16 v[60:63], v[124:127], v[176:179], v[60:63]\n" \
"v_pk_add_f16 v174, v174, v150\n" \
"v_pk_add_f16 v175, v175, v151\n" \
"ds_read_b128 v[164:167], v240 offset:4608\n" \
"v_mfma_f32_16x16x32_f16 v[84:87], v[124:127], v[180:183], v[84:87]\n" \
"v_pk_add_f16 v176, v176, v152\n" \
"v_pk_add_f16 v141, v125, v133\n" \
"v_mfma_f32_16x16x32_f16 v[72:75], v[120:123], v[180:183], v[72:75]\n" \
"v_pk_add_f16 v177, v177, v153\n" \
"v_pk_add_f16 v142, v126, v134\n" \
"v_mfma_f32_16x16x32_f16 v[96:99], v[120:123], v[184:187], v[96:99]\n" \
"v_pk_add_f16 v178, v178, v154\n" \
"v_pk_add_f16 v179, v179, v155\n" \
"ds_read_b64_tr_b16 v[188:189], v236 offset:16384\n" \
"v_mfma_f32_16x16x32_f16 v[108:111], v[124:127], v[184:187], v[108:111]\n" \
"v_pk_add_f16 v180, v180, v208\n" \
"v_pk_add_f16 v143, v127, v135\n" \
"ds_read_b64_tr_b16 v[192:193], v237 offset:16384\n" \
"v_mfma_f32_16x16x32_f16 v[4:7], v[128:131], v[144:147], v[4:7]\n" \
"v_pk_add_f16 v181, v181, v209\n" \
"ds_read_b64_tr_b16 v[120:121], v241 offset:0\n" \
"buffer_load_dwordx4 v232, s[36:39], s80 offen offset:1024 sc1 lds\n" \
"v_mfma_f32_16x16x32_f16 v[16:19], v[132:135], v[144:147], v[16:19]\n" \
"v_pk_add_f16 v182, v182, v210\n" \
"ds_read_b128 v[160:163], v240 offset:2560\n" \
"v_mfma_f32_16x16x32_f16 v[40:43], v[132:135], v[148:151], v[40:43]\n" \
"v_pk_add_f16 v183, v183, v211\n" \
"ds_read_b128 v[144:147], v240 offset:2048\n" \
"v_mfma_f32_16x16x32_f16 v[28:31], v[128:131], v[148:151], v[28:31]\n" \
"v_pk_add_f16 v184, v184, v156\n" \
"ds_read_b64_tr_b16 v[122:123], v241 offset:8192\n" \
"v_mfma_f32_16x16x32_f16 v[52:55], v[128:131], v[152:155], v[52:55]\n" \
"v_pk_add_f16 v185, v185, v157\n" \
"ds_read_b128 v[148:151], v240 offset:4096\n" \
"s_waitcnt lgkmcnt(6)\n" \
"v_mfma_f32_16x16x16_bf16 v[188:191], v[188:189], v[254:255], 0\n" \
"v_mfma_f32_16x16x32_f16 v[64:67], v[132:135], v[152:155], v[64:67]\n" \
"v_pk_add_f16 v186, v186, v158\n" \
"ds_read_b64_tr_b16 v[124:125], v242 offset:0\n" \
"s_waitcnt lgkmcnt(6)\n" \
"v_mfma_f32_16x16x16_bf16 v[192:195], v[192:193], v[254:255], 0\n" \
"v_mfma_f32_16x16x32_f16 v[88:91], v[132:135], v[208:211], v[88:91]\n" \
"v_pk_add_f16 v187, v187, v159\n" \
"ds_read_b128 v[152:155], v240 offset:6144\n" \
"buffer_load_dwordx4 v232, s[36:39], s80 offen offset:2048 sc1 lds\n" \
"v_mfma_f32_16x16x32_f16 v[76:79], v[128:131], v[208:211], v[76:79]\n" \
"s_setprio 0\n" \
"ds_read_b64_tr_b16 v[126:127], v242 offset:8192\n" \
"v_mfma_f32_16x16x32_f16 v[100:103], v[128:131], v[156:159], v[100:103]\n" \
"v_cvt_pk_f16_f32 v188, v188, v189\n" \
"ds_read_b128 v[208:211], v240 offset:8192\n" \
"v_mfma_f32_16x16x32_f16 v[112:115], v[132:135], v[156:159], v[112:115]\n" \
"v_cvt_pk_f16_f32 v189, v190, v191\n" \
"ds_read_b64_tr_b16 v[128:129], v241 offset:512\n" \
"v_mfma_f32_16x16x32_f16 v[8:11], v[136:139], v[168:171], v[8:11]\n" \
"v_cvt_pk_f16_f32 v190, v192, v193\n" \
"ds_read_b128 v[156:159], v240 offset:0\n" \
"v_mfma_f32_16x16x32_f16 v[20:23], v[140:143], v[168:171], v[20:23]\n" \
"v_cvt_pk_f16_f32 v191, v194, v195\n" \
"v_add_u32 v188, s91, v188\n" \
"ds_read_b128 v[168:171], v240 offset:6656\n" \
"v_mfma_f32_16x16x32_f16 v[44:47], v[140:143], v[172:175], v[44:47]\n" \
"ds_read_b64_tr_b16 v[130:131], v241 offset:8704\n" \
"ds_read_b64_tr_b16 v[132:133], v242 offset:512\n" \
"buffer_load_dwordx4 v232, s[36:39], s80 offen offset:3072 sc1 lds\n" \
"v_mfma_f32_16x16x32_f16 v[32:35], v[136:139], v[172:175], v[32:35]\n" \
"v_add_u32 v189, s91, v189\n" \
"v_add_u32 v190, s91, v190\n" \
"ds_read_b128 v[172:175], v240 offset:8704\n" \
"v_mfma_f32_16x16x32_f16 v[56:59], v[136:139], v[176:179], v[56:59]\n" \
"v_add_u32 v191, s91, v191\n" \
"ds_read_b64_tr_b16 v[134:135], v242 offset:8704\n" \
"v_mfma_f32_16x16x32_f16 v[68:71], v[140:143], v[176:179], v[68:71]\n" \
"v_and_b32 v188, s92, v188\n" \
"ds_read_b128 v[176:179], v240 offset:512\n" \
"v_mfma_f32_16x16x32_f16 v[92:95], v[140:143], v[180:183], v[92:95]\n" \
"v_and_b32 v189, s92, v189\n" \
"s_add_i32 m0, s45, 0xa000\n" \
"s_add_i32 s81, s47, 0x2c00000\n" \
"s_add_i32 s82, s49, 0x4800000\n" \
"v_mfma_f32_16x16x32_f16 v[80:83], v[136:139], v[180:183], v[80:83]\n" \
"v_and_b32 v190, s92, v190\n" \
"v_and_b32 v191, s92, v191\n" \
"buffer_load_dwordx4 v232, s[36:39], s81 offen sc1 lds\n" \
"v_mfma_f32_16x16x32_f16 v[104:107], v[136:139], v[184:187], v[104:107]\n" \
"buffer_store_dwordx4 v[188:191], v234, s[36:39], s82 offen sc1\n" \
"v_mfma_f32_16x16x32_f16 v[116:119], v[140:143], v[184:187], v[116:119]\n" \
"s_setprio 1\n" \
"s_waitcnt vmcnt(7) lgkmcnt(0)\n" \
"s_barrier\n" \
"v_mfma_f32_16x16x32_f16 v[0:3], v[120:123], v[144:147], v[0:3]\n" \
"v_pk_add_f16 v136, v120, v128\n" \
"v_pk_add_f16 v137, v121, v129\n" \
"v_mfma_f32_16x16x32_f16 v[12:15], v[124:127], v[144:147], v[12:15]\n" \
"v_pk_add_f16 v144, v144, v160\n" \
"v_pk_add_f16 v138, v122, v130\n" \
"v_mfma_f32_16x16x32_f16 v[36:39], v[124:127], v[148:151], v[36:39]\n" \
"v_pk_add_f16 v145, v145, v161\n" \
"v_pk_add_f16 v146, v146, v162\n" \
"v_pk_add_f16 v147, v147, v163\n" \
"v_mfma_f32_16x16x32_f16 v[24:27], v[120:123], v[148:151], v[24:27]\n" \
"v_pk_add_f16 v148, v148, v164\n" \
"v_pk_add_f16 v139, v123, v131\n" \
"s_add_i32 m0, s44, 0x0\n" \
"s_add_i32 s80, s46, 0x3000000\n" \
"v_mfma_f32_16x16x32_f16 v[48:51], v[120:123], v[152:155], v[48:51]\n" \
"v_pk_add_f16 v149, v149, v165\n" \
"v_pk_add_f16 v140, v124, v132\n" \
"buffer_load_dwordx4 v232, s[36:39], s80 offen sc1 lds\n" \
"v_mfma_f32_16x16x32_f16 v[60:63], v[124:127], v[152:155], v[60:63]\n" \
"v_pk_add_f16 v150, v150, v166\n" \
"v_pk_add_f16 v151, v151, v167\n" \
"ds_read_b128 v[184:187], v240 offset:25088\n" \
"v_mfma_f32_16x16x32_f16 v[84:87], v[124:127], v[208:211], v[84:87]\n" \
"v_pk_add_f16 v152, v152, v168\n" \
"v_pk_add_f16 v141, v125, v133\n" \
"v_mfma_f32_16x16x32_f16 v[72:75], v[120:123], v[208:211], v[72:75]\n" \
"v_pk_add_f16 v153, v153, v169\n" \
"v_pk_add_f16 v142, v126, v134\n" \
"v_mfma_f32_16x16x32_f16 v[96:99], v[120:123], v[156:159], v[96:99]\n" \
"v_pk_add_f16 v154, v154, v170\n" \
"v_pk_add_f16 v155, v155, v171\n" \
"ds_read_b64_tr_b16 v[188:189], v238 offset:16384\n" \
"v_mfma_f32_16x16x32_f16 v[108:111], v[124:127], v[156:159], v[108:111]\n" \
"v_pk_add_f16 v208, v208, v172\n" \
"v_pk_add_f16 v143, v127, v135\n" \
"ds_read_b64_tr_b16 v[192:193], v239 offset:16384\n" \
"v_mfma_f32_16x16x32_f16 v[4:7], v[128:131], v[160:163], v[4:7]\n" \
"v_pk_add_f16 v209, v209, v173\n" \
"ds_read_b64_tr_b16 v[120:121], v241 offset:16384\n" \
"buffer_load_dwordx4 v232, s[36:39], s80 offen offset:1024 sc1 lds\n" \
"v_mfma_f32_16x16x32_f16 v[16:19], v[132:135], v[160:163], v[16:19]\n" \
"v_pk_add_f16 v210, v210, v174\n" \
"ds_read_b128 v[180:183], v240 offset:23040\n" \
"v_mfma_f32_16x16x32_f16 v[40:43], v[132:135], v[164:167], v[40:43]\n" \
"v_pk_add_f16 v211, v211, v175\n" \
"ds_read_b128 v[160:163], v240 offset:22528\n" \
"v_mfma_f32_16x16x32_f16 v[28:31], v[128:131], v[164:167], v[28:31]\n" \
"v_pk_add_f16 v156, v156, v176\n" \
"ds_read_b64_tr_b16 v[122:123], v241 offset:24576\n" \
"v_mfma_f32_16x16x32_f16 v[52:55], v[128:131], v[168:171], v[52:55]\n" \
"v_pk_add_f16 v157, v157, v177\n" \
"ds_read_b128 v[164:167], v240 offset:24576\n" \
"s_waitcnt lgkmcnt(6)\n" \
"v_mfma_f32_16x16x16_bf16 v[188:191], v[188:189], v[254:255], 0\n" \
"v_mfma_f32_16x16x32_f16 v[64:67], v[132:135], v[168:171], v[64:67]\n" \
"v_pk_add_f16 v158, v158, v178\n" \
"ds_read_b64_tr_b16 v[124:125], v242 offset:16384\n" \
"s_waitcnt lgkmcnt(6)\n" \
"v_mfma_f32_16x16x16_bf16 v[192:195], v[192:193], v[254:255], 0\n" \
"v_mfma_f32_16x16x32_f16 v[88:91], v[132:135], v[172:175], v[88:91]\n" \
"v_pk_add_f16 v159, v159, v179\n" \
"ds_read_b128 v[168:171], v240 offset:26624\n" \
"buffer_load_dwordx4 v232, s[36:39], s80 offen offset:2048 sc1 lds\n" \
"v_mfma_f32_16x16x32_f16 v[76:79], v[128:131], v[172:175], v[76:79]\n" \
"s_setprio 0\n" \
"ds_read_b64_tr_b16 v[126:127], v242 offset:24576\n" \
"v_mfma_f32_16x16x32_f16 v[100:103], v[128:131], v[176:179], v[100:103]\n" \
"v_cvt_pk_f16_f32 v188, v188, v189\n" \
"ds_read_b128 v[172:175], v240 offset:28672\n" \
"v_mfma_f32_16x16x32_f16 v[112:115], v[132:135], v[176:179], v[112:115]\n" \
"v_cvt_pk_f16_f32 v189, v190, v191\n" \
"ds_read_b64_tr_b16 v[128:129], v241 offset:16896\n" \
"v_mfma_f32_16x16x32_f16 v[8:11], v[136:139], v[144:147], v[8:11]\n" \
"v_cvt_pk_f16_f32 v190, v192, v193\n" \
"ds_read_b128 v[176:179], v240 offset:20480\n" \
"v_mfma_f32_16x16x32_f16 v[20:23], v[140:143], v[144:147], v[20:23]\n" \
"v_cvt_pk_f16_f32 v191, v194, v195\n" \
"v_add_u32 v188, s91, v188\n" \
"ds_read_b128 v[144:147], v240 offset:27136\n" \
"v_mfma_f32_16x16x32_f16 v[44:47], v[140:143], v[148:151], v[44:47]\n" \
"ds_read_b64_tr_b16 v[130:131], v241 offset:25088\n" \
"ds_read_b64_tr_b16 v[132:133], v242 offset:16896\n" \
"buffer_load_dwordx4 v232, s[36:39], s80 offen offset:3072 sc1 lds\n" \
"v_mfma_f32_16x16x32_f16 v[32:35], v[136:139], v[148:151], v[32:35]\n" \
"v_add_u32 v189, s91, v189\n" \
"v_add_u32 v190, s91, v190\n" \
"ds_read_b128 v[148:151], v240 offset:29184\n" \
"v_mfma_f32_16x16x32_f16 v[56:59], v[136:139], v[152:155], v[56:59]\n" \
"v_add_u32 v191, s91, v191\n" \
"ds_read_b64_tr_b16 v[134:135], v242 offset:25088\n" \
"v_mfma_f32_16x16x32_f16 v[68:71], v[140:143], v[152:155], v[68:71]\n" \
"v_and_b32 v188, s92, v188\n" \
"ds_read_b128 v[152:155], v240 offset:20992\n" \
"v_mfma_f32_16x16x32_f16 v[92:95], v[140:143], v[208:211], v[92:95]\n" \
"v_and_b32 v189, s92, v189\n" \
"s_add_i32 m0, s45, 0x0\n" \
"s_add_i32 s81, s47, 0x3000000\n" \
"v_mfma_f32_16x16x32_f16 v[80:83], v[136:139], v[208:211], v[80:83]\n" \
"v_and_b32 v190, s92, v190\n" \
"v_and_b32 v191, s92, v191\n" \
"buffer_load_dwordx4 v232, s[36:39], s81 offen sc1 lds\n" \
"v_mfma_f32_16x16x32_f16 v[104:107], v[136:139], v[156:159], v[104:107]\n" \
"buffer_store_dwordx4 v[188:191], v234, s[36:39], s82 offen offset:1024 sc1\n" \
"v_mfma_f32_16x16x32_f16 v[116:119], v[140:143], v[156:159], v[116:119]\n" \
"s_setprio 1\n" \
"s_waitcnt vmcnt(7) lgkmcnt(0)\n" \
"s_barrier\n" \
"v_mfma_f32_16x16x32_f16 v[0:3], v[120:123], v[160:163], v[0:3]\n" \
"v_pk_add_f16 v136, v120, v128\n" \
"v_pk_add_f16 v137, v121, v129\n" \
"v_mfma_f32_16x16x32_f16 v[12:15], v[124:127], v[160:163], v[12:15]\n" \
"v_pk_add_f16 v160, v160, v180\n" \
"v_pk_add_f16 v138, v122, v130\n" \
"v_mfma_f32_16x16x32_f16 v[36:39], v[124:127], v[164:167], v[36:39]\n" \
"v_pk_add_f16 v161, v161, v181\n" \
"v_pk_add_f16 v162, v162, v182\n" \
"v_pk_add_f16 v163, v163, v183\n" \
"buffer_store_dwordx4 v[220:223], v235, s[36:39], s66 offen sc1\n" \
"v_mfma_f32_16x16x32_f16 v[24:27], v[120:123], v[164:167], v[24:27]\n" \
"v_pk_add_f16 v164, v164, v184\n" \
"v_pk_add_f16 v139, v123, v131\n" \
"s_add_i32 m0, s44, 0x5000\n" \
"s_add_i32 s80, s46, 0x3400000\n" \
"v_mfma_f32_16x16x32_f16 v[48:51], v[120:123], v[168:171], v[48:51]\n" \
"v_pk_add_f16 v165, v165, v185\n" \
"v_pk_add_f16 v140, v124, v132\n" \
"buffer_load_dwordx4 v232, s[36:39], s80 offen sc1 lds\n" \
"v_mfma_f32_16x16x32_f16 v[60:63], v[124:127], v[168:171], v[60:63]\n" \
"v_pk_add_f16 v166, v166, v186\n" \
"v_pk_add_f16 v167, v167, v187\n" \
"ds_read_b128 v[156:159], v240 offset:45568\n" \
"v_mfma_f32_16x16x32_f16 v[84:87], v[124:127], v[172:175], v[84:87]\n" \
"v_pk_add_f16 v168, v168, v144\n" \
"v_pk_add_f16 v141, v125, v133\n" \
"v_mfma_f32_16x16x32_f16 v[72:75], v[120:123], v[172:175], v[72:75]\n" \
"v_pk_add_f16 v169, v169, v145\n" \
"v_pk_add_f16 v142, v126, v134\n" \
"v_mfma_f32_16x16x32_f16 v[96:99], v[120:123], v[176:179], v[96:99]\n" \
"v_pk_add_f16 v170, v170, v146\n" \
"v_pk_add_f16 v171, v171, v147\n" \
"ds_read_b64_tr_b16 v[188:189], v236 offset:20480\n" \
"v_mfma_f32_16x16x32_f16 v[108:111], v[124:127], v[176:179], v[108:111]\n" \
"v_pk_add_f16 v172, v172, v148\n" \
"v_pk_add_f16 v143, v127, v135\n" \
"ds_read_b64_tr_b16 v[192:193], v237 offset:20480\n" \
"v_mfma_f32_16x16x32_f16 v[4:7], v[128:131], v[180:183], v[4:7]\n" \
"v_pk_add_f16 v173, v173, v149\n" \
"ds_read_b64_tr_b16 v[120:121], v241 offset:32768\n" \
"buffer_load_dwordx4 v232, s[36:39], s80 offen offset:1024 sc1 lds\n" \
"v_mfma_f32_16x16x32_f16 v[16:19], v[132:135], v[180:183], v[16:19]\n" \
"v_pk_add_f16 v174, v174, v150\n" \
"ds_read_b128 v[208:211], v240 offset:43520\n" \
"v_mfma_f32_16x16x32_f16 v[40:43], v[132:135], v[184:187], v[40:43]\n" \
"v_pk_add_f16 v175, v175, v151\n" \
"ds_read_b128 v[180:183], v240 offset:43008\n" \
"v_mfma_f32_16x16x32_f16 v[28:31], v[128:131], v[184:187], v[28:31]\n" \
"v_pk_add_f16 v176, v176, v152\n" \
"ds_read_b64_tr_b16 v[122:123], v241 offset:40960\n" \
"v_mfma_f32_16x16x32_f16 v[52:55], v[128:131], v[144:147], v[52:55]\n" \
"v_pk_add_f16 v177, v177, v153\n" \
"ds_read_b128 v[184:187], v240 offset:45056\n" \
"s_waitcnt lgkmcnt(6)\n" \
"v_mfma_f32_16x16x16_bf16 v[188:191], v[188:189], v[254:255], 0\n" \
"v_mfma_f32_16x16x32_f16 v[64:67], v[132:135], v[144:147], v[64:67]\n" \
"v_pk_add_f16 v178, v178, v154\n" \
"ds_read_b64_tr_b16 v[124:125], v242 offset:32768\n" \
"s_waitcnt lgkmcnt(6)\n" \
"v_mfma_f32_16x16x16_bf16 v[192:195], v[192:193], v[254:255], 0\n" \
"v_mfma_f32_16x16x32_f16 v[88:91], v[132:135], v[148:151], v[88:91]\n" \
"v_pk_add_f16 v179, v179, v155\n" \
"ds_read_b128 v[144:147], v240 offset:47104\n" \
"buffer_load_dwordx4 v232, s[36:39], s80 offen offset:2048 sc1 lds\n" \
"v_mfma_f32_16x16x32_f16 v[76:79], v[128:131], v[148:151], v[76:79]\n" \
"s_setprio 0\n" \
"ds_read_b64_tr_b16 v[126:127], v242 offset:40960\n" \
"v_mfma_f32_16x16x32_f16 v[100:103], v[128:131], v[152:155], v[100:103]\n" \
"v_cvt_pk_f16_f32 v188, v188, v189\n" \
"ds_read_b128 v[148:151], v240 offset:49152\n" \
"v_mfma_f32_16x16x32_f16 v[112:115], v[132:135], v[152:155], v[112:115]\n" \
"v_cvt_pk_f16_f32 v189, v190, v191\n" \
"ds_read_b64_tr_b16 v[128:129], v241 offset:33280\n" \
"v_mfma_f32_16x16x32_f16 v[8:11], v[136:139], v[160:163], v[8:11]\n" \
"v_cvt_pk_f16_f32 v190, v192, v193\n" \
"ds_read_b128 v[152:155], v240 offset:40960\n" \
"v_mfma_f32_16x16x32_f16 v[20:23], v[140:143], v[160:163], v[20:23]\n" \
"v_cvt_pk_f16_f32 v191, v194, v195\n" \
"v_add_u32 v188, s91, v188\n" \
"ds_read_b128 v[160:163], v240 offset:47616\n" \
"v_mfma_f32_16x16x32_f16 v[44:47], v[140:143], v[164:167], v[44:47]\n" \
"ds_read_b64_tr_b16 v[130:131], v241 offset:41472\n" \
"ds_read_b64_tr_b16 v[132:133], v242 offset:33280\n" \
"buffer_load_dwordx4 v232, s[36:39], s80 offen offset:3072 sc1 lds\n" \
"v_mfma_f32_16x16x32_f16 v[32:35], v[136:139], v[164:167], v[32:35]\n" \
"v_add_u32 v189, s91, v189\n" \
"v_add_u32 v190, s91, v190\n" \
"ds_read_b128 v[164:167], v240 offset:49664\n" \
"v_mfma_f32_16x16x32_f16 v[56:59], v[136:139], v[168:171], v[56:59]\n" \
"v_add_u32 v191, s91, v191\n" \
"ds_read_b64_tr_b16 v[134:135], v242 offset:41472\n" \
"v_mfma_f32_16x16x32_f16 v[68:71], v[140:143], v[168:171], v[68:71]\n" \
"v_and_b32 v188, s92, v188\n" \
"ds_read_b128 v[168:171], v240 offset:41472\n" \
"v_mfma_f32_16x16x32_f16 v[92:95], v[140:143], v[172:175], v[92:95]\n" \
"v_and_b32 v189, s92, v189\n" \
"s_add_i32 m0, s45, 0x5000\n" \
"s_add_i32 s81, s47, 0x3400000\n" \
"s_add_i32 s82, s50, 0x5000000\n" \
"v_mfma_f32_16x16x32_f16 v[80:83], v[136:139], v[172:175], v[80:83]\n" \
"v_and_b32 v190, s92, v190\n" \
"v_and_b32 v191, s92, v191\n" \
"buffer_load_dwordx4 v232, s[36:39], s81 offen sc1 lds\n" \
"v_mfma_f32_16x16x32_f16 v[104:107], v[136:139], v[176:179], v[104:107]\n" \
"buffer_store_dwordx4 v[188:191], v234, s[36:39], s82 offen sc1\n" \
"v_mfma_f32_16x16x32_f16 v[116:119], v[140:143], v[176:179], v[116:119]\n" \
"s_setprio 1\n" \
"s_waitcnt vmcnt(7) lgkmcnt(0)\n" \
"s_barrier\n" \
"v_mfma_f32_16x16x32_f16 v[0:3], v[120:123], v[180:183], v[0:3]\n" \
"v_pk_add_f16 v136, v120, v128\n" \
"v_pk_add_f16 v137, v121, v129\n" \
"v_mfma_f32_16x16x32_f16 v[12:15], v[124:127], v[180:183], v[12:15]\n" \
"v_pk_add_f16 v180, v180, v208\n" \
"v_pk_add_f16 v138, v122, v130\n" \
"v_mfma_f32_16x16x32_f16 v[36:39], v[124:127], v[184:187], v[36:39]\n" \
"v_pk_add_f16 v181, v181, v209\n" \
"v_pk_add_f16 v182, v182, v210\n" \
"v_pk_add_f16 v183, v183, v211\n" \
"v_mfma_f32_16x16x32_f16 v[24:27], v[120:123], v[184:187], v[24:27]\n" \
"v_pk_add_f16 v184, v184, v156\n" \
"v_pk_add_f16 v139, v123, v131\n" \
"s_add_i32 m0, s44, 0xa000\n" \
"s_add_i32 s80, s46, 0x3800000\n" \
"v_mfma_f32_16x16x32_f16 v[48:51], v[120:123], v[144:147], v[48:51]\n" \
"v_pk_add_f16 v185, v185, v157\n" \
"v_pk_add_f16 v140, v124, v132\n" \
"buffer_load_dwordx4 v232, s[36:39], s80 offen sc1 lds\n" \
"v_mfma_f32_16x16x32_f16 v[60:63], v[124:127], v[144:147], v[60:63]\n" \
"v_pk_add_f16 v186, v186, v158\n" \
"v_pk_add_f16 v187, v187, v159\n" \
"ds_read_b128 v[176:179], v240 offset:4608\n" \
"v_mfma_f32_16x16x32_f16 v[84:87], v[124:127], v[148:151], v[84:87]\n" \
"v_pk_add_f16 v144, v144, v160\n" \
"v_pk_add_f16 v141, v125, v133\n" \
"v_mfma_f32_16x16x32_f16 v[72:75], v[120:123], v[148:151], v[72:75]\n" \
"v_pk_add_f16 v145, v145, v161\n" \
"v_pk_add_f16 v142, v126, v134\n" \
"v_mfma_f32_16x16x32_f16 v[96:99], v[120:123], v[152:155], v[96:99]\n" \
"v_pk_add_f16 v146, v146, v162\n" \
"v_pk_add_f16 v147, v147, v163\n" \
"ds_read_b64_tr_b16 v[188:189], v238 offset:20480\n" \
"v_mfma_f32_16x16x32_f16 v[108:111], v[124:127], v[152:155], v[108:111]\n" \
"v_pk_add_f16 v148, v148, v164\n" \
"v_pk_add_f16 v143, v127, v135\n" \
"ds_read_b64_tr_b16 v[192:193], v239 offset:20480\n" \
"v_mfma_f32_16x16x32_f16 v[4:7], v[128:131], v[208:211], v[4:7]\n" \
"v_pk_add_f16 v149, v149, v165\n" \
"ds_read_b64_tr_b16 v[120:121], v241 offset:0\n" \
"buffer_load_dwordx4 v232, s[36:39], s80 offen offset:1024 sc1 lds\n" \
"v_mfma_f32_16x16x32_f16 v[16:19], v[132:135], v[208:211], v[16:19]\n" \
"v_pk_add_f16 v150, v150, v166\n" \
"ds_read_b128 v[172:175], v240 offset:2560\n" \
"v_mfma_f32_16x16x32_f16 v[40:43], v[132:135], v[156:159], v[40:43]\n" \
"v_pk_add_f16 v151, v151, v167\n" \
"ds_read_b128 v[208:211], v240 offset:2048\n" \
"v_mfma_f32_16x16x32_f16 v[28:31], v[128:131], v[156:159], v[28:31]\n" \
"v_pk_add_f16 v152, v152, v168\n" \
"ds_read_b64_tr_b16 v[122:123], v241 offset:8192\n" \
"v_mfma_f32_16x16x32_f16 v[52:55], v[128:131], v[160:163], v[52:55]\n" \
"v_pk_add_f16 v153, v153, v169\n" \
"ds_read_b128 v[156:159], v240 offset:4096\n" \
"s_waitcnt lgkmcnt(6)\n" \
"v_mfma_f32_16x16x16_bf16 v[188:191], v[188:189], v[254:255], 0\n" \
"v_mfma_f32_16x16x32_f16 v[64:67], v[132:135], v[160:163], v[64:67]\n" \
"v_pk_add_f16 v154, v154, v170\n" \
"ds_read_b64_tr_b16 v[124:125], v242 offset:0\n" \
"s_waitcnt lgkmcnt(6)\n" \
"v_mfma_f32_16x16x16_bf16 v[192:195], v[192:193], v[254:255], 0\n" \
"v_mfma_f32_16x16x32_f16 v[88:91], v[132:135], v[164:167], v[88:91]\n" \
"v_pk_add_f16 v155, v155, v171\n" \
"ds_read_b128 v[160:163], v240 offset:6144\n" \
"buffer_load_dwordx4 v232, s[36:39], s80 offen offset:2048 sc1 lds\n" \
"v_mfma_f32_16x16x32_f16 v[76:79], v[128:131], v[164:167], v[76:79]\n" \
"s_setprio 0\n" \
"ds_read_b64_tr_b16 v[126:127], v242 offset:8192\n" \
"v_mfma_f32_16x16x32_f16 v[100:103], v[128:131], v[168:171], v[100:103]\n" \
"v_cvt_pk_f16_f32 v188, v188, v189\n" \
"ds_read_b128 v[164:167], v240 offset:8192\n" \
"v_mfma_f32_16x16x32_f16 v[112:115], v[132:135], v[168:171], v[112:115]\n" \
"v_cvt_pk_f16_f32 v189, v190, v191\n" \
"ds_read_b64_tr_b16 v[128:129], v241 offset:512\n" \
"v_mfma_f32_16x16x32_f16 v[8:11], v[136:139], v[180:183], v[8:11]\n" \
"v_cvt_pk_f16_f32 v190, v192, v193\n" \
"ds_read_b128 v[168:171], v240 offset:0\n" \
"v_mfma_f32_16x16x32_f16 v[20:23], v[140:143], v[180:183], v[20:23]\n" \
"v_cvt_pk_f16_f32 v191, v194, v195\n" \
"v_add_u32 v188, s91, v188\n" \
"ds_read_b128 v[180:183], v240 offset:6656\n" \
"v_mfma_f32_16x16x32_f16 v[44:47], v[140:143], v[184:187], v[44:47]\n" \
"ds_read_b64_tr_b16 v[130:131], v241 offset:8704\n" \
"ds_read_b64_tr_b16 v[132:133], v242 offset:512\n" \
"buffer_load_dwordx4 v232, s[36:39], s80 offen offset:3072 sc1 lds\n" \
"v_mfma_f32_16x16x32_f16 v[32:35], v[136:139], v[184:187], v[32:35]\n" \
"v_add_u32 v189, s91, v189\n" \
"v_add_u32 v190, s91, v190\n" \
"ds_read_b128 v[184:187], v240 offset:8704\n" \
"v_mfma_f32_16x16x32_f16 v[56:59], v[136:139], v[144:147], v[56:59]\n" \
"v_add_u32 v191, s91, v191\n" \
"ds_read_b64_tr_b16 v[134:135], v242 offset:8704\n" \
"v_mfma_f32_16x16x32_f16 v[68:71], v[140:143], v[144:147], v[68:71]\n" \
"v_and_b32 v188, s92, v188\n" \
"ds_read_b128 v[144:147], v240 offset:512\n" \
"v_mfma_f32_16x16x32_f16 v[92:95], v[140:143], v[148:151], v[92:95]\n" \
"v_and_b32 v189, s92, v189\n" \
"s_add_i32 m0, s45, 0xa000\n" \
"s_add_i32 s81, s47, 0x3800000\n" \
"v_mfma_f32_16x16x32_f16 v[80:83], v[136:139], v[148:151], v[80:83]\n" \
"v_and_b32 v190, s92, v190\n" \
"v_and_b32 v191, s92, v191\n" \
"buffer_load_dwordx4 v232, s[36:39], s81 offen sc1 lds\n" \
"v_mfma_f32_16x16x32_f16 v[104:107], v[136:139], v[152:155], v[104:107]\n" \
"buffer_store_dwordx4 v[188:191], v234, s[36:39], s82 offen offset:1024 sc1\n" \
"v_mfma_f32_16x16x32_f16 v[116:119], v[140:143], v[152:155], v[116:119]\n" \
"s_setprio 1\n" \
"s_waitcnt vmcnt(7) lgkmcnt(0)\n" \
"s_barrier\n" \
"v_mfma_f32_16x16x32_f16 v[0:3], v[120:123], v[208:211], v[0:3]\n" \
"v_pk_add_f16 v136, v120, v128\n" \
"v_pk_add_f16 v137, v121, v129\n" \
"v_mfma_f32_16x16x32_f16 v[12:15], v[124:127], v[208:211], v[12:15]\n" \
"v_pk_add_f16 v208, v208, v172\n" \
"v_pk_add_f16 v138, v122, v130\n" \
"s_mov_b32 s84, 0x800c000\n" \
"v_mfma_f32_16x16x32_f16 v[36:39], v[124:127], v[156:159], v[36:39]\n" \
"v_pk_add_f16 v209, v209, v173\n" \
"v_pk_add_f16 v210, v210, v174\n" \
"v_pk_add_f16 v211, v211, v175\n" \
"buffer_load_dword v253, v252, s[36:39], s84 offen sc1\n" \
"buffer_store_dwordx4 v[224:227], v235, s[36:39], s67 offen sc1\n" \
"v_mfma_f32_16x16x32_f16 v[24:27], v[120:123], v[156:159], v[24:27]\n" \
"v_pk_add_f16 v156, v156, v176\n" \
"v_pk_add_f16 v139, v123, v131\n" \
"s_add_i32 m0, s44, 0x0\n" \
"s_add_i32 s80, s46, 0x3c00000\n" \
"v_mfma_f32_16x16x32_f16 v[48:51], v[120:123], v[160:163], v[48:51]\n" \
"v_pk_add_f16 v157, v157, v177\n" \
"v_pk_add_f16 v140, v124, v132\n" \
"buffer_load_dwordx4 v232, s[36:39], s80 offen sc1 lds\n" \
"v_mfma_f32_16x16x32_f16 v[60:63], v[124:127], v[160:163], v[60:63]\n" \
"v_pk_add_f16 v158, v158, v178\n" \
"v_pk_add_f16 v159, v159, v179\n" \
"ds_read_b128 v[152:155], v240 offset:25088\n" \
"v_mfma_f32_16x16x32_f16 v[84:87], v[124:127], v[164:167], v[84:87]\n" \
"v_pk_add_f16 v160, v160, v180\n" \
"v_pk_add_f16 v141, v125, v133\n" \
"v_mfma_f32_16x16x32_f16 v[72:75], v[120:123], v[164:167], v[72:75]\n" \
"v_pk_add_f16 v161, v161, v181\n" \
"v_pk_add_f16 v142, v126, v134\n" \
"v_mfma_f32_16x16x32_f16 v[96:99], v[120:123], v[168:171], v[96:99]\n" \
"v_pk_add_f16 v162, v162, v182\n" \
"v_pk_add_f16 v163, v163, v183\n" \
"ds_read_b64_tr_b16 v[188:189], v236 offset:24576\n" \
"v_mfma_f32_16x16x32_f16 v[108:111], v[124:127], v[168:171], v[108:111]\n" \
"v_pk_add_f16 v164, v164, v184\n" \
"v_pk_add_f16 v143, v127, v135\n" \
"ds_read_b64_tr_b16 v[192:193], v237 offset:24576\n" \
"v_mfma_f32_16x16x32_f16 v[4:7], v[128:131], v[172:175], v[4:7]\n" \
"v_pk_add_f16 v165, v165, v185\n" \
"ds_read_b64_tr_b16 v[120:121], v241 offset:16384\n" \
"buffer_load_dwordx4 v232, s[36:39], s80 offen offset:1024 sc1 lds\n" \
"v_mfma_f32_16x16x32_f16 v[16:19], v[132:135], v[172:175], v[16:19]\n" \
"v_pk_add_f16 v166, v166, v186\n" \
"ds_read_b128 v[148:151], v240 offset:23040\n" \
"v_mfma_f32_16x16x32_f16 v[40:43], v[132:135], v[176:179], v[40:43]\n" \
"v_pk_add_f16 v167, v167, v187\n" \
"ds_read_b128 v[172:175], v240 offset:22528\n" \
"v_mfma_f32_16x16x32_f16 v[28:31], v[128:131], v[176:179], v[28:31]\n" \
"v_pk_add_f16 v168, v168, v144\n" \
"ds_read_b64_tr_b16 v[122:123], v241 offset:24576\n" \
"v_mfma_f32_16x16x32_f16 v[52:55], v[128:131], v[180:183], v[52:55]\n" \
"v_pk_add_f16 v169, v169, v145\n" \
"ds_read_b128 v[176:179], v240 offset:24576\n" \
"s_waitcnt lgkmcnt(6)\n" \
"v_mfma_f32_16x16x16_bf16 v[188:191], v[188:189], v[254:255], 0\n" \
"v_mfma_f32_16x16x32_f16 v[64:67], v[132:135], v[180:183], v[64:67]\n" \
"v_pk_add_f16 v170, v170, v146\n" \
"ds_read_b64_tr_b16 v[124:125], v242 offset:16384\n" \
"s_waitcnt lgkmcnt(6)\n" \
"v_mfma_f32_16x16x16_bf16 v[192:195], v[192:193], v[254:255], 0\n" \
"v_mfma_f32_16x16x32_f16 v[88:91], v[132:135], v[184:187], v[88:91]\n" \
"v_pk_add_f16 v171, v171, v147\n" \
"ds_read_b128 v[180:183], v240 offset:26624\n" \
"buffer_load_dwordx4 v232, s[36:39], s80 offen offset:2048 sc1 lds\n" \
"v_mfma_f32_16x16x32_f16 v[76:79], v[128:131], v[184:187], v[76:79]\n" \
"s_setprio 0\n" \
"ds_read_b64_tr_b16 v[126:127], v242 offset:24576\n" \
"v_mfma_f32_16x16x32_f16 v[100:103], v[128:131], v[144:147], v[100:103]\n" \
"v_cvt_pk_f16_f32 v188, v188, v189\n" \
"ds_read_b128 v[184:187], v240 offset:28672\n" \
"v_mfma_f32_16x16x32_f16 v[112:115], v[132:135], v[144:147], v[112:115]\n" \
"v_cvt_pk_f16_f32 v189, v190, v191\n" \
"ds_read_b64_tr_b16 v[128:129], v241 offset:16896\n" \
"v_mfma_f32_16x16x32_f16 v[8:11], v[136:139], v[208:211], v[8:11]\n" \
"v_cvt_pk_f16_f32 v190, v192, v193\n" \
"ds_read_b128 v[144:147], v240 offset:20480\n" \
"v_mfma_f32_16x16x32_f16 v[20:23], v[140:143], v[208:211], v[20:23]\n" \
"v_cvt_pk_f16_f32 v191, v194, v195\n" \
"v_add_u32 v188, s91, v188\n" \
"ds_read_b128 v[208:211], v240 offset:27136\n" \
"v_mfma_f32_16x16x32_f16 v[44:47], v[140:143], v[156:159], v[44:47]\n" \
"ds_read_b64_tr_b16 v[130:131], v241 offset:25088\n" \
"ds_read_b64_tr_b16 v[132:133], v242 offset:16896\n" \
"buffer_load_dwordx4 v232, s[36:39], s80 offen offset:3072 sc1 lds\n" \
"v_mfma_f32_16x16x32_f16 v[32:35], v[136:139], v[156:159], v[32:35]\n" \
"v_add_u32 v189, s91, v189\n" \
"v_add_u32 v190, s91, v190\n" \
"ds_read_b128 v[156:159], v240 offset:29184\n" \
"v_mfma_f32_16x16x32_f16 v[56:59], v[136:139], v[160:163], v[56:59]\n" \
"v_add_u32 v191, s91, v191\n" \
"ds_read_b64_tr_b16 v[134:135], v242 offset:25088\n" \
"v_mfma_f32_16x16x32_f16 v[68:71], v[140:143], v[160:163], v[68:71]\n" \
"v_and_b32 v188, s92, v188\n" \
"v_and_b32 v189, s92, v189\n" \
"ds_read_b128 v[160:163], v240 offset:20992\n" \
"v_mfma_f32_16x16x32_f16 v[92:95], v[140:143], v[164:167], v[92:95]\n" \
"s_waitcnt vmcnt(5)\n" \
"v_cmp_eq_u32 vcc, 0, v253\n" \
"s_cbranch_vccnz .Ls16_%=\n" \
".Lr16_%=:\n" \
"s_add_i32 m0, s45, 0x0\n" \
"s_add_i32 s81, s47, 0x3c00000\n" \
"s_add_i32 s82, s51, 0x5800000\n" \
"v_mfma_f32_16x16x32_f16 v[80:83], v[136:139], v[164:167], v[80:83]\n" \
"v_and_b32 v190, s92, v190\n" \
"v_and_b32 v191, s92, v191\n" \
"buffer_load_dwordx4 v232, s[36:39], s81 offen sc1 lds\n" \
"v_mfma_f32_16x16x32_f16 v[104:107], v[136:139], v[168:171], v[104:107]\n" \
"buffer_store_dwordx4 v[188:191], v234, s[36:39], s82 offen sc1\n" \
"v_mfma_f32_16x16x32_f16 v[116:119], v[140:143], v[168:171], v[116:119]\n" \
"s_setprio 1\n" \
"s_waitcnt vmcnt(7) lgkmcnt(0)\n" \
"s_barrier\n" \
"v_mfma_f32_16x16x32_f16 v[0:3], v[120:123], v[172:175], v[0:3]\n" \
"v_pk_add_f16 v136, v120, v128\n" \
"v_pk_add_f16 v137, v121, v129\n" \
"v_mfma_f32_16x16x32_f16 v[12:15], v[124:127], v[172:175], v[12:15]\n" \
"v_pk_add_f16 v172, v172, v148\n" \
"v_pk_add_f16 v138, v122, v130\n" \
"v_mfma_f32_16x16x32_f16 v[36:39], v[124:127], v[176:179], v[36:39]\n" \
"v_pk_add_f16 v173, v173, v149\n" \
"v_pk_add_f16 v174, v174, v150\n" \
"v_pk_add_f16 v175, v175, v151\n" \
"v_mfma_f32_16x16x32_f16 v[24:27], v[120:123], v[176:179], v[24:27]\n" \
"v_pk_add_f16 v176, v176, v152\n" \
"v_pk_add_f16 v139, v123, v131\n" \
"s_add_i32 m0, s44, 0x5000\n" \
"s_add_i32 s80, s46, 0x4000000\n" \
"v_mfma_f32_16x16x32_f16 v[48:51], v[120:123], v[180:183], v[48:51]\n" \
"v_pk_add_f16 v177, v177, v153\n" \
"v_pk_add_f16 v140, v124, v132\n" \
"buffer_load_dwordx4 v232, s[36:39], s80 offen sc1 lds\n" \
"v_mfma_f32_16x16x32_f16 v[60:63], v[124:127], v[180:183], v[60:63]\n" \
"v_pk_add_f16 v178, v178, v154\n" \
"v_pk_add_f16 v179, v179, v155\n" \
"ds_read_b128 v[168:171], v240 offset:45568\n" \
"v_mfma_f32_16x16x32_f16 v[84:87], v[124:127], v[184:187], v[84:87]\n" \
"v_pk_add_f16 v180, v180, v208\n" \
"v_pk_add_f16 v141, v125, v133\n" \
"v_mfma_f32_16x16x32_f16 v[72:75], v[120:123], v[184:187], v[72:75]\n" \
"v_pk_add_f16 v181, v181, v209\n" \
"v_pk_add_f16 v142, v126, v134\n" \
"v_mfma_f32_16x16x32_f16 v[96:99], v[120:123], v[144:147], v[96:99]\n" \
"v_pk_add_f16 v182, v182, v210\n" \
"v_pk_add_f16 v183, v183, v211\n" \
"ds_read_b64_tr_b16 v[188:189], v238 offset:24576\n" \
"v_mfma_f32_16x16x32_f16 v[108:111], v[124:127], v[144:147], v[108:111]\n" \
"v_pk_add_f16 v184, v184, v156\n" \
"v_pk_add_f16 v143, v127, v135\n" \
"ds_read_b64_tr_b16 v[192:193], v239 offset:24576\n" \
"v_mfma_f32_16x16x32_f16 v[4:7], v[128:131], v[148:151], v[4:7]\n" \
"v_pk_add_f16 v185, v185, v157\n" \
"ds_read_b64_tr_b16 v[120:121], v241 offset:32768\n" \
"buffer_load_dwordx4 v232, s[36:39], s80 offen offset:1024 sc1 lds\n" \
"v_mfma_f32_16x16x32_f16 v[16:19], v[132:135], v[148:151], v[16:19]\n" \
"v_pk_add_f16 v186, v186, v158\n" \
"ds_read_b128 v[164:167], v240 offset:43520\n" \
"v_mfma_f32_16x16x32_f16 v[40:43], v[132:135], v[152:155], v[40:43]\n" \
"v_pk_add_f16 v187, v187, v159\n" \
"ds_read_b128 v[148:151], v240 offset:43008\n" \
"v_mfma_f32_16x16x32_f16 v[28:31], v[128:131], v[152:155], v[28:31]\n" \
"v_pk_add_f16 v144, v144, v160\n" \
"ds_read_b64_tr_b16 v[122:123], v241 offset:40960\n" \
"v_mfma_f32_16x16x32_f16 v[52:55], v[128:131], v[208:211], v[52:55]\n" \
"v_pk_add_f16 v145, v145, v161\n" \
"ds_read_b128 v[152:155], v240 offset:45056\n" \
"s_waitcnt lgkmcnt(6)\n" \
"v_mfma_f32_16x16x16_bf16 v[188:191], v[188:189], v[254:255], 0\n" \
"v_mfma_f32_16x16x32_f16 v[64:67], v[132:135], v[208:211], v[64:67]\n" \
"v_pk_add_f16 v146, v146, v162\n" \
"ds_read_b64_tr_b16 v[124:125], v242 offset:32768\n" \
"s_waitcnt lgkmcnt(6)\n" \
"v_mfma_f32_16x16x16_bf16 v[192:195], v[192:193], v[254:255], 0\n" \
"v_mfma_f32_16x16x32_f16 v[88:91], v[132:135], v[156:159], v[88:91]\n" \
"v_pk_add_f16 v147, v147, v163\n" \
"ds_read_b128 v[208:211], v240 offset:47104\n" \
"buffer_load_dwordx4 v232, s[36:39], s80 offen offset:2048 sc1 lds\n" \
"v_mfma_f32_16x16x32_f16 v[76:79], v[128:131], v[156:159], v[76:79]\n" \
"s_setprio 0\n" \
"ds_read_b64_tr_b16 v[126:127], v242 offset:40960\n" \
"v_mfma_f32_16x16x32_f16 v[100:103], v[128:131], v[160:163], v[100:103]\n" \
"v_cvt_pk_f16_f32 v188, v188, v189\n" \
"ds_read_b128 v[156:159], v240 offset:49152\n" \
"v_mfma_f32_16x16x32_f16 v[112:115], v[132:135], v[160:163], v[112:115]\n" \
"v_cvt_pk_f16_f32 v189, v190, v191\n" \
"ds_read_b64_tr_b16 v[128:129], v241 offset:33280\n" \
"v_mfma_f32_16x16x32_f16 v[8:11], v[136:139], v[172:175], v[8:11]\n" \
"v_cvt_pk_f16_f32 v190, v192, v193\n" \
"ds_read_b128 v[160:163], v240 offset:40960\n" \
"v_mfma_f32_16x16x32_f16 v[20:23], v[140:143], v[172:175], v[20:23]\n" \
"v_cvt_pk_f16_f32 v191, v194, v195\n" \
"v_add_u32 v188, s91, v188\n" \
"ds_read_b128 v[172:175], v240 offset:47616\n" \
"v_mfma_f32_16x16x32_f16 v[44:47], v[140:143], v[176:179], v[44:47]\n" \
"ds_read_b64_tr_b16 v[130:131], v241 offset:41472\n" \
"ds_read_b64_tr_b16 v[132:133], v242 offset:33280\n" \
"buffer_load_dwordx4 v232, s[36:39], s80 offen offset:3072 sc1 lds\n" \
"v_mfma_f32_16x16x32_f16 v[32:35], v[136:139], v[176:179], v[32:35]\n" \
"v_add_u32 v189, s91, v189\n" \
"v_add_u32 v190, s91, v190\n" \
"ds_read_b128 v[176:179], v240 offset:49664\n" \
"v_mfma_f32_16x16x32_f16 v[56:59], v[136:139], v[180:183], v[56:59]\n" \
"v_add_u32 v191, s91, v191\n" \
"ds_read_b64_tr_b16 v[134:135], v242 offset:41472\n" \
"v_mfma_f32_16x16x32_f16 v[68:71], v[140:143], v[180:183], v[68:71]\n" \
"v_and_b32 v188, s92, v188\n" \
"ds_read_b128 v[180:183], v240 offset:41472\n" \
"v_mfma_f32_16x16x32_f16 v[92:95], v[140:143], v[184:187], v[92:95]\n" \
"v_and_b32 v189, s92, v189\n" \
"s_add_i32 m0, s45, 0x5000\n" \
"s_add_i32 s81, s47, 0x4000000\n" \
"v_mfma_f32_16x16x32_f16 v[80:83], v[136:139], v[184:187], v[80:83]\n" \
"v_and_b32 v190, s92, v190\n" \
"v_and_b32 v191, s92, v191\n" \
"buffer_load_dwordx4 v232, s[36:39], s81 offen sc1 lds\n" \
"v_mfma_f32_16x16x32_f16 v[104:107], v[136:139], v[144:147], v[104:107]\n" \
"buffer_store_dwordx4 v[188:191], v234, s[36:39], s82 offen offset:1024 sc1\n" \
"v_mfma_f32_16x16x32_f16 v[116:119], v[140:143], v[144:147], v[116:119]\n" \
"s_setprio 1\n" \
"s_waitcnt vmcnt(7) lgkmcnt(0)\n" \
"s_barrier\n" \
"v_mfma_f32_16x16x32_f16 v[0:3], v[120:123], v[148:151], v[0:3]\n" \
"v_pk_add_f16 v136, v120, v128\n" \
"v_pk_add_f16 v137, v121, v129\n" \
"v_mfma_f32_16x16x32_f16 v[12:15], v[124:127], v[148:151], v[12:15]\n" \
"v_pk_add_f16 v148, v148, v164\n" \
"v_pk_add_f16 v138, v122, v130\n" \
"v_mfma_f32_16x16x32_f16 v[36:39], v[124:127], v[152:155], v[36:39]\n" \
"v_pk_add_f16 v149, v149, v165\n" \
"v_pk_add_f16 v150, v150, v166\n" \
"v_pk_add_f16 v151, v151, v167\n" \
"buffer_store_dwordx4 v[228:231], v235, s[36:39], s68 offen sc1\n" \
"v_mfma_f32_16x16x32_f16 v[24:27], v[120:123], v[152:155], v[24:27]\n" \
"v_pk_add_f16 v152, v152, v168\n" \
"v_pk_add_f16 v139, v123, v131\n" \
"s_add_i32 m0, s44, 0xa000\n" \
"s_add_i32 s80, s46, 0x4400000\n" \
"v_mfma_f32_16x16x32_f16 v[48:51], v[120:123], v[208:211], v[48:51]\n" \
"v_pk_add_f16 v153, v153, v169\n" \
"v_pk_add_f16 v140, v124, v132\n" \
"buffer_load_dwordx4 v232, s[36:39], s80 offen sc1 lds\n" \
"v_mfma_f32_16x16x32_f16 v[60:63], v[124:127], v[208:211], v[60:63]\n" \
"v_pk_add_f16 v154, v154, v170\n" \
"v_pk_add_f16 v155, v155, v171\n" \
"ds_read_b128 v[144:147], v240 offset:4608\n" \
"v_mfma_f32_16x16x32_f16 v[84:87], v[124:127], v[156:159], v[84:87]\n" \
"v_pk_add_f16 v208, v208, v172\n" \
"v_pk_add_f16 v141, v125, v133\n" \
"v_mfma_f32_16x16x32_f16 v[72:75], v[120:123], v[156:159], v[72:75]\n" \
"v_pk_add_f16 v209, v209, v173\n" \
"v_pk_add_f16 v142, v126, v134\n" \
"v_mfma_f32_16x16x32_f16 v[96:99], v[120:123], v[160:163], v[96:99]\n" \
"v_pk_add_f16 v210, v210, v174\n" \
"v_pk_add_f16 v211, v211, v175\n" \
"ds_read_b64_tr_b16 v[188:189], v236 offset:28672\n" \
"v_mfma_f32_16x16x32_f16 v[108:111], v[124:127], v[160:163], v[108:111]\n" \
"v_pk_add_f16 v156, v156, v176\n" \
"v_pk_add_f16 v143, v127, v135\n" \
"ds_read_b64_tr_b16 v[192:193], v237 offset:28672\n" \
"v_mfma_f32_16x16x32_f16 v[4:7], v[128:131], v[164:167], v[4:7]\n" \
"v_pk_add_f16 v157, v157, v177\n" \
"ds_read_b64_tr_b16 v[120:121], v241 offset:0\n" \
"buffer_load_dwordx4 v232, s[36:39], s80 offen offset:1024 sc1 lds\n" \
"v_mfma_f32_16x16x32_f16 v[16:19], v[132:135], v[164:167], v[16:19]\n" \
"v_pk_add_f16 v158, v158, v178\n" \
"ds_read_b128 v[184:187], v240 offset:2560\n" \
"v_mfma_f32_16x16x32_f16 v[40:43], v[132:135], v[168:171], v[40:43]\n" \
"v_pk_add_f16 v159, v159, v179\n" \
"ds_read_b128 v[164:167], v240 offset:2048\n" \
"v_mfma_f32_16x16x32_f16 v[28:31], v[128:131], v[168:171], v[28:31]\n" \
"v_pk_add_f16 v160, v160, v180\n" \
"ds_read_b64_tr_b16 v[122:123], v241 offset:8192\n" \
"v_mfma_f32_16x16x32_f16 v[52:55], v[128:131], v[172:175], v[52:55]\n" \
"v_pk_add_f16 v161, v161, v181\n" \
"ds_read_b128 v[168:171], v240 offset:4096\n" \
"s_waitcnt lgkmcnt(6)\n" \
"v_mfma_f32_16x16x16_bf16 v[188:191], v[188:189], v[254:255], 0\n" \
"v_mfma_f32_16x16x32_f16 v[64:67], v[132:135], v[172:175], v[64:67]\n" \
"v_pk_add_f16 v162, v162, v182\n" \
"ds_read_b64_tr_b16 v[124:125], v242 offset:0\n" \
"s_waitcnt lgkmcnt(6)\n" \
"v_mfma_f32_16x16x16_bf16 v[192:195], v[192:193], v[254:255], 0\n" \
"v_mfma_f32_16x16x32_f16 v[88:91], v[132:135], v[176:179], v[88:91]\n" \
"v_pk_add_f16 v163, v163, v183\n" \
"ds_read_b128 v[172:175], v240 offset:6144\n" \
"buffer_load_dwordx4 v232, s[36:39], s80 offen offset:2048 sc1 lds\n" \
"v_mfma_f32_16x16x32_f16 v[76:79], v[128:131], v[176:179], v[76:79]\n" \
"s_setprio 0\n" \
"ds_read_b64_tr_b16 v[126:127], v242 offset:8192\n" \
"v_mfma_f32_16x16x32_f16 v[100:103], v[128:131], v[180:183], v[100:103]\n" \
"v_cvt_pk_f16_f32 v188, v188, v189\n" \
"ds_read_b128 v[176:179], v240 offset:8192\n" \
"v_mfma_f32_16x16x32_f16 v[112:115], v[132:135], v[180:183], v[112:115]\n" \
"v_cvt_pk_f16_f32 v189, v190, v191\n" \
"ds_read_b64_tr_b16 v[128:129], v241 offset:512\n" \
"v_mfma_f32_16x16x32_f16 v[8:11], v[136:139], v[148:151], v[8:11]\n" \
"v_cvt_pk_f16_f32 v190, v192, v193\n" \
"ds_read_b128 v[180:183], v240 offset:0\n" \
"v_mfma_f32_16x16x32_f16 v[20:23], v[140:143], v[148:151], v[20:23]\n" \
"v_cvt_pk_f16_f32 v191, v194, v195\n" \
"v_add_u32 v188, s91, v188\n" \
"ds_read_b128 v[148:151], v240 offset:6656\n" \
"v_mfma_f32_16x16x32_f16 v[44:47], v[140:143], v[152:155], v[44:47]\n" \
"ds_read_b64_tr_b16 v[130:131], v241 offset:8704\n" \
"ds_read_b64_tr_b16 v[132:133], v242 offset:512\n" \
"buffer_load_dwordx4 v232, s[36:39], s80 offen offset:3072 sc1 lds\n" \
"v_mfma_f32_16x16x32_f16 v[32:35], v[136:139], v[152:155], v[32:35]\n" \
"v_add_u32 v189, s91, v189\n" \
"v_add_u32 v190, s91, v190\n" \
"ds_read_b128 v[152:155], v240 offset:8704\n" \
"v_mfma_f32_16x16x32_f16 v[56:59], v[136:139], v[208:211], v[56:59]\n" \
"v_add_u32 v191, s91, v191\n" \
"ds_read_b64_tr_b16 v[134:135], v242 offset:8704\n" \
"v_mfma_f32_16x16x32_f16 v[68:71], v[140:143], v[208:211], v[68:71]\n" \
"v_and_b32 v188, s92, v188\n" \
"ds_read_b128 v[208:211], v240 offset:512\n" \
"v_mfma_f32_16x16x32_f16 v[92:95], v[140:143], v[156:159], v[92:95]\n" \
"v_and_b32 v189, s92, v189\n" \
"s_add_i32 m0, s45, 0xa000\n" \
"s_add_i32 s81, s47, 0x4400000\n" \
"s_add_i32 s82, s48, 0x6000000\n" \
"v_mfma_f32_16x16x32_f16 v[80:83], v[136:139], v[156:159], v[80:83]\n" \
"v_and_b32 v190, s92, v190\n" \
"v_and_b32 v191, s92, v191\n" \
"buffer_load_dwordx4 v232, s[36:39], s81 offen sc1 lds\n" \
"v_mfma_f32_16x16x32_f16 v[104:107], v[136:139], v[160:163], v[104:107]\n" \
"buffer_store_dwordx4 v[188:191], v234, s[36:39], s82 offen sc1\n" \
"v_mfma_f32_16x16x32_f16 v[116:119], v[140:143], v[160:163], v[116:119]\n" \
"s_setprio 1\n" \
"s_waitcnt vmcnt(7) lgkmcnt(0)\n" \
"s_barrier\n" \
"v_mfma_f32_16x16x32_f16 v[0:3], v[120:123], v[164:167], v[0:3]\n" \
"v_pk_add_f16 v136, v120, v128\n" \
"v_pk_add_f16 v137, v121, v129\n" \
"v_mfma_f32_16x16x32_f16 v[12:15], v[124:127], v[164:167], v[12:15]\n" \
"v_pk_add_f16 v164, v164, v184\n" \
"v_pk_add_f16 v138, v122, v130\n" \
"v_mfma_f32_16x16x32_f16 v[36:39], v[124:127], v[168:171], v[36:39]\n" \
"v_pk_add_f16 v165, v165, v185\n" \
"v_pk_add_f16 v166, v166, v186\n" \
"v_pk_add_f16 v167, v167, v187\n" \
"v_mfma_f32_16x16x32_f16 v[24:27], v[120:123], v[168:171], v[24:27]\n" \
"v_pk_add_f16 v168, v168, v144\n" \
"v_pk_add_f16 v139, v123, v131\n" \
"s_add_i32 m0, s44, 0x0\n" \
"s_add_i32 s80, s46, 0x4800000\n" \
"v_mfma_f32_16x16x32_f16 v[48:51], v[120:123], v[172:175], v[48:51]\n" \
"v_pk_add_f16 v169, v169, v145\n" \
"v_pk_add_f16 v140, v124, v132\n" \
"buffer_load_dwordx4 v232, s[36:39], s80 offen sc1 lds\n" \
"v_mfma_f32_16x16x32_f16 v[60:63], v[124:127], v[172:175], v[60:63]\n" \
"v_pk_add_f16 v170, v170, v146\n" \
"v_pk_add_f16 v171, v171, v147\n" \
"ds_read_b128 v[160:163], v240 offset:25088\n" \
"v_mfma_f32_16x16x32_f16 v[84:87], v[124:127], v[176:179], v[84:87]\n" \
"v_pk_add_f16 v172, v172, v148\n" \
"v_pk_add_f16 v141, v125, v133\n" \
"v_mfma_f32_16x16x32_f16 v[72:75], v[120:123], v[176:179], v[72:75]\n" \
"v_pk_add_f16 v173, v173, v149\n" \
"v_pk_add_f16 v142, v126, v134\n" \
"v_mfma_f32_16x16x32_f16 v[96:99], v[120:123], v[180:183], v[96:99]\n" \
"v_pk_add_f16 v174, v174, v150\n" \
"v_pk_add_f16 v175, v175, v151\n" \
"ds_read_b64_tr_b16 v[188:189], v238 offset:28672\n" \
"v_mfma_f32_16x16x32_f16 v[108:111], v[124:127], v[180:183], v[108:111]\n" \
"v_pk_add_f16 v176, v176, v152\n" \
"v_pk_add_f16 v143, v127, v135\n" \
"ds_read_b64_tr_b16 v[192:193], v239 offset:28672\n" \
"v_mfma_f32_16x16x32_f16 v[4:7], v[128:131], v[184:187], v[4:7]\n" \
"v_pk_add_f16 v177, v177, v153\n" \
"ds_read_b64_tr_b16 v[120:121], v241 offset:16384\n" \
"buffer_load_dwordx4 v232, s[36:39], s80 offen offset:1024 sc1 lds\n" \
"v_mfma_f32_16x16x32_f16 v[16:19], v[132:135], v[184:187], v[16:19]\n" \
"v_pk_add_f16 v178, v178, v154\n" \
"ds_read_b128 v[156:159], v240 offset:23040\n" \
"v_mfma_f32_16x16x32_f16 v[40:43], v[132:135], v[144:147], v[40:43]\n" \
"v_pk_add_f16 v179, v179, v155\n" \
"ds_read_b128 v[184:187], v240 offset:22528\n" \
"v_mfma_f32_16x16x32_f16 v[28:31], v[128:131], v[144:147], v[28:31]\n" \
"v_pk_add_f16 v180, v180, v208\n" \
"ds_read_b64_tr_b16 v[122:123], v241 offset:24576\n" \
"v_mfma_f32_16x16x32_f16 v[52:55], v[128:131], v[148:151], v[52:55]\n" \
"v_pk_add_f16 v181, v181, v209\n" \
"ds_read_b128 v[144:147], v240 offset:24576\n" \
"s_waitcnt lgkmcnt(6)\n" \
"v_mfma_f32_16x16x16_bf16 v[188:191], v[188:189], v[254:255], 0\n" \
"v_mfma_f32_16x16x32_f16 v[64:67], v[132:135], v[148:151], v[64:67]\n" \
"v_pk_add_f16 v182, v182, v210\n" \
"ds_read_b64_tr_b16 v[124:125], v242 offset:16384\n" \
"s_waitcnt lgkmcnt(6)\n" \
"v_mfma_f32_16x16x16_bf16 v[192:195], v[192:193], v[254:255], 0\n" \
"v_mfma_f32_16x16x32_f16 v[88:91], v[132:135], v[152:155], v[88:91]\n" \
"v_pk_add_f16 v183, v183, v211\n" \
"ds_read_b128 v[148:151], v240 offset:26624\n" \
"buffer_load_dwordx4 v232, s[36:39], s80 offen offset:2048 sc1 lds\n" \
"v_mfma_f32_16x16x32_f16 v[76:79], v[128:131], v[152:155], v[76:79]\n" \
"s_setprio 0\n" \
"ds_read_b64_tr_b16 v[126:127], v242 offset:24576\n" \
"v_mfma_f32_16x16x32_f16 v[100:103], v[128:131], v[208:211], v[100:103]\n" \
"v_cvt_pk_f16_f32 v188, v188, v189\n" \
"ds_read_b128 v[152:155], v240 offset:28672\n" \
"v_mfma_f32_16x16x32_f16 v[112:115], v[132:135], v[208:211], v[112:115]\n" \
"v_cvt_pk_f16_f32 v189, v190, v191\n" \
"ds_read_b64_tr_b16 v[128:129], v241 offset:16896\n" \
"v_mfma_f32_16x16x32_f16 v[8:11], v[136:139], v[164:167], v[8:11]\n" \
"v_cvt_pk_f16_f32 v190, v192, v193\n" \
"ds_read_b128 v[208:211], v240 offset:20480\n" \
"v_mfma_f32_16x16x32_f16 v[20:23], v[140:143], v[164:167], v[20:23]\n" \
"v_cvt_pk_f16_f32 v191, v194, v195\n" \
"v_add_u32 v188, s91, v188\n" \
"ds_read_b128 v[164:167], v240 offset:27136\n" \
"v_mfma_f32_16x16x32_f16 v[44:47], v[140:143], v[168:171], v[44:47]\n" \
"ds_read_b64_tr_b16 v[130:131], v241 offset:25088\n" \
"ds_read_b64_tr_b16 v[132:133], v242 offset:16896\n" \
"buffer_load_dwordx4 v232, s[36:39], s80 offen offset:3072 sc1 lds\n" \
"v_mfma_f32_16x16x32_f16 v[32:35], v[136:139], v[168:171], v[32:35]\n" \
"v_add_u32 v189, s91, v189\n" \
"v_add_u32 v190, s91, v190\n" \
"ds_read_b128 v[168:171], v240 offset:29184\n" \
"v_mfma_f32_16x16x32_f16 v[56:59], v[136:139], v[172:175], v[56:59]\n" \
"v_add_u32 v191, s91, v191\n" \
"ds_read_b64_tr_b16 v[134:135], v242 offset:25088\n" \
"v_mfma_f32_16x16x32_f16 v[68:71], v[140:143], v[172:175], v[68:71]\n" \
"v_and_b32 v188, s92, v188\n" \
"ds_read_b128 v[172:175], v240 offset:20992\n" \
"v_mfma_f32_16x16x32_f16 v[92:95], v[140:143], v[176:179], v[92:95]\n" \
"v_and_b32 v189, s92, v189\n" \
"s_add_i32 m0, s45, 0x0\n" \
"s_add_i32 s81, s47, 0x4800000\n" \
"v_mfma_f32_16x16x32_f16 v[80:83], v[136:139], v[176:179], v[80:83]\n" \
"v_and_b32 v190, s92, v190\n" \
"v_and_b32 v191, s92, v191\n" \
"buffer_load_dwordx4 v232, s[36:39], s81 offen sc1 lds\n" \
"v_mfma_f32_16x16x32_f16 v[104:107], v[136:139], v[180:183], v[104:107]\n" \
"buffer_store_dwordx4 v[188:191], v234, s[36:39], s82 offen offset:1024 sc1\n" \
"v_mfma_f32_16x16x32_f16 v[116:119], v[140:143], v[180:183], v[116:119]\n" \
"s_setprio 1\n" \
"s_waitcnt vmcnt(7) lgkmcnt(0)\n" \
"s_barrier\n" \
"v_mfma_f32_16x16x32_f16 v[0:3], v[120:123], v[184:187], v[0:3]\n" \
"v_pk_add_f16 v136, v120, v128\n" \
"v_pk_add_f16 v137, v121, v129\n" \
"s_add_i32 m0, s54, 0x0\n" \
"s_mov_b32 s83, 0x0\n" \
"v_mfma_f32_16x16x32_f16 v[12:15], v[124:127], v[184:187], v[12:15]\n" \
"v_pk_add_f16 v184, v184, v156\n" \
"v_pk_add_f16 v138, v122, v130\n" \
"buffer_load_dwordx4 v233, s[36:39], s83 offen sc1 lds\n" \
"v_mfma_f32_16x16x32_f16 v[36:39], v[124:127], v[144:147], v[36:39]\n" \
"v_pk_add_f16 v185, v185, v157\n" \
"v_pk_add_f16 v186, v186, v158\n" \
"v_pk_add_f16 v187, v187, v159\n" \
"v_mfma_f32_16x16x32_f16 v[24:27], v[120:123], v[144:147], v[24:27]\n" \
"v_pk_add_f16 v144, v144, v160\n" \
"v_pk_add_f16 v139, v123, v131\n" \
"s_add_i32 m0, s44, 0x5000\n" \
"s_add_i32 s80, s46, 0x4c00000\n" \
"v_mfma_f32_16x16x32_f16 v[48:51], v[120:123], v[148:151], v[48:51]\n" \
"v_pk_add_f16 v145, v145, v161\n" \
"v_pk_add_f16 v140, v124, v132\n" \
"buffer_load_dwordx4 v232, s[36:39], s80 offen sc1 lds\n" \
"v_mfma_f32_16x16x32_f16 v[60:63], v[124:127], v[148:151], v[60:63]\n" \
"v_pk_add_f16 v146, v146, v162\n" \
"v_pk_add_f16 v147, v147, v163\n" \
"ds_read_b128 v[180:183], v240 offset:45568\n" \
"v_mfma_f32_16x16x32_f16 v[84:87], v[124:127], v[152:155], v[84:87]\n" \
"v_pk_add_f16 v148, v148, v164\n" \
"v_pk_add_f16 v141, v125, v133\n" \
"v_mfma_f32_16x16x32_f16 v[72:75], v[120:123], v[152:155], v[72:75]\n" \
"v_pk_add_f16 v149, v149, v165\n" \
"v_pk_add_f16 v142, v126, v134\n" \
"v_mfma_f32_16x16x32_f16 v[96:99], v[120:123], v[208:211], v[96:99]\n" \
"v_pk_add_f16 v150, v150, v166\n" \
"v_pk_add_f16 v151, v151, v167\n" \
"ds_read_b64_tr_b16 v[188:189], v236 offset:32768\n" \
"v_mfma_f32_16x16x32_f16 v[108:111], v[124:127], v[208:211], v[108:111]\n" \
"v_pk_add_f16 v152, v152, v168\n" \
"v_pk_add_f16 v143, v127, v135\n" \
"ds_read_b64_tr_b16 v[192:193], v237 offset:32768\n" \
"v_mfma_f32_16x16x32_f16 v[4:7], v[128:131], v[156:159], v[4:7]\n" \
"v_pk_add_f16 v153, v153, v169\n" \
"ds_read_b64_tr_b16 v[120:121], v241 offset:32768\n" \
"buffer_load_dwordx4 v232, s[36:39], s80 offen offset:1024 sc1 lds\n" \
"v_mfma_f32_16x16x32_f16 v[16:19], v[132:135], v[156:159], v[16:19]\n" \
"v_pk_add_f16 v154, v154, v170\n" \
"ds_read_b128 v[176:179], v240 offset:43520\n" \
"v_mfma_f32_16x16x32_f16 v[40:43], v[132:135], v[160:163], v[40:43]\n" \
"v_pk_add_f16 v155, v155, v171\n" \
"ds_read_b128 v[156:159], v240 offset:43008\n" \
"v_mfma_f32_16x16x32_f16 v[28:31], v[128:131], v[160:163], v[28:31]\n" \
"v_pk_add_f16 v208, v208, v172\n" \
"ds_read_b64_tr_b16 v[122:123], v241 offset:40960\n" \
"v_mfma_f32_16x16x32_f16 v[52:55], v[128:131], v[164:167], v[52:55]\n" \
"v_pk_add_f16 v209, v209, v173\n" \
"ds_read_b128 v[160:163], v240 offset:45056\n" \
"s_waitcnt lgkmcnt(6)\n" \
"v_mfma_f32_16x16x16_bf16 v[188:191], v[188:189], v[254:255], 0\n" \
"v_mfma_f32_16x16x32_f16 v[64:67], v[132:135], v[164:167], v[64:67]\n" \
"v_pk_add_f16 v210, v210, v174\n" \
"ds_read_b64_tr_b16 v[124:125], v242 offset:32768\n" \
"s_waitcnt lgkmcnt(6)\n" \
"v_mfma_f32_16x16x16_bf16 v[192:195], v[192:193], v[254:255], 0\n" \
"v_mfma_f32_16x16x32_f16 v[88:91], v[132:135], v[168:171], v[88:91]\n" \
"v_pk_add_f16 v211, v211, v175\n" \
"ds_read_b128 v[164:167], v240 offset:47104\n" \
"buffer_load_dwordx4 v232, s[36:39], s80 offen offset:2048 sc1 lds\n" \
"v_mfma_f32_16x16x32_f16 v[76:79], v[128:131], v[168:171], v[76:79]\n" \
"s_setprio 0\n" \
"ds_read_b64_tr_b16 v[126:127], v242 offset:40960\n" \
"v_mfma_f32_16x16x32_f16 v[100:103], v[128:131], v[172:175], v[100:103]\n" \
"v_cvt_pk_f16_f32 v188, v188, v189\n" \
"ds_read_b128 v[168:171], v240 offset:49152\n" \
"v_mfma_f32_16x16x32_f16 v[112:115], v[132:135], v[172:175], v[112:115]\n" \
"v_cvt_pk_f16_f32 v189, v190, v191\n" \
"ds_read_b64_tr_b16 v[128:129], v241 offset:33280\n" \
"v_mfma_f32_16x16x32_f16 v[8:11], v[136:139], v[184:187], v[8:11]\n" \
"v_cvt_pk_f16_f32 v190, v192, v193\n" \
"ds_read_b128 v[172:175], v240 offset:40960\n" \
"v_mfma_f32_16x16x32_f16 v[20:23], v[140:143], v[184:187], v[20:23]\n" \
"v_cvt_pk_f16_f32 v191, v194, v195\n" \
"v_add_u32 v188, s91, v188\n" \
"ds_read_b128 v[184:187], v240 offset:47616\n" \
"v_mfma_f32_16x16x32_f16 v[44:47], v[140:143], v[144:147], v[44:47]\n" \
"ds_read_b64_tr_b16 v[130:131], v241 offset:41472\n" \
"ds_read_b64_tr_b16 v[132:133], v242 offset:33280\n" \
"buffer_load_dwordx4 v232, s[36:39], s80 offen offset:3072 sc1 lds\n" \
"v_mfma_f32_16x16x32_f16 v[32:35], v[136:139], v[144:147], v[32:35]\n" \
"v_add_u32 v189, s91, v189\n" \
"v_add_u32 v190, s91, v190\n" \
"ds_read_b128 v[144:147], v240 offset:49664\n" \
"v_mfma_f32_16x16x32_f16 v[56:59], v[136:139], v[148:151], v[56:59]\n" \
"v_add_u32 v191, s91, v191\n" \
"ds_read_b64_tr_b16 v[134:135], v242 offset:41472\n" \
"v_mfma_f32_16x16x32_f16 v[68:71], v[140:143], v[148:151], v[68:71]\n" \
"v_and_b32 v188, s92, v188\n" \
"ds_read_b128 v[148:151], v240 offset:41472\n" \
"v_mfma_f32_16x16x32_f16 v[92:95], v[140:143], v[152:155], v[92:95]\n" \
"v_and_b32 v189, s92, v189\n" \
"s_add_i32 m0, s45, 0x5000\n" \
"s_add_i32 s81, s47, 0x4c00000\n" \
"s_add_i32 s82, s49, 0x6800000\n" \
"v_mfma_f32_16x16x32_f16 v[80:83], v[136:139], v[152:155], v[80:83]\n" \
"v_and_b32 v190, s92, v190\n" \
"v_and_b32 v191, s92, v191\n" \
"buffer_load_dwordx4 v232, s[36:39], s81 offen sc1 lds\n" \
"v_mfma_f32_16x16x32_f16 v[104:107], v[136:139], v[208:211], v[104:107]\n" \
"buffer_store_dwordx4 v[188:191], v234, s[36:39], s82 offen sc1\n" \
"v_mfma_f32_16x16x32_f16 v[116:119], v[140:143], v[208:211], v[116:119]\n" \
"s_setprio 1\n" \
"s_waitcnt vmcnt(7) lgkmcnt(0)\n" \
"s_barrier\n" \
"v_mfma_f32_16x16x32_f16 v[0:3], v[120:123], v[156:159], v[0:3]\n" \
"v_pk_add_f16 v136, v120, v128\n" \
"v_pk_add_f16 v137, v121, v129\n" \
"v_mfma_f32_16x16x32_f16 v[12:15], v[124:127], v[156:159], v[12:15]\n" \
"v_pk_add_f16 v156, v156, v176\n" \
"v_pk_add_f16 v138, v122, v130\n" \
"v_mfma_f32_16x16x32_f16 v[36:39], v[124:127], v[160:163], v[36:39]\n" \
"v_pk_add_f16 v157, v157, v177\n" \
"v_pk_add_f16 v158, v158, v178\n" \
"v_pk_add_f16 v159, v159, v179\n" \
"v_mfma_f32_16x16x32_f16 v[24:27], v[120:123], v[160:163], v[24:27]\n" \
"v_pk_add_f16 v160, v160, v180\n" \
"v_pk_add_f16 v139, v123, v131\n" \
"s_add_i32 m0, s44, 0xa000\n" \
"s_add_i32 s80, s46, 0x5000000\n" \
"v_mfma_f32_16x16x32_f16 v[48:51], v[120:123], v[164:167], v[48:51]\n" \
"v_pk_add_f16 v161, v161, v181\n" \
"v_pk_add_f16 v140, v124, v132\n" \
"buffer_load_dwordx4 v232, s[36:39], s80 offen sc1 lds\n" \
"v_mfma_f32_16x16x32_f16 v[60:63], v[124:127], v[164:167], v[60:63]\n" \
"v_pk_add_f16 v162, v162, v182\n" \
"v_pk_add_f16 v163, v163, v183\n" \
"ds_read_b128 v[208:211], v240 offset:4608\n" \
"v_mfma_f32_16x16x32_f16 v[84:87], v[124:127], v[168:171], v[84:87]\n" \
"v_pk_add_f16 v164, v164, v184\n" \
"v_pk_add_f16 v141, v125, v133\n" \
"v_mfma_f32_16x16x32_f16 v[72:75], v[120:123], v[168:171], v[72:75]\n" \
"v_pk_add_f16 v165, v165, v185\n" \
"v_pk_add_f16 v142, v126, v134\n" \
"v_mfma_f32_16x16x32_f16 v[96:99], v[120:123], v[172:175], v[96:99]\n" \
"v_pk_add_f16 v166, v166, v186\n" \
"v_pk_add_f16 v167, v167, v187\n" \
"ds_read_b64_tr_b16 v[188:189], v238 offset:32768\n" \
"v_mfma_f32_16x16x32_f16 v[108:111], v[124:127], v[172:175], v[108:111]\n" \
"v_pk_add_f16 v168, v168, v144\n" \
"v_pk_add_f16 v143, v127, v135\n" \
"ds_read_b64_tr_b16 v[192:193], v239 offset:32768\n" \
"v_mfma_f32_16x16x32_f16 v[4:7], v[128:131], v[176:179], v[4:7]\n" \
"v_pk_add_f16 v169, v169, v145\n" \
"ds_read_b64_tr_b16 v[120:121], v241 offset:0\n" \
"buffer_load_dwordx4 v232, s[36:39], s80 offen offset:1024 sc1 lds\n" \
"v_mfma_f32_16x16x32_f16 v[16:19], v[132:135], v[176:179], v[16:19]\n" \
"v_pk_add_f16 v170, v170, v146\n" \
"ds_read_b128 v[152:155], v240 offset:2560\n" \
"v_mfma_f32_16x16x32_f16 v[40:43], v[132:135], v[180:183], v[40:43]\n" \
"v_pk_add_f16 v171, v171, v147\n" \
"ds_read_b128 v[176:179], v240 offset:2048\n" \
"v_mfma_f32_16x16x32_f16 v[28:31], v[128:131], v[180:183], v[28:31]\n" \
"v_pk_add_f16 v172, v172, v148\n" \
"ds_read_b64_tr_b16 v[122:123], v241 offset:8192\n" \
"v_mfma_f32_16x16x32_f16 v[52:55], v[128:131], v[184:187], v[52:55]\n" \
"v_pk_add_f16 v173, v173, v149\n" \
"ds_read_b128 v[180:183], v240 offset:4096\n" \
"s_waitcnt lgkmcnt(6)\n" \
"v_mfma_f32_16x16x16_bf16 v[188:191], v[188:189], v[254:255], 0\n" \
"v_mfma_f32_16x16x32_f16 v[64:67], v[132:135], v[184:187], v[64:67]\n" \
"v_pk_add_f16 v174, v174, v150\n" \
"ds_read_b64_tr_b16 v[124:125], v242 offset:0\n" \
"s_waitcnt lgkmcnt(6)\n" \
"v_mfma_f32_16x16x16_bf16 v[192:195], v[192:193], v[254:255], 0\n" \
"v_mfma_f32_16x16x32_f16 v[88:91], v[132:135], v[144:147], v[88:91]\n" \
"v_pk_add_f16 v175, v175, v151\n" \
"ds_read_b128 v[184:187], v240 offset:6144\n" \
"buffer_load_dwordx4 v232, s[36:39], s80 offen offset:2048 sc1 lds\n" \
"v_mfma_f32_16x16x32_f16 v[76:79], v[128:131], v[144:147], v[76:79]\n" \
"s_setprio 0\n" \
"ds_read_b64_tr_b16 v[126:127], v242 offset:8192\n" \
"v_mfma_f32_16x16x32_f16 v[100:103], v[128:131], v[148:151], v[100:103]\n" \
"v_cvt_pk_f16_f32 v188, v188, v189\n" \
"ds_read_b128 v[144:147], v240 offset:8192\n" \
"v_mfma_f32_16x16x32_f16 v[112:115], v[132:135], v[148:151], v[112:115]\n" \
"v_cvt_pk_f16_f32 v189, v190, v191\n" \
"ds_read_b64_tr_b16 v[128:129], v241 offset:512\n" \
"v_mfma_f32_16x16x32_f16 v[8:11], v[136:139], v[156:159], v[8:11]\n" \
"v_cvt_pk_f16_f32 v190, v192, v193\n" \
"ds_read_b128 v[148:151], v240 offset:0\n" \
"v_mfma_f32_16x16x32_f16 v[20:23], v[140:143], v[156:159], v[20:23]\n" \
"v_cvt_pk_f16_f32 v191, v194, v195\n" \
"v_add_u32 v188, s91, v188\n" \
"ds_read_b128 v[156:159], v240 offset:6656\n" \
"v_mfma_f32_16x16x32_f16 v[44:47], v[140:143], v[160:163], v[44:47]\n" \
"ds_read_b64_tr_b16 v[130:131], v241 offset:8704\n" \
"ds_read_b64_tr_b16 v[132:133], v242 offset:512\n" \
"buffer_load_dwordx4 v232, s[36:39], s80 offen offset:3072 sc1 lds\n" \
"v_mfma_f32_16x16x32_f16 v[32:35], v[136:139], v[160:163], v[32:35]\n" \
"v_add_u32 v189, s91, v189\n" \
"v_add_u32 v190, s91, v190\n" \
"ds_read_b128 v[160:163], v240 offset:8704\n" \
"v_mfma_f32_16x16x32_f16 v[56:59], v[136:139], v[164:167], v[56:59]\n" \
"v_add_u32 v191, s91, v191\n" \
"ds_read_b64_tr_b16 v[134:135], v242 offset:8704\n" \
"v_mfma_f32_16x16x32_f16 v[68:71], v[140:143], v[164:167], v[68:71]\n" \
"v_and_b32 v188, s92, v188\n" \
"ds_read_b128 v[164:167], v240 offset:512\n" \
"v_mfma_f32_16x16x32_f16 v[92:95], v[140:143], v[168:171], v[92:95]\n" \
"v_and_b32 v189, s92, v189\n" \
"s_add_i32 m0, s45, 0xa000\n" \
"s_add_i32 s81, s47, 0x5000000\n" \
"v_mfma_f32_16x16x32_f16 v[80:83], v[136:139], v[168:171], v[80:83]\n" \
"v_and_b32 v190, s92, v190\n" \
"v_and_b32 v191, s92, v191\n" \
"buffer_load_dwordx4 v232, s[36:39], s81 offen sc1 lds\n" \
"v_mfma_f32_16x16x32_f16 v[104:107], v[136:139], v[172:175], v[104:107]\n" \
"buffer_store_dwordx4 v[188:191], v234, s[36:39], s82 offen offset:1024 sc1\n" \
"v_mfma_f32_16x16x32_f16 v[116:119], v[140:143], v[172:175], v[116:119]\n" \
"s_setprio 1\n" \
"s_waitcnt vmcnt(7) lgkmcnt(0)\n" \
"s_barrier\n" \
"v_mfma_f32_16x16x32_f16 v[0:3], v[120:123], v[176:179], v[0:3]\n" \
"v_pk_add_f16 v136, v120, v128\n" \
"v_pk_add_f16 v137, v121, v129\n" \
"s_add_i32 m0, s54, 0x1000\n" \
"s_mov_b32 s83, 0x100\n" \
"v_mfma_f32_16x16x32_f16 v[12:15], v[124:127], v[176:179], v[12:15]\n" \
"v_pk_add_f16 v176, v176, v152\n" \
"v_pk_add_f16 v138, v122, v130\n" \
"buffer_load_dwordx4 v233, s[36:39], s83 offen sc1 lds\n" \
"v_mfma_f32_16x16x32_f16 v[36:39], v[124:127], v[180:183], v[36:39]\n" \
"v_pk_add_f16 v177, v177, v153\n" \
"v_pk_add_f16 v178, v178, v154\n" \
"v_pk_add_f16 v179, v179, v155\n" \
"v_mfma_f32_16x16x32_f16 v[24:27], v[120:123], v[180:183], v[24:27]\n" \
"v_pk_add_f16 v180, v180, v208\n" \
"v_pk_add_f16 v139, v123, v131\n" \
"s_add_i32 m0, s44, 0x0\n" \
"s_add_i32 s80, s46, 0x5400000\n" \
"v_mfma_f32_16x16x32_f16 v[48:51], v[120:123], v[184:187], v[48:51]\n" \
"v_pk_add_f16 v181, v181, v209\n" \
"v_pk_add_f16 v140, v124, v132\n" \
"buffer_load_dwordx4 v232, s[36:39], s80 offen sc1 lds\n" \
"v_mfma_f32_16x16x32_f16 v[60:63], v[124:127], v[184:187], v[60:63]\n" \
"v_pk_add_f16 v182, v182, v210\n" \
"v_pk_add_f16 v183, v183, v211\n" \
"ds_read_b128 v[172:175], v240 offset:25088\n" \
"v_mfma_f32_16x16x32_f16 v[84:87], v[124:127], v[144:147], v[84:87]\n" \
"v_pk_add_f16 v184, v184, v156\n" \
"v_pk_add_f16 v141, v125, v133\n" \
"v_mfma_f32_16x16x32_f16 v[72:75], v[120:123], v[144:147], v[72:75]\n" \
"v_pk_add_f16 v185, v185, v157\n" \
"v_pk_add_f16 v142, v126, v134\n" \
"v_mfma_f32_16x16x32_f16 v[96:99], v[120:123], v[148:151], v[96:99]\n" \
"v_pk_add_f16 v186, v186, v158\n" \
"v_pk_add_f16 v187, v187, v159\n" \
"ds_read_b64_tr_b16 v[188:189], v236 offset:36864\n" \
"v_mfma_f32_16x16x32_f16 v[108:111], v[124:127], v[148:151], v[108:111]\n" \
"v_pk_add_f16 v144, v144, v160\n" \
"v_pk_add_f16 v143, v127, v135\n" \
"ds_read_b64_tr_b16 v[192:193], v237 offset:36864\n" \
"v_mfma_f32_16x16x32_f16 v[4:7], v[128:131], v[152:155], v[4:7]\n" \
"v_pk_add_f16 v145, v145, v161\n" \
"ds_read_b64_tr_b16 v[120:121], v241 offset:16384\n" \
"buffer_load_dwordx4 v232, s[36:39], s80 offen offset:1024 sc1 lds\n" \
"v_mfma_f32_16x16x32_f16 v[16:19], v[132:135], v[152:155], v[16:19]\n" \
"v_pk_add_f16 v146, v146, v162\n" \
"ds_read_b128 v[168:171], v240 offset:23040\n" \
"v_mfma_f32_16x16x32_f16 v[40:43], v[132:135], v[208:211], v[40:43]\n" \
"v_pk_add_f16 v147, v147, v163\n" \
"ds_read_b128 v[152:155], v240 offset:22528\n" \
"v_mfma_f32_16x16x32_f16 v[28:31], v[128:131], v[208:211], v[28:31]\n" \
"v_pk_add_f16 v148, v148, v164\n" \
"ds_read_b64_tr_b16 v[122:123], v241 offset:24576\n" \
"v_mfma_f32_16x16x32_f16 v[52:55], v[128:131], v[156:159], v[52:55]\n" \
"v_pk_add_f16 v149, v149, v165\n" \
"ds_read_b128 v[208:211], v240 offset:24576\n" \
"s_waitcnt lgkmcnt(6)\n" \
"v_mfma_f32_16x16x16_bf16 v[188:191], v[188:189], v[254:255], 0\n" \
"v_mfma_f32_16x16x32_f16 v[64:67], v[132:135], v[156:159], v[64:67]\n" \
"v_pk_add_f16 v150, v150, v166\n" \
"ds_read_b64_tr_b16 v[124:125], v242 offset:16384\n" \
"s_waitcnt lgkmcnt(6)\n" \
"v_mfma_f32_16x16x16_bf16 v[192:195], v[192:193], v[254:255], 0\n" \
"v_mfma_f32_16x16x32_f16 v[88:91], v[132:135], v[160:163], v[88:91]\n" \
"v_pk_add_f16 v151, v151, v167\n" \
"ds_read_b128 v[156:159], v240 offset:26624\n" \
"buffer_load_dwordx4 v232, s[36:39], s80 offen offset:2048 sc1 lds\n" \
"v_mfma_f32_16x16x32_f16 v[76:79], v[128:131], v[160:163], v[76:79]\n" \
"s_setprio 0\n" \
"ds_read_b64_tr_b16 v[126:127], v242 offset:24576\n" \
"v_mfma_f32_16x16x32_f16 v[100:103], v[128:131], v[164:167], v[100:103]\n" \
"v_cvt_pk_f16_f32 v188, v188, v189\n" \
"ds_read_b128 v[160:163], v240 offset:28672\n" \
"v_mfma_f32_16x16x32_f16 v[112:115], v[132:135], v[164:167], v[112:115]\n" \
"v_cvt_pk_f16_f32 v189, v190, v191\n" \
"ds_read_b64_tr_b16 v[128:129], v241 offset:16896\n" \
"v_mfma_f32_16x16x32_f16 v[8:11], v[136:139], v[176:179], v[8:11]\n" \
"v_cvt_pk_f16_f32 v190, v192, v193\n" \
"ds_read_b128 v[164:167], v240 offset:20480\n" \
"v_mfma_f32_16x16x32_f16 v[20:23], v[140:143], v[176:179], v[20:23]\n" \
"v_cvt_pk_f16_f32 v191, v194, v195\n" \
"v_add_u32 v188, s91, v188\n" \
"ds_read_b128 v[176:179], v240 offset:27136\n" \
"v_mfma_f32_16x16x32_f16 v[44:47], v[140:143], v[180:183], v[44:47]\n" \
"ds_read_b64_tr_b16 v[130:131], v241 offset:25088\n" \
"ds_read_b64_tr_b16 v[132:133], v242 offset:16896\n" \
"buffer_load_dwordx4 v232, s[36:39], s80 offen offset:3072 sc1 lds\n" \
"v_mfma_f32_16x16x32_f16 v[32:35], v[136:139], v[180:183], v[32:35]\n" \
"v_add_u32 v189, s91, v189\n" \
"v_add_u32 v190, s91, v190\n" \
"ds_read_b128 v[180:183], v240 offset:29184\n" \
"v_mfma_f32_16x16x32_f16 v[56:59], v[136:139], v[184:187], v[56:59]\n" \
"v_add_u32 v191, s91, v191\n" \
"ds_read_b64_tr_b16 v[134:135], v242 offset:25088\n" \
"v_mfma_f32_16x16x32_f16 v[68:71], v[140:143], v[184:187], v[68:71]\n" \
"v_and_b32 v188, s92, v188\n" \
"ds_read_b128 v[184:187], v240 offset:20992\n" \
"v_mfma_f32_16x16x32_f16 v[92:95], v[140:143], v[144:147], v[92:95]\n" \
"v_and_b32 v189, s92, v189\n" \
"s_add_i32 m0, s45, 0x0\n" \
"s_add_i32 s81, s47, 0x5400000\n" \
"s_add_i32 s82, s50, 0x7000000\n" \
"v_mfma_f32_16x16x32_f16 v[80:83], v[136:139], v[144:147], v[80:83]\n" \
"v_and_b32 v190, s92, v190\n" \
"v_and_b32 v191, s92, v191\n" \
"buffer_load_dwordx4 v232, s[36:39], s81 offen sc1 lds\n" \
"v_mfma_f32_16x16x32_f16 v[104:107], v[136:139], v[148:151], v[104:107]\n" \
"buffer_store_dwordx4 v[188:191], v234, s[36:39], s82 offen sc1\n" \
"v_mfma_f32_16x16x32_f16 v[116:119], v[140:143], v[148:151], v[116:119]\n" \
"s_setprio 1\n" \
"s_waitcnt vmcnt(7) lgkmcnt(0)\n" \
"s_barrier\n" \
"v_mfma_f32_16x16x32_f16 v[0:3], v[120:123], v[152:155], v[0:3]\n" \
"v_pk_add_f16 v136, v120, v128\n" \
"v_pk_add_f16 v137, v121, v129\n" \
"v_mfma_f32_16x16x32_f16 v[12:15], v[124:127], v[152:155], v[12:15]\n" \
"v_pk_add_f16 v152, v152, v168\n" \
"v_pk_add_f16 v138, v122, v130\n" \
"v_mfma_f32_16x16x32_f16 v[36:39], v[124:127], v[208:211], v[36:39]\n" \
"v_pk_add_f16 v153, v153, v169\n" \
"v_pk_add_f16 v154, v154, v170\n" \
"v_pk_add_f16 v155, v155, v171\n" \
"v_mfma_f32_16x16x32_f16 v[24:27], v[120:123], v[208:211], v[24:27]\n" \
"v_pk_add_f16 v208, v208, v172\n" \
"v_pk_add_f16 v139, v123, v131\n" \
"s_add_i32 m0, s44, 0x5000\n" \
"s_add_i32 s80, s46, 0x5800000\n" \
"v_mfma_f32_16x16x32_f16 v[48:51], v[120:123], v[156:159], v[48:51]\n" \
"v_pk_add_f16 v209, v209, v173\n" \
"v_pk_add_f16 v140, v124, v132\n" \
"buffer_load_dwordx4 v232, s[36:39], s80 offen sc1 lds\n" \
"v_mfma_f32_16x16x32_f16 v[60:63], v[124:127], v[156:159], v[60:63]\n" \
"v_pk_add_f16 v210, v210, v174\n" \
"v_pk_add_f16 v211, v211, v175\n" \
"ds_read_b128 v[148:151], v240 offset:45568\n" \
"v_mfma_f32_16x16x32_f16 v[84:87], v[124:127], v[160:163], v[84:87]\n" \
"v_pk_add_f16 v156, v156, v176\n" \
"v_pk_add_f16 v141, v125, v133\n" \
"v_mfma_f32_16x16x32_f16 v[72:75], v[120:123], v[160:163], v[72:75]\n" \
"v_pk_add_f16 v157, v157, v177\n" \
"v_pk_add_f16 v142, v126, v134\n" \
"v_mfma_f32_16x16x32_f16 v[96:99], v[120:123], v[164:167], v[96:99]\n" \
"v_pk_add_f16 v158, v158, v178\n" \
"v_pk_add_f16 v159, v159, v179\n" \
"ds_read_b64_tr_b16 v[188:189], v238 offset:36864\n" \
"v_mfma_f32_16x16x32_f16 v[108:111], v[124:127], v[164:167], v[108:111]\n" \
"v_pk_add_f16 v160, v160, v180\n" \
"v_pk_add_f16 v143, v127, v135\n" \
"ds_read_b64_tr_b16 v[192:193], v239 offset:36864\n" \
"v_mfma_f32_16x16x32_f16 v[4:7], v[128:131], v[168:171], v[4:7]\n" \
"v_pk_add_f16 v161, v161, v181\n" \
"ds_read_b64_tr_b16 v[120:121], v241 offset:32768\n" \
"buffer_load_dwordx4 v232, s[36:39], s80 offen offset:1024 sc1 lds\n" \
"v_mfma_f32_16x16x32_f16 v[16:19], v[132:135], v[168:171], v[16:19]\n" \
"v_pk_add_f16 v162, v162, v182\n" \
"ds_read_b128 v[144:147], v240 offset:43520\n" \
"v_mfma_f32_16x16x32_f16 v[40:43], v[132:135], v[172:175], v[40:43]\n" \
"v_pk_add_f16 v163, v163, v183\n" \
"ds_read_b128 v[168:171], v240 offset:43008\n" \
"v_mfma_f32_16x16x32_f16 v[28:31], v[128:131], v[172:175], v[28:31]\n" \
"v_pk_add_f16 v164, v164, v184\n" \
"ds_read_b64_tr_b16 v[122:123], v241 offset:40960\n" \
"v_mfma_f32_16x16x32_f16 v[52:55], v[128:131], v[176:179], v[52:55]\n" \
"v_pk_add_f16 v165, v165, v185\n" \
"ds_read_b128 v[172:175], v240 offset:45056\n" \
"s_waitcnt lgkmcnt(6)\n" \
"v_mfma_f32_16x16x16_bf16 v[188:191], v[188:189], v[254:255], 0\n" \
"v_mfma_f32_16x16x32_f16 v[64:67], v[132:135], v[176:179], v[64:67]\n" \
"v_pk_add_f16 v166, v166, v186\n" \
"ds_read_b64_tr_b16 v[124:125], v242 offset:32768\n" \
"s_waitcnt lgkmcnt(6)\n" \
"v_mfma_f32_16x16x16_bf16 v[192:195], v[192:193], v[254:255], 0\n" \
"v_mfma_f32_16x16x32_f16 v[88:91], v[132:135], v[180:183], v[88:91]\n" \
"v_pk_add_f16 v167, v167, v187\n" \
"ds_read_b128 v[176:179], v240 offset:47104\n" \
"buffer_load_dwordx4 v232, s[36:39], s80 offen offset:2048 sc1 lds\n" \
"v_mfma_f32_16x16x32_f16 v[76:79], v[128:131], v[180:183], v[76:79]\n" \
"s_setprio 0\n" \
"ds_read_b64_tr_b16 v[126:127], v242 offset:40960\n" \
"v_mfma_f32_16x16x32_f16 v[100:103], v[128:131], v[184:187], v[100:103]\n" \
"v_cvt_pk_f16_f32 v188, v188, v189\n" \
"ds_read_b128 v[180:183], v240 offset:49152\n" \
"v_mfma_f32_16x16x32_f16 v[112:115], v[132:135], v[184:187], v[112:115]\n" \
"v_cvt_pk_f16_f32 v189, v190, v191\n" \
"ds_read_b64_tr_b16 v[128:129], v241 offset:33280\n" \
"v_mfma_f32_16x16x32_f16 v[8:11], v[136:139], v[152:155], v[8:11]\n" \
"v_cvt_pk_f16_f32 v190, v192, v193\n" \
"ds_read_b128 v[184:187], v240 offset:40960\n" \
"v_mfma_f32_16x16x32_f16 v[20:23], v[140:143], v[152:155], v[20:23]\n" \
"v_cvt_pk_f16_f32 v191, v194, v195\n" \
"v_add_u32 v188, s91, v188\n" \
"ds_read_b128 v[152:155], v240 offset:47616\n" \
"v_mfma_f32_16x16x32_f16 v[44:47], v[140:143], v[208:211], v[44:47]\n" \
"ds_read_b64_tr_b16 v[130:131], v241 offset:41472\n" \
"ds_read_b64_tr_b16 v[132:133], v242 offset:33280\n" \
"buffer_load_dwordx4 v232, s[36:39], s80 offen offset:3072 sc1 lds\n" \
"v_mfma_f32_16x16x32_f16 v[32:35], v[136:139], v[208:211], v[32:35]\n" \
"v_add_u32 v189, s91, v189\n" \
"v_add_u32 v190, s91, v190\n" \
"ds_read_b128 v[208:211], v240 offset:49664\n" \
"v_mfma_f32_16x16x32_f16 v[56:59], v[136:139], v[156:159], v[56:59]\n" \
"v_add_u32 v191, s91, v191\n" \
"ds_read_b64_tr_b16 v[134:135], v242 offset:41472\n" \
"v_mfma_f32_16x16x32_f16 v[68:71], v[140:143], v[156:159], v[68:71]\n" \
"v_and_b32 v188, s92, v188\n" \
"ds_read_b128 v[156:159], v240 offset:41472\n" \
"v_mfma_f32_16x16x32_f16 v[92:95], v[140:143], v[160:163], v[92:95]\n" \
"v_and_b32 v189, s92, v189\n" \
"s_add_i32 m0, s45, 0x5000\n" \
"s_add_i32 s81, s47, 0x5800000\n" \
"v_mfma_f32_16x16x32_f16 v[80:83], v[136:139], v[160:163], v[80:83]\n" \
"v_and_b32 v190, s92, v190\n" \
"v_and_b32 v191, s92, v191\n" \
"buffer_load_dwordx4 v232, s[36:39], s81 offen sc1 lds\n" \
"v_mfma_f32_16x16x32_f16 v[104:107], v[136:139], v[164:167], v[104:107]\n" \
"buffer_store_dwordx4 v[188:191], v234, s[36:39], s82 offen offset:1024 sc1\n" \
"v_mfma_f32_16x16x32_f16 v[116:119], v[140:143], v[164:167], v[116:119]\n" \
"s_setprio 1\n" \
"s_waitcnt vmcnt(7) lgkmcnt(0)\n" \
"s_barrier\n" \
"v_mfma_f32_16x16x32_f16 v[0:3], v[120:123], v[168:171], v[0:3]\n" \
"v_pk_add_f16 v136, v120, v128\n" \
"v_pk_add_f16 v137, v121, v129\n" \
"ds_read_b64_tr_b16 v[188:189], v236 offset:40960\n" \
"s_add_i32 m0, s54, 0x0\n" \
"s_mov_b32 s83, 0x200\n" \
"v_mfma_f32_16x16x32_f16 v[12:15], v[124:127], v[168:171], v[12:15]\n" \
"v_pk_add_f16 v168, v168, v144\n" \
"v_pk_add_f16 v138, v122, v130\n" \
"s_mov_b32 s84, 0x8014000\n" \
"ds_read_b64_tr_b16 v[192:193], v237 offset:40960\n" \
"buffer_load_dwordx4 v233, s[36:39], s83 offen sc1 lds\n" \
"v_mfma_f32_16x16x32_f16 v[36:39], v[124:127], v[172:175], v[36:39]\n" \
"v_pk_add_f16 v169, v169, v145\n" \
"v_pk_add_f16 v170, v170, v146\n" \
"v_pk_add_f16 v171, v171, v147\n" \
"buffer_load_dword v253, v252, s[36:39], s84 offen sc1\n" \
"v_mfma_f32_16x16x32_f16 v[24:27], v[120:123], v[172:175], v[24:27]\n" \
"v_pk_add_f16 v172, v172, v148\n" \
"v_pk_add_f16 v139, v123, v131\n" \
"s_add_i32 m0, s44, 0xa000\n" \
"s_add_i32 s80, s46, 0x5c00000\n" \
"v_mfma_f32_16x16x32_f16 v[48:51], v[120:123], v[176:179], v[48:51]\n" \
"v_pk_add_f16 v173, v173, v149\n" \
"v_pk_add_f16 v140, v124, v132\n" \
"buffer_load_dwordx4 v232, s[36:39], s80 offen sc1 lds\n" \
"v_mfma_f32_16x16x32_f16 v[60:63], v[124:127], v[176:179], v[60:63]\n" \
"v_pk_add_f16 v174, v174, v150\n" \
"v_pk_add_f16 v175, v175, v151\n" \
"ds_read_b128 v[164:167], v240 offset:4608\n" \
"s_waitcnt lgkmcnt(2)\n" \
"v_mfma_f32_16x16x16_bf16 v[188:191], v[188:189], v[254:255], 0\n" \
"v_mfma_f32_16x16x32_f16 v[84:87], v[124:127], v[180:183], v[84:87]\n" \
"v_pk_add_f16 v176, v176, v152\n" \
"v_pk_add_f16 v141, v125, v133\n" \
"s_waitcnt lgkmcnt(1)\n" \
"v_mfma_f32_16x16x16_bf16 v[192:195], v[192:193], v[254:255], 0\n" \
"v_mfma_f32_16x16x32_f16 v[72:75], v[120:123], v[180:183], v[72:75]\n" \
"v_pk_add_f16 v177, v177, v153\n" \
"v_pk_add_f16 v142, v126, v134\n" \
"v_mfma_f32_16x16x32_f16 v[96:99], v[120:123], v[184:187], v[96:99]\n" \
"v_pk_add_f16 v178, v178, v154\n" \
"v_pk_add_f16 v179, v179, v155\n" \
"v_mfma_f32_16x16x32_f16 v[108:111], v[124:127], v[184:187], v[108:111]\n" \
"v_pk_add_f16 v180, v180, v208\n" \
"v_pk_add_f16 v143, v127, v135\n" \
"v_cvt_pk_f16_f32 v188, v188, v189\n" \
"v_cvt_pk_f16_f32 v189, v190, v191\n" \
"v_mfma_f32_16x16x32_f16 v[4:7], v[128:131], v[144:147], v[4:7]\n" \
"v_pk_add_f16 v181, v181, v209\n" \
"v_cvt_pk_f16_f32 v190, v192, v193\n" \
"ds_read_b64_tr_b16 v[120:121], v241 offset:0\n" \
"buffer_load_dwordx4 v232, s[36:39], s80 offen offset:1024 sc1 lds\n" \
"v_mfma_f32_16x16x32_f16 v[16:19], v[132:135], v[144:147], v[16:19]\n" \
"v_pk_add_f16 v182, v182, v210\n" \
"v_cvt_pk_f16_f32 v191, v194, v195\n" \
"v_add_u32 v188, s91, v188\n" \
"ds_read_b128 v[160:163], v240 offset:2560\n" \
"v_mfma_f32_16x16x32_f16 v[40:43], v[132:135], v[148:151], v[40:43]\n" \
"v_pk_add_f16 v183, v183, v211\n" \
"v_add_u32 v189, s91, v189\n" \
"v_add_u32 v190, s91, v190\n" \
"ds_read_b128 v[144:147], v240 offset:2048\n" \
"v_mfma_f32_16x16x32_f16 v[28:31], v[128:131], v[148:151], v[28:31]\n" \
"v_pk_add_f16 v184, v184, v156\n" \
"v_add_u32 v191, s91, v191\n" \
"v_and_b32 v188, s92, v188\n" \
"ds_read_b64_tr_b16 v[122:123], v241 offset:8192\n" \
"s_add_i32 s82, s51, 0x7800000\n" \
"v_mfma_f32_16x16x32_f16 v[52:55], v[128:131], v[152:155], v[52:55]\n" \
"v_pk_add_f16 v185, v185, v157\n" \
"v_and_b32 v189, s92, v189\n" \
"v_and_b32 v190, s92, v190\n" \
"v_and_b32 v191, s92, v191\n" \
"ds_read_b128 v[148:151], v240 offset:4096\n" \
"v_mfma_f32_16x16x32_f16 v[64:67], v[132:135], v[152:155], v[64:67]\n" \
"v_pk_add_f16 v186, v186, v158\n" \
"ds_read_b64_tr_b16 v[124:125], v242 offset:0\n" \
"buffer_store_dwordx4 v[188:191], v234, s[36:39], s82 offen sc1\n" \
"v_mfma_f32_16x16x32_f16 v[88:91], v[132:135], v[208:211], v[88:91]\n" \
"v_pk_add_f16 v187, v187, v159\n" \
"ds_read_b128 v[152:155], v240 offset:6144\n" \
"buffer_load_dwordx4 v232, s[36:39], s80 offen offset:2048 sc1 lds\n" \
"v_mfma_f32_16x16x32_f16 v[76:79], v[128:131], v[208:211], v[76:79]\n" \
"s_setprio 0\n" \
"ds_read_b64_tr_b16 v[126:127], v242 offset:8192\n" \
"ds_read_b64_tr_b16 v[188:189], v238 offset:40960\n" \
"v_mfma_f32_16x16x32_f16 v[100:103], v[128:131], v[156:159], v[100:103]\n" \
"ds_read_b128 v[208:211], v240 offset:8192\n" \
"ds_read_b64_tr_b16 v[192:193], v239 offset:40960\n" \
"v_mfma_f32_16x16x32_f16 v[112:115], v[132:135], v[156:159], v[112:115]\n" \
"ds_read_b64_tr_b16 v[128:129], v241 offset:512\n" \
"v_mfma_f32_16x16x32_f16 v[8:11], v[136:139], v[168:171], v[8:11]\n" \
"ds_read_b128 v[156:159], v240 offset:0\n" \
"s_waitcnt lgkmcnt(4)\n" \
"v_mfma_f32_16x16x16_bf16 v[188:191], v[188:189], v[254:255], 0\n" \
"v_mfma_f32_16x16x32_f16 v[20:23], v[140:143], v[168:171], v[20:23]\n" \
"ds_read_b128 v[168:171], v240 offset:6656\n" \
"s_waitcnt lgkmcnt(3)\n" \
"v_mfma_f32_16x16x16_bf16 v[192:195], v[192:193], v[254:255], 0\n" \
"v_mfma_f32_16x16x32_f16 v[44:47], v[140:143], v[172:175], v[44:47]\n" \
"ds_read_b64_tr_b16 v[130:131], v241 offset:8704\n" \
"ds_read_b64_tr_b16 v[132:133], v242 offset:512\n" \
"buffer_load_dwordx4 v232, s[36:39], s80 offen offset:3072 sc1 lds\n" \
"v_mfma_f32_16x16x32_f16 v[32:35], v[136:139], v[172:175], v[32:35]\n" \
"ds_read_b128 v[172:175], v240 offset:8704\n" \
"v_mfma_f32_16x16x32_f16 v[56:59], v[136:139], v[176:179], v[56:59]\n" \
"v_cvt_pk_f16_f32 v188, v188, v189\n" \
"v_cvt_pk_f16_f32 v189, v190, v191\n" \
"ds_read_b64_tr_b16 v[134:135], v242 offset:8704\n" \
"v_mfma_f32_16x16x32_f16 v[68:71], v[140:143], v[176:179], v[68:71]\n" \
"v_cvt_pk_f16_f32 v190, v192, v193\n" \
"v_cvt_pk_f16_f32 v191, v194, v195\n" \
"v_add_u32 v188, s91, v188\n" \
"v_add_u32 v189, s91, v189\n" \
"v_add_u32 v190, s91, v190\n" \
"ds_read_b128 v[176:179], v240 offset:512\n" \
"v_mfma_f32_16x16x32_f16 v[92:95], v[140:143], v[180:183], v[92:95]\n" \
"s_waitcnt vmcnt(5)\n" \
"v_cmp_eq_u32 vcc, 0, v253\n" \
"s_cbranch_vccnz .Ls110_%=\n" \
".Lr110_%=:\n" \
"s_add_i32 m0, s45, 0xa000\n" \
"s_add_i32 s81, s47, 0x5c00000\n" \
"v_mfma_f32_16x16x32_f16 v[80:83], v[136:139], v[180:183], v[80:83]\n" \
"v_add_u32 v191, s91, v191\n" \
"v_and_b32 v188, s92, v188\n" \
"buffer_load_dwordx4 v232, s[36:39], s81 offen sc1 lds\n" \
"v_mfma_f32_16x16x32_f16 v[104:107], v[136:139], v[184:187], v[104:107]\n" \
"v_and_b32 v189, s92, v189\n" \
"v_and_b32 v190, s92, v190\n" \
"v_and_b32 v191, s92, v191\n" \
"v_mfma_f32_16x16x32_f16 v[116:119], v[140:143], v[184:187], v[116:119]\n" \
"s_setprio 1\n" \
"buffer_store_dwordx4 v[188:191], v234, s[36:39], s82 offen offset:1024 sc1\n" \
"s_waitcnt vmcnt(7) lgkmcnt(0)\n" \
"s_barrier\n" \
"v_mfma_f32_16x16x32_f16 v[0:3], v[120:123], v[144:147], v[0:3]\n" \
"v_pk_add_f16 v136, v120, v128\n" \
"v_pk_add_f16 v137, v121, v129\n" \
"v_mfma_f32_16x16x32_f16 v[12:15], v[124:127], v[144:147], v[12:15]\n" \
"v_pk_add_f16 v144, v144, v160\n" \
"v_pk_add_f16 v138, v122, v130\n" \
"v_mfma_f32_16x16x32_f16 v[36:39], v[124:127], v[148:151], v[36:39]\n" \
"v_pk_add_f16 v145, v145, v161\n" \
"v_pk_add_f16 v146, v146, v162\n" \
"v_pk_add_f16 v147, v147, v163\n" \
"v_mfma_f32_16x16x32_f16 v[24:27], v[120:123], v[148:151], v[24:27]\n" \
"v_pk_add_f16 v148, v148, v164\n" \
"v_pk_add_f16 v139, v123, v131\n" \
"s_add_i32 m0, s44, 0x0\n" \
"s_add_i32 s80, s46, 0x6000000\n" \
"v_mfma_f32_16x16x32_f16 v[48:51], v[120:123], v[152:155], v[48:51]\n" \
"v_pk_add_f16 v149, v149, v165\n" \
"v_pk_add_f16 v140, v124, v132\n" \
"buffer_load_dwordx4 v232, s[36:39], s80 offen sc1 lds\n" \
"v_mfma_f32_16x16x32_f16 v[60:63], v[124:127], v[152:155], v[60:63]\n" \
"v_pk_add_f16 v150, v150, v166\n" \
"v_pk_add_f16 v151, v151, v167\n" \
"ds_read_b128 v[184:187], v240 offset:25088\n" \
"v_mfma_f32_16x16x32_f16 v[84:87], v[124:127], v[208:211], v[84:87]\n" \
"v_pk_add_f16 v152, v152, v168\n" \
"v_pk_add_f16 v141, v125, v133\n" \
"v_mfma_f32_16x16x32_f16 v[72:75], v[120:123], v[208:211], v[72:75]\n" \
"v_pk_add_f16 v153, v153, v169\n" \
"v_pk_add_f16 v142, v126, v134\n" \
"v_mfma_f32_16x16x32_f16 v[96:99], v[120:123], v[156:159], v[96:99]\n" \
"v_pk_add_f16 v154, v154, v170\n" \
"v_pk_add_f16 v155, v155, v171\n" \
"v_mfma_f32_16x16x32_f16 v[108:111], v[124:127], v[156:159], v[108:111]\n" \
"v_pk_add_f16 v208, v208, v172\n" \
"v_pk_add_f16 v143, v127, v135\n" \
"v_mfma_f32_16x16x32_f16 v[4:7], v[128:131], v[160:163], v[4:7]\n" \
"v_pk_add_f16 v209, v209, v173\n" \
"ds_read_b64_tr_b16 v[120:121], v241 offset:16384\n" \
"buffer_load_dwordx4 v232, s[36:39], s80 offen offset:1024 sc1 lds\n" \
"v_mfma_f32_16x16x32_f16 v[16:19], v[132:135], v[160:163], v[16:19]\n" \
"v_pk_add_f16 v210, v210, v174\n" \
"ds_read_b128 v[180:183], v240 offset:23040\n" \
"v_mfma_f32_16x16x32_f16 v[40:43], v[132:135], v[164:167], v[40:43]\n" \
"v_pk_add_f16 v211, v211, v175\n" \
"ds_read_b128 v[160:163], v240 offset:22528\n" \
"v_mfma_f32_16x16x32_f16 v[28:31], v[128:131], v[164:167], v[28:31]\n" \
"v_pk_add_f16 v156, v156, v176\n" \
"ds_read_b64_tr_b16 v[122:123], v241 offset:24576\n" \
"v_mfma_f32_16x16x32_f16 v[52:55], v[128:131], v[168:171], v[52:55]\n" \
"v_pk_add_f16 v157, v157, v177\n" \
"ds_read_b128 v[164:167], v240 offset:24576\n" \
"v_mfma_f32_16x16x32_f16 v[64:67], v[132:135], v[168:171], v[64:67]\n" \
"v_pk_add_f16 v158, v158, v178\n" \
"ds_read_b64_tr_b16 v[124:125], v242 offset:16384\n" \
"v_mfma_f32_16x16x32_f16 v[88:91], v[132:135], v[172:175], v[88:91]\n" \
"v_pk_add_f16 v159, v159, v179\n" \
"ds_read_b128 v[168:171], v240 offset:26624\n" \
"buffer_load_dwordx4 v232, s[36:39], s80 offen offset:2048 sc1 lds\n" \
"v_mfma_f32_16x16x32_f16 v[76:79], v[128:131], v[172:175], v[76:79]\n" \
"s_setprio 0\n" \
"ds_read_b64_tr_b16 v[126:127], v242 offset:24576\n" \
"v_mfma_f32_16x16x32_f16 v[100:103], v[128:131], v[176:179], v[100:103]\n" \
"ds_read_b128 v[172:175], v240 offset:28672\n" \
"v_mfma_f32_16x16x32_f16 v[112:115], v[132:135], v[176:179], v[112:115]\n" \
"ds_read_b64_tr_b16 v[128:129], v241 offset:16896\n" \
"v_mfma_f32_16x16x32_f16 v[8:11], v[136:139], v[144:147], v[8:11]\n" \
"ds_read_b128 v[176:179], v240 offset:20480\n" \
"v_mfma_f32_16x16x32_f16 v[20:23], v[140:143], v[144:147], v[20:23]\n" \
"ds_read_b128 v[144:147], v240 offset:27136\n" \
"v_mfma_f32_16x16x32_f16 v[44:47], v[140:143], v[148:151], v[44:47]\n" \
"ds_read_b64_tr_b16 v[130:131], v241 offset:25088\n" \
"ds_read_b64_tr_b16 v[132:133], v242 offset:16896\n" \
"buffer_load_dwordx4 v232, s[36:39], s80 offen offset:3072 sc1 lds\n" \
"v_mfma_f32_16x16x32_f16 v[32:35], v[136:139], v[148:151], v[32:35]\n" \
"ds_read_b128 v[148:151], v240 offset:29184\n" \
"v_mfma_f32_16x16x32_f16 v[56:59], v[136:139], v[152:155], v[56:59]\n" \
"ds_read_b64_tr_b16 v[134:135], v242 offset:25088\n" \
"v_mfma_f32_16x16x32_f16 v[68:71], v[140:143], v[152:155], v[68:71]\n" \
"ds_read_b128 v[152:155], v240 offset:20992\n" \
"v_mfma_f32_16x16x32_f16 v[92:95], v[140:143], v[208:211], v[92:95]\n" \
"s_add_i32 m0, s45, 0x0\n" \
"s_add_i32 s81, s47, 0x6000000\n" \
"v_mfma_f32_16x16x32_f16 v[80:83], v[136:139], v[208:211], v[80:83]\n" \
"buffer_load_dwordx4 v232, s[36:39], s81 offen sc1 lds\n" \
"v_mfma_f32_16x16x32_f16 v[104:107], v[136:139], v[156:159], v[104:107]\n" \
"v_mfma_f32_16x16x32_f16 v[116:119], v[140:143], v[156:159], v[116:119]\n" \
"s_setprio 1\n" \
"s_waitcnt vmcnt(5) lgkmcnt(0)\n" \
"s_barrier\n" \
"v_mfma_f32_16x16x32_f16 v[0:3], v[120:123], v[160:163], v[0:3]\n" \
"v_pk_add_f16 v136, v120, v128\n" \
"v_pk_add_f16 v137, v121, v129\n" \
"s_add_i32 m0, s54, 0x1000\n" \
"s_mov_b32 s83, 0x300\n" \
"v_mfma_f32_16x16x32_f16 v[12:15], v[124:127], v[160:163], v[12:15]\n" \
"v_pk_add_f16 v160, v160, v180\n" \
"v_pk_add_f16 v138, v122, v130\n" \
"buffer_load_dwordx4 v233, s[36:39], s83 offen sc1 lds\n" \
"v_mfma_f32_16x16x32_f16 v[36:39], v[124:127], v[164:167], v[36:39]\n" \
"v_pk_add_f16 v161, v161, v181\n" \
"v_pk_add_f16 v162, v162, v182\n" \
"v_pk_add_f16 v163, v163, v183\n" \
"v_mfma_f32_16x16x32_f16 v[24:27], v[120:123], v[164:167], v[24:27]\n" \
"v_pk_add_f16 v164, v164, v184\n" \
"v_pk_add_f16 v139, v123, v131\n" \
"s_add_i32 m0, s44, 0x5000\n" \
"s_add_i32 s80, s46, 0x6400000\n" \
"v_mfma_f32_16x16x32_f16 v[48:51], v[120:123], v[168:171], v[48:51]\n" \
"v_pk_add_f16 v165, v165, v185\n" \
"v_pk_add_f16 v140, v124, v132\n" \
"buffer_load_dwordx4 v232, s[36:39], s80 offen sc1 lds\n" \
"v_mfma_f32_16x16x32_f16 v[60:63], v[124:127], v[168:171], v[60:63]\n" \
"v_pk_add_f16 v166, v166, v186\n" \
"v_pk_add_f16 v167, v167, v187\n" \
"ds_read_b128 v[156:159], v240 offset:45568\n" \
"v_mfma_f32_16x16x32_f16 v[84:87], v[124:127], v[172:175], v[84:87]\n" \
"v_pk_add_f16 v168, v168, v144\n" \
"v_pk_add_f16 v141, v125, v133\n" \
"v_mfma_f32_16x16x32_f16 v[72:75], v[120:123], v[172:175], v[72:75]\n" \
"v_pk_add_f16 v169, v169, v145\n" \
"v_pk_add_f16 v142, v126, v134\n" \
"v_mfma_f32_16x16x32_f16 v[96:99], v[120:123], v[176:179], v[96:99]\n" \
"v_pk_add_f16 v170, v170, v146\n" \
"v_pk_add_f16 v171, v171, v147\n" \
"v_mfma_f32_16x16x32_f16 v[108:111], v[124:127], v[176:179], v[108:111]\n" \
"v_pk_add_f16 v172, v172, v148\n" \
"v_pk_add_f16 v143, v127, v135\n" \
"v_mfma_f32_16x16x32_f16 v[4:7], v[128:131], v[180:183], v[4:7]\n" \
"v_pk_add_f16 v173, v173, v149\n" \
"ds_read_b64_tr_b16 v[120:121], v241 offset:32768\n" \
"buffer_load_dwordx4 v232, s[36:39], s80 offen offset:1024 sc1 lds\n" \
"v_mfma_f32_16x16x32_f16 v[16:19], v[132:135], v[180:183], v[16:19]\n" \
"v_pk_add_f16 v174, v174, v150\n" \
"ds_read_b128 v[208:211], v240 offset:43520\n" \
"v_mfma_f32_16x16x32_f16 v[40:43], v[132:135], v[184:187], v[40:43]\n" \
"v_pk_add_f16 v175, v175, v151\n" \
"ds_read_b128 v[180:183], v240 offset:43008\n" \
"v_mfma_f32_16x16x32_f16 v[28:31], v[128:131], v[184:187], v[28:31]\n" \
"v_pk_add_f16 v176, v176, v152\n" \
"ds_read_b64_tr_b16 v[122:123], v241 offset:40960\n" \
"v_mfma_f32_16x16x32_f16 v[52:55], v[128:131], v[144:147], v[52:55]\n" \
"v_pk_add_f16 v177, v177, v153\n" \
"ds_read_b128 v[184:187], v240 offset:45056\n" \
"v_mfma_f32_16x16x32_f16 v[64:67], v[132:135], v[144:147], v[64:67]\n" \
"v_pk_add_f16 v178, v178, v154\n" \
"ds_read_b64_tr_b16 v[124:125], v242 offset:32768\n" \
"v_mfma_f32_16x16x32_f16 v[88:91], v[132:135], v[148:151], v[88:91]\n" \
"v_pk_add_f16 v179, v179, v155\n" \
"ds_read_b128 v[144:147], v240 offset:47104\n" \
"buffer_load_dwordx4 v232, s[36:39], s80 offen offset:2048 sc1 lds\n" \
"v_mfma_f32_16x16x32_f16 v[76:79], v[128:131], v[148:151], v[76:79]\n" \
"s_setprio 0\n" \
"ds_read_b64_tr_b16 v[126:127], v242 offset:40960\n" \
"v_mfma_f32_16x16x32_f16 v[100:103], v[128:131], v[152:155], v[100:103]\n" \
"ds_read_b128 v[148:151], v240 offset:49152\n" \
"v_mfma_f32_16x16x32_f16 v[112:115], v[132:135], v[152:155], v[112:115]\n" \
"ds_read_b64_tr_b16 v[128:129], v241 offset:33280\n" \
"v_mfma_f32_16x16x32_f16 v[8:11], v[136:139], v[160:163], v[8:11]\n" \
"ds_read_b128 v[152:155], v240 offset:40960\n" \
"v_mfma_f32_16x16x32_f16 v[20:23], v[140:143], v[160:163], v[20:23]\n" \
"ds_read_b128 v[160:163], v240 offset:47616\n" \
"v_mfma_f32_16x16x32_f16 v[44:47], v[140:143], v[164:167], v[44:47]\n" \
"ds_read_b64_tr_b16 v[130:131], v241 offset:41472\n" \
"ds_read_b64_tr_b16 v[132:133], v242 offset:33280\n" \
"buffer_load_dwordx4 v232, s[36:39], s80 offen offset:3072 sc1 lds\n" \
"v_mfma_f32_16x16x32_f16 v[32:35], v[136:139], v[164:167], v[32:35]\n" \
"ds_read_b128 v[164:167], v240 offset:49664\n" \
"v_mfma_f32_16x16x32_f16 v[56:59], v[136:139], v[168:171], v[56:59]\n" \
"ds_read_b64_tr_b16 v[134:135], v242 offset:41472\n" \
"v_mfma_f32_16x16x32_f16 v[68:71], v[140:143], v[168:171], v[68:71]\n" \
"ds_read_b128 v[168:171], v240 offset:41472\n" \
"v_mfma_f32_16x16x32_f16 v[92:95], v[140:143], v[172:175], v[92:95]\n" \
"s_add_i32 m0, s45, 0x5000\n" \
"s_add_i32 s81, s47, 0x6400000\n" \
"v_mfma_f32_16x16x32_f16 v[80:83], v[136:139], v[172:175], v[80:83]\n" \
"buffer_load_dwordx4 v232, s[36:39], s81 offen sc1 lds\n" \
"v_mfma_f32_16x16x32_f16 v[104:107], v[136:139], v[176:179], v[104:107]\n" \
"v_mfma_f32_16x16x32_f16 v[116:119], v[140:143], v[176:179], v[116:119]\n" \
"s_setprio 1\n" \
"s_waitcnt vmcnt(6) lgkmcnt(0)\n" \
"s_barrier\n" \
"v_mfma_f32_16x16x32_f16 v[0:3], v[120:123], v[180:183], v[0:3]\n" \
"v_pk_add_f16 v136, v120, v128\n" \
"v_pk_add_f16 v137, v121, v129\n" \
"v_mfma_f32_16x16x32_f16 v[12:15], v[124:127], v[180:183], v[12:15]\n" \
"v_pk_add_f16 v180, v180, v208\n" \
"v_pk_add_f16 v138, v122, v130\n" \
"v_mfma_f32_16x16x32_f16 v[36:39], v[124:127], v[184:187], v[36:39]\n" \
"v_pk_add_f16 v181, v181, v209\n" \
"v_pk_add_f16 v182, v182, v210\n" \
"v_pk_add_f16 v183, v183, v211\n" \
"v_mfma_f32_16x16x32_f16 v[24:27], v[120:123], v[184:187], v[24:27]\n" \
"v_pk_add_f16 v184, v184, v156\n" \
"v_pk_add_f16 v139, v123, v131\n" \
"s_add_i32 m0, s44, 0xa000\n" \
"s_add_i32 s80, s46, 0x6800000\n" \
"v_mfma_f32_16x16x32_f16 v[48:51], v[120:123], v[144:147], v[48:51]\n" \
"v_pk_add_f16 v185, v185, v157\n" \
"v_pk_add_f16 v140, v124, v132\n" \
"buffer_load_dwordx4 v232, s[36:39], s80 offen sc1 lds\n" \
"v_mfma_f32_16x16x32_f16 v[60:63], v[124:127], v[144:147], v[60:63]\n" \
"v_pk_add_f16 v186, v186, v158\n" \
"v_pk_add_f16 v187, v187, v159\n" \
"ds_read_b128 v[176:179], v240 offset:4608\n" \
"v_mfma_f32_16x16x32_f16 v[84:87], v[124:127], v[148:151], v[84:87]\n" \
"v_pk_add_f16 v144, v144, v160\n" \
"v_pk_add_f16 v141, v125, v133\n" \
"v_mfma_f32_16x16x32_f16 v[72:75], v[120:123], v[148:151], v[72:75]\n" \
"v_pk_add_f16 v145, v145, v161\n" \
"v_pk_add_f16 v142, v126, v134\n" \
"v_mfma_f32_16x16x32_f16 v[96:99], v[120:123], v[152:155], v[96:99]\n" \
"v_pk_add_f16 v146, v146, v162\n" \
"v_pk_add_f16 v147, v147, v163\n" \
"v_mfma_f32_16x16x32_f16 v[108:111], v[124:127], v[152:155], v[108:111]\n" \
"v_pk_add_f16 v148, v148, v164\n" \
"v_pk_add_f16 v143, v127, v135\n" \
"v_mfma_f32_16x16x32_f16 v[4:7], v[128:131], v[208:211], v[4:7]\n" \
"v_pk_add_f16 v149, v149, v165\n" \
"ds_read_b64_tr_b16 v[120:121], v241 offset:0\n" \
"buffer_load_dwordx4 v232, s[36:39], s80 offen offset:1024 sc1 lds\n" \
"v_mfma_f32_16x16x32_f16 v[16:19], v[132:135], v[208:211], v[16:19]\n" \
"v_pk_add_f16 v150, v150, v166\n" \
"ds_read_b128 v[172:175], v240 offset:2560\n" \
"v_mfma_f32_16x16x32_f16 v[40:43], v[132:135], v[156:159], v[40:43]\n" \
"v_pk_add_f16 v151, v151, v167\n" \
"ds_read_b128 v[208:211], v240 offset:2048\n" \
"v_mfma_f32_16x16x32_f16 v[28:31], v[128:131], v[156:159], v[28:31]\n" \
"v_pk_add_f16 v152, v152, v168\n" \
"ds_read_b64_tr_b16 v[122:123], v241 offset:8192\n" \
"v_mfma_f32_16x16x32_f16 v[52:55], v[128:131], v[160:163], v[52:55]\n" \
"v_pk_add_f16 v153, v153, v169\n" \
"ds_read_b128 v[156:159], v240 offset:4096\n" \
"v_mfma_f32_16x16x32_f16 v[64:67], v[132:135], v[160:163], v[64:67]\n" \
"v_pk_add_f16 v154, v154, v170\n" \
"ds_read_b64_tr_b16 v[124:125], v242 offset:0\n" \
"v_mfma_f32_16x16x32_f16 v[88:91], v[132:135], v[164:167], v[88:91]\n" \
"v_pk_add_f16 v155, v155, v171\n" \
"ds_read_b128 v[160:163], v240 offset:6144\n" \
"buffer_load_dwordx4 v232, s[36:39], s80 offen offset:2048 sc1 lds\n" \
"v_mfma_f32_16x16x32_f16 v[76:79], v[128:131], v[164:167], v[76:79]\n" \
"s_setprio 0\n" \
"ds_read_b64_tr_b16 v[126:127], v242 offset:8192\n" \
"v_mfma_f32_16x16x32_f16 v[100:103], v[128:131], v[168:171], v[100:103]\n" \
"ds_read_b128 v[164:167], v240 offset:8192\n" \
"v_mfma_f32_16x16x32_f16 v[112:115], v[132:135], v[168:171], v[112:115]\n" \
"ds_read_b64_tr_b16 v[128:129], v241 offset:512\n" \
"v_mfma_f32_16x16x32_f16 v[8:11], v[136:139], v[180:183], v[8:11]\n" \
"ds_read_b128 v[168:171], v240 offset:0\n" \
"v_mfma_f32_16x16x32_f16 v[20:23], v[140:143], v[180:183], v[20:23]\n" \
"ds_read_b128 v[180:183], v240 offset:6656\n" \
"v_mfma_f32_16x16x32_f16 v[44:47], v[140:143], v[184:187], v[44:47]\n" \
"ds_read_b64_tr_b16 v[130:131], v241 offset:8704\n" \
"ds_read_b64_tr_b16 v[132:133], v242 offset:512\n" \
"buffer_load_dwordx4 v232, s[36:39], s80 offen offset:3072 sc1 lds\n" \
"v_mfma_f32_16x16x32_f16 v[32:35], v[136:139], v[184:187], v[32:35]\n" \
"ds_read_b128 v[184:187], v240 offset:8704\n" \
"v_mfma_f32_16x16x32_f16 v[56:59], v[136:139], v[144:147], v[56:59]\n" \
"ds_read_b64_tr_b16 v[134:135], v242 offset:8704\n" \
"v_mfma_f32_16x16x32_f16 v[68:71], v[140:143], v[144:147], v[68:71]\n" \
"ds_read_b128 v[144:147], v240 offset:512\n" \
"v_mfma_f32_16x16x32_f16 v[92:95], v[140:143], v[148:151], v[92:95]\n" \
"s_add_i32 m0, s45, 0xa000\n" \
"s_add_i32 s81, s47, 0x6800000\n" \
"v_mfma_f32_16x16x32_f16 v[80:83], v[136:139], v[148:151], v[80:83]\n" \
"buffer_load_dwordx4 v232, s[36:39], s81 offen sc1 lds\n" \
"v_mfma_f32_16x16x32_f16 v[104:107], v[136:139], v[152:155], v[104:107]\n" \
"v_mfma_f32_16x16x32_f16 v[116:119], v[140:143], v[152:155], v[116:119]\n" \
"s_setprio 1\n" \
"s_waitcnt vmcnt(5) lgkmcnt(0)\n" \
"s_barrier\n" \
"v_mfma_f32_16x16x32_f16 v[0:3], v[120:123], v[208:211], v[0:3]\n" \
"v_pk_add_f16 v136, v120, v128\n" \
"v_pk_add_f16 v137, v121, v129\n" \
"v_mfma_f32_16x16x32_f16 v[12:15], v[124:127], v[208:211], v[12:15]\n" \
"v_pk_add_f16 v208, v208, v172\n" \
"v_pk_add_f16 v138, v122, v130\n" \
"v_mfma_f32_16x16x32_f16 v[36:39], v[124:127], v[156:159], v[36:39]\n" \
"v_pk_add_f16 v209, v209, v173\n" \
"v_pk_add_f16 v210, v210, v174\n" \
"v_pk_add_f16 v211, v211, v175\n" \
"v_mfma_f32_16x16x32_f16 v[24:27], v[120:123], v[156:159], v[24:27]\n" \
"v_pk_add_f16 v156, v156, v176\n" \
"v_pk_add_f16 v139, v123, v131\n" \
"s_add_i32 m0, s44, 0x0\n" \
"s_add_i32 s80, s46, 0x6c00000\n" \
"v_mfma_f32_16x16x32_f16 v[48:51], v[120:123], v[160:163], v[48:51]\n" \
"v_pk_add_f16 v157, v157, v177\n" \
"v_pk_add_f16 v140, v124, v132\n" \
"buffer_load_dwordx4 v232, s[36:39], s80 offen sc1 lds\n" \
"v_mfma_f32_16x16x32_f16 v[60:63], v[124:127], v[160:163], v[60:63]\n" \
"v_pk_add_f16 v158, v158, v178\n" \
"v_pk_add_f16 v159, v159, v179\n" \
"ds_read_b128 v[152:155], v240 offset:25088\n" \
"v_mfma_f32_16x16x32_f16 v[84:87], v[124:127], v[164:167], v[84:87]\n" \
"v_pk_add_f16 v160, v160, v180\n" \
"v_pk_add_f16 v141, v125, v133\n" \
"v_mfma_f32_16x16x32_f16 v[72:75], v[120:123], v[164:167], v[72:75]\n" \
"v_pk_add_f16 v161, v161, v181\n" \
"v_pk_add_f16 v142, v126, v134\n" \
"v_mfma_f32_16x16x32_f16 v[96:99], v[120:123], v[168:171], v[96:99]\n" \
"v_pk_add_f16 v162, v162, v182\n" \
"v_pk_add_f16 v163, v163, v183\n" \
"v_mfma_f32_16x16x32_f16 v[108:111], v[124:127], v[168:171], v[108:111]\n" \
"v_pk_add_f16 v164, v164, v184\n" \
"v_pk_add_f16 v143, v127, v135\n" \
"v_mfma_f32_16x16x32_f16 v[4:7], v[128:131], v[172:175], v[4:7]\n" \
"v_pk_add_f16 v165, v165, v185\n" \
"ds_read_b64_tr_b16 v[120:121], v241 offset:16384\n" \
"buffer_load_dwordx4 v232, s[36:39], s80 offen offset:1024 sc1 lds\n" \
"v_mfma_f32_16x16x32_f16 v[16:19], v[132:135], v[172:175], v[16:19]\n" \
"v_pk_add_f16 v166, v166, v186\n" \
"ds_read_b128 v[148:151], v240 offset:23040\n" \
"v_mfma_f32_16x16x32_f16 v[40:43], v[132:135], v[176:179], v[40:43]\n" \
"v_pk_add_f16 v167, v167, v187\n" \
"ds_read_b128 v[172:175], v240 offset:22528\n" \
"v_mfma_f32_16x16x32_f16 v[28:31], v[128:131], v[176:179], v[28:31]\n" \
"v_pk_add_f16 v168, v168, v144\n" \
"ds_read_b64_tr_b16 v[122:123], v241 offset:24576\n" \
"v_mfma_f32_16x16x32_f16 v[52:55], v[128:131], v[180:183], v[52:55]\n" \
"v_pk_add_f16 v169, v169, v145\n" \
"ds_read_b128 v[176:179], v240 offset:24576\n" \
"v_mfma_f32_16x16x32_f16 v[64:67], v[132:135], v[180:183], v[64:67]\n" \
"v_pk_add_f16 v170, v170, v146\n" \
"ds_read_b64_tr_b16 v[124:125], v242 offset:16384\n" \
"v_mfma_f32_16x16x32_f16 v[88:91], v[132:135], v[184:187], v[88:91]\n" \
"v_pk_add_f16 v171, v171, v147\n" \
"ds_read_b128 v[180:183], v240 offset:26624\n" \
"buffer_load_dwordx4 v232, s[36:39], s80 offen offset:2048 sc1 lds\n" \
"v_mfma_f32_16x16x32_f16 v[76:79], v[128:131], v[184:187], v[76:79]\n" \
"s_setprio 0\n" \
"ds_read_b64_tr_b16 v[126:127], v242 offset:24576\n" \
"v_mfma_f32_16x16x32_f16 v[100:103], v[128:131], v[144:147], v[100:103]\n" \
"ds_read_b128 v[184:187], v240 offset:28672\n" \
"v_mfma_f32_16x16x32_f16 v[112:115], v[132:135], v[144:147], v[112:115]\n" \
"ds_read_b64_tr_b16 v[128:129], v241 offset:16896\n" \
"v_mfma_f32_16x16x32_f16 v[8:11], v[136:139], v[208:211], v[8:11]\n" \
"ds_read_b128 v[144:147], v240 offset:20480\n" \
"v_mfma_f32_16x16x32_f16 v[20:23], v[140:143], v[208:211], v[20:23]\n" \
"ds_read_b128 v[208:211], v240 offset:27136\n" \
"v_mfma_f32_16x16x32_f16 v[44:47], v[140:143], v[156:159], v[44:47]\n" \
"ds_read_b64_tr_b16 v[130:131], v241 offset:25088\n" \
"ds_read_b64_tr_b16 v[132:133], v242 offset:16896\n" \
"buffer_load_dwordx4 v232, s[36:39], s80 offen offset:3072 sc1 lds\n" \
"v_mfma_f32_16x16x32_f16 v[32:35], v[136:139], v[156:159], v[32:35]\n" \
"ds_read_b128 v[156:159], v240 offset:29184\n" \
"v_mfma_f32_16x16x32_f16 v[56:59], v[136:139], v[160:163], v[56:59]\n" \
"ds_read_b64_tr_b16 v[134:135], v242 offset:25088\n" \
"v_mfma_f32_16x16x32_f16 v[68:71], v[140:143], v[160:163], v[68:71]\n" \
"ds_read_b128 v[160:163], v240 offset:20992\n" \
"v_mfma_f32_16x16x32_f16 v[92:95], v[140:143], v[164:167], v[92:95]\n" \
"s_add_i32 m0, s45, 0x0\n" \
"s_add_i32 s81, s47, 0x6c00000\n" \
"v_mfma_f32_16x16x32_f16 v[80:83], v[136:139], v[164:167], v[80:83]\n" \
"buffer_load_dwordx4 v232, s[36:39], s81 offen sc1 lds\n" \
"v_mfma_f32_16x16x32_f16 v[104:107], v[136:139], v[168:171], v[104:107]\n" \
"v_mfma_f32_16x16x32_f16 v[116:119], v[140:143], v[168:171], v[116:119]\n" \
"s_setprio 1\n" \
"s_waitcnt vmcnt(5) lgkmcnt(0)\n" \
"s_barrier\n" \
"v_mfma_f32_16x16x32_f16 v[0:3], v[120:123], v[172:175], v[0:3]\n" \
"v_pk_add_f16 v136, v120, v128\n" \
"v_pk_add_f16 v137, v121, v129\n" \
"v_mfma_f32_16x16x32_f16 v[12:15], v[124:127], v[172:175], v[12:15]\n" \
"v_pk_add_f16 v172, v172, v148\n" \
"v_pk_add_f16 v138, v122, v130\n" \
"v_mfma_f32_16x16x32_f16 v[36:39], v[124:127], v[176:179], v[36:39]\n" \
"v_pk_add_f16 v173, v173, v149\n" \
"v_pk_add_f16 v174, v174, v150\n" \
"v_pk_add_f16 v175, v175, v151\n" \
"s_add_i32 m0, s44, 0x5000\n" \
"s_add_i32 s80, s46, 0x7000000\n" \
"v_mfma_f32_16x16x32_f16 v[24:27], v[120:123], v[176:179], v[24:27]\n" \
"v_pk_add_f16 v176, v176, v152\n" \
"v_pk_add_f16 v139, v123, v131\n" \
"buffer_load_dwordx4 v232, s[36:39], s80 offen sc1 lds\n" \
"v_mfma_f32_16x16x32_f16 v[48:51], v[120:123], v[180:183], v[48:51]\n" \
"v_pk_add_f16 v177, v177, v153\n" \
"v_pk_add_f16 v140, v124, v132\n" \
"v_mfma_f32_16x16x32_f16 v[60:63], v[124:127], v[180:183], v[60:63]\n" \
"v_pk_add_f16 v178, v178, v154\n" \
"v_pk_add_f16 v179, v179, v155\n" \
"ds_read_b128 v[168:171], v240 offset:45568\n" \
"v_mfma_f32_16x16x32_f16 v[84:87], v[124:127], v[184:187], v[84:87]\n" \
"v_pk_add_f16 v180, v180, v208\n" \
"v_pk_add_f16 v141, v125, v133\n" \
"buffer_load_dwordx4 v232, s[36:39], s80 offen offset:1024 sc1 lds\n" \
"v_mfma_f32_16x16x32_f16 v[72:75], v[120:123], v[184:187], v[72:75]\n" \
"v_pk_add_f16 v181, v181, v209\n" \
"v_pk_add_f16 v142, v126, v134\n" \
"v_mfma_f32_16x16x32_f16 v[96:99], v[120:123], v[144:147], v[96:99]\n" \
"v_pk_add_f16 v182, v182, v210\n" \
"v_pk_add_f16 v183, v183, v211\n" \
"v_mfma_f32_16x16x32_f16 v[108:111], v[124:127], v[144:147], v[108:111]\n" \
"v_pk_add_f16 v184, v184, v156\n" \
"v_pk_add_f16 v143, v127, v135\n" \
"buffer_load_dwordx4 v232, s[36:39], s80 offen offset:2048 sc1 lds\n" \
"v_mfma_f32_16x16x32_f16 v[4:7], v[128:131], v[148:151], v[4:7]\n" \
"v_pk_add_f16 v185, v185, v157\n" \
"ds_read_b64_tr_b16 v[120:121], v241 offset:32768\n" \
"v_mfma_f32_16x16x32_f16 v[16:19], v[132:135], v[148:151], v[16:19]\n" \
"v_pk_add_f16 v186, v186, v158\n" \
"ds_read_b128 v[164:167], v240 offset:43520\n" \
"v_mfma_f32_16x16x32_f16 v[40:43], v[132:135], v[152:155], v[40:43]\n" \
"v_pk_add_f16 v187, v187, v159\n" \
"ds_read_b128 v[148:151], v240 offset:43008\n" \
"buffer_load_dwordx4 v232, s[36:39], s80 offen offset:3072 sc1 lds\n" \
"v_mfma_f32_16x16x32_f16 v[28:31], v[128:131], v[152:155], v[28:31]\n" \
"v_pk_add_f16 v144, v144, v160\n" \
"ds_read_b64_tr_b16 v[122:123], v241 offset:40960\n" \
"v_mfma_f32_16x16x32_f16 v[52:55], v[128:131], v[208:211], v[52:55]\n" \
"v_pk_add_f16 v145, v145, v161\n" \
"ds_read_b128 v[152:155], v240 offset:45056\n" \
"s_add_i32 m0, s45, 0x5000\n" \
"s_add_i32 s81, s47, 0x7000000\n" \
"v_mfma_f32_16x16x32_f16 v[64:67], v[132:135], v[208:211], v[64:67]\n" \
"v_pk_add_f16 v146, v146, v162\n" \
"ds_read_b64_tr_b16 v[124:125], v242 offset:32768\n" \
"buffer_load_dwordx4 v232, s[36:39], s81 offen sc1 lds\n" \
"v_mfma_f32_16x16x32_f16 v[88:91], v[132:135], v[156:159], v[88:91]\n" \
"v_pk_add_f16 v147, v147, v163\n" \
"ds_read_b128 v[208:211], v240 offset:47104\n" \
"v_mfma_f32_16x16x32_f16 v[76:79], v[128:131], v[156:159], v[76:79]\n" \
"s_setprio 0\n" \
"ds_read_b64_tr_b16 v[126:127], v242 offset:40960\n" \
"s_add_i32 m0, s52, 0x0\n" \
"s_add_i32 s80, s55, 0x0\n" \
"v_mfma_f32_16x16x32_f16 v[100:103], v[128:131], v[160:163], v[100:103]\n" \
"ds_read_b128 v[156:159], v240 offset:49152\n" \
"buffer_load_dwordx4 v232, s[36:39], s80 offen sc1 lds\n" \
"v_mfma_f32_16x16x32_f16 v[112:115], v[132:135], v[160:163], v[112:115]\n" \
"ds_read_b64_tr_b16 v[128:129], v241 offset:33280\n" \
"v_mfma_f32_16x16x32_f16 v[8:11], v[136:139], v[172:175], v[8:11]\n" \
"ds_read_b128 v[160:163], v240 offset:40960\n" \
"v_mfma_f32_16x16x32_f16 v[20:23], v[140:143], v[172:175], v[20:23]\n" \
"ds_read_b128 v[172:175], v240 offset:47616\n" \
"buffer_load_dwordx4 v232, s[36:39], s80 offen offset:1024 sc1 lds\n" \
"v_mfma_f32_16x16x32_f16 v[44:47], v[140:143], v[176:179], v[44:47]\n" \
"ds_read_b64_tr_b16 v[130:131], v241 offset:41472\n" \
"ds_read_b64_tr_b16 v[132:133], v242 offset:33280\n" \
"v_mfma_f32_16x16x32_f16 v[32:35], v[136:139], v[176:179], v[32:35]\n" \
"ds_read_b128 v[176:179], v240 offset:49664\n" \
"v_mfma_f32_16x16x32_f16 v[56:59], v[136:139], v[180:183], v[56:59]\n" \
"ds_read_b64_tr_b16 v[134:135], v242 offset:41472\n" \
"buffer_load_dwordx4 v232, s[36:39], s80 offen offset:2048 sc1 lds\n" \
"v_mfma_f32_16x16x32_f16 v[68:71], v[140:143], v[180:183], v[68:71]\n" \
"ds_read_b128 v[180:183], v240 offset:41472\n" \
"v_mfma_f32_16x16x32_f16 v[92:95], v[140:143], v[184:187], v[92:95]\n" \
"v_mfma_f32_16x16x32_f16 v[80:83], v[136:139], v[184:187], v[80:83]\n" \
"buffer_load_dwordx4 v232, s[36:39], s80 offen offset:3072 sc1 lds\n" \
"v_mfma_f32_16x16x32_f16 v[104:107], v[136:139], v[144:147], v[104:107]\n" \
"v_mfma_f32_16x16x32_f16 v[116:119], v[140:143], v[144:147], v[116:119]\n" \
"s_setprio 1\n" \
"s_waitcnt vmcnt(9) lgkmcnt(0)\n" \
"s_barrier\n" \
"v_mfma_f32_16x16x32_f16 v[0:3], v[120:123], v[148:151], v[0:3]\n" \
"v_pk_add_f16 v136, v120, v128\n" \
"v_pk_add_f16 v137, v121, v129\n" \
"v_mfma_f32_16x16x32_f16 v[12:15], v[124:127], v[148:151], v[12:15]\n" \
"v_pk_add_f16 v148, v148, v164\n" \
"v_pk_add_f16 v138, v122, v130\n" \
"v_mfma_f32_16x16x32_f16 v[36:39], v[124:127], v[152:155], v[36:39]\n" \
"v_pk_add_f16 v149, v149, v165\n" \
"v_pk_add_f16 v150, v150, v166\n" \
"v_pk_add_f16 v151, v151, v167\n" \
"v_mfma_f32_16x16x32_f16 v[24:27], v[120:123], v[152:155], v[24:27]\n" \
"v_pk_add_f16 v152, v152, v168\n" \
"v_pk_add_f16 v139, v123, v131\n" \
"s_add_i32 m0, s44, 0xa000\n" \
"s_add_i32 s80, s46, 0x7400000\n" \
"v_mfma_f32_16x16x32_f16 v[48:51], v[120:123], v[208:211], v[48:51]\n" \
"v_pk_add_f16 v153, v153, v169\n" \
"v_pk_add_f16 v140, v124, v132\n" \
"buffer_load_dwordx4 v232, s[36:39], s80 offen sc1 lds\n" \
"v_mfma_f32_16x16x32_f16 v[60:63], v[124:127], v[208:211], v[60:63]\n" \
"v_pk_add_f16 v154, v154, v170\n" \
"v_pk_add_f16 v155, v155, v171\n" \
"ds_read_b128 v[144:147], v240 offset:4608\n" \
"v_mfma_f32_16x16x32_f16 v[84:87], v[124:127], v[156:159], v[84:87]\n" \
"v_pk_add_f16 v208, v208, v172\n" \
"v_pk_add_f16 v141, v125, v133\n" \
"v_mfma_f32_16x16x32_f16 v[72:75], v[120:123], v[156:159], v[72:75]\n" \
"v_pk_add_f16 v209, v209, v173\n" \
"v_pk_add_f16 v142, v126, v134\n" \
"v_mfma_f32_16x16x32_f16 v[96:99], v[120:123], v[160:163], v[96:99]\n" \
"v_pk_add_f16 v210, v210, v174\n" \
"v_pk_add_f16 v211, v211, v175\n" \
"v_mfma_f32_16x16x32_f16 v[108:111], v[124:127], v[160:163], v[108:111]\n" \
"v_pk_add_f16 v156, v156, v176\n" \
"v_pk_add_f16 v143, v127, v135\n" \
"v_mfma_f32_16x16x32_f16 v[4:7], v[128:131], v[164:167], v[4:7]\n" \
"v_pk_add_f16 v157, v157, v177\n" \
"ds_read_b64_tr_b16 v[120:121], v241 offset:0\n" \
"buffer_load_dwordx4 v232, s[36:39], s80 offen offset:1024 sc1 lds\n" \
"v_mfma_f32_16x16x32_f16 v[16:19], v[132:135], v[164:167], v[16:19]\n" \
"v_pk_add_f16 v158, v158, v178\n" \
"ds_read_b128 v[184:187], v240 offset:2560\n" \
"v_mfma_f32_16x16x32_f16 v[40:43], v[132:135], v[168:171], v[40:43]\n" \
"v_pk_add_f16 v159, v159, v179\n" \
"ds_read_b128 v[164:167], v240 offset:2048\n" \
"v_mfma_f32_16x16x32_f16 v[28:31], v[128:131], v[168:171], v[28:31]\n" \
"v_pk_add_f16 v160, v160, v180\n" \
"ds_read_b64_tr_b16 v[122:123], v241 offset:8192\n" \
"v_mfma_f32_16x16x32_f16 v[52:55], v[128:131], v[172:175], v[52:55]\n" \
"v_pk_add_f16 v161, v161, v181\n" \
"ds_read_b128 v[168:171], v240 offset:4096\n" \
"v_mfma_f32_16x16x32_f16 v[64:67], v[132:135], v[172:175], v[64:67]\n" \
"v_pk_add_f16 v162, v162, v182\n" \
"ds_read_b64_tr_b16 v[124:125], v242 offset:0\n" \
"v_mfma_f32_16x16x32_f16 v[88:91], v[132:135], v[176:179], v[88:91]\n" \
"v_pk_add_f16 v163, v163, v183\n" \
"ds_read_b128 v[172:175], v240 offset:6144\n" \
"buffer_load_dwordx4 v232, s[36:39], s80 offen offset:2048 sc1 lds\n" \
"v_mfma_f32_16x16x32_f16 v[76:79], v[128:131], v[176:179], v[76:79]\n" \
"s_setprio 0\n" \
"ds_read_b64_tr_b16 v[126:127], v242 offset:8192\n" \
"v_mfma_f32_16x16x32_f16 v[100:103], v[128:131], v[180:183], v[100:103]\n" \
"ds_read_b128 v[176:179], v240 offset:8192\n" \
"v_mfma_f32_16x16x32_f16 v[112:115], v[132:135], v[180:183], v[112:115]\n" \
"ds_read_b64_tr_b16 v[128:129], v241 offset:512\n" \
"v_mfma_f32_16x16x32_f16 v[8:11], v[136:139], v[148:151], v[8:11]\n" \
"ds_read_b128 v[180:183], v240 offset:0\n" \
"v_mfma_f32_16x16x32_f16 v[20:23], v[140:143], v[148:151], v[20:23]\n" \
"ds_read_b128 v[148:151], v240 offset:6656\n" \
"v_mfma_f32_16x16x32_f16 v[44:47], v[140:143], v[152:155], v[44:47]\n" \
"ds_read_b64_tr_b16 v[130:131], v241 offset:8704\n" \
"ds_read_b64_tr_b16 v[132:133], v242 offset:512\n" \
"buffer_load_dwordx4 v232, s[36:39], s80 offen offset:3072 sc1 lds\n" \
"v_mfma_f32_16x16x32_f16 v[32:35], v[136:139], v[152:155], v[32:35]\n" \
"ds_read_b128 v[152:155], v240 offset:8704\n" \
"v_mfma_f32_16x16x32_f16 v[56:59], v[136:139], v[208:211], v[56:59]\n" \
"ds_read_b64_tr_b16 v[134:135], v242 offset:8704\n" \
"v_mfma_f32_16x16x32_f16 v[68:71], v[140:143], v[208:211], v[68:71]\n" \
"ds_read_b128 v[208:211], v240 offset:512\n" \
"v_mfma_f32_16x16x32_f16 v[92:95], v[140:143], v[156:159], v[92:95]\n" \
"s_add_i32 m0, s45, 0xa000\n" \
"s_add_i32 s81, s47, 0x7400000\n" \
"v_mfma_f32_16x16x32_f16 v[80:83], v[136:139], v[156:159], v[80:83]\n" \
"buffer_load_dwordx4 v232, s[36:39], s81 offen sc1 lds\n" \
"v_mfma_f32_16x16x32_f16 v[104:107], v[136:139], v[160:163], v[104:107]\n" \
"v_mfma_f32_16x16x32_f16 v[116:119], v[140:143], v[160:163], v[116:119]\n" \
"s_setprio 1\n" \
"s_waitcnt vmcnt(9) lgkmcnt(0)\n" \
"s_barrier\n" \
"v_mfma_f32_16x16x32_f16 v[0:3], v[120:123], v[164:167], v[0:3]\n" \
"v_pk_add_f16 v136, v120, v128\n" \
"v_pk_add_f16 v137, v121, v129\n" \
"v_mfma_f32_16x16x32_f16 v[12:15], v[124:127], v[164:167], v[12:15]\n" \
"v_pk_add_f16 v164, v164, v184\n" \
"v_pk_add_f16 v138, v122, v130\n" \
"v_mfma_f32_16x16x32_f16 v[36:39], v[124:127], v[168:171], v[36:39]\n" \
"v_pk_add_f16 v165, v165, v185\n" \
"v_pk_add_f16 v166, v166, v186\n" \
"v_pk_add_f16 v167, v167, v187\n" \
"v_mfma_f32_16x16x32_f16 v[24:27], v[120:123], v[168:171], v[24:27]\n" \
"v_pk_add_f16 v168, v168, v144\n" \
"v_pk_add_f16 v139, v123, v131\n" \
"s_add_i32 m0, s44, 0x0\n" \
"s_add_i32 s80, s46, 0x7800000\n" \
"s_bitcmp0_b32 s47, 10\n" \
"s_cselect_b32 s80, 0x80000000, s80\n" \
"v_mfma_f32_16x16x32_f16 v[48:51], v[120:123], v[172:175], v[48:51]\n" \
"v_pk_add_f16 v169, v169, v145\n" \
"v_pk_add_f16 v140, v124, v132\n" \
"buffer_load_dwordx4 v232, s[36:39], s80 offen sc1 lds\n" \
"v_mfma_f32_16x16x32_f16 v[60:63], v[124:127], v[172:175], v[60:63]\n" \
"v_pk_add_f16 v170, v170, v146\n" \
"v_pk_add_f16 v171, v171, v147\n" \
"v_mfma_f32_16x16x32_f16 v[84:87], v[124:127], v[176:179], v[84:87]\n" \
"v_pk_add_f16 v172, v172, v148\n" \
"v_pk_add_f16 v141, v125, v133\n" \
"v_mfma_f32_16x16x32_f16 v[72:75], v[120:123], v[176:179], v[72:75]\n" \
"v_pk_add_f16 v173, v173, v149\n" \
"v_pk_add_f16 v142, v126, v134\n" \
"v_mfma_f32_16x16x32_f16 v[96:99], v[120:123], v[180:183], v[96:99]\n" \
"v_pk_add_f16 v174, v174, v150\n" \
"v_pk_add_f16 v175, v175, v151\n" \
"v_mfma_f32_16x16x32_f16 v[108:111], v[124:127], v[180:183], v[108:111]\n" \
"v_pk_add_f16 v176, v176, v152\n" \
"v_pk_add_f16 v143, v127, v135\n" \
"v_mfma_f32_16x16x32_f16 v[4:7], v[128:131], v[184:187], v[4:7]\n" \
"v_pk_add_f16 v177, v177, v153\n" \
"ds_read_b64_tr_b16 v[120:121], v241 offset:16384\n" \
"buffer_load_dwordx4 v232, s[36:39], s80 offen offset:1024 sc1 lds\n" \
"v_mfma_f32_16x16x32_f16 v[16:19], v[132:135], v[184:187], v[16:19]\n" \
"v_pk_add_f16 v178, v178, v154\n" \
"ds_read_b64_tr_b16 v[122:123], v241 offset:24576\n" \
"v_mfma_f32_16x16x32_f16 v[40:43], v[132:135], v[144:147], v[40:43]\n" \
"v_pk_add_f16 v179, v179, v155\n" \
"ds_read_b128 v[184:187], v240 offset:22528\n" \
"v_mfma_f32_16x16x32_f16 v[28:31], v[128:131], v[144:147], v[28:31]\n" \
"v_pk_add_f16 v180, v180, v208\n" \
"ds_read_b64_tr_b16 v[124:125], v242 offset:16384\n" \
"v_mfma_f32_16x16x32_f16 v[52:55], v[128:131], v[148:151], v[52:55]\n" \
"v_pk_add_f16 v181, v181, v209\n" \
"ds_read_b128 v[144:147], v240 offset:23040\n" \
"v_mfma_f32_16x16x32_f16 v[64:67], v[132:135], v[148:151], v[64:67]\n" \
"v_pk_add_f16 v182, v182, v210\n" \
"v_mfma_f32_16x16x32_f16 v[88:91], v[132:135], v[152:155], v[88:91]\n" \
"v_pk_add_f16 v183, v183, v211\n" \
"ds_read_b128 v[148:151], v240 offset:24576\n" \
"buffer_load_dwordx4 v232, s[36:39], s80 offen offset:2048 sc1 lds\n" \
"v_mfma_f32_16x16x32_f16 v[76:79], v[128:131], v[152:155], v[76:79]\n" \
"s_setprio 0\n" \
"ds_read_b64_tr_b16 v[126:127], v242 offset:24576\n" \
"v_mfma_f32_16x16x32_f16 v[100:103], v[128:131], v[208:211], v[100:103]\n" \
"ds_read_b128 v[152:155], v240 offset:25088\n" \
"v_mfma_f32_16x16x32_f16 v[112:115], v[132:135], v[208:211], v[112:115]\n" \
"ds_read_b64_tr_b16 v[128:129], v241 offset:16896\n" \
"v_mfma_f32_16x16x32_f16 v[8:11], v[136:139], v[164:167], v[8:11]\n" \
"ds_read_b64_tr_b16 v[130:131], v241 offset:25088\n" \
"v_mfma_f32_16x16x32_f16 v[20:23], v[140:143], v[164:167], v[20:23]\n" \
"ds_read_b64_tr_b16 v[132:133], v242 offset:16896\n" \
"v_mfma_f32_16x16x32_f16 v[44:47], v[140:143], v[168:171], v[44:47]\n" \
"v_add_u32 v243, 0x1b000, v241\n" \
"ds_read_b64_tr_b16 v[134:135], v242 offset:25088\n" \
"buffer_load_dwordx4 v232, s[36:39], s80 offen offset:3072 sc1 lds\n" \
"v_mfma_f32_16x16x32_f16 v[32:35], v[136:139], v[168:171], v[32:35]\n" \
"v_xor_b32 v244, 32, v243\n" \
"v_mfma_f32_16x16x32_f16 v[56:59], v[136:139], v[172:175], v[56:59]\n" \
"v_mfma_f32_16x16x32_f16 v[68:71], v[140:143], v[172:175], v[68:71]\n" \
"v_add_u32 v246, 0x15000, v240\n" \
"v_mfma_f32_16x16x32_f16 v[92:95], v[140:143], v[176:179], v[92:95]\n" \
"s_add_i32 m0, s45, 0x0\n" \
"s_add_i32 s81, s47, 0x7800000\n" \
"v_mfma_f32_16x16x32_f16 v[80:83], v[136:139], v[176:179], v[80:83]\n" \
"buffer_load_dwordx4 v232, s[36:39], s81 offen sc1 lds\n" \
"v_mfma_f32_16x16x32_f16 v[104:107], v[136:139], v[180:183], v[104:107]\n" \
"s_mov_b32 m0, 0x27c00\n" \
"v_mfma_f32_16x16x32_f16 v[116:119], v[140:143], v[180:183], v[116:119]\n" \
"global_load_lds_dword v[248:249], off\n" \
"s_waitcnt vmcnt(6) lgkmcnt(0)\n" \
"s_barrier\n" \
"v_mfma_f32_16x16x32_f16 v[0:3], v[120:123], v[184:187], v[0:3]\n" \
"v_pk_add_f16 v136, v120, v128\n" \
"v_pk_add_f16 v137, v121, v129\n" \
"v_pk_add_f16 v138, v122, v130\n" \
"v_pk_add_f16 v139, v123, v131\n" \
"ds_read_b128 v[156:159], v240 offset:43008\n" \
"ds_read_b64_tr_b16 v[164:165], v241 offset:32768\n" \
"v_mfma_f32_16x16x32_f16 v[12:15], v[124:127], v[184:187], v[12:15]\n" \
"v_pk_add_f16 v184, v184, v144\n" \
"v_pk_add_f16 v185, v185, v145\n" \
"v_pk_add_f16 v140, v124, v132\n" \
"ds_read_b64_tr_b16 v[166:167], v241 offset:40960\n" \
"ds_read_b64_tr_b16 v[168:169], v242 offset:32768\n" \
"v_mfma_f32_16x16x32_f16 v[4:7], v[128:131], v[144:147], v[4:7]\n" \
"v_pk_add_f16 v186, v186, v146\n" \
"v_pk_add_f16 v187, v187, v147\n" \
"v_pk_add_f16 v141, v125, v133\n" \
"s_add_i32 m0, s44, 0xffff7000\n" \
"s_add_i32 s80, s46, 0x7c00000\n" \
"s_bitcmp1_b32 s47, 10\n" \
"s_cselect_b64 exec, -1, 0\n" \
"buffer_load_dwordx4 v232, s[36:39], s80 offen sc1 lds\n" \
"buffer_load_dwordx4 v232, s[36:39], s80 offen offset:1024 sc1 lds\n" \
"buffer_load_dwordx4 v232, s[36:39], s80 offen offset:2048 sc1 lds\n" \
"buffer_load_dwordx4 v232, s[36:39], s80 offen offset:3072 sc1 lds\n" \
"s_mov_b64 exec, -1\n" \
"v_mfma_f32_16x16x32_f16 v[16:19], v[132:135], v[144:147], v[16:19]\n" \
"v_pk_add_f16 v142, v126, v134\n" \
"v_pk_add_f16 v143, v127, v135\n" \
"ds_read_b128 v[160:163], v240 offset:43520\n" \
"ds_read_b64_tr_b16 v[170:171], v242 offset:40960\n" \
"ds_read_b64_tr_b16 v[172:173], v241 offset:33280\n" \
"v_mfma_f32_16x16x32_f16 v[8:11], v[136:139], v[184:187], v[8:11]\n" \
"s_add_i32 m0, s45, 0xffff7000\n" \
"s_add_i32 s81, s47, 0x7c00000\n" \
"ds_read_b64_tr_b16 v[174:175], v241 offset:41472\n" \
"v_mfma_f32_16x16x32_f16 v[20:23], v[140:143], v[184:187], v[20:23]\n" \
"buffer_load_dwordx4 v232, s[36:39], s81 offen sc1 lds\n" \
"ds_read_b64_tr_b16 v[176:177], v242 offset:33280\n" \
"ds_read_b64_tr_b16 v[178:179], v242 offset:41472\n" \
"v_mfma_f32_16x16x32_f16 v[24:27], v[120:123], v[148:151], v[24:27]\n" \
"ds_read_b128 v[184:187], v246 offset:2048\n" \
"ds_read_b64_tr_b16 v[188:189], v243 offset:0\n" \
"ds_read_b64_tr_b16 v[190:191], v243 offset:8192\n" \
"v_mfma_f32_16x16x32_f16 v[36:39], v[124:127], v[148:151], v[36:39]\n" \
"v_pk_add_f16 v148, v148, v152\n" \
"v_pk_add_f16 v149, v149, v153\n" \
"ds_read_b64_tr_b16 v[192:193], v244 offset:0\n" \
"v_mfma_f32_16x16x32_f16 v[28:31], v[128:131], v[152:155], v[28:31]\n" \
"v_pk_add_f16 v150, v150, v154\n" \
"v_pk_add_f16 v151, v151, v155\n" \
"v_mfma_f32_16x16x32_f16 v[40:43], v[132:135], v[152:155], v[40:43]\n" \
"s_waitcnt lgkmcnt(7)\n" \
"v_pk_add_f16 v180, v164, v172\n" \
"ds_read_b128 v[144:147], v246 offset:2560\n" \
"ds_read_b64_tr_b16 v[194:195], v244 offset:8192\n" \
"ds_read_b64_tr_b16 v[196:197], v243 offset:512\n" \
"v_mfma_f32_16x16x32_f16 v[32:35], v[136:139], v[148:151], v[32:35]\n" \
"v_pk_add_f16 v181, v165, v173\n" \
"s_waitcnt lgkmcnt(9)\n" \
"v_pk_add_f16 v182, v166, v174\n" \
"ds_read_b64_tr_b16 v[198:199], v243 offset:8704\n" \
"v_mfma_f32_16x16x32_f16 v[44:47], v[140:143], v[148:151], v[44:47]\n" \
"v_pk_add_f16 v183, v167, v175\n" \
"s_waitcnt lgkmcnt(8)\n" \
"v_pk_add_f16 v236, v168, v176\n" \
"ds_read_b64_tr_b16 v[200:201], v244 offset:512\n" \
"ds_read_b64_tr_b16 v[202:203], v244 offset:8704\n" \
"v_mfma_f32_16x16x32_f16 v[0:3], v[164:167], v[156:159], v[0:3]\n" \
"v_pk_add_f16 v237, v169, v177\n" \
"ds_read_b128 v[148:151], v246 offset:10240\n" \
"ds_read_b64_tr_b16 v[212:213], v243 offset:16384\n" \
"ds_read_b64_tr_b16 v[214:215], v243 offset:24576\n" \
"v_mfma_f32_16x16x32_f16 v[12:15], v[168:171], v[156:159], v[12:15]\n" \
"v_pk_add_f16 v156, v156, v160\n" \
"v_pk_add_f16 v157, v157, v161\n" \
"ds_read_b64_tr_b16 v[216:217], v244 offset:16384\n" \
"v_mfma_f32_16x16x32_f16 v[4:7], v[172:175], v[160:163], v[4:7]\n" \
"v_pk_add_f16 v158, v158, v162\n" \
"v_pk_add_f16 v159, v159, v163\n" \
"v_pk_add_f16 v238, v170, v178\n" \
"v_pk_add_f16 v239, v171, v179\n" \
"v_mfma_f32_16x16x32_f16 v[16:19], v[176:179], v[160:163], v[16:19]\n" \
"s_waitcnt lgkmcnt(7)\n" \
"v_pk_add_f16 v204, v188, v196\n" \
"ds_read_b128 v[152:155], v246 offset:10752\n" \
"ds_read_b64_tr_b16 v[218:219], v244 offset:24576\n" \
"ds_read_b64_tr_b16 v[220:221], v243 offset:16896\n" \
"v_mfma_f32_16x16x32_f16 v[8:11], v[180:183], v[156:159], v[8:11]\n" \
"v_pk_add_f16 v205, v189, v197\n" \
"s_waitcnt lgkmcnt(8)\n" \
"v_pk_add_f16 v206, v190, v198\n" \
"ds_read_b64_tr_b16 v[222:223], v243 offset:25088\n" \
"v_mfma_f32_16x16x32_f16 v[20:23], v[236:239], v[156:159], v[20:23]\n" \
"ds_read_b64_tr_b16 v[224:225], v244 offset:16896\n" \
"ds_read_b64_tr_b16 v[226:227], v244 offset:25088\n" \
"v_mfma_f32_16x16x32_f16 v[0:3], v[188:191], v[184:187], v[0:3]\n" \
"v_pk_add_f16 v207, v191, v199\n" \
"v_pk_add_f16 v208, v192, v200\n" \
"ds_read_b128 v[156:159], v240 offset:45056\n" \
"v_mfma_f32_16x16x32_f16 v[12:15], v[192:195], v[184:187], v[12:15]\n" \
"v_pk_add_f16 v184, v184, v144\n" \
"v_pk_add_f16 v185, v185, v145\n" \
"v_pk_add_f16 v209, v193, v201\n" \
"v_mfma_f32_16x16x32_f16 v[4:7], v[196:199], v[144:147], v[4:7]\n" \
"v_pk_add_f16 v186, v186, v146\n" \
"v_pk_add_f16 v187, v187, v147\n" \
"s_waitcnt lgkmcnt(7)\n" \
"v_pk_add_f16 v210, v194, v202\n" \
"v_pk_add_f16 v211, v195, v203\n" \
"v_mfma_f32_16x16x32_f16 v[16:19], v[200:203], v[144:147], v[16:19]\n" \
"s_waitcnt lgkmcnt(4)\n" \
"v_pk_add_f16 v228, v212, v220\n" \
"ds_read_b128 v[160:163], v240 offset:45568\n" \
"v_mfma_f32_16x16x32_f16 v[8:11], v[204:207], v[184:187], v[8:11]\n" \
"v_pk_add_f16 v229, v213, v221\n" \
"v_mfma_f32_16x16x32_f16 v[20:23], v[208:211], v[184:187], v[20:23]\n" \
"s_waitcnt lgkmcnt(4)\n" \
"v_pk_add_f16 v230, v214, v222\n" \
"v_pk_add_f16 v231, v215, v223\n" \
"v_mfma_f32_16x16x32_f16 v[0:3], v[212:215], v[148:151], v[0:3]\n" \
"s_waitcnt lgkmcnt(2)\n" \
"v_pk_add_f16 v232, v216, v224\n" \
"v_pk_add_f16 v233, v217, v225\n" \
"ds_read_b128 v[184:187], v246 offset:4096\n" \
"v_mfma_f32_16x16x32_f16 v[12:15], v[216:219], v[148:151], v[12:15]\n" \
"v_pk_add_f16 v148, v148, v152\n" \
"v_pk_add_f16 v149, v149, v153\n" \
"v_pk_add_f16 v234, v218, v226\n" \
"v_mfma_f32_16x16x32_f16 v[4:7], v[220:223], v[152:155], v[4:7]\n" \
"v_pk_add_f16 v150, v150, v154\n" \
"v_pk_add_f16 v151, v151, v155\n" \
"v_pk_add_f16 v235, v219, v227\n" \
"v_mfma_f32_16x16x32_f16 v[16:19], v[224:227], v[152:155], v[16:19]\n" \
"ds_read_b128 v[144:147], v246 offset:4608\n" \
"v_mfma_f32_16x16x32_f16 v[8:11], v[228:231], v[148:151], v[8:11]\n" \
"v_mfma_f32_16x16x32_f16 v[20:23], v[232:235], v[148:151], v[20:23]\n" \
"v_add_u32 v241, 0xffff7000, v240\n" \
"s_waitcnt lgkmcnt(2)\n" \
"v_mfma_f32_16x16x32_f16 v[24:27], v[164:167], v[156:159], v[24:27]\n" \
"ds_read_b128 v[148:151], v246 offset:12288\n" \
"v_mfma_f32_16x16x32_f16 v[36:39], v[168:171], v[156:159], v[36:39]\n" \
"v_pk_add_f16 v156, v156, v160\n" \
"v_pk_add_f16 v157, v157, v161\n" \
"v_mfma_f32_16x16x32_f16 v[28:31], v[172:175], v[160:163], v[28:31]\n" \
"v_pk_add_f16 v158, v158, v162\n" \
"v_pk_add_f16 v159, v159, v163\n" \
"v_mfma_f32_16x16x32_f16 v[40:43], v[176:179], v[160:163], v[40:43]\n" \
"ds_read_b128 v[152:155], v246 offset:12800\n" \
"v_fmac_f32 v0, s56, v8\n" \
"v_fmac_f32 v1, s56, v9\n" \
"v_fmac_f32 v2, s56, v10\n" \
"v_fmac_f32 v3, s56, v11\n" \
"v_mfma_f32_16x16x32_f16 v[32:35], v[180:183], v[156:159], v[32:35]\n" \
"v_fmac_f32 v12, s56, v20\n" \
"v_fmac_f32 v13, s56, v21\n" \
"v_fmac_f32 v14, s56, v22\n" \
"v_fmac_f32 v15, s56, v23\n" \
"v_mfma_f32_16x16x32_f16 v[44:47], v[236:239], v[156:159], v[44:47]\n" \
"v_cvt_pk_bf16_f32 v0, v0, v1\n" \
"v_cvt_pk_bf16_f32 v1, v2, v3\n" \
"v_cvt_pk_bf16_f32 v2, v12, v13\n" \
"v_cvt_pk_bf16_f32 v3, v14, v15\n" \
"s_waitcnt lgkmcnt(2)\n" \
"v_mfma_f32_16x16x32_f16 v[24:27], v[188:191], v[184:187], v[24:27]\n" \
"ds_read_b128 v[156:159], v240 offset:26624\n" \
"buffer_store_dwordx4 v[0:3], v245, s[40:43], s69 offen sc1\n" \
"v_fmac_f32 v4, s56, v8\n" \
"v_fmac_f32 v5, s56, v9\n" \
"v_fmac_f32 v6, s56, v10\n" \
"v_fmac_f32 v7, s56, v11\n" \
"v_mfma_f32_16x16x32_f16 v[36:39], v[192:195], v[184:187], v[36:39]\n" \
"v_pk_add_f16 v184, v184, v144\n" \
"v_pk_add_f16 v185, v185, v145\n" \
"v_fmac_f32 v16, s56, v20\n" \
"v_fmac_f32 v17, s56, v21\n" \
"v_fmac_f32 v18, s56, v22\n" \
"v_fmac_f32 v19, s56, v23\n" \
"v_mfma_f32_16x16x32_f16 v[28:31], v[196:199], v[144:147], v[28:31]\n" \
"v_pk_add_f16 v186, v186, v146\n" \
"v_pk_add_f16 v187, v187, v147\n" \
"v_cvt_pk_bf16_f32 v4, v4, v5\n" \
"v_cvt_pk_bf16_f32 v5, v6, v7\n" \
"v_cvt_pk_bf16_f32 v6, v16, v17\n" \
"v_cvt_pk_bf16_f32 v7, v18, v19\n" \
"buffer_store_dwordx4 v[4:7], v245, s[40:43], s69 offen offset:128 sc1\n" \
"v_mfma_f32_16x16x32_f16 v[40:43], v[200:203], v[144:147], v[40:43]\n" \
"ds_read_b128 v[160:163], v240 offset:27136\n" \
"v_mfma_f32_16x16x32_f16 v[32:35], v[204:207], v[184:187], v[32:35]\n" \
"v_mfma_f32_16x16x32_f16 v[44:47], v[208:211], v[184:187], v[44:47]\n" \
"s_waitcnt lgkmcnt(1)\n" \
"v_mfma_f32_16x16x32_f16 v[24:27], v[212:215], v[148:151], v[24:27]\n" \
"ds_read_b128 v[184:187], v240 offset:47104\n" \
"v_mfma_f32_16x16x32_f16 v[36:39], v[216:219], v[148:151], v[36:39]\n" \
"v_pk_add_f16 v148, v148, v152\n" \
"v_pk_add_f16 v149, v149, v153\n" \
"v_mfma_f32_16x16x32_f16 v[28:31], v[220:223], v[152:155], v[28:31]\n" \
"v_pk_add_f16 v150, v150, v154\n" \
"v_pk_add_f16 v151, v151, v155\n" \
"v_mfma_f32_16x16x32_f16 v[40:43], v[224:227], v[152:155], v[40:43]\n" \
"ds_read_b128 v[144:147], v240 offset:47616\n" \
"v_mfma_f32_16x16x32_f16 v[32:35], v[228:231], v[148:151], v[32:35]\n" \
"s_waitcnt vmcnt(2)\n" \
"s_barrier\n" \
"v_mfma_f32_16x16x32_f16 v[44:47], v[232:235], v[148:151], v[44:47]\n" \
"v_mfma_f32_16x16x32_f16 v[48:51], v[120:123], v[156:159], v[48:51]\n" \
"ds_read_b128 v[148:151], v240 offset:6144\n" \
"v_mfma_f32_16x16x32_f16 v[60:63], v[124:127], v[156:159], v[60:63]\n" \
"s_waitcnt lgkmcnt(3)\n" \
"v_pk_add_f16 v156, v156, v160\n" \
"v_pk_add_f16 v157, v157, v161\n" \
"v_mfma_f32_16x16x32_f16 v[52:55], v[128:131], v[160:163], v[52:55]\n" \
"v_pk_add_f16 v158, v158, v162\n" \
"v_pk_add_f16 v159, v159, v163\n" \
"v_mfma_f32_16x16x32_f16 v[64:67], v[132:135], v[160:163], v[64:67]\n" \
"ds_read_b128 v[152:155], v240 offset:6656\n" \
"v_fmac_f32 v24, s56, v32\n" \
"v_fmac_f32 v25, s56, v33\n" \
"v_fmac_f32 v26, s56, v34\n" \
"v_mfma_f32_16x16x32_f16 v[56:59], v[136:139], v[156:159], v[56:59]\n" \
"v_fmac_f32 v27, s56, v35\n" \
"v_fmac_f32 v36, s56, v44\n" \
"v_fmac_f32 v37, s56, v45\n" \
"v_mfma_f32_16x16x32_f16 v[68:71], v[140:143], v[156:159], v[68:71]\n" \
"v_fmac_f32 v38, s56, v46\n" \
"v_fmac_f32 v39, s56, v47\n" \
"v_cvt_pk_bf16_f32 v24, v24, v25\n" \
"s_waitcnt lgkmcnt(2)\n" \
"v_mfma_f32_16x16x32_f16 v[48:51], v[164:167], v[184:187], v[48:51]\n" \
"ds_read_b128 v[156:159], v241 offset:6144\n" \
"v_cvt_pk_bf16_f32 v25, v26, v27\n" \
"v_cvt_pk_bf16_f32 v26, v36, v37\n" \
"v_cvt_pk_bf16_f32 v27, v38, v39\n" \
"v_mfma_f32_16x16x32_f16 v[60:63], v[168:171], v[184:187], v[60:63]\n" \
"v_pk_add_f16 v184, v184, v144\n" \
"v_pk_add_f16 v185, v185, v145\n" \
"buffer_store_dwordx4 v[24:27], v245, s[40:43], s70 offen sc1\n" \
"v_fmac_f32 v28, s56, v32\n" \
"v_fmac_f32 v29, s56, v33\n" \
"v_fmac_f32 v30, s56, v34\n" \
"v_mfma_f32_16x16x32_f16 v[52:55], v[172:175], v[144:147], v[52:55]\n" \
"v_pk_add_f16 v186, v186, v146\n" \
"v_pk_add_f16 v187, v187, v147\n" \
"v_fmac_f32 v31, s56, v35\n" \
"v_fmac_f32 v40, s56, v44\n" \
"v_fmac_f32 v41, s56, v45\n" \
"v_mfma_f32_16x16x32_f16 v[64:67], v[176:179], v[144:147], v[64:67]\n" \
"ds_read_b128 v[160:163], v241 offset:6656\n" \
"v_fmac_f32 v42, s56, v46\n" \
"v_fmac_f32 v43, s56, v47\n" \
"v_cvt_pk_bf16_f32 v28, v28, v29\n" \
"v_mfma_f32_16x16x32_f16 v[56:59], v[180:183], v[184:187], v[56:59]\n" \
"v_cvt_pk_bf16_f32 v29, v30, v31\n" \
"v_cvt_pk_bf16_f32 v30, v40, v41\n" \
"v_cvt_pk_bf16_f32 v31, v42, v43\n" \
"buffer_store_dwordx4 v[28:31], v245, s[40:43], s70 offen offset:128 sc1\n" \
"v_mfma_f32_16x16x32_f16 v[68:71], v[236:239], v[184:187], v[68:71]\n" \
"s_waitcnt lgkmcnt(1)\n" \
"v_mfma_f32_16x16x32_f16 v[48:51], v[188:191], v[148:151], v[48:51]\n" \
"ds_read_b128 v[184:187], v240 offset:28672\n" \
"v_mfma_f32_16x16x32_f16 v[60:63], v[192:195], v[148:151], v[60:63]\n" \
"v_pk_add_f16 v148, v148, v152\n" \
"v_pk_add_f16 v149, v149, v153\n" \
"v_mfma_f32_16x16x32_f16 v[52:55], v[196:199], v[152:155], v[52:55]\n" \
"v_pk_add_f16 v150, v150, v154\n" \
"v_pk_add_f16 v151, v151, v155\n" \
"v_mfma_f32_16x16x32_f16 v[64:67], v[200:203], v[152:155], v[64:67]\n" \
"ds_read_b128 v[144:147], v240 offset:29184\n" \
"v_mfma_f32_16x16x32_f16 v[56:59], v[204:207], v[148:151], v[56:59]\n" \
"v_mfma_f32_16x16x32_f16 v[68:71], v[208:211], v[148:151], v[68:71]\n" \
"v_mfma_f32_16x16x32_f16 v[48:51], v[212:215], v[156:159], v[48:51]\n" \
"ds_read_b128 v[148:151], v240 offset:49152\n" \
"v_mfma_f32_16x16x32_f16 v[60:63], v[216:219], v[156:159], v[60:63]\n" \
"s_waitcnt lgkmcnt(3)\n" \
"v_pk_add_f16 v156, v156, v160\n" \
"v_pk_add_f16 v157, v157, v161\n" \
"v_mfma_f32_16x16x32_f16 v[52:55], v[220:223], v[160:163], v[52:55]\n" \
"v_pk_add_f16 v158, v158, v162\n" \
"v_pk_add_f16 v159, v159, v163\n" \
"v_mfma_f32_16x16x32_f16 v[64:67], v[224:227], v[160:163], v[64:67]\n" \
"ds_read_b128 v[152:155], v240 offset:49664\n" \
"v_mfma_f32_16x16x32_f16 v[56:59], v[228:231], v[156:159], v[56:59]\n" \
"v_mfma_f32_16x16x32_f16 v[68:71], v[232:235], v[156:159], v[68:71]\n" \
"s_waitcnt lgkmcnt(3)\n" \
"v_mfma_f32_16x16x32_f16 v[72:75], v[120:123], v[184:187], v[72:75]\n" \
"ds_read_b128 v[156:159], v240 offset:8192\n" \
"v_mfma_f32_16x16x32_f16 v[84:87], v[124:127], v[184:187], v[84:87]\n" \
"s_waitcnt lgkmcnt(3)\n" \
"v_pk_add_f16 v184, v184, v144\n" \
"v_pk_add_f16 v185, v185, v145\n" \
"v_mfma_f32_16x16x32_f16 v[76:79], v[128:131], v[144:147], v[76:79]\n" \
"v_pk_add_f16 v186, v186, v146\n" \
"v_pk_add_f16 v187, v187, v147\n" \
"v_mfma_f32_16x16x32_f16 v[88:91], v[132:135], v[144:147], v[88:91]\n" \
"ds_read_b128 v[160:163], v240 offset:8704\n" \
"v_fmac_f32 v48, s56, v56\n" \
"v_fmac_f32 v49, s56, v57\n" \
"v_mfma_f32_16x16x32_f16 v[80:83], v[136:139], v[184:187], v[80:83]\n" \
"v_fmac_f32 v50, s56, v58\n" \
"v_fmac_f32 v51, s56, v59\n" \
"v_fmac_f32 v60, s56, v68\n" \
"v_mfma_f32_16x16x32_f16 v[92:95], v[140:143], v[184:187], v[92:95]\n" \
"v_fmac_f32 v61, s56, v69\n" \
"v_fmac_f32 v62, s56, v70\n" \
"s_waitcnt lgkmcnt(2)\n" \
"v_mfma_f32_16x16x32_f16 v[72:75], v[164:167], v[148:151], v[72:75]\n" \
"ds_read_b128 v[184:187], v241 offset:8192\n" \
"v_fmac_f32 v63, s56, v71\n" \
"v_cvt_pk_bf16_f32 v48, v48, v49\n" \
"v_cvt_pk_bf16_f32 v49, v50, v51\n" \
"v_mfma_f32_16x16x32_f16 v[84:87], v[168:171], v[148:151], v[84:87]\n" \
"v_pk_add_f16 v148, v148, v152\n" \
"v_pk_add_f16 v149, v149, v153\n" \
"v_cvt_pk_bf16_f32 v50, v60, v61\n" \
"v_cvt_pk_bf16_f32 v51, v62, v63\n" \
"v_mfma_f32_16x16x32_f16 v[76:79], v[172:175], v[152:155], v[76:79]\n" \
"v_pk_add_f16 v150, v150, v154\n" \
"v_pk_add_f16 v151, v151, v155\n" \
"buffer_store_dwordx4 v[48:51], v245, s[40:43], s71 offen sc1\n" \
"v_fmac_f32 v52, s56, v56\n" \
"v_fmac_f32 v53, s56, v57\n" \
"v_mfma_f32_16x16x32_f16 v[88:91], v[176:179], v[152:155], v[88:91]\n" \
"ds_read_b128 v[144:147], v241 offset:8704\n" \
"v_fmac_f32 v54, s56, v58\n" \
"v_fmac_f32 v55, s56, v59\n" \
"v_fmac_f32 v64, s56, v68\n" \
"v_mfma_f32_16x16x32_f16 v[80:83], v[180:183], v[148:151], v[80:83]\n" \
"v_fmac_f32 v65, s56, v69\n" \
"v_fmac_f32 v66, s56, v70\n" \
"v_mfma_f32_16x16x32_f16 v[92:95], v[236:239], v[148:151], v[92:95]\n" \
"v_fmac_f32 v67, s56, v71\n" \
"v_cvt_pk_bf16_f32 v52, v52, v53\n" \
"v_cvt_pk_bf16_f32 v53, v54, v55\n" \
"s_waitcnt lgkmcnt(1)\n" \
"v_mfma_f32_16x16x32_f16 v[72:75], v[188:191], v[156:159], v[72:75]\n" \
"ds_read_b128 v[148:151], v240 offset:20480\n" \
"v_cvt_pk_bf16_f32 v54, v64, v65\n" \
"v_cvt_pk_bf16_f32 v55, v66, v67\n" \
"buffer_store_dwordx4 v[52:55], v245, s[40:43], s71 offen offset:128 sc1\n" \
"v_mfma_f32_16x16x32_f16 v[84:87], v[192:195], v[156:159], v[84:87]\n" \
"v_pk_add_f16 v156, v156, v160\n" \
"v_pk_add_f16 v157, v157, v161\n" \
"v_mfma_f32_16x16x32_f16 v[76:79], v[196:199], v[160:163], v[76:79]\n" \
"v_pk_add_f16 v158, v158, v162\n" \
"v_pk_add_f16 v159, v159, v163\n" \
"v_mfma_f32_16x16x32_f16 v[88:91], v[200:203], v[160:163], v[88:91]\n" \
"ds_read_b128 v[152:155], v240 offset:20992\n" \
"v_mfma_f32_16x16x32_f16 v[80:83], v[204:207], v[156:159], v[80:83]\n" \
"v_mfma_f32_16x16x32_f16 v[92:95], v[208:211], v[156:159], v[92:95]\n" \
"v_mfma_f32_16x16x32_f16 v[72:75], v[212:215], v[184:187], v[72:75]\n" \
"ds_read_b128 v[156:159], v240 offset:40960\n" \
"v_mfma_f32_16x16x32_f16 v[84:87], v[216:219], v[184:187], v[84:87]\n" \
"s_waitcnt lgkmcnt(3)\n" \
"v_pk_add_f16 v184, v184, v144\n" \
"v_pk_add_f16 v185, v185, v145\n" \
"v_mfma_f32_16x16x32_f16 v[76:79], v[220:223], v[144:147], v[76:79]\n" \
"v_pk_add_f16 v186, v186, v146\n" \
"v_pk_add_f16 v187, v187, v147\n" \
"v_mfma_f32_16x16x32_f16 v[88:91], v[224:227], v[144:147], v[88:91]\n" \
"ds_read_b128 v[160:163], v240 offset:41472\n" \
"v_mfma_f32_16x16x32_f16 v[80:83], v[228:231], v[184:187], v[80:83]\n" \
"v_mfma_f32_16x16x32_f16 v[92:95], v[232:235], v[184:187], v[92:95]\n" \
"s_waitcnt lgkmcnt(3)\n" \
"v_mfma_f32_16x16x32_f16 v[96:99], v[120:123], v[148:151], v[96:99]\n" \
"ds_read_b128 v[184:187], v240 offset:0\n" \
"v_mfma_f32_16x16x32_f16 v[108:111], v[124:127], v[148:151], v[108:111]\n" \
"s_waitcnt lgkmcnt(3)\n" \
"v_pk_add_f16 v148, v148, v152\n" \
"v_pk_add_f16 v149, v149, v153\n" \
"v_mfma_f32_16x16x32_f16 v[100:103], v[128:131], v[152:155], v[100:103]\n" \
"v_pk_add_f16 v150, v150, v154\n" \
"v_pk_add_f16 v151, v151, v155\n" \
"v_mfma_f32_16x16x32_f16 v[112:115], v[132:135], v[152:155], v[112:115]\n" \
"ds_read_b128 v[144:147], v240 offset:512\n" \
"v_fmac_f32 v72, s56, v80\n" \
"v_fmac_f32 v73, s56, v81\n" \
"v_mfma_f32_16x16x32_f16 v[104:107], v[136:139], v[148:151], v[104:107]\n" \
"v_fmac_f32 v74, s56, v82\n" \
"v_fmac_f32 v75, s56, v83\n" \
"v_fmac_f32 v84, s56, v92\n" \
"v_mfma_f32_16x16x32_f16 v[116:119], v[140:143], v[148:151], v[116:119]\n" \
"v_fmac_f32 v85, s56, v93\n" \
"v_fmac_f32 v86, s56, v94\n" \
"s_waitcnt lgkmcnt(2)\n" \
"v_mfma_f32_16x16x32_f16 v[96:99], v[164:167], v[156:159], v[96:99]\n" \
"ds_read_b128 v[148:151], v241 offset:0\n" \
"v_fmac_f32 v87, s56, v95\n" \
"v_cvt_pk_bf16_f32 v72, v72, v73\n" \
"v_cvt_pk_bf16_f32 v73, v74, v75\n" \
"v_mfma_f32_16x16x32_f16 v[108:111], v[168:171], v[156:159], v[108:111]\n" \
"v_pk_add_f16 v156, v156, v160\n" \
"v_pk_add_f16 v157, v157, v161\n" \
"v_cvt_pk_bf16_f32 v74, v84, v85\n" \
"v_cvt_pk_bf16_f32 v75, v86, v87\n" \
"v_mfma_f32_16x16x32_f16 v[100:103], v[172:175], v[160:163], v[100:103]\n" \
"v_pk_add_f16 v158, v158, v162\n" \
"v_pk_add_f16 v159, v159, v163\n" \
"buffer_store_dwordx4 v[72:75], v245, s[40:43], s72 offen sc1\n" \
"v_fmac_f32 v76, s56, v80\n" \
"v_fmac_f32 v77, s56, v81\n" \
"v_mfma_f32_16x16x32_f16 v[112:115], v[176:179], v[160:163], v[112:115]\n" \
"ds_read_b128 v[152:155], v241 offset:512\n" \
"v_fmac_f32 v78, s56, v82\n" \
"v_fmac_f32 v79, s56, v83\n" \
"v_fmac_f32 v88, s56, v92\n" \
"v_mfma_f32_16x16x32_f16 v[104:107], v[180:183], v[156:159], v[104:107]\n" \
"v_fmac_f32 v89, s56, v93\n" \
"v_fmac_f32 v90, s56, v94\n" \
"v_mfma_f32_16x16x32_f16 v[116:119], v[236:239], v[156:159], v[116:119]\n" \
"v_fmac_f32 v91, s56, v95\n" \
"v_cvt_pk_bf16_f32 v76, v76, v77\n" \
"v_cvt_pk_bf16_f32 v77, v78, v79\n" \
"s_waitcnt lgkmcnt(1)\n" \
"v_mfma_f32_16x16x32_f16 v[96:99], v[188:191], v[184:187], v[96:99]\n" \
"v_cvt_pk_bf16_f32 v78, v88, v89\n" \
"v_cvt_pk_bf16_f32 v79, v90, v91\n" \
"buffer_store_dwordx4 v[76:79], v245, s[40:43], s72 offen offset:128 sc1\n" \
"v_mfma_f32_16x16x32_f16 v[108:111], v[192:195], v[184:187], v[108:111]\n" \
"v_pk_add_f16 v184, v184, v144\n" \
"v_pk_add_f16 v185, v185, v145\n" \
"v_mfma_f32_16x16x32_f16 v[100:103], v[196:199], v[144:147], v[100:103]\n" \
"v_pk_add_f16 v186, v186, v146\n" \
"v_pk_add_f16 v187, v187, v147\n" \
"v_mfma_f32_16x16x32_f16 v[112:115], v[200:203], v[144:147], v[112:115]\n" \
"s_nop 0\n" \
"v_mfma_f32_16x16x32_f16 v[104:107], v[204:207], v[184:187], v[104:107]\n" \
"v_mfma_f32_16x16x32_f16 v[116:119], v[208:211], v[184:187], v[116:119]\n" \
"v_mfma_f32_16x16x32_f16 v[96:99], v[212:215], v[148:151], v[96:99]\n" \
"v_mfma_f32_16x16x32_f16 v[108:111], v[216:219], v[148:151], v[108:111]\n" \
"s_waitcnt lgkmcnt(0)\n" \
"v_pk_add_f16 v148, v148, v152\n" \
"v_pk_add_f16 v149, v149, v153\n" \
"v_mfma_f32_16x16x32_f16 v[100:103], v[220:223], v[152:155], v[100:103]\n" \
"v_pk_add_f16 v150, v150, v154\n" \
"v_pk_add_f16 v151, v151, v155\n" \
"v_mfma_f32_16x16x32_f16 v[112:115], v[224:227], v[152:155], v[112:115]\n" \
"s_nop 0\n" \
"v_mfma_f32_16x16x32_f16 v[104:107], v[228:231], v[148:151], v[104:107]\n" \
"v_mfma_f32_16x16x32_f16 v[116:119], v[232:235], v[148:151], v[116:119]\n" \
"s_nop 7\n" \
"v_fmac_f32 v96, s56, v104\n" \
"v_fmac_f32 v97, s56, v105\n" \
"v_fmac_f32 v98, s56, v106\n" \
"v_fmac_f32 v99, s56, v107\n" \
"v_fmac_f32 v108, s56, v116\n" \
"v_fmac_f32 v109, s56, v117\n" \
"v_fmac_f32 v110, s56, v118\n" \
"v_fmac_f32 v111, s56, v119\n" \
"v_cvt_pk_bf16_f32 v96, v96, v97\n" \
"v_cvt_pk_bf16_f32 v97, v98, v99\n" \
"v_cvt_pk_bf16_f32 v98, v108, v109\n" \
"v_cvt_pk_bf16_f32 v99, v110, v111\n" \
"buffer_store_dwordx4 v[96:99], v245, s[40:43], s73 offen sc1\n" \
"v_fmac_f32 v100, s56, v104\n" \
"v_fmac_f32 v101, s56, v105\n" \
"v_fmac_f32 v102, s56, v106\n" \
"v_fmac_f32 v103, s56, v107\n" \
"v_fmac_f32 v112, s56, v116\n" \
"v_fmac_f32 v113, s56, v117\n" \
"v_fmac_f32 v114, s56, v118\n" \
"v_fmac_f32 v115, s56, v119\n" \
"v_cvt_pk_bf16_f32 v100, v100, v101\n" \
"v_cvt_pk_bf16_f32 v101, v102, v103\n" \
"v_cvt_pk_bf16_f32 v102, v112, v113\n" \
"v_cvt_pk_bf16_f32 v103, v114, v115\n" \
"buffer_store_dwordx4 v[100:103], v245, s[40:43], s73 offen offset:128 sc1\n" \
"s_waitcnt vmcnt(0)\n" \
"s_barrier\n" \
"s_branch .Lcf_e\n" \
".Ls12_%=:\n" \
"s_mov_b32 s87, 0x200000\n" \
"s_mov_b32 s86, 0x8004000\n" \
".Lt12_%=:\n" \
"s_sleep 3\n" \
"buffer_load_dword v253, v252, s[36:39], s86 offen sc1\n" \
"s_waitcnt vmcnt(0)\n" \
"s_sub_u32 s87, s87, 1\n" \
"s_cbranch_scc1 .Lr12_%=\n" \
"v_cmp_eq_u32 vcc, 0, v253\n" \
"s_cbranch_vccnz .Lt12_%=\n" \
"s_branch .Lr12_%=\n" \
".Ls16_%=:\n" \
"s_mov_b32 s87, 0x200000\n" \
"s_mov_b32 s86, 0x800c000\n" \
".Lt16_%=:\n" \
"s_sleep 3\n" \
"buffer_load_dword v253, v252, s[36:39], s86 offen sc1\n" \
"s_waitcnt vmcnt(0)\n" \
"s_sub_u32 s87, s87, 1\n" \
"s_cbranch_scc1 .Lr16_%=\n" \
"v_cmp_eq_u32 vcc, 0, v253\n" \
"s_cbranch_vccnz .Lt16_%=\n" \
"s_branch .Lr16_%=\n" \
".Ls110_%=:\n" \
"s_mov_b32 s87, 0x200000\n" \
"s_mov_b32 s86, 0x8014000\n" \
".Lt110_%=:\n" \
"s_sleep 3\n" \
"buffer_load_dword v253, v252, s[36:39], s86 offen sc1\n" \
"s_waitcnt vmcnt(0)\n" \
"s_sub_u32 s87, s87, 1\n" \
"s_cbranch_scc1 .Lr110_%=\n" \
"v_cmp_eq_u32 vcc, 0, v253\n" \
"s_cbranch_vccnz .Lt110_%=\n" \
"s_branch .Lr110_%=\n" \
""
#define CLOBBERS "v0", "v1", "v2", "v3", "v4", "v5", "v6", "v7", "v8", "v9", "v10", "v11", "v12", "v13", "v14", "v15", "v16", "v17", "v18", "v19", "v20", "v21", "v22", "v23", "v24", "v25", "v26", "v27", "v28", "v29", "v30", "v31", "v32", "v33", "v34", "v35", "v36", "v37", "v38", "v39", "v40", "v41", "v42", "v43", "v44", "v45", "v46", "v47", "v48", "v49", "v50", "v51", "v52", "v53", "v54", "v55", "v56", "v57", "v58", "v59", "v60", "v61", "v62", "v63", "v64", "v65", "v66", "v67", "v68", "v69", "v70", "v71", "v84", "v85", "v86", "v87", "v88", "v89", "v90", "v91", "v92", "v93", "v94", "v95", "v120", "v121", "v122", "v123", "v124", "v125", "v126", "v127", "v128", "v129", "v130", "v131", "v132", "v133", "v134", "v135", "v136", "v137", "v138", "v139", "v140", "v141", "v142", "v143", "v144", "v145", "v146", "v147", "v148", "v149", "v150", "v151", "v152", "v153", "v154", "v155", "v156", "v157", "v158", "v159", "v160", "v161", "v162", "v163", "v164", "v165", "v166", "v167", "v168", "v169", "v170", "v171", "v172", "v173", "v174", "v175", "v176", "v177", "v178", "v179", "v180", "v181", "v182", "v183", "v184", "v185", "v186", "v187", "v188", "v189", "v190", "v191", "v76", "v77", "v78", "v79", "v80", "v81", "v82", "v83", "v253", "v192", "v193", "v194", "v195", "s80", "s81", "s82", "s83", "s84", "s85", "s86", "s87", "s88", "s89", "s90", "s91", "s92", "s75", "s94", "s95"
template <int NM>
__device__ __forceinline__ void run1(Ctx& cx, const unsigned short* __restrict__ x, const unsigned short* __restrict__ w, RQ& rq) {
  unsigned xs;
  phase1<NM>(cx, x, w, rq, xs, rq.vb, [&]() __attribute__((always_inline)) {
  static_assert(NS - st_first(PG) == 3 && NH == 10 && NQW == 11 && RAWL == 110592, "constants of the generated code");
  const unsigned bid = cx.vb, idx = bid >> 3, slot = (bid & 7) * 4 + (idx >> 3), lane = cx.lane4 >> 2, h = (idx >> 2) & 1, wv = cx.wv;
  const unsigned lds0 = (unsigned)(uintptr_t)(lds_void*)cx.smem;
  unsigned* sa = rq.s, *sb = rq.s + 16;
  sa[0] = cx.ldg; sa[1] = cx.ldf; sa[2] = cx.sg; sa[3] = cx.sf;
#pragma unroll
  for (int j = 0; j < 4; ++j) {
    sa[4 + j] = ((bid + j) & 3) * 16384;
    sa[8 + j] = ((bid + j) & 3) * 256;
  }
  if (NM == 5) {
    sa[8] = lds0 + RAWL + 32768 + (wv & 1) * 8192 + (wv & 2) * 2048;
    sa[11] = (30 + (wv & 1)) * SBLK + (wv & 2) * 10240;
    const unsigned long long pace = wv == 7 ? (bid & 1 ? bytes(105, 100, 90, 88, 86, 84, 82) : bytes(150, 140, 120, 100, 90, 88, 86)) : 0ull;
    sa[9] = (unsigned)pace;
    sa[13] = (unsigned)(pace >> 32);
    sa[10] = lds0 + CORD + (wv & 3) * 1024;
  }
  sa[12] = min(slot, 1u) * 0xbf800000u;
  if (NM == 6) {
    const unsigned fg = WS_GRP - WS_S + (idx & 3) * 256 + h * 128;
    sa[13] = wv == 1 ? 255u : 0u;
    sa[14] = fg + slot * 4;
    sa[15] = wv == 0 ? fg + ((bid & 7) + (bid & 4)) * 1024 : 0x80000000u;
    sb[14] = slot * 4;
  }
#pragma unroll
  for (int n = 0; n < NH; ++n) {
    const unsigned blk = (xs & 255u) + n % 5;
    rq.s[n ? 15 + n : 31] = (xs & ~255u) + (n / 5) * (8 * SBLK) + (blk + (blk & 192u)) * 128u;
  }
#pragma unroll
  for (int m = 0; m < 5; ++m) {
    const unsigned t = NM == 5 ? (m + 1) % 5 : m;
    sb[9 + m] = t * (16 * 65536) + ((h * 10 + (wv >> 2) * 5 + (wv & 2) + t) & 2) * 4096;
  }
#pragma unroll
  for (int i = 0; i < 32; ++i) rq.s[i] = __builtin_amdgcn_readfirstlane(rq.s[i]);
  const unsigned prd = cx.prd + (RAWL - RAWB);
  const unsigned yo = (h * NBLK + (wv >> 2) * 80 + (lane & 14)) * 65536 + (idx & 3) * 32768 + (wv & 1) * 16384 + slot * 256 + (lane & 1) * 64 + (lane >> 4) * 16;
  unsigned* v = rq.v;
  v[0] = cx.voff; v[1] = cx.voff5; v[2] = cx.poff; v[3] = cx.xo; v[4] = prd; v[5] = prd ^ 32u; v[6] = prd ^ 64u; v[7] = prd ^ 96u;
  v[8] = cx.xbase; v[9] = cx.wbase; v[10] = cx.wbase ^ 32u; v[11] = cx.fvo; v[12] = lane * 16; v[13] = yo; v[14] = cx.pwr + (RAWL - RAWB);
  if (NM == 6) {
    v[15] = cx.crd;
    v[16] = CBUF + min(lane & 15, 10u) * 272 + wv * 64 + (cx.lane4 >> 6) * 16;
    v[17] = CORD + lane * 16;
    v[18] = (bid & 7) * 1024 + (idx & 3) * 256 + lane * 4;
  } else {
    const unsigned co = (((bid >> 3) & 3) * 2 + ((bid >> 2) & 1)) * 128u + (cx.lane4 & 127u);
    const unsigned long long p = (unsigned long long)((const char*)(cx.lane4 < 128 ? cx.gamma : cx.beta) + co);
    v[15] = 0u;
    v[16] = (unsigned)p;
    v[17] = (unsigned)(p >> 32);
    v[18] = cx.fvo;
  }
#pragma unroll
  for (int i = 0; i < 32; ++i) asm volatile("" : "+s"(rq.s[i]));
#pragma unroll
  for (int i = 0; i < 19; ++i) asm volatile("" : "+v"(rq.v[i]));
  SB0;
  });
}

template <int CTRL>
__device__ __forceinline__ float bfly(float a, float b, bool hi) {
  const float give = hi ? a : b, keep = hi ? b : a;
  return keep + __builtin_bit_cast(float, __builtin_amdgcn_update_dpp(0, __builtin_bit_cast(int, give), CTRL, 0xf, 0xf, false));
}
__device__ __forceinline__ float halves_sum(float v) {
  const auto r = __builtin_amdgcn_permlane32_swap(__float_as_uint(v), __float_as_uint(v), false, false);
  return __uint_as_float(r[0]) + __uint_as_float(r[1]);
}

extern "C" __global__ __launch_bounds__(512, 2) void conv_fft(char* ws, const unsigned short* __restrict__ x,
                                                   const unsigned short* __restrict__ w,
                                                   const unsigned short* __restrict__ gamma,
                                                   const unsigned short* __restrict__ beta,
                                                   unsigned short* __restrict__ out) {
  __shared__ __attribute__((aligned(16))) char smem[160 * 1024];
  const unsigned lds0 = (unsigned)(uintptr_t)(lds_void*)smem;
  const int wv = __builtin_amdgcn_readfirstlane(threadIdx.x >> 6);
  Ctx cx;
  {
    unsigned len;
    asm volatile("s_getpc_b64 %0\n.Lcf_a:\n\ts_mov_b32 %1, .Lcf_b-.Lcf_a" : "=s"(cx.pc), "=s"(len));
    cx.wv = wv;
    cx.pfo = len;
    unsigned xcc;
    asm volatile("s_getreg_b32 %0, hwreg(HW_REG_XCC_ID, 0, 3)" : "=s"(xcc));
    unsigned id = blockIdx.x;
    asm volatile("" : "+s"(id));
    cx.id = id;
    cx.vb = (int)((id & 248u) | (id & 256u ? (id >> 10) & 7u : xcc));
    unsigned xa = x_addr(cx.vb, 0, wv), xo = ((cx.vb >> 2) & 1) * (T_ * D_ * 2), l1 = lds0 + wv * 1024;
    asm volatile("" : "+v"(xa));
    asm volatile("" : "+s"(xo), "+s"(l1));
    SB0;
    const char* pb = ws + WS_PUB;
    u32x4 sx, sw;
    if (((id | wv) == 0)) {
      const unsigned l = threadIdx.x;
      const u32x4 z = {0u, 0u, 0u, 0u};
      {
#pragma unroll
        for (int k = 0; k < 2; ++k)
          asm volatile("global_store_dwordx4 %0, %1, %2 sc1" :: "v"(32768 + (l & 7) * 16 + (l & 8) * 512 + (l & 16) * 1024 + (l & 32) * 32 + k * 8192), "v"(z), "s"(pb) : "memory");
      }
      {
        const unsigned so = l * (31u * 1024u + 16u) + (4u << 20);
        asm volatile("global_store_dwordx4 %2, %3, %4 sc1\n\tglobal_load_dwordx4 %0, %5, %6\n\tglobal_load_dwordx4 %1, %7, %8"
                     : "=&v"(sx), "=&v"(sw) : "v"(l * 16 + 1024), "v"(z), "s"(pb), "v"(so + (l & 3u) * (8u << 20)), "s"(x), "v"(so + (l & 1u) * (6u << 20)), "s"(w) : "memory");
      }
    }
    const u32x4 rx = make_rsrc((const char*)x + xo, T_ * D_ * 2);
    issue_x(0, 0, xa, rx, l1, 0, 2);
    if (id) code_touch(cx, 0, 1 & 1 ? 1 : 2);
    SB0;
    issue_x(0, wv, xa, rx, lds0, 2, 9);
    SB0;
    if (((id | wv) == 0)) {
      wait_vm<9>();
      asm volatile("" : "+v"(sx), "+v"(sw));
      unsigned m = expo(amax(amax(amax(amax(0, sx[0]), sx[1]), sx[2]), sx[3])) | expo(amax(amax(amax(amax(0, sw[0]), sw[1]), sw[2]), sw[3])) << 16;
      m = amax(m, (unsigned)__builtin_amdgcn_update_dpp(0, (int)m, 0x128, 0xf, 0xf, false));
      m = amax(m, (unsigned)__builtin_amdgcn_update_dpp(0, (int)m, 0x124, 0xf, 0xf, false));
      m = amax(m, (unsigned)__builtin_amdgcn_update_dpp(0, (int)m, 0x122, 0xf, 0xf, false));
      m = amax(m, (unsigned)__builtin_amdgcn_update_dpp(0, (int)m, 0x121, 0xf, 0xf, false));
      unsigned ex = 28, ew = 28;
#pragma unroll
      for (int l = 0; l < 64; l += 16) {
        const unsigned v = (unsigned)__builtin_amdgcn_readlane((int)m, l);
        ex = max(ex + 2, v & 255u) - 2;
        ew = max(ew + 3, v >> 16) - 3;
      }
      const unsigned long long tg = __builtin_amdgcn_s_memrealtime();
      const unsigned lo = (unsigned)tg << 4 | xcc << 1, hi = ((unsigned)(tg >> 28) & 0xffffu) | min(ex, 220u) << 16 | min(ew, 220u) << 24;
      asm volatile("global_store_dwordx4 %0, %1, %2 sc1\n\ts_nop 1" :: "v"(threadIdx.x * 16), "v"(u32x4{lo, hi, ~lo, ~hi}), "s"(pb) : "memory");
      cx.tag[0] = lo;
      cx.tag[1] = hi;
    }
  }
  cx.smem = smem;
  cx.ws = ws;
  cx.out = (const char*)out;
  cx.gamma = gamma;
  cx.beta = beta;
  bf16x8 gb[8];
  unsigned fv;
  {
    RQ rq;
    if (wv < 4) run1<6>(cx, x, w, rq); else run1<5>(cx, x, w, rq);
    __builtin_amdgcn_s_barrier();
    asm volatile("s_cmp_gt_u32 %[wv], 3\n\ts_cbranch_scc1 .Lcf_1\n" LOOP0 ".Lcf_1:\n" LOOP1 ".Lcf_e:"
      :
        "+{v[72:75]}"(rq.h[0]), "+{v232}"(rq.v[0]), "+{v233}"(rq.v[1]), "+{v234}"(rq.v[2]),
        "+{v235}"(rq.v[3]), "+{v236}"(rq.v[4]), "+{v237}"(rq.v[5]), "+{v238}"(rq.v[6]), "+{v239}"(rq.v[7]), "+{v[196:199]}"(rq.h[1]),
        "+{v[200:203]}"(rq.h[2]), "+{v[204:207]}"(rq.h[3]), "+{v[96:97]}"(rq.raw[0]), "+{v[98:99]}"(rq.raw[1]), "+{v[100:101]}"(rq.raw[2]),
        "+{v[102:103]}"(rq.raw[3]), "+{v[104:105]}"(rq.raw[4]), "+{v[106:107]}"(rq.raw[5]), "+{v[108:109]}"(rq.raw[6]), "+{v[110:111]}"(rq.raw[7]),
        "+{v[112:113]}"(rq.raw[8]), "+{v[114:115]}"(rq.raw[9]), "+{v[116:117]}"(rq.raw[10]), "+{v241}"(rq.v[9]), "+{v243}"(rq.v[11]), "+{v244}"(rq.v[12]), "+{v252}"(rq.v[18]), "+{v[254:255]}"(rq.vb),
        "+{v[208:211]}"(rq.h[4]), "+{v[212:215]}"(rq.h[5]), "+{v[216:219]}"(rq.h[6]), "+{v[220:223]}"(rq.h[7]), "+{v[224:227]}"(rq.h[8]), "+{v[228:231]}"(rq.h[9]),
        "+{v248}"(rq.v[16]), "+{v249}"(rq.v[17]), "+{v246}"(rq.v[14]), "+{v247}"(rq.v[15])
      :
        "{v240}"(rq.v[8]), "{v242}"(rq.v[10]), "{v245}"(rq.v[13]),

            "{s44}"(rq.s[0]), "{s45}"(rq.s[1]),
        "{s46}"(rq.s[2]), "{s47}"(rq.s[3]), "{s48}"(rq.s[4]), "{s49}"(rq.s[5]), "{s50}"(rq.s[6]), "{s51}"(rq.s[7]), "{s52}"(rq.s[8]), "{s53}"(rq.s[9]),
        "{s54}"(rq.s[10]), "{s55}"(rq.s[11]), "{s56}"(rq.s[12]), "{s57}"(rq.s[13]), "{s58}"(rq.s[14]), "{s59}"(rq.s[15]), "{s60}"(rq.s[16]),
        "{s61}"(rq.s[17]), "{s62}"(rq.s[18]), "{s63}"(rq.s[19]), "{s64}"(rq.s[20]), "{s65}"(rq.s[21]), "{s66}"(rq.s[22]), "{s67}"(rq.s[23]),
        "{s68}"(rq.s[24]), "{s69}"(rq.s[25]), "{s70}"(rq.s[26]), "{s71}"(rq.s[27]), "{s72}"(rq.s[28]), "{s73}"(rq.s[29]), "{s74}"(rq.s[30]), "{s93}"(rq.s[31]),
        "{s[36:39]}"(cx.rs), "{s[40:43]}"(make_rsrc(cx.ws + WS_Y, ROWS * 65536)), "{s[76:77]}"(&TAB.G[0][0][0][0]), [wv] "s"(wv)
      : "memory", "vcc", "scc", "v118", "v119", CLOBBERS);
    fv = rq.v[17];
  }
  ws = cx.ws;
  const int bid = cx.vb, idx = bid >> 3;
  const int slot = (bid & 7) * 4 + (idx >> 3), b = (idx >> 2) & 1, nt = idx & 3;
  const int lane = (int)__builtin_amdgcn_mbcnt_hi(~0u, __builtin_amdgcn_mbcnt_lo(~0u, 0u));
  const int c64i = nt * 2 + ((bid >> 2) & 1), rg = (bid & 1) * 8 + (slot & 1) * 4 + (bid & 2) + (idx >> 4);
  const int hw = wv >> 2, wp = hw ? 7 - wv : wv, cg = c64i * 2 + hw;
  const int bs = 10 * rg;
  const int ntk = wp < 2 ? 3 : 2;
  const int g = lane >> 4, qq = (lane >> 2) & 3, pp = lane & 3, fr = lane & 15;
  const int ch0 = cg * 32 + 8 * g;
  const unsigned cnt = lds0 + 8 * 12288 + 3072 + hw * 8;
  if (wv == 2) asm volatile("ds_write_b32 %0, %1\n\ts_waitcnt lgkmcnt(0)" :: "v"(cnt + (lane & 3) * 4), "v"(0u) : "memory");
#pragma unroll
  for (int i = 0; i < 8; ++i) gb[i] = *reinterpret_cast<const bf16x8*>(smem + i * 1024 + lane * 16);
  const int be_ = fr < 4 ? 2 * wp + (fr >> 1) : 8 + wp;
  const int l0 = HOP * (bs + 2 * wp) + fr, l2 = HOP * (8 - wp), le = (fr >> 1) < ntk ? HOP * (bs + be_) + 48 + (fr & 1) : 1 << 20;
  const int lend = min(HOP * (bs + 10), L_);
  f32x4 nk, rk;
  float rn = 1.0f / (float)(lend - HOP * bs);
#pragma unroll
  for (int k = 0; k < 4; ++k) {
    nk[k] = (float)(2 * HOP + min(max(lend - HOP * (bs + k + 8), 0), HOP));
    rk[k] = 1.0f / nk[k];
  }
  asm volatile("" : "+v"(nk), "+v"(rk), "+v"(rn));
  f32x4 cin[2];
#pragma unroll
    for (int s = 0; s < 2; ++s) {
      cin[s] = *reinterpret_cast<const f32x4*>(smem + CBUF + min(be_, 10) * 272 + hw * 128 + g * 32 + s * 16);
#pragma unroll
      for (int r = 0; r < 4; ++r) cin[s][r] = fr & 1 ? __builtin_amdgcn_ldexpf(cin[s][r], cx.es) : 0.f;
    }
    asm volatile("" : "+v"(cin[0]), "+v"(cin[1]));
#pragma unroll
  for (int i = 0; i < 8; i += 4) asm volatile("" : "+v"(gb[i]), "+v"(gb[i + 1]), "+v"(gb[i + 2]), "+v"(gb[i + 3]));
  SB0;
  unsigned so[2], vo[5];
#pragma unroll
  for (int j = 0; j < 4; ++j) vo[j] = (unsigned)((lane >> 2) * 128 + (((lane & 3) ^ ((lane >> 3) & 2)) << 4) + j * 1024);
  vo[4] = (unsigned)((lane ^ ((lane >> 3) & 6)) << 4);
#pragma unroll
  for (int i = 0; i < 2; ++i) {
    const unsigned yr = (unsigned)(b * NBLK + bs + (i ? 8 + wp : 2 * wp));
    so[i] = i + 2 <= ntk ? (yr & ~1u) * 65536 + (yr & 1) * 64 + nt * 32768 + hw * 16384 + ((c64i ^ (yr >> 5)) & 1) * 8192 : 0xf0000000u;
  }
  asm volatile("" : "+s"(so[0]), "+s"(so[1]));
  asm volatile("s_waitcnt vmcnt(0)" : "+v"(fv), "+v"(vo[0]), "+v"(vo[1]), "+v"(vo[2]), "+v"(vo[3]), "+v"(vo[4]) :: "memory");
  if (wv == 0 && !__all(fv != 0u)) spin_flags(ws + WS_GRP + nt * 256 + b * 128 + ((bid & 7) + (bid & 4)) * 1024 + ((lane & 31) ^ ((lane & 31) == slot)) * 4);
  __builtin_amdgcn_s_barrier();
  char* my = smem + wv * 12288;
  if (1) { if (1 & 4 && !hw) __builtin_amdgcn_s_setprio(3); else __builtin_amdgcn_s_setprio(2); }
  if (24 && hw) __builtin_amdgcn_s_sleep(24);
  if (12 && (nt & 1)) __builtin_amdgcn_s_sleep(12);
  if (0 && b) __builtin_amdgcn_s_sleep(0);
  {
    const u32x4 rs = make_rsrc(ws + WS_Y, ROWS * 65536);
#pragma unroll
    for (int i = 0; i < 3; ++i)
      asm volatile("s_mov_b32 m0, %0\n\ts_nop 0\n\tbuffer_load_dwordx4 %1, %2, %3 offen sc1 lds\n\tbuffer_load_dwordx4 %4, %2, %3 offen offset:1024 sc1 lds\n\t"
                   "buffer_load_dwordx4 %5, %2, %3 offen offset:2048 sc1 lds\n\tbuffer_load_dwordx4 %6, %2, %3 offen offset:3072 sc1 lds"
                   :: "s"(lds0 + wv * 12288 + i * 4096), "v"(vo[i < 2 ? 4 : 0]), "s"(rs), "s"(i < 2 ? so[0] + i * 4096 : so[1]), "v"(vo[i < 2 ? 4 : 1]), "v"(vo[i < 2 ? 4 : 2]),
                      "v"(vo[i < 2 ? 4 : 3]) : "memory");
  }
  f32x4 y[24], npv[2];
  float s1[8], s2[8];
  {
    bf16x8 a[12];
    unsigned sp = wv * 12288 + (4 * g + qq) * 128 + (((2 * g + (qq >> 1)) & 3) << 5) + pp * 8, st = wv * 12288 + 8192 + (4 * g + qq) * 64 + ((g & 1) << 5) + pp * 8;
    auto rd = [&](int q) __attribute__((always_inline)) {
      const char* p = smem + (q < 8 ? (sp ^ (unsigned)((q & 6) << 4)) + (q & 1) * 4096 : (st ^ (unsigned)((q & 2) << 4)) + (q & 1) * 2048);
      a[q] = cat(tr_read(p), tr_read(p + (q < 8 ? 2048 : 1024)));
    };
    auto sums = [&](int cp, int q) __attribute__((always_inline)) {
      const int e = (cp & 4) + q;
      if ((cp & 3) == 3) {
        if (cp >= 8) y[cp & 7][q] = (fr >> 1) == (cp >> 3) ? y[cp][q] : y[cp & 7][q];
        return;
      }
      float d = y[cp][q];
      if (cp >= 16 && l0 + l2 + 16 * (cp & 3) >= lend) d = 0.f;
      if (cp == 0 || cp == 4) { s1[e] = d; s2[e] = d * d; }
      else { s1[e] += d; s2[e] = __builtin_fmaf(d, d, s2[e]); }
      asm volatile("" : "+v"(s1[e]), "+v"(s2[e]));
    };
    unsigned sv = wv * 12288 + g * 8;
    asm volatile("" : "+v"(sp), "+v"(st), "+v"(sv));
    wait_vm<8>();
    SB0;
    u32x2 pv[2];
#pragma unroll
    for (int s = 0; s < 2; ++s) pv[s] = *reinterpret_cast<const u32x2*>(smem + sv + s * 32);
    SB0;
#pragma unroll
    for (int q = 0; q < 8; q += 2) { rd(q); SB0; }
#pragma unroll
    for (int s = 0; s < 2; ++s) {
      npv[s] = f32x4{bf_lo(pv[s][0]), bf_hi(pv[s][0]), bf_lo(pv[s][1]), bf_hi(pv[s][1])} * (-1.0f / 64.0f);
      cin[s] += npv[s];
    }
    SB0;
#pragma unroll
    for (int c = 0; c < 16; ++c) {
      if (c == 8) wait_vm<4>();
      y[c] = MFMA(a[2 * (c >> 2)], gb[2 * (c & 3)], (c & 3) == 3 ? cin[(c >> 2) & 1] : npv[(c >> 2) & 1]);
      SB0;
      if (c >= 8 && c < 12) rd(2 * (c - 8) + 1);
      SB0;
    }
    auto chain = [&](auto cc) __attribute__((always_inline)) {
      constexpr int c = cc.v, is = c >> 2, tt = c & 3;
      if (c == 14) wait_vm<0>();
#pragma unroll
      for (int q = 0; q < 4; ++q) {
        if (c < 24 && q == (c < 16 ? 2 : q & 2)) y[c % 24] = MFMA(a[(2 * is + (q >> 1)) % 12], gb[2 * tt + (q >> 1)], q ? y[c % 24] : tt == 3 ? cin[is & 1] : npv[is & 1]);
        SB0;
        if (c == 14 || c == 15) { if (q & 1) rd(8 + 2 * (c - 14) + (q >> 1)); }
        if (c >= 2) sums(c - 2, q);
        SB0;
      }
    };
    sfor<0, 16>(chain);
    if (ntk == 3) {
      sfor<16, 26>(chain);
    } else {
#pragma unroll
      for (int q = 0; q < 8; ++q) sums(14 + (q >> 2), q & 3);
    }
#pragma unroll
    for (int e = 0; e < 8; ++e) {
      const float d = le < lend ? y[3 + (e & 4)][e & 3] : 0.f;
      s1[e] += d;
      s2[e] = __builtin_fmaf(d, d, s2[e]);
    }
  }
  float* red = reinterpret_cast<float*>(smem + 8 * 12288);
  {
    float v[8];
#pragma unroll
    for (int j = 0; j < 8; ++j) v[j] = bfly<0x140>(s1[j], s2[j], fr & 8);
#pragma unroll
    for (int j = 0; j < 4; ++j) v[j] = bfly<0x141>(v[j], v[j + 4], fr & 4);
#pragma unroll
    for (int j = 0; j < 2; ++j) v[j] = bfly<0x4E>(v[j], v[j + 2], fr & 2);
    red[((fr >> 3) * 8 + 2 * wp + hw) * 32 + 8 * g + (fr & 7)] = bfly<0xB1>(v[0], v[1], fr & 1);
    if (fr < 2) *reinterpret_cast<f32x4*>(red + (16 + 2 * wp + hw) * 32 + 8 * g + 4 * fr) = fr ? npv[1] : npv[0];
  }
  unsigned arr = 1u;
  if (lane == 0) asm volatile("ds_add_rtn_u32 %0, %1, %0\n\ts_waitcnt lgkmcnt(0)" : "+v"(arr) : "v"(cnt) : "memory");
  unsigned long long* stats = reinterpret_cast<unsigned long long*>(ws + WS_STATS);
  if (__builtin_amdgcn_readfirstlane(arr) == 3u && lane < 32) {
    const int tid = hw * 32 + lane;
    float mk[4], sm = 0.f, m2 = 0.f;
#pragma unroll
    for (int k = 0; k < 4; ++k) {
      const float* e = red + 64 * k + tid;
      const float dm = e[0] * rk[k];
      mk[k] = dm - e[512];
      m2 += fmaxf(e[256] - e[0] * dm, 0.f);
      sm += nk[k] * mk[k];
    }
    const float mean = sm * rn;
#pragma unroll
    for (int k = 0; k < 4; ++k) m2 += nk[k] * (mk[k] - mean) * (mk[k] - mean);
    unsigned long long pk = (unsigned long long)__float_as_uint(mean) | ((unsigned long long)__float_as_uint(m2) << 32);
    if (pk == ~0ull) pk = ~1ull;
    __hip_atomic_store((gu64*)(stats + (b * 16 + rg + (rg & 12) + (c64i & 1) * 4 + b * 16) * 256 + nt * 64 + tid), pk, RLX_AGENT);
  }
  constexpr float GS = -0.4686686930532063f, GA = -2.0f * 0.7978845608028654f * 1.4426950408889634f / GS;
  const unsigned short* gp = reinterpret_cast<const unsigned short*>(smem + GAM + hw * 64) + (lane & 31);
  float gs = bf_lo(gp[0]) * GS, bt = bf_lo(gp[64]) * GS, eps = fmaxf(__builtin_amdgcn_ldexpf(1e-5f, 2 * cx.es), 1e-30f);
  float cl = lane < 32 ? 0.f : -15.f / 500.f;
  asm volatile("" : "+v"(gs), "+v"(bt), "+v"(eps), "+v"(cl));
  unsigned long long* gl = reinterpret_cast<unsigned long long*>(smem + 8 * 12288 + 4096);
  {
    unsigned long long pk[2];
    const int row = 4 * wp + (lane >> 5), col = hw * 32 + (lane & 31);
    for (unsigned spins = 0;; ++spins) {
#pragma unroll
      for (int r = 0; r < 2; ++r)
        pk[r] = __hip_atomic_load((gu64*)(stats + (b * 32 + row + 2 * r + 4 * wp + (c64i & 1) * 4) * 256 + nt * 64 + col), RLX_AGENT);
      if (__all(pk[0] != ~0ull && pk[1] != ~0ull) || spins > (1u << 20)) break;
      if (1) __builtin_amdgcn_s_sleep(1);
    }
    gl[row * 66 + col] = pk[0];
    gl[(row + 2) * 66 + col] = pk[1];
    if (lane == 0) asm volatile("ds_add_u32 %0, %1" :: "v"(cnt + 4), "v"(1u) : "memory");
    unsigned c;
    do asm volatile("ds_read_b32 %0, %1\n\ts_waitcnt lgkmcnt(0)" : "=v"(c) : "v"(cnt + 4) : "memory");
    while (__builtin_amdgcn_readfirstlane(c) < 4u);
  }
  f32x4 ga[2], be[2];
  {
    const f32x2* gr = reinterpret_cast<const f32x2*>(gl + hw * 32 + (lane & 31));
    const f32x2 p = {gr[0][0], 0.f};
    f32x2 d, sm = {0.f, 0.f};
    float q = 0.f;
#pragma unroll
    for (int k = 0; k < 8; ++k) {
      d = gr[((lane >> 5) * 8 + k) * 66] - p;
      sm += d;
      q = __builtin_fmaf(d[0], d[0], q);
    }
    const float dl = cl * d[0];
    const float dm = halves_sum(sm[0] + dl) * (500.f / L_), var = halves_sum(sm[1] + 500.f * __builtin_fmaf(dl, d[0], q)) * (1.f / L_) - dm * dm;
    gs *= __builtin_amdgcn_rsqf(fmaxf(var, 0.f) + eps);
    float* am = reinterpret_cast<float*>(my);
    am[lane] = lane < 32 ? gs : bt - (p[0] + dm) * gs;
#pragma unroll
    for (int s = 0; s < 2; ++s) {
      ga[s] = *reinterpret_cast<const f32x4*>(am + 8 * g + 4 * s);
      be[s] = *reinterpret_cast<const f32x4*>(am + 32 + 8 * g + 4 * s) - npv[s] * ga[s];
    }
  }
  {
    if (1) __builtin_amdgcn_s_setprio(0);
    f32x4 k1 = {GA, GA, GA, GA}, k2 = {GS, GS, GS, GS};
    asm volatile("" : "+v"(k1), "+v"(k2));
    const u32x4 ro = make_rsrc(out + (size_t)b * L_ * D_, lend * (D_ * 2));
    const unsigned oo = (unsigned)((l0 * D_ + ch0) * 2);
#pragma unroll
    for (int i = 0; i < 3; ++i)
      if (i < 2 || ntk == 3)
#pragma unroll
      for (int tt = 0; tt < 4; ++tt) {
        if (tt == 3 && i) break;
        f32x4 o[2];
#pragma unroll
        for (int s = 0; s < 2; ++s) {
          const f32x4 z = y[8 * i + 4 * s + tt] * ga[s] + be[s];
          const f32x4 a = z * (z * z + k1);
          f32x4 e;
#pragma unroll
          for (int r = 0; r < 4; ++r) e[r] = __builtin_amdgcn_exp2f(a[r]);
          e = e * k2 + k2;
#pragma unroll
          for (int r = 0; r < 4; ++r) e[r] = __builtin_amdgcn_rcpf(e[r]);
          o[s] = z * e;
        }
        st16(pack8(o[0], o[1]), ro, tt < 3 ? oo : (unsigned)((le * D_ + ch0) * 2), tt < 3 ? ((i < 2 ? HOP * i : l2) + 16 * tt) * (D_ * 2) : 0);
      }
  }
  asm volatile(".Lcf_b:\n\ts_nop 0" ::: "memory");
}

extern "C" void kernel_launch(void* const* d_in, const int* in_sizes, int n_in,
                              void* d_out, int out_size, void* d_ws, size_t ws_size,
                              hipStream_t stream) {
  const unsigned short* x = (const unsigned short*)d_in[0];
  const unsigned short* w = (const unsigned short*)d_in[1];
  const unsigned short* gamma = (const unsigned short*)d_in[2];
  const unsigned short* beta = (const unsigned short*)d_in[3];
  char* ws = (char*)(((uintptr_t)d_ws + (2u << 20) - 1) & ~(uintptr_t)((2u << 20) - 1));
  conv_fft<<<256, 512, 0, stream>>>(ws, x, w, gamma, beta, (unsigned short*)d_out);
}
```

```cpp
#include <hip/hip_runtime.h>
#include <stdint.h>

typedef __attribute__((ext_vector_type(8))) short bf16x8;
typedef __attribute__((ext_vector_type(4))) short s16x4;
typedef __attribute__((ext_vector_type(4))) float f32x4;
typedef __attribute__((ext_vector_type(2))) float f32x2;
typedef __attribute__((ext_vector_type(2))) __bf16 bf16x2v;
typedef __attribute__((ext_vector_type(4))) unsigned int u32x4;
typedef __attribute__((ext_vector_type(2))) unsigned int u32x2;
constexpr int NH = 10;
typedef __attribute__((ext_vector_type(8))) unsigned int u32x8;
typedef __attribute__((ext_vector_type(16))) unsigned int u32x16;
typedef __attribute__((ext_vector_type(8))) _Float16 f16x8;
typedef __attribute__((ext_vector_type(2))) _Float16 f16x2;
typedef __attribute__((ext_vector_type(2))) unsigned short u16x2;
typedef __attribute__((address_space(1))) unsigned long long gu64;
typedef __attribute__((address_space(1))) unsigned gu32;
typedef __attribute__((address_space(3))) void lds_void;
typedef __attribute__((address_space(3))) s16x4 lds_s16x4;

constexpr unsigned long long bytes(unsigned a, unsigned b = 0, unsigned c = 0, unsigned d = 0, unsigned e = 0, unsigned f = 0, unsigned g = 0, unsigned h = 0) {
  return a | b << 8 | c << 16 | d << 24 | (unsigned long long)(e | f << 8 | g << 16 | h << 24) << 32;
}
constexpr int T_ = 16000, D_ = 512, L_ = 7985;
constexpr int HOP = 50, NBLK = 160, ROWS = 2 * NBLK;

constexpr unsigned WS_S = 0;
constexpr unsigned SBLK = 4u << 20;
constexpr unsigned sbase(unsigned sl) { return sl * 131072 + (sl >> 4) * 8192; }
constexpr unsigned FLG = 32u * SBLK;
constexpr unsigned WS_GRP = WS_S + FLG + 16 * 8192;
constexpr unsigned WS_D = WS_S + FLG + 20 * 8192;
constexpr unsigned WS_L = WS_D + ROWS * 1024;
constexpr unsigned WS_Y = 40u << 20;
constexpr unsigned WS_STATS = 129u << 20;
constexpr unsigned WS_FA = WS_STATS + (1u << 17);
constexpr unsigned WS_PUB = WS_STATS + (1u << 19);
static_assert(WS_L + D_ * D_ * 2 <= WS_STATS && FLG % (16 * 8192) == 0, "workspace layout");

#define RLX_AGENT __ATOMIC_RELAXED, __HIP_MEMORY_SCOPE_AGENT

constexpr double COS64[17] = {1.0, 0.99518472667219688624, 0.98078528040323044913, 0.95694033573220886494,
    0.92387953251128675613, 0.88192126434835502971, 0.83146961230254523708, 0.77301045336273696081,
    0.70710678118654752440, 0.63439328416364549822, 0.55557023301960222474, 0.47139673682599764856,
    0.38268343236508977173, 0.29028467725446236764, 0.19509032201612826785, 0.09801714032956060199, 0.0};
constexpr double c64(int k) { k &= 63; if (k > 32) k = 64 - k; return k <= 16 ? COS64[k] : -COS64[32 - k]; }
constexpr double s64(int k) { return c64(k + 48); }
constexpr unsigned short bf_rne(double v) {
  const unsigned u = __builtin_bit_cast(unsigned, (float)v);
  return (unsigned short)((u + 0x7fffu + ((u >> 16) & 1u)) >> 16);
}
constexpr double R3 = 0.86602540378443864676;
constexpr double dft(int c, int t, double sgn) {
  return (c >> 1) == 0 ? ((c & t) & 1 ? -1.0 : 1.0)
                       : ((c & 1) ? -0.5 * c64((c >> 1) * t) - R3 * sgn * s64((c >> 1) * t) : c64((c >> 1) * t));
}
constexpr double idft(int c, int t) {
  return (c >> 1) == 0 ? ((c & t) & 1 ? -1.0 : 1.0) / 64.0
                       : ((c & 1) ? -s64((c >> 1) * t) / R3 - c64((c >> 1) * t) * (2.0 / 3.0) : c64((c >> 1) * t) * (4.0 / 3.0)) / 32.0;
}
constexpr int kidx(int g, int j) { return j < 4 ? 4 * g + j : 12 + 4 * g + j; }
struct Tables {
  unsigned short G[4][2][64][8];
  unsigned short F[4][2][64][8];
  unsigned short V[4][64][4];
};
constexpr Tables make_tables() {
  Tables tb{};
  for (int l = 0; l < 64; ++l)
    for (int j = 0; j < 8; ++j)
      for (int t = 0; t < 4; ++t) {
        const int g = l >> 4, n = l & 15;
        for (int ks = 0; ks < 2; ++ks) {
          tb.F[t][ks][l][j] = bf_rne(dft(16 * t + n, 32 * ks + kidx(g, j), -1.0));
          tb.G[t][ks][l][j] = bf_rne(idft(32 * ks + kidx(g, j), 16 * t + (t < 3 ? n : n & 1)));
        }
        tb.V[t][l][j & 3] = bf_rne(dft(16 * t + n, 4 * g + (j & 3), 1.0));
      }
  return tb;
}
__device__ const __attribute__((aligned(16))) Tables TAB = make_tables();

__device__ __forceinline__ unsigned pk_bf16(float lo, float hi) {
  return __builtin_bit_cast(unsigned, __builtin_convertvector(f32x2{lo, hi}, bf16x2v));
}
__device__ __forceinline__ float bf_lo(unsigned v) { return __uint_as_float(v << 16); }
__device__ __forceinline__ float bf_hi(unsigned v) { return __uint_as_float(v & 0xffff0000u); }
__device__ __forceinline__ u32x4 pack8(f32x4 a, f32x4 b) {
  return u32x4{pk_bf16(a[0], a[1]), pk_bf16(a[2], a[3]), pk_bf16(b[0], b[1]), pk_bf16(b[2], b[3])};
}
__device__ __forceinline__ unsigned pk_f16(float lo, float hi) {
  return (__builtin_bit_cast(unsigned, __builtin_convertvector(f32x2{lo, hi}, f16x2)) + 0x40004u) & 0xfff8fff8u;
}
__device__ __forceinline__ u32x4 pack8h(f32x4 a, f32x4 b) {
  return u32x4{pk_f16(a[0], a[1]), pk_f16(a[2], a[3]), pk_f16(b[0], b[1]), pk_f16(b[2], b[3])};
}
__device__ __forceinline__ unsigned add_h(const bf16x8& a, const bf16x8& b, int d) {
  const unsigned x = __builtin_bit_cast(u32x4, a)[d], y = __builtin_bit_cast(u32x4, b)[d];
  unsigned r = __builtin_bit_cast(unsigned, __builtin_bit_cast(f16x2, x) + __builtin_bit_cast(f16x2, y));
  return r;
}
__device__ __forceinline__ unsigned amax(unsigned m, unsigned v) {
  return __builtin_bit_cast(unsigned, __builtin_elementwise_max(__builtin_bit_cast(u16x2, m), __builtin_bit_cast(u16x2, v & 0x7fff7fffu)));
}
struct Rng { f16x2 hi = {0, 0}, lo = {0, 0}; };
__device__ __forceinline__ void widen(Rng& r, unsigned a, unsigned b) {
  const f16x2 x = __builtin_bit_cast(f16x2, a), y = __builtin_bit_cast(f16x2, b);
  r.hi = __builtin_elementwise_maximum(__builtin_elementwise_maximum(r.hi, x), y);
  r.lo = __builtin_elementwise_minimum(__builtin_elementwise_minimum(r.lo, x), y);
}
__device__ __forceinline__ unsigned amax(const Rng& r) { return amax(amax(0, __builtin_bit_cast(unsigned, r.hi)), __builtin_bit_cast(unsigned, r.lo)); }
__device__ __forceinline__ unsigned expo(unsigned m) { return max(m & 0xffffu, m >> 16) >> 7; }
__device__ __forceinline__ unsigned wave_max(unsigned v) {
  unsigned m = 0;
#pragma unroll 1
  for (int l = 0; l < 64; ++l) m = max(m, (unsigned)__builtin_amdgcn_readlane((int)v, l));
  return m;
}
__device__ __forceinline__ unsigned scale2(unsigned u, int e) {
  return __builtin_amdgcn_perm(__float_as_uint(__builtin_amdgcn_ldexpf(bf_hi(u), e)), __float_as_uint(__builtin_amdgcn_ldexpf(bf_lo(u), e)), 0x07060302u);
}
__device__ __forceinline__ void restart(unsigned id) {
  asm volatile("s_waitcnt vmcnt(0)\n\ts_barrier\n\ts_branch conv_fft" :: "{s[0:1]}"(__builtin_amdgcn_kernarg_segment_ptr()), "{s2}"(id), "{v0}"(threadIdx.x) : "memory");
}
__device__ __forceinline__ s16x4 tr_read(const char* p) {
  return __builtin_amdgcn_ds_read_tr16_b64_v4i16((lds_s16x4*)p);
}
__device__ __forceinline__ bf16x8 cat(s16x4 a, s16x4 b) {
  const u32x2 x = __builtin_bit_cast(u32x2, a), y = __builtin_bit_cast(u32x2, b);
  return __builtin_bit_cast(bf16x8, u32x4{x[0], x[1], y[0], y[1]});
}
#define MFMA(a, b, c) __builtin_amdgcn_mfma_f32_16x16x32_bf16(a, b, c, 0, 0, 0)
#define MFMS(a, b) __builtin_amdgcn_mfma_f32_16x16x16bf16_1k(a, __builtin_bit_cast(s16x4, b), ZERO4, 0, 0, 0)
#define SB0 __builtin_amdgcn_sched_barrier(0)
#define ZERO4 (f32x4{0.f, 0.f, 0.f, 0.f})
#define DMA16(POL, rs, lds, voff, soff) \
  asm volatile("s_mov_b32 m0, %0\n\ts_nop 0\n\tbuffer_load_dwordx4 %1, %2, %3 offen " POL " lds" \
               :: "s"(lds), "v"(voff), "s"(rs), "s"(soff) : "memory")
__device__ __forceinline__ void st16(const u32x4& v, const u32x4& rs, unsigned voff, unsigned soff) {
  asm volatile("buffer_store_dwordx4 %0, %1, %2, %3 offen sc1\n\ts_nop 1" :: "v"(v), "v"(voff), "s"(rs), "s"(soff) : "memory");
}
template <int N> __device__ __forceinline__ void wait_vm() { asm volatile("s_waitcnt vmcnt(%0)" :: "i"(N) : "memory"); }
__device__ __forceinline__ u32x4 make_rsrc(const void* p, unsigned bytes) {
  const unsigned long long a = (unsigned long long)p;
  return u32x4{(unsigned)a, (unsigned)(a >> 32) & 0xffffu, bytes, 0x00020000u};
}
__device__ __forceinline__ void spin_flags(const char* p) {
  for (unsigned spins = 0; !__all(__hip_atomic_load((const gu32*)p, RLX_AGENT) != 0u) && spins < (1u << 21); ++spins)
    __builtin_amdgcn_s_sleep(3);
}

constexpr int GW = 10;
constexpr int NQ1 = GW / 2, NQW = 16 - NQ1;
constexpr int WSL = 16384, XSL = 20480, XRING = 3 * WSL;
constexpr int RAWB = 67 * 1024, XT1 = RAWB + NQ1 * 4096, SINK = XT1 + RAWB;
constexpr int RAWL = XRING + 3 * XSL;
constexpr int CORR = RAWL + 4096, CORD = RAWL + NQW * 4096;
constexpr int GAM = RAWL + 52224, CBUF = RAWL + 49152;

struct Ctx {
  char* smem;
  char* ws;
  const char* out;
  const unsigned short *gamma, *beta;
  u32x4 rs;
  unsigned ldg, ldf, sg, sf;
  unsigned voff, voff5, crd;
  unsigned xbase, wbase;
  unsigned pwr, prd, poff;
  unsigned xo;
  unsigned lane4, fvo;
  unsigned id;
  int es;
  int wv, vb;
  unsigned long long pc;
  unsigned pfo;
};
__device__ __forceinline__ void code_touch(const Ctx& cx, int h0 = 0, int h1 = 2) {
  if (((cx.wv ^ (cx.vb >> 3)) & 3) | (cx.vb & (h0 || h1 == 1 ? 0 : 0))) return;
#pragma unroll
  for (int h = h0; h < h1; ++h)
    asm volatile("s_mov_b32 m0, %0\n\ts_nop 4\n\tglobal_load_lds_dword %1, %2" :: "s"(SINK + cx.wv * 256), "v"(min(__builtin_amdgcn_mbcnt_hi(~0u, __builtin_amdgcn_mbcnt_lo(~0u, cx.wv * 64u + h * 512u)) * 128u, cx.pfo)), "s"(cx.pc) : "memory");
}
__device__ __forceinline__ void x_store(const Ctx& cx, const u32x4& v, unsigned kb, unsigned blk) {
  st16(v, cx.rs, cx.xo, kb + (blk + (blk & 192u)) * 128u);
}
template <int NM>
__device__ __forceinline__ void stage_issue(const Ctx& cx, int kc) {
  const unsigned slot = (unsigned)kc * (NM == 6 ? WSL : XSL);
  asm volatile("s_mov_b32 m0, %0\n\ts_nop 0\n\tbuffer_load_dwordx4 %1, %2, %3 offen sc1 lds\n\tbuffer_load_dwordx4 %1, %2, %3 offen offset:1024 sc1 lds\n\t"
                 "buffer_load_dwordx4 %1, %2, %3 offen offset:2048 sc1 lds\n\tbuffer_load_dwordx4 %1, %2, %3 offen offset:3072 sc1 lds"
                 :: "s"(cx.ldg + slot), "v"(cx.voff), "s"(cx.rs), "s"(cx.sg + kc * SBLK) : "memory");
  if (NM == 5) DMA16("sc1", cx.rs, cx.ldf + slot, cx.voff, cx.sf + kc * SBLK);
}
template <int V> struct IC { static constexpr int v = V; constexpr operator int() const { return V; } };
template <int A, int B, class F> __device__ __forceinline__ void sfor(F f) {
  if constexpr (A < B) { f(IC<A>{}); sfor<A + 1, B>(f); }
}
struct RQ { u32x4 h[NH]; u32x2 raw[NQW]; u32x2 vb; unsigned s[32], v[19]; };

__device__ __forceinline__ int run_bs(int vb, int t) { return 5 * ((vb >> 4) + 16 * ((vb ^ t) & 1)); }
__device__ __forceinline__ int run_q(int vb, int t) { return (((vb >> 1) & 1) + ((vb >> 2) & 2) + 2 * t) & 3; }
__device__ __forceinline__ unsigned x_addr(int vb, int t, int wv) {
  const int lane = threadIdx.x & 63, q = run_q(vb, t);
  const unsigned c = (unsigned)((lane & 15) ^ (((4 * wv + (lane >> 4)) & 7) << 1));
  return (unsigned)(2 * (HOP * run_bs(vb, t) + 4 * wv + (lane >> 4)) + ((t ^ q ^ (q >> 1)) & 1)) * 1024u + c * 16 + q * 256;
}
__device__ __forceinline__ u32x4 x_rsrc(const unsigned short* x, int vb) { return make_rsrc(x + (size_t)((vb >> 2) & 1) * T_ * D_, T_ * D_ * 2); }
__device__ __forceinline__ void issue_x(int t, int wv, unsigned a, const u32x4& rx, unsigned lds0, int j0 = 0, int j1 = 9) {
#pragma unroll
  for (int jj = 0; jj < 9; ++jj)
    if (jj >= j0 && jj < j1 && (jj < 8 || wv < 3)) DMA16("nt", rx, lds0 + t * XT1 + wv * 1024 + jj * 8192, a, jj * 65536u);
}
struct XR { s16x4 t[8]; };
constexpr int P0 = 3, P1 = 23;
constexpr int PG = 20, NFA = 1;
constexpr int TE = 4;
constexpr int NIT = 23, NS = 30 - NH, NA = 7, NB = 13;
constexpr int ord(int p) { return p < 0 || p >= NIT ? -1 : p == 0 ? 0 : p < 6 ? p + 2 : p < 8 ? p - 5 : p; }
constexpr int st_w(int j) { return j < 2 ? j : j < 7 ? -1 : j < 15 ? j - 5 : -1; }
constexpr int st_x(int j) { return j < 7 ? j - 2 : j - 10; }
constexpr int st_pos(int j) { return st_w(j) >= 0 ? (j < 9 ? 0 : j < 13 ? 6 : 7) : j < 7 ? j - 1 : j - 7; }
constexpr int st_first(int p) {
  int j = p <= P0 ? 0 : p >= P1 ? NS : (p - P0) * NS / (P1 - P0);
  while (j > 0 && p <= NIT && st_pos(j - 1) + 2 > p - 1) --j;
  return j;
}
template <int NM, class F>
__device__ __forceinline__ void phase1(Ctx& cx, const unsigned short* __restrict__ x, const unsigned short* __restrict__ w,
                                       RQ& rqv, unsigned& xs, u32x2& vt, F setup) {
  u32x2 rq[NQW];
  static_assert(NQ1 == 5, "phase-1 wait counts assume five quanta");
  char* smem = cx.smem;
  const unsigned lds0 = (unsigned)(uintptr_t)(lds_void*)smem;
  const int idx = cx.vb >> 3, wv = cx.wv, lane = threadIdx.x & 63;
  const u32x4 rw = make_rsrc(w, 31 * D_ * D_ * 2);
  const int tap = threadIdx.x >> 5, pc = threadIdx.x & 31;
  const unsigned roff = (unsigned)(tap * (2 * D_ * D_ * 2) + pc * 8);
  const int ng = wv & 3, mg = wv >> 2;
  const int g = lane >> 4, qq = (lane >> 2) & 3, pp = lane & 3;
  u32x2 r1[NQ1];
  u32x4 fb[2];
  unsigned long long tag;
  const char* rec = cx.ws + WS_PUB + ((cx.id & 7) * 8 + wv) * 16;
  u32x4 pr, wl;
  {
    const int vb = cx.vb;
    const int kr = (vb >> 7) * 32 + (vb & 4) * 4 + (idx & 15);
    auto raw_load = [&](u32x2& r, int q) {
      const unsigned so = (unsigned)(kr + 64 * (2 * (q & 3) + ((q >> 2) & 1)) + 512 * (((q >> 3) ^ q ^ (q >> 1)) & 1)) * 1024u + (unsigned)((vb + q) & 3) * 256u;
      asm volatile("buffer_load_dwordx2 %0, %1, %2, %3 offen nt" : "=v"(r) : "v"(roff), "s"(rw), "s"(so) : "memory");
    };
#pragma unroll
    for (int k = 0; k < NQ1; ++k) raw_load(r1[k], k);
    const unsigned xa1 = x_addr(vb, 1, wv);
    issue_x(1, wv, xa1, x_rsrc(x, vb), lds0, 0, TE);
    {
#pragma unroll
      for (int i = 0; i < 2; ++i) asm volatile("global_load_dwordx4 %0, %1, off" : "=v"(fb[i]) : "v"(TAB.F[ng][i][lane]) : "memory");
      asm volatile("global_load_dwordx2 %0, %1, off" : "=v"(vt) : "v"(TAB.V[ng][lane]) : "memory");
      asm volatile("global_load_dwordx4 %0, %1, %2 sc1" : "=v"(pr) : "v"(0u), "s"(rec) : "memory");
    }
    if (NM == 5 && wv >= 6)
      asm volatile("buffer_load_dwordx4 %0, %1, %2, %3 offen nt" : "=v"(wl) : "v"((threadIdx.x & 127) * 16), "s"(rw), "s"(30 * D_ * D_ * 2 + vb * 2048) : "memory");
    if (cx.id == 0 && wv >= TE - 1)
      DMA16("nt", x_rsrc(x, vb), lds0 + XT1 + (wv + 1) * 8192, x_addr(vb, 1, 0), (wv + 1) * 65536u);
    if (NM == 5 || (cx.id | wv)) issue_x(1, wv, xa1, x_rsrc(x, vb), lds0, TE, 9);
#pragma unroll
    for (int k = 0; k < NQW; ++k) raw_load(rq[k], NQ1 + k);
    if (1 == 1 && cx.id) code_touch(cx, 1, 2);
    if (1 && NM == 5 && wv == 5) {
      const char* p = lane < 2 ? cx.ws + (idx + 32 * lane) * 2097152u + 4096 : cx.out + ((lane - 2) * 32 + idx) * (511040 / 1 & ~127);
      asm volatile("s_mov_b32 m0, %0\n\ts_mov_b64 exec, %2\n\tglobal_load_lds_dword %1, off nt\n\ts_mov_b64 exec, -1" :: "s"(SINK + 5 * 256), "v"(p + (vb & 7) * 128), "s"(~0ull >> (64 - 2 - 1)) : "memory");
    }
    {
      const int bid = vb, xb = (bid >> 2) & 1, krow = kr;
      const int slot = (bid & 7) * 4 + (idx >> 3), b = (idx >> 2) & 1, nt = idx & 3;
      const int rt = 4 * g + qq;
      cx.lane4 = lane * 4;
      cx.fvo = NM == 5 ? (unsigned)((bid & 7) * 1024 + ng * 256 + lane * 4) : wv == 0 && lane < 8 ? (unsigned)(lane * 1024 + (bid >> 2) * 4) : 0x80000000u;
      cx.pwr = (unsigned)(RAWB + tap * 256 + (((pc >> 2) ^ (tap & 7)) << 5) + (pc & 3) * 8);
      cx.prd = (unsigned)(RAWB + rt * 256 + (((4 * mg) ^ (rt & 7)) << 5) + pp * 8);
      cx.poff = (unsigned)(sbase(8 * ng + ((lane >> 1) & 7)) + (krow >> 5) * SBLK + (krow & 24) * 512 + (krow & 7) * 128 +
                           mg * 2048 + (lane & 1) * 64 + g * 16);
      cx.xo = (unsigned)(sbase(8 * ng + ((lane >> 1) & 7)) + mg * SBLK + 65536 + xb * 8192 + (lane & 1) * 64 + g * 16);
      cx.xbase = (unsigned)(XRING + mg * 5 * 2048 + (lane & 8) * 128 + (lane & 7) * 64 + ((g ^ ((lane >> 1) & 2)) << 4));
      cx.wbase = (unsigned)((rt >> 3) * 4096 + ng * 1024 + (rt & 7) * 64 + (rt & 4) * 8 + pp * 8);
      const int pat = ((lane >> 2) & 7) * 128 + (lane >> 5) * 64 + (((lane & 3) ^ ((lane >> 3) & 2)) << 4);
      if (NM == 6) {
        cx.voff = (unsigned)(sbase(slot) + nt * 16384 + ng * 4096 + pat);
        cx.ldg = lds0 + ng * 4096;
        cx.ldf = lds0 + CORR + ng * 1024;
        cx.sg = 0;
        cx.sf = 0;
        const int kr8 = lane >> 3;
        cx.voff5 = (unsigned)(WS_L - WS_S + (8 * ng + kr8) * 1024 + (nt * 2 + xb) * 128 + (((lane & 7) ^ ((kr8 & 2) | (ng & 1) << 2)) << 4));
        cx.crd = (unsigned)(CORR + (8 * g + qq) * 128 + ((ng ^ ((qq >> 1) | (g & 1) << 1)) << 5) + pp * 8);
      } else {
        const int rg = (bid & 1) * 8 + (slot & 1) * 4 + (bid & 2) + (idx >> 4);
        cx.voff = (unsigned)(sbase(slot) + 65536 + b * 8192 + pat);
        cx.ldg = lds0 + XRING + (ng + (ng >> 1)) * 4096;
        cx.ldf = lds0 + XRING + 8192 + ng * 1024;
        cx.sg = (ng + (ng >> 1) * 3 + ((ng + 1) >> 2) * 2) * 4096;
        cx.sf = 16384 + ng * 1024;
        cx.voff5 = (unsigned)(WS_D - WS_S + (b * NBLK + 10 * rg + min(lane & 15, 9)) * 1024 + ng * 64 + g * 16);
      }
    }
    asm volatile("" : "+v"(cx.fvo), "+v"(cx.poff), "+v"(cx.xo), "+v"(cx.voff), "+v"(cx.voff5), "+v"(cx.lane4));
  }
  Rng gx, gw;
  auto scan = [&](int t) __attribute__((always_inline)) {
    u32x4 v[9];
#pragma unroll
    for (int j = 0; j < 9; ++j)
      if (j < 8 || NM == 6) v[j] = *reinterpret_cast<const u32x4*>(smem + t * XT1 + (j < 8 || wv < 3 ? j : 7) * 8192 + threadIdx.x * 16);
    SB0;
#pragma unroll
    for (int j = 0; j < 9; ++j)
      if (j < 8 || NM == 6) {
        widen(gx, v[j][0], v[j][1]);
        widen(gx, v[j][2], v[j][3]);
      }
    asm volatile("" : "+v"(gx.hi), "+v"(gx.lo));
    SB0;
  };
  if (NM == 6 && (cx.id | wv) == 0) wait_vm<NQW>();
  wait_vm<8 + 4 + NQW>();
  unsigned near;
  if (NM == 5 || (cx.id | wv)) scan(0);
#pragma unroll
  for (int q = 0; q < NQ1; ++q) {
    asm volatile("" : "+v"(r1[q]));
    widen(gw, r1[q][0], r1[q][1]);
  }
  {
    wait_vm<NQW + 8 - TE>();
    unsigned d0, d1;
    for (unsigned spins = 0;; ++spins) {
      asm volatile("" : "+v"(pr));
      d0 = __builtin_amdgcn_readfirstlane(pr[0]);
      d1 = __builtin_amdgcn_readfirstlane(pr[1]);
      const unsigned d2 = __builtin_amdgcn_readfirstlane(pr[2]), d3 = __builtin_amdgcn_readfirstlane(pr[3]);
      if (__builtin_expect((d0 == ~d2 && d1 == ~d3) || spins > (1u << 21), 1)) break;
      __builtin_amdgcn_s_sleep(2);
      asm volatile("global_load_dwordx4 %0, %1, %2 sc1\n\ts_waitcnt vmcnt(0)" : "=v"(pr) : "v"(0u), "s"(rec) : "memory");
    }
    tag = d0 | (unsigned long long)d1 << 32;
    const unsigned vn = (cx.id + (d0 >> 1)) & 7u;
    if (__builtin_expect(vn != (cx.vb & 7u), 0)) restart(cx.id | 256u | vn << 10);
  }
  const unsigned ee = (cx.id & 512u ? cx.id : (unsigned)(tag >> 32)) >> 16, ex = ee & 255u, ew = ee >> 8;
  cx.es = 256 - (int)(ex + ew);
  char* ws;
  const int bid = cx.vb;
  u32x4 f0, f1;
  char* fab;
  const u32x4* fa;
  u32x2 ov;
  unsigned pollv;
  const int rb[2] = {run_bs(bid, 0), run_bs(bid, 1)};
  unsigned xb0[4];
#pragma unroll
  for (int k = 0; k < 4; ++k) xb0[k] = (unsigned)((4 * g + qq) * 256 + (((2 * mg) ^ ((4 * g + qq + 2 * k) & 7)) << 5) + pp * 8);
#pragma unroll
  for (int q = 0; q < NQ1; ++q) *reinterpret_cast<u32x2*>(smem + cx.pwr + q * 4096) = r1[q];
  asm volatile("" : "+v"(fb[0]), "+v"(fb[1]), "+v"(vt));
#pragma unroll
  for (int i = 0; i < 2; ++i) vt[i] = scale2(vt[i], 128 - (int)ew);
#pragma unroll
  for (int i = 0; i < 8; ++i) fb[i >> 2][i & 3] = scale2(fb[i >> 2][i & 3], 128 - (int)ex);
  asm volatile("" : "+v"(fb[0]), "+v"(fb[1]), "+v"(vt));
  asm volatile("s_waitcnt lgkmcnt(0)" : "+v"(xb0[0]), "+v"(xb0[1]), "+v"(xb0[2]), "+v"(xb0[3]) :: "memory");
  wait_vm<NQW>();
  asm volatile("global_load_dwordx2 %0, %1, %2 sc1" : "=v"(ov) : "v"(2048u), "s"(rec) : "memory");
  __builtin_amdgcn_s_barrier();
  SB0;
  u32x4 hw[6][2], hx[20];
  f32x4 pend[2];
  auto rd = [&](int it, int i) __attribute__((always_inline)) {
    if (it < 3) return tr_read(smem + ((cx.prd ^ (unsigned)((i & 3) << 5)) + (2 * it + (i >> 2)) * 4096));
    const int n = it - 3;
    const unsigned base = (xb0[n % 5 & 3] ^ (unsigned)((4 * ((n / 5) & 1) + (i & 1)) << 5)) + n / 10 * XT1;
    return tr_read(smem + (base + n % 5 * (HOP * 256) + (i >> 1) * (16 * 256)));
  };
  auto dst = [&](int it) -> u32x4& { return it < 3 ? hw[it == 2 ? 4 : 2 * it + 1][1] : hx[it - 3]; };
  auto item = [&](int it, int nx, int pv, XR& xr, auto side) __attribute__((always_inline)) {
    const XR c = xr;
    const int nm = it < 2 ? 8 : 4, nr = nx == 2 ? 4 : 8;
    f32x4 r[8];
#pragma unroll
    for (int i = 0; i < 8; ++i) {
      if (i >= nm) break;
      const int s2 = i & 1, ks = (i >> 1) & 1;
      if (it < 3) r[i] = MFMS(c.t[i], vt);
      else r[i] = MFMA(cat(c.t[4 * ks + s2], c.t[4 * ks + 2 + s2]), __builtin_bit_cast(bf16x8, fb[ks]), i < 2 ? ZERO4 : r[i - 2]);
      SB0;
      if (nx >= 0) {
#pragma unroll
        for (int j = 0; j < 8; ++j) if (j < nr && j * nm / nr == i) xr.t[j] = rd(nx, j);
      }
      if (pv >= 0 && (nm == 4 ? i >= 2 : i >= 1 && i <= 4)) {
        u32x4& d = dst(pv);
#pragma unroll
        for (int e = 0; e < 4; ++e)
          if ((nm == 4 ? 2 + (e >> 1) : 1 + e) == i) d[e] = pk_f16(pend[e >> 1][(e & 1) * 2], pend[e >> 1][(e & 1) * 2 + 1]);
        if (i == (nm == 4 ? 3 : 4)) asm volatile("" : "+v"(d));
      }
      if (it < 3 && i >= 3 && (i & 1)) {
        u32x4& d = hw[2 * it + ((i - 3) >> 2)][((i - 3) >> 1) & 1];
        d = pack8h(r[i - 3], r[i - 2]);
        asm volatile("" : "+v"(d));
      }
      side(i * (8 / nm));
      if (nm == 4) side(2 * i + 1);
      SB0;
    }
    pend[0] = r[nm - 2];
    pend[1] = r[nm - 1];
  };
  const unsigned so0 = (unsigned)(2 * run_q(bid, 0)) * SBLK, so1 = (unsigned)(16 + 2 * run_q(bid, 1)) * SBLK;
  auto store = [&](int j) __attribute__((always_inline)) {
    if (j == 0) {
      wait_vm<0>();
      for (unsigned spins = 0;; ++spins) {
        asm volatile("" : "+v"(ov));
        near = __builtin_amdgcn_readfirstlane(ov[0]) ^ (unsigned)tag;
        if (__builtin_expect((near < 2u && __builtin_amdgcn_readfirstlane(ov[1]) == (unsigned)(tag >> 32)) || spins > (1u << 21), 1)) break;
        __builtin_amdgcn_s_sleep(2);
        asm volatile("global_load_dwordx2 %0, %1, %2 sc1\n\ts_waitcnt vmcnt(0)" : "=v"(ov) : "v"(2048u), "s"(rec) : "memory");
      }
      ws = cx.ws += (near & 1u) * 8192;
      cx.rs = make_rsrc(ws + WS_S, WS_L - WS_S + D_ * D_ * 2);
      fab = ws + WS_FA + (cx.id & 512u) * 32;
      fa = reinterpret_cast<const u32x4*>(fab + (bid & 7) * 2048) + lane;
      if (NM == 6 && wv == 1) {
        const unsigned o = (lane < 32 ? WS_STATS : WS_S + FLG + 16384 - 512) + bid * 512 + lane * 16;
        const unsigned v = lane < 32 ? ~0u : 0u;
        asm volatile("global_store_dwordx4 %0, %1, %2 sc1\n\ts_nop 1" :: "v"(o), "v"(u32x4{v, v, v, v}), "s"(ws) : "memory");
        if (lane < 8) __hip_atomic_store((gu32*)(ws + WS_S + FLG + lane * 1024 + ((bid + 2) & 3) * 256 + (bid >> 2) * 4), 0u, RLX_AGENT);
      }
    }
    if (j == NB + 1 && NM == 6) {
      if (((bid >> 1) ^ (bid >> 3) ^ 1) & 1 && threadIdx.x < 160) {
        const int t = threadIdx.x >= 80, blk = (threadIdx.x >> 4) - 5 * t;
        const unsigned o = (unsigned)(t * XT1 + blk * (HOP * 256) + (((lane & 15) ^ ((blk & 3) << 2)) << 4));
        const u32x4 lo = *reinterpret_cast<const u32x4*>(smem + o), hi = *reinterpret_cast<const u32x4*>(smem + o + 64 * 256);
        u32x4 d;
#pragma unroll
        for (int e = 0; e < 4; ++e) d[e] = pk_bf16(bf_lo(hi[e]) - bf_lo(lo[e]), bf_hi(hi[e]) - bf_hi(lo[e]));
        st16(d, cx.rs, (unsigned)((((bid >> 2) & 1) * NBLK + (t ? rb[1] : rb[0]) + blk) * 1024 + run_q(bid, t) * 256 + (lane & 15) * 16), WS_D - WS_S);
      }
    }
    if (j == NB + 1 && NM == 5) {
      if (wv >= 6) {
        asm volatile("" : "+v"(wl));
        st16(wl, cx.rs, (threadIdx.x & 127) * 16, WS_L - WS_S + bid * 2048);
      }
    }
    const int wq = st_w(j), xn = st_x(j);
    if (wq >= 0) st16(hw[wq >> 1][wq & 1], cx.rs, cx.poff, (unsigned)(wq >> 1) * (2 * SBLK) + (unsigned)((bid + (wq >> 1)) & 3) * 16384u + (wq & 1) * 1024u);
    else x_store(cx, hx[xn], (xn < 10 ? so0 : so1) + ((xn / 5) & 1) * (8 * SBLK), (unsigned)(rb[xn / 10] + xn % 5));
    if (j == NA + NFA - 1) {
      if (NM == 6 && !(cx.id | wv)) scan(0);
      scan(1);
#pragma unroll
      for (int k = 0; k < NQW; ++k) {
        asm volatile("" : "+v"(rq[k]));
        widen(gw, rq[k][0], rq[k][1]);
      }
      const unsigned mx = amax(gx), mw = amax(gw);
      if (__builtin_expect(__any(expo(mx) > ex + 7 || expo(mw) > ew + 9) && !(cx.id & 512u), 0)) {
        const u32x2 m = {wave_max(expo(mx)), wave_max(expo(mw))};
        asm volatile("global_atomic_umax %0, %1, %3 sc1\n\tglobal_atomic_umax %0, %2, %3 offset:4 sc1\n\ts_waitcnt vmcnt(0)"
                     :: "v"(lane * 16 + 1024), "v"(m[0]), "v"(m[1]), "s"(rec - ((cx.id & 7) * 8 + wv) * 16) : "memory");
      }
    }
    if (j == NA + NFA - 1 || j == NB + NFA - 1) {
      wait_vm<NFA>();
      __builtin_amdgcn_s_barrier();
      if (wv == 0 && lane < 8) {
        if (j < NB) __hip_atomic_store((gu64*)(fab + lane * 2048) + bid, tag, RLX_AGENT);
        else __hip_atomic_store((gu32*)(ws + WS_S + FLG + lane * 1024 + ((bid + 2) & 3) * 256 + (bid >> 2) * 4), 1u, RLX_AGENT);
      }
    }
    SB0;
  };
  XR xr;
#pragma unroll
  for (int j = 0; j < 8; ++j) xr.t[j] = rd(0, j);
  SB0;
  static_assert(st_first(NIT) == NS && PG >= 14 && st_first(PG - 2) >= NB + 2 && NA + NFA <= NB && NFA >= 1, "order of the hand-off steps");
  sfor<0, NIT>([&](auto p) __attribute__((always_inline)) {
    constexpr int j0 = st_first(p.v), j1 = st_first(p.v + 1);
    if (p >= 12 && p < PG) { if (!(bid & 1) && cx.id) __builtin_amdgcn_s_sleep(3); }
    if (p == PG - 2) {
      if (!0) code_touch(cx);
      if (NM == 6 && wv == 0)
        asm volatile("global_load_dwordx4 %0, %2, off sc1\n\tglobal_load_dwordx4 %1, %2, off offset:1024 sc1" : "=&v"(f0), "=&v"(f1) : "v"(fa) : "memory");
    }
    if (p == PG) {
      if (NM == 6 && wv == 0) {
        wait_vm<st_first(PG) - st_first(PG - 2)>();
        for (unsigned spins = 0; spins < (1u << 21); ++spins) {
          asm volatile("" : "+v"(f0), "+v"(f1));
          const unsigned t0 = (unsigned)tag, t1 = (unsigned)(tag >> 32);
          if (__all(f0[0] == t0 && f0[1] == t1 && f0[2] == t0 && f0[3] == t1 && f1[0] == t0 && f1[1] == t1 && f1[2] == t0 && f1[3] == t1)) break;
          __builtin_amdgcn_s_sleep(2);
          asm volatile("global_load_dwordx4 %0, %2, off sc1\n\tglobal_load_dwordx4 %1, %2, off offset:1024 sc1\n\ts_waitcnt vmcnt(0)"
                       : "=&v"(f0), "=&v"(f1) : "v"(fa) : "memory");
        }
      }
      __builtin_amdgcn_s_barrier();
      stage_issue<NM>(cx, 0);
      asm volatile("global_load_dwordx2 %0, %1, %2 sc1" : "=v"(ov) : "v"(1024u), "s"(rec) : "memory");
      stage_issue<NM>(cx, 1);
      SB0;
    }
    if (p == NIT - 1 && NM == 5) {
      asm volatile("buffer_load_dword %0, %1, %2, %3 offen sc1" : "=v"(pollv) : "v"(cx.fvo), "s"(cx.rs), "s"(FLG) : "memory");
    }
    item(ord(p), ord(p + 1), ord(p - 1), xr, [&](int i) __attribute__((always_inline)) {
      if ((i * (j1 - j0) + 7) / 8 < ((i + 1) * (j1 - j0) + 7) / 8) store(j0 + (i * (j1 - j0) + 7) / 8);
    });
  });
  hx[19] = pack8h(pend[0], pend[1]);
  xs = so1 + (unsigned)rb[1];
#pragma unroll
  for (int e = 0; e < NH; ++e) rqv.h[e] = hx[20 - NH + e];
  asm volatile("" : "+v"(rqv.h[NH - 1]));
  setup();
  wait_vm<NM == 5 ? NS - st_first(NIT - 1) : 10 - NM + NS - st_first(PG)>();
  {
    asm volatile("" : "+v"(ov));
    const unsigned ox = __builtin_amdgcn_readfirstlane(ov[0]), ow = __builtin_amdgcn_readfirstlane(ov[1]);
    if (__builtin_expect((ox | ow) && !(cx.id & 512u), 0))
      restart((cx.id & 255u) | 768u | (bid & 7u) << 10 | min(max(ex + 7, ox) - 7, 220u) << 16 | min(max(ew + 9, ow) - 9, 220u) << 24);
    if (NM == 6 && (cx.id & 255u) == 0 && wv == 0)
      asm volatile("global_store_dwordx4 %0, %1, %2 sc1\n\ts_nop 1" :: "v"(lane * 16), "v"(u32x4{0u, 0u, 0u, 0u}), "s"(rec) : "memory");
  }
  if (NM == 5) {
    asm volatile("" : "+v"(pollv));
    if (!__all(pollv != 0u)) spin_flags(ws + WS_S + FLG + cx.fvo);
  }
#pragma unroll
  for (int k = 0; k < NQW; ++k) rqv.raw[k] = rq[k];
}

#define LOOP0 \
"s_mov_b32 s91, 0x40004\n" \
"s_mov_b32 s92, 0xfff8fff8\n" \
"ds_read_b64_tr_b16 v[120:121], v241 offset:0\n" \
"ds_read_b64_tr_b16 v[122:123], v241 offset:8192\n" \
"ds_read_b128 v[144:147], v240 offset:0\n" \
"ds_read_b64_tr_b16 v[124:125], v242 offset:0\n" \
"ds_read_b64_tr_b16 v[126:127], v242 offset:8192\n" \
"ds_read_b64_tr_b16 v[128:129], v241 offset:512\n" \
"ds_read_b64_tr_b16 v[130:131], v241 offset:8704\n" \
"ds_read_b64_tr_b16 v[132:133], v242 offset:512\n" \
"ds_read_b64_tr_b16 v[134:135], v242 offset:8704\n" \
"ds_read_b128 v[164:167], v240 offset:512\n" \
"ds_read_b128 v[168:171], v240 offset:2560\n" \
"ds_read_b128 v[148:151], v240 offset:2048\n" \
"ds_read_b128 v[172:175], v240 offset:4608\n" \
"ds_read_b128 v[152:155], v240 offset:4096\n" \
"ds_read_b128 v[176:179], v240 offset:6656\n" \
"ds_read_b128 v[156:159], v240 offset:6144\n" \
"ds_read_b128 v[180:183], v240 offset:8704\n" \
"ds_read_b128 v[160:163], v240 offset:8192\n" \
"s_waitcnt lgkmcnt(15)\n" \
"v_mfma_f32_16x16x32_f16 v[0:3], v[120:123], v[144:147], 0\n" \
"s_waitcnt lgkmcnt(12)\n" \
"v_pk_add_f16 v136, v120, v128\n" \
"v_pk_add_f16 v137, v121, v129\n" \
"s_add_i32 m0, s44, 0x8000\n" \
"s_mov_b32 s80, 0x800000\n" \
"ds_write_b64 v246, v[96:97] offset:0\n" \
"v_mfma_f32_16x16x32_f16 v[12:15], v[124:127], v[144:147], 0\n" \
"s_waitcnt lgkmcnt(9)\n" \
"v_pk_add_f16 v144, v144, v164\n" \
"v_pk_add_f16 v138, v122, v130\n" \
"buffer_load_dwordx4 v232, s[36:39], s80 offen sc1 lds\n" \
"ds_write_b64 v246, v[98:99] offset:4096\n" \
"s_waitcnt lgkmcnt(8)\n" \
"v_mfma_f32_16x16x32_f16 v[36:39], v[124:127], v[148:151], 0\n" \
"v_pk_add_f16 v145, v145, v165\n" \
"v_pk_add_f16 v146, v146, v166\n" \
"v_pk_add_f16 v147, v147, v167\n" \
"ds_write_b64 v246, v[100:101] offset:8192\n" \
"ds_write_b64 v246, v[102:103] offset:12288\n" \
"v_mfma_f32_16x16x32_f16 v[24:27], v[120:123], v[148:151], 0\n" \
"v_pk_add_f16 v148, v148, v168\n" \
"v_pk_add_f16 v139, v123, v131\n" \
"buffer_load_dwordx4 v232, s[36:39], s80 offen offset:1024 sc1 lds\n" \
"ds_write_b64 v246, v[104:105] offset:16384\n" \
"s_waitcnt lgkmcnt(5)\n" \
"v_mfma_f32_16x16x32_f16 v[48:51], v[120:123], v[152:155], 0\n" \
"v_pk_add_f16 v149, v149, v169\n" \
"v_pk_add_f16 v140, v124, v132\n" \
"buffer_store_dwordx4 v[72:75], v235, s[36:39], s93 offen sc1\n" \
"ds_write_b64 v246, v[106:107] offset:20480\n" \
"v_mfma_f32_16x16x32_f16 v[60:63], v[124:127], v[152:155], 0\n" \
"v_pk_add_f16 v150, v150, v170\n" \
"v_pk_add_f16 v151, v151, v171\n" \
"buffer_load_dwordx4 v232, s[36:39], s80 offen offset:2048 sc1 lds\n" \
"ds_write_b64 v246, v[108:109] offset:24576\n" \
"ds_write_b64 v246, v[110:111] offset:28672\n" \
"v_mfma_f32_16x16x32_f16 v[84:87], v[124:127], v[156:159], 0\n" \
"v_pk_add_f16 v152, v152, v172\n" \
"v_pk_add_f16 v141, v125, v133\n" \
"ds_write_b64 v246, v[112:113] offset:32768\n" \
"ds_write_b64 v246, v[114:115] offset:36864\n" \
"v_mfma_f32_16x16x32_f16 v[72:75], v[120:123], v[156:159], 0\n" \
"v_pk_add_f16 v153, v153, v173\n" \
"v_pk_add_f16 v142, v126, v134\n" \
"buffer_load_dwordx4 v232, s[36:39], s80 offen offset:3072 sc1 lds\n" \
"ds_write_b64 v246, v[116:117] offset:40960\n" \
"v_mfma_f32_16x16x32_f16 v[96:99], v[120:123], v[160:163], 0\n" \
"v_pk_add_f16 v154, v154, v174\n" \
"v_pk_add_f16 v155, v155, v175\n" \
"v_mfma_f32_16x16x32_f16 v[108:111], v[124:127], v[160:163], 0\n" \
"v_pk_add_f16 v156, v156, v176\n" \
"v_pk_add_f16 v143, v127, v135\n" \
"s_waitcnt vmcnt(8)\n" \
"s_barrier\n" \
"v_mfma_f32_16x16x32_f16 v[4:7], v[128:131], v[164:167], 0\n" \
"v_pk_add_f16 v157, v157, v177\n" \
"ds_read_b64_tr_b16 v[120:121], v241 offset:16384\n" \
"v_mfma_f32_16x16x32_f16 v[16:19], v[132:135], v[164:167], 0\n" \
"v_pk_add_f16 v158, v158, v178\n" \
"ds_read_b128 v[184:187], v240 offset:20992\n" \
"v_mfma_f32_16x16x32_f16 v[40:43], v[132:135], v[168:171], 0\n" \
"v_pk_add_f16 v159, v159, v179\n" \
"ds_read_b128 v[164:167], v240 offset:20480\n" \
"buffer_store_dwordx4 v[196:199], v235, s[36:39], s60 offen sc1\n" \
"v_mfma_f32_16x16x32_f16 v[28:31], v[128:131], v[168:171], 0\n" \
"v_pk_add_f16 v160, v160, v180\n" \
"ds_read_b64_tr_b16 v[122:123], v241 offset:24576\n" \
"s_add_i32 m0, s44, 0x0\n" \
"s_mov_b32 s80, 0xc00000\n" \
"v_mfma_f32_16x16x32_f16 v[52:55], v[128:131], v[172:175], 0\n" \
"v_pk_add_f16 v161, v161, v181\n" \
"ds_read_b128 v[168:171], v240 offset:22528\n" \
"buffer_load_dwordx4 v232, s[36:39], s80 offen sc1 lds\n" \
"v_mfma_f32_16x16x32_f16 v[64:67], v[132:135], v[172:175], 0\n" \
"v_pk_add_f16 v162, v162, v182\n" \
"ds_read_b64_tr_b16 v[124:125], v242 offset:16384\n" \
"v_mfma_f32_16x16x32_f16 v[88:91], v[132:135], v[176:179], 0\n" \
"v_pk_add_f16 v163, v163, v183\n" \
"ds_read_b128 v[172:175], v240 offset:24576\n" \
"v_mfma_f32_16x16x32_f16 v[76:79], v[128:131], v[176:179], 0\n" \
"ds_read_b64_tr_b16 v[126:127], v242 offset:24576\n" \
"buffer_load_dwordx4 v232, s[36:39], s80 offen offset:1024 sc1 lds\n" \
"v_mfma_f32_16x16x32_f16 v[100:103], v[128:131], v[180:183], 0\n" \
"ds_read_b128 v[176:179], v240 offset:26624\n" \
"buffer_store_dwordx4 v[200:203], v235, s[36:39], s61 offen sc1\n" \
"v_mfma_f32_16x16x32_f16 v[112:115], v[132:135], v[180:183], 0\n" \
"ds_read_b64_tr_b16 v[128:129], v241 offset:16896\n" \
"v_mfma_f32_16x16x32_f16 v[8:11], v[136:139], v[144:147], 0\n" \
"ds_read_b128 v[180:183], v240 offset:28672\n" \
"buffer_load_dwordx4 v232, s[36:39], s80 offen offset:2048 sc1 lds\n" \
"v_mfma_f32_16x16x32_f16 v[20:23], v[140:143], v[144:147], 0\n" \
"ds_read_b128 v[144:147], v240 offset:23040\n" \
"v_mfma_f32_16x16x32_f16 v[44:47], v[140:143], v[148:151], 0\n" \
"ds_read_b64_tr_b16 v[130:131], v241 offset:25088\n" \
"ds_read_b64_tr_b16 v[132:133], v242 offset:16896\n" \
"v_mfma_f32_16x16x32_f16 v[32:35], v[136:139], v[148:151], 0\n" \
"ds_read_b128 v[148:151], v240 offset:25088\n" \
"buffer_load_dwordx4 v232, s[36:39], s80 offen offset:3072 sc1 lds\n" \
"v_mfma_f32_16x16x32_f16 v[56:59], v[136:139], v[152:155], 0\n" \
"ds_read_b64_tr_b16 v[134:135], v242 offset:25088\n" \
"v_mfma_f32_16x16x32_f16 v[68:71], v[140:143], v[152:155], 0\n" \
"ds_read_b128 v[152:155], v240 offset:27136\n" \
"v_mfma_f32_16x16x32_f16 v[92:95], v[140:143], v[156:159], 0\n" \
"v_mfma_f32_16x16x32_f16 v[80:83], v[136:139], v[156:159], 0\n" \
"ds_read_b128 v[156:159], v240 offset:29184\n" \
"v_mfma_f32_16x16x32_f16 v[104:107], v[136:139], v[160:163], 0\n" \
"v_mfma_f32_16x16x32_f16 v[116:119], v[140:143], v[160:163], 0\n" \
"s_waitcnt vmcnt(6) lgkmcnt(0)\n" \
"s_barrier\n" \
"v_mfma_f32_16x16x32_f16 v[0:3], v[120:123], v[164:167], v[0:3]\n" \
"v_pk_add_f16 v136, v120, v128\n" \
"v_pk_add_f16 v137, v121, v129\n" \
"ds_read_b64_tr_b16 v[188:189], v236 offset:0\n" \
"v_mfma_f32_16x16x32_f16 v[12:15], v[124:127], v[164:167], v[12:15]\n" \
"v_pk_add_f16 v164, v164, v184\n" \
"v_pk_add_f16 v138, v122, v130\n" \
"ds_read_b64_tr_b16 v[192:193], v237 offset:0\n" \
"v_mfma_f32_16x16x32_f16 v[36:39], v[124:127], v[168:171], v[36:39]\n" \
"v_pk_add_f16 v165, v165, v185\n" \
"v_pk_add_f16 v166, v166, v186\n" \
"v_pk_add_f16 v167, v167, v187\n" \
"v_mfma_f32_16x16x32_f16 v[24:27], v[120:123], v[168:171], v[24:27]\n" \
"v_pk_add_f16 v168, v168, v144\n" \
"v_pk_add_f16 v139, v123, v131\n" \
"s_add_i32 m0, s44, 0x4000\n" \
"s_mov_b32 s80, 0x1000000\n" \
"v_mfma_f32_16x16x32_f16 v[48:51], v[120:123], v[172:175], v[48:51]\n" \
"v_pk_add_f16 v169, v169, v145\n" \
"v_pk_add_f16 v140, v124, v132\n" \
"buffer_load_dwordx4 v232, s[36:39], s80 offen sc1 lds\n" \
"v_mfma_f32_16x16x32_f16 v[60:63], v[124:127], v[172:175], v[60:63]\n" \
"v_pk_add_f16 v170, v170, v146\n" \
"v_pk_add_f16 v171, v171, v147\n" \
"s_waitcnt lgkmcnt(1)\n" \
"v_mfma_f32_16x16x16_bf16 v[188:191], v[188:189], v[254:255], 0\n" \
"v_mfma_f32_16x16x32_f16 v[84:87], v[124:127], v[176:179], v[84:87]\n" \
"v_pk_add_f16 v172, v172, v148\n" \
"v_pk_add_f16 v141, v125, v133\n" \
"s_add_i32 s84, s52, 0x8004000\n" \
"s_waitcnt lgkmcnt(0)\n" \
"v_mfma_f32_16x16x16_bf16 v[192:195], v[192:193], v[254:255], 0\n" \
"v_mfma_f32_16x16x32_f16 v[72:75], v[120:123], v[176:179], v[72:75]\n" \
"v_pk_add_f16 v173, v173, v149\n" \
"v_pk_add_f16 v142, v126, v134\n" \
"buffer_store_dword v240, v243, s[36:39], s84 offen sc1\n" \
"v_mfma_f32_16x16x32_f16 v[96:99], v[120:123], v[180:183], v[96:99]\n" \
"v_pk_add_f16 v174, v174, v150\n" \
"v_pk_add_f16 v175, v175, v151\n" \
"v_mfma_f32_16x16x32_f16 v[108:111], v[124:127], v[180:183], v[108:111]\n" \
"v_pk_add_f16 v176, v176, v152\n" \
"v_pk_add_f16 v143, v127, v135\n" \
"v_cvt_pk_f16_f32 v188, v188, v189\n" \
"v_cvt_pk_f16_f32 v189, v190, v191\n" \
"v_mfma_f32_16x16x32_f16 v[4:7], v[128:131], v[184:187], v[4:7]\n" \
"v_pk_add_f16 v177, v177, v153\n" \
"v_cvt_pk_f16_f32 v190, v192, v193\n" \
"ds_read_b64_tr_b16 v[120:121], v241 offset:32768\n" \
"buffer_load_dwordx4 v232, s[36:39], s80 offen offset:1024 sc1 lds\n" \
"v_mfma_f32_16x16x32_f16 v[16:19], v[132:135], v[184:187], v[16:19]\n" \
"v_pk_add_f16 v178, v178, v154\n" \
"v_cvt_pk_f16_f32 v191, v194, v195\n" \
"v_add_u32 v188, s91, v188\n" \
"ds_read_b128 v[160:163], v240 offset:41472\n" \
"v_mfma_f32_16x16x32_f16 v[40:43], v[132:135], v[144:147], v[40:43]\n" \
"v_pk_add_f16 v179, v179, v155\n" \
"v_add_u32 v189, s91, v189\n" \
"v_add_u32 v190, s91, v190\n" \
"ds_read_b128 v[184:187], v240 offset:40960\n" \
"v_mfma_f32_16x16x32_f16 v[28:31], v[128:131], v[144:147], v[28:31]\n" \
"v_pk_add_f16 v180, v180, v156\n" \
"v_add_u32 v191, s91, v191\n" \
"v_and_b32 v188, s92, v188\n" \
"ds_read_b64_tr_b16 v[122:123], v241 offset:40960\n" \
"s_add_i32 s82, s49, 0x2800000\n" \
"v_mfma_f32_16x16x32_f16 v[52:55], v[128:131], v[148:151], v[52:55]\n" \
"v_pk_add_f16 v181, v181, v157\n" \
"v_and_b32 v189, s92, v189\n" \
"v_and_b32 v190, s92, v190\n" \
"v_and_b32 v191, s92, v191\n" \
"ds_read_b128 v[144:147], v240 offset:43008\n" \
"v_mfma_f32_16x16x32_f16 v[64:67], v[132:135], v[148:151], v[64:67]\n" \
"v_pk_add_f16 v182, v182, v158\n" \
"ds_read_b64_tr_b16 v[124:125], v242 offset:32768\n" \
"buffer_store_dwordx4 v[188:191], v234, s[36:39], s82 offen sc1\n" \
"v_mfma_f32_16x16x32_f16 v[88:91], v[132:135], v[152:155], v[88:91]\n" \
"v_pk_add_f16 v183, v183, v159\n" \
"ds_read_b128 v[148:151], v240 offset:45056\n" \
"buffer_load_dwordx4 v232, s[36:39], s80 offen offset:2048 sc1 lds\n" \
"v_mfma_f32_16x16x32_f16 v[76:79], v[128:131], v[152:155], v[76:79]\n" \
"ds_read_b64_tr_b16 v[126:127], v242 offset:40960\n" \
"ds_read_b64_tr_b16 v[188:189], v238 offset:0\n" \
"v_mfma_f32_16x16x32_f16 v[100:103], v[128:131], v[156:159], v[100:103]\n" \
"ds_read_b128 v[152:155], v240 offset:47104\n" \
"ds_read_b64_tr_b16 v[192:193], v239 offset:0\n" \
"v_mfma_f32_16x16x32_f16 v[112:115], v[132:135], v[156:159], v[112:115]\n" \
"ds_read_b64_tr_b16 v[128:129], v241 offset:33280\n" \
"v_mfma_f32_16x16x32_f16 v[8:11], v[136:139], v[164:167], v[8:11]\n" \
"ds_read_b128 v[156:159], v240 offset:49152\n" \
"s_waitcnt lgkmcnt(4)\n" \
"v_mfma_f32_16x16x16_bf16 v[188:191], v[188:189], v[254:255], 0\n" \
"v_mfma_f32_16x16x32_f16 v[20:23], v[140:143], v[164:167], v[20:23]\n" \
"ds_read_b128 v[164:167], v240 offset:43520\n" \
"s_waitcnt lgkmcnt(3)\n" \
"v_mfma_f32_16x16x16_bf16 v[192:195], v[192:193], v[254:255], 0\n" \
"v_mfma_f32_16x16x32_f16 v[44:47], v[140:143], v[168:171], v[44:47]\n" \
"ds_read_b64_tr_b16 v[130:131], v241 offset:41472\n" \
"ds_read_b64_tr_b16 v[132:133], v242 offset:33280\n" \
"buffer_load_dwordx4 v232, s[36:39], s80 offen offset:3072 sc1 lds\n" \
"v_mfma_f32_16x16x32_f16 v[32:35], v[136:139], v[168:171], v[32:35]\n" \
"ds_read_b128 v[168:171], v240 offset:45568\n" \
"v_mfma_f32_16x16x32_f16 v[56:59], v[136:139], v[172:175], v[56:59]\n" \
"v_cvt_pk_f16_f32 v188, v188, v189\n" \
"v_cvt_pk_f16_f32 v189, v190, v191\n" \
"ds_read_b64_tr_b16 v[134:135], v242 offset:41472\n" \
"v_mfma_f32_16x16x32_f16 v[68:71], v[140:143], v[172:175], v[68:71]\n" \
"v_cvt_pk_f16_f32 v190, v192, v193\n" \
"v_cvt_pk_f16_f32 v191, v194, v195\n" \
"ds_read_b128 v[172:175], v240 offset:47616\n" \
"v_mfma_f32_16x16x32_f16 v[92:95], v[140:143], v[176:179], v[92:95]\n" \
"v_add_u32 v188, s91, v188\n" \
"v_add_u32 v189, s91, v189\n" \
"v_add_u32 v190, s91, v190\n" \
"v_mfma_f32_16x16x32_f16 v[80:83], v[136:139], v[176:179], v[80:83]\n" \
"v_add_u32 v191, s91, v191\n" \
"v_and_b32 v188, s92, v188\n" \
"ds_read_b128 v[176:179], v240 offset:49664\n" \
"v_mfma_f32_16x16x32_f16 v[104:107], v[136:139], v[180:183], v[104:107]\n" \
"v_and_b32 v189, s92, v189\n" \
"v_and_b32 v190, s92, v190\n" \
"v_and_b32 v191, s92, v191\n" \
"v_mfma_f32_16x16x32_f16 v[116:119], v[140:143], v[180:183], v[116:119]\n" \
"buffer_store_dwordx4 v[188:191], v234, s[36:39], s82 offen offset:1024 sc1\n" \
"s_waitcnt vmcnt(7) lgkmcnt(0)\n" \
"s_barrier\n" \
"v_mfma_f32_16x16x32_f16 v[0:3], v[120:123], v[184:187], v[0:3]\n" \
"v_pk_add_f16 v136, v120, v128\n" \
"v_pk_add_f16 v137, v121, v129\n" \
"v_mfma_f32_16x16x32_f16 v[12:15], v[124:127], v[184:187], v[12:15]\n" \
"v_pk_add_f16 v184, v184, v160\n" \
"v_pk_add_f16 v138, v122, v130\n" \
"v_mfma_f32_16x16x32_f16 v[36:39], v[124:127], v[144:147], v[36:39]\n" \
"v_pk_add_f16 v185, v185, v161\n" \
"v_pk_add_f16 v186, v186, v162\n" \
"v_pk_add_f16 v187, v187, v163\n" \
"buffer_store_dwordx4 v[204:207], v235, s[36:39], s62 offen sc1\n" \
"v_mfma_f32_16x16x32_f16 v[24:27], v[120:123], v[144:147], v[24:27]\n" \
"v_pk_add_f16 v144, v144, v164\n" \
"v_pk_add_f16 v139, v123, v131\n" \
"s_add_i32 m0, s44, 0x8000\n" \
"s_mov_b32 s80, 0x1400000\n" \
"v_mfma_f32_16x16x32_f16 v[48:51], v[120:123], v[148:151], v[48:51]\n" \
"v_pk_add_f16 v145, v145, v165\n" \
"v_pk_add_f16 v140, v124, v132\n" \
"buffer_load_dwordx4 v232, s[36:39], s80 offen sc1 lds\n" \
"v_mfma_f32_16x16x32_f16 v[60:63], v[124:127], v[148:151], v[60:63]\n" \
"v_pk_add_f16 v146, v146, v166\n" \
"v_pk_add_f16 v147, v147, v167\n" \
"v_mfma_f32_16x16x32_f16 v[84:87], v[124:127], v[152:155], v[84:87]\n" \
"v_pk_add_f16 v148, v148, v168\n" \
"v_pk_add_f16 v141, v125, v133\n" \
"v_mfma_f32_16x16x32_f16 v[72:75], v[120:123], v[152:155], v[72:75]\n" \
"v_pk_add_f16 v149, v149, v169\n" \
"v_pk_add_f16 v142, v126, v134\n" \
"v_mfma_f32_16x16x32_f16 v[96:99], v[120:123], v[156:159], v[96:99]\n" \
"v_pk_add_f16 v150, v150, v170\n" \
"v_pk_add_f16 v151, v151, v171\n" \
"ds_read_b64_tr_b16 v[188:189], v236 offset:4096\n" \
"v_mfma_f32_16x16x32_f16 v[108:111], v[124:127], v[156:159], v[108:111]\n" \
"v_pk_add_f16 v152, v152, v172\n" \
"v_pk_add_f16 v143, v127, v135\n" \
"ds_read_b64_tr_b16 v[192:193], v237 offset:4096\n" \
"v_mfma_f32_16x16x32_f16 v[4:7], v[128:131], v[160:163], v[4:7]\n" \
"v_pk_add_f16 v153, v153, v173\n" \
"ds_read_b64_tr_b16 v[120:121], v241 offset:0\n" \
"buffer_load_dwordx4 v232, s[36:39], s80 offen offset:1024 sc1 lds\n" \
"v_mfma_f32_16x16x32_f16 v[16:19], v[132:135], v[160:163], v[16:19]\n" \
"v_pk_add_f16 v154, v154, v174\n" \
"ds_read_b128 v[180:183], v240 offset:512\n" \
"v_mfma_f32_16x16x32_f16 v[40:43], v[132:135], v[164:167], v[40:43]\n" \
"v_pk_add_f16 v155, v155, v175\n" \
"ds_read_b128 v[160:163], v240 offset:0\n" \
"v_mfma_f32_16x16x32_f16 v[28:31], v[128:131], v[164:167], v[28:31]\n" \
"v_pk_add_f16 v156, v156, v176\n" \
"ds_read_b64_tr_b16 v[122:123], v241 offset:8192\n" \
"v_mfma_f32_16x16x32_f16 v[52:55], v[128:131], v[168:171], v[52:55]\n" \
"v_pk_add_f16 v157, v157, v177\n" \
"ds_read_b128 v[164:167], v240 offset:2048\n" \
"s_waitcnt lgkmcnt(6)\n" \
"v_mfma_f32_16x16x16_bf16 v[188:191], v[188:189], v[254:255], 0\n" \
"v_mfma_f32_16x16x32_f16 v[64:67], v[132:135], v[168:171], v[64:67]\n" \
"v_pk_add_f16 v158, v158, v178\n" \
"ds_read_b64_tr_b16 v[124:125], v242 offset:0\n" \
"s_waitcnt lgkmcnt(6)\n" \
"v_mfma_f32_16x16x16_bf16 v[192:195], v[192:193], v[254:255], 0\n" \
"v_mfma_f32_16x16x32_f16 v[88:91], v[132:135], v[172:175], v[88:91]\n" \
"v_pk_add_f16 v159, v159, v179\n" \
"ds_read_b128 v[168:171], v240 offset:4096\n" \
"buffer_load_dwordx4 v232, s[36:39], s80 offen offset:2048 sc1 lds\n" \
"v_mfma_f32_16x16x32_f16 v[76:79], v[128:131], v[172:175], v[76:79]\n" \
"ds_read_b64_tr_b16 v[126:127], v242 offset:8192\n" \
"v_mfma_f32_16x16x32_f16 v[100:103], v[128:131], v[176:179], v[100:103]\n" \
"v_cvt_pk_f16_f32 v188, v188, v189\n" \
"ds_read_b128 v[172:175], v240 offset:6144\n" \
"v_mfma_f32_16x16x32_f16 v[112:115], v[132:135], v[176:179], v[112:115]\n" \
"v_cvt_pk_f16_f32 v189, v190, v191\n" \
"ds_read_b64_tr_b16 v[128:129], v241 offset:512\n" \
"v_mfma_f32_16x16x32_f16 v[8:11], v[136:139], v[184:187], v[8:11]\n" \
"v_cvt_pk_f16_f32 v190, v192, v193\n" \
"ds_read_b128 v[176:179], v240 offset:8192\n" \
"v_mfma_f32_16x16x32_f16 v[20:23], v[140:143], v[184:187], v[20:23]\n" \
"v_cvt_pk_f16_f32 v191, v194, v195\n" \
"v_add_u32 v188, s91, v188\n" \
"ds_read_b128 v[184:187], v240 offset:2560\n" \
"v_mfma_f32_16x16x32_f16 v[44:47], v[140:143], v[144:147], v[44:47]\n" \
"ds_read_b64_tr_b16 v[130:131], v241 offset:8704\n" \
"ds_read_b64_tr_b16 v[132:133], v242 offset:512\n" \
"buffer_load_dwordx4 v232, s[36:39], s80 offen offset:3072 sc1 lds\n" \
"v_mfma_f32_16x16x32_f16 v[32:35], v[136:139], v[144:147], v[32:35]\n" \
"v_add_u32 v189, s91, v189\n" \
"v_add_u32 v190, s91, v190\n" \
"ds_read_b128 v[144:147], v240 offset:4608\n" \
"v_mfma_f32_16x16x32_f16 v[56:59], v[136:139], v[148:151], v[56:59]\n" \
"v_add_u32 v191, s91, v191\n" \
"ds_read_b64_tr_b16 v[134:135], v242 offset:8704\n" \
"v_mfma_f32_16x16x32_f16 v[68:71], v[140:143], v[148:151], v[68:71]\n" \
"v_and_b32 v188, s92, v188\n" \
"ds_read_b128 v[148:151], v240 offset:6656\n" \
"v_mfma_f32_16x16x32_f16 v[92:95], v[140:143], v[152:155], v[92:95]\n" \
"v_and_b32 v189, s92, v189\n" \
"v_and_b32 v190, s92, v190\n" \
"s_add_i32 s82, s50, 0x3000000\n" \
"v_mfma_f32_16x16x32_f16 v[80:83], v[136:139], v[152:155], v[80:83]\n" \
"v_and_b32 v191, s92, v191\n" \
"ds_read_b128 v[152:155], v240 offset:8704\n" \
"v_mfma_f32_16x16x32_f16 v[104:107], v[136:139], v[156:159], v[104:107]\n" \
"buffer_store_dwordx4 v[188:191], v234, s[36:39], s82 offen sc1\n" \
"v_mfma_f32_16x16x32_f16 v[116:119], v[140:143], v[156:159], v[116:119]\n" \
"s_waitcnt vmcnt(6) lgkmcnt(0)\n" \
"s_barrier\n" \
"v_mfma_f32_16x16x32_f16 v[0:3], v[120:123], v[160:163], v[0:3]\n" \
"v_pk_add_f16 v136, v120, v128\n" \
"v_pk_add_f16 v137, v121, v129\n" \
"v_mfma_f32_16x16x32_f16 v[12:15], v[124:127], v[160:163], v[12:15]\n" \
"v_pk_add_f16 v160, v160, v180\n" \
"v_pk_add_f16 v138, v122, v130\n" \
"v_mfma_f32_16x16x32_f16 v[36:39], v[124:127], v[164:167], v[36:39]\n" \
"v_pk_add_f16 v161, v161, v181\n" \
"v_pk_add_f16 v162, v162, v182\n" \
"v_pk_add_f16 v163, v163, v183\n" \
"v_mfma_f32_16x16x32_f16 v[24:27], v[120:123], v[164:167], v[24:27]\n" \
"v_pk_add_f16 v164, v164, v184\n" \
"v_pk_add_f16 v139, v123, v131\n" \
"s_add_i32 m0, s44, 0x0\n" \
"s_mov_b32 s80, 0x1800000\n" \
"v_mfma_f32_16x16x32_f16 v[48:51], v[120:123], v[168:171], v[48:51]\n" \
"v_pk_add_f16 v165, v165, v185\n" \
"v_pk_add_f16 v140, v124, v132\n" \
"buffer_load_dwordx4 v232, s[36:39], s80 offen sc1 lds\n" \
"v_mfma_f32_16x16x32_f16 v[60:63], v[124:127], v[168:171], v[60:63]\n" \
"v_pk_add_f16 v166, v166, v186\n" \
"v_pk_add_f16 v167, v167, v187\n" \
"v_mfma_f32_16x16x32_f16 v[84:87], v[124:127], v[172:175], v[84:87]\n" \
"v_pk_add_f16 v168, v168, v144\n" \
"v_pk_add_f16 v141, v125, v133\n" \
"s_add_i32 s84, s53, 0x8006000\n" \
"v_mfma_f32_16x16x32_f16 v[72:75], v[120:123], v[172:175], v[72:75]\n" \
"v_pk_add_f16 v169, v169, v145\n" \
"v_pk_add_f16 v142, v126, v134\n" \
"buffer_store_dword v240, v243, s[36:39], s84 offen sc1\n" \
"v_mfma_f32_16x16x32_f16 v[96:99], v[120:123], v[176:179], v[96:99]\n" \
"v_pk_add_f16 v170, v170, v146\n" \
"v_pk_add_f16 v171, v171, v147\n" \
"ds_read_b64_tr_b16 v[188:189], v238 offset:4096\n" \
"v_mfma_f32_16x16x32_f16 v[108:111], v[124:127], v[176:179], v[108:111]\n" \
"v_pk_add_f16 v172, v172, v148\n" \
"v_pk_add_f16 v143, v127, v135\n" \
"ds_read_b64_tr_b16 v[192:193], v239 offset:4096\n" \
"v_mfma_f32_16x16x32_f16 v[4:7], v[128:131], v[180:183], v[4:7]\n" \
"v_pk_add_f16 v173, v173, v149\n" \
"ds_read_b64_tr_b16 v[120:121], v241 offset:16384\n" \
"buffer_load_dwordx4 v232, s[36:39], s80 offen offset:1024 sc1 lds\n" \
"v_mfma_f32_16x16x32_f16 v[16:19], v[132:135], v[180:183], v[16:19]\n" \
"v_pk_add_f16 v174, v174, v150\n" \
"ds_read_b128 v[156:159], v240 offset:20992\n" \
"v_mfma_f32_16x16x32_f16 v[40:43], v[132:135], v[184:187], v[40:43]\n" \
"v_pk_add_f16 v175, v175, v151\n" \
"ds_read_b128 v[180:183], v240 offset:20480\n" \
"v_mfma_f32_16x16x32_f16 v[28:31], v[128:131], v[184:187], v[28:31]\n" \
"v_pk_add_f16 v176, v176, v152\n" \
"ds_read_b64_tr_b16 v[122:123], v241 offset:24576\n" \
"v_mfma_f32_16x16x32_f16 v[52:55], v[128:131], v[144:147], v[52:55]\n" \
"v_pk_add_f16 v177, v177, v153\n" \
"ds_read_b128 v[184:187], v240 offset:22528\n" \
"s_waitcnt lgkmcnt(6)\n" \
"v_mfma_f32_16x16x16_bf16 v[188:191], v[188:189], v[254:255], 0\n" \
"v_mfma_f32_16x16x32_f16 v[64:67], v[132:135], v[144:147], v[64:67]\n" \
"v_pk_add_f16 v178, v178, v154\n" \
"ds_read_b64_tr_b16 v[124:125], v242 offset:16384\n" \
"s_waitcnt lgkmcnt(6)\n" \
"v_mfma_f32_16x16x16_bf16 v[192:195], v[192:193], v[254:255], 0\n" \
"v_mfma_f32_16x16x32_f16 v[88:91], v[132:135], v[148:151], v[88:91]\n" \
"v_pk_add_f16 v179, v179, v155\n" \
"ds_read_b128 v[144:147], v240 offset:24576\n" \
"buffer_load_dwordx4 v232, s[36:39], s80 offen offset:2048 sc1 lds\n" \
"v_mfma_f32_16x16x32_f16 v[76:79], v[128:131], v[148:151], v[76:79]\n" \
"ds_read_b64_tr_b16 v[126:127], v242 offset:24576\n" \
"v_mfma_f32_16x16x32_f16 v[100:103], v[128:131], v[152:155], v[100:103]\n" \
"v_cvt_pk_f16_f32 v188, v188, v189\n" \
"ds_read_b128 v[148:151], v240 offset:26624\n" \
"v_mfma_f32_16x16x32_f16 v[112:115], v[132:135], v[152:155], v[112:115]\n" \
"v_cvt_pk_f16_f32 v189, v190, v191\n" \
"ds_read_b64_tr_b16 v[128:129], v241 offset:16896\n" \
"v_mfma_f32_16x16x32_f16 v[8:11], v[136:139], v[160:163], v[8:11]\n" \
"v_cvt_pk_f16_f32 v190, v192, v193\n" \
"ds_read_b128 v[152:155], v240 offset:28672\n" \
"v_mfma_f32_16x16x32_f16 v[20:23], v[140:143], v[160:163], v[20:23]\n" \
"v_cvt_pk_f16_f32 v191, v194, v195\n" \
"v_add_u32 v188, s91, v188\n" \
"ds_read_b128 v[160:163], v240 offset:23040\n" \
"v_mfma_f32_16x16x32_f16 v[44:47], v[140:143], v[164:167], v[44:47]\n" \
"ds_read_b64_tr_b16 v[130:131], v241 offset:25088\n" \
"ds_read_b64_tr_b16 v[132:133], v242 offset:16896\n" \
"buffer_load_dwordx4 v232, s[36:39], s80 offen offset:3072 sc1 lds\n" \
"v_mfma_f32_16x16x32_f16 v[32:35], v[136:139], v[164:167], v[32:35]\n" \
"v_add_u32 v189, s91, v189\n" \
"v_add_u32 v190, s91, v190\n" \
"ds_read_b128 v[164:167], v240 offset:25088\n" \
"v_mfma_f32_16x16x32_f16 v[56:59], v[136:139], v[168:171], v[56:59]\n" \
"v_add_u32 v191, s91, v191\n" \
"ds_read_b64_tr_b16 v[134:135], v242 offset:25088\n" \
"v_mfma_f32_16x16x32_f16 v[68:71], v[140:143], v[168:171], v[68:71]\n" \
"v_and_b32 v188, s92, v188\n" \
"ds_read_b128 v[168:171], v240 offset:27136\n" \
"v_mfma_f32_16x16x32_f16 v[92:95], v[140:143], v[172:175], v[92:95]\n" \
"v_and_b32 v189, s92, v189\n" \
"v_and_b32 v190, s92, v190\n" \
"v_mfma_f32_16x16x32_f16 v[80:83], v[136:139], v[172:175], v[80:83]\n" \
"v_and_b32 v191, s92, v191\n" \
"ds_read_b128 v[172:175], v240 offset:29184\n" \
"v_mfma_f32_16x16x32_f16 v[104:107], v[136:139], v[176:179], v[104:107]\n" \
"buffer_store_dwordx4 v[188:191], v234, s[36:39], s82 offen offset:1024 sc1\n" \
"v_mfma_f32_16x16x32_f16 v[116:119], v[140:143], v[176:179], v[116:119]\n" \
"s_waitcnt vmcnt(7) lgkmcnt(0)\n" \
"s_barrier\n" \
"v_mfma_f32_16x16x32_f16 v[0:3], v[120:123], v[180:183], v[0:3]\n" \
"v_pk_add_f16 v136, v120, v128\n" \
"v_pk_add_f16 v137, v121, v129\n" \
"v_mfma_f32_16x16x32_f16 v[12:15], v[124:127], v[180:183], v[12:15]\n" \
"v_pk_add_f16 v180, v180, v156\n" \
"v_pk_add_f16 v138, v122, v130\n" \
"v_mfma_f32_16x16x32_f16 v[36:39], v[124:127], v[184:187], v[36:39]\n" \
"v_pk_add_f16 v181, v181, v157\n" \
"v_pk_add_f16 v182, v182, v158\n" \
"v_pk_add_f16 v183, v183, v159\n" \
"buffer_store_dwordx4 v[208:211], v235, s[36:39], s63 offen sc1\n" \
"v_mfma_f32_16x16x32_f16 v[24:27], v[120:123], v[184:187], v[24:27]\n" \
"v_pk_add_f16 v184, v184, v160\n" \
"v_pk_add_f16 v139, v123, v131\n" \
"s_add_i32 m0, s44, 0x4000\n" \
"s_mov_b32 s80, 0x1c00000\n" \
"v_mfma_f32_16x16x32_f16 v[48:51], v[120:123], v[144:147], v[48:51]\n" \
"v_pk_add_f16 v185, v185, v161\n" \
"v_pk_add_f16 v140, v124, v132\n" \
"buffer_load_dwordx4 v232, s[36:39], s80 offen sc1 lds\n" \
"v_mfma_f32_16x16x32_f16 v[60:63], v[124:127], v[144:147], v[60:63]\n" \
"v_pk_add_f16 v186, v186, v162\n" \
"v_pk_add_f16 v187, v187, v163\n" \
"v_mfma_f32_16x16x32_f16 v[84:87], v[124:127], v[148:151], v[84:87]\n" \
"v_pk_add_f16 v144, v144, v164\n" \
"v_pk_add_f16 v141, v125, v133\n" \
"v_mfma_f32_16x16x32_f16 v[72:75], v[120:123], v[148:151], v[72:75]\n" \
"v_pk_add_f16 v145, v145, v165\n" \
"v_pk_add_f16 v142, v126, v134\n" \
"v_mfma_f32_16x16x32_f16 v[96:99], v[120:123], v[152:155], v[96:99]\n" \
"v_pk_add_f16 v146, v146, v166\n" \
"v_pk_add_f16 v147, v147, v167\n" \
"ds_read_b64_tr_b16 v[188:189], v236 offset:8192\n" \
"v_mfma_f32_16x16x32_f16 v[108:111], v[124:127], v[152:155], v[108:111]\n" \
"v_pk_add_f16 v148, v148, v168\n" \
"v_pk_add_f16 v143, v127, v135\n" \
"ds_read_b64_tr_b16 v[192:193], v237 offset:8192\n" \
"v_mfma_f32_16x16x32_f16 v[4:7], v[128:131], v[156:159], v[4:7]\n" \
"v_pk_add_f16 v149, v149, v169\n" \
"ds_read_b64_tr_b16 v[120:121], v241 offset:32768\n" \
"buffer_load_dwordx4 v232, s[36:39], s80 offen offset:1024 sc1 lds\n" \
"v_mfma_f32_16x16x32_f16 v[16:19], v[132:135], v[156:159], v[16:19]\n" \
"v_pk_add_f16 v150, v150, v170\n" \
"ds_read_b128 v[176:179], v240 offset:41472\n" \
"v_mfma_f32_16x16x32_f16 v[40:43], v[132:135], v[160:163], v[40:43]\n" \
"v_pk_add_f16 v151, v151, v171\n" \
"ds_read_b128 v[156:159], v240 offset:40960\n" \
"v_mfma_f32_16x16x32_f16 v[28:31], v[128:131], v[160:163], v[28:31]\n" \
"v_pk_add_f16 v152, v152, v172\n" \
"ds_read_b64_tr_b16 v[122:123], v241 offset:40960\n" \
"v_mfma_f32_16x16x32_f16 v[52:55], v[128:131], v[164:167], v[52:55]\n" \
"v_pk_add_f16 v153, v153, v173\n" \
"ds_read_b128 v[160:163], v240 offset:43008\n" \
"s_waitcnt lgkmcnt(6)\n" \
"v_mfma_f32_16x16x16_bf16 v[188:191], v[188:189], v[254:255], 0\n" \
"v_mfma_f32_16x16x32_f16 v[64:67], v[132:135], v[164:167], v[64:67]\n" \
"v_pk_add_f16 v154, v154, v174\n" \
"ds_read_b64_tr_b16 v[124:125], v242 offset:32768\n" \
"s_waitcnt lgkmcnt(6)\n" \
"v_mfma_f32_16x16x16_bf16 v[192:195], v[192:193], v[254:255], 0\n" \
"v_mfma_f32_16x16x32_f16 v[88:91], v[132:135], v[168:171], v[88:91]\n" \
"v_pk_add_f16 v155, v155, v175\n" \
"ds_read_b128 v[164:167], v240 offset:45056\n" \
"buffer_load_dwordx4 v232, s[36:39], s80 offen offset:2048 sc1 lds\n" \
"v_mfma_f32_16x16x32_f16 v[76:79], v[128:131], v[168:171], v[76:79]\n" \
"ds_read_b64_tr_b16 v[126:127], v242 offset:40960\n" \
"v_mfma_f32_16x16x32_f16 v[100:103], v[128:131], v[172:175], v[100:103]\n" \
"v_cvt_pk_f16_f32 v188, v188, v189\n" \
"ds_read_b128 v[168:171], v240 offset:47104\n" \
"v_mfma_f32_16x16x32_f16 v[112:115], v[132:135], v[172:175], v[112:115]\n" \
"v_cvt_pk_f16_f32 v189, v190, v191\n" \
"ds_read_b64_tr_b16 v[128:129], v241 offset:33280\n" \
"v_mfma_f32_16x16x32_f16 v[8:11], v[136:139], v[180:183], v[8:11]\n" \
"v_cvt_pk_f16_f32 v190, v192, v193\n" \
"ds_read_b128 v[172:175], v240 offset:49152\n" \
"v_mfma_f32_16x16x32_f16 v[20:23], v[140:143], v[180:183], v[20:23]\n" \
"v_cvt_pk_f16_f32 v191, v194, v195\n" \
"v_add_u32 v188, s91, v188\n" \
"ds_read_b128 v[180:183], v240 offset:43520\n" \
"v_mfma_f32_16x16x32_f16 v[44:47], v[140:143], v[184:187], v[44:47]\n" \
"ds_read_b64_tr_b16 v[130:131], v241 offset:41472\n" \
"ds_read_b64_tr_b16 v[132:133], v242 offset:33280\n" \
"buffer_load_dwordx4 v232, s[36:39], s80 offen offset:3072 sc1 lds\n" \
"v_mfma_f32_16x16x32_f16 v[32:35], v[136:139], v[184:187], v[32:35]\n" \
"v_add_u32 v189, s91, v189\n" \
"v_add_u32 v190, s91, v190\n" \
"ds_read_b128 v[184:187], v240 offset:45568\n" \
"v_mfma_f32_16x16x32_f16 v[56:59], v[136:139], v[144:147], v[56:59]\n" \
"v_add_u32 v191, s91, v191\n" \
"ds_read_b64_tr_b16 v[134:135], v242 offset:41472\n" \
"v_mfma_f32_16x16x32_f16 v[68:71], v[140:143], v[144:147], v[68:71]\n" \
"v_and_b32 v188, s92, v188\n" \
"ds_read_b128 v[144:147], v240 offset:47616\n" \
"v_mfma_f32_16x16x32_f16 v[92:95], v[140:143], v[148:151], v[92:95]\n" \
"v_and_b32 v189, s92, v189\n" \
"v_and_b32 v190, s92, v190\n" \
"s_add_i32 s82, s51, 0x3800000\n" \
"v_mfma_f32_16x16x32_f16 v[80:83], v[136:139], v[148:151], v[80:83]\n" \
"v_and_b32 v191, s92, v191\n" \
"ds_read_b128 v[148:151], v240 offset:49664\n" \
"v_mfma_f32_16x16x32_f16 v[104:107], v[136:139], v[152:155], v[104:107]\n" \
"buffer_store_dwordx4 v[188:191], v234, s[36:39], s82 offen sc1\n" \
"v_mfma_f32_16x16x32_f16 v[116:119], v[140:143], v[152:155], v[116:119]\n" \
"s_waitcnt vmcnt(6) lgkmcnt(0)\n" \
"s_barrier\n" \
"v_mfma_f32_16x16x32_f16 v[0:3], v[120:123], v[156:159], v[0:3]\n" \
"v_pk_add_f16 v136, v120, v128\n" \
"v_pk_add_f16 v137, v121, v129\n" \
"v_mfma_f32_16x16x32_f16 v[12:15], v[124:127], v[156:159], v[12:15]\n" \
"v_pk_add_f16 v156, v156, v176\n" \
"v_pk_add_f16 v138, v122, v130\n" \
"v_mfma_f32_16x16x32_f16 v[36:39], v[124:127], v[160:163], v[36:39]\n" \
"v_pk_add_f16 v157, v157, v177\n" \
"v_pk_add_f16 v158, v158, v178\n" \
"v_pk_add_f16 v159, v159, v179\n" \
"v_mfma_f32_16x16x32_f16 v[24:27], v[120:123], v[160:163], v[24:27]\n" \
"v_pk_add_f16 v160, v160, v180\n" \
"v_pk_add_f16 v139, v123, v131\n" \
"s_add_i32 m0, s44, 0x8000\n" \
"s_mov_b32 s80, 0x2000000\n" \
"v_mfma_f32_16x16x32_f16 v[48:51], v[120:123], v[164:167], v[48:51]\n" \
"v_pk_add_f16 v161, v161, v181\n" \
"v_pk_add_f16 v140, v124, v132\n" \
"buffer_load_dwordx4 v232, s[36:39], s80 offen sc1 lds\n" \
"v_mfma_f32_16x16x32_f16 v[60:63], v[124:127], v[164:167], v[60:63]\n" \
"v_pk_add_f16 v162, v162, v182\n" \
"v_pk_add_f16 v163, v163, v183\n" \
"ds_read_b128 v[208:211], v240 offset:2560\n" \
"v_mfma_f32_16x16x32_f16 v[84:87], v[124:127], v[168:171], v[84:87]\n" \
"v_pk_add_f16 v164, v164, v184\n" \
"v_pk_add_f16 v141, v125, v133\n" \
"s_add_i32 s84, s54, 0x8008000\n" \
"v_mfma_f32_16x16x32_f16 v[72:75], v[120:123], v[168:171], v[72:75]\n" \
"v_pk_add_f16 v165, v165, v185\n" \
"v_pk_add_f16 v142, v126, v134\n" \
"buffer_store_dword v240, v243, s[36:39], s84 offen sc1\n" \
"v_mfma_f32_16x16x32_f16 v[96:99], v[120:123], v[172:175], v[96:99]\n" \
"v_pk_add_f16 v166, v166, v186\n" \
"v_pk_add_f16 v167, v167, v187\n" \
"ds_read_b64_tr_b16 v[188:189], v238 offset:8192\n" \
"v_mfma_f32_16x16x32_f16 v[108:111], v[124:127], v[172:175], v[108:111]\n" \
"v_pk_add_f16 v168, v168, v144\n" \
"v_pk_add_f16 v143, v127, v135\n" \
"ds_read_b64_tr_b16 v[192:193], v239 offset:8192\n" \
"v_mfma_f32_16x16x32_f16 v[4:7], v[128:131], v[176:179], v[4:7]\n" \
"v_pk_add_f16 v169, v169, v145\n" \
"ds_read_b64_tr_b16 v[120:121], v241 offset:0\n" \
"buffer_load_dwordx4 v232, s[36:39], s80 offen offset:1024 sc1 lds\n" \
"v_mfma_f32_16x16x32_f16 v[16:19], v[132:135], v[176:179], v[16:19]\n" \
"v_pk_add_f16 v170, v170, v146\n" \
"ds_read_b128 v[152:155], v240 offset:512\n" \
"v_mfma_f32_16x16x32_f16 v[40:43], v[132:135], v[180:183], v[40:43]\n" \
"v_pk_add_f16 v171, v171, v147\n" \
"ds_read_b128 v[176:179], v240 offset:0\n" \
"v_mfma_f32_16x16x32_f16 v[28:31], v[128:131], v[180:183], v[28:31]\n" \
"v_pk_add_f16 v172, v172, v148\n" \
"ds_read_b64_tr_b16 v[122:123], v241 offset:8192\n" \
"v_mfma_f32_16x16x32_f16 v[52:55], v[128:131], v[184:187], v[52:55]\n" \
"v_pk_add_f16 v173, v173, v149\n" \
"ds_read_b128 v[180:183], v240 offset:2048\n" \
"s_waitcnt lgkmcnt(6)\n" \
"v_mfma_f32_16x16x16_bf16 v[188:191], v[188:189], v[254:255], 0\n" \
"v_mfma_f32_16x16x32_f16 v[64:67], v[132:135], v[184:187], v[64:67]\n" \
"v_pk_add_f16 v174, v174, v150\n" \
"ds_read_b64_tr_b16 v[124:125], v242 offset:0\n" \
"s_waitcnt lgkmcnt(6)\n" \
"v_mfma_f32_16x16x16_bf16 v[192:195], v[192:193], v[254:255], 0\n" \
"v_mfma_f32_16x16x32_f16 v[88:91], v[132:135], v[144:147], v[88:91]\n" \
"v_pk_add_f16 v175, v175, v151\n" \
"ds_read_b128 v[184:187], v240 offset:4096\n" \
"buffer_load_dwordx4 v232, s[36:39], s80 offen offset:2048 sc1 lds\n" \
"v_mfma_f32_16x16x32_f16 v[76:79], v[128:131], v[144:147], v[76:79]\n" \
"ds_read_b64_tr_b16 v[126:127], v242 offset:8192\n" \
"v_mfma_f32_16x16x32_f16 v[100:103], v[128:131], v[148:151], v[100:103]\n" \
"v_cvt_pk_f16_f32 v188, v188, v189\n" \
"ds_read_b128 v[144:147], v240 offset:6144\n" \
"v_mfma_f32_16x16x32_f16 v[112:115], v[132:135], v[148:151], v[112:115]\n" \
"v_cvt_pk_f16_f32 v189, v190, v191\n" \
"ds_read_b64_tr_b16 v[128:129], v241 offset:512\n" \
"v_mfma_f32_16x16x32_f16 v[8:11], v[136:139], v[156:159], v[8:11]\n" \
"v_cvt_pk_f16_f32 v190, v192, v193\n" \
"ds_read_b128 v[148:151], v240 offset:8192\n" \
"v_mfma_f32_16x16x32_f16 v[20:23], v[140:143], v[156:159], v[20:23]\n" \
"v_cvt_pk_f16_f32 v191, v194, v195\n" \
"v_add_u32 v188, s91, v188\n" \
"ds_read_b128 v[156:159], v240 offset:4608\n" \
"v_mfma_f32_16x16x32_f16 v[44:47], v[140:143], v[160:163], v[44:47]\n" \
"ds_read_b64_tr_b16 v[130:131], v241 offset:8704\n" \
"ds_read_b64_tr_b16 v[132:133], v242 offset:512\n" \
"buffer_load_dwordx4 v232, s[36:39], s80 offen offset:3072 sc1 lds\n" \
"v_mfma_f32_16x16x32_f16 v[32:35], v[136:139], v[160:163], v[32:35]\n" \
"v_add_u32 v189, s91, v189\n" \
"v_add_u32 v190, s91, v190\n" \
"ds_read_b128 v[160:163], v240 offset:6656\n" \
"v_mfma_f32_16x16x32_f16 v[56:59], v[136:139], v[164:167], v[56:59]\n" \
"v_add_u32 v191, s91, v191\n" \
"ds_read_b64_tr_b16 v[134:135], v242 offset:8704\n" \
"v_mfma_f32_16x16x32_f16 v[68:71], v[140:143], v[164:167], v[68:71]\n" \
"v_and_b32 v188, s92, v188\n" \
"ds_read_b128 v[164:167], v240 offset:8704\n" \
"v_mfma_f32_16x16x32_f16 v[92:95], v[140:143], v[168:171], v[92:95]\n" \
"v_and_b32 v189, s92, v189\n" \
"v_mfma_f32_16x16x32_f16 v[80:83], v[136:139], v[168:171], v[80:83]\n" \
"v_and_b32 v190, s92, v190\n" \
"v_and_b32 v191, s92, v191\n" \
"v_mfma_f32_16x16x32_f16 v[104:107], v[136:139], v[172:175], v[104:107]\n" \
"buffer_store_dwordx4 v[188:191], v234, s[36:39], s82 offen offset:1024 sc1\n" \
"v_mfma_f32_16x16x32_f16 v[116:119], v[140:143], v[172:175], v[116:119]\n" \
"s_waitcnt vmcnt(7) lgkmcnt(0)\n" \
"s_barrier\n" \
"v_mfma_f32_16x16x32_f16 v[0:3], v[120:123], v[176:179], v[0:3]\n" \
"v_pk_add_f16 v136, v120, v128\n" \
"v_pk_add_f16 v137, v121, v129\n" \
"s_mov_b32 s81, 0x8006000\n" \
"v_mfma_f32_16x16x32_f16 v[12:15], v[124:127], v[176:179], v[12:15]\n" \
"v_pk_add_f16 v176, v176, v152\n" \
"v_pk_add_f16 v138, v122, v130\n" \
"buffer_load_dword v253, v252, s[36:39], s81 offen sc1\n" \
"v_mfma_f32_16x16x32_f16 v[36:39], v[124:127], v[180:183], v[36:39]\n" \
"v_pk_add_f16 v177, v177, v153\n" \
"v_pk_add_f16 v178, v178, v154\n" \
"v_pk_add_f16 v179, v179, v155\n" \
"buffer_store_dwordx4 v[212:215], v235, s[36:39], s64 offen sc1\n" \
"v_mfma_f32_16x16x32_f16 v[24:27], v[120:123], v[180:183], v[24:27]\n" \
"v_pk_add_f16 v180, v180, v208\n" \
"v_pk_add_f16 v139, v123, v131\n" \
"s_add_i32 m0, s44, 0x0\n" \
"s_mov_b32 s80, 0x2400000\n" \
"v_mfma_f32_16x16x32_f16 v[48:51], v[120:123], v[184:187], v[48:51]\n" \
"v_pk_add_f16 v181, v181, v209\n" \
"v_pk_add_f16 v140, v124, v132\n" \
"buffer_load_dwordx4 v232, s[36:39], s80 offen sc1 lds\n" \
"v_mfma_f32_16x16x32_f16 v[60:63], v[124:127], v[184:187], v[60:63]\n" \
"v_pk_add_f16 v182, v182, v210\n" \
"v_pk_add_f16 v183, v183, v211\n" \
"ds_read_b128 v[172:175], v240 offset:23040\n" \
"v_mfma_f32_16x16x32_f16 v[84:87], v[124:127], v[144:147], v[84:87]\n" \
"v_pk_add_f16 v184, v184, v156\n" \
"v_pk_add_f16 v141, v125, v133\n" \
"v_mfma_f32_16x16x32_f16 v[72:75], v[120:123], v[144:147], v[72:75]\n" \
"v_pk_add_f16 v185, v185, v157\n" \
"v_pk_add_f16 v142, v126, v134\n" \
"v_mfma_f32_16x16x32_f16 v[96:99], v[120:123], v[148:151], v[96:99]\n" \
"v_pk_add_f16 v186, v186, v158\n" \
"v_pk_add_f16 v187, v187, v159\n" \
"ds_read_b64_tr_b16 v[188:189], v236 offset:12288\n" \
"v_mfma_f32_16x16x32_f16 v[108:111], v[124:127], v[148:151], v[108:111]\n" \
"v_pk_add_f16 v144, v144, v160\n" \
"v_pk_add_f16 v143, v127, v135\n" \
"ds_read_b64_tr_b16 v[192:193], v237 offset:12288\n" \
"v_mfma_f32_16x16x32_f16 v[4:7], v[128:131], v[152:155], v[4:7]\n" \
"v_pk_add_f16 v145, v145, v161\n" \
"ds_read_b64_tr_b16 v[120:121], v241 offset:16384\n" \
"buffer_load_dwordx4 v232, s[36:39], s80 offen offset:1024 sc1 lds\n" \
"v_mfma_f32_16x16x32_f16 v[16:19], v[132:135], v[152:155], v[16:19]\n" \
"v_pk_add_f16 v146, v146, v162\n" \
"ds_read_b128 v[168:171], v240 offset:20992\n" \
"v_mfma_f32_16x16x32_f16 v[40:43], v[132:135], v[208:211], v[40:43]\n" \
"v_pk_add_f16 v147, v147, v163\n" \
"ds_read_b128 v[152:155], v240 offset:20480\n" \
"v_mfma_f32_16x16x32_f16 v[28:31], v[128:131], v[208:211], v[28:31]\n" \
"v_pk_add_f16 v148, v148, v164\n" \
"ds_read_b64_tr_b16 v[122:123], v241 offset:24576\n" \
"v_mfma_f32_16x16x32_f16 v[52:55], v[128:131], v[156:159], v[52:55]\n" \
"v_pk_add_f16 v149, v149, v165\n" \
"ds_read_b128 v[208:211], v240 offset:22528\n" \
"s_waitcnt lgkmcnt(6)\n" \
"v_mfma_f32_16x16x16_bf16 v[188:191], v[188:189], v[254:255], 0\n" \
"v_mfma_f32_16x16x32_f16 v[64:67], v[132:135], v[156:159], v[64:67]\n" \
"v_pk_add_f16 v150, v150, v166\n" \
"ds_read_b64_tr_b16 v[124:125], v242 offset:16384\n" \
"s_waitcnt lgkmcnt(6)\n" \
"v_mfma_f32_16x16x16_bf16 v[192:195], v[192:193], v[254:255], 0\n" \
"v_mfma_f32_16x16x32_f16 v[88:91], v[132:135], v[160:163], v[88:91]\n" \
"v_pk_add_f16 v151, v151, v167\n" \
"ds_read_b128 v[156:159], v240 offset:24576\n" \
"buffer_load_dwordx4 v232, s[36:39], s80 offen offset:2048 sc1 lds\n" \
"v_mfma_f32_16x16x32_f16 v[76:79], v[128:131], v[160:163], v[76:79]\n" \
"ds_read_b64_tr_b16 v[126:127], v242 offset:24576\n" \
"v_mfma_f32_16x16x32_f16 v[100:103], v[128:131], v[164:167], v[100:103]\n" \
"v_cvt_pk_f16_f32 v188, v188, v189\n" \
"ds_read_b128 v[160:163], v240 offset:26624\n" \
"v_mfma_f32_16x16x32_f16 v[112:115], v[132:135], v[164:167], v[112:115]\n" \
"v_cvt_pk_f16_f32 v189, v190, v191\n" \
"ds_read_b64_tr_b16 v[128:129], v241 offset:16896\n" \
"v_mfma_f32_16x16x32_f16 v[8:11], v[136:139], v[176:179], v[8:11]\n" \
"v_cvt_pk_f16_f32 v190, v192, v193\n" \
"ds_read_b128 v[164:167], v240 offset:28672\n" \
"v_mfma_f32_16x16x32_f16 v[20:23], v[140:143], v[176:179], v[20:23]\n" \
"v_cvt_pk_f16_f32 v191, v194, v195\n" \
"v_add_u32 v188, s91, v188\n" \
"ds_read_b128 v[176:179], v240 offset:25088\n" \
"v_mfma_f32_16x16x32_f16 v[44:47], v[140:143], v[180:183], v[44:47]\n" \
"ds_read_b64_tr_b16 v[130:131], v241 offset:25088\n" \
"ds_read_b64_tr_b16 v[132:133], v242 offset:16896\n" \
"buffer_load_dwordx4 v232, s[36:39], s80 offen offset:3072 sc1 lds\n" \
"v_mfma_f32_16x16x32_f16 v[32:35], v[136:139], v[180:183], v[32:35]\n" \
"v_add_u32 v189, s91, v189\n" \
"v_add_u32 v190, s91, v190\n" \
"ds_read_b128 v[180:183], v240 offset:27136\n" \
"v_mfma_f32_16x16x32_f16 v[56:59], v[136:139], v[184:187], v[56:59]\n" \
"v_add_u32 v191, s91, v191\n" \
"ds_read_b64_tr_b16 v[134:135], v242 offset:25088\n" \
"v_mfma_f32_16x16x32_f16 v[68:71], v[140:143], v[184:187], v[68:71]\n" \
"v_and_b32 v188, s92, v188\n" \
"ds_read_b128 v[184:187], v240 offset:29184\n" \
"v_mfma_f32_16x16x32_f16 v[92:95], v[140:143], v[144:147], v[92:95]\n" \
"v_and_b32 v189, s92, v189\n" \
"s_add_i32 s82, s48, 0x4000000\n" \
"v_mfma_f32_16x16x32_f16 v[80:83], v[136:139], v[144:147], v[80:83]\n" \
"v_and_b32 v190, s92, v190\n" \
"v_and_b32 v191, s92, v191\n" \
"v_mfma_f32_16x16x32_f16 v[104:107], v[136:139], v[148:151], v[104:107]\n" \
"buffer_store_dwordx4 v[188:191], v234, s[36:39], s82 offen sc1\n" \
"v_mfma_f32_16x16x32_f16 v[116:119], v[140:143], v[148:151], v[116:119]\n" \
"s_waitcnt vmcnt(6) lgkmcnt(0)\n" \
"s_barrier\n" \
"v_mfma_f32_16x16x32_f16 v[0:3], v[120:123], v[152:155], v[0:3]\n" \
"v_pk_add_f16 v136, v120, v128\n" \
"v_pk_add_f16 v137, v121, v129\n" \
"v_mfma_f32_16x16x32_f16 v[12:15], v[124:127], v[152:155], v[12:15]\n" \
"v_pk_add_f16 v152, v152, v168\n" \
"v_pk_add_f16 v138, v122, v130\n" \
"v_mfma_f32_16x16x32_f16 v[36:39], v[124:127], v[208:211], v[36:39]\n" \
"v_pk_add_f16 v153, v153, v169\n" \
"v_pk_add_f16 v154, v154, v170\n" \
"v_pk_add_f16 v155, v155, v171\n" \
"v_cmp_eq_u32 vcc, 0, v253\n" \
"s_cbranch_vccnz .Ls03_%=\n" \
".Lr03_%=:\n" \
"v_mfma_f32_16x16x32_f16 v[24:27], v[120:123], v[208:211], v[24:27]\n" \
"v_pk_add_f16 v208, v208, v172\n" \
"v_pk_add_f16 v139, v123, v131\n" \
"s_add_i32 m0, s44, 0x4000\n" \
"s_mov_b32 s80, 0x2800000\n" \
"v_mfma_f32_16x16x32_f16 v[48:51], v[120:123], v[156:159], v[48:51]\n" \
"v_pk_add_f16 v209, v209, v173\n" \
"v_pk_add_f16 v140, v124, v132\n" \
"buffer_load_dwordx4 v232, s[36:39], s80 offen sc1 lds\n" \
"v_mfma_f32_16x16x32_f16 v[60:63], v[124:127], v[156:159], v[60:63]\n" \
"v_pk_add_f16 v210, v210, v174\n" \
"v_pk_add_f16 v211, v211, v175\n" \
"ds_read_b128 v[148:151], v240 offset:43520\n" \
"v_mfma_f32_16x16x32_f16 v[84:87], v[124:127], v[160:163], v[84:87]\n" \
"v_pk_add_f16 v156, v156, v176\n" \
"v_pk_add_f16 v141, v125, v133\n" \
"s_add_i32 s84, s55, 0x800a000\n" \
"v_mfma_f32_16x16x32_f16 v[72:75], v[120:123], v[160:163], v[72:75]\n" \
"v_pk_add_f16 v157, v157, v177\n" \
"v_pk_add_f16 v142, v126, v134\n" \
"buffer_store_dword v240, v243, s[36:39], s84 offen sc1\n" \
"v_mfma_f32_16x16x32_f16 v[96:99], v[120:123], v[164:167], v[96:99]\n" \
"v_pk_add_f16 v158, v158, v178\n" \
"v_pk_add_f16 v159, v159, v179\n" \
"ds_read_b64_tr_b16 v[188:189], v238 offset:12288\n" \
"v_mfma_f32_16x16x32_f16 v[108:111], v[124:127], v[164:167], v[108:111]\n" \
"v_pk_add_f16 v160, v160, v180\n" \
"v_pk_add_f16 v143, v127, v135\n" \
"ds_read_b64_tr_b16 v[192:193], v239 offset:12288\n" \
"v_mfma_f32_16x16x32_f16 v[4:7], v[128:131], v[168:171], v[4:7]\n" \
"v_pk_add_f16 v161, v161, v181\n" \
"ds_read_b64_tr_b16 v[120:121], v241 offset:32768\n" \
"buffer_load_dwordx4 v232, s[36:39], s80 offen offset:1024 sc1 lds\n" \
"v_mfma_f32_16x16x32_f16 v[16:19], v[132:135], v[168:171], v[16:19]\n" \
"v_pk_add_f16 v162, v162, v182\n" \
"ds_read_b128 v[144:147], v240 offset:41472\n" \
"v_mfma_f32_16x16x32_f16 v[40:43], v[132:135], v[172:175], v[40:43]\n" \
"v_pk_add_f16 v163, v163, v183\n" \
"ds_read_b128 v[168:171], v240 offset:40960\n" \
"v_mfma_f32_16x16x32_f16 v[28:31], v[128:131], v[172:175], v[28:31]\n" \
"v_pk_add_f16 v164, v164, v184\n" \
"ds_read_b64_tr_b16 v[122:123], v241 offset:40960\n" \
"v_mfma_f32_16x16x32_f16 v[52:55], v[128:131], v[176:179], v[52:55]\n" \
"v_pk_add_f16 v165, v165, v185\n" \
"ds_read_b128 v[172:175], v240 offset:43008\n" \
"s_waitcnt lgkmcnt(6)\n" \
"v_mfma_f32_16x16x16_bf16 v[188:191], v[188:189], v[254:255], 0\n" \
"v_mfma_f32_16x16x32_f16 v[64:67], v[132:135], v[176:179], v[64:67]\n" \
"v_pk_add_f16 v166, v166, v186\n" \
"ds_read_b64_tr_b16 v[124:125], v242 offset:32768\n" \
"s_waitcnt lgkmcnt(6)\n" \
"v_mfma_f32_16x16x16_bf16 v[192:195], v[192:193], v[254:255], 0\n" \
"v_mfma_f32_16x16x32_f16 v[88:91], v[132:135], v[180:183], v[88:91]\n" \
"v_pk_add_f16 v167, v167, v187\n" \
"ds_read_b128 v[176:179], v240 offset:45056\n" \
"buffer_load_dwordx4 v232, s[36:39], s80 offen offset:2048 sc1 lds\n" \
"v_mfma_f32_16x16x32_f16 v[76:79], v[128:131], v[180:183], v[76:79]\n" \
"ds_read_b64_tr_b16 v[126:127], v242 offset:40960\n" \
"v_mfma_f32_16x16x32_f16 v[100:103], v[128:131], v[184:187], v[100:103]\n" \
"v_cvt_pk_f16_f32 v188, v188, v189\n" \
"ds_read_b128 v[180:183], v240 offset:47104\n" \
"v_mfma_f32_16x16x32_f16 v[112:115], v[132:135], v[184:187], v[112:115]\n" \
"v_cvt_pk_f16_f32 v189, v190, v191\n" \
"ds_read_b64_tr_b16 v[128:129], v241 offset:33280\n" \
"v_mfma_f32_16x16x32_f16 v[8:11], v[136:139], v[152:155], v[8:11]\n" \
"v_cvt_pk_f16_f32 v190, v192, v193\n" \
"ds_read_b128 v[184:187], v240 offset:49152\n" \
"v_mfma_f32_16x16x32_f16 v[20:23], v[140:143], v[152:155], v[20:23]\n" \
"v_cvt_pk_f16_f32 v191, v194, v195\n" \
"v_add_u32 v188, s91, v188\n" \
"ds_read_b128 v[152:155], v240 offset:45568\n" \
"v_mfma_f32_16x16x32_f16 v[44:47], v[140:143], v[208:211], v[44:47]\n" \
"ds_read_b64_tr_b16 v[130:131], v241 offset:41472\n" \
"ds_read_b64_tr_b16 v[132:133], v242 offset:33280\n" \
"buffer_load_dwordx4 v232, s[36:39], s80 offen offset:3072 sc1 lds\n" \
"v_mfma_f32_16x16x32_f16 v[32:35], v[136:139], v[208:211], v[32:35]\n" \
"v_add_u32 v189, s91, v189\n" \
"v_add_u32 v190, s91, v190\n" \
"ds_read_b128 v[208:211], v240 offset:47616\n" \
"v_mfma_f32_16x16x32_f16 v[56:59], v[136:139], v[156:159], v[56:59]\n" \
"v_add_u32 v191, s91, v191\n" \
"ds_read_b64_tr_b16 v[134:135], v242 offset:41472\n" \
"v_mfma_f32_16x16x32_f16 v[68:71], v[140:143], v[156:159], v[68:71]\n" \
"v_and_b32 v188, s92, v188\n" \
"ds_read_b128 v[156:159], v240 offset:49664\n" \
"v_mfma_f32_16x16x32_f16 v[92:95], v[140:143], v[160:163], v[92:95]\n" \
"v_and_b32 v189, s92, v189\n" \
"v_mfma_f32_16x16x32_f16 v[80:83], v[136:139], v[160:163], v[80:83]\n" \
"v_and_b32 v190, s92, v190\n" \
"v_and_b32 v191, s92, v191\n" \
"v_mfma_f32_16x16x32_f16 v[104:107], v[136:139], v[164:167], v[104:107]\n" \
"buffer_store_dwordx4 v[188:191], v234, s[36:39], s82 offen offset:1024 sc1\n" \
"v_mfma_f32_16x16x32_f16 v[116:119], v[140:143], v[164:167], v[116:119]\n" \
"s_waitcnt vmcnt(7) lgkmcnt(0)\n" \
"s_barrier\n" \
"v_mfma_f32_16x16x32_f16 v[0:3], v[120:123], v[168:171], v[0:3]\n" \
"v_pk_add_f16 v136, v120, v128\n" \
"v_pk_add_f16 v137, v121, v129\n" \
"s_mov_b32 s81, 0x8008000\n" \
"v_mfma_f32_16x16x32_f16 v[12:15], v[124:127], v[168:171], v[12:15]\n" \
"v_pk_add_f16 v168, v168, v144\n" \
"v_pk_add_f16 v138, v122, v130\n" \
"buffer_load_dword v253, v252, s[36:39], s81 offen sc1\n" \
"v_mfma_f32_16x16x32_f16 v[36:39], v[124:127], v[172:175], v[36:39]\n" \
"v_pk_add_f16 v169, v169, v145\n" \
"v_pk_add_f16 v170, v170, v146\n" \
"v_pk_add_f16 v171, v171, v147\n" \
"buffer_store_dwordx4 v[216:219], v235, s[36:39], s65 offen sc1\n" \
"v_mfma_f32_16x16x32_f16 v[24:27], v[120:123], v[172:175], v[24:27]\n" \
"v_pk_add_f16 v172, v172, v148\n" \
"v_pk_add_f16 v139, v123, v131\n" \
"s_add_i32 m0, s44, 0x8000\n" \
"s_mov_b32 s80, 0x2c00000\n" \
"v_mfma_f32_16x16x32_f16 v[48:51], v[120:123], v[176:179], v[48:51]\n" \
"v_pk_add_f16 v173, v173, v149\n" \
"v_pk_add_f16 v140, v124, v132\n" \
"buffer_load_dwordx4 v232, s[36:39], s80 offen sc1 lds\n" \
"v_mfma_f32_16x16x32_f16 v[60:63], v[124:127], v[176:179], v[60:63]\n" \
"v_pk_add_f16 v174, v174, v150\n" \
"v_pk_add_f16 v175, v175, v151\n" \
"ds_read_b128 v[164:167], v240 offset:2560\n" \
"v_mfma_f32_16x16x32_f16 v[84:87], v[124:127], v[180:183], v[84:87]\n" \
"v_pk_add_f16 v176, v176, v152\n" \
"v_pk_add_f16 v141, v125, v133\n" \
"v_mfma_f32_16x16x32_f16 v[72:75], v[120:123], v[180:183], v[72:75]\n" \
"v_pk_add_f16 v177, v177, v153\n" \
"v_pk_add_f16 v142, v126, v134\n" \
"v_mfma_f32_16x16x32_f16 v[96:99], v[120:123], v[184:187], v[96:99]\n" \
"v_pk_add_f16 v178, v178, v154\n" \
"v_pk_add_f16 v179, v179, v155\n" \
"ds_read_b64_tr_b16 v[188:189], v236 offset:16384\n" \
"v_mfma_f32_16x16x32_f16 v[108:111], v[124:127], v[184:187], v[108:111]\n" \
"v_pk_add_f16 v180, v180, v208\n" \
"v_pk_add_f16 v143, v127, v135\n" \
"ds_read_b64_tr_b16 v[192:193], v237 offset:16384\n" \
"v_mfma_f32_16x16x32_f16 v[4:7], v[128:131], v[144:147], v[4:7]\n" \
"v_pk_add_f16 v181, v181, v209\n" \
"ds_read_b64_tr_b16 v[120:121], v241 offset:0\n" \
"buffer_load_dwordx4 v232, s[36:39], s80 offen offset:1024 sc1 lds\n" \
"v_mfma_f32_16x16x32_f16 v[16:19], v[132:135], v[144:147], v[16:19]\n" \
"v_pk_add_f16 v182, v182, v210\n" \
"ds_read_b128 v[160:163], v240 offset:512\n" \
"v_mfma_f32_16x16x32_f16 v[40:43], v[132:135], v[148:151], v[40:43]\n" \
"v_pk_add_f16 v183, v183, v211\n" \
"ds_read_b128 v[144:147], v240 offset:0\n" \
"v_mfma_f32_16x16x32_f16 v[28:31], v[128:131], v[148:151], v[28:31]\n" \
"v_pk_add_f16 v184, v184, v156\n" \
"ds_read_b64_tr_b16 v[122:123], v241 offset:8192\n" \
"v_mfma_f32_16x16x32_f16 v[52:55], v[128:131], v[152:155], v[52:55]\n" \
"v_pk_add_f16 v185, v185, v157\n" \
"ds_read_b128 v[148:151], v240 offset:2048\n" \
"s_waitcnt lgkmcnt(6)\n" \
"v_mfma_f32_16x16x16_bf16 v[188:191], v[188:189], v[254:255], 0\n" \
"v_mfma_f32_16x16x32_f16 v[64:67], v[132:135], v[152:155], v[64:67]\n" \
"v_pk_add_f16 v186, v186, v158\n" \
"ds_read_b64_tr_b16 v[124:125], v242 offset:0\n" \
"s_waitcnt lgkmcnt(6)\n" \
"v_mfma_f32_16x16x16_bf16 v[192:195], v[192:193], v[254:255], 0\n" \
"v_mfma_f32_16x16x32_f16 v[88:91], v[132:135], v[208:211], v[88:91]\n" \
"v_pk_add_f16 v187, v187, v159\n" \
"ds_read_b128 v[152:155], v240 offset:4096\n" \
"buffer_load_dwordx4 v232, s[36:39], s80 offen offset:2048 sc1 lds\n" \
"v_mfma_f32_16x16x32_f16 v[76:79], v[128:131], v[208:211], v[76:79]\n" \
"ds_read_b64_tr_b16 v[126:127], v242 offset:8192\n" \
"v_mfma_f32_16x16x32_f16 v[100:103], v[128:131], v[156:159], v[100:103]\n" \
"v_cvt_pk_f16_f32 v188, v188, v189\n" \
"ds_read_b128 v[208:211], v240 offset:6144\n" \
"v_mfma_f32_16x16x32_f16 v[112:115], v[132:135], v[156:159], v[112:115]\n" \
"v_cvt_pk_f16_f32 v189, v190, v191\n" \
"ds_read_b64_tr_b16 v[128:129], v241 offset:512\n" \
"v_mfma_f32_16x16x32_f16 v[8:11], v[136:139], v[168:171], v[8:11]\n" \
"v_cvt_pk_f16_f32 v190, v192, v193\n" \
"ds_read_b128 v[156:159], v240 offset:8192\n" \
"v_mfma_f32_16x16x32_f16 v[20:23], v[140:143], v[168:171], v[20:23]\n" \
"v_cvt_pk_f16_f32 v191, v194, v195\n" \
"v_add_u32 v188, s91, v188\n" \
"ds_read_b128 v[168:171], v240 offset:4608\n" \
"v_mfma_f32_16x16x32_f16 v[44:47], v[140:143], v[172:175], v[44:47]\n" \
"ds_read_b64_tr_b16 v[130:131], v241 offset:8704\n" \
"ds_read_b64_tr_b16 v[132:133], v242 offset:512\n" \
"buffer_load_dwordx4 v232, s[36:39], s80 offen offset:3072 sc1 lds\n" \
"v_mfma_f32_16x16x32_f16 v[32:35], v[136:139], v[172:175], v[32:35]\n" \
"v_add_u32 v189, s91, v189\n" \
"v_add_u32 v190, s91, v190\n" \
"ds_read_b128 v[172:175], v240 offset:6656\n" \
"v_mfma_f32_16x16x32_f16 v[56:59], v[136:139], v[176:179], v[56:59]\n" \
"v_add_u32 v191, s91, v191\n" \
"ds_read_b64_tr_b16 v[134:135], v242 offset:8704\n" \
"v_mfma_f32_16x16x32_f16 v[68:71], v[140:143], v[176:179], v[68:71]\n" \
"v_and_b32 v188, s92, v188\n" \
"ds_read_b128 v[176:179], v240 offset:8704\n" \
"v_mfma_f32_16x16x32_f16 v[92:95], v[140:143], v[180:183], v[92:95]\n" \
"v_and_b32 v189, s92, v189\n" \
"s_add_i32 s82, s49, 0x4800000\n" \
"v_mfma_f32_16x16x32_f16 v[80:83], v[136:139], v[180:183], v[80:83]\n" \
"v_and_b32 v190, s92, v190\n" \
"v_and_b32 v191, s92, v191\n" \
"v_mfma_f32_16x16x32_f16 v[104:107], v[136:139], v[184:187], v[104:107]\n" \
"buffer_store_dwordx4 v[188:191], v234, s[36:39], s82 offen sc1\n" \
"v_mfma_f32_16x16x32_f16 v[116:119], v[140:143], v[184:187], v[116:119]\n" \
"s_waitcnt vmcnt(6) lgkmcnt(0)\n" \
"s_barrier\n" \
"v_mfma_f32_16x16x32_f16 v[0:3], v[120:123], v[144:147], v[0:3]\n" \
"v_pk_add_f16 v136, v120, v128\n" \
"v_pk_add_f16 v137, v121, v129\n" \
"v_mfma_f32_16x16x32_f16 v[12:15], v[124:127], v[144:147], v[12:15]\n" \
"v_pk_add_f16 v144, v144, v160\n" \
"v_pk_add_f16 v138, v122, v130\n" \
"v_mfma_f32_16x16x32_f16 v[36:39], v[124:127], v[148:151], v[36:39]\n" \
"v_pk_add_f16 v145, v145, v161\n" \
"v_pk_add_f16 v146, v146, v162\n" \
"v_pk_add_f16 v147, v147, v163\n" \
"v_cmp_eq_u32 vcc, 0, v253\n" \
"s_cbranch_vccnz .Ls04_%=\n" \
".Lr04_%=:\n" \
"v_mfma_f32_16x16x32_f16 v[24:27], v[120:123], v[148:151], v[24:27]\n" \
"v_pk_add_f16 v148, v148, v164\n" \
"v_pk_add_f16 v139, v123, v131\n" \
"s_add_i32 m0, s44, 0x0\n" \
"s_mov_b32 s80, 0x3000000\n" \
"v_mfma_f32_16x16x32_f16 v[48:51], v[120:123], v[152:155], v[48:51]\n" \
"v_pk_add_f16 v149, v149, v165\n" \
"v_pk_add_f16 v140, v124, v132\n" \
"buffer_load_dwordx4 v232, s[36:39], s80 offen sc1 lds\n" \
"v_mfma_f32_16x16x32_f16 v[60:63], v[124:127], v[152:155], v[60:63]\n" \
"v_pk_add_f16 v150, v150, v166\n" \
"v_pk_add_f16 v151, v151, v167\n" \
"ds_read_b128 v[184:187], v240 offset:23040\n" \
"v_mfma_f32_16x16x32_f16 v[84:87], v[124:127], v[208:211], v[84:87]\n" \
"v_pk_add_f16 v152, v152, v168\n" \
"v_pk_add_f16 v141, v125, v133\n" \
"s_add_i32 s84, s52, 0x800c000\n" \
"v_mfma_f32_16x16x32_f16 v[72:75], v[120:123], v[208:211], v[72:75]\n" \
"v_pk_add_f16 v153, v153, v169\n" \
"v_pk_add_f16 v142, v126, v134\n" \
"buffer_store_dword v240, v243, s[36:39], s84 offen sc1\n" \
"v_mfma_f32_16x16x32_f16 v[96:99], v[120:123], v[156:159], v[96:99]\n" \
"v_pk_add_f16 v154, v154, v170\n" \
"v_pk_add_f16 v155, v155, v171\n" \
"ds_read_b64_tr_b16 v[188:189], v238 offset:16384\n" \
"v_mfma_f32_16x16x32_f16 v[108:111], v[124:127], v[156:159], v[108:111]\n" \
"v_pk_add_f16 v208, v208, v172\n" \
"v_pk_add_f16 v143, v127, v135\n" \
"ds_read_b64_tr_b16 v[192:193], v239 offset:16384\n" \
"v_mfma_f32_16x16x32_f16 v[4:7], v[128:131], v[160:163], v[4:7]\n" \
"v_pk_add_f16 v209, v209, v173\n" \
"ds_read_b64_tr_b16 v[120:121], v241 offset:16384\n" \
"buffer_load_dwordx4 v232, s[36:39], s80 offen offset:1024 sc1 lds\n" \
"v_mfma_f32_16x16x32_f16 v[16:19], v[132:135], v[160:163], v[16:19]\n" \
"v_pk_add_f16 v210, v210, v174\n" \
"ds_read_b128 v[180:183], v240 offset:20992\n" \
"v_mfma_f32_16x16x32_f16 v[40:43], v[132:135], v[164:167], v[40:43]\n" \
"v_pk_add_f16 v211, v211, v175\n" \
"ds_read_b128 v[160:163], v240 offset:20480\n" \
"v_mfma_f32_16x16x32_f16 v[28:31], v[128:131], v[164:167], v[28:31]\n" \
"v_pk_add_f16 v156, v156, v176\n" \
"ds_read_b64_tr_b16 v[122:123], v241 offset:24576\n" \
"v_mfma_f32_16x16x32_f16 v[52:55], v[128:131], v[168:171], v[52:55]\n" \
"v_pk_add_f16 v157, v157, v177\n" \
"ds_read_b128 v[164:167], v240 offset:22528\n" \
"s_waitcnt lgkmcnt(6)\n" \
"v_mfma_f32_16x16x16_bf16 v[188:191], v[188:189], v[254:255], 0\n" \
"v_mfma_f32_16x16x32_f16 v[64:67], v[132:135], v[168:171], v[64:67]\n" \
"v_pk_add_f16 v158, v158, v178\n" \
"ds_read_b64_tr_b16 v[124:125], v242 offset:16384\n" \
"s_waitcnt lgkmcnt(6)\n" \
"v_mfma_f32_16x16x16_bf16 v[192:195], v[192:193], v[254:255], 0\n" \
"v_mfma_f32_16x16x32_f16 v[88:91], v[132:135], v[172:175], v[88:91]\n" \
"v_pk_add_f16 v159, v159, v179\n" \
"ds_read_b128 v[168:171], v240 offset:24576\n" \
"buffer_load_dwordx4 v232, s[36:39], s80 offen offset:2048 sc1 lds\n" \
"v_mfma_f32_16x16x32_f16 v[76:79], v[128:131], v[172:175], v[76:79]\n" \
"ds_read_b64_tr_b16 v[126:127], v242 offset:24576\n" \
"v_mfma_f32_16x16x32_f16 v[100:103], v[128:131], v[176:179], v[100:103]\n" \
"v_cvt_pk_f16_f32 v188, v188, v189\n" \
"ds_read_b128 v[172:175], v240 offset:26624\n" \
"v_mfma_f32_16x16x32_f16 v[112:115], v[132:135], v[176:179], v[112:115]\n" \
"v_cvt_pk_f16_f32 v189, v190, v191\n" \
"ds_read_b64_tr_b16 v[128:129], v241 offset:16896\n" \
"v_mfma_f32_16x16x32_f16 v[8:11], v[136:139], v[144:147], v[8:11]\n" \
"v_cvt_pk_f16_f32 v190, v192, v193\n" \
"ds_read_b128 v[176:179], v240 offset:28672\n" \
"v_mfma_f32_16x16x32_f16 v[20:23], v[140:143], v[144:147], v[20:23]\n" \
"v_cvt_pk_f16_f32 v191, v194, v195\n" \
"v_add_u32 v188, s91, v188\n" \
"ds_read_b128 v[144:147], v240 offset:25088\n" \
"v_mfma_f32_16x16x32_f16 v[44:47], v[140:143], v[148:151], v[44:47]\n" \
"ds_read_b64_tr_b16 v[130:131], v241 offset:25088\n" \
"ds_read_b64_tr_b16 v[132:133], v242 offset:16896\n" \
"buffer_load_dwordx4 v232, s[36:39], s80 offen offset:3072 sc1 lds\n" \
"v_mfma_f32_16x16x32_f16 v[32:35], v[136:139], v[148:151], v[32:35]\n" \
"v_add_u32 v189, s91, v189\n" \
"v_add_u32 v190, s91, v190\n" \
"ds_read_b128 v[148:151], v240 offset:27136\n" \
"v_mfma_f32_16x16x32_f16 v[56:59], v[136:139], v[152:155], v[56:59]\n" \
"v_add_u32 v191, s91, v191\n" \
"ds_read_b64_tr_b16 v[134:135], v242 offset:25088\n" \
"v_mfma_f32_16x16x32_f16 v[68:71], v[140:143], v[152:155], v[68:71]\n" \
"v_and_b32 v188, s92, v188\n" \
"ds_read_b128 v[152:155], v240 offset:29184\n" \
"v_mfma_f32_16x16x32_f16 v[92:95], v[140:143], v[208:211], v[92:95]\n" \
"v_and_b32 v189, s92, v189\n" \
"v_mfma_f32_16x16x32_f16 v[80:83], v[136:139], v[208:211], v[80:83]\n" \
"v_and_b32 v190, s92, v190\n" \
"v_and_b32 v191, s92, v191\n" \
"v_mfma_f32_16x16x32_f16 v[104:107], v[136:139], v[156:159], v[104:107]\n" \
"buffer_store_dwordx4 v[188:191], v234, s[36:39], s82 offen offset:1024 sc1\n" \
"v_mfma_f32_16x16x32_f16 v[116:119], v[140:143], v[156:159], v[116:119]\n" \
"s_waitcnt vmcnt(7) lgkmcnt(0)\n" \
"s_barrier\n" \
"v_mfma_f32_16x16x32_f16 v[0:3], v[120:123], v[160:163], v[0:3]\n" \
"v_pk_add_f16 v136, v120, v128\n" \
"v_pk_add_f16 v137, v121, v129\n" \
"s_mov_b32 s81, 0x800a000\n" \
"v_mfma_f32_16x16x32_f16 v[12:15], v[124:127], v[160:163], v[12:15]\n" \
"v_pk_add_f16 v160, v160, v180\n" \
"v_pk_add_f16 v138, v122, v130\n" \
"buffer_load_dword v253, v252, s[36:39], s81 offen sc1\n" \
"v_mfma_f32_16x16x32_f16 v[36:39], v[124:127], v[164:167], v[36:39]\n" \
"v_pk_add_f16 v161, v161, v181\n" \
"v_pk_add_f16 v162, v162, v182\n" \
"v_pk_add_f16 v163, v163, v183\n" \
"buffer_store_dwordx4 v[220:223], v235, s[36:39], s66 offen sc1\n" \
"v_mfma_f32_16x16x32_f16 v[24:27], v[120:123], v[164:167], v[24:27]\n" \
"v_pk_add_f16 v164, v164, v184\n" \
"v_pk_add_f16 v139, v123, v131\n" \
"s_add_i32 m0, s44, 0x4000\n" \
"s_mov_b32 s80, 0x3400000\n" \
"v_mfma_f32_16x16x32_f16 v[48:51], v[120:123], v[168:171], v[48:51]\n" \
"v_pk_add_f16 v165, v165, v185\n" \
"v_pk_add_f16 v140, v124, v132\n" \
"buffer_load_dwordx4 v232, s[36:39], s80 offen sc1 lds\n" \
"v_mfma_f32_16x16x32_f16 v[60:63], v[124:127], v[168:171], v[60:63]\n" \
"v_pk_add_f16 v166, v166, v186\n" \
"v_pk_add_f16 v167, v167, v187\n" \
"ds_read_b128 v[156:159], v240 offset:43520\n" \
"v_mfma_f32_16x16x32_f16 v[84:87], v[124:127], v[172:175], v[84:87]\n" \
"v_pk_add_f16 v168, v168, v144\n" \
"v_pk_add_f16 v141, v125, v133\n" \
"v_mfma_f32_16x16x32_f16 v[72:75], v[120:123], v[172:175], v[72:75]\n" \
"v_pk_add_f16 v169, v169, v145\n" \
"v_pk_add_f16 v142, v126, v134\n" \
"v_mfma_f32_16x16x32_f16 v[96:99], v[120:123], v[176:179], v[96:99]\n" \
"v_pk_add_f16 v170, v170, v146\n" \
"v_pk_add_f16 v171, v171, v147\n" \
"ds_read_b64_tr_b16 v[188:189], v236 offset:20480\n" \
"v_mfma_f32_16x16x32_f16 v[108:111], v[124:127], v[176:179], v[108:111]\n" \
"v_pk_add_f16 v172, v172, v148\n" \
"v_pk_add_f16 v143, v127, v135\n" \
"ds_read_b64_tr_b16 v[192:193], v237 offset:20480\n" \
"v_mfma_f32_16x16x32_f16 v[4:7], v[128:131], v[180:183], v[4:7]\n" \
"v_pk_add_f16 v173, v173, v149\n" \
"ds_read_b64_tr_b16 v[120:121], v241 offset:32768\n" \
"buffer_load_dwordx4 v232, s[36:39], s80 offen offset:1024 sc1 lds\n" \
"v_mfma_f32_16x16x32_f16 v[16:19], v[132:135], v[180:183], v[16:19]\n" \
"v_pk_add_f16 v174, v174, v150\n" \
"ds_read_b128 v[208:211], v240 offset:41472\n" \
"v_mfma_f32_16x16x32_f16 v[40:43], v[132:135], v[184:187], v[40:43]\n" \
"v_pk_add_f16 v175, v175, v151\n" \
"ds_read_b128 v[180:183], v240 offset:40960\n" \
"v_mfma_f32_16x16x32_f16 v[28:31], v[128:131], v[184:187], v[28:31]\n" \
"v_pk_add_f16 v176, v176, v152\n" \
"ds_read_b64_tr_b16 v[122:123], v241 offset:40960\n" \
"v_mfma_f32_16x16x32_f16 v[52:55], v[128:131], v[144:147], v[52:55]\n" \
"v_pk_add_f16 v177, v177, v153\n" \
"ds_read_b128 v[184:187], v240 offset:43008\n" \
"s_waitcnt lgkmcnt(6)\n" \
"v_mfma_f32_16x16x16_bf16 v[188:191], v[188:189], v[254:255], 0\n" \
"v_mfma_f32_16x16x32_f16 v[64:67], v[132:135], v[144:147], v[64:67]\n" \
"v_pk_add_f16 v178, v178, v154\n" \
"ds_read_b64_tr_b16 v[124:125], v242 offset:32768\n" \
"s_waitcnt lgkmcnt(6)\n" \
"v_mfma_f32_16x16x16_bf16 v[192:195], v[192:193], v[254:255], 0\n" \
"v_mfma_f32_16x16x32_f16 v[88:91], v[132:135], v[148:151], v[88:91]\n" \
"v_pk_add_f16 v179, v179, v155\n" \
"ds_read_b128 v[144:147], v240 offset:45056\n" \
"buffer_load_dwordx4 v232, s[36:39], s80 offen offset:2048 sc1 lds\n" \
"v_mfma_f32_16x16x32_f16 v[76:79], v[128:131], v[148:151], v[76:79]\n" \
"ds_read_b64_tr_b16 v[126:127], v242 offset:40960\n" \
"v_mfma_f32_16x16x32_f16 v[100:103], v[128:131], v[152:155], v[100:103]\n" \
"v_cvt_pk_f16_f32 v188, v188, v189\n" \
"ds_read_b128 v[148:151], v240 offset:47104\n" \
"v_mfma_f32_16x16x32_f16 v[112:115], v[132:135], v[152:155], v[112:115]\n" \
"v_cvt_pk_f16_f32 v189, v190, v191\n" \
"ds_read_b64_tr_b16 v[128:129], v241 offset:33280\n" \
"v_mfma_f32_16x16x32_f16 v[8:11], v[136:139], v[160:163], v[8:11]\n" \
"v_cvt_pk_f16_f32 v190, v192, v193\n" \
"ds_read_b128 v[152:155], v240 offset:49152\n" \
"v_mfma_f32_16x16x32_f16 v[20:23], v[140:143], v[160:163], v[20:23]\n" \
"v_cvt_pk_f16_f32 v191, v194, v195\n" \
"v_add_u32 v188, s91, v188\n" \
"ds_read_b128 v[160:163], v240 offset:45568\n" \
"v_mfma_f32_16x16x32_f16 v[44:47], v[140:143], v[164:167], v[44:47]\n" \
"ds_read_b64_tr_b16 v[130:131], v241 offset:41472\n" \
"ds_read_b64_tr_b16 v[132:133], v242 offset:33280\n" \
"buffer_load_dwordx4 v232, s[36:39], s80 offen offset:3072 sc1 lds\n" \
"v_mfma_f32_16x16x32_f16 v[32:35], v[136:139], v[164:167], v[32:35]\n" \
"v_add_u32 v189, s91, v189\n" \
"v_add_u32 v190, s91, v190\n" \
"ds_read_b128 v[164:167], v240 offset:47616\n" \
"v_mfma_f32_16x16x32_f16 v[56:59], v[136:139], v[168:171], v[56:59]\n" \
"v_add_u32 v191, s91, v191\n" \
"ds_read_b64_tr_b16 v[134:135], v242 offset:41472\n" \
"v_mfma_f32_16x16x32_f16 v[68:71], v[140:143], v[168:171], v[68:71]\n" \
"v_and_b32 v188, s92, v188\n" \
"ds_read_b128 v[168:171], v240 offset:49664\n" \
"v_mfma_f32_16x16x32_f16 v[92:95], v[140:143], v[172:175], v[92:95]\n" \
"v_and_b32 v189, s92, v189\n" \
"s_add_i32 s82, s50, 0x5000000\n" \
"v_mfma_f32_16x16x32_f16 v[80:83], v[136:139], v[172:175], v[80:83]\n" \
"v_and_b32 v190, s92, v190\n" \
"v_and_b32 v191, s92, v191\n" \
"v_mfma_f32_16x16x32_f16 v[104:107], v[136:139], v[176:179], v[104:107]\n" \
"buffer_store_dwordx4 v[188:191], v234, s[36:39], s82 offen sc1\n" \
"v_mfma_f32_16x16x32_f16 v[116:119], v[140:143], v[176:179], v[116:119]\n" \
"s_waitcnt vmcnt(6) lgkmcnt(0)\n" \
"s_barrier\n" \
"v_mfma_f32_16x16x32_f16 v[0:3], v[120:123], v[180:183], v[0:3]\n" \
"v_pk_add_f16 v136, v120, v128\n" \
"v_pk_add_f16 v137, v121, v129\n" \
"v_mfma_f32_16x16x32_f16 v[12:15], v[124:127], v[180:183], v[12:15]\n" \
"v_pk_add_f16 v180, v180, v208\n" \
"v_pk_add_f16 v138, v122, v130\n" \
"v_mfma_f32_16x16x32_f16 v[36:39], v[124:127], v[184:187], v[36:39]\n" \
"v_pk_add_f16 v181, v181, v209\n" \
"v_pk_add_f16 v182, v182, v210\n" \
"v_pk_add_f16 v183, v183, v211\n" \
"v_cmp_eq_u32 vcc, 0, v253\n" \
"s_cbranch_vccnz .Ls05_%=\n" \
".Lr05_%=:\n" \
"v_mfma_f32_16x16x32_f16 v[24:27], v[120:123], v[184:187], v[24:27]\n" \
"v_pk_add_f16 v184, v184, v156\n" \
"v_pk_add_f16 v139, v123, v131\n" \
"s_add_i32 m0, s44, 0x8000\n" \
"s_mov_b32 s80, 0x3800000\n" \
"v_mfma_f32_16x16x32_f16 v[48:51], v[120:123], v[144:147], v[48:51]\n" \
"v_pk_add_f16 v185, v185, v157\n" \
"v_pk_add_f16 v140, v124, v132\n" \
"buffer_load_dwordx4 v232, s[36:39], s80 offen sc1 lds\n" \
"v_mfma_f32_16x16x32_f16 v[60:63], v[124:127], v[144:147], v[60:63]\n" \
"v_pk_add_f16 v186, v186, v158\n" \
"v_pk_add_f16 v187, v187, v159\n" \
"ds_read_b128 v[176:179], v240 offset:2560\n" \
"v_mfma_f32_16x16x32_f16 v[84:87], v[124:127], v[148:151], v[84:87]\n" \
"v_pk_add_f16 v144, v144, v160\n" \
"v_pk_add_f16 v141, v125, v133\n" \
"s_add_i32 s84, s53, 0x800e000\n" \
"v_mfma_f32_16x16x32_f16 v[72:75], v[120:123], v[148:151], v[72:75]\n" \
"v_pk_add_f16 v145, v145, v161\n" \
"v_pk_add_f16 v142, v126, v134\n" \
"buffer_store_dword v240, v243, s[36:39], s84 offen sc1\n" \
"v_mfma_f32_16x16x32_f16 v[96:99], v[120:123], v[152:155], v[96:99]\n" \
"v_pk_add_f16 v146, v146, v162\n" \
"v_pk_add_f16 v147, v147, v163\n" \
"ds_read_b64_tr_b16 v[188:189], v238 offset:20480\n" \
"v_mfma_f32_16x16x32_f16 v[108:111], v[124:127], v[152:155], v[108:111]\n" \
"v_pk_add_f16 v148, v148, v164\n" \
"v_pk_add_f16 v143, v127, v135\n" \
"ds_read_b64_tr_b16 v[192:193], v239 offset:20480\n" \
"v_mfma_f32_16x16x32_f16 v[4:7], v[128:131], v[208:211], v[4:7]\n" \
"v_pk_add_f16 v149, v149, v165\n" \
"ds_read_b64_tr_b16 v[120:121], v241 offset:0\n" \
"buffer_load_dwordx4 v232, s[36:39], s80 offen offset:1024 sc1 lds\n" \
"v_mfma_f32_16x16x32_f16 v[16:19], v[132:135], v[208:211], v[16:19]\n" \
"v_pk_add_f16 v150, v150, v166\n" \
"ds_read_b128 v[172:175], v240 offset:512\n" \
"v_mfma_f32_16x16x32_f16 v[40:43], v[132:135], v[156:159], v[40:43]\n" \
"v_pk_add_f16 v151, v151, v167\n" \
"ds_read_b128 v[208:211], v240 offset:0\n" \
"v_mfma_f32_16x16x32_f16 v[28:31], v[128:131], v[156:159], v[28:31]\n" \
"v_pk_add_f16 v152, v152, v168\n" \
"ds_read_b64_tr_b16 v[122:123], v241 offset:8192\n" \
"v_mfma_f32_16x16x32_f16 v[52:55], v[128:131], v[160:163], v[52:55]\n" \
"v_pk_add_f16 v153, v153, v169\n" \
"ds_read_b128 v[156:159], v240 offset:2048\n" \
"s_waitcnt lgkmcnt(6)\n" \
"v_mfma_f32_16x16x16_bf16 v[188:191], v[188:189], v[254:255], 0\n" \
"v_mfma_f32_16x16x32_f16 v[64:67], v[132:135], v[160:163], v[64:67]\n" \
"v_pk_add_f16 v154, v154, v170\n" \
"ds_read_b64_tr_b16 v[124:125], v242 offset:0\n" \
"s_waitcnt lgkmcnt(6)\n" \
"v_mfma_f32_16x16x16_bf16 v[192:195], v[192:193], v[254:255], 0\n" \
"v_mfma_f32_16x16x32_f16 v[88:91], v[132:135], v[164:167], v[88:91]\n" \
"v_pk_add_f16 v155, v155, v171\n" \
"ds_read_b128 v[160:163], v240 offset:4096\n" \
"buffer_load_dwordx4 v232, s[36:39], s80 offen offset:2048 sc1 lds\n" \
"v_mfma_f32_16x16x32_f16 v[76:79], v[128:131], v[164:167], v[76:79]\n" \
"ds_read_b64_tr_b16 v[126:127], v242 offset:8192\n" \
"v_mfma_f32_16x16x32_f16 v[100:103], v[128:131], v[168:171], v[100:103]\n" \
"v_cvt_pk_f16_f32 v188, v188, v189\n" \
"ds_read_b128 v[164:167], v240 offset:6144\n" \
"v_mfma_f32_16x16x32_f16 v[112:115], v[132:135], v[168:171], v[112:115]\n" \
"v_cvt_pk_f16_f32 v189, v190, v191\n" \
"ds_read_b64_tr_b16 v[128:129], v241 offset:512\n" \
"v_mfma_f32_16x16x32_f16 v[8:11], v[136:139], v[180:183], v[8:11]\n" \
"v_cvt_pk_f16_f32 v190, v192, v193\n" \
"ds_read_b128 v[168:171], v240 offset:8192\n" \
"v_mfma_f32_16x16x32_f16 v[20:23], v[140:143], v[180:183], v[20:23]\n" \
"v_cvt_pk_f16_f32 v191, v194, v195\n" \
"v_add_u32 v188, s91, v188\n" \
"ds_read_b128 v[180:183], v240 offset:4608\n" \
"v_mfma_f32_16x16x32_f16 v[44:47], v[140:143], v[184:187], v[44:47]\n" \
"ds_read_b64_tr_b16 v[130:131], v241 offset:8704\n" \
"ds_read_b64_tr_b16 v[132:133], v242 offset:512\n" \
"buffer_load_dwordx4 v232, s[36:39], s80 offen offset:3072 sc1 lds\n" \
"v_mfma_f32_16x16x32_f16 v[32:35], v[136:139], v[184:187], v[32:35]\n" \
"v_add_u32 v189, s91, v189\n" \
"v_add_u32 v190, s91, v190\n" \
"ds_read_b128 v[184:187], v240 offset:6656\n" \
"v_mfma_f32_16x16x32_f16 v[56:59], v[136:139], v[144:147], v[56:59]\n" \
"v_add_u32 v191, s91, v191\n" \
"ds_read_b64_tr_b16 v[134:135], v242 offset:8704\n" \
"v_mfma_f32_16x16x32_f16 v[68:71], v[140:143], v[144:147], v[68:71]\n" \
"v_and_b32 v188, s92, v188\n" \
"ds_read_b128 v[144:147], v240 offset:8704\n" \
"v_mfma_f32_16x16x32_f16 v[92:95], v[140:143], v[148:151], v[92:95]\n" \
"v_and_b32 v189, s92, v189\n" \
"v_mfma_f32_16x16x32_f16 v[80:83], v[136:139], v[148:151], v[80:83]\n" \
"v_and_b32 v190, s92, v190\n" \
"v_and_b32 v191, s92, v191\n" \
"v_mfma_f32_16x16x32_f16 v[104:107], v[136:139], v[152:155], v[104:107]\n" \
"buffer_store_dwordx4 v[188:191], v234, s[36:39], s82 offen offset:1024 sc1\n" \
"v_mfma_f32_16x16x32_f16 v[116:119], v[140:143], v[152:155], v[116:119]\n" \
"s_waitcnt vmcnt(7) lgkmcnt(0)\n" \
"s_barrier\n" \
"v_mfma_f32_16x16x32_f16 v[0:3], v[120:123], v[208:211], v[0:3]\n" \
"v_pk_add_f16 v136, v120, v128\n" \
"v_pk_add_f16 v137, v121, v129\n" \
"v_mfma_f32_16x16x32_f16 v[12:15], v[124:127], v[208:211], v[12:15]\n" \
"v_pk_add_f16 v208, v208, v172\n" \
"v_pk_add_f16 v138, v122, v130\n" \
"v_mfma_f32_16x16x32_f16 v[36:39], v[124:127], v[156:159], v[36:39]\n" \
"v_pk_add_f16 v209, v209, v173\n" \
"v_pk_add_f16 v210, v210, v174\n" \
"v_pk_add_f16 v211, v211, v175\n" \
"buffer_store_dwordx4 v[224:227], v235, s[36:39], s67 offen sc1\n" \
"v_mfma_f32_16x16x32_f16 v[24:27], v[120:123], v[156:159], v[24:27]\n" \
"v_pk_add_f16 v156, v156, v176\n" \
"v_pk_add_f16 v139, v123, v131\n" \
"s_add_i32 m0, s44, 0x0\n" \
"s_mov_b32 s80, 0x3c00000\n" \
"v_mfma_f32_16x16x32_f16 v[48:51], v[120:123], v[160:163], v[48:51]\n" \
"v_pk_add_f16 v157, v157, v177\n" \
"v_pk_add_f16 v140, v124, v132\n" \
"buffer_load_dwordx4 v232, s[36:39], s80 offen sc1 lds\n" \
"v_mfma_f32_16x16x32_f16 v[60:63], v[124:127], v[160:163], v[60:63]\n" \
"v_pk_add_f16 v158, v158, v178\n" \
"v_pk_add_f16 v159, v159, v179\n" \
"ds_read_b128 v[152:155], v240 offset:23040\n" \
"v_mfma_f32_16x16x32_f16 v[84:87], v[124:127], v[164:167], v[84:87]\n" \
"v_pk_add_f16 v160, v160, v180\n" \
"v_pk_add_f16 v141, v125, v133\n" \
"v_mfma_f32_16x16x32_f16 v[72:75], v[120:123], v[164:167], v[72:75]\n" \
"v_pk_add_f16 v161, v161, v181\n" \
"v_pk_add_f16 v142, v126, v134\n" \
"v_mfma_f32_16x16x32_f16 v[96:99], v[120:123], v[168:171], v[96:99]\n" \
"v_pk_add_f16 v162, v162, v182\n" \
"v_pk_add_f16 v163, v163, v183\n" \
"ds_read_b64_tr_b16 v[188:189], v236 offset:24576\n" \
"v_mfma_f32_16x16x32_f16 v[108:111], v[124:127], v[168:171], v[108:111]\n" \
"v_pk_add_f16 v164, v164, v184\n" \
"v_pk_add_f16 v143, v127, v135\n" \
"ds_read_b64_tr_b16 v[192:193], v237 offset:24576\n" \
"v_mfma_f32_16x16x32_f16 v[4:7], v[128:131], v[172:175], v[4:7]\n" \
"v_pk_add_f16 v165, v165, v185\n" \
"ds_read_b64_tr_b16 v[120:121], v241 offset:16384\n" \
"buffer_load_dwordx4 v232, s[36:39], s80 offen offset:1024 sc1 lds\n" \
"v_mfma_f32_16x16x32_f16 v[16:19], v[132:135], v[172:175], v[16:19]\n" \
"v_pk_add_f16 v166, v166, v186\n" \
"ds_read_b128 v[148:151], v240 offset:20992\n" \
"v_mfma_f32_16x16x32_f16 v[40:43], v[132:135], v[176:179], v[40:43]\n" \
"v_pk_add_f16 v167, v167, v187\n" \
"ds_read_b128 v[172:175], v240 offset:20480\n" \
"v_mfma_f32_16x16x32_f16 v[28:31], v[128:131], v[176:179], v[28:31]\n" \
"v_pk_add_f16 v168, v168, v144\n" \
"ds_read_b64_tr_b16 v[122:123], v241 offset:24576\n" \
"v_mfma_f32_16x16x32_f16 v[52:55], v[128:131], v[180:183], v[52:55]\n" \
"v_pk_add_f16 v169, v169, v145\n" \
"ds_read_b128 v[176:179], v240 offset:22528\n" \
"s_waitcnt lgkmcnt(6)\n" \
"v_mfma_f32_16x16x16_bf16 v[188:191], v[188:189], v[254:255], 0\n" \
"v_mfma_f32_16x16x32_f16 v[64:67], v[132:135], v[180:183], v[64:67]\n" \
"v_pk_add_f16 v170, v170, v146\n" \
"ds_read_b64_tr_b16 v[124:125], v242 offset:16384\n" \
"s_waitcnt lgkmcnt(6)\n" \
"v_mfma_f32_16x16x16_bf16 v[192:195], v[192:193], v[254:255], 0\n" \
"v_mfma_f32_16x16x32_f16 v[88:91], v[132:135], v[184:187], v[88:91]\n" \
"v_pk_add_f16 v171, v171, v147\n" \
"ds_read_b128 v[180:183], v240 offset:24576\n" \
"buffer_load_dwordx4 v232, s[36:39], s80 offen offset:2048 sc1 lds\n" \
"v_mfma_f32_16x16x32_f16 v[76:79], v[128:131], v[184:187], v[76:79]\n" \
"ds_read_b64_tr_b16 v[126:127], v242 offset:24576\n" \
"v_mfma_f32_16x16x32_f16 v[100:103], v[128:131], v[144:147], v[100:103]\n" \
"v_cvt_pk_f16_f32 v188, v188, v189\n" \
"ds_read_b128 v[184:187], v240 offset:26624\n" \
"v_mfma_f32_16x16x32_f16 v[112:115], v[132:135], v[144:147], v[112:115]\n" \
"v_cvt_pk_f16_f32 v189, v190, v191\n" \
"ds_read_b64_tr_b16 v[128:129], v241 offset:16896\n" \
"v_mfma_f32_16x16x32_f16 v[8:11], v[136:139], v[208:211], v[8:11]\n" \
"v_cvt_pk_f16_f32 v190, v192, v193\n" \
"ds_read_b128 v[144:147], v240 offset:28672\n" \
"v_mfma_f32_16x16x32_f16 v[20:23], v[140:143], v[208:211], v[20:23]\n" \
"v_cvt_pk_f16_f32 v191, v194, v195\n" \
"v_add_u32 v188, s91, v188\n" \
"ds_read_b128 v[208:211], v240 offset:25088\n" \
"v_mfma_f32_16x16x32_f16 v[44:47], v[140:143], v[156:159], v[44:47]\n" \
"ds_read_b64_tr_b16 v[130:131], v241 offset:25088\n" \
"ds_read_b64_tr_b16 v[132:133], v242 offset:16896\n" \
"buffer_load_dwordx4 v232, s[36:39], s80 offen offset:3072 sc1 lds\n" \
"v_mfma_f32_16x16x32_f16 v[32:35], v[136:139], v[156:159], v[32:35]\n" \
"v_add_u32 v189, s91, v189\n" \
"v_add_u32 v190, s91, v190\n" \
"ds_read_b128 v[156:159], v240 offset:27136\n" \
"v_mfma_f32_16x16x32_f16 v[56:59], v[136:139], v[160:163], v[56:59]\n" \
"v_add_u32 v191, s91, v191\n" \
"ds_read_b64_tr_b16 v[134:135], v242 offset:25088\n" \
"v_mfma_f32_16x16x32_f16 v[68:71], v[140:143], v[160:163], v[68:71]\n" \
"v_and_b32 v188, s92, v188\n" \
"ds_read_b128 v[160:163], v240 offset:29184\n" \
"v_mfma_f32_16x16x32_f16 v[92:95], v[140:143], v[164:167], v[92:95]\n" \
"v_and_b32 v189, s92, v189\n" \
"s_add_i32 s82, s51, 0x5800000\n" \
"v_mfma_f32_16x16x32_f16 v[80:83], v[136:139], v[164:167], v[80:83]\n" \
"v_and_b32 v190, s92, v190\n" \
"v_and_b32 v191, s92, v191\n" \
"v_mfma_f32_16x16x32_f16 v[104:107], v[136:139], v[168:171], v[104:107]\n" \
"buffer_store_dwordx4 v[188:191], v234, s[36:39], s82 offen sc1\n" \
"v_mfma_f32_16x16x32_f16 v[116:119], v[140:143], v[168:171], v[116:119]\n" \
"s_waitcnt vmcnt(6) lgkmcnt(0)\n" \
"s_barrier\n" \
"v_mfma_f32_16x16x32_f16 v[0:3], v[120:123], v[172:175], v[0:3]\n" \
"v_pk_add_f16 v136, v120, v128\n" \
"v_pk_add_f16 v137, v121, v129\n" \
"v_mfma_f32_16x16x32_f16 v[12:15], v[124:127], v[172:175], v[12:15]\n" \
"v_pk_add_f16 v172, v172, v148\n" \
"v_pk_add_f16 v138, v122, v130\n" \
"v_mfma_f32_16x16x32_f16 v[36:39], v[124:127], v[176:179], v[36:39]\n" \
"v_pk_add_f16 v173, v173, v149\n" \
"v_pk_add_f16 v174, v174, v150\n" \
"v_pk_add_f16 v175, v175, v151\n" \
"v_mfma_f32_16x16x32_f16 v[24:27], v[120:123], v[176:179], v[24:27]\n" \
"v_pk_add_f16 v176, v176, v152\n" \
"v_pk_add_f16 v139, v123, v131\n" \
"s_add_i32 m0, s44, 0x4000\n" \
"s_mov_b32 s80, 0x4000000\n" \
"v_mfma_f32_16x16x32_f16 v[48:51], v[120:123], v[180:183], v[48:51]\n" \
"v_pk_add_f16 v177, v177, v153\n" \
"v_pk_add_f16 v140, v124, v132\n" \
"buffer_load_dwordx4 v232, s[36:39], s80 offen sc1 lds\n" \
"v_mfma_f32_16x16x32_f16 v[60:63], v[124:127], v[180:183], v[60:63]\n" \
"v_pk_add_f16 v178, v178, v154\n" \
"v_pk_add_f16 v179, v179, v155\n" \
"ds_read_b128 v[168:171], v240 offset:43520\n" \
"v_mfma_f32_16x16x32_f16 v[84:87], v[124:127], v[184:187], v[84:87]\n" \
"v_pk_add_f16 v180, v180, v208\n" \
"v_pk_add_f16 v141, v125, v133\n" \
"s_add_i32 s84, s54, 0x8010000\n" \
"v_mfma_f32_16x16x32_f16 v[72:75], v[120:123], v[184:187], v[72:75]\n" \
"v_pk_add_f16 v181, v181, v209\n" \
"v_pk_add_f16 v142, v126, v134\n" \
"buffer_store_dword v240, v243, s[36:39], s84 offen sc1\n" \
"v_mfma_f32_16x16x32_f16 v[96:99], v[120:123], v[144:147], v[96:99]\n" \
"v_pk_add_f16 v182, v182, v210\n" \
"v_pk_add_f16 v183, v183, v211\n" \
"ds_read_b64_tr_b16 v[188:189], v238 offset:24576\n" \
"v_mfma_f32_16x16x32_f16 v[108:111], v[124:127], v[144:147], v[108:111]\n" \
"v_pk_add_f16 v184, v184, v156\n" \
"v_pk_add_f16 v143, v127, v135\n" \
"ds_read_b64_tr_b16 v[192:193], v239 offset:24576\n" \
"v_mfma_f32_16x16x32_f16 v[4:7], v[128:131], v[148:151], v[4:7]\n" \
"v_pk_add_f16 v185, v185, v157\n" \
"ds_read_b64_tr_b16 v[120:121], v241 offset:32768\n" \
"buffer_load_dwordx4 v232, s[36:39], s80 offen offset:1024 sc1 lds\n" \
"v_mfma_f32_16x16x32_f16 v[16:19], v[132:135], v[148:151], v[16:19]\n" \
"v_pk_add_f16 v186, v186, v158\n" \
"ds_read_b128 v[164:167], v240 offset:41472\n" \
"v_mfma_f32_16x16x32_f16 v[40:43], v[132:135], v[152:155], v[40:43]\n" \
"v_pk_add_f16 v187, v187, v159\n" \
"ds_read_b128 v[148:151], v240 offset:40960\n" \
"v_mfma_f32_16x16x32_f16 v[28:31], v[128:131], v[152:155], v[28:31]\n" \
"v_pk_add_f16 v144, v144, v160\n" \
"ds_read_b64_tr_b16 v[122:123], v241 offset:40960\n" \
"v_mfma_f32_16x16x32_f16 v[52:55], v[128:131], v[208:211], v[52:55]\n" \
"v_pk_add_f16 v145, v145, v161\n" \
"ds_read_b128 v[152:155], v240 offset:43008\n" \
"s_waitcnt lgkmcnt(6)\n" \
"v_mfma_f32_16x16x16_bf16 v[188:191], v[188:189], v[254:255], 0\n" \
"v_mfma_f32_16x16x32_f16 v[64:67], v[132:135], v[208:211], v[64:67]\n" \
"v_pk_add_f16 v146, v146, v162\n" \
"ds_read_b64_tr_b16 v[124:125], v242 offset:32768\n" \
"s_waitcnt lgkmcnt(6)\n" \
"v_mfma_f32_16x16x16_bf16 v[192:195], v[192:193], v[254:255], 0\n" \
"v_mfma_f32_16x16x32_f16 v[88:91], v[132:135], v[156:159], v[88:91]\n" \
"v_pk_add_f16 v147, v147, v163\n" \
"ds_read_b128 v[208:211], v240 offset:45056\n" \
"buffer_load_dwordx4 v232, s[36:39], s80 offen offset:2048 sc1 lds\n" \
"v_mfma_f32_16x16x32_f16 v[76:79], v[128:131], v[156:159], v[76:79]\n" \
"ds_read_b64_tr_b16 v[126:127], v242 offset:40960\n" \
"v_mfma_f32_16x16x32_f16 v[100:103], v[128:131], v[160:163], v[100:103]\n" \
"v_cvt_pk_f16_f32 v188, v188, v189\n" \
"ds_read_b128 v[156:159], v240 offset:47104\n" \
"v_mfma_f32_16x16x32_f16 v[112:115], v[132:135], v[160:163], v[112:115]\n" \
"v_cvt_pk_f16_f32 v189, v190, v191\n" \
"ds_read_b64_tr_b16 v[128:129], v241 offset:33280\n" \
"v_mfma_f32_16x16x32_f16 v[8:11], v[136:139], v[172:175], v[8:11]\n" \
"v_cvt_pk_f16_f32 v190, v192, v193\n" \
"ds_read_b128 v[160:163], v240 offset:49152\n" \
"v_mfma_f32_16x16x32_f16 v[20:23], v[140:143], v[172:175], v[20:23]\n" \
"v_cvt_pk_f16_f32 v191, v194, v195\n" \
"v_add_u32 v188, s91, v188\n" \
"ds_read_b128 v[172:175], v240 offset:45568\n" \
"v_mfma_f32_16x16x32_f16 v[44:47], v[140:143], v[176:179], v[44:47]\n" \
"ds_read_b64_tr_b16 v[130:131], v241 offset:41472\n" \
"ds_read_b64_tr_b16 v[132:133], v242 offset:33280\n" \
"buffer_load_dwordx4 v232, s[36:39], s80 offen offset:3072 sc1 lds\n" \
"v_mfma_f32_16x16x32_f16 v[32:35], v[136:139], v[176:179], v[32:35]\n" \
"v_add_u32 v189, s91, v189\n" \
"v_add_u32 v190, s91, v190\n" \
"ds_read_b128 v[176:179], v240 offset:47616\n" \
"v_mfma_f32_16x16x32_f16 v[56:59], v[136:139], v[180:183], v[56:59]\n" \
"v_add_u32 v191, s91, v191\n" \
"ds_read_b64_tr_b16 v[134:135], v242 offset:41472\n" \
"v_mfma_f32_16x16x32_f16 v[68:71], v[140:143], v[180:183], v[68:71]\n" \
"v_and_b32 v188, s92, v188\n" \
"ds_read_b128 v[180:183], v240 offset:49664\n" \
"v_mfma_f32_16x16x32_f16 v[92:95], v[140:143], v[184:187], v[92:95]\n" \
"v_and_b32 v189, s92, v189\n" \
"v_mfma_f32_16x16x32_f16 v[80:83], v[136:139], v[184:187], v[80:83]\n" \
"v_and_b32 v190, s92, v190\n" \
"v_and_b32 v191, s92, v191\n" \
"v_mfma_f32_16x16x32_f16 v[104:107], v[136:139], v[144:147], v[104:107]\n" \
"buffer_store_dwordx4 v[188:191], v234, s[36:39], s82 offen offset:1024 sc1\n" \
"v_mfma_f32_16x16x32_f16 v[116:119], v[140:143], v[144:147], v[116:119]\n" \
"s_waitcnt vmcnt(7) lgkmcnt(0)\n" \
"s_barrier\n" \
"v_mfma_f32_16x16x32_f16 v[0:3], v[120:123], v[148:151], v[0:3]\n" \
"v_pk_add_f16 v136, v120, v128\n" \
"v_pk_add_f16 v137, v121, v129\n" \
"s_mov_b32 s81, 0x800e000\n" \
"v_mfma_f32_16x16x32_f16 v[12:15], v[124:127], v[148:151], v[12:15]\n" \
"v_pk_add_f16 v148, v148, v164\n" \
"v_pk_add_f16 v138, v122, v130\n" \
"buffer_load_dword v253, v252, s[36:39], s81 offen sc1\n" \
"v_mfma_f32_16x16x32_f16 v[36:39], v[124:127], v[152:155], v[36:39]\n" \
"v_pk_add_f16 v149, v149, v165\n" \
"v_pk_add_f16 v150, v150, v166\n" \
"v_pk_add_f16 v151, v151, v167\n" \
"buffer_store_dwordx4 v[228:231], v235, s[36:39], s68 offen sc1\n" \
"v_mfma_f32_16x16x32_f16 v[24:27], v[120:123], v[152:155], v[24:27]\n" \
"v_pk_add_f16 v152, v152, v168\n" \
"v_pk_add_f16 v139, v123, v131\n" \
"s_add_i32 m0, s44, 0x8000\n" \
"s_mov_b32 s80, 0x4400000\n" \
"v_mfma_f32_16x16x32_f16 v[48:51], v[120:123], v[208:211], v[48:51]\n" \
"v_pk_add_f16 v153, v153, v169\n" \
"v_pk_add_f16 v140, v124, v132\n" \
"buffer_load_dwordx4 v232, s[36:39], s80 offen sc1 lds\n" \
"v_mfma_f32_16x16x32_f16 v[60:63], v[124:127], v[208:211], v[60:63]\n" \
"v_pk_add_f16 v154, v154, v170\n" \
"v_pk_add_f16 v155, v155, v171\n" \
"ds_read_b128 v[144:147], v240 offset:2560\n" \
"v_mfma_f32_16x16x32_f16 v[84:87], v[124:127], v[156:159], v[84:87]\n" \
"v_pk_add_f16 v208, v208, v172\n" \
"v_pk_add_f16 v141, v125, v133\n" \
"v_mfma_f32_16x16x32_f16 v[72:75], v[120:123], v[156:159], v[72:75]\n" \
"v_pk_add_f16 v209, v209, v173\n" \
"v_pk_add_f16 v142, v126, v134\n" \
"v_mfma_f32_16x16x32_f16 v[96:99], v[120:123], v[160:163], v[96:99]\n" \
"v_pk_add_f16 v210, v210, v174\n" \
"v_pk_add_f16 v211, v211, v175\n" \
"ds_read_b64_tr_b16 v[188:189], v236 offset:28672\n" \
"v_mfma_f32_16x16x32_f16 v[108:111], v[124:127], v[160:163], v[108:111]\n" \
"v_pk_add_f16 v156, v156, v176\n" \
"v_pk_add_f16 v143, v127, v135\n" \
"ds_read_b64_tr_b16 v[192:193], v237 offset:28672\n" \
"v_mfma_f32_16x16x32_f16 v[4:7], v[128:131], v[164:167], v[4:7]\n" \
"v_pk_add_f16 v157, v157, v177\n" \
"ds_read_b64_tr_b16 v[120:121], v241 offset:0\n" \
"buffer_load_dwordx4 v232, s[36:39], s80 offen offset:1024 sc1 lds\n" \
"v_mfma_f32_16x16x32_f16 v[16:19], v[132:135], v[164:167], v[16:19]\n" \
"v_pk_add_f16 v158, v158, v178\n" \
"ds_read_b128 v[184:187], v240 offset:512\n" \
"v_mfma_f32_16x16x32_f16 v[40:43], v[132:135], v[168:171], v[40:43]\n" \
"v_pk_add_f16 v159, v159, v179\n" \
"ds_read_b128 v[164:167], v240 offset:0\n" \
"v_mfma_f32_16x16x32_f16 v[28:31], v[128:131], v[168:171], v[28:31]\n" \
"v_pk_add_f16 v160, v160, v180\n" \
"ds_read_b64_tr_b16 v[122:123], v241 offset:8192\n" \
"v_mfma_f32_16x16x32_f16 v[52:55], v[128:131], v[172:175], v[52:55]\n" \
"v_pk_add_f16 v161, v161, v181\n" \
"ds_read_b128 v[168:171], v240 offset:2048\n" \
"s_waitcnt lgkmcnt(6)\n" \
"v_mfma_f32_16x16x16_bf16 v[188:191], v[188:189], v[254:255], 0\n" \
"v_mfma_f32_16x16x32_f16 v[64:67], v[132:135], v[172:175], v[64:67]\n" \
"v_pk_add_f16 v162, v162, v182\n" \
"ds_read_b64_tr_b16 v[124:125], v242 offset:0\n" \
"s_waitcnt lgkmcnt(6)\n" \
"v_mfma_f32_16x16x16_bf16 v[192:195], v[192:193], v[254:255], 0\n" \
"v_mfma_f32_16x16x32_f16 v[88:91], v[132:135], v[176:179], v[88:91]\n" \
"v_pk_add_f16 v163, v163, v183\n" \
"ds_read_b128 v[172:175], v240 offset:4096\n" \
"buffer_load_dwordx4 v232, s[36:39], s80 offen offset:2048 sc1 lds\n" \
"v_mfma_f32_16x16x32_f16 v[76:79], v[128:131], v[176:179], v[76:79]\n" \
"ds_read_b64_tr_b16 v[126:127], v242 offset:8192\n" \
"v_mfma_f32_16x16x32_f16 v[100:103], v[128:131], v[180:183], v[100:103]\n" \
"v_cvt_pk_f16_f32 v188, v188, v189\n" \
"ds_read_b128 v[176:179], v240 offset:6144\n" \
"v_mfma_f32_16x16x32_f16 v[112:115], v[132:135], v[180:183], v[112:115]\n" \
"v_cvt_pk_f16_f32 v189, v190, v191\n" \
"ds_read_b64_tr_b16 v[128:129], v241 offset:512\n" \
"v_mfma_f32_16x16x32_f16 v[8:11], v[136:139], v[148:151], v[8:11]\n" \
"v_cvt_pk_f16_f32 v190, v192, v193\n" \
"ds_read_b128 v[180:183], v240 offset:8192\n" \
"v_mfma_f32_16x16x32_f16 v[20:23], v[140:143], v[148:151], v[20:23]\n" \
"v_cvt_pk_f16_f32 v191, v194, v195\n" \
"v_add_u32 v188, s91, v188\n" \
"ds_read_b128 v[148:151], v240 offset:4608\n" \
"v_mfma_f32_16x16x32_f16 v[44:47], v[140:143], v[152:155], v[44:47]\n" \
"ds_read_b64_tr_b16 v[130:131], v241 offset:8704\n" \
"ds_read_b64_tr_b16 v[132:133], v242 offset:512\n" \
"buffer_load_dwordx4 v232, s[36:39], s80 offen offset:3072 sc1 lds\n" \
"v_mfma_f32_16x16x32_f16 v[32:35], v[136:139], v[152:155], v[32:35]\n" \
"v_add_u32 v189, s91, v189\n" \
"v_add_u32 v190, s91, v190\n" \
"ds_read_b128 v[152:155], v240 offset:6656\n" \
"v_mfma_f32_16x16x32_f16 v[56:59], v[136:139], v[208:211], v[56:59]\n" \
"v_add_u32 v191, s91, v191\n" \
"ds_read_b64_tr_b16 v[134:135], v242 offset:8704\n" \
"v_mfma_f32_16x16x32_f16 v[68:71], v[140:143], v[208:211], v[68:71]\n" \
"v_and_b32 v188, s92, v188\n" \
"ds_read_b128 v[208:211], v240 offset:8704\n" \
"v_mfma_f32_16x16x32_f16 v[92:95], v[140:143], v[156:159], v[92:95]\n" \
"v_and_b32 v189, s92, v189\n" \
"s_add_i32 s82, s48, 0x6000000\n" \
"v_mfma_f32_16x16x32_f16 v[80:83], v[136:139], v[156:159], v[80:83]\n" \
"v_and_b32 v190, s92, v190\n" \
"v_and_b32 v191, s92, v191\n" \
"v_mfma_f32_16x16x32_f16 v[104:107], v[136:139], v[160:163], v[104:107]\n" \
"buffer_store_dwordx4 v[188:191], v234, s[36:39], s82 offen sc1\n" \
"v_mfma_f32_16x16x32_f16 v[116:119], v[140:143], v[160:163], v[116:119]\n" \
"s_waitcnt vmcnt(6) lgkmcnt(0)\n" \
"s_barrier\n" \
"v_mfma_f32_16x16x32_f16 v[0:3], v[120:123], v[164:167], v[0:3]\n" \
"v_pk_add_f16 v136, v120, v128\n" \
"v_pk_add_f16 v137, v121, v129\n" \
"v_mfma_f32_16x16x32_f16 v[12:15], v[124:127], v[164:167], v[12:15]\n" \
"v_pk_add_f16 v164, v164, v184\n" \
"v_pk_add_f16 v138, v122, v130\n" \
"v_mfma_f32_16x16x32_f16 v[36:39], v[124:127], v[168:171], v[36:39]\n" \
"v_pk_add_f16 v165, v165, v185\n" \
"v_pk_add_f16 v166, v166, v186\n" \
"v_pk_add_f16 v167, v167, v187\n" \
"v_cmp_eq_u32 vcc, 0, v253\n" \
"s_cbranch_vccnz .Ls07_%=\n" \
".Lr07_%=:\n" \
"v_mfma_f32_16x16x32_f16 v[24:27], v[120:123], v[168:171], v[24:27]\n" \
"v_pk_add_f16 v168, v168, v144\n" \
"v_pk_add_f16 v139, v123, v131\n" \
"s_add_i32 m0, s44, 0x0\n" \
"s_mov_b32 s80, 0x4800000\n" \
"v_mfma_f32_16x16x32_f16 v[48:51], v[120:123], v[172:175], v[48:51]\n" \
"v_pk_add_f16 v169, v169, v145\n" \
"v_pk_add_f16 v140, v124, v132\n" \
"buffer_load_dwordx4 v232, s[36:39], s80 offen sc1 lds\n" \
"v_mfma_f32_16x16x32_f16 v[60:63], v[124:127], v[172:175], v[60:63]\n" \
"v_pk_add_f16 v170, v170, v146\n" \
"v_pk_add_f16 v171, v171, v147\n" \
"ds_read_b128 v[160:163], v240 offset:23040\n" \
"v_mfma_f32_16x16x32_f16 v[84:87], v[124:127], v[176:179], v[84:87]\n" \
"v_pk_add_f16 v172, v172, v148\n" \
"v_pk_add_f16 v141, v125, v133\n" \
"s_add_i32 s84, s55, 0x8012000\n" \
"v_mfma_f32_16x16x32_f16 v[72:75], v[120:123], v[176:179], v[72:75]\n" \
"v_pk_add_f16 v173, v173, v149\n" \
"v_pk_add_f16 v142, v126, v134\n" \
"buffer_store_dword v240, v243, s[36:39], s84 offen sc1\n" \
"v_mfma_f32_16x16x32_f16 v[96:99], v[120:123], v[180:183], v[96:99]\n" \
"v_pk_add_f16 v174, v174, v150\n" \
"v_pk_add_f16 v175, v175, v151\n" \
"ds_read_b64_tr_b16 v[188:189], v238 offset:28672\n" \
"v_mfma_f32_16x16x32_f16 v[108:111], v[124:127], v[180:183], v[108:111]\n" \
"v_pk_add_f16 v176, v176, v152\n" \
"v_pk_add_f16 v143, v127, v135\n" \
"ds_read_b64_tr_b16 v[192:193], v239 offset:28672\n" \
"v_mfma_f32_16x16x32_f16 v[4:7], v[128:131], v[184:187], v[4:7]\n" \
"v_pk_add_f16 v177, v177, v153\n" \
"ds_read_b64_tr_b16 v[120:121], v241 offset:16384\n" \
"buffer_load_dwordx4 v232, s[36:39], s80 offen offset:1024 sc1 lds\n" \
"v_mfma_f32_16x16x32_f16 v[16:19], v[132:135], v[184:187], v[16:19]\n" \
"v_pk_add_f16 v178, v178, v154\n" \
"ds_read_b128 v[156:159], v240 offset:20992\n" \
"v_mfma_f32_16x16x32_f16 v[40:43], v[132:135], v[144:147], v[40:43]\n" \
"v_pk_add_f16 v179, v179, v155\n" \
"ds_read_b128 v[184:187], v240 offset:20480\n" \
"v_mfma_f32_16x16x32_f16 v[28:31], v[128:131], v[144:147], v[28:31]\n" \
"v_pk_add_f16 v180, v180, v208\n" \
"ds_read_b64_tr_b16 v[122:123], v241 offset:24576\n" \
"v_mfma_f32_16x16x32_f16 v[52:55], v[128:131], v[148:151], v[52:55]\n" \
"v_pk_add_f16 v181, v181, v209\n" \
"ds_read_b128 v[144:147], v240 offset:22528\n" \
"s_waitcnt lgkmcnt(6)\n" \
"v_mfma_f32_16x16x16_bf16 v[188:191], v[188:189], v[254:255], 0\n" \
"v_mfma_f32_16x16x32_f16 v[64:67], v[132:135], v[148:151], v[64:67]\n" \
"v_pk_add_f16 v182, v182, v210\n" \
"ds_read_b64_tr_b16 v[124:125], v242 offset:16384\n" \
"s_waitcnt lgkmcnt(6)\n" \
"v_mfma_f32_16x16x16_bf16 v[192:195], v[192:193], v[254:255], 0\n" \
"v_mfma_f32_16x16x32_f16 v[88:91], v[132:135], v[152:155], v[88:91]\n" \
"v_pk_add_f16 v183, v183, v211\n" \
"ds_read_b128 v[148:151], v240 offset:24576\n" \
"buffer_load_dwordx4 v232, s[36:39], s80 offen offset:2048 sc1 lds\n" \
"v_mfma_f32_16x16x32_f16 v[76:79], v[128:131], v[152:155], v[76:79]\n" \
"ds_read_b64_tr_b16 v[126:127], v242 offset:24576\n" \
"v_mfma_f32_16x16x32_f16 v[100:103], v[128:131], v[208:211], v[100:103]\n" \
"v_cvt_pk_f16_f32 v188, v188, v189\n" \
"ds_read_b128 v[152:155], v240 offset:26624\n" \
"v_mfma_f32_16x16x32_f16 v[112:115], v[132:135], v[208:211], v[112:115]\n" \
"v_cvt_pk_f16_f32 v189, v190, v191\n" \
"ds_read_b64_tr_b16 v[128:129], v241 offset:16896\n" \
"v_mfma_f32_16x16x32_f16 v[8:11], v[136:139], v[164:167], v[8:11]\n" \
"v_cvt_pk_f16_f32 v190, v192, v193\n" \
"ds_read_b128 v[208:211], v240 offset:28672\n" \
"v_mfma_f32_16x16x32_f16 v[20:23], v[140:143], v[164:167], v[20:23]\n" \
"v_cvt_pk_f16_f32 v191, v194, v195\n" \
"v_add_u32 v188, s91, v188\n" \
"ds_read_b128 v[164:167], v240 offset:25088\n" \
"v_mfma_f32_16x16x32_f16 v[44:47], v[140:143], v[168:171], v[44:47]\n" \
"ds_read_b64_tr_b16 v[130:131], v241 offset:25088\n" \
"ds_read_b64_tr_b16 v[132:133], v242 offset:16896\n" \
"buffer_load_dwordx4 v232, s[36:39], s80 offen offset:3072 sc1 lds\n" \
"v_mfma_f32_16x16x32_f16 v[32:35], v[136:139], v[168:171], v[32:35]\n" \
"v_add_u32 v189, s91, v189\n" \
"v_add_u32 v190, s91, v190\n" \
"ds_read_b128 v[168:171], v240 offset:27136\n" \
"v_mfma_f32_16x16x32_f16 v[56:59], v[136:139], v[172:175], v[56:59]\n" \
"v_add_u32 v191, s91, v191\n" \
"ds_read_b64_tr_b16 v[134:135], v242 offset:25088\n" \
"v_mfma_f32_16x16x32_f16 v[68:71], v[140:143], v[172:175], v[68:71]\n" \
"v_and_b32 v188, s92, v188\n" \
"ds_read_b128 v[172:175], v240 offset:29184\n" \
"v_mfma_f32_16x16x32_f16 v[92:95], v[140:143], v[176:179], v[92:95]\n" \
"v_and_b32 v189, s92, v189\n" \
"v_mfma_f32_16x16x32_f16 v[80:83], v[136:139], v[176:179], v[80:83]\n" \
"v_and_b32 v190, s92, v190\n" \
"v_and_b32 v191, s92, v191\n" \
"v_mfma_f32_16x16x32_f16 v[104:107], v[136:139], v[180:183], v[104:107]\n" \
"buffer_store_dwordx4 v[188:191], v234, s[36:39], s82 offen offset:1024 sc1\n" \
"v_mfma_f32_16x16x32_f16 v[116:119], v[140:143], v[180:183], v[116:119]\n" \
"s_waitcnt vmcnt(7) lgkmcnt(0)\n" \
"s_barrier\n" \
"v_mfma_f32_16x16x32_f16 v[0:3], v[120:123], v[184:187], v[0:3]\n" \
"v_pk_add_f16 v136, v120, v128\n" \
"v_pk_add_f16 v137, v121, v129\n" \
"s_mov_b32 s81, 0x8010000\n" \
"s_add_i32 m0, s45, 0x0\n" \
"s_mov_b32 s83, 0x0\n" \
"v_mfma_f32_16x16x32_f16 v[12:15], v[124:127], v[184:187], v[12:15]\n" \
"v_pk_add_f16 v184, v184, v156\n" \
"v_pk_add_f16 v138, v122, v130\n" \
"buffer_load_dword v253, v252, s[36:39], s81 offen sc1\n" \
"buffer_load_dwordx4 v233, s[36:39], s83 offen sc1 lds\n" \
"v_mfma_f32_16x16x32_f16 v[36:39], v[124:127], v[144:147], v[36:39]\n" \
"v_pk_add_f16 v185, v185, v157\n" \
"v_pk_add_f16 v186, v186, v158\n" \
"v_pk_add_f16 v187, v187, v159\n" \
"v_mfma_f32_16x16x32_f16 v[24:27], v[120:123], v[144:147], v[24:27]\n" \
"v_pk_add_f16 v144, v144, v160\n" \
"v_pk_add_f16 v139, v123, v131\n" \
"s_add_i32 m0, s44, 0x4000\n" \
"s_mov_b32 s80, 0x4c00000\n" \
"v_mfma_f32_16x16x32_f16 v[48:51], v[120:123], v[148:151], v[48:51]\n" \
"v_pk_add_f16 v145, v145, v161\n" \
"v_pk_add_f16 v140, v124, v132\n" \
"buffer_load_dwordx4 v232, s[36:39], s80 offen sc1 lds\n" \
"v_mfma_f32_16x16x32_f16 v[60:63], v[124:127], v[148:151], v[60:63]\n" \
"v_pk_add_f16 v146, v146, v162\n" \
"v_pk_add_f16 v147, v147, v163\n" \
"ds_read_b128 v[180:183], v240 offset:43520\n" \
"v_mfma_f32_16x16x32_f16 v[84:87], v[124:127], v[152:155], v[84:87]\n" \
"v_pk_add_f16 v148, v148, v164\n" \
"v_pk_add_f16 v141, v125, v133\n" \
"v_mfma_f32_16x16x32_f16 v[72:75], v[120:123], v[152:155], v[72:75]\n" \
"v_pk_add_f16 v149, v149, v165\n" \
"v_pk_add_f16 v142, v126, v134\n" \
"v_mfma_f32_16x16x32_f16 v[96:99], v[120:123], v[208:211], v[96:99]\n" \
"v_pk_add_f16 v150, v150, v166\n" \
"v_pk_add_f16 v151, v151, v167\n" \
"ds_read_b64_tr_b16 v[188:189], v236 offset:32768\n" \
"v_mfma_f32_16x16x32_f16 v[108:111], v[124:127], v[208:211], v[108:111]\n" \
"v_pk_add_f16 v152, v152, v168\n" \
"v_pk_add_f16 v143, v127, v135\n" \
"ds_read_b64_tr_b16 v[192:193], v237 offset:32768\n" \
"v_mfma_f32_16x16x32_f16 v[4:7], v[128:131], v[156:159], v[4:7]\n" \
"v_pk_add_f16 v153, v153, v169\n" \
"ds_read_b64_tr_b16 v[120:121], v241 offset:32768\n" \
"buffer_load_dwordx4 v232, s[36:39], s80 offen offset:1024 sc1 lds\n" \
"v_mfma_f32_16x16x32_f16 v[16:19], v[132:135], v[156:159], v[16:19]\n" \
"v_pk_add_f16 v154, v154, v170\n" \
"ds_read_b128 v[176:179], v240 offset:41472\n" \
"v_mfma_f32_16x16x32_f16 v[40:43], v[132:135], v[160:163], v[40:43]\n" \
"v_pk_add_f16 v155, v155, v171\n" \
"ds_read_b128 v[156:159], v240 offset:40960\n" \
"v_mfma_f32_16x16x32_f16 v[28:31], v[128:131], v[160:163], v[28:31]\n" \
"v_pk_add_f16 v208, v208, v172\n" \
"ds_read_b64_tr_b16 v[122:123], v241 offset:40960\n" \
"v_mfma_f32_16x16x32_f16 v[52:55], v[128:131], v[164:167], v[52:55]\n" \
"v_pk_add_f16 v209, v209, v173\n" \
"ds_read_b128 v[160:163], v240 offset:43008\n" \
"s_waitcnt lgkmcnt(6)\n" \
"v_mfma_f32_16x16x16_bf16 v[188:191], v[188:189], v[254:255], 0\n" \
"v_mfma_f32_16x16x32_f16 v[64:67], v[132:135], v[164:167], v[64:67]\n" \
"v_pk_add_f16 v210, v210, v174\n" \
"ds_read_b64_tr_b16 v[124:125], v242 offset:32768\n" \
"s_waitcnt lgkmcnt(6)\n" \
"v_mfma_f32_16x16x16_bf16 v[192:195], v[192:193], v[254:255], 0\n" \
"v_mfma_f32_16x16x32_f16 v[88:91], v[132:135], v[168:171], v[88:91]\n" \
"v_pk_add_f16 v211, v211, v175\n" \
"ds_read_b128 v[164:167], v240 offset:45056\n" \
"buffer_load_dwordx4 v232, s[36:39], s80 offen offset:2048 sc1 lds\n" \
"v_mfma_f32_16x16x32_f16 v[76:79], v[128:131], v[168:171], v[76:79]\n" \
"ds_read_b64_tr_b16 v[126:127], v242 offset:40960\n" \
"v_mfma_f32_16x16x32_f16 v[100:103], v[128:131], v[172:175], v[100:103]\n" \
"v_cvt_pk_f16_f32 v188, v188, v189\n" \
"ds_read_b128 v[168:171], v240 offset:47104\n" \
"v_mfma_f32_16x16x32_f16 v[112:115], v[132:135], v[172:175], v[112:115]\n" \
"v_cvt_pk_f16_f32 v189, v190, v191\n" \
"ds_read_b64_tr_b16 v[128:129], v241 offset:33280\n" \
"v_mfma_f32_16x16x32_f16 v[8:11], v[136:139], v[184:187], v[8:11]\n" \
"v_cvt_pk_f16_f32 v190, v192, v193\n" \
"ds_read_b128 v[172:175], v240 offset:49152\n" \
"v_mfma_f32_16x16x32_f16 v[20:23], v[140:143], v[184:187], v[20:23]\n" \
"v_cvt_pk_f16_f32 v191, v194, v195\n" \
"v_add_u32 v188, s91, v188\n" \
"ds_read_b128 v[184:187], v240 offset:45568\n" \
"v_mfma_f32_16x16x32_f16 v[44:47], v[140:143], v[144:147], v[44:47]\n" \
"ds_read_b64_tr_b16 v[130:131], v241 offset:41472\n" \
"ds_read_b64_tr_b16 v[132:133], v242 offset:33280\n" \
"buffer_load_dwordx4 v232, s[36:39], s80 offen offset:3072 sc1 lds\n" \
"v_mfma_f32_16x16x32_f16 v[32:35], v[136:139], v[144:147], v[32:35]\n" \
"v_add_u32 v189, s91, v189\n" \
"v_add_u32 v190, s91, v190\n" \
"ds_read_b128 v[144:147], v240 offset:47616\n" \
"v_mfma_f32_16x16x32_f16 v[56:59], v[136:139], v[148:151], v[56:59]\n" \
"v_add_u32 v191, s91, v191\n" \
"ds_read_b64_tr_b16 v[134:135], v242 offset:41472\n" \
"s_add_i32 m0, s45, 0x1000\n" \
"s_mov_b32 s83, 0x8000\n" \
"v_mfma_f32_16x16x32_f16 v[68:71], v[140:143], v[148:151], v[68:71]\n" \
"v_and_b32 v188, s92, v188\n" \
"ds_read_b128 v[148:151], v240 offset:49664\n" \
"buffer_load_dwordx4 v233, s[36:39], s83 offen sc1 lds\n" \
"v_mfma_f32_16x16x32_f16 v[92:95], v[140:143], v[152:155], v[92:95]\n" \
"v_and_b32 v189, s92, v189\n" \
"s_add_i32 s82, s49, 0x6800000\n" \
"v_mfma_f32_16x16x32_f16 v[80:83], v[136:139], v[152:155], v[80:83]\n" \
"v_and_b32 v190, s92, v190\n" \
"v_and_b32 v191, s92, v191\n" \
"v_mfma_f32_16x16x32_f16 v[104:107], v[136:139], v[208:211], v[104:107]\n" \
"buffer_store_dwordx4 v[188:191], v234, s[36:39], s82 offen sc1\n" \
"v_mfma_f32_16x16x32_f16 v[116:119], v[140:143], v[208:211], v[116:119]\n" \
"s_waitcnt vmcnt(7) lgkmcnt(0)\n" \
"s_barrier\n" \
"v_mfma_f32_16x16x32_f16 v[0:3], v[120:123], v[156:159], v[0:3]\n" \
"v_pk_add_f16 v136, v120, v128\n" \
"v_pk_add_f16 v137, v121, v129\n" \
"s_add_i32 m0, s45, 0x2000\n" \
"s_mov_b32 s83, 0x10000\n" \
"v_mfma_f32_16x16x32_f16 v[12:15], v[124:127], v[156:159], v[12:15]\n" \
"v_pk_add_f16 v156, v156, v176\n" \
"v_pk_add_f16 v138, v122, v130\n" \
"buffer_load_dwordx4 v233, s[36:39], s83 offen sc1 lds\n" \
"v_mfma_f32_16x16x32_f16 v[36:39], v[124:127], v[160:163], v[36:39]\n" \
"v_pk_add_f16 v157, v157, v177\n" \
"v_pk_add_f16 v158, v158, v178\n" \
"v_pk_add_f16 v159, v159, v179\n" \
"v_cmp_eq_u32 vcc, 0, v253\n" \
"s_cbranch_vccnz .Ls08_%=\n" \
".Lr08_%=:\n" \
"v_mfma_f32_16x16x32_f16 v[24:27], v[120:123], v[160:163], v[24:27]\n" \
"v_pk_add_f16 v160, v160, v180\n" \
"v_pk_add_f16 v139, v123, v131\n" \
"s_add_i32 m0, s44, 0x8000\n" \
"s_mov_b32 s80, 0x5000000\n" \
"v_mfma_f32_16x16x32_f16 v[48:51], v[120:123], v[164:167], v[48:51]\n" \
"v_pk_add_f16 v161, v161, v181\n" \
"v_pk_add_f16 v140, v124, v132\n" \
"buffer_load_dwordx4 v232, s[36:39], s80 offen sc1 lds\n" \
"v_mfma_f32_16x16x32_f16 v[60:63], v[124:127], v[164:167], v[60:63]\n" \
"v_pk_add_f16 v162, v162, v182\n" \
"v_pk_add_f16 v163, v163, v183\n" \
"ds_read_b128 v[208:211], v240 offset:2560\n" \
"v_mfma_f32_16x16x32_f16 v[84:87], v[124:127], v[168:171], v[84:87]\n" \
"v_pk_add_f16 v164, v164, v184\n" \
"v_pk_add_f16 v141, v125, v133\n" \
"s_add_i32 s84, s52, 0x8014000\n" \
"v_mfma_f32_16x16x32_f16 v[72:75], v[120:123], v[168:171], v[72:75]\n" \
"v_pk_add_f16 v165, v165, v185\n" \
"v_pk_add_f16 v142, v126, v134\n" \
"buffer_store_dword v240, v243, s[36:39], s84 offen sc1\n" \
"v_mfma_f32_16x16x32_f16 v[96:99], v[120:123], v[172:175], v[96:99]\n" \
"v_pk_add_f16 v166, v166, v186\n" \
"v_pk_add_f16 v167, v167, v187\n" \
"ds_read_b64_tr_b16 v[188:189], v238 offset:32768\n" \
"v_mfma_f32_16x16x32_f16 v[108:111], v[124:127], v[172:175], v[108:111]\n" \
"v_pk_add_f16 v168, v168, v144\n" \
"v_pk_add_f16 v143, v127, v135\n" \
"ds_read_b64_tr_b16 v[192:193], v239 offset:32768\n" \
"v_mfma_f32_16x16x32_f16 v[4:7], v[128:131], v[176:179], v[4:7]\n" \
"v_pk_add_f16 v169, v169, v145\n" \
"ds_read_b64_tr_b16 v[120:121], v241 offset:0\n" \
"buffer_load_dwordx4 v232, s[36:39], s80 offen offset:1024 sc1 lds\n" \
"v_mfma_f32_16x16x32_f16 v[16:19], v[132:135], v[176:179], v[16:19]\n" \
"v_pk_add_f16 v170, v170, v146\n" \
"ds_read_b128 v[152:155], v240 offset:512\n" \
"v_mfma_f32_16x16x32_f16 v[40:43], v[132:135], v[180:183], v[40:43]\n" \
"v_pk_add_f16 v171, v171, v147\n" \
"ds_read_b128 v[176:179], v240 offset:0\n" \
"v_mfma_f32_16x16x32_f16 v[28:31], v[128:131], v[180:183], v[28:31]\n" \
"v_pk_add_f16 v172, v172, v148\n" \
"ds_read_b64_tr_b16 v[122:123], v241 offset:8192\n" \
"v_mfma_f32_16x16x32_f16 v[52:55], v[128:131], v[184:187], v[52:55]\n" \
"v_pk_add_f16 v173, v173, v149\n" \
"ds_read_b128 v[180:183], v240 offset:2048\n" \
"s_waitcnt lgkmcnt(6)\n" \
"v_mfma_f32_16x16x16_bf16 v[188:191], v[188:189], v[254:255], 0\n" \
"v_mfma_f32_16x16x32_f16 v[64:67], v[132:135], v[184:187], v[64:67]\n" \
"v_pk_add_f16 v174, v174, v150\n" \
"ds_read_b64_tr_b16 v[124:125], v242 offset:0\n" \
"s_waitcnt lgkmcnt(6)\n" \
"v_mfma_f32_16x16x16_bf16 v[192:195], v[192:193], v[254:255], 0\n" \
"v_mfma_f32_16x16x32_f16 v[88:91], v[132:135], v[144:147], v[88:91]\n" \
"v_pk_add_f16 v175, v175, v151\n" \
"ds_read_b128 v[184:187], v240 offset:4096\n" \
"buffer_load_dwordx4 v232, s[36:39], s80 offen offset:2048 sc1 lds\n" \
"v_mfma_f32_16x16x32_f16 v[76:79], v[128:131], v[144:147], v[76:79]\n" \
"ds_read_b64_tr_b16 v[126:127], v242 offset:8192\n" \
"v_mfma_f32_16x16x32_f16 v[100:103], v[128:131], v[148:151], v[100:103]\n" \
"v_cvt_pk_f16_f32 v188, v188, v189\n" \
"ds_read_b128 v[144:147], v240 offset:6144\n" \
"v_mfma_f32_16x16x32_f16 v[112:115], v[132:135], v[148:151], v[112:115]\n" \
"v_cvt_pk_f16_f32 v189, v190, v191\n" \
"ds_read_b64_tr_b16 v[128:129], v241 offset:512\n" \
"v_mfma_f32_16x16x32_f16 v[8:11], v[136:139], v[156:159], v[8:11]\n" \
"v_cvt_pk_f16_f32 v190, v192, v193\n" \
"ds_read_b128 v[148:151], v240 offset:8192\n" \
"v_mfma_f32_16x16x32_f16 v[20:23], v[140:143], v[156:159], v[20:23]\n" \
"v_cvt_pk_f16_f32 v191, v194, v195\n" \
"v_add_u32 v188, s91, v188\n" \
"ds_read_b128 v[156:159], v240 offset:4608\n" \
"v_mfma_f32_16x16x32_f16 v[44:47], v[140:143], v[160:163], v[44:47]\n" \
"ds_read_b64_tr_b16 v[130:131], v241 offset:8704\n" \
"ds_read_b64_tr_b16 v[132:133], v242 offset:512\n" \
"buffer_load_dwordx4 v232, s[36:39], s80 offen offset:3072 sc1 lds\n" \
"v_mfma_f32_16x16x32_f16 v[32:35], v[136:139], v[160:163], v[32:35]\n" \
"v_add_u32 v189, s91, v189\n" \
"v_add_u32 v190, s91, v190\n" \
"ds_read_b128 v[160:163], v240 offset:6656\n" \
"v_mfma_f32_16x16x32_f16 v[56:59], v[136:139], v[164:167], v[56:59]\n" \
"v_add_u32 v191, s91, v191\n" \
"ds_read_b64_tr_b16 v[134:135], v242 offset:8704\n" \
"s_add_i32 m0, s45, 0x3000\n" \
"s_mov_b32 s83, 0x18000\n" \
"v_mfma_f32_16x16x32_f16 v[68:71], v[140:143], v[164:167], v[68:71]\n" \
"v_and_b32 v188, s92, v188\n" \
"ds_read_b128 v[164:167], v240 offset:8704\n" \
"buffer_load_dwordx4 v233, s[36:39], s83 offen sc1 lds\n" \
"v_mfma_f32_16x16x32_f16 v[92:95], v[140:143], v[168:171], v[92:95]\n" \
"v_and_b32 v189, s92, v189\n" \
"v_mfma_f32_16x16x32_f16 v[80:83], v[136:139], v[168:171], v[80:83]\n" \
"v_and_b32 v190, s92, v190\n" \
"v_and_b32 v191, s92, v191\n" \
"v_mfma_f32_16x16x32_f16 v[104:107], v[136:139], v[172:175], v[104:107]\n" \
"buffer_store_dwordx4 v[188:191], v234, s[36:39], s82 offen offset:1024 sc1\n" \
"v_mfma_f32_16x16x32_f16 v[116:119], v[140:143], v[172:175], v[116:119]\n" \
"s_waitcnt vmcnt(9) lgkmcnt(0)\n" \
"s_barrier\n" \
"v_mfma_f32_16x16x32_f16 v[0:3], v[120:123], v[176:179], v[0:3]\n" \
"v_pk_add_f16 v136, v120, v128\n" \
"v_pk_add_f16 v137, v121, v129\n" \
"s_mov_b32 s81, 0x8012000\n" \
"s_add_i32 m0, s45, 0x4000\n" \
"s_mov_b32 s83, 0x20000\n" \
"v_mfma_f32_16x16x32_f16 v[12:15], v[124:127], v[176:179], v[12:15]\n" \
"v_pk_add_f16 v176, v176, v152\n" \
"v_pk_add_f16 v138, v122, v130\n" \
"buffer_load_dword v253, v252, s[36:39], s81 offen sc1\n" \
"buffer_load_dwordx4 v233, s[36:39], s83 offen sc1 lds\n" \
"v_mfma_f32_16x16x32_f16 v[36:39], v[124:127], v[180:183], v[36:39]\n" \
"v_pk_add_f16 v177, v177, v153\n" \
"v_pk_add_f16 v178, v178, v154\n" \
"v_pk_add_f16 v179, v179, v155\n" \
"ds_read_b128 v[196:199], v249 offset:0\n" \
"v_mfma_f32_16x16x32_f16 v[24:27], v[120:123], v[180:183], v[24:27]\n" \
"v_pk_add_f16 v180, v180, v208\n" \
"v_pk_add_f16 v139, v123, v131\n" \
"s_add_i32 m0, s44, 0x0\n" \
"s_mov_b32 s80, 0x5400000\n" \
"v_mfma_f32_16x16x32_f16 v[48:51], v[120:123], v[184:187], v[48:51]\n" \
"v_pk_add_f16 v181, v181, v209\n" \
"v_pk_add_f16 v140, v124, v132\n" \
"buffer_load_dwordx4 v232, s[36:39], s80 offen sc1 lds\n" \
"v_mfma_f32_16x16x32_f16 v[60:63], v[124:127], v[184:187], v[60:63]\n" \
"v_pk_add_f16 v182, v182, v210\n" \
"v_pk_add_f16 v183, v183, v211\n" \
"ds_read_b128 v[172:175], v240 offset:23040\n" \
"ds_read_b64_tr_b16 v[200:201], v247 offset:0\n" \
"v_mfma_f32_16x16x32_f16 v[84:87], v[124:127], v[144:147], v[84:87]\n" \
"v_pk_add_f16 v184, v184, v156\n" \
"v_pk_add_f16 v141, v125, v133\n" \
"ds_read_b64_tr_b16 v[202:203], v247 offset:512\n" \
"v_mfma_f32_16x16x32_f16 v[72:75], v[120:123], v[144:147], v[72:75]\n" \
"v_pk_add_f16 v185, v185, v157\n" \
"v_pk_add_f16 v142, v126, v134\n" \
"v_mfma_f32_16x16x32_f16 v[96:99], v[120:123], v[148:151], v[96:99]\n" \
"v_pk_add_f16 v186, v186, v158\n" \
"v_pk_add_f16 v187, v187, v159\n" \
"ds_read_b64_tr_b16 v[188:189], v236 offset:36864\n" \
"v_mfma_f32_16x16x32_f16 v[108:111], v[124:127], v[148:151], v[108:111]\n" \
"v_pk_add_f16 v144, v144, v160\n" \
"v_pk_add_f16 v143, v127, v135\n" \
"ds_read_b64_tr_b16 v[192:193], v237 offset:36864\n" \
"v_mfma_f32_16x16x32_f16 v[4:7], v[128:131], v[152:155], v[4:7]\n" \
"v_pk_add_f16 v145, v145, v161\n" \
"ds_read_b64_tr_b16 v[120:121], v241 offset:16384\n" \
"buffer_load_dwordx4 v232, s[36:39], s80 offen offset:1024 sc1 lds\n" \
"s_waitcnt lgkmcnt(3)\n" \
"v_mfma_f32_16x16x32_bf16 v[204:207], v[200:203], v[196:199], 0\n" \
"v_mfma_f32_16x16x32_f16 v[16:19], v[132:135], v[152:155], v[16:19]\n" \
"v_pk_add_f16 v146, v146, v162\n" \
"ds_read_b128 v[168:171], v240 offset:20992\n" \
"ds_read_b128 v[196:199], v249 offset:1024\n" \
"v_mfma_f32_16x16x32_f16 v[40:43], v[132:135], v[208:211], v[40:43]\n" \
"v_pk_add_f16 v147, v147, v163\n" \
"ds_read_b128 v[152:155], v240 offset:20480\n" \
"ds_read_b64_tr_b16 v[200:201], v247 offset:4096\n" \
"v_mfma_f32_16x16x32_f16 v[28:31], v[128:131], v[208:211], v[28:31]\n" \
"v_pk_add_f16 v148, v148, v164\n" \
"ds_read_b64_tr_b16 v[122:123], v241 offset:24576\n" \
"v_mfma_f32_16x16x32_f16 v[52:55], v[128:131], v[156:159], v[52:55]\n" \
"v_pk_add_f16 v149, v149, v165\n" \
"ds_read_b128 v[208:211], v240 offset:22528\n" \
"s_waitcnt lgkmcnt(8)\n" \
"v_mfma_f32_16x16x16_bf16 v[188:191], v[188:189], v[254:255], 0\n" \
"v_mfma_f32_16x16x32_f16 v[64:67], v[132:135], v[156:159], v[64:67]\n" \
"v_pk_add_f16 v150, v150, v166\n" \
"ds_read_b64_tr_b16 v[124:125], v242 offset:16384\n" \
"s_waitcnt lgkmcnt(8)\n" \
"v_mfma_f32_16x16x16_bf16 v[192:195], v[192:193], v[254:255], 0\n" \
"v_mfma_f32_16x16x32_f16 v[88:91], v[132:135], v[160:163], v[88:91]\n" \
"v_pk_add_f16 v151, v151, v167\n" \
"ds_read_b128 v[156:159], v240 offset:24576\n" \
"buffer_load_dwordx4 v232, s[36:39], s80 offen offset:2048 sc1 lds\n" \
"v_mfma_f32_16x16x32_f16 v[76:79], v[128:131], v[160:163], v[76:79]\n" \
"ds_read_b64_tr_b16 v[126:127], v242 offset:24576\n" \
"ds_read_b64_tr_b16 v[202:203], v247 offset:4608\n" \
"v_mfma_f32_16x16x32_f16 v[100:103], v[128:131], v[164:167], v[100:103]\n" \
"v_cvt_pk_f16_f32 v188, v188, v189\n" \
"ds_read_b128 v[160:163], v240 offset:26624\n" \
"v_mfma_f32_16x16x32_f16 v[112:115], v[132:135], v[164:167], v[112:115]\n" \
"v_cvt_pk_f16_f32 v189, v190, v191\n" \
"ds_read_b64_tr_b16 v[128:129], v241 offset:16896\n" \
"v_mfma_f32_16x16x32_f16 v[8:11], v[136:139], v[176:179], v[8:11]\n" \
"v_cvt_pk_f16_f32 v190, v192, v193\n" \
"ds_read_b128 v[164:167], v240 offset:28672\n" \
"v_mfma_f32_16x16x32_f16 v[20:23], v[140:143], v[176:179], v[20:23]\n" \
"v_cvt_pk_f16_f32 v191, v194, v195\n" \
"v_add_u32 v188, s91, v188\n" \
"ds_read_b128 v[176:179], v240 offset:25088\n" \
"v_mfma_f32_16x16x32_f16 v[44:47], v[140:143], v[180:183], v[44:47]\n" \
"ds_read_b64_tr_b16 v[130:131], v241 offset:25088\n" \
"ds_read_b64_tr_b16 v[132:133], v242 offset:16896\n" \
"buffer_load_dwordx4 v232, s[36:39], s80 offen offset:3072 sc1 lds\n" \
"v_mfma_f32_16x16x32_f16 v[32:35], v[136:139], v[180:183], v[32:35]\n" \
"v_add_u32 v189, s91, v189\n" \
"v_add_u32 v190, s91, v190\n" \
"ds_read_b128 v[180:183], v240 offset:27136\n" \
"v_mfma_f32_16x16x32_f16 v[56:59], v[136:139], v[184:187], v[56:59]\n" \
"v_add_u32 v191, s91, v191\n" \
"ds_read_b64_tr_b16 v[134:135], v242 offset:25088\n" \
"s_add_i32 m0, s45, 0x5000\n" \
"s_mov_b32 s83, 0x28000\n" \
"s_waitcnt lgkmcnt(7)\n" \
"v_mfma_f32_16x16x32_bf16 v[204:207], v[200:203], v[196:199], v[204:207]\n" \
"v_mfma_f32_16x16x32_f16 v[68:71], v[140:143], v[184:187], v[68:71]\n" \
"v_and_b32 v188, s92, v188\n" \
"ds_read_b128 v[184:187], v240 offset:29184\n" \
"buffer_load_dwordx4 v233, s[36:39], s83 offen sc1 lds\n" \
"v_mfma_f32_16x16x32_f16 v[92:95], v[140:143], v[144:147], v[92:95]\n" \
"v_and_b32 v189, s92, v189\n" \
"s_add_i32 s82, s50, 0x7000000\n" \
"v_mfma_f32_16x16x32_f16 v[80:83], v[136:139], v[144:147], v[80:83]\n" \
"v_and_b32 v190, s92, v190\n" \
"v_and_b32 v191, s92, v191\n" \
"v_mfma_f32_16x16x32_f16 v[104:107], v[136:139], v[148:151], v[104:107]\n" \
"buffer_store_dwordx4 v[188:191], v234, s[36:39], s82 offen sc1\n" \
"v_mfma_f32_16x16x32_f16 v[116:119], v[140:143], v[148:151], v[116:119]\n" \
"s_waitcnt vmcnt(7) lgkmcnt(0)\n" \
"s_barrier\n" \
"v_mfma_f32_16x16x32_f16 v[0:3], v[120:123], v[152:155], v[0:3]\n" \
"v_pk_add_f16 v136, v120, v128\n" \
"v_pk_add_f16 v137, v121, v129\n" \
"s_add_i32 m0, s45, 0x0\n" \
"s_mov_b32 s83, 0x30000\n" \
"v_mfma_f32_16x16x32_f16 v[12:15], v[124:127], v[152:155], v[12:15]\n" \
"v_pk_add_f16 v152, v152, v168\n" \
"v_pk_add_f16 v138, v122, v130\n" \
"buffer_load_dwordx4 v233, s[36:39], s83 offen sc1 lds\n" \
"v_mfma_f32_16x16x32_f16 v[36:39], v[124:127], v[208:211], v[36:39]\n" \
"v_pk_add_f16 v153, v153, v169\n" \
"v_pk_add_f16 v154, v154, v170\n" \
"v_pk_add_f16 v155, v155, v171\n" \
"v_cmp_eq_u32 vcc, 0, v253\n" \
"s_cbranch_vccnz .Ls09_%=\n" \
".Lr09_%=:\n" \
"v_mfma_f32_16x16x32_f16 v[24:27], v[120:123], v[208:211], v[24:27]\n" \
"v_pk_add_f16 v208, v208, v172\n" \
"v_pk_add_f16 v139, v123, v131\n" \
"s_add_i32 m0, s44, 0x4000\n" \
"s_mov_b32 s80, 0x5800000\n" \
"ds_read_b128 v[196:199], v249 offset:2048\n" \
"v_mfma_f32_16x16x32_f16 v[48:51], v[120:123], v[156:159], v[48:51]\n" \
"v_pk_add_f16 v209, v209, v173\n" \
"v_pk_add_f16 v140, v124, v132\n" \
"buffer_load_dwordx4 v232, s[36:39], s80 offen sc1 lds\n" \
"v_mfma_f32_16x16x32_f16 v[60:63], v[124:127], v[156:159], v[60:63]\n" \
"v_pk_add_f16 v210, v210, v174\n" \
"v_pk_add_f16 v211, v211, v175\n" \
"ds_read_b128 v[148:151], v240 offset:43520\n" \
"ds_read_b64_tr_b16 v[200:201], v247 offset:8192\n" \
"v_mfma_f32_16x16x32_f16 v[84:87], v[124:127], v[160:163], v[84:87]\n" \
"v_pk_add_f16 v156, v156, v176\n" \
"v_pk_add_f16 v141, v125, v133\n" \
"s_add_i32 s84, s53, 0x8016000\n" \
"ds_read_b64_tr_b16 v[202:203], v247 offset:8704\n" \
"v_mfma_f32_16x16x32_f16 v[72:75], v[120:123], v[160:163], v[72:75]\n" \
"v_pk_add_f16 v157, v157, v177\n" \
"v_pk_add_f16 v142, v126, v134\n" \
"buffer_store_dword v240, v243, s[36:39], s84 offen sc1\n" \
"v_mfma_f32_16x16x32_f16 v[96:99], v[120:123], v[164:167], v[96:99]\n" \
"v_pk_add_f16 v158, v158, v178\n" \
"v_pk_add_f16 v159, v159, v179\n" \
"ds_read_b64_tr_b16 v[188:189], v238 offset:36864\n" \
"v_mfma_f32_16x16x32_f16 v[108:111], v[124:127], v[164:167], v[108:111]\n" \
"v_pk_add_f16 v160, v160, v180\n" \
"v_pk_add_f16 v143, v127, v135\n" \
"ds_read_b64_tr_b16 v[192:193], v239 offset:36864\n" \
"v_mfma_f32_16x16x32_f16 v[4:7], v[128:131], v[168:171], v[4:7]\n" \
"v_pk_add_f16 v161, v161, v181\n" \
"ds_read_b64_tr_b16 v[120:121], v241 offset:32768\n" \
"buffer_load_dwordx4 v232, s[36:39], s80 offen offset:1024 sc1 lds\n" \
"s_waitcnt lgkmcnt(3)\n" \
"v_mfma_f32_16x16x32_bf16 v[204:207], v[200:203], v[196:199], v[204:207]\n" \
"v_mfma_f32_16x16x32_f16 v[16:19], v[132:135], v[168:171], v[16:19]\n" \
"v_pk_add_f16 v162, v162, v182\n" \
"ds_read_b128 v[144:147], v240 offset:41472\n" \
"ds_read_b128 v[196:199], v249 offset:3072\n" \
"v_mfma_f32_16x16x32_f16 v[40:43], v[132:135], v[172:175], v[40:43]\n" \
"v_pk_add_f16 v163, v163, v183\n" \
"ds_read_b128 v[168:171], v240 offset:40960\n" \
"ds_read_b64_tr_b16 v[200:201], v247 offset:12288\n" \
"v_mfma_f32_16x16x32_f16 v[28:31], v[128:131], v[172:175], v[28:31]\n" \
"v_pk_add_f16 v164, v164, v184\n" \
"ds_read_b64_tr_b16 v[122:123], v241 offset:40960\n" \
"v_mfma_f32_16x16x32_f16 v[52:55], v[128:131], v[176:179], v[52:55]\n" \
"v_pk_add_f16 v165, v165, v185\n" \
"ds_read_b128 v[172:175], v240 offset:43008\n" \
"s_waitcnt lgkmcnt(8)\n" \
"v_mfma_f32_16x16x16_bf16 v[188:191], v[188:189], v[254:255], 0\n" \
"v_mfma_f32_16x16x32_f16 v[64:67], v[132:135], v[176:179], v[64:67]\n" \
"v_pk_add_f16 v166, v166, v186\n" \
"ds_read_b64_tr_b16 v[124:125], v242 offset:32768\n" \
"s_waitcnt lgkmcnt(8)\n" \
"v_mfma_f32_16x16x16_bf16 v[192:195], v[192:193], v[254:255], 0\n" \
"v_mfma_f32_16x16x32_f16 v[88:91], v[132:135], v[180:183], v[88:91]\n" \
"v_pk_add_f16 v167, v167, v187\n" \
"ds_read_b128 v[176:179], v240 offset:45056\n" \
"buffer_load_dwordx4 v232, s[36:39], s80 offen offset:2048 sc1 lds\n" \
"v_mfma_f32_16x16x32_f16 v[76:79], v[128:131], v[180:183], v[76:79]\n" \
"ds_read_b64_tr_b16 v[126:127], v242 offset:40960\n" \
"ds_read_b64_tr_b16 v[202:203], v247 offset:12800\n" \
"v_mfma_f32_16x16x32_f16 v[100:103], v[128:131], v[184:187], v[100:103]\n" \
"v_cvt_pk_f16_f32 v188, v188, v189\n" \
"ds_read_b128 v[180:183], v240 offset:47104\n" \
"v_mfma_f32_16x16x32_f16 v[112:115], v[132:135], v[184:187], v[112:115]\n" \
"v_cvt_pk_f16_f32 v189, v190, v191\n" \
"ds_read_b64_tr_b16 v[128:129], v241 offset:33280\n" \
"v_mfma_f32_16x16x32_f16 v[8:11], v[136:139], v[152:155], v[8:11]\n" \
"v_cvt_pk_f16_f32 v190, v192, v193\n" \
"ds_read_b128 v[184:187], v240 offset:49152\n" \
"v_mfma_f32_16x16x32_f16 v[20:23], v[140:143], v[152:155], v[20:23]\n" \
"v_cvt_pk_f16_f32 v191, v194, v195\n" \
"v_add_u32 v188, s91, v188\n" \
"ds_read_b128 v[152:155], v240 offset:45568\n" \
"v_mfma_f32_16x16x32_f16 v[44:47], v[140:143], v[208:211], v[44:47]\n" \
"ds_read_b64_tr_b16 v[130:131], v241 offset:41472\n" \
"ds_read_b64_tr_b16 v[132:133], v242 offset:33280\n" \
"buffer_load_dwordx4 v232, s[36:39], s80 offen offset:3072 sc1 lds\n" \
"v_mfma_f32_16x16x32_f16 v[32:35], v[136:139], v[208:211], v[32:35]\n" \
"v_add_u32 v189, s91, v189\n" \
"v_add_u32 v190, s91, v190\n" \
"ds_read_b128 v[208:211], v240 offset:47616\n" \
"v_mfma_f32_16x16x32_f16 v[56:59], v[136:139], v[156:159], v[56:59]\n" \
"v_add_u32 v191, s91, v191\n" \
"ds_read_b64_tr_b16 v[134:135], v242 offset:41472\n" \
"s_add_i32 m0, s45, 0x1000\n" \
"s_mov_b32 s83, 0x38000\n" \
"s_waitcnt lgkmcnt(7)\n" \
"v_mfma_f32_16x16x32_bf16 v[204:207], v[200:203], v[196:199], v[204:207]\n" \
"v_mfma_f32_16x16x32_f16 v[68:71], v[140:143], v[156:159], v[68:71]\n" \
"v_and_b32 v188, s92, v188\n" \
"ds_read_b128 v[156:159], v240 offset:49664\n" \
"buffer_load_dwordx4 v233, s[36:39], s83 offen sc1 lds\n" \
"v_mfma_f32_16x16x32_f16 v[92:95], v[140:143], v[160:163], v[92:95]\n" \
"v_and_b32 v189, s92, v189\n" \
"v_mfma_f32_16x16x32_f16 v[80:83], v[136:139], v[160:163], v[80:83]\n" \
"v_and_b32 v190, s92, v190\n" \
"v_and_b32 v191, s92, v191\n" \
"v_mfma_f32_16x16x32_f16 v[104:107], v[136:139], v[164:167], v[104:107]\n" \
"buffer_store_dwordx4 v[188:191], v234, s[36:39], s82 offen offset:1024 sc1\n" \
"v_mfma_f32_16x16x32_f16 v[116:119], v[140:143], v[164:167], v[116:119]\n" \
"s_waitcnt vmcnt(9) lgkmcnt(0)\n" \
"s_barrier\n" \
"v_mfma_f32_16x16x32_f16 v[0:3], v[120:123], v[168:171], v[0:3]\n" \
"v_pk_add_f16 v136, v120, v128\n" \
"v_pk_add_f16 v137, v121, v129\n" \
"ds_read_b64_tr_b16 v[188:189], v236 offset:40960\n" \
"s_add_i32 m0, s45, 0x2000\n" \
"s_mov_b32 s83, 0x40000\n" \
"v_mfma_f32_16x16x32_f16 v[12:15], v[124:127], v[168:171], v[12:15]\n" \
"v_pk_add_f16 v168, v168, v144\n" \
"v_pk_add_f16 v138, v122, v130\n" \
"ds_read_b64_tr_b16 v[192:193], v237 offset:40960\n" \
"buffer_load_dwordx4 v233, s[36:39], s83 offen sc1 lds\n" \
"v_mfma_f32_16x16x32_f16 v[36:39], v[124:127], v[172:175], v[36:39]\n" \
"v_pk_add_f16 v169, v169, v145\n" \
"v_pk_add_f16 v170, v170, v146\n" \
"v_pk_add_f16 v171, v171, v147\n" \
"ds_read_b128 v[196:199], v249 offset:4096\n" \
"v_mfma_f32_16x16x32_f16 v[24:27], v[120:123], v[172:175], v[24:27]\n" \
"v_pk_add_f16 v172, v172, v148\n" \
"v_pk_add_f16 v139, v123, v131\n" \
"s_add_i32 m0, s44, 0x8000\n" \
"s_mov_b32 s80, 0x5c00000\n" \
"v_mfma_f32_16x16x32_f16 v[48:51], v[120:123], v[176:179], v[48:51]\n" \
"v_pk_add_f16 v173, v173, v149\n" \
"v_pk_add_f16 v140, v124, v132\n" \
"buffer_load_dwordx4 v232, s[36:39], s80 offen sc1 lds\n" \
"v_mfma_f32_16x16x32_f16 v[60:63], v[124:127], v[176:179], v[60:63]\n" \
"v_pk_add_f16 v174, v174, v150\n" \
"v_pk_add_f16 v175, v175, v151\n" \
"ds_read_b128 v[164:167], v240 offset:2560\n" \
"ds_read_b64_tr_b16 v[200:201], v247 offset:16384\n" \
"s_waitcnt lgkmcnt(4)\n" \
"v_mfma_f32_16x16x16_bf16 v[188:191], v[188:189], v[254:255], 0\n" \
"v_mfma_f32_16x16x32_f16 v[84:87], v[124:127], v[180:183], v[84:87]\n" \
"v_pk_add_f16 v176, v176, v152\n" \
"v_pk_add_f16 v141, v125, v133\n" \
"ds_read_b64_tr_b16 v[202:203], v247 offset:16896\n" \
"s_waitcnt lgkmcnt(4)\n" \
"v_mfma_f32_16x16x16_bf16 v[192:195], v[192:193], v[254:255], 0\n" \
"v_mfma_f32_16x16x32_f16 v[72:75], v[120:123], v[180:183], v[72:75]\n" \
"v_pk_add_f16 v177, v177, v153\n" \
"v_pk_add_f16 v142, v126, v134\n" \
"v_mfma_f32_16x16x32_f16 v[96:99], v[120:123], v[184:187], v[96:99]\n" \
"v_pk_add_f16 v178, v178, v154\n" \
"v_pk_add_f16 v179, v179, v155\n" \
"v_mfma_f32_16x16x32_f16 v[108:111], v[124:127], v[184:187], v[108:111]\n" \
"v_pk_add_f16 v180, v180, v208\n" \
"v_pk_add_f16 v143, v127, v135\n" \
"v_cvt_pk_f16_f32 v188, v188, v189\n" \
"v_cvt_pk_f16_f32 v189, v190, v191\n" \
"v_mfma_f32_16x16x32_f16 v[4:7], v[128:131], v[144:147], v[4:7]\n" \
"v_pk_add_f16 v181, v181, v209\n" \
"v_cvt_pk_f16_f32 v190, v192, v193\n" \
"ds_read_b64_tr_b16 v[120:121], v241 offset:0\n" \
"buffer_load_dwordx4 v232, s[36:39], s80 offen offset:1024 sc1 lds\n" \
"s_waitcnt lgkmcnt(1)\n" \
"v_mfma_f32_16x16x32_bf16 v[204:207], v[200:203], v[196:199], v[204:207]\n" \
"v_mfma_f32_16x16x32_f16 v[16:19], v[132:135], v[144:147], v[16:19]\n" \
"v_pk_add_f16 v182, v182, v210\n" \
"v_cvt_pk_f16_f32 v191, v194, v195\n" \
"v_add_u32 v188, s91, v188\n" \
"ds_read_b128 v[160:163], v240 offset:512\n" \
"ds_read_b128 v[196:199], v249 offset:5120\n" \
"v_mfma_f32_16x16x32_f16 v[40:43], v[132:135], v[148:151], v[40:43]\n" \
"v_pk_add_f16 v183, v183, v211\n" \
"v_add_u32 v189, s91, v189\n" \
"v_add_u32 v190, s91, v190\n" \
"ds_read_b128 v[144:147], v240 offset:0\n" \
"v_mfma_f32_16x16x32_f16 v[28:31], v[128:131], v[148:151], v[28:31]\n" \
"v_pk_add_f16 v184, v184, v156\n" \
"v_add_u32 v191, s91, v191\n" \
"v_and_b32 v188, s92, v188\n" \
"ds_read_b64_tr_b16 v[122:123], v241 offset:8192\n" \
"s_add_i32 s82, s51, 0x7800000\n" \
"v_mfma_f32_16x16x32_f16 v[52:55], v[128:131], v[152:155], v[52:55]\n" \
"v_pk_add_f16 v185, v185, v157\n" \
"v_and_b32 v189, s92, v189\n" \
"v_and_b32 v190, s92, v190\n" \
"v_and_b32 v191, s92, v191\n" \
"ds_read_b128 v[148:151], v240 offset:2048\n" \
"v_mfma_f32_16x16x32_f16 v[64:67], v[132:135], v[152:155], v[64:67]\n" \
"v_pk_add_f16 v186, v186, v158\n" \
"ds_read_b64_tr_b16 v[124:125], v242 offset:0\n" \
"buffer_store_dwordx4 v[188:191], v234, s[36:39], s82 offen sc1\n" \
"ds_read_b64_tr_b16 v[200:201], v247 offset:20480\n" \
"v_mfma_f32_16x16x32_f16 v[88:91], v[132:135], v[208:211], v[88:91]\n" \
"v_pk_add_f16 v187, v187, v159\n" \
"ds_read_b128 v[152:155], v240 offset:4096\n" \
"buffer_load_dwordx4 v232, s[36:39], s80 offen offset:2048 sc1 lds\n" \
"v_mfma_f32_16x16x32_f16 v[76:79], v[128:131], v[208:211], v[76:79]\n" \
"ds_read_b64_tr_b16 v[126:127], v242 offset:8192\n" \
"ds_read_b64_tr_b16 v[188:189], v238 offset:40960\n" \
"ds_read_b64_tr_b16 v[202:203], v247 offset:20992\n" \
"v_mfma_f32_16x16x32_f16 v[100:103], v[128:131], v[156:159], v[100:103]\n" \
"ds_read_b128 v[208:211], v240 offset:6144\n" \
"ds_read_b64_tr_b16 v[192:193], v239 offset:40960\n" \
"v_mfma_f32_16x16x32_f16 v[112:115], v[132:135], v[156:159], v[112:115]\n" \
"ds_read_b64_tr_b16 v[128:129], v241 offset:512\n" \
"v_mfma_f32_16x16x32_f16 v[8:11], v[136:139], v[168:171], v[8:11]\n" \
"ds_read_b128 v[156:159], v240 offset:8192\n" \
"s_waitcnt lgkmcnt(5)\n" \
"v_mfma_f32_16x16x16_bf16 v[188:191], v[188:189], v[254:255], 0\n" \
"v_mfma_f32_16x16x32_f16 v[20:23], v[140:143], v[168:171], v[20:23]\n" \
"ds_read_b128 v[168:171], v240 offset:4608\n" \
"s_waitcnt lgkmcnt(3)\n" \
"v_mfma_f32_16x16x16_bf16 v[192:195], v[192:193], v[254:255], 0\n" \
"v_mfma_f32_16x16x32_f16 v[44:47], v[140:143], v[172:175], v[44:47]\n" \
"ds_read_b64_tr_b16 v[130:131], v241 offset:8704\n" \
"ds_read_b64_tr_b16 v[132:133], v242 offset:512\n" \
"buffer_load_dwordx4 v232, s[36:39], s80 offen offset:3072 sc1 lds\n" \
"v_mfma_f32_16x16x32_f16 v[32:35], v[136:139], v[172:175], v[32:35]\n" \
"ds_read_b128 v[172:175], v240 offset:6656\n" \
"v_mfma_f32_16x16x32_f16 v[56:59], v[136:139], v[176:179], v[56:59]\n" \
"v_cvt_pk_f16_f32 v188, v188, v189\n" \
"v_cvt_pk_f16_f32 v189, v190, v191\n" \
"ds_read_b64_tr_b16 v[134:135], v242 offset:8704\n" \
"s_add_i32 m0, s45, 0x3000\n" \
"s_mov_b32 s83, 0x48000\n" \
"v_mfma_f32_16x16x32_bf16 v[204:207], v[200:203], v[196:199], v[204:207]\n" \
"v_mfma_f32_16x16x32_f16 v[68:71], v[140:143], v[176:179], v[68:71]\n" \
"v_cvt_pk_f16_f32 v190, v192, v193\n" \
"v_cvt_pk_f16_f32 v191, v194, v195\n" \
"ds_read_b128 v[176:179], v240 offset:8704\n" \
"buffer_load_dwordx4 v233, s[36:39], s83 offen sc1 lds\n" \
"v_mfma_f32_16x16x32_f16 v[92:95], v[140:143], v[180:183], v[92:95]\n" \
"v_add_u32 v188, s91, v188\n" \
"v_add_u32 v189, s91, v189\n" \
"v_add_u32 v190, s91, v190\n" \
"v_mfma_f32_16x16x32_f16 v[80:83], v[136:139], v[180:183], v[80:83]\n" \
"v_add_u32 v191, s91, v191\n" \
"v_and_b32 v188, s92, v188\n" \
"v_mfma_f32_16x16x32_f16 v[104:107], v[136:139], v[184:187], v[104:107]\n" \
"v_and_b32 v189, s92, v189\n" \
"v_and_b32 v190, s92, v190\n" \
"v_and_b32 v191, s92, v191\n" \
"v_mfma_f32_16x16x32_f16 v[116:119], v[140:143], v[184:187], v[116:119]\n" \
"buffer_store_dwordx4 v[188:191], v234, s[36:39], s82 offen offset:1024 sc1\n" \
"s_waitcnt vmcnt(8) lgkmcnt(0)\n" \
"s_barrier\n" \
"v_mfma_f32_16x16x32_f16 v[0:3], v[120:123], v[144:147], v[0:3]\n" \
"v_pk_add_f16 v136, v120, v128\n" \
"v_pk_add_f16 v137, v121, v129\n" \
"s_add_i32 m0, s45, 0x4000\n" \
"s_mov_b32 s83, 0x50000\n" \
"v_mfma_f32_16x16x32_f16 v[12:15], v[124:127], v[144:147], v[12:15]\n" \
"v_pk_add_f16 v144, v144, v160\n" \
"v_pk_add_f16 v138, v122, v130\n" \
"buffer_load_dwordx4 v233, s[36:39], s83 offen sc1 lds\n" \
"v_mfma_f32_16x16x32_f16 v[36:39], v[124:127], v[148:151], v[36:39]\n" \
"v_pk_add_f16 v145, v145, v161\n" \
"v_pk_add_f16 v146, v146, v162\n" \
"v_pk_add_f16 v147, v147, v163\n" \
"ds_read_b128 v[196:199], v249 offset:6144\n" \
"v_mfma_f32_16x16x32_f16 v[24:27], v[120:123], v[148:151], v[24:27]\n" \
"v_pk_add_f16 v148, v148, v164\n" \
"v_pk_add_f16 v139, v123, v131\n" \
"s_add_i32 m0, s44, 0x0\n" \
"s_mov_b32 s80, 0x6000000\n" \
"v_mfma_f32_16x16x32_f16 v[48:51], v[120:123], v[152:155], v[48:51]\n" \
"v_pk_add_f16 v149, v149, v165\n" \
"v_pk_add_f16 v140, v124, v132\n" \
"buffer_load_dwordx4 v232, s[36:39], s80 offen sc1 lds\n" \
"v_mfma_f32_16x16x32_f16 v[60:63], v[124:127], v[152:155], v[60:63]\n" \
"v_pk_add_f16 v150, v150, v166\n" \
"v_pk_add_f16 v151, v151, v167\n" \
"ds_read_b128 v[184:187], v240 offset:23040\n" \
"ds_read_b64_tr_b16 v[200:201], v247 offset:0\n" \
"v_mfma_f32_16x16x32_f16 v[84:87], v[124:127], v[208:211], v[84:87]\n" \
"v_pk_add_f16 v152, v152, v168\n" \
"v_pk_add_f16 v141, v125, v133\n" \
"s_add_i32 s84, s54, 0x8018000\n" \
"ds_read_b64_tr_b16 v[202:203], v247 offset:512\n" \
"v_mfma_f32_16x16x32_f16 v[72:75], v[120:123], v[208:211], v[72:75]\n" \
"v_pk_add_f16 v153, v153, v169\n" \
"v_pk_add_f16 v142, v126, v134\n" \
"buffer_store_dword v240, v243, s[36:39], s84 offen sc1\n" \
"v_mfma_f32_16x16x32_f16 v[96:99], v[120:123], v[156:159], v[96:99]\n" \
"v_pk_add_f16 v154, v154, v170\n" \
"v_pk_add_f16 v155, v155, v171\n" \
"v_mfma_f32_16x16x32_f16 v[108:111], v[124:127], v[156:159], v[108:111]\n" \
"v_pk_add_f16 v208, v208, v172\n" \
"v_pk_add_f16 v143, v127, v135\n" \
"v_mfma_f32_16x16x32_f16 v[4:7], v[128:131], v[160:163], v[4:7]\n" \
"v_pk_add_f16 v209, v209, v173\n" \
"ds_read_b64_tr_b16 v[120:121], v241 offset:16384\n" \
"buffer_load_dwordx4 v232, s[36:39], s80 offen offset:1024 sc1 lds\n" \
"s_waitcnt lgkmcnt(1)\n" \
"v_mfma_f32_16x16x32_bf16 v[204:207], v[200:203], v[196:199], v[204:207]\n" \
"v_mfma_f32_16x16x32_f16 v[16:19], v[132:135], v[160:163], v[16:19]\n" \
"v_pk_add_f16 v210, v210, v174\n" \
"ds_read_b128 v[180:183], v240 offset:20992\n" \
"ds_read_b128 v[196:199], v249 offset:7168\n" \
"v_mfma_f32_16x16x32_f16 v[40:43], v[132:135], v[164:167], v[40:43]\n" \
"v_pk_add_f16 v211, v211, v175\n" \
"ds_read_b128 v[160:163], v240 offset:20480\n" \
"ds_read_b64_tr_b16 v[200:201], v247 offset:4096\n" \
"v_mfma_f32_16x16x32_f16 v[28:31], v[128:131], v[164:167], v[28:31]\n" \
"v_pk_add_f16 v156, v156, v176\n" \
"ds_read_b64_tr_b16 v[122:123], v241 offset:24576\n" \
"v_mfma_f32_16x16x32_f16 v[52:55], v[128:131], v[168:171], v[52:55]\n" \
"v_pk_add_f16 v157, v157, v177\n" \
"ds_read_b128 v[164:167], v240 offset:22528\n" \
"v_mfma_f32_16x16x32_f16 v[64:67], v[132:135], v[168:171], v[64:67]\n" \
"v_pk_add_f16 v158, v158, v178\n" \
"ds_read_b64_tr_b16 v[124:125], v242 offset:16384\n" \
"v_mfma_f32_16x16x32_f16 v[88:91], v[132:135], v[172:175], v[88:91]\n" \
"v_pk_add_f16 v159, v159, v179\n" \
"ds_read_b128 v[168:171], v240 offset:24576\n" \
"buffer_load_dwordx4 v232, s[36:39], s80 offen offset:2048 sc1 lds\n" \
"v_mfma_f32_16x16x32_f16 v[76:79], v[128:131], v[172:175], v[76:79]\n" \
"ds_read_b64_tr_b16 v[126:127], v242 offset:24576\n" \
"ds_read_b64_tr_b16 v[202:203], v247 offset:4608\n" \
"v_mfma_f32_16x16x32_f16 v[100:103], v[128:131], v[176:179], v[100:103]\n" \
"ds_read_b128 v[172:175], v240 offset:26624\n" \
"v_mfma_f32_16x16x32_f16 v[112:115], v[132:135], v[176:179], v[112:115]\n" \
"ds_read_b64_tr_b16 v[128:129], v241 offset:16896\n" \
"v_mfma_f32_16x16x32_f16 v[8:11], v[136:139], v[144:147], v[8:11]\n" \
"ds_read_b128 v[176:179], v240 offset:28672\n" \
"v_mfma_f32_16x16x32_f16 v[20:23], v[140:143], v[144:147], v[20:23]\n" \
"ds_read_b128 v[144:147], v240 offset:25088\n" \
"v_mfma_f32_16x16x32_f16 v[44:47], v[140:143], v[148:151], v[44:47]\n" \
"ds_read_b64_tr_b16 v[130:131], v241 offset:25088\n" \
"ds_read_b64_tr_b16 v[132:133], v242 offset:16896\n" \
"buffer_load_dwordx4 v232, s[36:39], s80 offen offset:3072 sc1 lds\n" \
"v_mfma_f32_16x16x32_f16 v[32:35], v[136:139], v[148:151], v[32:35]\n" \
"ds_read_b128 v[148:151], v240 offset:27136\n" \
"v_mfma_f32_16x16x32_f16 v[56:59], v[136:139], v[152:155], v[56:59]\n" \
"ds_read_b64_tr_b16 v[134:135], v242 offset:25088\n" \
"s_add_i32 m0, s45, 0x5000\n" \
"s_mov_b32 s83, 0x58000\n" \
"s_waitcnt lgkmcnt(8)\n" \
"v_mfma_f32_16x16x32_bf16 v[204:207], v[200:203], v[196:199], v[204:207]\n" \
"v_mfma_f32_16x16x32_f16 v[68:71], v[140:143], v[152:155], v[68:71]\n" \
"ds_read_b128 v[152:155], v240 offset:29184\n" \
"buffer_load_dwordx4 v233, s[36:39], s83 offen sc1 lds\n" \
"v_mfma_f32_16x16x32_f16 v[92:95], v[140:143], v[208:211], v[92:95]\n" \
"v_mfma_f32_16x16x32_f16 v[80:83], v[136:139], v[208:211], v[80:83]\n" \
"v_mfma_f32_16x16x32_f16 v[104:107], v[136:139], v[156:159], v[104:107]\n" \
"v_mfma_f32_16x16x32_f16 v[116:119], v[140:143], v[156:159], v[116:119]\n" \
"s_waitcnt vmcnt(7) lgkmcnt(0)\n" \
"s_barrier\n" \
"v_mfma_f32_16x16x32_f16 v[0:3], v[120:123], v[160:163], v[0:3]\n" \
"v_pk_add_f16 v136, v120, v128\n" \
"v_pk_add_f16 v137, v121, v129\n" \
"s_mov_b32 s81, 0x8016000\n" \
"s_add_i32 m0, s45, 0x0\n" \
"s_mov_b32 s83, 0x60000\n" \
"v_mfma_f32_16x16x32_f16 v[12:15], v[124:127], v[160:163], v[12:15]\n" \
"v_pk_add_f16 v160, v160, v180\n" \
"v_pk_add_f16 v138, v122, v130\n" \
"buffer_load_dword v253, v252, s[36:39], s81 offen sc1\n" \
"buffer_load_dwordx4 v233, s[36:39], s83 offen sc1 lds\n" \
"v_mfma_f32_16x16x32_f16 v[36:39], v[124:127], v[164:167], v[36:39]\n" \
"v_pk_add_f16 v161, v161, v181\n" \
"v_pk_add_f16 v162, v162, v182\n" \
"v_pk_add_f16 v163, v163, v183\n" \
"ds_read_b128 v[196:199], v249 offset:0\n" \
"v_mfma_f32_16x16x32_f16 v[24:27], v[120:123], v[164:167], v[24:27]\n" \
"v_pk_add_f16 v164, v164, v184\n" \
"v_pk_add_f16 v139, v123, v131\n" \
"s_add_i32 m0, s44, 0x4000\n" \
"s_mov_b32 s80, 0x6400000\n" \
"v_mfma_f32_16x16x32_f16 v[48:51], v[120:123], v[168:171], v[48:51]\n" \
"v_pk_add_f16 v165, v165, v185\n" \
"v_pk_add_f16 v140, v124, v132\n" \
"buffer_load_dwordx4 v232, s[36:39], s80 offen sc1 lds\n" \
"v_mfma_f32_16x16x32_f16 v[60:63], v[124:127], v[168:171], v[60:63]\n" \
"v_pk_add_f16 v166, v166, v186\n" \
"v_pk_add_f16 v167, v167, v187\n" \
"ds_read_b128 v[156:159], v240 offset:43520\n" \
"ds_read_b64_tr_b16 v[200:201], v247 offset:8192\n" \
"v_mfma_f32_16x16x32_f16 v[84:87], v[124:127], v[172:175], v[84:87]\n" \
"v_pk_add_f16 v168, v168, v144\n" \
"v_pk_add_f16 v141, v125, v133\n" \
"s_add_i32 s84, s55, 0x801a000\n" \
"ds_read_b64_tr_b16 v[202:203], v247 offset:8704\n" \
"v_mfma_f32_16x16x32_f16 v[72:75], v[120:123], v[172:175], v[72:75]\n" \
"v_pk_add_f16 v169, v169, v145\n" \
"v_pk_add_f16 v142, v126, v134\n" \
"buffer_store_dword v240, v243, s[36:39], s84 offen sc1\n" \
"v_mfma_f32_16x16x32_f16 v[96:99], v[120:123], v[176:179], v[96:99]\n" \
"v_pk_add_f16 v170, v170, v146\n" \
"v_pk_add_f16 v171, v171, v147\n" \
"v_mfma_f32_16x16x32_f16 v[108:111], v[124:127], v[176:179], v[108:111]\n" \
"v_pk_add_f16 v172, v172, v148\n" \
"v_pk_add_f16 v143, v127, v135\n" \
"v_mfma_f32_16x16x32_f16 v[4:7], v[128:131], v[180:183], v[4:7]\n" \
"v_pk_add_f16 v173, v173, v149\n" \
"ds_read_b64_tr_b16 v[120:121], v241 offset:32768\n" \
"buffer_load_dwordx4 v232, s[36:39], s80 offen offset:1024 sc1 lds\n" \
"s_waitcnt lgkmcnt(1)\n" \
"v_mfma_f32_16x16x32_bf16 v[204:207], v[200:203], v[196:199], v[204:207]\n" \
"v_mfma_f32_16x16x32_f16 v[16:19], v[132:135], v[180:183], v[16:19]\n" \
"v_pk_add_f16 v174, v174, v150\n" \
"ds_read_b128 v[208:211], v240 offset:41472\n" \
"ds_read_b128 v[196:199], v249 offset:1024\n" \
"v_mfma_f32_16x16x32_f16 v[40:43], v[132:135], v[184:187], v[40:43]\n" \
"v_pk_add_f16 v175, v175, v151\n" \
"ds_read_b128 v[180:183], v240 offset:40960\n" \
"ds_read_b64_tr_b16 v[200:201], v247 offset:12288\n" \
"v_mfma_f32_16x16x32_f16 v[28:31], v[128:131], v[184:187], v[28:31]\n" \
"v_pk_add_f16 v176, v176, v152\n" \
"ds_read_b64_tr_b16 v[122:123], v241 offset:40960\n" \
"v_mfma_f32_16x16x32_f16 v[52:55], v[128:131], v[144:147], v[52:55]\n" \
"v_pk_add_f16 v177, v177, v153\n" \
"ds_read_b128 v[184:187], v240 offset:43008\n" \
"v_mfma_f32_16x16x32_f16 v[64:67], v[132:135], v[144:147], v[64:67]\n" \
"v_pk_add_f16 v178, v178, v154\n" \
"ds_read_b64_tr_b16 v[124:125], v242 offset:32768\n" \
"v_mfma_f32_16x16x32_f16 v[88:91], v[132:135], v[148:151], v[88:91]\n" \
"v_pk_add_f16 v179, v179, v155\n" \
"ds_read_b128 v[144:147], v240 offset:45056\n" \
"buffer_load_dwordx4 v232, s[36:39], s80 offen offset:2048 sc1 lds\n" \
"v_mfma_f32_16x16x32_f16 v[76:79], v[128:131], v[148:151], v[76:79]\n" \
"ds_read_b64_tr_b16 v[126:127], v242 offset:40960\n" \
"ds_read_b64_tr_b16 v[202:203], v247 offset:12800\n" \
"v_mfma_f32_16x16x32_f16 v[100:103], v[128:131], v[152:155], v[100:103]\n" \
"ds_read_b128 v[148:151], v240 offset:47104\n" \
"v_mfma_f32_16x16x32_f16 v[112:115], v[132:135], v[152:155], v[112:115]\n" \
"ds_read_b64_tr_b16 v[128:129], v241 offset:33280\n" \
"v_mfma_f32_16x16x32_f16 v[8:11], v[136:139], v[160:163], v[8:11]\n" \
"ds_read_b128 v[152:155], v240 offset:49152\n" \
"v_mfma_f32_16x16x32_f16 v[20:23], v[140:143], v[160:163], v[20:23]\n" \
"ds_read_b128 v[160:163], v240 offset:45568\n" \
"v_mfma_f32_16x16x32_f16 v[44:47], v[140:143], v[164:167], v[44:47]\n" \
"ds_read_b64_tr_b16 v[130:131], v241 offset:41472\n" \
"ds_read_b64_tr_b16 v[132:133], v242 offset:33280\n" \
"buffer_load_dwordx4 v232, s[36:39], s80 offen offset:3072 sc1 lds\n" \
"v_mfma_f32_16x16x32_f16 v[32:35], v[136:139], v[164:167], v[32:35]\n" \
"ds_read_b128 v[164:167], v240 offset:47616\n" \
"v_mfma_f32_16x16x32_f16 v[56:59], v[136:139], v[168:171], v[56:59]\n" \
"ds_read_b64_tr_b16 v[134:135], v242 offset:41472\n" \
"s_add_i32 m0, s45, 0x1000\n" \
"s_mov_b32 s83, 0x68000\n" \
"s_waitcnt lgkmcnt(8)\n" \
"v_mfma_f32_16x16x32_bf16 v[204:207], v[200:203], v[196:199], v[204:207]\n" \
"v_mfma_f32_16x16x32_f16 v[68:71], v[140:143], v[168:171], v[68:71]\n" \
"ds_read_b128 v[168:171], v240 offset:49664\n" \
"buffer_load_dwordx4 v233, s[36:39], s83 offen sc1 lds\n" \
"v_mfma_f32_16x16x32_f16 v[92:95], v[140:143], v[172:175], v[92:95]\n" \
"v_mfma_f32_16x16x32_f16 v[80:83], v[136:139], v[172:175], v[80:83]\n" \
"v_mfma_f32_16x16x32_f16 v[104:107], v[136:139], v[176:179], v[104:107]\n" \
"v_mfma_f32_16x16x32_f16 v[116:119], v[140:143], v[176:179], v[116:119]\n" \
"s_waitcnt vmcnt(7) lgkmcnt(0)\n" \
"s_barrier\n" \
"v_mfma_f32_16x16x32_f16 v[0:3], v[120:123], v[180:183], v[0:3]\n" \
"v_pk_add_f16 v136, v120, v128\n" \
"v_pk_add_f16 v137, v121, v129\n" \
"s_add_i32 m0, s45, 0x2000\n" \
"s_mov_b32 s83, 0x70000\n" \
"v_mfma_f32_16x16x32_f16 v[12:15], v[124:127], v[180:183], v[12:15]\n" \
"v_pk_add_f16 v180, v180, v208\n" \
"v_pk_add_f16 v138, v122, v130\n" \
"buffer_load_dwordx4 v233, s[36:39], s83 offen sc1 lds\n" \
"v_mfma_f32_16x16x32_f16 v[36:39], v[124:127], v[184:187], v[36:39]\n" \
"v_pk_add_f16 v181, v181, v209\n" \
"v_pk_add_f16 v182, v182, v210\n" \
"v_pk_add_f16 v183, v183, v211\n" \
"v_cmp_eq_u32 vcc, 0, v253\n" \
"s_cbranch_vccnz .Ls011_%=\n" \
".Lr011_%=:\n" \
"v_mfma_f32_16x16x32_f16 v[24:27], v[120:123], v[184:187], v[24:27]\n" \
"v_pk_add_f16 v184, v184, v156\n" \
"v_pk_add_f16 v139, v123, v131\n" \
"s_add_i32 m0, s44, 0x8000\n" \
"s_mov_b32 s80, 0x6800000\n" \
"ds_read_b128 v[196:199], v249 offset:2048\n" \
"v_mfma_f32_16x16x32_f16 v[48:51], v[120:123], v[144:147], v[48:51]\n" \
"v_pk_add_f16 v185, v185, v157\n" \
"v_pk_add_f16 v140, v124, v132\n" \
"buffer_load_dwordx4 v232, s[36:39], s80 offen sc1 lds\n" \
"v_mfma_f32_16x16x32_f16 v[60:63], v[124:127], v[144:147], v[60:63]\n" \
"v_pk_add_f16 v186, v186, v158\n" \
"v_pk_add_f16 v187, v187, v159\n" \
"ds_read_b128 v[176:179], v240 offset:2560\n" \
"ds_read_b64_tr_b16 v[200:201], v247 offset:16384\n" \
"v_mfma_f32_16x16x32_f16 v[84:87], v[124:127], v[148:151], v[84:87]\n" \
"v_pk_add_f16 v144, v144, v160\n" \
"v_pk_add_f16 v141, v125, v133\n" \
"ds_read_b64_tr_b16 v[202:203], v247 offset:16896\n" \
"v_mfma_f32_16x16x32_f16 v[72:75], v[120:123], v[148:151], v[72:75]\n" \
"v_pk_add_f16 v145, v145, v161\n" \
"v_pk_add_f16 v142, v126, v134\n" \
"v_mfma_f32_16x16x32_f16 v[96:99], v[120:123], v[152:155], v[96:99]\n" \
"v_pk_add_f16 v146, v146, v162\n" \
"v_pk_add_f16 v147, v147, v163\n" \
"v_mfma_f32_16x16x32_f16 v[108:111], v[124:127], v[152:155], v[108:111]\n" \
"v_pk_add_f16 v148, v148, v164\n" \
"v_pk_add_f16 v143, v127, v135\n" \
"v_mfma_f32_16x16x32_f16 v[4:7], v[128:131], v[208:211], v[4:7]\n" \
"v_pk_add_f16 v149, v149, v165\n" \
"ds_read_b64_tr_b16 v[120:121], v241 offset:0\n" \
"buffer_load_dwordx4 v232, s[36:39], s80 offen offset:1024 sc1 lds\n" \
"s_waitcnt lgkmcnt(1)\n" \
"v_mfma_f32_16x16x32_bf16 v[204:207], v[200:203], v[196:199], v[204:207]\n" \
"v_mfma_f32_16x16x32_f16 v[16:19], v[132:135], v[208:211], v[16:19]\n" \
"v_pk_add_f16 v150, v150, v166\n" \
"ds_read_b128 v[172:175], v240 offset:512\n" \
"ds_read_b128 v[196:199], v249 offset:3072\n" \
"v_mfma_f32_16x16x32_f16 v[40:43], v[132:135], v[156:159], v[40:43]\n" \
"v_pk_add_f16 v151, v151, v167\n" \
"ds_read_b128 v[208:211], v240 offset:0\n" \
"ds_read_b64_tr_b16 v[200:201], v247 offset:20480\n" \
"v_mfma_f32_16x16x32_f16 v[28:31], v[128:131], v[156:159], v[28:31]\n" \
"v_pk_add_f16 v152, v152, v168\n" \
"ds_read_b64_tr_b16 v[122:123], v241 offset:8192\n" \
"v_mfma_f32_16x16x32_f16 v[52:55], v[128:131], v[160:163], v[52:55]\n" \
"v_pk_add_f16 v153, v153, v169\n" \
"ds_read_b128 v[156:159], v240 offset:2048\n" \
"v_mfma_f32_16x16x32_f16 v[64:67], v[132:135], v[160:163], v[64:67]\n" \
"v_pk_add_f16 v154, v154, v170\n" \
"ds_read_b64_tr_b16 v[124:125], v242 offset:0\n" \
"v_mfma_f32_16x16x32_f16 v[88:91], v[132:135], v[164:167], v[88:91]\n" \
"v_pk_add_f16 v155, v155, v171\n" \
"ds_read_b128 v[160:163], v240 offset:4096\n" \
"buffer_load_dwordx4 v232, s[36:39], s80 offen offset:2048 sc1 lds\n" \
"v_mfma_f32_16x16x32_f16 v[76:79], v[128:131], v[164:167], v[76:79]\n" \
"ds_read_b64_tr_b16 v[126:127], v242 offset:8192\n" \
"ds_read_b64_tr_b16 v[202:203], v247 offset:20992\n" \
"v_mfma_f32_16x16x32_f16 v[100:103], v[128:131], v[168:171], v[100:103]\n" \
"ds_read_b128 v[164:167], v240 offset:6144\n" \
"v_mfma_f32_16x16x32_f16 v[112:115], v[132:135], v[168:171], v[112:115]\n" \
"ds_read_b64_tr_b16 v[128:129], v241 offset:512\n" \
"v_mfma_f32_16x16x32_f16 v[8:11], v[136:139], v[180:183], v[8:11]\n" \
"ds_read_b128 v[168:171], v240 offset:8192\n" \
"v_mfma_f32_16x16x32_f16 v[20:23], v[140:143], v[180:183], v[20:23]\n" \
"ds_read_b128 v[180:183], v240 offset:4608\n" \
"v_mfma_f32_16x16x32_f16 v[44:47], v[140:143], v[184:187], v[44:47]\n" \
"ds_read_b64_tr_b16 v[130:131], v241 offset:8704\n" \
"ds_read_b64_tr_b16 v[132:133], v242 offset:512\n" \
"buffer_load_dwordx4 v232, s[36:39], s80 offen offset:3072 sc1 lds\n" \
"v_mfma_f32_16x16x32_f16 v[32:35], v[136:139], v[184:187], v[32:35]\n" \
"ds_read_b128 v[184:187], v240 offset:6656\n" \
"v_mfma_f32_16x16x32_f16 v[56:59], v[136:139], v[144:147], v[56:59]\n" \
"ds_read_b64_tr_b16 v[134:135], v242 offset:8704\n" \
"s_add_i32 m0, s45, 0x3000\n" \
"s_mov_b32 s83, 0x78000\n" \
"s_waitcnt lgkmcnt(8)\n" \
"v_mfma_f32_16x16x32_bf16 v[204:207], v[200:203], v[196:199], v[204:207]\n" \
"v_mfma_f32_16x16x32_f16 v[68:71], v[140:143], v[144:147], v[68:71]\n" \
"ds_read_b128 v[144:147], v240 offset:8704\n" \
"buffer_load_dwordx4 v233, s[36:39], s83 offen sc1 lds\n" \
"v_mfma_f32_16x16x32_f16 v[92:95], v[140:143], v[148:151], v[92:95]\n" \
"v_mfma_f32_16x16x32_f16 v[80:83], v[136:139], v[148:151], v[80:83]\n" \
"v_mfma_f32_16x16x32_f16 v[104:107], v[136:139], v[152:155], v[104:107]\n" \
"v_mfma_f32_16x16x32_f16 v[116:119], v[140:143], v[152:155], v[116:119]\n" \
"s_waitcnt vmcnt(6) lgkmcnt(0)\n" \
"s_barrier\n" \
"v_mfma_f32_16x16x32_f16 v[0:3], v[120:123], v[208:211], v[0:3]\n" \
"v_pk_add_f16 v136, v120, v128\n" \
"v_pk_add_f16 v137, v121, v129\n" \
"s_mov_b32 s81, 0x8018000\n" \
"v_mfma_f32_16x16x32_f16 v[12:15], v[124:127], v[208:211], v[12:15]\n" \
"v_pk_add_f16 v208, v208, v172\n" \
"v_pk_add_f16 v138, v122, v130\n" \
"buffer_load_dword v253, v252, s[36:39], s81 offen sc1\n" \
"v_mfma_f32_16x16x32_f16 v[36:39], v[124:127], v[156:159], v[36:39]\n" \
"v_pk_add_f16 v209, v209, v173\n" \
"v_pk_add_f16 v210, v210, v174\n" \
"v_pk_add_f16 v211, v211, v175\n" \
"ds_read_b128 v[196:199], v249 offset:4096\n" \
"v_mfma_f32_16x16x32_f16 v[24:27], v[120:123], v[156:159], v[24:27]\n" \
"v_pk_add_f16 v156, v156, v176\n" \
"v_pk_add_f16 v139, v123, v131\n" \
"s_add_i32 m0, s44, 0x0\n" \
"s_mov_b32 s80, 0x6c00000\n" \
"v_mfma_f32_16x16x32_f16 v[48:51], v[120:123], v[160:163], v[48:51]\n" \
"v_pk_add_f16 v157, v157, v177\n" \
"v_pk_add_f16 v140, v124, v132\n" \
"buffer_load_dwordx4 v232, s[36:39], s80 offen sc1 lds\n" \
"v_mfma_f32_16x16x32_f16 v[60:63], v[124:127], v[160:163], v[60:63]\n" \
"v_pk_add_f16 v158, v158, v178\n" \
"v_pk_add_f16 v159, v159, v179\n" \
"ds_read_b128 v[152:155], v240 offset:23040\n" \
"ds_read_b64_tr_b16 v[200:201], v247 offset:0\n" \
"v_mfma_f32_16x16x32_f16 v[84:87], v[124:127], v[164:167], v[84:87]\n" \
"v_pk_add_f16 v160, v160, v180\n" \
"v_pk_add_f16 v141, v125, v133\n" \
"ds_read_b64_tr_b16 v[202:203], v247 offset:512\n" \
"v_mfma_f32_16x16x32_f16 v[72:75], v[120:123], v[164:167], v[72:75]\n" \
"v_pk_add_f16 v161, v161, v181\n" \
"v_pk_add_f16 v142, v126, v134\n" \
"v_mfma_f32_16x16x32_f16 v[96:99], v[120:123], v[168:171], v[96:99]\n" \
"v_pk_add_f16 v162, v162, v182\n" \
"v_pk_add_f16 v163, v163, v183\n" \
"v_mfma_f32_16x16x32_f16 v[108:111], v[124:127], v[168:171], v[108:111]\n" \
"v_pk_add_f16 v164, v164, v184\n" \
"v_pk_add_f16 v143, v127, v135\n" \
"v_mfma_f32_16x16x32_f16 v[4:7], v[128:131], v[172:175], v[4:7]\n" \
"v_pk_add_f16 v165, v165, v185\n" \
"ds_read_b64_tr_b16 v[120:121], v241 offset:16384\n" \
"buffer_load_dwordx4 v232, s[36:39], s80 offen offset:1024 sc1 lds\n" \
"s_waitcnt lgkmcnt(1)\n" \
"v_mfma_f32_16x16x32_bf16 v[204:207], v[200:203], v[196:199], v[204:207]\n" \
"v_mfma_f32_16x16x32_f16 v[16:19], v[132:135], v[172:175], v[16:19]\n" \
"v_pk_add_f16 v166, v166, v186\n" \
"ds_read_b128 v[148:151], v240 offset:20992\n" \
"ds_read_b128 v[196:199], v249 offset:5120\n" \
"v_mfma_f32_16x16x32_f16 v[40:43], v[132:135], v[176:179], v[40:43]\n" \
"v_pk_add_f16 v167, v167, v187\n" \
"ds_read_b128 v[172:175], v240 offset:20480\n" \
"ds_read_b64_tr_b16 v[200:201], v247 offset:4096\n" \
"v_mfma_f32_16x16x32_f16 v[28:31], v[128:131], v[176:179], v[28:31]\n" \
"v_pk_add_f16 v168, v168, v144\n" \
"ds_read_b64_tr_b16 v[122:123], v241 offset:24576\n" \
"v_mfma_f32_16x16x32_f16 v[52:55], v[128:131], v[180:183], v[52:55]\n" \
"v_pk_add_f16 v169, v169, v145\n" \
"ds_read_b128 v[176:179], v240 offset:22528\n" \
"v_mfma_f32_16x16x32_f16 v[64:67], v[132:135], v[180:183], v[64:67]\n" \
"v_pk_add_f16 v170, v170, v146\n" \
"ds_read_b64_tr_b16 v[124:125], v242 offset:16384\n" \
"v_mfma_f32_16x16x32_f16 v[88:91], v[132:135], v[184:187], v[88:91]\n" \
"v_pk_add_f16 v171, v171, v147\n" \
"ds_read_b128 v[180:183], v240 offset:24576\n" \
"buffer_load_dwordx4 v232, s[36:39], s80 offen offset:2048 sc1 lds\n" \
"v_mfma_f32_16x16x32_f16 v[76:79], v[128:131], v[184:187], v[76:79]\n" \
"ds_read_b64_tr_b16 v[126:127], v242 offset:24576\n" \
"ds_read_b64_tr_b16 v[202:203], v247 offset:4608\n" \
"v_mfma_f32_16x16x32_f16 v[100:103], v[128:131], v[144:147], v[100:103]\n" \
"ds_read_b128 v[184:187], v240 offset:26624\n" \
"v_mfma_f32_16x16x32_f16 v[112:115], v[132:135], v[144:147], v[112:115]\n" \
"ds_read_b64_tr_b16 v[128:129], v241 offset:16896\n" \
"v_mfma_f32_16x16x32_f16 v[8:11], v[136:139], v[208:211], v[8:11]\n" \
"ds_read_b128 v[144:147], v240 offset:28672\n" \
"v_mfma_f32_16x16x32_f16 v[20:23], v[140:143], v[208:211], v[20:23]\n" \
"ds_read_b128 v[208:211], v240 offset:25088\n" \
"v_mfma_f32_16x16x32_f16 v[44:47], v[140:143], v[156:159], v[44:47]\n" \
"ds_read_b64_tr_b16 v[130:131], v241 offset:25088\n" \
"ds_read_b64_tr_b16 v[132:133], v242 offset:16896\n" \
"buffer_load_dwordx4 v232, s[36:39], s80 offen offset:3072 sc1 lds\n" \
"v_mfma_f32_16x16x32_f16 v[32:35], v[136:139], v[156:159], v[32:35]\n" \
"ds_read_b128 v[156:159], v240 offset:27136\n" \
"v_mfma_f32_16x16x32_f16 v[56:59], v[136:139], v[160:163], v[56:59]\n" \
"ds_read_b64_tr_b16 v[134:135], v242 offset:25088\n" \
"s_waitcnt lgkmcnt(8)\n" \
"v_mfma_f32_16x16x32_bf16 v[204:207], v[200:203], v[196:199], v[204:207]\n" \
"v_mfma_f32_16x16x32_f16 v[68:71], v[140:143], v[160:163], v[68:71]\n" \
"ds_read_b128 v[160:163], v240 offset:29184\n" \
"v_mfma_f32_16x16x32_f16 v[92:95], v[140:143], v[164:167], v[92:95]\n" \
"v_mfma_f32_16x16x32_f16 v[80:83], v[136:139], v[164:167], v[80:83]\n" \
"v_mfma_f32_16x16x32_f16 v[104:107], v[136:139], v[168:171], v[104:107]\n" \
"v_mfma_f32_16x16x32_f16 v[116:119], v[140:143], v[168:171], v[116:119]\n" \
"s_waitcnt vmcnt(4) lgkmcnt(0)\n" \
"s_barrier\n" \
"v_mfma_f32_16x16x32_f16 v[0:3], v[120:123], v[172:175], v[0:3]\n" \
"v_pk_add_f16 v136, v120, v128\n" \
"v_pk_add_f16 v137, v121, v129\n" \
"v_mfma_f32_16x16x32_f16 v[12:15], v[124:127], v[172:175], v[12:15]\n" \
"v_pk_add_f16 v172, v172, v148\n" \
"v_pk_add_f16 v138, v122, v130\n" \
"v_mfma_f32_16x16x32_f16 v[36:39], v[124:127], v[176:179], v[36:39]\n" \
"v_pk_add_f16 v173, v173, v149\n" \
"v_pk_add_f16 v174, v174, v150\n" \
"v_pk_add_f16 v175, v175, v151\n" \
"v_cmp_eq_u32 vcc, 0, v253\n" \
"s_cbranch_vccnz .Ls012_%=\n" \
".Lr012_%=:\n" \
"s_mov_b32 s81, 0x801a000\n" \
"v_mfma_f32_16x16x32_f16 v[24:27], v[120:123], v[176:179], v[24:27]\n" \
"v_pk_add_f16 v176, v176, v152\n" \
"v_pk_add_f16 v139, v123, v131\n" \
"buffer_load_dword v253, v252, s[36:39], s81 offen sc1\n" \
"s_add_i32 m0, s44, 0x4000\n" \
"s_mov_b32 s80, 0x7000000\n" \
"ds_read_b128 v[196:199], v249 offset:6144\n" \
"v_mfma_f32_16x16x32_f16 v[48:51], v[120:123], v[180:183], v[48:51]\n" \
"v_pk_add_f16 v177, v177, v153\n" \
"v_pk_add_f16 v140, v124, v132\n" \
"buffer_load_dwordx4 v232, s[36:39], s80 offen sc1 lds\n" \
"v_mfma_f32_16x16x32_f16 v[60:63], v[124:127], v[180:183], v[60:63]\n" \
"v_pk_add_f16 v178, v178, v154\n" \
"v_pk_add_f16 v179, v179, v155\n" \
"ds_read_b128 v[168:171], v240 offset:43520\n" \
"ds_read_b64_tr_b16 v[200:201], v247 offset:8192\n" \
"v_mfma_f32_16x16x32_f16 v[84:87], v[124:127], v[184:187], v[84:87]\n" \
"v_pk_add_f16 v180, v180, v208\n" \
"v_pk_add_f16 v141, v125, v133\n" \
"ds_read_b64_tr_b16 v[202:203], v247 offset:8704\n" \
"v_mfma_f32_16x16x32_f16 v[72:75], v[120:123], v[184:187], v[72:75]\n" \
"v_pk_add_f16 v181, v181, v209\n" \
"v_pk_add_f16 v142, v126, v134\n" \
"v_mfma_f32_16x16x32_f16 v[96:99], v[120:123], v[144:147], v[96:99]\n" \
"v_pk_add_f16 v182, v182, v210\n" \
"v_pk_add_f16 v183, v183, v211\n" \
"v_mfma_f32_16x16x32_f16 v[108:111], v[124:127], v[144:147], v[108:111]\n" \
"v_pk_add_f16 v184, v184, v156\n" \
"v_pk_add_f16 v143, v127, v135\n" \
"v_mfma_f32_16x16x32_f16 v[4:7], v[128:131], v[148:151], v[4:7]\n" \
"v_pk_add_f16 v185, v185, v157\n" \
"ds_read_b64_tr_b16 v[120:121], v241 offset:32768\n" \
"buffer_load_dwordx4 v232, s[36:39], s80 offen offset:1024 sc1 lds\n" \
"s_waitcnt lgkmcnt(1)\n" \
"v_mfma_f32_16x16x32_bf16 v[204:207], v[200:203], v[196:199], v[204:207]\n" \
"v_mfma_f32_16x16x32_f16 v[16:19], v[132:135], v[148:151], v[16:19]\n" \
"v_pk_add_f16 v186, v186, v158\n" \
"ds_read_b128 v[164:167], v240 offset:41472\n" \
"ds_read_b128 v[196:199], v249 offset:7168\n" \
"v_mfma_f32_16x16x32_f16 v[40:43], v[132:135], v[152:155], v[40:43]\n" \
"v_pk_add_f16 v187, v187, v159\n" \
"ds_read_b128 v[148:151], v240 offset:40960\n" \
"ds_read_b64_tr_b16 v[200:201], v247 offset:12288\n" \
"v_mfma_f32_16x16x32_f16 v[28:31], v[128:131], v[152:155], v[28:31]\n" \
"v_pk_add_f16 v144, v144, v160\n" \
"ds_read_b64_tr_b16 v[122:123], v241 offset:40960\n" \
"v_mfma_f32_16x16x32_f16 v[52:55], v[128:131], v[208:211], v[52:55]\n" \
"v_pk_add_f16 v145, v145, v161\n" \
"ds_read_b128 v[152:155], v240 offset:43008\n" \
"v_mfma_f32_16x16x32_f16 v[64:67], v[132:135], v[208:211], v[64:67]\n" \
"v_pk_add_f16 v146, v146, v162\n" \
"ds_read_b64_tr_b16 v[124:125], v242 offset:32768\n" \
"v_mfma_f32_16x16x32_f16 v[88:91], v[132:135], v[156:159], v[88:91]\n" \
"v_pk_add_f16 v147, v147, v163\n" \
"ds_read_b128 v[208:211], v240 offset:45056\n" \
"buffer_load_dwordx4 v232, s[36:39], s80 offen offset:2048 sc1 lds\n" \
"v_mfma_f32_16x16x32_f16 v[76:79], v[128:131], v[156:159], v[76:79]\n" \
"ds_read_b64_tr_b16 v[126:127], v242 offset:40960\n" \
"ds_read_b64_tr_b16 v[202:203], v247 offset:12800\n" \
"v_mfma_f32_16x16x32_f16 v[100:103], v[128:131], v[160:163], v[100:103]\n" \
"ds_read_b128 v[156:159], v240 offset:47104\n" \
"v_mfma_f32_16x16x32_f16 v[112:115], v[132:135], v[160:163], v[112:115]\n" \
"ds_read_b64_tr_b16 v[128:129], v241 offset:33280\n" \
"v_mfma_f32_16x16x32_f16 v[8:11], v[136:139], v[172:175], v[8:11]\n" \
"ds_read_b128 v[160:163], v240 offset:49152\n" \
"v_mfma_f32_16x16x32_f16 v[20:23], v[140:143], v[172:175], v[20:23]\n" \
"ds_read_b128 v[172:175], v240 offset:45568\n" \
"v_mfma_f32_16x16x32_f16 v[44:47], v[140:143], v[176:179], v[44:47]\n" \
"ds_read_b64_tr_b16 v[130:131], v241 offset:41472\n" \
"ds_read_b64_tr_b16 v[132:133], v242 offset:33280\n" \
"buffer_load_dwordx4 v232, s[36:39], s80 offen offset:3072 sc1 lds\n" \
"v_mfma_f32_16x16x32_f16 v[32:35], v[136:139], v[176:179], v[32:35]\n" \
"ds_read_b128 v[176:179], v240 offset:47616\n" \
"v_mfma_f32_16x16x32_f16 v[56:59], v[136:139], v[180:183], v[56:59]\n" \
"ds_read_b64_tr_b16 v[134:135], v242 offset:41472\n" \
"s_waitcnt lgkmcnt(8)\n" \
"v_mfma_f32_16x16x32_bf16 v[204:207], v[200:203], v[196:199], v[204:207]\n" \
"v_mfma_f32_16x16x32_f16 v[68:71], v[140:143], v[180:183], v[68:71]\n" \
"ds_read_b128 v[180:183], v240 offset:49664\n" \
"v_mfma_f32_16x16x32_f16 v[92:95], v[140:143], v[184:187], v[92:95]\n" \
"v_mfma_f32_16x16x32_f16 v[80:83], v[136:139], v[184:187], v[80:83]\n" \
"v_mfma_f32_16x16x32_f16 v[104:107], v[136:139], v[144:147], v[104:107]\n" \
"v_mfma_f32_16x16x32_f16 v[116:119], v[140:143], v[144:147], v[116:119]\n" \
"s_waitcnt vmcnt(4) lgkmcnt(0)\n" \
"s_barrier\n" \
"v_mfma_f32_16x16x32_f16 v[0:3], v[120:123], v[148:151], v[0:3]\n" \
"v_pk_add_f16 v136, v120, v128\n" \
"v_pk_add_f16 v137, v121, v129\n" \
"v_mfma_f32_16x16x32_f16 v[12:15], v[124:127], v[148:151], v[12:15]\n" \
"v_pk_add_f16 v148, v148, v164\n" \
"v_pk_add_f16 v138, v122, v130\n" \
"s_add_i32 m0, s44, 0x8000\n" \
"s_mov_b32 s80, 0x7400000\n" \
"v_mfma_f32_16x16x32_f16 v[36:39], v[124:127], v[152:155], v[36:39]\n" \
"v_pk_add_f16 v149, v149, v165\n" \
"v_pk_add_f16 v150, v150, v166\n" \
"v_pk_add_f16 v151, v151, v167\n" \
"buffer_load_dwordx4 v232, s[36:39], s80 offen sc1 lds\n" \
"v_mfma_f32_16x16x32_f16 v[24:27], v[120:123], v[152:155], v[24:27]\n" \
"v_pk_add_f16 v152, v152, v168\n" \
"v_pk_add_f16 v139, v123, v131\n" \
"v_mfma_f32_16x16x32_f16 v[48:51], v[120:123], v[208:211], v[48:51]\n" \
"v_pk_add_f16 v153, v153, v169\n" \
"v_pk_add_f16 v140, v124, v132\n" \
"buffer_load_dwordx4 v232, s[36:39], s80 offen offset:1024 sc1 lds\n" \
"v_mfma_f32_16x16x32_f16 v[60:63], v[124:127], v[208:211], v[60:63]\n" \
"v_pk_add_f16 v154, v154, v170\n" \
"v_pk_add_f16 v155, v155, v171\n" \
"ds_read_b128 v[144:147], v240 offset:2560\n" \
"ds_write_b128 v248, v[204:207]\n" \
"v_mfma_f32_16x16x32_f16 v[84:87], v[124:127], v[156:159], v[84:87]\n" \
"v_pk_add_f16 v208, v208, v172\n" \
"v_pk_add_f16 v141, v125, v133\n" \
"buffer_load_dwordx4 v232, s[36:39], s80 offen offset:2048 sc1 lds\n" \
"v_mfma_f32_16x16x32_f16 v[72:75], v[120:123], v[156:159], v[72:75]\n" \
"v_pk_add_f16 v209, v209, v173\n" \
"v_pk_add_f16 v142, v126, v134\n" \
"v_mfma_f32_16x16x32_f16 v[96:99], v[120:123], v[160:163], v[96:99]\n" \
"v_pk_add_f16 v210, v210, v174\n" \
"v_pk_add_f16 v211, v211, v175\n" \
"v_mfma_f32_16x16x32_f16 v[108:111], v[124:127], v[160:163], v[108:111]\n" \
"v_pk_add_f16 v156, v156, v176\n" \
"v_pk_add_f16 v143, v127, v135\n" \
"buffer_load_dwordx4 v232, s[36:39], s80 offen offset:3072 sc1 lds\n" \
"v_mfma_f32_16x16x32_f16 v[4:7], v[128:131], v[164:167], v[4:7]\n" \
"v_pk_add_f16 v157, v157, v177\n" \
"ds_read_b64_tr_b16 v[120:121], v241 offset:0\n" \
"v_cmp_eq_u32 vcc, 0, v253\n" \
"s_cbranch_vccnz .Ls013_%=\n" \
".Lr013_%=:\n" \
"v_mfma_f32_16x16x32_f16 v[16:19], v[132:135], v[164:167], v[16:19]\n" \
"v_pk_add_f16 v158, v158, v178\n" \
"ds_read_b128 v[184:187], v240 offset:512\n" \
"s_add_i32 m0, s44, 0x1b000\n" \
"s_mov_b32 s80, 0x7800000\n" \
"v_mfma_f32_16x16x32_f16 v[40:43], v[132:135], v[168:171], v[40:43]\n" \
"v_pk_add_f16 v159, v159, v179\n" \
"ds_read_b128 v[164:167], v240 offset:0\n" \
"buffer_load_dwordx4 v232, s[36:39], s80 offen sc1 lds\n" \
"v_mfma_f32_16x16x32_f16 v[28:31], v[128:131], v[168:171], v[28:31]\n" \
"v_pk_add_f16 v160, v160, v180\n" \
"ds_read_b64_tr_b16 v[122:123], v241 offset:8192\n" \
"v_mfma_f32_16x16x32_f16 v[52:55], v[128:131], v[172:175], v[52:55]\n" \
"v_pk_add_f16 v161, v161, v181\n" \
"ds_read_b128 v[168:171], v240 offset:2048\n" \
"buffer_load_dwordx4 v232, s[36:39], s80 offen offset:1024 sc1 lds\n" \
"v_mfma_f32_16x16x32_f16 v[64:67], v[132:135], v[172:175], v[64:67]\n" \
"v_pk_add_f16 v162, v162, v182\n" \
"ds_read_b64_tr_b16 v[124:125], v242 offset:0\n" \
"v_mfma_f32_16x16x32_f16 v[88:91], v[132:135], v[176:179], v[88:91]\n" \
"v_pk_add_f16 v163, v163, v183\n" \
"ds_read_b128 v[172:175], v240 offset:4096\n" \
"buffer_load_dwordx4 v232, s[36:39], s80 offen offset:2048 sc1 lds\n" \
"v_mfma_f32_16x16x32_f16 v[76:79], v[128:131], v[176:179], v[76:79]\n" \
"ds_read_b64_tr_b16 v[126:127], v242 offset:8192\n" \
"v_mfma_f32_16x16x32_f16 v[100:103], v[128:131], v[180:183], v[100:103]\n" \
"ds_read_b128 v[176:179], v240 offset:6144\n" \
"buffer_load_dwordx4 v232, s[36:39], s80 offen offset:3072 sc1 lds\n" \
"v_mfma_f32_16x16x32_f16 v[112:115], v[132:135], v[180:183], v[112:115]\n" \
"ds_read_b64_tr_b16 v[128:129], v241 offset:512\n" \
"v_mfma_f32_16x16x32_f16 v[8:11], v[136:139], v[148:151], v[8:11]\n" \
"ds_read_b128 v[180:183], v240 offset:8192\n" \
"s_add_i32 m0, s44, 0x1f000\n" \
"s_mov_b32 s80, 0x7c00000\n" \
"v_mfma_f32_16x16x32_f16 v[20:23], v[140:143], v[148:151], v[20:23]\n" \
"ds_read_b128 v[148:151], v240 offset:4608\n" \
"buffer_load_dwordx4 v232, s[36:39], s80 offen sc1 lds\n" \
"v_mfma_f32_16x16x32_f16 v[44:47], v[140:143], v[152:155], v[44:47]\n" \
"ds_read_b64_tr_b16 v[130:131], v241 offset:8704\n" \
"ds_read_b64_tr_b16 v[132:133], v242 offset:512\n" \
"v_mfma_f32_16x16x32_f16 v[32:35], v[136:139], v[152:155], v[32:35]\n" \
"ds_read_b128 v[152:155], v240 offset:6656\n" \
"buffer_load_dwordx4 v232, s[36:39], s80 offen offset:1024 sc1 lds\n" \
"v_mfma_f32_16x16x32_f16 v[56:59], v[136:139], v[208:211], v[56:59]\n" \
"ds_read_b64_tr_b16 v[134:135], v242 offset:8704\n" \
"v_mfma_f32_16x16x32_f16 v[68:71], v[140:143], v[208:211], v[68:71]\n" \
"ds_read_b128 v[208:211], v240 offset:8704\n" \
"buffer_load_dwordx4 v232, s[36:39], s80 offen offset:2048 sc1 lds\n" \
"v_mfma_f32_16x16x32_f16 v[92:95], v[140:143], v[156:159], v[92:95]\n" \
"v_mfma_f32_16x16x32_f16 v[80:83], v[136:139], v[156:159], v[80:83]\n" \
"buffer_load_dwordx4 v232, s[36:39], s80 offen offset:3072 sc1 lds\n" \
"v_mfma_f32_16x16x32_f16 v[104:107], v[136:139], v[160:163], v[104:107]\n" \
"v_mfma_f32_16x16x32_f16 v[116:119], v[140:143], v[160:163], v[116:119]\n" \
"s_waitcnt vmcnt(12) lgkmcnt(0)\n" \
"s_barrier\n" \
"v_mfma_f32_16x16x32_f16 v[0:3], v[120:123], v[164:167], v[0:3]\n" \
"v_pk_add_f16 v136, v120, v128\n" \
"v_pk_add_f16 v137, v121, v129\n" \
"v_mfma_f32_16x16x32_f16 v[12:15], v[124:127], v[164:167], v[12:15]\n" \
"v_pk_add_f16 v164, v164, v184\n" \
"v_pk_add_f16 v138, v122, v130\n" \
"s_lshr_b32 m0, s44, 1\n" \
"v_mfma_f32_16x16x32_f16 v[36:39], v[124:127], v[168:171], v[36:39]\n" \
"v_pk_add_f16 v165, v165, v185\n" \
"v_pk_add_f16 v166, v166, v186\n" \
"v_pk_add_f16 v167, v167, v187\n" \
"v_add_u32 v252, m0, v244\n" \
"v_mfma_f32_16x16x32_f16 v[24:27], v[120:123], v[168:171], v[24:27]\n" \
"v_pk_add_f16 v168, v168, v144\n" \
"v_pk_add_f16 v139, v123, v131\n" \
"v_mfma_f32_16x16x32_f16 v[48:51], v[120:123], v[172:175], v[48:51]\n" \
"v_pk_add_f16 v169, v169, v145\n" \
"v_pk_add_f16 v140, v124, v132\n" \
"global_load_lds_dwordx4 v252, s[76:77]\n" \
"v_mfma_f32_16x16x32_f16 v[60:63], v[124:127], v[172:175], v[60:63]\n" \
"v_pk_add_f16 v170, v170, v146\n" \
"v_pk_add_f16 v171, v171, v147\n" \
"v_mfma_f32_16x16x32_f16 v[84:87], v[124:127], v[176:179], v[84:87]\n" \
"v_pk_add_f16 v172, v172, v148\n" \
"v_pk_add_f16 v141, v125, v133\n" \
"v_mfma_f32_16x16x32_f16 v[72:75], v[120:123], v[176:179], v[72:75]\n" \
"v_pk_add_f16 v173, v173, v149\n" \
"v_pk_add_f16 v142, v126, v134\n" \
"v_mfma_f32_16x16x32_f16 v[96:99], v[120:123], v[180:183], v[96:99]\n" \
"v_pk_add_f16 v174, v174, v150\n" \
"v_pk_add_f16 v175, v175, v151\n" \
"v_mfma_f32_16x16x32_f16 v[108:111], v[124:127], v[180:183], v[108:111]\n" \
"v_pk_add_f16 v176, v176, v152\n" \
"v_pk_add_f16 v143, v127, v135\n" \
"v_mfma_f32_16x16x32_f16 v[4:7], v[128:131], v[184:187], v[4:7]\n" \
"v_pk_add_f16 v177, v177, v153\n" \
"ds_read_b64_tr_b16 v[120:121], v241 offset:16384\n" \
"global_load_lds_dwordx4 v252, s[76:77] offset:1024\n" \
"v_mfma_f32_16x16x32_f16 v[16:19], v[132:135], v[184:187], v[16:19]\n" \
"v_pk_add_f16 v178, v178, v154\n" \
"v_and_b32 v248, 64, v244\n" \
"ds_read_b64_tr_b16 v[122:123], v241 offset:24576\n" \
"v_mfma_f32_16x16x32_f16 v[40:43], v[132:135], v[144:147], v[40:43]\n" \
"v_pk_add_f16 v179, v179, v155\n" \
"v_add_lshl_u32 v248, v248, v244, 6\n" \
"ds_read_b128 v[184:187], v240 offset:20480\n" \
"v_mfma_f32_16x16x32_f16 v[28:31], v[128:131], v[144:147], v[28:31]\n" \
"v_pk_add_f16 v180, v180, v208\n" \
"ds_read_b64_tr_b16 v[124:125], v242 offset:16384\n" \
"v_mfma_f32_16x16x32_f16 v[52:55], v[128:131], v[148:151], v[52:55]\n" \
"v_pk_add_f16 v181, v181, v209\n" \
"ds_read_b128 v[144:147], v240 offset:20992\n" \
"v_mfma_f32_16x16x32_f16 v[64:67], v[132:135], v[148:151], v[64:67]\n" \
"v_pk_add_f16 v182, v182, v210\n" \
"v_bfe_u32 v249, v244, 2, 7\n" \
"v_mfma_f32_16x16x32_f16 v[88:91], v[132:135], v[152:155], v[88:91]\n" \
"v_pk_add_f16 v183, v183, v211\n" \
"ds_read_b128 v[148:151], v240 offset:22528\n" \
"v_mfma_f32_16x16x32_f16 v[76:79], v[128:131], v[152:155], v[76:79]\n" \
"v_cmp_eq_u32 vcc, s74, v249\n" \
"ds_read_b64_tr_b16 v[126:127], v242 offset:24576\n" \
"v_mfma_f32_16x16x32_f16 v[100:103], v[128:131], v[208:211], v[100:103]\n" \
"v_xor_b32 v253, 4, v249\n" \
"ds_read_b128 v[152:155], v240 offset:23040\n" \
"v_mfma_f32_16x16x32_f16 v[112:115], v[132:135], v[208:211], v[112:115]\n" \
"v_cndmask_b32 v249, v249, v253, vcc\n" \
"ds_read_b64_tr_b16 v[128:129], v241 offset:16896\n" \
"v_mfma_f32_16x16x32_f16 v[8:11], v[136:139], v[164:167], v[8:11]\n" \
"ds_read_b64_tr_b16 v[130:131], v241 offset:25088\n" \
"v_mfma_f32_16x16x32_f16 v[20:23], v[140:143], v[164:167], v[20:23]\n" \
"ds_read_b64_tr_b16 v[132:133], v242 offset:16896\n" \
"v_mfma_f32_16x16x32_f16 v[44:47], v[140:143], v[168:171], v[44:47]\n" \
"v_add_u32 v243, 0x1b000, v241\n" \
"ds_read_b64_tr_b16 v[134:135], v242 offset:25088\n" \
"v_mfma_f32_16x16x32_f16 v[32:35], v[136:139], v[168:171], v[32:35]\n" \
"v_xor_b32 v244, 32, v243\n" \
"v_mfma_f32_16x16x32_f16 v[56:59], v[136:139], v[172:175], v[56:59]\n" \
"v_mfma_f32_16x16x32_f16 v[68:71], v[140:143], v[172:175], v[68:71]\n" \
"v_add_u32 v246, 0x17000, v240\n" \
"v_mfma_f32_16x16x32_f16 v[92:95], v[140:143], v[176:179], v[92:95]\n" \
"v_mfma_f32_16x16x32_f16 v[80:83], v[136:139], v[176:179], v[80:83]\n" \
"v_mfma_f32_16x16x32_f16 v[104:107], v[136:139], v[180:183], v[104:107]\n" \
"v_mfma_f32_16x16x32_f16 v[116:119], v[140:143], v[180:183], v[116:119]\n" \
"s_waitcnt vmcnt(2) lgkmcnt(0)\n" \
"s_barrier\n" \
"v_mfma_f32_16x16x32_f16 v[0:3], v[120:123], v[184:187], v[0:3]\n" \
"v_pk_add_f16 v136, v120, v128\n" \
"v_pk_add_f16 v137, v121, v129\n" \
"v_pk_add_f16 v138, v122, v130\n" \
"v_pk_add_f16 v139, v123, v131\n" \
"ds_read_b128 v[156:159], v240 offset:40960\n" \
"ds_read_b64_tr_b16 v[164:165], v241 offset:32768\n" \
"v_mfma_f32_16x16x32_f16 v[12:15], v[124:127], v[184:187], v[12:15]\n" \
"v_pk_add_f16 v184, v184, v144\n" \
"v_pk_add_f16 v185, v185, v145\n" \
"v_pk_add_f16 v140, v124, v132\n" \
"ds_read_b64_tr_b16 v[166:167], v241 offset:40960\n" \
"v_mfma_f32_16x16x32_f16 v[4:7], v[128:131], v[144:147], v[4:7]\n" \
"v_pk_add_f16 v186, v186, v146\n" \
"v_pk_add_f16 v187, v187, v147\n" \
"v_pk_add_f16 v141, v125, v133\n" \
"ds_read_b64_tr_b16 v[168:169], v242 offset:32768\n" \
"v_mfma_f32_16x16x32_f16 v[16:19], v[132:135], v[144:147], v[16:19]\n" \
"v_pk_add_f16 v142, v126, v134\n" \
"v_pk_add_f16 v143, v127, v135\n" \
"ds_read_b128 v[160:163], v240 offset:41472\n" \
"ds_read_b64_tr_b16 v[170:171], v242 offset:40960\n" \
"ds_read_b64_tr_b16 v[172:173], v241 offset:33280\n" \
"v_mfma_f32_16x16x32_f16 v[8:11], v[136:139], v[184:187], v[8:11]\n" \
"ds_read_b64_tr_b16 v[174:175], v241 offset:41472\n" \
"v_mfma_f32_16x16x32_f16 v[20:23], v[140:143], v[184:187], v[20:23]\n" \
"ds_read_b64_tr_b16 v[176:177], v242 offset:33280\n" \
"ds_read_b64_tr_b16 v[178:179], v242 offset:41472\n" \
"v_mfma_f32_16x16x32_f16 v[24:27], v[120:123], v[148:151], v[24:27]\n" \
"ds_read_b128 v[184:187], v246 offset:0\n" \
"ds_read_b64_tr_b16 v[188:189], v243 offset:0\n" \
"ds_read_b64_tr_b16 v[190:191], v243 offset:8192\n" \
"v_mfma_f32_16x16x32_f16 v[36:39], v[124:127], v[148:151], v[36:39]\n" \
"v_pk_add_f16 v148, v148, v152\n" \
"v_pk_add_f16 v149, v149, v153\n" \
"ds_read_b64_tr_b16 v[192:193], v244 offset:0\n" \
"v_mfma_f32_16x16x32_f16 v[28:31], v[128:131], v[152:155], v[28:31]\n" \
"v_pk_add_f16 v150, v150, v154\n" \
"v_pk_add_f16 v151, v151, v155\n" \
"v_mfma_f32_16x16x32_f16 v[40:43], v[132:135], v[152:155], v[40:43]\n" \
"s_waitcnt lgkmcnt(7)\n" \
"v_pk_add_f16 v180, v164, v172\n" \
"ds_read_b128 v[144:147], v246 offset:512\n" \
"ds_read_b64_tr_b16 v[194:195], v244 offset:8192\n" \
"ds_read_b64_tr_b16 v[196:197], v243 offset:512\n" \
"v_mfma_f32_16x16x32_f16 v[32:35], v[136:139], v[148:151], v[32:35]\n" \
"v_pk_add_f16 v181, v165, v173\n" \
"s_waitcnt lgkmcnt(9)\n" \
"v_pk_add_f16 v182, v166, v174\n" \
"ds_read_b64_tr_b16 v[198:199], v243 offset:8704\n" \
"v_mfma_f32_16x16x32_f16 v[44:47], v[140:143], v[148:151], v[44:47]\n" \
"v_pk_add_f16 v183, v167, v175\n" \
"s_waitcnt lgkmcnt(8)\n" \
"v_pk_add_f16 v236, v168, v176\n" \
"ds_read_b64_tr_b16 v[200:201], v244 offset:512\n" \
"ds_read_b64_tr_b16 v[202:203], v244 offset:8704\n" \
"v_mfma_f32_16x16x32_f16 v[0:3], v[164:167], v[156:159], v[0:3]\n" \
"v_pk_add_f16 v237, v169, v177\n" \
"ds_read_b128 v[148:151], v246 offset:8192\n" \
"ds_read_b64_tr_b16 v[212:213], v243 offset:16384\n" \
"ds_read_b64_tr_b16 v[214:215], v243 offset:24576\n" \
"v_mfma_f32_16x16x32_f16 v[12:15], v[168:171], v[156:159], v[12:15]\n" \
"v_pk_add_f16 v156, v156, v160\n" \
"v_pk_add_f16 v157, v157, v161\n" \
"ds_read_b64_tr_b16 v[216:217], v244 offset:16384\n" \
"v_mfma_f32_16x16x32_f16 v[4:7], v[172:175], v[160:163], v[4:7]\n" \
"v_pk_add_f16 v158, v158, v162\n" \
"v_pk_add_f16 v159, v159, v163\n" \
"v_pk_add_f16 v238, v170, v178\n" \
"v_pk_add_f16 v239, v171, v179\n" \
"v_mfma_f32_16x16x32_f16 v[16:19], v[176:179], v[160:163], v[16:19]\n" \
"s_waitcnt lgkmcnt(7)\n" \
"v_pk_add_f16 v204, v188, v196\n" \
"ds_read_b128 v[152:155], v246 offset:8704\n" \
"ds_read_b64_tr_b16 v[218:219], v244 offset:24576\n" \
"ds_read_b64_tr_b16 v[220:221], v243 offset:16896\n" \
"v_mfma_f32_16x16x32_f16 v[8:11], v[180:183], v[156:159], v[8:11]\n" \
"v_pk_add_f16 v205, v189, v197\n" \
"s_waitcnt lgkmcnt(8)\n" \
"v_pk_add_f16 v206, v190, v198\n" \
"ds_read_b64_tr_b16 v[222:223], v243 offset:25088\n" \
"v_mfma_f32_16x16x32_f16 v[20:23], v[236:239], v[156:159], v[20:23]\n" \
"ds_read_b64_tr_b16 v[224:225], v244 offset:16896\n" \
"ds_read_b64_tr_b16 v[226:227], v244 offset:25088\n" \
"v_mfma_f32_16x16x32_f16 v[0:3], v[188:191], v[184:187], v[0:3]\n" \
"v_pk_add_f16 v207, v191, v199\n" \
"v_pk_add_f16 v208, v192, v200\n" \
"ds_read_b128 v[156:159], v240 offset:43008\n" \
"v_mfma_f32_16x16x32_f16 v[12:15], v[192:195], v[184:187], v[12:15]\n" \
"v_pk_add_f16 v184, v184, v144\n" \
"v_pk_add_f16 v185, v185, v145\n" \
"v_pk_add_f16 v209, v193, v201\n" \
"v_mfma_f32_16x16x32_f16 v[4:7], v[196:199], v[144:147], v[4:7]\n" \
"v_pk_add_f16 v186, v186, v146\n" \
"v_pk_add_f16 v187, v187, v147\n" \
"s_waitcnt lgkmcnt(7)\n" \
"v_pk_add_f16 v210, v194, v202\n" \
"v_pk_add_f16 v211, v195, v203\n" \
"v_mfma_f32_16x16x32_f16 v[16:19], v[200:203], v[144:147], v[16:19]\n" \
"s_waitcnt lgkmcnt(4)\n" \
"v_pk_add_f16 v228, v212, v220\n" \
"ds_read_b128 v[160:163], v240 offset:43520\n" \
"v_mfma_f32_16x16x32_f16 v[8:11], v[204:207], v[184:187], v[8:11]\n" \
"v_pk_add_f16 v229, v213, v221\n" \
"v_mfma_f32_16x16x32_f16 v[20:23], v[208:211], v[184:187], v[20:23]\n" \
"s_waitcnt lgkmcnt(4)\n" \
"v_pk_add_f16 v230, v214, v222\n" \
"v_pk_add_f16 v231, v215, v223\n" \
"v_mfma_f32_16x16x32_f16 v[0:3], v[212:215], v[148:151], v[0:3]\n" \
"s_waitcnt lgkmcnt(2)\n" \
"v_pk_add_f16 v232, v216, v224\n" \
"v_pk_add_f16 v233, v217, v225\n" \
"ds_read_b128 v[184:187], v246 offset:2048\n" \
"v_mfma_f32_16x16x32_f16 v[12:15], v[216:219], v[148:151], v[12:15]\n" \
"v_pk_add_f16 v148, v148, v152\n" \
"v_pk_add_f16 v149, v149, v153\n" \
"v_pk_add_f16 v234, v218, v226\n" \
"v_mfma_f32_16x16x32_f16 v[4:7], v[220:223], v[152:155], v[4:7]\n" \
"v_pk_add_f16 v150, v150, v154\n" \
"v_pk_add_f16 v151, v151, v155\n" \
"v_pk_add_f16 v235, v219, v227\n" \
"v_mfma_f32_16x16x32_f16 v[16:19], v[224:227], v[152:155], v[16:19]\n" \
"ds_read_b128 v[144:147], v246 offset:2560\n" \
"v_mfma_f32_16x16x32_f16 v[8:11], v[228:231], v[148:151], v[8:11]\n" \
"v_mfma_f32_16x16x32_f16 v[20:23], v[232:235], v[148:151], v[20:23]\n" \
"v_add_u32 v241, 0xffff7000, v240\n" \
"s_waitcnt lgkmcnt(2)\n" \
"v_mfma_f32_16x16x32_f16 v[24:27], v[164:167], v[156:159], v[24:27]\n" \
"ds_read_b128 v[148:151], v246 offset:10240\n" \
"v_mfma_f32_16x16x32_f16 v[36:39], v[168:171], v[156:159], v[36:39]\n" \
"v_pk_add_f16 v156, v156, v160\n" \
"v_pk_add_f16 v157, v157, v161\n" \
"v_mfma_f32_16x16x32_f16 v[28:31], v[172:175], v[160:163], v[28:31]\n" \
"v_pk_add_f16 v158, v158, v162\n" \
"v_pk_add_f16 v159, v159, v163\n" \
"v_mfma_f32_16x16x32_f16 v[40:43], v[176:179], v[160:163], v[40:43]\n" \
"ds_read_b128 v[152:155], v246 offset:10752\n" \
"v_fmac_f32 v0, s56, v8\n" \
"v_fmac_f32 v1, s56, v9\n" \
"v_fmac_f32 v2, s56, v10\n" \
"v_fmac_f32 v3, s56, v11\n" \
"v_mfma_f32_16x16x32_f16 v[32:35], v[180:183], v[156:159], v[32:35]\n" \
"v_fmac_f32 v12, s56, v20\n" \
"v_fmac_f32 v13, s56, v21\n" \
"v_fmac_f32 v14, s56, v22\n" \
"v_fmac_f32 v15, s56, v23\n" \
"v_mfma_f32_16x16x32_f16 v[44:47], v[236:239], v[156:159], v[44:47]\n" \
"v_cvt_pk_bf16_f32 v0, v0, v1\n" \
"v_cvt_pk_bf16_f32 v1, v2, v3\n" \
"v_cvt_pk_bf16_f32 v2, v12, v13\n" \
"v_cvt_pk_bf16_f32 v3, v14, v15\n" \
"s_waitcnt lgkmcnt(2)\n" \
"v_mfma_f32_16x16x32_f16 v[24:27], v[188:191], v[184:187], v[24:27]\n" \
"ds_read_b128 v[156:159], v240 offset:24576\n" \
"buffer_store_dwordx4 v[0:3], v245, s[40:43], s69 offen sc1\n" \
"v_fmac_f32 v4, s56, v8\n" \
"v_fmac_f32 v5, s56, v9\n" \
"v_fmac_f32 v6, s56, v10\n" \
"v_fmac_f32 v7, s56, v11\n" \
"v_mfma_f32_16x16x32_f16 v[36:39], v[192:195], v[184:187], v[36:39]\n" \
"v_pk_add_f16 v184, v184, v144\n" \
"v_pk_add_f16 v185, v185, v145\n" \
"v_fmac_f32 v16, s56, v20\n" \
"v_fmac_f32 v17, s56, v21\n" \
"v_fmac_f32 v18, s56, v22\n" \
"v_fmac_f32 v19, s56, v23\n" \
"v_mfma_f32_16x16x32_f16 v[28:31], v[196:199], v[144:147], v[28:31]\n" \
"v_pk_add_f16 v186, v186, v146\n" \
"v_pk_add_f16 v187, v187, v147\n" \
"v_cvt_pk_bf16_f32 v4, v4, v5\n" \
"v_cvt_pk_bf16_f32 v5, v6, v7\n" \
"v_cvt_pk_bf16_f32 v6, v16, v17\n" \
"v_cvt_pk_bf16_f32 v7, v18, v19\n" \
"buffer_store_dwordx4 v[4:7], v245, s[40:43], s69 offen offset:128 sc1\n" \
"v_mfma_f32_16x16x32_f16 v[40:43], v[200:203], v[144:147], v[40:43]\n" \
"ds_read_b128 v[160:163], v240 offset:25088\n" \
"v_mfma_f32_16x16x32_f16 v[32:35], v[204:207], v[184:187], v[32:35]\n" \
"v_mfma_f32_16x16x32_f16 v[44:47], v[208:211], v[184:187], v[44:47]\n" \
"s_waitcnt lgkmcnt(1)\n" \
"v_mfma_f32_16x16x32_f16 v[24:27], v[212:215], v[148:151], v[24:27]\n" \
"ds_read_b128 v[184:187], v240 offset:45056\n" \
"v_mfma_f32_16x16x32_f16 v[36:39], v[216:219], v[148:151], v[36:39]\n" \
"v_pk_add_f16 v148, v148, v152\n" \
"v_pk_add_f16 v149, v149, v153\n" \
"v_mfma_f32_16x16x32_f16 v[28:31], v[220:223], v[152:155], v[28:31]\n" \
"v_pk_add_f16 v150, v150, v154\n" \
"v_pk_add_f16 v151, v151, v155\n" \
"v_mfma_f32_16x16x32_f16 v[40:43], v[224:227], v[152:155], v[40:43]\n" \
"ds_read_b128 v[144:147], v240 offset:45568\n" \
"v_mfma_f32_16x16x32_f16 v[32:35], v[228:231], v[148:151], v[32:35]\n" \
"s_waitcnt vmcnt(2)\n" \
"s_barrier\n" \
"v_mfma_f32_16x16x32_f16 v[44:47], v[232:235], v[148:151], v[44:47]\n" \
"v_mfma_f32_16x16x32_f16 v[48:51], v[120:123], v[156:159], v[48:51]\n" \
"ds_read_b128 v[148:151], v240 offset:4096\n" \
"v_mfma_f32_16x16x32_f16 v[60:63], v[124:127], v[156:159], v[60:63]\n" \
"s_waitcnt lgkmcnt(3)\n" \
"v_pk_add_f16 v156, v156, v160\n" \
"v_pk_add_f16 v157, v157, v161\n" \
"v_mfma_f32_16x16x32_f16 v[52:55], v[128:131], v[160:163], v[52:55]\n" \
"v_pk_add_f16 v158, v158, v162\n" \
"v_pk_add_f16 v159, v159, v163\n" \
"v_mfma_f32_16x16x32_f16 v[64:67], v[132:135], v[160:163], v[64:67]\n" \
"ds_read_b128 v[152:155], v240 offset:4608\n" \
"v_fmac_f32 v24, s56, v32\n" \
"v_fmac_f32 v25, s56, v33\n" \
"v_fmac_f32 v26, s56, v34\n" \
"v_mfma_f32_16x16x32_f16 v[56:59], v[136:139], v[156:159], v[56:59]\n" \
"v_fmac_f32 v27, s56, v35\n" \
"v_fmac_f32 v36, s56, v44\n" \
"v_fmac_f32 v37, s56, v45\n" \
"v_mfma_f32_16x16x32_f16 v[68:71], v[140:143], v[156:159], v[68:71]\n" \
"v_fmac_f32 v38, s56, v46\n" \
"v_fmac_f32 v39, s56, v47\n" \
"v_cvt_pk_bf16_f32 v24, v24, v25\n" \
"s_waitcnt lgkmcnt(2)\n" \
"v_mfma_f32_16x16x32_f16 v[48:51], v[164:167], v[184:187], v[48:51]\n" \
"ds_read_b128 v[156:159], v241 offset:4096\n" \
"v_cvt_pk_bf16_f32 v25, v26, v27\n" \
"v_cvt_pk_bf16_f32 v26, v36, v37\n" \
"v_cvt_pk_bf16_f32 v27, v38, v39\n" \
"v_mfma_f32_16x16x32_f16 v[60:63], v[168:171], v[184:187], v[60:63]\n" \
"v_pk_add_f16 v184, v184, v144\n" \
"v_pk_add_f16 v185, v185, v145\n" \
"buffer_store_dwordx4 v[24:27], v245, s[40:43], s70 offen sc1\n" \
"v_fmac_f32 v28, s56, v32\n" \
"v_fmac_f32 v29, s56, v33\n" \
"v_fmac_f32 v30, s56, v34\n" \
"v_mfma_f32_16x16x32_f16 v[52:55], v[172:175], v[144:147], v[52:55]\n" \
"v_pk_add_f16 v186, v186, v146\n" \
"v_pk_add_f16 v187, v187, v147\n" \
"v_fmac_f32 v31, s56, v35\n" \
"v_fmac_f32 v40, s56, v44\n" \
"v_fmac_f32 v41, s56, v45\n" \
"v_mfma_f32_16x16x32_f16 v[64:67], v[176:179], v[144:147], v[64:67]\n" \
"ds_read_b128 v[160:163], v241 offset:4608\n" \
"v_fmac_f32 v42, s56, v46\n" \
"v_fmac_f32 v43, s56, v47\n" \
"v_cvt_pk_bf16_f32 v28, v28, v29\n" \
"v_mfma_f32_16x16x32_f16 v[56:59], v[180:183], v[184:187], v[56:59]\n" \
"v_cvt_pk_bf16_f32 v29, v30, v31\n" \
"v_cvt_pk_bf16_f32 v30, v40, v41\n" \
"v_cvt_pk_bf16_f32 v31, v42, v43\n" \
"buffer_store_dwordx4 v[28:31], v245, s[40:43], s70 offen offset:128 sc1\n" \
"v_mfma_f32_16x16x32_f16 v[68:71], v[236:239], v[184:187], v[68:71]\n" \
"s_waitcnt lgkmcnt(1)\n" \
"v_mfma_f32_16x16x32_f16 v[48:51], v[188:191], v[148:151], v[48:51]\n" \
"ds_read_b128 v[184:187], v240 offset:26624\n" \
"v_mfma_f32_16x16x32_f16 v[60:63], v[192:195], v[148:151], v[60:63]\n" \
"v_pk_add_f16 v148, v148, v152\n" \
"v_pk_add_f16 v149, v149, v153\n" \
"v_mfma_f32_16x16x32_f16 v[52:55], v[196:199], v[152:155], v[52:55]\n" \
"v_pk_add_f16 v150, v150, v154\n" \
"v_pk_add_f16 v151, v151, v155\n" \
"v_mfma_f32_16x16x32_f16 v[64:67], v[200:203], v[152:155], v[64:67]\n" \
"ds_read_b128 v[144:147], v240 offset:27136\n" \
"v_mfma_f32_16x16x32_f16 v[56:59], v[204:207], v[148:151], v[56:59]\n" \
"v_mfma_f32_16x16x32_f16 v[68:71], v[208:211], v[148:151], v[68:71]\n" \
"v_mfma_f32_16x16x32_f16 v[48:51], v[212:215], v[156:159], v[48:51]\n" \
"ds_read_b128 v[148:151], v240 offset:47104\n" \
"v_mfma_f32_16x16x32_f16 v[60:63], v[216:219], v[156:159], v[60:63]\n" \
"s_waitcnt lgkmcnt(3)\n" \
"v_pk_add_f16 v156, v156, v160\n" \
"v_pk_add_f16 v157, v157, v161\n" \
"v_mfma_f32_16x16x32_f16 v[52:55], v[220:223], v[160:163], v[52:55]\n" \
"v_pk_add_f16 v158, v158, v162\n" \
"v_pk_add_f16 v159, v159, v163\n" \
"v_mfma_f32_16x16x32_f16 v[64:67], v[224:227], v[160:163], v[64:67]\n" \
"ds_read_b128 v[152:155], v240 offset:47616\n" \
"v_mfma_f32_16x16x32_f16 v[56:59], v[228:231], v[156:159], v[56:59]\n" \
"v_mfma_f32_16x16x32_f16 v[68:71], v[232:235], v[156:159], v[68:71]\n" \
"s_waitcnt lgkmcnt(3)\n" \
"v_mfma_f32_16x16x32_f16 v[72:75], v[120:123], v[184:187], v[72:75]\n" \
"ds_read_b128 v[156:159], v240 offset:6144\n" \
"v_mfma_f32_16x16x32_f16 v[84:87], v[124:127], v[184:187], v[84:87]\n" \
"s_waitcnt lgkmcnt(3)\n" \
"v_pk_add_f16 v184, v184, v144\n" \
"v_pk_add_f16 v185, v185, v145\n" \
"v_mfma_f32_16x16x32_f16 v[76:79], v[128:131], v[144:147], v[76:79]\n" \
"v_pk_add_f16 v186, v186, v146\n" \
"v_pk_add_f16 v187, v187, v147\n" \
"v_mfma_f32_16x16x32_f16 v[88:91], v[132:135], v[144:147], v[88:91]\n" \
"ds_read_b128 v[160:163], v240 offset:6656\n" \
"v_fmac_f32 v48, s56, v56\n" \
"v_fmac_f32 v49, s56, v57\n" \
"v_mfma_f32_16x16x32_f16 v[80:83], v[136:139], v[184:187], v[80:83]\n" \
"v_fmac_f32 v50, s56, v58\n" \
"v_fmac_f32 v51, s56, v59\n" \
"v_fmac_f32 v60, s56, v68\n" \
"v_mfma_f32_16x16x32_f16 v[92:95], v[140:143], v[184:187], v[92:95]\n" \
"v_fmac_f32 v61, s56, v69\n" \
"v_fmac_f32 v62, s56, v70\n" \
"s_waitcnt lgkmcnt(2)\n" \
"v_mfma_f32_16x16x32_f16 v[72:75], v[164:167], v[148:151], v[72:75]\n" \
"ds_read_b128 v[184:187], v241 offset:6144\n" \
"v_fmac_f32 v63, s56, v71\n" \
"v_cvt_pk_bf16_f32 v48, v48, v49\n" \
"v_cvt_pk_bf16_f32 v49, v50, v51\n" \
"v_mfma_f32_16x16x32_f16 v[84:87], v[168:171], v[148:151], v[84:87]\n" \
"v_pk_add_f16 v148, v148, v152\n" \
"v_pk_add_f16 v149, v149, v153\n" \
"v_cvt_pk_bf16_f32 v50, v60, v61\n" \
"v_cvt_pk_bf16_f32 v51, v62, v63\n" \
"v_mfma_f32_16x16x32_f16 v[76:79], v[172:175], v[152:155], v[76:79]\n" \
"v_pk_add_f16 v150, v150, v154\n" \
"v_pk_add_f16 v151, v151, v155\n" \
"buffer_store_dwordx4 v[48:51], v245, s[40:43], s71 offen sc1\n" \
"v_fmac_f32 v52, s56, v56\n" \
"v_fmac_f32 v53, s56, v57\n" \
"v_mfma_f32_16x16x32_f16 v[88:91], v[176:179], v[152:155], v[88:91]\n" \
"ds_read_b128 v[144:147], v241 offset:6656\n" \
"v_fmac_f32 v54, s56, v58\n" \
"v_fmac_f32 v55, s56, v59\n" \
"v_fmac_f32 v64, s56, v68\n" \
"v_mfma_f32_16x16x32_f16 v[80:83], v[180:183], v[148:151], v[80:83]\n" \
"v_fmac_f32 v65, s56, v69\n" \
"v_fmac_f32 v66, s56, v70\n" \
"v_mfma_f32_16x16x32_f16 v[92:95], v[236:239], v[148:151], v[92:95]\n" \
"v_fmac_f32 v67, s56, v71\n" \
"v_cvt_pk_bf16_f32 v52, v52, v53\n" \
"v_cvt_pk_bf16_f32 v53, v54, v55\n" \
"s_waitcnt lgkmcnt(1)\n" \
"v_mfma_f32_16x16x32_f16 v[72:75], v[188:191], v[156:159], v[72:75]\n" \
"ds_read_b128 v[148:151], v240 offset:28672\n" \
"v_cvt_pk_bf16_f32 v54, v64, v65\n" \
"v_cvt_pk_bf16_f32 v55, v66, v67\n" \
"buffer_store_dwordx4 v[52:55], v245, s[40:43], s71 offen offset:128 sc1\n" \
"v_mfma_f32_16x16x32_f16 v[84:87], v[192:195], v[156:159], v[84:87]\n" \
"v_pk_add_f16 v156, v156, v160\n" \
"v_pk_add_f16 v157, v157, v161\n" \
"v_mfma_f32_16x16x32_f16 v[76:79], v[196:199], v[160:163], v[76:79]\n" \
"v_pk_add_f16 v158, v158, v162\n" \
"v_pk_add_f16 v159, v159, v163\n" \
"v_mfma_f32_16x16x32_f16 v[88:91], v[200:203], v[160:163], v[88:91]\n" \
"ds_read_b128 v[152:155], v240 offset:29184\n" \
"v_mfma_f32_16x16x32_f16 v[80:83], v[204:207], v[156:159], v[80:83]\n" \
"v_mfma_f32_16x16x32_f16 v[92:95], v[208:211], v[156:159], v[92:95]\n" \
"v_mfma_f32_16x16x32_f16 v[72:75], v[212:215], v[184:187], v[72:75]\n" \
"ds_read_b128 v[156:159], v240 offset:49152\n" \
"v_mfma_f32_16x16x32_f16 v[84:87], v[216:219], v[184:187], v[84:87]\n" \
"s_waitcnt lgkmcnt(3)\n" \
"v_pk_add_f16 v184, v184, v144\n" \
"v_pk_add_f16 v185, v185, v145\n" \
"v_mfma_f32_16x16x32_f16 v[76:79], v[220:223], v[144:147], v[76:79]\n" \
"v_pk_add_f16 v186, v186, v146\n" \
"v_pk_add_f16 v187, v187, v147\n" \
"v_mfma_f32_16x16x32_f16 v[88:91], v[224:227], v[144:147], v[88:91]\n" \
"ds_read_b128 v[160:163], v240 offset:49664\n" \
"v_mfma_f32_16x16x32_f16 v[80:83], v[228:231], v[184:187], v[80:83]\n" \
"v_mfma_f32_16x16x32_f16 v[92:95], v[232:235], v[184:187], v[92:95]\n" \
"s_waitcnt lgkmcnt(3)\n" \
"v_mfma_f32_16x16x32_f16 v[96:99], v[120:123], v[148:151], v[96:99]\n" \
"ds_read_b128 v[184:187], v240 offset:8192\n" \
"v_mfma_f32_16x16x32_f16 v[108:111], v[124:127], v[148:151], v[108:111]\n" \
"s_waitcnt lgkmcnt(3)\n" \
"v_pk_add_f16 v148, v148, v152\n" \
"v_pk_add_f16 v149, v149, v153\n" \
"v_mfma_f32_16x16x32_f16 v[100:103], v[128:131], v[152:155], v[100:103]\n" \
"v_pk_add_f16 v150, v150, v154\n" \
"v_pk_add_f16 v151, v151, v155\n" \
"v_mfma_f32_16x16x32_f16 v[112:115], v[132:135], v[152:155], v[112:115]\n" \
"ds_read_b128 v[144:147], v240 offset:8704\n" \
"v_fmac_f32 v72, s56, v80\n" \
"v_fmac_f32 v73, s56, v81\n" \
"v_mfma_f32_16x16x32_f16 v[104:107], v[136:139], v[148:151], v[104:107]\n" \
"v_fmac_f32 v74, s56, v82\n" \
"v_fmac_f32 v75, s56, v83\n" \
"v_fmac_f32 v84, s56, v92\n" \
"v_mfma_f32_16x16x32_f16 v[116:119], v[140:143], v[148:151], v[116:119]\n" \
"v_fmac_f32 v85, s56, v93\n" \
"v_fmac_f32 v86, s56, v94\n" \
"s_waitcnt lgkmcnt(2)\n" \
"v_mfma_f32_16x16x32_f16 v[96:99], v[164:167], v[156:159], v[96:99]\n" \
"ds_read_b128 v[148:151], v241 offset:8192\n" \
"v_fmac_f32 v87, s56, v95\n" \
"v_cvt_pk_bf16_f32 v72, v72, v73\n" \
"v_cvt_pk_bf16_f32 v73, v74, v75\n" \
"v_mfma_f32_16x16x32_f16 v[108:111], v[168:171], v[156:159], v[108:111]\n" \
"v_pk_add_f16 v156, v156, v160\n" \
"v_pk_add_f16 v157, v157, v161\n" \
"v_cvt_pk_bf16_f32 v74, v84, v85\n" \
"v_cvt_pk_bf16_f32 v75, v86, v87\n" \
"v_mfma_f32_16x16x32_f16 v[100:103], v[172:175], v[160:163], v[100:103]\n" \
"v_pk_add_f16 v158, v158, v162\n" \
"v_pk_add_f16 v159, v159, v163\n" \
"buffer_store_dwordx4 v[72:75], v245, s[40:43], s72 offen sc1\n" \
"v_fmac_f32 v76, s56, v80\n" \
"v_fmac_f32 v77, s56, v81\n" \
"v_mfma_f32_16x16x32_f16 v[112:115], v[176:179], v[160:163], v[112:115]\n" \
"ds_read_b128 v[152:155], v241 offset:8704\n" \
"v_fmac_f32 v78, s56, v82\n" \
"v_fmac_f32 v79, s56, v83\n" \
"v_fmac_f32 v88, s56, v92\n" \
"v_mfma_f32_16x16x32_f16 v[104:107], v[180:183], v[156:159], v[104:107]\n" \
"v_fmac_f32 v89, s56, v93\n" \
"v_fmac_f32 v90, s56, v94\n" \
"v_mfma_f32_16x16x32_f16 v[116:119], v[236:239], v[156:159], v[116:119]\n" \
"v_fmac_f32 v91, s56, v95\n" \
"v_cvt_pk_bf16_f32 v76, v76, v77\n" \
"v_cvt_pk_bf16_f32 v77, v78, v79\n" \
"s_waitcnt lgkmcnt(1)\n" \
"v_mfma_f32_16x16x32_f16 v[96:99], v[188:191], v[184:187], v[96:99]\n" \
"v_cvt_pk_bf16_f32 v78, v88, v89\n" \
"v_cvt_pk_bf16_f32 v79, v90, v91\n" \
"buffer_store_dwordx4 v[76:79], v245, s[40:43], s72 offen offset:128 sc1\n" \
"v_mfma_f32_16x16x32_f16 v[108:111], v[192:195], v[184:187], v[108:111]\n" \
"v_pk_add_f16 v184, v184, v144\n" \
"v_pk_add_f16 v185, v185, v145\n" \
"v_mfma_f32_16x16x32_f16 v[100:103], v[196:199], v[144:147], v[100:103]\n" \
"v_pk_add_f16 v186, v186, v146\n" \
"v_pk_add_f16 v187, v187, v147\n" \
"v_mfma_f32_16x16x32_f16 v[112:115], v[200:203], v[144:147], v[112:115]\n" \
"s_nop 0\n" \
"v_mfma_f32_16x16x32_f16 v[104:107], v[204:207], v[184:187], v[104:107]\n" \
"v_mfma_f32_16x16x32_f16 v[116:119], v[208:211], v[184:187], v[116:119]\n" \
"v_mfma_f32_16x16x32_f16 v[96:99], v[212:215], v[148:151], v[96:99]\n" \
"v_mfma_f32_16x16x32_f16 v[108:111], v[216:219], v[148:151], v[108:111]\n" \
"s_waitcnt lgkmcnt(0)\n" \
"v_pk_add_f16 v148, v148, v152\n" \
"v_pk_add_f16 v149, v149, v153\n" \
"v_mfma_f32_16x16x32_f16 v[100:103], v[220:223], v[152:155], v[100:103]\n" \
"v_pk_add_f16 v150, v150, v154\n" \
"v_pk_add_f16 v151, v151, v155\n" \
"v_mfma_f32_16x16x32_f16 v[112:115], v[224:227], v[152:155], v[112:115]\n" \
"s_nop 0\n" \
"v_mfma_f32_16x16x32_f16 v[104:107], v[228:231], v[148:151], v[104:107]\n" \
"v_mfma_f32_16x16x32_f16 v[116:119], v[232:235], v[148:151], v[116:119]\n" \
"s_nop 7\n" \
"v_fmac_f32 v96, s56, v104\n" \
"v_fmac_f32 v97, s56, v105\n" \
"v_fmac_f32 v98, s56, v106\n" \
"v_fmac_f32 v99, s56, v107\n" \
"v_fmac_f32 v108, s56, v116\n" \
"v_fmac_f32 v109, s56, v117\n" \
"v_fmac_f32 v110, s56, v118\n" \
"v_fmac_f32 v111, s56, v119\n" \
"v_cvt_pk_bf16_f32 v96, v96, v97\n" \
"v_cvt_pk_bf16_f32 v97, v98, v99\n" \
"v_cvt_pk_bf16_f32 v98, v108, v109\n" \
"v_cvt_pk_bf16_f32 v99, v110, v111\n" \
"buffer_store_dwordx4 v[96:99], v245, s[40:43], s73 offen sc1\n" \
"v_fmac_f32 v100, s56, v104\n" \
"v_fmac_f32 v101, s56, v105\n" \
"v_fmac_f32 v102, s56, v106\n" \
"v_fmac_f32 v103, s56, v107\n" \
"v_fmac_f32 v112, s56, v116\n" \
"v_fmac_f32 v113, s56, v117\n" \
"v_fmac_f32 v114, s56, v118\n" \
"v_fmac_f32 v115, s56, v119\n" \
"v_cvt_pk_bf16_f32 v100, v100, v101\n" \
"v_cvt_pk_bf16_f32 v101, v102, v103\n" \
"v_cvt_pk_bf16_f32 v102, v112, v113\n" \
"v_cvt_pk_bf16_f32 v103, v114, v115\n" \
"buffer_store_dwordx4 v[100:103], v245, s[40:43], s73 offen offset:128 sc1\n" \
"s_waitcnt vmcnt(0)\n" \
"s_barrier\n" \
"s_mov_b32 exec_lo, s57\n" \
"s_mov_b32 exec_hi, 0\n" \
"buffer_store_dword v240, v248, s[36:39], s58 offen sc1\n" \
"s_mov_b64 exec, -1\n" \
"buffer_load_dword v249, v249, s[36:39], s59 offen sc1\n" \
"s_branch .Lcf_e\n" \
".Ls03_%=:\n" \
"s_mov_b32 s87, 0x200000\n" \
"s_mov_b32 s86, 0x8006000\n" \
".Lt03_%=:\n" \
"s_sleep 3\n" \
"buffer_load_dword v253, v252, s[36:39], s86 offen sc1\n" \
"s_waitcnt vmcnt(0)\n" \
"s_sub_u32 s87, s87, 1\n" \
"s_cbranch_scc1 .Lr03_%=\n" \
"v_cmp_eq_u32 vcc, 0, v253\n" \
"s_cbranch_vccnz .Lt03_%=\n" \
"s_branch .Lr03_%=\n" \
".Ls04_%=:\n" \
"s_mov_b32 s87, 0x200000\n" \
"s_mov_b32 s86, 0x8008000\n" \
".Lt04_%=:\n" \
"s_sleep 3\n" \
"buffer_load_dword v253, v252, s[36:39], s86 offen sc1\n" \
"s_waitcnt vmcnt(0)\n" \
"s_sub_u32 s87, s87, 1\n" \
"s_cbranch_scc1 .Lr04_%=\n" \
"v_cmp_eq_u32 vcc, 0, v253\n" \
"s_cbranch_vccnz .Lt04_%=\n" \
"s_branch .Lr04_%=\n" \
".Ls05_%=:\n" \
"s_mov_b32 s87, 0x200000\n" \
"s_mov_b32 s86, 0x800a000\n" \
".Lt05_%=:\n" \
"s_sleep 3\n" \
"buffer_load_dword v253, v252, s[36:39], s86 offen sc1\n" \
"s_waitcnt vmcnt(0)\n" \
"s_sub_u32 s87, s87, 1\n" \
"s_cbranch_scc1 .Lr05_%=\n" \
"v_cmp_eq_u32 vcc, 0, v253\n" \
"s_cbranch_vccnz .Lt05_%=\n" \
"s_branch .Lr05_%=\n" \
".Ls07_%=:\n" \
"s_mov_b32 s87, 0x200000\n" \
"s_mov_b32 s86, 0x800e000\n" \
".Lt07_%=:\n" \
"s_sleep 3\n" \
"buffer_load_dword v253, v252, s[36:39], s86 offen sc1\n" \
"s_waitcnt vmcnt(0)\n" \
"s_sub_u32 s87, s87, 1\n" \
"s_cbranch_scc1 .Lr07_%=\n" \
"v_cmp_eq_u32 vcc, 0, v253\n" \
"s_cbranch_vccnz .Lt07_%=\n" \
"s_branch .Lr07_%=\n" \
".Ls08_%=:\n" \
"s_mov_b32 s87, 0x200000\n" \
"s_mov_b32 s86, 0x8010000\n" \
".Lt08_%=:\n" \
"s_sleep 3\n" \
"buffer_load_dword v253, v252, s[36:39], s86 offen sc1\n" \
"s_waitcnt vmcnt(0)\n" \
"s_sub_u32 s87, s87, 1\n" \
"s_cbranch_scc1 .Lr08_%=\n" \
"v_cmp_eq_u32 vcc, 0, v253\n" \
"s_cbranch_vccnz .Lt08_%=\n" \
"s_branch .Lr08_%=\n" \
".Ls09_%=:\n" \
"s_mov_b32 s87, 0x200000\n" \
"s_mov_b32 s86, 0x8012000\n" \
".Lt09_%=:\n" \
"s_sleep 3\n" \
"buffer_load_dword v253, v252, s[36:39], s86 offen sc1\n" \
"s_waitcnt vmcnt(0)\n" \
"s_sub_u32 s87, s87, 1\n" \
"s_cbranch_scc1 .Lr09_%=\n" \
"v_cmp_eq_u32 vcc, 0, v253\n" \
"s_cbranch_vccnz .Lt09_%=\n" \
"s_branch .Lr09_%=\n" \
".Ls011_%=:\n" \
"s_mov_b32 s87, 0x200000\n" \
"s_mov_b32 s86, 0x8016000\n" \
".Lt011_%=:\n" \
"s_sleep 3\n" \
"buffer_load_dword v253, v252, s[36:39], s86 offen sc1\n" \
"s_waitcnt vmcnt(0)\n" \
"s_sub_u32 s87, s87, 1\n" \
"s_cbranch_scc1 .Lr011_%=\n" \
"v_cmp_eq_u32 vcc, 0, v253\n" \
"s_cbranch_vccnz .Lt011_%=\n" \
"s_branch .Lr011_%=\n" \
".Ls012_%=:\n" \
"s_mov_b32 s87, 0x200000\n" \
"s_mov_b32 s86, 0x8018000\n" \
".Lt012_%=:\n" \
"s_sleep 3\n" \
"buffer_load_dword v253, v252, s[36:39], s86 offen sc1\n" \
"s_waitcnt vmcnt(0)\n" \
"s_sub_u32 s87, s87, 1\n" \
"s_cbranch_scc1 .Lr012_%=\n" \
"v_cmp_eq_u32 vcc, 0, v253\n" \
"s_cbranch_vccnz .Lt012_%=\n" \
"s_branch .Lr012_%=\n" \
".Ls013_%=:\n" \
"s_mov_b32 s87, 0x200000\n" \
"s_mov_b32 s86, 0x801a000\n" \
".Lt013_%=:\n" \
"s_sleep 3\n" \
"buffer_load_dword v253, v252, s[36:39], s86 offen sc1\n" \
"s_waitcnt vmcnt(0)\n" \
"s_sub_u32 s87, s87, 1\n" \
"s_cbranch_scc1 .Lr013_%=\n" \
"v_cmp_eq_u32 vcc, 0, v253\n" \
"s_cbranch_vccnz .Lt013_%=\n" \
"s_branch .Lr013_%=\n" \
""
#define LOOP1 \
"s_mov_b32 s91, 0x40004\n" \
"s_mov_b32 s92, 0xfff8fff8\n" \
"s_mov_b32 s94, s53\n" \
"s_mov_b32 s95, s57\n" \
"s_setprio 1\n" \
"s_cmp_eq_u32 s94, 0\n" \
"s_cbranch_scc1 .Lp0_%=\n" \
".Lq0_%=:\n" \
"s_memrealtime s[86:87]\n" \
"s_waitcnt lgkmcnt(0)\n" \
"s_and_b32 s87, s94, 0xff\n" \
"s_add_i32 s75, s86, s87\n" \
".Lp0_%=:\n" \
"ds_read_b64_tr_b16 v[120:121], v241 offset:0\n" \
"ds_read_b64_tr_b16 v[122:123], v241 offset:8192\n" \
"ds_read_b128 v[144:147], v240 offset:2048\n" \
"ds_read_b64_tr_b16 v[124:125], v242 offset:0\n" \
"ds_read_b64_tr_b16 v[126:127], v242 offset:8192\n" \
"ds_read_b64_tr_b16 v[128:129], v241 offset:512\n" \
"ds_read_b64_tr_b16 v[130:131], v241 offset:8704\n" \
"ds_read_b64_tr_b16 v[132:133], v242 offset:512\n" \
"ds_read_b64_tr_b16 v[134:135], v242 offset:8704\n" \
"ds_read_b128 v[164:167], v240 offset:2560\n" \
"ds_read_b128 v[168:171], v240 offset:4608\n" \
"ds_read_b128 v[148:151], v240 offset:4096\n" \
"ds_read_b128 v[172:175], v240 offset:6656\n" \
"ds_read_b128 v[152:155], v240 offset:6144\n" \
"ds_read_b128 v[176:179], v240 offset:8704\n" \
"ds_read_b128 v[156:159], v240 offset:8192\n" \
"ds_read_b128 v[180:183], v240 offset:512\n" \
"ds_read_b128 v[160:163], v240 offset:0\n" \
"s_waitcnt lgkmcnt(15)\n" \
"v_mfma_f32_16x16x32_f16 v[0:3], v[120:123], v[144:147], 0\n" \
"s_waitcnt lgkmcnt(12)\n" \
"v_pk_add_f16 v136, v120, v128\n" \
"v_pk_add_f16 v137, v121, v129\n" \
"s_add_i32 m0, s44, 0xa000\n" \
"s_add_i32 s80, s46, 0x800000\n" \
"ds_write_b64 v246, v[96:97] offset:0\n" \
"v_mfma_f32_16x16x32_f16 v[12:15], v[124:127], v[144:147], 0\n" \
"s_waitcnt lgkmcnt(9)\n" \
"v_pk_add_f16 v144, v144, v164\n" \
"v_pk_add_f16 v138, v122, v130\n" \
"buffer_load_dwordx4 v232, s[36:39], s80 offen sc1 lds\n" \
"ds_write_b64 v246, v[98:99] offset:4096\n" \
"s_waitcnt lgkmcnt(8)\n" \
"v_mfma_f32_16x16x32_f16 v[36:39], v[124:127], v[148:151], 0\n" \
"v_pk_add_f16 v145, v145, v165\n" \
"v_pk_add_f16 v146, v146, v166\n" \
"v_pk_add_f16 v147, v147, v167\n" \
"ds_write_b64 v246, v[100:101] offset:8192\n" \
"ds_write_b64 v246, v[102:103] offset:12288\n" \
"v_mfma_f32_16x16x32_f16 v[24:27], v[120:123], v[148:151], 0\n" \
"v_pk_add_f16 v148, v148, v168\n" \
"v_pk_add_f16 v139, v123, v131\n" \
"buffer_load_dwordx4 v232, s[36:39], s80 offen offset:1024 sc1 lds\n" \
"ds_write_b64 v246, v[104:105] offset:16384\n" \
"s_waitcnt lgkmcnt(5)\n" \
"v_mfma_f32_16x16x32_f16 v[48:51], v[120:123], v[152:155], 0\n" \
"v_pk_add_f16 v149, v149, v169\n" \
"v_pk_add_f16 v140, v124, v132\n" \
"buffer_store_dwordx4 v[72:75], v235, s[36:39], s93 offen sc1\n" \
"ds_write_b64 v246, v[106:107] offset:20480\n" \
"v_mfma_f32_16x16x32_f16 v[60:63], v[124:127], v[152:155], 0\n" \
"v_pk_add_f16 v150, v150, v170\n" \
"v_pk_add_f16 v151, v151, v171\n" \
"buffer_load_dwordx4 v232, s[36:39], s80 offen offset:2048 sc1 lds\n" \
"ds_write_b64 v246, v[108:109] offset:24576\n" \
"ds_write_b64 v246, v[110:111] offset:28672\n" \
"v_mfma_f32_16x16x32_f16 v[84:87], v[124:127], v[156:159], 0\n" \
"v_pk_add_f16 v152, v152, v172\n" \
"v_pk_add_f16 v141, v125, v133\n" \
"ds_write_b64 v246, v[112:113] offset:32768\n" \
"ds_write_b64 v246, v[114:115] offset:36864\n" \
"v_mfma_f32_16x16x32_f16 v[72:75], v[120:123], v[156:159], 0\n" \
"v_pk_add_f16 v153, v153, v173\n" \
"v_pk_add_f16 v142, v126, v134\n" \
"buffer_load_dwordx4 v232, s[36:39], s80 offen offset:3072 sc1 lds\n" \
"ds_write_b64 v246, v[116:117] offset:40960\n" \
"v_mfma_f32_16x16x32_f16 v[96:99], v[120:123], v[160:163], 0\n" \
"v_pk_add_f16 v154, v154, v174\n" \
"v_pk_add_f16 v155, v155, v175\n" \
"v_mfma_f32_16x16x32_f16 v[108:111], v[124:127], v[160:163], 0\n" \
"v_pk_add_f16 v156, v156, v176\n" \
"v_pk_add_f16 v143, v127, v135\n" \
"s_waitcnt vmcnt(8)\n" \
"s_barrier\n" \
"v_mfma_f32_16x16x32_f16 v[4:7], v[128:131], v[164:167], 0\n" \
"v_pk_add_f16 v157, v157, v177\n" \
"ds_read_b64_tr_b16 v[120:121], v241 offset:16384\n" \
"s_add_i32 m0, s45, 0xa000\n" \
"s_add_i32 s81, s47, 0x800000\n" \
"v_mfma_f32_16x16x32_f16 v[16:19], v[132:135], v[164:167], 0\n" \
"v_pk_add_f16 v158, v158, v178\n" \
"ds_read_b128 v[184:187], v240 offset:23040\n" \
"buffer_load_dwordx4 v232, s[36:39], s81 offen sc1 lds\n" \
"v_mfma_f32_16x16x32_f16 v[40:43], v[132:135], v[168:171], 0\n" \
"v_pk_add_f16 v159, v159, v179\n" \
"ds_read_b128 v[164:167], v240 offset:22528\n" \
"buffer_store_dwordx4 v[196:199], v235, s[36:39], s60 offen sc1\n" \
"v_mfma_f32_16x16x32_f16 v[28:31], v[128:131], v[168:171], 0\n" \
"v_pk_add_f16 v160, v160, v180\n" \
"ds_read_b64_tr_b16 v[122:123], v241 offset:24576\n" \
"s_add_i32 m0, s44, 0x0\n" \
"s_add_i32 s80, s46, 0xc00000\n" \
"v_mfma_f32_16x16x32_f16 v[52:55], v[128:131], v[172:175], 0\n" \
"v_pk_add_f16 v161, v161, v181\n" \
"ds_read_b128 v[168:171], v240 offset:24576\n" \
"buffer_load_dwordx4 v232, s[36:39], s80 offen sc1 lds\n" \
"v_mfma_f32_16x16x32_f16 v[64:67], v[132:135], v[172:175], 0\n" \
"v_pk_add_f16 v162, v162, v182\n" \
"ds_read_b64_tr_b16 v[124:125], v242 offset:16384\n" \
"v_mfma_f32_16x16x32_f16 v[88:91], v[132:135], v[176:179], 0\n" \
"v_pk_add_f16 v163, v163, v183\n" \
"ds_read_b128 v[172:175], v240 offset:26624\n" \
"v_mfma_f32_16x16x32_f16 v[76:79], v[128:131], v[176:179], 0\n" \
"s_setprio 0\n" \
"ds_read_b64_tr_b16 v[126:127], v242 offset:24576\n" \
"buffer_load_dwordx4 v232, s[36:39], s80 offen offset:1024 sc1 lds\n" \
"v_mfma_f32_16x16x32_f16 v[100:103], v[128:131], v[180:183], 0\n" \
"ds_read_b128 v[176:179], v240 offset:28672\n" \
"buffer_store_dwordx4 v[200:203], v235, s[36:39], s61 offen sc1\n" \
"v_mfma_f32_16x16x32_f16 v[112:115], v[132:135], v[180:183], 0\n" \
"ds_read_b64_tr_b16 v[128:129], v241 offset:16896\n" \
"v_mfma_f32_16x16x32_f16 v[8:11], v[136:139], v[144:147], 0\n" \
"ds_read_b128 v[180:183], v240 offset:20480\n" \
"buffer_load_dwordx4 v232, s[36:39], s80 offen offset:2048 sc1 lds\n" \
"v_mfma_f32_16x16x32_f16 v[20:23], v[140:143], v[144:147], 0\n" \
"ds_read_b128 v[144:147], v240 offset:25088\n" \
"v_mfma_f32_16x16x32_f16 v[44:47], v[140:143], v[148:151], 0\n" \
"ds_read_b64_tr_b16 v[130:131], v241 offset:25088\n" \
"ds_read_b64_tr_b16 v[132:133], v242 offset:16896\n" \
"v_mfma_f32_16x16x32_f16 v[32:35], v[136:139], v[148:151], 0\n" \
"ds_read_b128 v[148:151], v240 offset:27136\n" \
"buffer_load_dwordx4 v232, s[36:39], s80 offen offset:3072 sc1 lds\n" \
"v_mfma_f32_16x16x32_f16 v[56:59], v[136:139], v[152:155], 0\n" \
"ds_read_b64_tr_b16 v[134:135], v242 offset:25088\n" \
"v_mfma_f32_16x16x32_f16 v[68:71], v[140:143], v[152:155], 0\n" \
"ds_read_b128 v[152:155], v240 offset:29184\n" \
"v_mfma_f32_16x16x32_f16 v[92:95], v[140:143], v[156:159], 0\n" \
"s_add_i32 m0, s45, 0x0\n" \
"s_add_i32 s81, s47, 0xc00000\n" \
"v_mfma_f32_16x16x32_f16 v[80:83], v[136:139], v[156:159], 0\n" \
"ds_read_b128 v[156:159], v240 offset:20992\n" \
"buffer_load_dwordx4 v232, s[36:39], s81 offen sc1 lds\n" \
"v_mfma_f32_16x16x32_f16 v[104:107], v[136:139], v[160:163], 0\n" \
"v_mfma_f32_16x16x32_f16 v[116:119], v[140:143], v[160:163], 0\n" \
"s_setprio 1\n" \
"s_waitcnt vmcnt(7) lgkmcnt(0)\n" \
"s_cmp_eq_u32 s94, 0\n" \
"s_cbranch_scc1 .Lp1_%=\n" \
".Lq1_%=:\n" \
"s_memrealtime s[86:87]\n" \
"s_waitcnt lgkmcnt(0)\n" \
"s_sub_i32 s87, s86, s75\n" \
"s_cmp_lt_i32 s87, 0\n" \
"s_cbranch_scc1 .Lq1_%=\n" \
"s_lshr_b64 s[94:95], s[94:95], 8\n" \
"s_and_b32 s87, s94, 0xff\n" \
"s_add_i32 s75, s86, s87\n" \
".Lp1_%=:\n" \
"s_barrier\n" \
"v_mfma_f32_16x16x32_f16 v[0:3], v[120:123], v[164:167], v[0:3]\n" \
"v_pk_add_f16 v136, v120, v128\n" \
"v_pk_add_f16 v137, v121, v129\n" \
"ds_read_b64_tr_b16 v[188:189], v236 offset:0\n" \
"v_mfma_f32_16x16x32_f16 v[12:15], v[124:127], v[164:167], v[12:15]\n" \
"v_pk_add_f16 v164, v164, v184\n" \
"v_pk_add_f16 v138, v122, v130\n" \
"ds_read_b64_tr_b16 v[192:193], v237 offset:0\n" \
"v_mfma_f32_16x16x32_f16 v[36:39], v[124:127], v[168:171], v[36:39]\n" \
"v_pk_add_f16 v165, v165, v185\n" \
"v_pk_add_f16 v166, v166, v186\n" \
"v_pk_add_f16 v167, v167, v187\n" \
"v_mfma_f32_16x16x32_f16 v[24:27], v[120:123], v[168:171], v[24:27]\n" \
"v_pk_add_f16 v168, v168, v144\n" \
"v_pk_add_f16 v139, v123, v131\n" \
"s_add_i32 m0, s44, 0x5000\n" \
"s_add_i32 s80, s46, 0x1000000\n" \
"v_mfma_f32_16x16x32_f16 v[48:51], v[120:123], v[172:175], v[48:51]\n" \
"v_pk_add_f16 v169, v169, v145\n" \
"v_pk_add_f16 v140, v124, v132\n" \
"buffer_load_dwordx4 v232, s[36:39], s80 offen sc1 lds\n" \
"v_mfma_f32_16x16x32_f16 v[60:63], v[124:127], v[172:175], v[60:63]\n" \
"v_pk_add_f16 v170, v170, v146\n" \
"v_pk_add_f16 v171, v171, v147\n" \
"s_waitcnt lgkmcnt(1)\n" \
"v_mfma_f32_16x16x16_bf16 v[188:191], v[188:189], v[254:255], 0\n" \
"v_mfma_f32_16x16x32_f16 v[84:87], v[124:127], v[176:179], v[84:87]\n" \
"v_pk_add_f16 v172, v172, v148\n" \
"v_pk_add_f16 v141, v125, v133\n" \
"s_waitcnt lgkmcnt(0)\n" \
"v_mfma_f32_16x16x16_bf16 v[192:195], v[192:193], v[254:255], 0\n" \
"v_mfma_f32_16x16x32_f16 v[72:75], v[120:123], v[176:179], v[72:75]\n" \
"v_pk_add_f16 v173, v173, v149\n" \
"v_pk_add_f16 v142, v126, v134\n" \
"v_mfma_f32_16x16x32_f16 v[96:99], v[120:123], v[180:183], v[96:99]\n" \
"v_pk_add_f16 v174, v174, v150\n" \
"v_pk_add_f16 v175, v175, v151\n" \
"v_mfma_f32_16x16x32_f16 v[108:111], v[124:127], v[180:183], v[108:111]\n" \
"v_pk_add_f16 v176, v176, v152\n" \
"v_pk_add_f16 v143, v127, v135\n" \
"v_cvt_pk_f16_f32 v188, v188, v189\n" \
"v_cvt_pk_f16_f32 v189, v190, v191\n" \
"v_mfma_f32_16x16x32_f16 v[4:7], v[128:131], v[184:187], v[4:7]\n" \
"v_pk_add_f16 v177, v177, v153\n" \
"v_cvt_pk_f16_f32 v190, v192, v193\n" \
"ds_read_b64_tr_b16 v[120:121], v241 offset:32768\n" \
"buffer_load_dwordx4 v232, s[36:39], s80 offen offset:1024 sc1 lds\n" \
"v_mfma_f32_16x16x32_f16 v[16:19], v[132:135], v[184:187], v[16:19]\n" \
"v_pk_add_f16 v178, v178, v154\n" \
"v_cvt_pk_f16_f32 v191, v194, v195\n" \
"v_add_u32 v188, s91, v188\n" \
"ds_read_b128 v[160:163], v240 offset:43520\n" \
"v_mfma_f32_16x16x32_f16 v[40:43], v[132:135], v[144:147], v[40:43]\n" \
"v_pk_add_f16 v179, v179, v155\n" \
"v_add_u32 v189, s91, v189\n" \
"v_add_u32 v190, s91, v190\n" \
"ds_read_b128 v[184:187], v240 offset:43008\n" \
"v_mfma_f32_16x16x32_f16 v[28:31], v[128:131], v[144:147], v[28:31]\n" \
"v_pk_add_f16 v180, v180, v156\n" \
"v_add_u32 v191, s91, v191\n" \
"v_and_b32 v188, s92, v188\n" \
"ds_read_b64_tr_b16 v[122:123], v241 offset:40960\n" \
"s_add_i32 s82, s49, 0x2800000\n" \
"v_mfma_f32_16x16x32_f16 v[52:55], v[128:131], v[148:151], v[52:55]\n" \
"v_pk_add_f16 v181, v181, v157\n" \
"v_and_b32 v189, s92, v189\n" \
"v_and_b32 v190, s92, v190\n" \
"v_and_b32 v191, s92, v191\n" \
"ds_read_b128 v[144:147], v240 offset:45056\n" \
"v_mfma_f32_16x16x32_f16 v[64:67], v[132:135], v[148:151], v[64:67]\n" \
"v_pk_add_f16 v182, v182, v158\n" \
"ds_read_b64_tr_b16 v[124:125], v242 offset:32768\n" \
"buffer_store_dwordx4 v[188:191], v234, s[36:39], s82 offen sc1\n" \
"v_mfma_f32_16x16x32_f16 v[88:91], v[132:135], v[152:155], v[88:91]\n" \
"v_pk_add_f16 v183, v183, v159\n" \
"ds_read_b128 v[148:151], v240 offset:47104\n" \
"buffer_load_dwordx4 v232, s[36:39], s80 offen offset:2048 sc1 lds\n" \
"v_mfma_f32_16x16x32_f16 v[76:79], v[128:131], v[152:155], v[76:79]\n" \
"s_setprio 0\n" \
"ds_read_b64_tr_b16 v[126:127], v242 offset:40960\n" \
"ds_read_b64_tr_b16 v[188:189], v238 offset:0\n" \
"v_mfma_f32_16x16x32_f16 v[100:103], v[128:131], v[156:159], v[100:103]\n" \
"ds_read_b128 v[152:155], v240 offset:49152\n" \
"ds_read_b64_tr_b16 v[192:193], v239 offset:0\n" \
"v_mfma_f32_16x16x32_f16 v[112:115], v[132:135], v[156:159], v[112:115]\n" \
"ds_read_b64_tr_b16 v[128:129], v241 offset:33280\n" \
"v_mfma_f32_16x16x32_f16 v[8:11], v[136:139], v[164:167], v[8:11]\n" \
"ds_read_b128 v[156:159], v240 offset:40960\n" \
"s_waitcnt lgkmcnt(4)\n" \
"v_mfma_f32_16x16x16_bf16 v[188:191], v[188:189], v[254:255], 0\n" \
"v_mfma_f32_16x16x32_f16 v[20:23], v[140:143], v[164:167], v[20:23]\n" \
"ds_read_b128 v[164:167], v240 offset:45568\n" \
"s_waitcnt lgkmcnt(3)\n" \
"v_mfma_f32_16x16x16_bf16 v[192:195], v[192:193], v[254:255], 0\n" \
"v_mfma_f32_16x16x32_f16 v[44:47], v[140:143], v[168:171], v[44:47]\n" \
"ds_read_b64_tr_b16 v[130:131], v241 offset:41472\n" \
"ds_read_b64_tr_b16 v[132:133], v242 offset:33280\n" \
"buffer_load_dwordx4 v232, s[36:39], s80 offen offset:3072 sc1 lds\n" \
"v_mfma_f32_16x16x32_f16 v[32:35], v[136:139], v[168:171], v[32:35]\n" \
"ds_read_b128 v[168:171], v240 offset:47616\n" \
"v_mfma_f32_16x16x32_f16 v[56:59], v[136:139], v[172:175], v[56:59]\n" \
"v_cvt_pk_f16_f32 v188, v188, v189\n" \
"v_cvt_pk_f16_f32 v189, v190, v191\n" \
"ds_read_b64_tr_b16 v[134:135], v242 offset:41472\n" \
"v_mfma_f32_16x16x32_f16 v[68:71], v[140:143], v[172:175], v[68:71]\n" \
"v_cvt_pk_f16_f32 v190, v192, v193\n" \
"v_cvt_pk_f16_f32 v191, v194, v195\n" \
"ds_read_b128 v[172:175], v240 offset:49664\n" \
"v_mfma_f32_16x16x32_f16 v[92:95], v[140:143], v[176:179], v[92:95]\n" \
"v_add_u32 v188, s91, v188\n" \
"v_add_u32 v189, s91, v189\n" \
"v_add_u32 v190, s91, v190\n" \
"s_add_i32 m0, s45, 0x5000\n" \
"s_add_i32 s81, s47, 0x1000000\n" \
"v_mfma_f32_16x16x32_f16 v[80:83], v[136:139], v[176:179], v[80:83]\n" \
"v_add_u32 v191, s91, v191\n" \
"v_and_b32 v188, s92, v188\n" \
"ds_read_b128 v[176:179], v240 offset:41472\n" \
"buffer_load_dwordx4 v232, s[36:39], s81 offen sc1 lds\n" \
"v_mfma_f32_16x16x32_f16 v[104:107], v[136:139], v[180:183], v[104:107]\n" \
"v_and_b32 v189, s92, v189\n" \
"v_and_b32 v190, s92, v190\n" \
"v_and_b32 v191, s92, v191\n" \
"v_mfma_f32_16x16x32_f16 v[116:119], v[140:143], v[180:183], v[116:119]\n" \
"s_setprio 1\n" \
"buffer_store_dwordx4 v[188:191], v234, s[36:39], s82 offen offset:1024 sc1\n" \
"s_waitcnt vmcnt(7) lgkmcnt(0)\n" \
"s_cmp_eq_u32 s94, 0\n" \
"s_cbranch_scc1 .Lp2_%=\n" \
".Lq2_%=:\n" \
"s_memrealtime s[86:87]\n" \
"s_waitcnt lgkmcnt(0)\n" \
"s_sub_i32 s87, s86, s75\n" \
"s_cmp_lt_i32 s87, 0\n" \
"s_cbranch_scc1 .Lq2_%=\n" \
"s_lshr_b64 s[94:95], s[94:95], 8\n" \
"s_and_b32 s87, s94, 0xff\n" \
"s_add_i32 s75, s86, s87\n" \
".Lp2_%=:\n" \
"s_barrier\n" \
"v_mfma_f32_16x16x32_f16 v[0:3], v[120:123], v[184:187], v[0:3]\n" \
"v_pk_add_f16 v136, v120, v128\n" \
"v_pk_add_f16 v137, v121, v129\n" \
"v_mfma_f32_16x16x32_f16 v[12:15], v[124:127], v[184:187], v[12:15]\n" \
"v_pk_add_f16 v184, v184, v160\n" \
"v_pk_add_f16 v138, v122, v130\n" \
"v_mfma_f32_16x16x32_f16 v[36:39], v[124:127], v[144:147], v[36:39]\n" \
"v_pk_add_f16 v185, v185, v161\n" \
"v_pk_add_f16 v186, v186, v162\n" \
"v_pk_add_f16 v187, v187, v163\n" \
"buffer_store_dwordx4 v[204:207], v235, s[36:39], s62 offen sc1\n" \
"v_mfma_f32_16x16x32_f16 v[24:27], v[120:123], v[144:147], v[24:27]\n" \
"v_pk_add_f16 v144, v144, v164\n" \
"v_pk_add_f16 v139, v123, v131\n" \
"s_add_i32 m0, s44, 0xa000\n" \
"s_add_i32 s80, s46, 0x1400000\n" \
"v_mfma_f32_16x16x32_f16 v[48:51], v[120:123], v[148:151], v[48:51]\n" \
"v_pk_add_f16 v145, v145, v165\n" \
"v_pk_add_f16 v140, v124, v132\n" \
"buffer_load_dwordx4 v232, s[36:39], s80 offen sc1 lds\n" \
"v_mfma_f32_16x16x32_f16 v[60:63], v[124:127], v[148:151], v[60:63]\n" \
"v_pk_add_f16 v146, v146, v166\n" \
"v_pk_add_f16 v147, v147, v167\n" \
"v_mfma_f32_16x16x32_f16 v[84:87], v[124:127], v[152:155], v[84:87]\n" \
"v_pk_add_f16 v148, v148, v168\n" \
"v_pk_add_f16 v141, v125, v133\n" \
"v_mfma_f32_16x16x32_f16 v[72:75], v[120:123], v[152:155], v[72:75]\n" \
"v_pk_add_f16 v149, v149, v169\n" \
"v_pk_add_f16 v142, v126, v134\n" \
"v_mfma_f32_16x16x32_f16 v[96:99], v[120:123], v[156:159], v[96:99]\n" \
"v_pk_add_f16 v150, v150, v170\n" \
"v_pk_add_f16 v151, v151, v171\n" \
"ds_read_b64_tr_b16 v[188:189], v236 offset:4096\n" \
"v_mfma_f32_16x16x32_f16 v[108:111], v[124:127], v[156:159], v[108:111]\n" \
"v_pk_add_f16 v152, v152, v172\n" \
"v_pk_add_f16 v143, v127, v135\n" \
"ds_read_b64_tr_b16 v[192:193], v237 offset:4096\n" \
"v_mfma_f32_16x16x32_f16 v[4:7], v[128:131], v[160:163], v[4:7]\n" \
"v_pk_add_f16 v153, v153, v173\n" \
"ds_read_b64_tr_b16 v[120:121], v241 offset:0\n" \
"buffer_load_dwordx4 v232, s[36:39], s80 offen offset:1024 sc1 lds\n" \
"v_mfma_f32_16x16x32_f16 v[16:19], v[132:135], v[160:163], v[16:19]\n" \
"v_pk_add_f16 v154, v154, v174\n" \
"ds_read_b128 v[180:183], v240 offset:2560\n" \
"v_mfma_f32_16x16x32_f16 v[40:43], v[132:135], v[164:167], v[40:43]\n" \
"v_pk_add_f16 v155, v155, v175\n" \
"ds_read_b128 v[160:163], v240 offset:2048\n" \
"v_mfma_f32_16x16x32_f16 v[28:31], v[128:131], v[164:167], v[28:31]\n" \
"v_pk_add_f16 v156, v156, v176\n" \
"ds_read_b64_tr_b16 v[122:123], v241 offset:8192\n" \
"v_mfma_f32_16x16x32_f16 v[52:55], v[128:131], v[168:171], v[52:55]\n" \
"v_pk_add_f16 v157, v157, v177\n" \
"ds_read_b128 v[164:167], v240 offset:4096\n" \
"s_waitcnt lgkmcnt(6)\n" \
"v_mfma_f32_16x16x16_bf16 v[188:191], v[188:189], v[254:255], 0\n" \
"v_mfma_f32_16x16x32_f16 v[64:67], v[132:135], v[168:171], v[64:67]\n" \
"v_pk_add_f16 v158, v158, v178\n" \
"ds_read_b64_tr_b16 v[124:125], v242 offset:0\n" \
"s_waitcnt lgkmcnt(6)\n" \
"v_mfma_f32_16x16x16_bf16 v[192:195], v[192:193], v[254:255], 0\n" \
"v_mfma_f32_16x16x32_f16 v[88:91], v[132:135], v[172:175], v[88:91]\n" \
"v_pk_add_f16 v159, v159, v179\n" \
"ds_read_b128 v[168:171], v240 offset:6144\n" \
"buffer_load_dwordx4 v232, s[36:39], s80 offen offset:2048 sc1 lds\n" \
"v_mfma_f32_16x16x32_f16 v[76:79], v[128:131], v[172:175], v[76:79]\n" \
"s_setprio 0\n" \
"ds_read_b64_tr_b16 v[126:127], v242 offset:8192\n" \
"v_mfma_f32_16x16x32_f16 v[100:103], v[128:131], v[176:179], v[100:103]\n" \
"v_cvt_pk_f16_f32 v188, v188, v189\n" \
"ds_read_b128 v[172:175], v240 offset:8192\n" \
"v_mfma_f32_16x16x32_f16 v[112:115], v[132:135], v[176:179], v[112:115]\n" \
"v_cvt_pk_f16_f32 v189, v190, v191\n" \
"ds_read_b64_tr_b16 v[128:129], v241 offset:512\n" \
"v_mfma_f32_16x16x32_f16 v[8:11], v[136:139], v[184:187], v[8:11]\n" \
"v_cvt_pk_f16_f32 v190, v192, v193\n" \
"ds_read_b128 v[176:179], v240 offset:0\n" \
"v_mfma_f32_16x16x32_f16 v[20:23], v[140:143], v[184:187], v[20:23]\n" \
"v_cvt_pk_f16_f32 v191, v194, v195\n" \
"v_add_u32 v188, s91, v188\n" \
"ds_read_b128 v[184:187], v240 offset:4608\n" \
"v_mfma_f32_16x16x32_f16 v[44:47], v[140:143], v[144:147], v[44:47]\n" \
"ds_read_b64_tr_b16 v[130:131], v241 offset:8704\n" \
"ds_read_b64_tr_b16 v[132:133], v242 offset:512\n" \
"buffer_load_dwordx4 v232, s[36:39], s80 offen offset:3072 sc1 lds\n" \
"v_mfma_f32_16x16x32_f16 v[32:35], v[136:139], v[144:147], v[32:35]\n" \
"v_add_u32 v189, s91, v189\n" \
"v_add_u32 v190, s91, v190\n" \
"ds_read_b128 v[144:147], v240 offset:6656\n" \
"v_mfma_f32_16x16x32_f16 v[56:59], v[136:139], v[148:151], v[56:59]\n" \
"v_add_u32 v191, s91, v191\n" \
"ds_read_b64_tr_b16 v[134:135], v242 offset:8704\n" \
"v_mfma_f32_16x16x32_f16 v[68:71], v[140:143], v[148:151], v[68:71]\n" \
"v_and_b32 v188, s92, v188\n" \
"ds_read_b128 v[148:151], v240 offset:8704\n" \
"v_mfma_f32_16x16x32_f16 v[92:95], v[140:143], v[152:155], v[92:95]\n" \
"v_and_b32 v189, s92, v189\n" \
"s_add_i32 m0, s45, 0xa000\n" \
"s_add_i32 s81, s47, 0x1400000\n" \
"s_add_i32 s82, s50, 0x3000000\n" \
"v_mfma_f32_16x16x32_f16 v[80:83], v[136:139], v[152:155], v[80:83]\n" \
"v_and_b32 v190, s92, v190\n" \
"v_and_b32 v191, s92, v191\n" \
"ds_read_b128 v[152:155], v240 offset:512\n" \
"buffer_load_dwordx4 v232, s[36:39], s81 offen sc1 lds\n" \
"v_mfma_f32_16x16x32_f16 v[104:107], v[136:139], v[156:159], v[104:107]\n" \
"buffer_store_dwordx4 v[188:191], v234, s[36:39], s82 offen sc1\n" \
"v_mfma_f32_16x16x32_f16 v[116:119], v[140:143], v[156:159], v[116:119]\n" \
"s_setprio 1\n" \
"s_waitcnt vmcnt(7) lgkmcnt(0)\n" \
"s_cmp_eq_u32 s94, 0\n" \
"s_cbranch_scc1 .Lp3_%=\n" \
".Lq3_%=:\n" \
"s_memrealtime s[86:87]\n" \
"s_waitcnt lgkmcnt(0)\n" \
"s_sub_i32 s87, s86, s75\n" \
"s_cmp_lt_i32 s87, 0\n" \
"s_cbranch_scc1 .Lq3_%=\n" \
"s_lshr_b64 s[94:95], s[94:95], 8\n" \
"s_and_b32 s87, s94, 0xff\n" \
"s_add_i32 s75, s86, s87\n" \
".Lp3_%=:\n" \
"s_barrier\n" \
"v_mfma_f32_16x16x32_f16 v[0:3], v[120:123], v[160:163], v[0:3]\n" \
"v_pk_add_f16 v136, v120, v128\n" \
"v_pk_add_f16 v137, v121, v129\n" \
"v_mfma_f32_16x16x32_f16 v[12:15], v[124:127], v[160:163], v[12:15]\n" \
"v_pk_add_f16 v160, v160, v180\n" \
"v_pk_add_f16 v138, v122, v130\n" \
"v_mfma_f32_16x16x32_f16 v[36:39], v[124:127], v[164:167], v[36:39]\n" \
"v_pk_add_f16 v161, v161, v181\n" \
"v_pk_add_f16 v162, v162, v182\n" \
"v_pk_add_f16 v163, v163, v183\n" \
"v_mfma_f32_16x16x32_f16 v[24:27], v[120:123], v[164:167], v[24:27]\n" \
"v_pk_add_f16 v164, v164, v184\n" \
"v_pk_add_f16 v139, v123, v131\n" \
"s_add_i32 m0, s44, 0x0\n" \
"s_add_i32 s80, s46, 0x1800000\n" \
"v_mfma_f32_16x16x32_f16 v[48:51], v[120:123], v[168:171], v[48:51]\n" \
"v_pk_add_f16 v165, v165, v185\n" \
"v_pk_add_f16 v140, v124, v132\n" \
"buffer_load_dwordx4 v232, s[36:39], s80 offen sc1 lds\n" \
"v_mfma_f32_16x16x32_f16 v[60:63], v[124:127], v[168:171], v[60:63]\n" \
"v_pk_add_f16 v166, v166, v186\n" \
"v_pk_add_f16 v167, v167, v187\n" \
"v_mfma_f32_16x16x32_f16 v[84:87], v[124:127], v[172:175], v[84:87]\n" \
"v_pk_add_f16 v168, v168, v144\n" \
"v_pk_add_f16 v141, v125, v133\n" \
"v_mfma_f32_16x16x32_f16 v[72:75], v[120:123], v[172:175], v[72:75]\n" \
"v_pk_add_f16 v169, v169, v145\n" \
"v_pk_add_f16 v142, v126, v134\n" \
"v_mfma_f32_16x16x32_f16 v[96:99], v[120:123], v[176:179], v[96:99]\n" \
"v_pk_add_f16 v170, v170, v146\n" \
"v_pk_add_f16 v171, v171, v147\n" \
"ds_read_b64_tr_b16 v[188:189], v238 offset:4096\n" \
"v_mfma_f32_16x16x32_f16 v[108:111], v[124:127], v[176:179], v[108:111]\n" \
"v_pk_add_f16 v172, v172, v148\n" \
"v_pk_add_f16 v143, v127, v135\n" \
"ds_read_b64_tr_b16 v[192:193], v239 offset:4096\n" \
"v_mfma_f32_16x16x32_f16 v[4:7], v[128:131], v[180:183], v[4:7]\n" \
"v_pk_add_f16 v173, v173, v149\n" \
"ds_read_b64_tr_b16 v[120:121], v241 offset:16384\n" \
"buffer_load_dwordx4 v232, s[36:39], s80 offen offset:1024 sc1 lds\n" \
"v_mfma_f32_16x16x32_f16 v[16:19], v[132:135], v[180:183], v[16:19]\n" \
"v_pk_add_f16 v174, v174, v150\n" \
"ds_read_b128 v[156:159], v240 offset:23040\n" \
"v_mfma_f32_16x16x32_f16 v[40:43], v[132:135], v[184:187], v[40:43]\n" \
"v_pk_add_f16 v175, v175, v151\n" \
"ds_read_b128 v[180:183], v240 offset:22528\n" \
"v_mfma_f32_16x16x32_f16 v[28:31], v[128:131], v[184:187], v[28:31]\n" \
"v_pk_add_f16 v176, v176, v152\n" \
"ds_read_b64_tr_b16 v[122:123], v241 offset:24576\n" \
"v_mfma_f32_16x16x32_f16 v[52:55], v[128:131], v[144:147], v[52:55]\n" \
"v_pk_add_f16 v177, v177, v153\n" \
"ds_read_b128 v[184:187], v240 offset:24576\n" \
"s_waitcnt lgkmcnt(6)\n" \
"v_mfma_f32_16x16x16_bf16 v[188:191], v[188:189], v[254:255], 0\n" \
"v_mfma_f32_16x16x32_f16 v[64:67], v[132:135], v[144:147], v[64:67]\n" \
"v_pk_add_f16 v178, v178, v154\n" \
"ds_read_b64_tr_b16 v[124:125], v242 offset:16384\n" \
"s_waitcnt lgkmcnt(6)\n" \
"v_mfma_f32_16x16x16_bf16 v[192:195], v[192:193], v[254:255], 0\n" \
"v_mfma_f32_16x16x32_f16 v[88:91], v[132:135], v[148:151], v[88:91]\n" \
"v_pk_add_f16 v179, v179, v155\n" \
"ds_read_b128 v[144:147], v240 offset:26624\n" \
"buffer_load_dwordx4 v232, s[36:39], s80 offen offset:2048 sc1 lds\n" \
"v_mfma_f32_16x16x32_f16 v[76:79], v[128:131], v[148:151], v[76:79]\n" \
"s_setprio 0\n" \
"ds_read_b64_tr_b16 v[126:127], v242 offset:24576\n" \
"v_mfma_f32_16x16x32_f16 v[100:103], v[128:131], v[152:155], v[100:103]\n" \
"v_cvt_pk_f16_f32 v188, v188, v189\n" \
"ds_read_b128 v[148:151], v240 offset:28672\n" \
"v_mfma_f32_16x16x32_f16 v[112:115], v[132:135], v[152:155], v[112:115]\n" \
"v_cvt_pk_f16_f32 v189, v190, v191\n" \
"ds_read_b64_tr_b16 v[128:129], v241 offset:16896\n" \
"v_mfma_f32_16x16x32_f16 v[8:11], v[136:139], v[160:163], v[8:11]\n" \
"v_cvt_pk_f16_f32 v190, v192, v193\n" \
"ds_read_b128 v[152:155], v240 offset:20480\n" \
"v_mfma_f32_16x16x32_f16 v[20:23], v[140:143], v[160:163], v[20:23]\n" \
"v_cvt_pk_f16_f32 v191, v194, v195\n" \
"v_add_u32 v188, s91, v188\n" \
"ds_read_b128 v[160:163], v240 offset:25088\n" \
"v_mfma_f32_16x16x32_f16 v[44:47], v[140:143], v[164:167], v[44:47]\n" \
"ds_read_b64_tr_b16 v[130:131], v241 offset:25088\n" \
"ds_read_b64_tr_b16 v[132:133], v242 offset:16896\n" \
"buffer_load_dwordx4 v232, s[36:39], s80 offen offset:3072 sc1 lds\n" \
"v_mfma_f32_16x16x32_f16 v[32:35], v[136:139], v[164:167], v[32:35]\n" \
"v_add_u32 v189, s91, v189\n" \
"v_add_u32 v190, s91, v190\n" \
"ds_read_b128 v[164:167], v240 offset:27136\n" \
"v_mfma_f32_16x16x32_f16 v[56:59], v[136:139], v[168:171], v[56:59]\n" \
"v_add_u32 v191, s91, v191\n" \
"ds_read_b64_tr_b16 v[134:135], v242 offset:25088\n" \
"v_mfma_f32_16x16x32_f16 v[68:71], v[140:143], v[168:171], v[68:71]\n" \
"v_and_b32 v188, s92, v188\n" \
"ds_read_b128 v[168:171], v240 offset:29184\n" \
"v_mfma_f32_16x16x32_f16 v[92:95], v[140:143], v[172:175], v[92:95]\n" \
"v_and_b32 v189, s92, v189\n" \
"s_add_i32 m0, s45, 0x0\n" \
"s_add_i32 s81, s47, 0x1800000\n" \
"v_mfma_f32_16x16x32_f16 v[80:83], v[136:139], v[172:175], v[80:83]\n" \
"v_and_b32 v190, s92, v190\n" \
"v_and_b32 v191, s92, v191\n" \
"ds_read_b128 v[172:175], v240 offset:20992\n" \
"buffer_load_dwordx4 v232, s[36:39], s81 offen sc1 lds\n" \
"v_mfma_f32_16x16x32_f16 v[104:107], v[136:139], v[176:179], v[104:107]\n" \
"buffer_store_dwordx4 v[188:191], v234, s[36:39], s82 offen offset:1024 sc1\n" \
"v_mfma_f32_16x16x32_f16 v[116:119], v[140:143], v[176:179], v[116:119]\n" \
"s_setprio 1\n" \
"s_waitcnt vmcnt(7) lgkmcnt(0)\n" \
"s_cmp_eq_u32 s94, 0\n" \
"s_cbranch_scc1 .Lp4_%=\n" \
".Lq4_%=:\n" \
"s_memrealtime s[86:87]\n" \
"s_waitcnt lgkmcnt(0)\n" \
"s_sub_i32 s87, s86, s75\n" \
"s_cmp_lt_i32 s87, 0\n" \
"s_cbranch_scc1 .Lq4_%=\n" \
"s_lshr_b64 s[94:95], s[94:95], 8\n" \
"s_and_b32 s87, s94, 0xff\n" \
"s_add_i32 s75, s86, s87\n" \
".Lp4_%=:\n" \
"s_barrier\n" \
"v_mfma_f32_16x16x32_f16 v[0:3], v[120:123], v[180:183], v[0:3]\n" \
"v_pk_add_f16 v136, v120, v128\n" \
"v_pk_add_f16 v137, v121, v129\n" \
"v_mfma_f32_16x16x32_f16 v[12:15], v[124:127], v[180:183], v[12:15]\n" \
"v_pk_add_f16 v180, v180, v156\n" \
"v_pk_add_f16 v138, v122, v130\n" \
"s_mov_b32 s84, 0x8004000\n" \
"v_mfma_f32_16x16x32_f16 v[36:39], v[124:127], v[184:187], v[36:39]\n" \
"v_pk_add_f16 v181, v181, v157\n" \
"v_pk_add_f16 v182, v182, v158\n" \
"v_pk_add_f16 v183, v183, v159\n" \
"buffer_load_dword v253, v252, s[36:39], s84 offen sc1\n" \
"buffer_store_dwordx4 v[208:211], v235, s[36:39], s63 offen sc1\n" \
"v_mfma_f32_16x16x32_f16 v[24:27], v[120:123], v[184:187], v[24:27]\n" \
"v_pk_add_f16 v184, v184, v160\n" \
"v_pk_add_f16 v139, v123, v131\n" \
"s_add_i32 m0, s44, 0x5000\n" \
"s_add_i32 s80, s46, 0x1c00000\n" \
"v_mfma_f32_16x16x32_f16 v[48:51], v[120:123], v[144:147], v[48:51]\n" \
"v_pk_add_f16 v185, v185, v161\n" \
"v_pk_add_f16 v140, v124, v132\n" \
"buffer_load_dwordx4 v232, s[36:39], s80 offen sc1 lds\n" \
"v_mfma_f32_16x16x32_f16 v[60:63], v[124:127], v[144:147], v[60:63]\n" \
"v_pk_add_f16 v186, v186, v162\n" \
"v_pk_add_f16 v187, v187, v163\n" \
"v_mfma_f32_16x16x32_f16 v[84:87], v[124:127], v[148:151], v[84:87]\n" \
"v_pk_add_f16 v144, v144, v164\n" \
"v_pk_add_f16 v141, v125, v133\n" \
"v_mfma_f32_16x16x32_f16 v[72:75], v[120:123], v[148:151], v[72:75]\n" \
"v_pk_add_f16 v145, v145, v165\n" \
"v_pk_add_f16 v142, v126, v134\n" \
"v_mfma_f32_16x16x32_f16 v[96:99], v[120:123], v[152:155], v[96:99]\n" \
"v_pk_add_f16 v146, v146, v166\n" \
"v_pk_add_f16 v147, v147, v167\n" \
"ds_read_b64_tr_b16 v[188:189], v236 offset:8192\n" \
"v_mfma_f32_16x16x32_f16 v[108:111], v[124:127], v[152:155], v[108:111]\n" \
"v_pk_add_f16 v148, v148, v168\n" \
"v_pk_add_f16 v143, v127, v135\n" \
"ds_read_b64_tr_b16 v[192:193], v237 offset:8192\n" \
"v_mfma_f32_16x16x32_f16 v[4:7], v[128:131], v[156:159], v[4:7]\n" \
"v_pk_add_f16 v149, v149, v169\n" \
"ds_read_b64_tr_b16 v[120:121], v241 offset:32768\n" \
"buffer_load_dwordx4 v232, s[36:39], s80 offen offset:1024 sc1 lds\n" \
"v_mfma_f32_16x16x32_f16 v[16:19], v[132:135], v[156:159], v[16:19]\n" \
"v_pk_add_f16 v150, v150, v170\n" \
"ds_read_b128 v[176:179], v240 offset:43520\n" \
"v_mfma_f32_16x16x32_f16 v[40:43], v[132:135], v[160:163], v[40:43]\n" \
"v_pk_add_f16 v151, v151, v171\n" \
"ds_read_b128 v[156:159], v240 offset:43008\n" \
"v_mfma_f32_16x16x32_f16 v[28:31], v[128:131], v[160:163], v[28:31]\n" \
"v_pk_add_f16 v152, v152, v172\n" \
"ds_read_b64_tr_b16 v[122:123], v241 offset:40960\n" \
"v_mfma_f32_16x16x32_f16 v[52:55], v[128:131], v[164:167], v[52:55]\n" \
"v_pk_add_f16 v153, v153, v173\n" \
"ds_read_b128 v[160:163], v240 offset:45056\n" \
"s_waitcnt lgkmcnt(6)\n" \
"v_mfma_f32_16x16x16_bf16 v[188:191], v[188:189], v[254:255], 0\n" \
"v_mfma_f32_16x16x32_f16 v[64:67], v[132:135], v[164:167], v[64:67]\n" \
"v_pk_add_f16 v154, v154, v174\n" \
"ds_read_b64_tr_b16 v[124:125], v242 offset:32768\n" \
"s_waitcnt lgkmcnt(6)\n" \
"v_mfma_f32_16x16x16_bf16 v[192:195], v[192:193], v[254:255], 0\n" \
"v_mfma_f32_16x16x32_f16 v[88:91], v[132:135], v[168:171], v[88:91]\n" \
"v_pk_add_f16 v155, v155, v175\n" \
"ds_read_b128 v[164:167], v240 offset:47104\n" \
"buffer_load_dwordx4 v232, s[36:39], s80 offen offset:2048 sc1 lds\n" \
"v_mfma_f32_16x16x32_f16 v[76:79], v[128:131], v[168:171], v[76:79]\n" \
"s_setprio 0\n" \
"ds_read_b64_tr_b16 v[126:127], v242 offset:40960\n" \
"v_mfma_f32_16x16x32_f16 v[100:103], v[128:131], v[172:175], v[100:103]\n" \
"v_cvt_pk_f16_f32 v188, v188, v189\n" \
"ds_read_b128 v[168:171], v240 offset:49152\n" \
"v_mfma_f32_16x16x32_f16 v[112:115], v[132:135], v[172:175], v[112:115]\n" \
"v_cvt_pk_f16_f32 v189, v190, v191\n" \
"ds_read_b64_tr_b16 v[128:129], v241 offset:33280\n" \
"v_mfma_f32_16x16x32_f16 v[8:11], v[136:139], v[180:183], v[8:11]\n" \
"v_cvt_pk_f16_f32 v190, v192, v193\n" \
"ds_read_b128 v[172:175], v240 offset:40960\n" \
"v_mfma_f32_16x16x32_f16 v[20:23], v[140:143], v[180:183], v[20:23]\n" \
"v_cvt_pk_f16_f32 v191, v194, v195\n" \
"v_add_u32 v188, s91, v188\n" \
"ds_read_b128 v[180:183], v240 offset:45568\n" \
"v_mfma_f32_16x16x32_f16 v[44:47], v[140:143], v[184:187], v[44:47]\n" \
"ds_read_b64_tr_b16 v[130:131], v241 offset:41472\n" \
"ds_read_b64_tr_b16 v[132:133], v242 offset:33280\n" \
"buffer_load_dwordx4 v232, s[36:39], s80 offen offset:3072 sc1 lds\n" \
"v_mfma_f32_16x16x32_f16 v[32:35], v[136:139], v[184:187], v[32:35]\n" \
"v_add_u32 v189, s91, v189\n" \
"v_add_u32 v190, s91, v190\n" \
"ds_read_b128 v[184:187], v240 offset:47616\n" \
"v_mfma_f32_16x16x32_f16 v[56:59], v[136:139], v[144:147], v[56:59]\n" \
"v_add_u32 v191, s91, v191\n" \
"ds_read_b64_tr_b16 v[134:135], v242 offset:41472\n" \
"v_mfma_f32_16x16x32_f16 v[68:71], v[140:143], v[144:147], v[68:71]\n" \
"v_and_b32 v188, s92, v188\n" \
"v_and_b32 v189, s92, v189\n" \
"ds_read_b128 v[144:147], v240 offset:49664\n" \
"v_mfma_f32_16x16x32_f16 v[92:95], v[140:143], v[148:151], v[92:95]\n" \
"s_waitcnt vmcnt(5)\n" \
"v_cmp_eq_u32 vcc, 0, v253\n" \
"s_cbranch_vccnz .Ls12_%=\n" \
".Lr12_%=:\n" \
"s_add_i32 m0, s45, 0x5000\n" \
"s_add_i32 s81, s47, 0x1c00000\n" \
"s_add_i32 s82, s51, 0x3800000\n" \
"v_mfma_f32_16x16x32_f16 v[80:83], v[136:139], v[148:151], v[80:83]\n" \
"v_and_b32 v190, s92, v190\n" \
"v_and_b32 v191, s92, v191\n" \
"ds_read_b128 v[148:151], v240 offset:41472\n" \
"buffer_load_dwordx4 v232, s[36:39], s81 offen sc1 lds\n" \
"v_mfma_f32_16x16x32_f16 v[104:107], v[136:139], v[152:155], v[104:107]\n" \
"buffer_store_dwordx4 v[188:191], v234, s[36:39], s82 offen sc1\n" \
"v_mfma_f32_16x16x32_f16 v[116:119], v[140:143], v[152:155], v[116:119]\n" \
"s_setprio 1\n" \
"s_waitcnt vmcnt(7) lgkmcnt(0)\n" \
"s_cmp_eq_u32 s94, 0\n" \
"s_cbranch_scc1 .Lp5_%=\n" \
".Lq5_%=:\n" \
"s_memrealtime s[86:87]\n" \
"s_waitcnt lgkmcnt(0)\n" \
"s_sub_i32 s87, s86, s75\n" \
"s_cmp_lt_i32 s87, 0\n" \
"s_cbranch_scc1 .Lq5_%=\n" \
"s_lshr_b64 s[94:95], s[94:95], 8\n" \
"s_and_b32 s87, s94, 0xff\n" \
"s_add_i32 s75, s86, s87\n" \
".Lp5_%=:\n" \
"s_barrier\n" \
"v_mfma_f32_16x16x32_f16 v[0:3], v[120:123], v[156:159], v[0:3]\n" \
"v_pk_add_f16 v136, v120, v128\n" \
"v_pk_add_f16 v137, v121, v129\n" \
"v_mfma_f32_16x16x32_f16 v[12:15], v[124:127], v[156:159], v[12:15]\n" \
"v_pk_add_f16 v156, v156, v176\n" \
"v_pk_add_f16 v138, v122, v130\n" \
"v_mfma_f32_16x16x32_f16 v[36:39], v[124:127], v[160:163], v[36:39]\n" \
"v_pk_add_f16 v157, v157, v177\n" \
"v_pk_add_f16 v158, v158, v178\n" \
"v_pk_add_f16 v159, v159, v179\n" \
"v_mfma_f32_16x16x32_f16 v[24:27], v[120:123], v[160:163], v[24:27]\n" \
"v_pk_add_f16 v160, v160, v180\n" \
"v_pk_add_f16 v139, v123, v131\n" \
"s_add_i32 m0, s44, 0xa000\n" \
"s_add_i32 s80, s46, 0x2000000\n" \
"v_mfma_f32_16x16x32_f16 v[48:51], v[120:123], v[164:167], v[48:51]\n" \
"v_pk_add_f16 v161, v161, v181\n" \
"v_pk_add_f16 v140, v124, v132\n" \
"buffer_load_dwordx4 v232, s[36:39], s80 offen sc1 lds\n" \
"v_mfma_f32_16x16x32_f16 v[60:63], v[124:127], v[164:167], v[60:63]\n" \
"v_pk_add_f16 v162, v162, v182\n" \
"v_pk_add_f16 v163, v163, v183\n" \
"ds_read_b128 v[208:211], v240 offset:4608\n" \
"v_mfma_f32_16x16x32_f16 v[84:87], v[124:127], v[168:171], v[84:87]\n" \
"v_pk_add_f16 v164, v164, v184\n" \
"v_pk_add_f16 v141, v125, v133\n" \
"v_mfma_f32_16x16x32_f16 v[72:75], v[120:123], v[168:171], v[72:75]\n" \
"v_pk_add_f16 v165, v165, v185\n" \
"v_pk_add_f16 v142, v126, v134\n" \
"v_mfma_f32_16x16x32_f16 v[96:99], v[120:123], v[172:175], v[96:99]\n" \
"v_pk_add_f16 v166, v166, v186\n" \
"v_pk_add_f16 v167, v167, v187\n" \
"ds_read_b64_tr_b16 v[188:189], v238 offset:8192\n" \
"v_mfma_f32_16x16x32_f16 v[108:111], v[124:127], v[172:175], v[108:111]\n" \
"v_pk_add_f16 v168, v168, v144\n" \
"v_pk_add_f16 v143, v127, v135\n" \
"ds_read_b64_tr_b16 v[192:193], v239 offset:8192\n" \
"v_mfma_f32_16x16x32_f16 v[4:7], v[128:131], v[176:179], v[4:7]\n" \
"v_pk_add_f16 v169, v169, v145\n" \
"ds_read_b64_tr_b16 v[120:121], v241 offset:0\n" \
"buffer_load_dwordx4 v232, s[36:39], s80 offen offset:1024 sc1 lds\n" \
"v_mfma_f32_16x16x32_f16 v[16:19], v[132:135], v[176:179], v[16:19]\n" \
"v_pk_add_f16 v170, v170, v146\n" \
"ds_read_b128 v[152:155], v240 offset:2560\n" \
"v_mfma_f32_16x16x32_f16 v[40:43], v[132:135], v[180:183], v[40:43]\n" \
"v_pk_add_f16 v171, v171, v147\n" \
"ds_read_b128 v[176:179], v240 offset:2048\n" \
"v_mfma_f32_16x16x32_f16 v[28:31], v[128:131], v[180:183], v[28:31]\n" \
"v_pk_add_f16 v172, v172, v148\n" \
"ds_read_b64_tr_b16 v[122:123], v241 offset:8192\n" \
"v_mfma_f32_16x16x32_f16 v[52:55], v[128:131], v[184:187], v[52:55]\n" \
"v_pk_add_f16 v173, v173, v149\n" \
"ds_read_b128 v[180:183], v240 offset:4096\n" \
"s_waitcnt lgkmcnt(6)\n" \
"v_mfma_f32_16x16x16_bf16 v[188:191], v[188:189], v[254:255], 0\n" \
"v_mfma_f32_16x16x32_f16 v[64:67], v[132:135], v[184:187], v[64:67]\n" \
"v_pk_add_f16 v174, v174, v150\n" \
"ds_read_b64_tr_b16 v[124:125], v242 offset:0\n" \
"s_waitcnt lgkmcnt(6)\n" \
"v_mfma_f32_16x16x16_bf16 v[192:195], v[192:193], v[254:255], 0\n" \
"v_mfma_f32_16x16x32_f16 v[88:91], v[132:135], v[144:147], v[88:91]\n" \
"v_pk_add_f16 v175, v175, v151\n" \
"ds_read_b128 v[184:187], v240 offset:6144\n" \
"buffer_load_dwordx4 v232, s[36:39], s80 offen offset:2048 sc1 lds\n" \
"v_mfma_f32_16x16x32_f16 v[76:79], v[128:131], v[144:147], v[76:79]\n" \
"s_setprio 0\n" \
"ds_read_b64_tr_b16 v[126:127], v242 offset:8192\n" \
"v_mfma_f32_16x16x32_f16 v[100:103], v[128:131], v[148:151], v[100:103]\n" \
"v_cvt_pk_f16_f32 v188, v188, v189\n" \
"ds_read_b128 v[144:147], v240 offset:8192\n" \
"v_mfma_f32_16x16x32_f16 v[112:115], v[132:135], v[148:151], v[112:115]\n" \
"v_cvt_pk_f16_f32 v189, v190, v191\n" \
"ds_read_b64_tr_b16 v[128:129], v241 offset:512\n" \
"v_mfma_f32_16x16x32_f16 v[8:11], v[136:139], v[156:159], v[8:11]\n" \
"v_cvt_pk_f16_f32 v190, v192, v193\n" \
"ds_read_b128 v[148:151], v240 offset:0\n" \
"v_mfma_f32_16x16x32_f16 v[20:23], v[140:143], v[156:159], v[20:23]\n" \
"v_cvt_pk_f16_f32 v191, v194, v195\n" \
"v_add_u32 v188, s91, v188\n" \
"ds_read_b128 v[156:159], v240 offset:6656\n" \
"v_mfma_f32_16x16x32_f16 v[44:47], v[140:143], v[160:163], v[44:47]\n" \
"ds_read_b64_tr_b16 v[130:131], v241 offset:8704\n" \
"ds_read_b64_tr_b16 v[132:133], v242 offset:512\n" \
"buffer_load_dwordx4 v232, s[36:39], s80 offen offset:3072 sc1 lds\n" \
"v_mfma_f32_16x16x32_f16 v[32:35], v[136:139], v[160:163], v[32:35]\n" \
"v_add_u32 v189, s91, v189\n" \
"v_add_u32 v190, s91, v190\n" \
"ds_read_b128 v[160:163], v240 offset:8704\n" \
"v_mfma_f32_16x16x32_f16 v[56:59], v[136:139], v[164:167], v[56:59]\n" \
"v_add_u32 v191, s91, v191\n" \
"ds_read_b64_tr_b16 v[134:135], v242 offset:8704\n" \
"v_mfma_f32_16x16x32_f16 v[68:71], v[140:143], v[164:167], v[68:71]\n" \
"v_and_b32 v188, s92, v188\n" \
"ds_read_b128 v[164:167], v240 offset:512\n" \
"v_mfma_f32_16x16x32_f16 v[92:95], v[140:143], v[168:171], v[92:95]\n" \
"v_and_b32 v189, s92, v189\n" \
"s_add_i32 m0, s45, 0xa000\n" \
"s_add_i32 s81, s47, 0x2000000\n" \
"v_mfma_f32_16x16x32_f16 v[80:83], v[136:139], v[168:171], v[80:83]\n" \
"v_and_b32 v190, s92, v190\n" \
"v_and_b32 v191, s92, v191\n" \
"buffer_load_dwordx4 v232, s[36:39], s81 offen sc1 lds\n" \
"v_mfma_f32_16x16x32_f16 v[104:107], v[136:139], v[172:175], v[104:107]\n" \
"buffer_store_dwordx4 v[188:191], v234, s[36:39], s82 offen offset:1024 sc1\n" \
"v_mfma_f32_16x16x32_f16 v[116:119], v[140:143], v[172:175], v[116:119]\n" \
"s_setprio 1\n" \
"s_waitcnt vmcnt(7) lgkmcnt(0)\n" \
"s_cmp_eq_u32 s94, 0\n" \
"s_cbranch_scc1 .Lp6_%=\n" \
".Lq6_%=:\n" \
"s_memrealtime s[86:87]\n" \
"s_waitcnt lgkmcnt(0)\n" \
"s_sub_i32 s87, s86, s75\n" \
"s_cmp_lt_i32 s87, 0\n" \
"s_cbranch_scc1 .Lq6_%=\n" \
"s_lshr_b64 s[94:95], s[94:95], 8\n" \
"s_and_b32 s87, s94, 0xff\n" \
"s_add_i32 s75, s86, s87\n" \
".Lp6_%=:\n" \
"s_barrier\n" \
"v_mfma_f32_16x16x32_f16 v[0:3], v[120:123], v[176:179], v[0:3]\n" \
"v_pk_add_f16 v136, v120, v128\n" \
"v_pk_add_f16 v137, v121, v129\n" \
"v_mfma_f32_16x16x32_f16 v[12:15], v[124:127], v[176:179], v[12:15]\n" \
"v_pk_add_f16 v176, v176, v152\n" \
"v_pk_add_f16 v138, v122, v130\n" \
"v_mfma_f32_16x16x32_f16 v[36:39], v[124:127], v[180:183], v[36:39]\n" \
"v_pk_add_f16 v177, v177, v153\n" \
"v_pk_add_f16 v178, v178, v154\n" \
"v_pk_add_f16 v179, v179, v155\n" \
"buffer_store_dwordx4 v[212:215], v235, s[36:39], s64 offen sc1\n" \
"v_mfma_f32_16x16x32_f16 v[24:27], v[120:123], v[180:183], v[24:27]\n" \
"v_pk_add_f16 v180, v180, v208\n" \
"v_pk_add_f16 v139, v123, v131\n" \
"s_add_i32 m0, s44, 0x0\n" \
"s_add_i32 s80, s46, 0x2400000\n" \
"v_mfma_f32_16x16x32_f16 v[48:51], v[120:123], v[184:187], v[48:51]\n" \
"v_pk_add_f16 v181, v181, v209\n" \
"v_pk_add_f16 v140, v124, v132\n" \
"buffer_load_dwordx4 v232, s[36:39], s80 offen sc1 lds\n" \
"v_mfma_f32_16x16x32_f16 v[60:63], v[124:127], v[184:187], v[60:63]\n" \
"v_pk_add_f16 v182, v182, v210\n" \
"v_pk_add_f16 v183, v183, v211\n" \
"ds_read_b128 v[172:175], v240 offset:25088\n" \
"v_mfma_f32_16x16x32_f16 v[84:87], v[124:127], v[144:147], v[84:87]\n" \
"v_pk_add_f16 v184, v184, v156\n" \
"v_pk_add_f16 v141, v125, v133\n" \
"v_mfma_f32_16x16x32_f16 v[72:75], v[120:123], v[144:147], v[72:75]\n" \
"v_pk_add_f16 v185, v185, v157\n" \
"v_pk_add_f16 v142, v126, v134\n" \
"v_mfma_f32_16x16x32_f16 v[96:99], v[120:123], v[148:151], v[96:99]\n" \
"v_pk_add_f16 v186, v186, v158\n" \
"v_pk_add_f16 v187, v187, v159\n" \
"ds_read_b64_tr_b16 v[188:189], v236 offset:12288\n" \
"v_mfma_f32_16x16x32_f16 v[108:111], v[124:127], v[148:151], v[108:111]\n" \
"v_pk_add_f16 v144, v144, v160\n" \
"v_pk_add_f16 v143, v127, v135\n" \
"ds_read_b64_tr_b16 v[192:193], v237 offset:12288\n" \
"v_mfma_f32_16x16x32_f16 v[4:7], v[128:131], v[152:155], v[4:7]\n" \
"v_pk_add_f16 v145, v145, v161\n" \
"ds_read_b64_tr_b16 v[120:121], v241 offset:16384\n" \
"buffer_load_dwordx4 v232, s[36:39], s80 offen offset:1024 sc1 lds\n" \
"v_mfma_f32_16x16x32_f16 v[16:19], v[132:135], v[152:155], v[16:19]\n" \
"v_pk_add_f16 v146, v146, v162\n" \
"ds_read_b128 v[168:171], v240 offset:23040\n" \
"v_mfma_f32_16x16x32_f16 v[40:43], v[132:135], v[208:211], v[40:43]\n" \
"v_pk_add_f16 v147, v147, v163\n" \
"ds_read_b128 v[152:155], v240 offset:22528\n" \
"v_mfma_f32_16x16x32_f16 v[28:31], v[128:131], v[208:211], v[28:31]\n" \
"v_pk_add_f16 v148, v148, v164\n" \
"ds_read_b64_tr_b16 v[122:123], v241 offset:24576\n" \
"v_mfma_f32_16x16x32_f16 v[52:55], v[128:131], v[156:159], v[52:55]\n" \
"v_pk_add_f16 v149, v149, v165\n" \
"ds_read_b128 v[208:211], v240 offset:24576\n" \
"s_waitcnt lgkmcnt(6)\n" \
"v_mfma_f32_16x16x16_bf16 v[188:191], v[188:189], v[254:255], 0\n" \
"v_mfma_f32_16x16x32_f16 v[64:67], v[132:135], v[156:159], v[64:67]\n" \
"v_pk_add_f16 v150, v150, v166\n" \
"ds_read_b64_tr_b16 v[124:125], v242 offset:16384\n" \
"s_waitcnt lgkmcnt(6)\n" \
"v_mfma_f32_16x16x16_bf16 v[192:195], v[192:193], v[254:255], 0\n" \
"v_mfma_f32_16x16x32_f16 v[88:91], v[132:135], v[160:163], v[88:91]\n" \
"v_pk_add_f16 v151, v151, v167\n" \
"ds_read_b128 v[156:159], v240 offset:26624\n" \
"buffer_load_dwordx4 v232, s[36:39], s80 offen offset:2048 sc1 lds\n" \
"v_mfma_f32_16x16x32_f16 v[76:79], v[128:131], v[160:163], v[76:79]\n" \
"s_setprio 0\n" \
"ds_read_b64_tr_b16 v[126:127], v242 offset:24576\n" \
"v_mfma_f32_16x16x32_f16 v[100:103], v[128:131], v[164:167], v[100:103]\n" \
"v_cvt_pk_f16_f32 v188, v188, v189\n" \
"ds_read_b128 v[160:163], v240 offset:28672\n" \
"v_mfma_f32_16x16x32_f16 v[112:115], v[132:135], v[164:167], v[112:115]\n" \
"v_cvt_pk_f16_f32 v189, v190, v191\n" \
"ds_read_b64_tr_b16 v[128:129], v241 offset:16896\n" \
"v_mfma_f32_16x16x32_f16 v[8:11], v[136:139], v[176:179], v[8:11]\n" \
"v_cvt_pk_f16_f32 v190, v192, v193\n" \
"ds_read_b128 v[164:167], v240 offset:20480\n" \
"v_mfma_f32_16x16x32_f16 v[20:23], v[140:143], v[176:179], v[20:23]\n" \
"v_cvt_pk_f16_f32 v191, v194, v195\n" \
"v_add_u32 v188, s91, v188\n" \
"ds_read_b128 v[176:179], v240 offset:27136\n" \
"v_mfma_f32_16x16x32_f16 v[44:47], v[140:143], v[180:183], v[44:47]\n" \
"ds_read_b64_tr_b16 v[130:131], v241 offset:25088\n" \
"ds_read_b64_tr_b16 v[132:133], v242 offset:16896\n" \
"buffer_load_dwordx4 v232, s[36:39], s80 offen offset:3072 sc1 lds\n" \
"v_mfma_f32_16x16x32_f16 v[32:35], v[136:139], v[180:183], v[32:35]\n" \
"v_add_u32 v189, s91, v189\n" \
"v_add_u32 v190, s91, v190\n" \
"ds_read_b128 v[180:183], v240 offset:29184\n" \
"v_mfma_f32_16x16x32_f16 v[56:59], v[136:139], v[184:187], v[56:59]\n" \
"v_add_u32 v191, s91, v191\n" \
"ds_read_b64_tr_b16 v[134:135], v242 offset:25088\n" \
"v_mfma_f32_16x16x32_f16 v[68:71], v[140:143], v[184:187], v[68:71]\n" \
"v_and_b32 v188, s92, v188\n" \
"ds_read_b128 v[184:187], v240 offset:20992\n" \
"v_mfma_f32_16x16x32_f16 v[92:95], v[140:143], v[144:147], v[92:95]\n" \
"v_and_b32 v189, s92, v189\n" \
"s_add_i32 m0, s45, 0x0\n" \
"s_add_i32 s81, s47, 0x2400000\n" \
"s_add_i32 s82, s48, 0x4000000\n" \
"v_mfma_f32_16x16x32_f16 v[80:83], v[136:139], v[144:147], v[80:83]\n" \
"v_and_b32 v190, s92, v190\n" \
"v_and_b32 v191, s92, v191\n" \
"buffer_load_dwordx4 v232, s[36:39], s81 offen sc1 lds\n" \
"v_mfma_f32_16x16x32_f16 v[104:107], v[136:139], v[148:151], v[104:107]\n" \
"buffer_store_dwordx4 v[188:191], v234, s[36:39], s82 offen sc1\n" \
"v_mfma_f32_16x16x32_f16 v[116:119], v[140:143], v[148:151], v[116:119]\n" \
"s_setprio 1\n" \
"s_waitcnt vmcnt(7) lgkmcnt(0)\n" \
"s_cmp_eq_u32 s94, 0\n" \
"s_cbranch_scc1 .Lp7_%=\n" \
".Lq7_%=:\n" \
"s_memrealtime s[86:87]\n" \
"s_waitcnt lgkmcnt(0)\n" \
"s_sub_i32 s87, s86, s75\n" \
"s_cmp_lt_i32 s87, 0\n" \
"s_cbranch_scc1 .Lq7_%=\n" \
"s_lshr_b64 s[94:95], s[94:95], 8\n" \
"s_and_b32 s87, s94, 0xff\n" \
"s_add_i32 s75, s86, s87\n" \
".Lp7_%=:\n" \
"s_barrier\n" \
"v_mfma_f32_16x16x32_f16 v[0:3], v[120:123], v[152:155], v[0:3]\n" \
"v_pk_add_f16 v136, v120, v128\n" \
"v_pk_add_f16 v137, v121, v129\n" \
"v_mfma_f32_16x16x32_f16 v[12:15], v[124:127], v[152:155], v[12:15]\n" \
"v_pk_add_f16 v152, v152, v168\n" \
"v_pk_add_f16 v138, v122, v130\n" \
"v_mfma_f32_16x16x32_f16 v[36:39], v[124:127], v[208:211], v[36:39]\n" \
"v_pk_add_f16 v153, v153, v169\n" \
"v_pk_add_f16 v154, v154, v170\n" \
"v_pk_add_f16 v155, v155, v171\n" \
"v_mfma_f32_16x16x32_f16 v[24:27], v[120:123], v[208:211], v[24:27]\n" \
"v_pk_add_f16 v208, v208, v172\n" \
"v_pk_add_f16 v139, v123, v131\n" \
"s_add_i32 m0, s44, 0x5000\n" \
"s_add_i32 s80, s46, 0x2800000\n" \
"v_mfma_f32_16x16x32_f16 v[48:51], v[120:123], v[156:159], v[48:51]\n" \
"v_pk_add_f16 v209, v209, v173\n" \
"v_pk_add_f16 v140, v124, v132\n" \
"buffer_load_dwordx4 v232, s[36:39], s80 offen sc1 lds\n" \
"v_mfma_f32_16x16x32_f16 v[60:63], v[124:127], v[156:159], v[60:63]\n" \
"v_pk_add_f16 v210, v210, v174\n" \
"v_pk_add_f16 v211, v211, v175\n" \
"ds_read_b128 v[148:151], v240 offset:45568\n" \
"v_mfma_f32_16x16x32_f16 v[84:87], v[124:127], v[160:163], v[84:87]\n" \
"v_pk_add_f16 v156, v156, v176\n" \
"v_pk_add_f16 v141, v125, v133\n" \
"v_mfma_f32_16x16x32_f16 v[72:75], v[120:123], v[160:163], v[72:75]\n" \
"v_pk_add_f16 v157, v157, v177\n" \
"v_pk_add_f16 v142, v126, v134\n" \
"v_mfma_f32_16x16x32_f16 v[96:99], v[120:123], v[164:167], v[96:99]\n" \
"v_pk_add_f16 v158, v158, v178\n" \
"v_pk_add_f16 v159, v159, v179\n" \
"ds_read_b64_tr_b16 v[188:189], v238 offset:12288\n" \
"v_mfma_f32_16x16x32_f16 v[108:111], v[124:127], v[164:167], v[108:111]\n" \
"v_pk_add_f16 v160, v160, v180\n" \
"v_pk_add_f16 v143, v127, v135\n" \
"ds_read_b64_tr_b16 v[192:193], v239 offset:12288\n" \
"v_mfma_f32_16x16x32_f16 v[4:7], v[128:131], v[168:171], v[4:7]\n" \
"v_pk_add_f16 v161, v161, v181\n" \
"ds_read_b64_tr_b16 v[120:121], v241 offset:32768\n" \
"buffer_load_dwordx4 v232, s[36:39], s80 offen offset:1024 sc1 lds\n" \
"v_mfma_f32_16x16x32_f16 v[16:19], v[132:135], v[168:171], v[16:19]\n" \
"v_pk_add_f16 v162, v162, v182\n" \
"ds_read_b128 v[144:147], v240 offset:43520\n" \
"v_mfma_f32_16x16x32_f16 v[40:43], v[132:135], v[172:175], v[40:43]\n" \
"v_pk_add_f16 v163, v163, v183\n" \
"ds_read_b128 v[168:171], v240 offset:43008\n" \
"v_mfma_f32_16x16x32_f16 v[28:31], v[128:131], v[172:175], v[28:31]\n" \
"v_pk_add_f16 v164, v164, v184\n" \
"ds_read_b64_tr_b16 v[122:123], v241 offset:40960\n" \
"v_mfma_f32_16x16x32_f16 v[52:55], v[128:131], v[176:179], v[52:55]\n" \
"v_pk_add_f16 v165, v165, v185\n" \
"ds_read_b128 v[172:175], v240 offset:45056\n" \
"s_waitcnt lgkmcnt(6)\n" \
"v_mfma_f32_16x16x16_bf16 v[188:191], v[188:189], v[254:255], 0\n" \
"v_mfma_f32_16x16x32_f16 v[64:67], v[132:135], v[176:179], v[64:67]\n" \
"v_pk_add_f16 v166, v166, v186\n" \
"ds_read_b64_tr_b16 v[124:125], v242 offset:32768\n" \
"s_waitcnt lgkmcnt(6)\n" \
"v_mfma_f32_16x16x16_bf16 v[192:195], v[192:193], v[254:255], 0\n" \
"v_mfma_f32_16x16x32_f16 v[88:91], v[132:135], v[180:183], v[88:91]\n" \
"v_pk_add_f16 v167, v167, v187\n" \
"ds_read_b128 v[176:179], v240 offset:47104\n" \
"buffer_load_dwordx4 v232, s[36:39], s80 offen offset:2048 sc1 lds\n" \
"v_mfma_f32_16x16x32_f16 v[76:79], v[128:131], v[180:183], v[76:79]\n" \
"s_setprio 0\n" \
"ds_read_b64_tr_b16 v[126:127], v242 offset:40960\n" \
"v_mfma_f32_16x16x32_f16 v[100:103], v[128:131], v[184:187], v[100:103]\n" \
"v_cvt_pk_f16_f32 v188, v188, v189\n" \
"ds_read_b128 v[180:183], v240 offset:49152\n" \
"v_mfma_f32_16x16x32_f16 v[112:115], v[132:135], v[184:187], v[112:115]\n" \
"v_cvt_pk_f16_f32 v189, v190, v191\n" \
"ds_read_b64_tr_b16 v[128:129], v241 offset:33280\n" \
"v_mfma_f32_16x16x32_f16 v[8:11], v[136:139], v[152:155], v[8:11]\n" \
"v_cvt_pk_f16_f32 v190, v192, v193\n" \
"ds_read_b128 v[184:187], v240 offset:40960\n" \
"v_mfma_f32_16x16x32_f16 v[20:23], v[140:143], v[152:155], v[20:23]\n" \
"v_cvt_pk_f16_f32 v191, v194, v195\n" \
"v_add_u32 v188, s91, v188\n" \
"ds_read_b128 v[152:155], v240 offset:47616\n" \
"v_mfma_f32_16x16x32_f16 v[44:47], v[140:143], v[208:211], v[44:47]\n" \
"ds_read_b64_tr_b16 v[130:131], v241 offset:41472\n" \
"ds_read_b64_tr_b16 v[132:133], v242 offset:33280\n" \
"buffer_load_dwordx4 v232, s[36:39], s80 offen offset:3072 sc1 lds\n" \
"v_mfma_f32_16x16x32_f16 v[32:35], v[136:139], v[208:211], v[32:35]\n" \
"v_add_u32 v189, s91, v189\n" \
"v_add_u32 v190, s91, v190\n" \
"ds_read_b128 v[208:211], v240 offset:49664\n" \
"v_mfma_f32_16x16x32_f16 v[56:59], v[136:139], v[156:159], v[56:59]\n" \
"v_add_u32 v191, s91, v191\n" \
"ds_read_b64_tr_b16 v[134:135], v242 offset:41472\n" \
"v_mfma_f32_16x16x32_f16 v[68:71], v[140:143], v[156:159], v[68:71]\n" \
"v_and_b32 v188, s92, v188\n" \
"ds_read_b128 v[156:159], v240 offset:41472\n" \
"v_mfma_f32_16x16x32_f16 v[92:95], v[140:143], v[160:163], v[92:95]\n" \
"v_and_b32 v189, s92, v189\n" \
"s_add_i32 m0, s45, 0x5000\n" \
"s_add_i32 s81, s47, 0x2800000\n" \
"v_mfma_f32_16x16x32_f16 v[80:83], v[136:139], v[160:163], v[80:83]\n" \
"v_and_b32 v190, s92, v190\n" \
"v_and_b32 v191, s92, v191\n" \
"buffer_load_dwordx4 v232, s[36:39], s81 offen sc1 lds\n" \
"v_mfma_f32_16x16x32_f16 v[104:107], v[136:139], v[164:167], v[104:107]\n" \
"buffer_store_dwordx4 v[188:191], v234, s[36:39], s82 offen offset:1024 sc1\n" \
"v_mfma_f32_16x16x32_f16 v[116:119], v[140:143], v[164:167], v[116:119]\n" \
"s_setprio 1\n" \
"s_waitcnt vmcnt(7) lgkmcnt(0)\n" \
"s_barrier\n" \
"v_mfma_f32_16x16x32_f16 v[0:3], v[120:123], v[168:171], v[0:3]\n" \
"v_pk_add_f16 v136, v120, v128\n" \
"v_pk_add_f16 v137, v121, v129\n" \
"v_mfma_f32_16x16x32_f16 v[12:15], v[124:127], v[168:171], v[12:15]\n" \
"v_pk_add_f16 v168, v168, v144\n" \
"v_pk_add_f16 v138, v122, v130\n" \
"v_mfma_f32_16x16x32_f16 v[36:39], v[124:127], v[172:175], v[36:39]\n" \
"v_pk_add_f16 v169, v169, v145\n" \
"v_pk_add_f16 v170, v170, v146\n" \
"v_pk_add_f16 v171, v171, v147\n" \
"buffer_store_dwordx4 v[216:219], v235, s[36:39], s65 offen sc1\n" \
"v_mfma_f32_16x16x32_f16 v[24:27], v[120:123], v[172:175], v[24:27]\n" \
"v_pk_add_f16 v172, v172, v148\n" \
"v_pk_add_f16 v139, v123, v131\n" \
"s_add_i32 m0, s44, 0xa000\n" \
"s_add_i32 s80, s46, 0x2c00000\n" \
"v_mfma_f32_16x16x32_f16 v[48:51], v[120:123], v[176:179], v[48:51]\n" \
"v_pk_add_f16 v173, v173, v149\n" \
"v_pk_add_f16 v140, v124, v132\n" \
"buffer_load_dwordx4 v232, s[36:39], s80 offen sc1 lds\n" \
"v_mfma_f32_16x16x32_f16 v[60:63], v[124:127], v[176:179], v[60:63]\n" \
"v_pk_add_f16 v174, v174, v150\n" \
"v_pk_add_f16 v175, v175, v151\n" \
"ds_read_b128 v[164:167], v240 offset:4608\n" \
"v_mfma_f32_16x16x32_f16 v[84:87], v[124:127], v[180:183], v[84:87]\n" \
"v_pk_add_f16 v176, v176, v152\n" \
"v_pk_add_f16 v141, v125, v133\n" \
"v_mfma_f32_16x16x32_f16 v[72:75], v[120:123], v[180:183], v[72:75]\n" \
"v_pk_add_f16 v177, v177, v153\n" \
"v_pk_add_f16 v142, v126, v134\n" \
"v_mfma_f32_16x16x32_f16 v[96:99], v[120:123], v[184:187], v[96:99]\n" \
"v_pk_add_f16 v178, v178, v154\n" \
"v_pk_add_f16 v179, v179, v155\n" \
"ds_read_b64_tr_b16 v[188:189], v236 offset:16384\n" \
"v_mfma_f32_16x16x32_f16 v[108:111], v[124:127], v[184:187], v[108:111]\n" \
"v_pk_add_f16 v180, v180, v208\n" \
"v_pk_add_f16 v143, v127, v135\n" \
"ds_read_b64_tr_b16 v[192:193], v237 offset:16384\n" \
"v_mfma_f32_16x16x32_f16 v[4:7], v[128:131], v[144:147], v[4:7]\n" \
"v_pk_add_f16 v181, v181, v209\n" \
"ds_read_b64_tr_b16 v[120:121], v241 offset:0\n" \
"buffer_load_dwordx4 v232, s[36:39], s80 offen offset:1024 sc1 lds\n" \
"v_mfma_f32_16x16x32_f16 v[16:19], v[132:135], v[144:147], v[16:19]\n" \
"v_pk_add_f16 v182, v182, v210\n" \
"ds_read_b128 v[160:163], v240 offset:2560\n" \
"v_mfma_f32_16x16x32_f16 v[40:43], v[132:135], v[148:151], v[40:43]\n" \
"v_pk_add_f16 v183, v183, v211\n" \
"ds_read_b128 v[144:147], v240 offset:2048\n" \
"v_mfma_f32_16x16x32_f16 v[28:31], v[128:131], v[148:151], v[28:31]\n" \
"v_pk_add_f16 v184, v184, v156\n" \
"ds_read_b64_tr_b16 v[122:123], v241 offset:8192\n" \
"v_mfma_f32_16x16x32_f16 v[52:55], v[128:131], v[152:155], v[52:55]\n" \
"v_pk_add_f16 v185, v185, v157\n" \
"ds_read_b128 v[148:151], v240 offset:4096\n" \
"s_waitcnt lgkmcnt(6)\n" \
"v_mfma_f32_16x16x16_bf16 v[188:191], v[188:189], v[254:255], 0\n" \
"v_mfma_f32_16x16x32_f16 v[64:67], v[132:135], v[152:155], v[64:67]\n" \
"v_pk_add_f16 v186, v186, v158\n" \
"ds_read_b64_tr_b16 v[124:125], v242 offset:0\n" \
"s_waitcnt lgkmcnt(6)\n" \
"v_mfma_f32_16x16x16_bf16 v[192:195], v[192:193], v[254:255], 0\n" \
"v_mfma_f32_16x16x32_f16 v[88:91], v[132:135], v[208:211], v[88:91]\n" \
"v_pk_add_f16 v187, v187, v159\n" \
"ds_read_b128 v[152:155], v240 offset:6144\n" \
"buffer_load_dwordx4 v232, s[36:39], s80 offen offset:2048 sc1 lds\n" \
"v_mfma_f32_16x16x32_f16 v[76:79], v[128:131], v[208:211], v[76:79]\n" \
"s_setprio 0\n" \
"ds_read_b64_tr_b16 v[126:127], v242 offset:8192\n" \
"v_mfma_f32_16x16x32_f16 v[100:103], v[128:131], v[156:159], v[100:103]\n" \
"v_cvt_pk_f16_f32 v188, v188, v189\n" \
"ds_read_b128 v[208:211], v240 offset:8192\n" \
"v_mfma_f32_16x16x32_f16 v[112:115], v[132:135], v[156:159], v[112:115]\n" \
"v_cvt_pk_f16_f32 v189, v190, v191\n" \
"ds_read_b64_tr_b16 v[128:129], v241 offset:512\n" \
"v_mfma_f32_16x16x32_f16 v[8:11], v[136:139], v[168:171], v[8:11]\n" \
"v_cvt_pk_f16_f32 v190, v192, v193\n" \
"ds_read_b128 v[156:159], v240 offset:0\n" \
"v_mfma_f32_16x16x32_f16 v[20:23], v[140:143], v[168:171], v[20:23]\n" \
"v_cvt_pk_f16_f32 v191, v194, v195\n" \
"v_add_u32 v188, s91, v188\n" \
"ds_read_b128 v[168:171], v240 offset:6656\n" \
"v_mfma_f32_16x16x32_f16 v[44:47], v[140:143], v[172:175], v[44:47]\n" \
"ds_read_b64_tr_b16 v[130:131], v241 offset:8704\n" \
"ds_read_b64_tr_b16 v[132:133], v242 offset:512\n" \
"buffer_load_dwordx4 v232, s[36:39], s80 offen offset:3072 sc1 lds\n" \
"v_mfma_f32_16x16x32_f16 v[32:35], v[136:139], v[172:175], v[32:35]\n" \
"v_add_u32 v189, s91, v189\n" \
"v_add_u32 v190, s91, v190\n" \
"ds_read_b128 v[172:175], v240 offset:8704\n" \
"v_mfma_f32_16x16x32_f16 v[56:59], v[136:139], v[176:179], v[56:59]\n" \
"v_add_u32 v191, s91, v191\n" \
"ds_read_b64_tr_b16 v[134:135], v242 offset:8704\n" \
"v_mfma_f32_16x16x32_f16 v[68:71], v[140:143], v[176:179], v[68:71]\n" \
"v_and_b32 v188, s92, v188\n" \
"ds_read_b128 v[176:179], v240 offset:512\n" \
"v_mfma_f32_16x16x32_f16 v[92:95], v[140:143], v[180:183], v[92:95]\n" \
"v_and_b32 v189, s92, v189\n" \
"s_add_i32 m0, s45, 0xa000\n" \
"s_add_i32 s81, s47, 0x2c00000\n" \
"s_add_i32 s82, s49, 0x4800000\n" \
"v_mfma_f32_16x16x32_f16 v[80:83], v[136:139], v[180:183], v[80:83]\n" \
"v_and_b32 v190, s92, v190\n" \
"v_and_b32 v191, s92, v191\n" \
"buffer_load_dwordx4 v232, s[36:39], s81 offen sc1 lds\n" \
"v_mfma_f32_16x16x32_f16 v[104:107], v[136:139], v[184:187], v[104:107]\n" \
"buffer_store_dwordx4 v[188:191], v234, s[36:39], s82 offen sc1\n" \
"v_mfma_f32_16x16x32_f16 v[116:119], v[140:143], v[184:187], v[116:119]\n" \
"s_setprio 1\n" \
"s_waitcnt vmcnt(7) lgkmcnt(0)\n" \
"s_barrier\n" \
"v_mfma_f32_16x16x32_f16 v[0:3], v[120:123], v[144:147], v[0:3]\n" \
"v_pk_add_f16 v136, v120, v128\n" \
"v_pk_add_f16 v137, v121, v129\n" \
"v_mfma_f32_16x16x32_f16 v[12:15], v[124:127], v[144:147], v[12:15]\n" \
"v_pk_add_f16 v144, v144, v160\n" \
"v_pk_add_f16 v138, v122, v130\n" \
"v_mfma_f32_16x16x32_f16 v[36:39], v[124:127], v[148:151], v[36:39]\n" \
"v_pk_add_f16 v145, v145, v161\n" \
"v_pk_add_f16 v146, v146, v162\n" \
"v_pk_add_f16 v147, v147, v163\n" \
"v_mfma_f32_16x16x32_f16 v[24:27], v[120:123], v[148:151], v[24:27]\n" \
"v_pk_add_f16 v148, v148, v164\n" \
"v_pk_add_f16 v139, v123, v131\n" \
"s_add_i32 m0, s44, 0x0\n" \
"s_add_i32 s80, s46, 0x3000000\n" \
"v_mfma_f32_16x16x32_f16 v[48:51], v[120:123], v[152:155], v[48:51]\n" \
"v_pk_add_f16 v149, v149, v165\n" \
"v_pk_add_f16 v140, v124, v132\n" \
"buffer_load_dwordx4 v232, s[36:39], s80 offen sc1 lds\n" \
"v_mfma_f32_16x16x32_f16 v[60:63], v[124:127], v[152:155], v[60:63]\n" \
"v_pk_add_f16 v150, v150, v166\n" \
"v_pk_add_f16 v151, v151, v167\n" \
"ds_read_b128 v[184:187], v240 offset:25088\n" \
"v_mfma_f32_16x16x32_f16 v[84:87], v[124:127], v[208:211], v[84:87]\n" \
"v_pk_add_f16 v152, v152, v168\n" \
"v_pk_add_f16 v141, v125, v133\n" \
"v_mfma_f32_16x16x32_f16 v[72:75], v[120:123], v[208:211], v[72:75]\n" \
"v_pk_add_f16 v153, v153, v169\n" \
"v_pk_add_f16 v142, v126, v134\n" \
"v_mfma_f32_16x16x32_f16 v[96:99], v[120:123], v[156:159], v[96:99]\n" \
"v_pk_add_f16 v154, v154, v170\n" \
"v_pk_add_f16 v155, v155, v171\n" \
"ds_read_b64_tr_b16 v[188:189], v238 offset:16384\n" \
"v_mfma_f32_16x16x32_f16 v[108:111], v[124:127], v[156:159], v[108:111]\n" \
"v_pk_add_f16 v208, v208, v172\n" \
"v_pk_add_f16 v143, v127, v135\n" \
"ds_read_b64_tr_b16 v[192:193], v239 offset:16384\n" \
"v_mfma_f32_16x16x32_f16 v[4:7], v[128:131], v[160:163], v[4:7]\n" \
"v_pk_add_f16 v209, v209, v173\n" \
"ds_read_b64_tr_b16 v[120:121], v241 offset:16384\n" \
"buffer_load_dwordx4 v232, s[36:39], s80 offen offset:1024 sc1 lds\n" \
"v_mfma_f32_16x16x32_f16 v[16:19], v[132:135], v[160:163], v[16:19]\n" \
"v_pk_add_f16 v210, v210, v174\n" \
"ds_read_b128 v[180:183], v240 offset:23040\n" \
"v_mfma_f32_16x16x32_f16 v[40:43], v[132:135], v[164:167], v[40:43]\n" \
"v_pk_add_f16 v211, v211, v175\n" \
"ds_read_b128 v[160:163], v240 offset:22528\n" \
"v_mfma_f32_16x16x32_f16 v[28:31], v[128:131], v[164:167], v[28:31]\n" \
"v_pk_add_f16 v156, v156, v176\n" \
"ds_read_b64_tr_b16 v[122:123], v241 offset:24576\n" \
"v_mfma_f32_16x16x32_f16 v[52:55], v[128:131], v[168:171], v[52:55]\n" \
"v_pk_add_f16 v157, v157, v177\n" \
"ds_read_b128 v[164:167], v240 offset:24576\n" \
"s_waitcnt lgkmcnt(6)\n" \
"v_mfma_f32_16x16x16_bf16 v[188:191], v[188:189], v[254:255], 0\n" \
"v_mfma_f32_16x16x32_f16 v[64:67], v[132:135], v[168:171], v[64:67]\n" \
"v_pk_add_f16 v158, v158, v178\n" \
"ds_read_b64_tr_b16 v[124:125], v242 offset:16384\n" \
"s_waitcnt lgkmcnt(6)\n" \
"v_mfma_f32_16x16x16_bf16 v[192:195], v[192:193], v[254:255], 0\n" \
"v_mfma_f32_16x16x32_f16 v[88:91], v[132:135], v[172:175], v[88:91]\n" \
"v_pk_add_f16 v159, v159, v179\n" \
"ds_read_b128 v[168:171], v240 offset:26624\n" \
"buffer_load_dwordx4 v232, s[36:39], s80 offen offset:2048 sc1 lds\n" \
"v_mfma_f32_16x16x32_f16 v[76:79], v[128:131], v[172:175], v[76:79]\n" \
"s_setprio 0\n" \
"ds_read_b64_tr_b16 v[126:127], v242 offset:24576\n" \
"v_mfma_f32_16x16x32_f16 v[100:103], v[128:131], v[176:179], v[100:103]\n" \
"v_cvt_pk_f16_f32 v188, v188, v189\n" \
"ds_read_b128 v[172:175], v240 offset:28672\n" \
"v_mfma_f32_16x16x32_f16 v[112:115], v[132:135], v[176:179], v[112:115]\n" \
"v_cvt_pk_f16_f32 v189, v190, v191\n" \
"ds_read_b64_tr_b16 v[128:129], v241 offset:16896\n" \
"v_mfma_f32_16x16x32_f16 v[8:11], v[136:139], v[144:147], v[8:11]\n" \
"v_cvt_pk_f16_f32 v190, v192, v193\n" \
"ds_read_b128 v[176:179], v240 offset:20480\n" \
"v_mfma_f32_16x16x32_f16 v[20:23], v[140:143], v[144:147], v[20:23]\n" \
"v_cvt_pk_f16_f32 v191, v194, v195\n" \
"v_add_u32 v188, s91, v188\n" \
"ds_read_b128 v[144:147], v240 offset:27136\n" \
"v_mfma_f32_16x16x32_f16 v[44:47], v[140:143], v[148:151], v[44:47]\n" \
"ds_read_b64_tr_b16 v[130:131], v241 offset:25088\n" \
"ds_read_b64_tr_b16 v[132:133], v242 offset:16896\n" \
"buffer_load_dwordx4 v232, s[36:39], s80 offen offset:3072 sc1 lds\n" \
"v_mfma_f32_16x16x32_f16 v[32:35], v[136:139], v[148:151], v[32:35]\n" \
"v_add_u32 v189, s91, v189\n" \
"v_add_u32 v190, s91, v190\n" \
"ds_read_b128 v[148:151], v240 offset:29184\n" \
"v_mfma_f32_16x16x32_f16 v[56:59], v[136:139], v[152:155], v[56:59]\n" \
"v_add_u32 v191, s91, v191\n" \
"ds_read_b64_tr_b16 v[134:135], v242 offset:25088\n" \
"v_mfma_f32_16x16x32_f16 v[68:71], v[140:143], v[152:155], v[68:71]\n" \
"v_and_b32 v188, s92, v188\n" \
"ds_read_b128 v[152:155], v240 offset:20992\n" \
"v_mfma_f32_16x16x32_f16 v[92:95], v[140:143], v[208:211], v[92:95]\n" \
"v_and_b32 v189, s92, v189\n" \
"s_add_i32 m0, s45, 0x0\n" \
"s_add_i32 s81, s47, 0x3000000\n" \
"v_mfma_f32_16x16x32_f16 v[80:83], v[136:139], v[208:211], v[80:83]\n" \
"v_and_b32 v190, s92, v190\n" \
"v_and_b32 v191, s92, v191\n" \
"buffer_load_dwordx4 v232, s[36:39], s81 offen sc1 lds\n" \
"v_mfma_f32_16x16x32_f16 v[104:107], v[136:139], v[156:159], v[104:107]\n" \
"buffer_store_dwordx4 v[188:191], v234, s[36:39], s82 offen offset:1024 sc1\n" \
"v_mfma_f32_16x16x32_f16 v[116:119], v[140:143], v[156:159], v[116:119]\n" \
"s_setprio 1\n" \
"s_waitcnt vmcnt(7) lgkmcnt(0)\n" \
"s_barrier\n" \
"v_mfma_f32_16x16x32_f16 v[0:3], v[120:123], v[160:163], v[0:3]\n" \
"v_pk_add_f16 v136, v120, v128\n" \
"v_pk_add_f16 v137, v121, v129\n" \
"v_mfma_f32_16x16x32_f16 v[12:15], v[124:127], v[160:163], v[12:15]\n" \
"v_pk_add_f16 v160, v160, v180\n" \
"v_pk_add_f16 v138, v122, v130\n" \
"v_mfma_f32_16x16x32_f16 v[36:39], v[124:127], v[164:167], v[36:39]\n" \
"v_pk_add_f16 v161, v161, v181\n" \
"v_pk_add_f16 v162, v162, v182\n" \
"v_pk_add_f16 v163, v163, v183\n" \
"buffer_store_dwordx4 v[220:223], v235, s[36:39], s66 offen sc1\n" \
"v_mfma_f32_16x16x32_f16 v[24:27], v[120:123], v[164:167], v[24:27]\n" \
"v_pk_add_f16 v164, v164, v184\n" \
"v_pk_add_f16 v139, v123, v131\n" \
"s_add_i32 m0, s44, 0x5000\n" \
"s_add_i32 s80, s46, 0x3400000\n" \
"v_mfma_f32_16x16x32_f16 v[48:51], v[120:123], v[168:171], v[48:51]\n" \
"v_pk_add_f16 v165, v165, v185\n" \
"v_pk_add_f16 v140, v124, v132\n" \
"buffer_load_dwordx4 v232, s[36:39], s80 offen sc1 lds\n" \
"v_mfma_f32_16x16x32_f16 v[60:63], v[124:127], v[168:171], v[60:63]\n" \
"v_pk_add_f16 v166, v166, v186\n" \
"v_pk_add_f16 v167, v167, v187\n" \
"ds_read_b128 v[156:159], v240 offset:45568\n" \
"v_mfma_f32_16x16x32_f16 v[84:87], v[124:127], v[172:175], v[84:87]\n" \
"v_pk_add_f16 v168, v168, v144\n" \
"v_pk_add_f16 v141, v125, v133\n" \
"v_mfma_f32_16x16x32_f16 v[72:75], v[120:123], v[172:175], v[72:75]\n" \
"v_pk_add_f16 v169, v169, v145\n" \
"v_pk_add_f16 v142, v126, v134\n" \
"v_mfma_f32_16x16x32_f16 v[96:99], v[120:123], v[176:179], v[96:99]\n" \
"v_pk_add_f16 v170, v170, v146\n" \
"v_pk_add_f16 v171, v171, v147\n" \
"ds_read_b64_tr_b16 v[188:189], v236 offset:20480\n" \
"v_mfma_f32_16x16x32_f16 v[108:111], v[124:127], v[176:179], v[108:111]\n" \
"v_pk_add_f16 v172, v172, v148\n" \
"v_pk_add_f16 v143, v127, v135\n" \
"ds_read_b64_tr_b16 v[192:193], v237 offset:20480\n" \
"v_mfma_f32_16x16x32_f16 v[4:7], v[128:131], v[180:183], v[4:7]\n" \
"v_pk_add_f16 v173, v173, v149\n" \
"ds_read_b64_tr_b16 v[120:121], v241 offset:32768\n" \
"buffer_load_dwordx4 v232, s[36:39], s80 offen offset:1024 sc1 lds\n" \
"v_mfma_f32_16x16x32_f16 v[16:19], v[132:135], v[180:183], v[16:19]\n" \
"v_pk_add_f16 v174, v174, v150\n" \
"ds_read_b128 v[208:211], v240 offset:43520\n" \
"v_mfma_f32_16x16x32_f16 v[40:43], v[132:135], v[184:187], v[40:43]\n" \
"v_pk_add_f16 v175, v175, v151\n" \
"ds_read_b128 v[180:183], v240 offset:43008\n" \
"v_mfma_f32_16x16x32_f16 v[28:31], v[128:131], v[184:187], v[28:31]\n" \
"v_pk_add_f16 v176, v176, v152\n" \
"ds_read_b64_tr_b16 v[122:123], v241 offset:40960\n" \
"v_mfma_f32_16x16x32_f16 v[52:55], v[128:131], v[144:147], v[52:55]\n" \
"v_pk_add_f16 v177, v177, v153\n" \
"ds_read_b128 v[184:187], v240 offset:45056\n" \
"s_waitcnt lgkmcnt(6)\n" \
"v_mfma_f32_16x16x16_bf16 v[188:191], v[188:189], v[254:255], 0\n" \
"v_mfma_f32_16x16x32_f16 v[64:67], v[132:135], v[144:147], v[64:67]\n" \
"v_pk_add_f16 v178, v178, v154\n" \
"ds_read_b64_tr_b16 v[124:125], v242 offset:32768\n" \
"s_waitcnt lgkmcnt(6)\n" \
"v_mfma_f32_16x16x16_bf16 v[192:195], v[192:193], v[254:255], 0\n" \
"v_mfma_f32_16x16x32_f16 v[88:91], v[132:135], v[148:151], v[88:91]\n" \
"v_pk_add_f16 v179, v179, v155\n" \
"ds_read_b128 v[144:147], v240 offset:47104\n" \
"buffer_load_dwordx4 v232, s[36:39], s80 offen offset:2048 sc1 lds\n" \
"v_mfma_f32_16x16x32_f16 v[76:79], v[128:131], v[148:151], v[76:79]\n" \
"s_setprio 0\n" \
"ds_read_b64_tr_b16 v[126:127], v242 offset:40960\n" \
"v_mfma_f32_16x16x32_f16 v[100:103], v[128:131], v[152:155], v[100:103]\n" \
"v_cvt_pk_f16_f32 v188, v188, v189\n" \
"ds_read_b128 v[148:151], v240 offset:49152\n" \
"v_mfma_f32_16x16x32_f16 v[112:115], v[132:135], v[152:155], v[112:115]\n" \
"v_cvt_pk_f16_f32 v189, v190, v191\n" \
"ds_read_b64_tr_b16 v[128:129], v241 offset:33280\n" \
"v_mfma_f32_16x16x32_f16 v[8:11], v[136:139], v[160:163], v[8:11]\n" \
"v_cvt_pk_f16_f32 v190, v192, v193\n" \
"ds_read_b128 v[152:155], v240 offset:40960\n" \
"v_mfma_f32_16x16x32_f16 v[20:23], v[140:143], v[160:163], v[20:23]\n" \
"v_cvt_pk_f16_f32 v191, v194, v195\n" \
"v_add_u32 v188, s91, v188\n" \
"ds_read_b128 v[160:163], v240 offset:47616\n" \
"v_mfma_f32_16x16x32_f16 v[44:47], v[140:143], v[164:167], v[44:47]\n" \
"ds_read_b64_tr_b16 v[130:131], v241 offset:41472\n" \
"ds_read_b64_tr_b16 v[132:133], v242 offset:33280\n" \
"buffer_load_dwordx4 v232, s[36:39], s80 offen offset:3072 sc1 lds\n" \
"v_mfma_f32_16x16x32_f16 v[32:35], v[136:139], v[164:167], v[32:35]\n" \
"v_add_u32 v189, s91, v189\n" \
"v_add_u32 v190, s91, v190\n" \
"ds_read_b128 v[164:167], v240 offset:49664\n" \
"v_mfma_f32_16x16x32_f16 v[56:59], v[136:139], v[168:171], v[56:59]\n" \
"v_add_u32 v191, s91, v191\n" \
"ds_read_b64_tr_b16 v[134:135], v242 offset:41472\n" \
"v_mfma_f32_16x16x32_f16 v[68:71], v[140:143], v[168:171], v[68:71]\n" \
"v_and_b32 v188, s92, v188\n" \
"ds_read_b128 v[168:171], v240 offset:41472\n" \
"v_mfma_f32_16x16x32_f16 v[92:95], v[140:143], v[172:175], v[92:95]\n" \
"v_and_b32 v189, s92, v189\n" \
"s_add_i32 m0, s45, 0x5000\n" \
"s_add_i32 s81, s47, 0x3400000\n" \
"s_add_i32 s82, s50, 0x5000000\n" \
"v_mfma_f32_16x16x32_f16 v[80:83], v[136:139], v[172:175], v[80:83]\n" \
"v_and_b32 v190, s92, v190\n" \
"v_and_b32 v191, s92, v191\n" \
"buffer_load_dwordx4 v232, s[36:39], s81 offen sc1 lds\n" \
"v_mfma_f32_16x16x32_f16 v[104:107], v[136:139], v[176:179], v[104:107]\n" \
"buffer_store_dwordx4 v[188:191], v234, s[36:39], s82 offen sc1\n" \
"v_mfma_f32_16x16x32_f16 v[116:119], v[140:143], v[176:179], v[116:119]\n" \
"s_setprio 1\n" \
"s_waitcnt vmcnt(7) lgkmcnt(0)\n" \
"s_barrier\n" \
"v_mfma_f32_16x16x32_f16 v[0:3], v[120:123], v[180:183], v[0:3]\n" \
"v_pk_add_f16 v136, v120, v128\n" \
"v_pk_add_f16 v137, v121, v129\n" \
"v_mfma_f32_16x16x32_f16 v[12:15], v[124:127], v[180:183], v[12:15]\n" \
"v_pk_add_f16 v180, v180, v208\n" \
"v_pk_add_f16 v138, v122, v130\n" \
"v_mfma_f32_16x16x32_f16 v[36:39], v[124:127], v[184:187], v[36:39]\n" \
"v_pk_add_f16 v181, v181, v209\n" \
"v_pk_add_f16 v182, v182, v210\n" \
"v_pk_add_f16 v183, v183, v211\n" \
"v_mfma_f32_16x16x32_f16 v[24:27], v[120:123], v[184:187], v[24:27]\n" \
"v_pk_add_f16 v184, v184, v156\n" \
"v_pk_add_f16 v139, v123, v131\n" \
"s_add_i32 m0, s44, 0xa000\n" \
"s_add_i32 s80, s46, 0x3800000\n" \
"v_mfma_f32_16x16x32_f16 v[48:51], v[120:123], v[144:147], v[48:51]\n" \
"v_pk_add_f16 v185, v185, v157\n" \
"v_pk_add_f16 v140, v124, v132\n" \
"buffer_load_dwordx4 v232, s[36:39], s80 offen sc1 lds\n" \
"v_mfma_f32_16x16x32_f16 v[60:63], v[124:127], v[144:147], v[60:63]\n" \
"v_pk_add_f16 v186, v186, v158\n" \
"v_pk_add_f16 v187, v187, v159\n" \
"ds_read_b128 v[176:179], v240 offset:4608\n" \
"v_mfma_f32_16x16x32_f16 v[84:87], v[124:127], v[148:151], v[84:87]\n" \
"v_pk_add_f16 v144, v144, v160\n" \
"v_pk_add_f16 v141, v125, v133\n" \
"v_mfma_f32_16x16x32_f16 v[72:75], v[120:123], v[148:151], v[72:75]\n" \
"v_pk_add_f16 v145, v145, v161\n" \
"v_pk_add_f16 v142, v126, v134\n" \
"v_mfma_f32_16x16x32_f16 v[96:99], v[120:123], v[152:155], v[96:99]\n" \
"v_pk_add_f16 v146, v146, v162\n" \
"v_pk_add_f16 v147, v147, v163\n" \
"ds_read_b64_tr_b16 v[188:189], v238 offset:20480\n" \
"v_mfma_f32_16x16x32_f16 v[108:111], v[124:127], v[152:155], v[108:111]\n" \
"v_pk_add_f16 v148, v148, v164\n" \
"v_pk_add_f16 v143, v127, v135\n" \
"ds_read_b64_tr_b16 v[192:193], v239 offset:20480\n" \
"v_mfma_f32_16x16x32_f16 v[4:7], v[128:131], v[208:211], v[4:7]\n" \
"v_pk_add_f16 v149, v149, v165\n" \
"ds_read_b64_tr_b16 v[120:121], v241 offset:0\n" \
"buffer_load_dwordx4 v232, s[36:39], s80 offen offset:1024 sc1 lds\n" \
"v_mfma_f32_16x16x32_f16 v[16:19], v[132:135], v[208:211], v[16:19]\n" \
"v_pk_add_f16 v150, v150, v166\n" \
"ds_read_b128 v[172:175], v240 offset:2560\n" \
"v_mfma_f32_16x16x32_f16 v[40:43], v[132:135], v[156:159], v[40:43]\n" \
"v_pk_add_f16 v151, v151, v167\n" \
"ds_read_b128 v[208:211], v240 offset:2048\n" \
"v_mfma_f32_16x16x32_f16 v[28:31], v[128:131], v[156:159], v[28:31]\n" \
"v_pk_add_f16 v152, v152, v168\n" \
"ds_read_b64_tr_b16 v[122:123], v241 offset:8192\n" \
"v_mfma_f32_16x16x32_f16 v[52:55], v[128:131], v[160:163], v[52:55]\n" \
"v_pk_add_f16 v153, v153, v169\n" \
"ds_read_b128 v[156:159], v240 offset:4096\n" \
"s_waitcnt lgkmcnt(6)\n" \
"v_mfma_f32_16x16x16_bf16 v[188:191], v[188:189], v[254:255], 0\n" \
"v_mfma_f32_16x16x32_f16 v[64:67], v[132:135], v[160:163], v[64:67]\n" \
"v_pk_add_f16 v154, v154, v170\n" \
"ds_read_b64_tr_b16 v[124:125], v242 offset:0\n" \
"s_waitcnt lgkmcnt(6)\n" \
"v_mfma_f32_16x16x16_bf16 v[192:195], v[192:193], v[254:255], 0\n" \
"v_mfma_f32_16x16x32_f16 v[88:91], v[132:135], v[164:167], v[88:91]\n" \
"v_pk_add_f16 v155, v155, v171\n" \
"ds_read_b128 v[160:163], v240 offset:6144\n" \
"buffer_load_dwordx4 v232, s[36:39], s80 offen offset:2048 sc1 lds\n" \
"v_mfma_f32_16x16x32_f16 v[76:79], v[128:131], v[164:167], v[76:79]\n" \
"s_setprio 0\n" \
"ds_read_b64_tr_b16 v[126:127], v242 offset:8192\n" \
"v_mfma_f32_16x16x32_f16 v[100:103], v[128:131], v[168:171], v[100:103]\n" \
"v_cvt_pk_f16_f32 v188, v188, v189\n" \
"ds_read_b128 v[164:167], v240 offset:8192\n" \
"v_mfma_f32_16x16x32_f16 v[112:115], v[132:135], v[168:171], v[112:115]\n" \
"v_cvt_pk_f16_f32 v189, v190, v191\n" \
"ds_read_b64_tr_b16 v[128:129], v241 offset:512\n" \
"v_mfma_f32_16x16x32_f16 v[8:11], v[136:139], v[180:183], v[8:11]\n" \
"v_cvt_pk_f16_f32 v190, v192, v193\n" \
"ds_read_b128 v[168:171], v240 offset:0\n" \
"v_mfma_f32_16x16x32_f16 v[20:23], v[140:143], v[180:183], v[20:23]\n" \
"v_cvt_pk_f16_f32 v191, v194, v195\n" \
"v_add_u32 v188, s91, v188\n" \
"ds_read_b128 v[180:183], v240 offset:6656\n" \
"v_mfma_f32_16x16x32_f16 v[44:47], v[140:143], v[184:187], v[44:47]\n" \
"ds_read_b64_tr_b16 v[130:131], v241 offset:8704\n" \
"ds_read_b64_tr_b16 v[132:133], v242 offset:512\n" \
"buffer_load_dwordx4 v232, s[36:39], s80 offen offset:3072 sc1 lds\n" \
"v_mfma_f32_16x16x32_f16 v[32:35], v[136:139], v[184:187], v[32:35]\n" \
"v_add_u32 v189, s91, v189\n" \
"v_add_u32 v190, s91, v190\n" \
"ds_read_b128 v[184:187], v240 offset:8704\n" \
"v_mfma_f32_16x16x32_f16 v[56:59], v[136:139], v[144:147], v[56:59]\n" \
"v_add_u32 v191, s91, v191\n" \
"ds_read_b64_tr_b16 v[134:135], v242 offset:8704\n" \
"v_mfma_f32_16x16x32_f16 v[68:71], v[140:143], v[144:147], v[68:71]\n" \
"v_and_b32 v188, s92, v188\n" \
"ds_read_b128 v[144:147], v240 offset:512\n" \
"v_mfma_f32_16x16x32_f16 v[92:95], v[140:143], v[148:151], v[92:95]\n" \
"v_and_b32 v189, s92, v189\n" \
"s_add_i32 m0, s45, 0xa000\n" \
"s_add_i32 s81, s47, 0x3800000\n" \
"v_mfma_f32_16x16x32_f16 v[80:83], v[136:139], v[148:151], v[80:83]\n" \
"v_and_b32 v190, s92, v190\n" \
"v_and_b32 v191, s92, v191\n" \
"buffer_load_dwordx4 v232, s[36:39], s81 offen sc1 lds\n" \
"v_mfma_f32_16x16x32_f16 v[104:107], v[136:139], v[152:155], v[104:107]\n" \
"buffer_store_dwordx4 v[188:191], v234, s[36:39], s82 offen offset:1024 sc1\n" \
"v_mfma_f32_16x16x32_f16 v[116:119], v[140:143], v[152:155], v[116:119]\n" \
"s_setprio 1\n" \
"s_waitcnt vmcnt(7) lgkmcnt(0)\n" \
"s_barrier\n" \
"v_mfma_f32_16x16x32_f16 v[0:3], v[120:123], v[208:211], v[0:3]\n" \
"v_pk_add_f16 v136, v120, v128\n" \
"v_pk_add_f16 v137, v121, v129\n" \
"v_mfma_f32_16x16x32_f16 v[12:15], v[124:127], v[208:211], v[12:15]\n" \
"v_pk_add_f16 v208, v208, v172\n" \
"v_pk_add_f16 v138, v122, v130\n" \
"s_mov_b32 s84, 0x800c000\n" \
"v_mfma_f32_16x16x32_f16 v[36:39], v[124:127], v[156:159], v[36:39]\n" \
"v_pk_add_f16 v209, v209, v173\n" \
"v_pk_add_f16 v210, v210, v174\n" \
"v_pk_add_f16 v211, v211, v175\n" \
"buffer_load_dword v253, v252, s[36:39], s84 offen sc1\n" \
"buffer_store_dwordx4 v[224:227], v235, s[36:39], s67 offen sc1\n" \
"v_mfma_f32_16x16x32_f16 v[24:27], v[120:123], v[156:159], v[24:27]\n" \
"v_pk_add_f16 v156, v156, v176\n" \
"v_pk_add_f16 v139, v123, v131\n" \
"s_add_i32 m0, s44, 0x0\n" \
"s_add_i32 s80, s46, 0x3c00000\n" \
"v_mfma_f32_16x16x32_f16 v[48:51], v[120:123], v[160:163], v[48:51]\n" \
"v_pk_add_f16 v157, v157, v177\n" \
"v_pk_add_f16 v140, v124, v132\n" \
"buffer_load_dwordx4 v232, s[36:39], s80 offen sc1 lds\n" \
"v_mfma_f32_16x16x32_f16 v[60:63], v[124:127], v[160:163], v[60:63]\n" \
"v_pk_add_f16 v158, v158, v178\n" \
"v_pk_add_f16 v159, v159, v179\n" \
"ds_read_b128 v[152:155], v240 offset:25088\n" \
"v_mfma_f32_16x16x32_f16 v[84:87], v[124:127], v[164:167], v[84:87]\n" \
"v_pk_add_f16 v160, v160, v180\n" \
"v_pk_add_f16 v141, v125, v133\n" \
"v_mfma_f32_16x16x32_f16 v[72:75], v[120:123], v[164:167], v[72:75]\n" \
"v_pk_add_f16 v161, v161, v181\n" \
"v_pk_add_f16 v142, v126, v134\n" \
"v_mfma_f32_16x16x32_f16 v[96:99], v[120:123], v[168:171], v[96:99]\n" \
"v_pk_add_f16 v162, v162, v182\n" \
"v_pk_add_f16 v163, v163, v183\n" \
"ds_read_b64_tr_b16 v[188:189], v236 offset:24576\n" \
"v_mfma_f32_16x16x32_f16 v[108:111], v[124:127], v[168:171], v[108:111]\n" \
"v_pk_add_f16 v164, v164, v184\n" \
"v_pk_add_f16 v143, v127, v135\n" \
"ds_read_b64_tr_b16 v[192:193], v237 offset:24576\n" \
"v_mfma_f32_16x16x32_f16 v[4:7], v[128:131], v[172:175], v[4:7]\n" \
"v_pk_add_f16 v165, v165, v185\n" \
"ds_read_b64_tr_b16 v[120:121], v241 offset:16384\n" \
"buffer_load_dwordx4 v232, s[36:39], s80 offen offset:1024 sc1 lds\n" \
"v_mfma_f32_16x16x32_f16 v[16:19], v[132:135], v[172:175], v[16:19]\n" \
"v_pk_add_f16 v166, v166, v186\n" \
"ds_read_b128 v[148:151], v240 offset:23040\n" \
"v_mfma_f32_16x16x32_f16 v[40:43], v[132:135], v[176:179], v[40:43]\n" \
"v_pk_add_f16 v167, v167, v187\n" \
"ds_read_b128 v[172:175], v240 offset:22528\n" \
"v_mfma_f32_16x16x32_f16 v[28:31], v[128:131], v[176:179], v[28:31]\n" \
"v_pk_add_f16 v168, v168, v144\n" \
"ds_read_b64_tr_b16 v[122:123], v241 offset:24576\n" \
"v_mfma_f32_16x16x32_f16 v[52:55], v[128:131], v[180:183], v[52:55]\n" \
"v_pk_add_f16 v169, v169, v145\n" \
"ds_read_b128 v[176:179], v240 offset:24576\n" \
"s_waitcnt lgkmcnt(6)\n" \
"v_mfma_f32_16x16x16_bf16 v[188:191], v[188:189], v[254:255], 0\n" \
"v_mfma_f32_16x16x32_f16 v[64:67], v[132:135], v[180:183], v[64:67]\n" \
"v_pk_add_f16 v170, v170, v146\n" \
"ds_read_b64_tr_b16 v[124:125], v242 offset:16384\n" \
"s_waitcnt lgkmcnt(6)\n" \
"v_mfma_f32_16x16x16_bf16 v[192:195], v[192:193], v[254:255], 0\n" \
"v_mfma_f32_16x16x32_f16 v[88:91], v[132:135], v[184:187], v[88:91]\n" \
"v_pk_add_f16 v171, v171, v147\n" \
"ds_read_b128 v[180:183], v240 offset:26624\n" \
"buffer_load_dwordx4 v232, s[36:39], s80 offen offset:2048 sc1 lds\n" \
"v_mfma_f32_16x16x32_f16 v[76:79], v[128:131], v[184:187], v[76:79]\n" \
"s_setprio 0\n" \
"ds_read_b64_tr_b16 v[126:127], v242 offset:24576\n" \
"v_mfma_f32_16x16x32_f16 v[100:103], v[128:131], v[144:147], v[100:103]\n" \
"v_cvt_pk_f16_f32 v188, v188, v189\n" \
"ds_read_b128 v[184:187], v240 offset:28672\n" \
"v_mfma_f32_16x16x32_f16 v[112:115], v[132:135], v[144:147], v[112:115]\n" \
"v_cvt_pk_f16_f32 v189, v190, v191\n" \
"ds_read_b64_tr_b16 v[128:129], v241 offset:16896\n" \
"v_mfma_f32_16x16x32_f16 v[8:11], v[136:139], v[208:211], v[8:11]\n" \
"v_cvt_pk_f16_f32 v190, v192, v193\n" \
"ds_read_b128 v[144:147], v240 offset:20480\n" \
"v_mfma_f32_16x16x32_f16 v[20:23], v[140:143], v[208:211], v[20:23]\n" \
"v_cvt_pk_f16_f32 v191, v194, v195\n" \
"v_add_u32 v188, s91, v188\n" \
"ds_read_b128 v[208:211], v240 offset:27136\n" \
"v_mfma_f32_16x16x32_f16 v[44:47], v[140:143], v[156:159], v[44:47]\n" \
"ds_read_b64_tr_b16 v[130:131], v241 offset:25088\n" \
"ds_read_b64_tr_b16 v[132:133], v242 offset:16896\n" \
"buffer_load_dwordx4 v232, s[36:39], s80 offen offset:3072 sc1 lds\n" \
"v_mfma_f32_16x16x32_f16 v[32:35], v[136:139], v[156:159], v[32:35]\n" \
"v_add_u32 v189, s91, v189\n" \
"v_add_u32 v190, s91, v190\n" \
"ds_read_b128 v[156:159], v240 offset:29184\n" \
"v_mfma_f32_16x16x32_f16 v[56:59], v[136:139], v[160:163], v[56:59]\n" \
"v_add_u32 v191, s91, v191\n" \
"ds_read_b64_tr_b16 v[134:135], v242 offset:25088\n" \
"v_mfma_f32_16x16x32_f16 v[68:71], v[140:143], v[160:163], v[68:71]\n" \
"v_and_b32 v188, s92, v188\n" \
"v_and_b32 v189, s92, v189\n" \
"ds_read_b128 v[160:163], v240 offset:20992\n" \
"v_mfma_f32_16x16x32_f16 v[92:95], v[140:143], v[164:167], v[92:95]\n" \
"s_waitcnt vmcnt(5)\n" \
"v_cmp_eq_u32 vcc, 0, v253\n" \
"s_cbranch_vccnz .Ls16_%=\n" \
".Lr16_%=:\n" \
"s_add_i32 m0, s45, 0x0\n" \
"s_add_i32 s81, s47, 0x3c00000\n" \
"s_add_i32 s82, s51, 0x5800000\n" \
"v_mfma_f32_16x16x32_f16 v[80:83], v[136:139], v[164:167], v[80:83]\n" \
"v_and_b32 v190, s92, v190\n" \
"v_and_b32 v191, s92, v191\n" \
"buffer_load_dwordx4 v232, s[36:39], s81 offen sc1 lds\n" \
"v_mfma_f32_16x16x32_f16 v[104:107], v[136:139], v[168:171], v[104:107]\n" \
"buffer_store_dwordx4 v[188:191], v234, s[36:39], s82 offen sc1\n" \
"v_mfma_f32_16x16x32_f16 v[116:119], v[140:143], v[168:171], v[116:119]\n" \
"s_setprio 1\n" \
"s_waitcnt vmcnt(7) lgkmcnt(0)\n" \
"s_barrier\n" \
"v_mfma_f32_16x16x32_f16 v[0:3], v[120:123], v[172:175], v[0:3]\n" \
"v_pk_add_f16 v136, v120, v128\n" \
"v_pk_add_f16 v137, v121, v129\n" \
"v_mfma_f32_16x16x32_f16 v[12:15], v[124:127], v[172:175], v[12:15]\n" \
"v_pk_add_f16 v172, v172, v148\n" \
"v_pk_add_f16 v138, v122, v130\n" \
"v_mfma_f32_16x16x32_f16 v[36:39], v[124:127], v[176:179], v[36:39]\n" \
"v_pk_add_f16 v173, v173, v149\n" \
"v_pk_add_f16 v174, v174, v150\n" \
"v_pk_add_f16 v175, v175, v151\n" \
"v_mfma_f32_16x16x32_f16 v[24:27], v[120:123], v[176:179], v[24:27]\n" \
"v_pk_add_f16 v176, v176, v152\n" \
"v_pk_add_f16 v139, v123, v131\n" \
"s_add_i32 m0, s44, 0x5000\n" \
"s_add_i32 s80, s46, 0x4000000\n" \
"v_mfma_f32_16x16x32_f16 v[48:51], v[120:123], v[180:183], v[48:51]\n" \
"v_pk_add_f16 v177, v177, v153\n" \
"v_pk_add_f16 v140, v124, v132\n" \
"buffer_load_dwordx4 v232, s[36:39], s80 offen sc1 lds\n" \
"v_mfma_f32_16x16x32_f16 v[60:63], v[124:127], v[180:183], v[60:63]\n" \
"v_pk_add_f16 v178, v178, v154\n" \
"v_pk_add_f16 v179, v179, v155\n" \
"ds_read_b128 v[168:171], v240 offset:45568\n" \
"v_mfma_f32_16x16x32_f16 v[84:87], v[124:127], v[184:187], v[84:87]\n" \
"v_pk_add_f16 v180, v180, v208\n" \
"v_pk_add_f16 v141, v125, v133\n" \
"v_mfma_f32_16x16x32_f16 v[72:75], v[120:123], v[184:187], v[72:75]\n" \
"v_pk_add_f16 v181, v181, v209\n" \
"v_pk_add_f16 v142, v126, v134\n" \
"v_mfma_f32_16x16x32_f16 v[96:99], v[120:123], v[144:147], v[96:99]\n" \
"v_pk_add_f16 v182, v182, v210\n" \
"v_pk_add_f16 v183, v183, v211\n" \
"ds_read_b64_tr_b16 v[188:189], v238 offset:24576\n" \
"v_mfma_f32_16x16x32_f16 v[108:111], v[124:127], v[144:147], v[108:111]\n" \
"v_pk_add_f16 v184, v184, v156\n" \
"v_pk_add_f16 v143, v127, v135\n" \
"ds_read_b64_tr_b16 v[192:193], v239 offset:24576\n" \
"v_mfma_f32_16x16x32_f16 v[4:7], v[128:131], v[148:151], v[4:7]\n" \
"v_pk_add_f16 v185, v185, v157\n" \
"ds_read_b64_tr_b16 v[120:121], v241 offset:32768\n" \
"buffer_load_dwordx4 v232, s[36:39], s80 offen offset:1024 sc1 lds\n" \
"v_mfma_f32_16x16x32_f16 v[16:19], v[132:135], v[148:151], v[16:19]\n" \
"v_pk_add_f16 v186, v186, v158\n" \
"ds_read_b128 v[164:167], v240 offset:43520\n" \
"v_mfma_f32_16x16x32_f16 v[40:43], v[132:135], v[152:155], v[40:43]\n" \
"v_pk_add_f16 v187, v187, v159\n" \
"ds_read_b128 v[148:151], v240 offset:43008\n" \
"v_mfma_f32_16x16x32_f16 v[28:31], v[128:131], v[152:155], v[28:31]\n" \
"v_pk_add_f16 v144, v144, v160\n" \
"ds_read_b64_tr_b16 v[122:123], v241 offset:40960\n" \
"v_mfma_f32_16x16x32_f16 v[52:55], v[128:131], v[208:211], v[52:55]\n" \
"v_pk_add_f16 v145, v145, v161\n" \
"ds_read_b128 v[152:155], v240 offset:45056\n" \
"s_waitcnt lgkmcnt(6)\n" \
"v_mfma_f32_16x16x16_bf16 v[188:191], v[188:189], v[254:255], 0\n" \
"v_mfma_f32_16x16x32_f16 v[64:67], v[132:135], v[208:211], v[64:67]\n" \
"v_pk_add_f16 v146, v146, v162\n" \
"ds_read_b64_tr_b16 v[124:125], v242 offset:32768\n" \
"s_waitcnt lgkmcnt(6)\n" \
"v_mfma_f32_16x16x16_bf16 v[192:195], v[192:193], v[254:255], 0\n" \
"v_mfma_f32_16x16x32_f16 v[88:91], v[132:135], v[156:159], v[88:91]\n" \
"v_pk_add_f16 v147, v147, v163\n" \
"ds_read_b128 v[208:211], v240 offset:47104\n" \
"buffer_load_dwordx4 v232, s[36:39], s80 offen offset:2048 sc1 lds\n" \
"v_mfma_f32_16x16x32_f16 v[76:79], v[128:131], v[156:159], v[76:79]\n" \
"s_setprio 0\n" \
"ds_read_b64_tr_b16 v[126:127], v242 offset:40960\n" \
"v_mfma_f32_16x16x32_f16 v[100:103], v[128:131], v[160:163], v[100:103]\n" \
"v_cvt_pk_f16_f32 v188, v188, v189\n" \
"ds_read_b128 v[156:159], v240 offset:49152\n" \
"v_mfma_f32_16x16x32_f16 v[112:115], v[132:135], v[160:163], v[112:115]\n" \
"v_cvt_pk_f16_f32 v189, v190, v191\n" \
"ds_read_b64_tr_b16 v[128:129], v241 offset:33280\n" \
"v_mfma_f32_16x16x32_f16 v[8:11], v[136:139], v[172:175], v[8:11]\n" \
"v_cvt_pk_f16_f32 v190, v192, v193\n" \
"ds_read_b128 v[160:163], v240 offset:40960\n" \
"v_mfma_f32_16x16x32_f16 v[20:23], v[140:143], v[172:175], v[20:23]\n" \
"v_cvt_pk_f16_f32 v191, v194, v195\n" \
"v_add_u32 v188, s91, v188\n" \
"ds_read_b128 v[172:175], v240 offset:47616\n" \
"v_mfma_f32_16x16x32_f16 v[44:47], v[140:143], v[176:179], v[44:47]\n" \
"ds_read_b64_tr_b16 v[130:131], v241 offset:41472\n" \
"ds_read_b64_tr_b16 v[132:133], v242 offset:33280\n" \
"buffer_load_dwordx4 v232, s[36:39], s80 offen offset:3072 sc1 lds\n" \
"v_mfma_f32_16x16x32_f16 v[32:35], v[136:139], v[176:179], v[32:35]\n" \
"v_add_u32 v189, s91, v189\n" \
"v_add_u32 v190, s91, v190\n" \
"ds_read_b128 v[176:179], v240 offset:49664\n" \
"v_mfma_f32_16x16x32_f16 v[56:59], v[136:139], v[180:183], v[56:59]\n" \
"v_add_u32 v191, s91, v191\n" \
"ds_read_b64_tr_b16 v[134:135], v242 offset:41472\n" \
"v_mfma_f32_16x16x32_f16 v[68:71], v[140:143], v[180:183], v[68:71]\n" \
"v_and_b32 v188, s92, v188\n" \
"ds_read_b128 v[180:183], v240 offset:41472\n" \
"v_mfma_f32_16x16x32_f16 v[92:95], v[140:143], v[184:187], v[92:95]\n" \
"v_and_b32 v189, s92, v189\n" \
"s_add_i32 m0, s45, 0x5000\n" \
"s_add_i32 s81, s47, 0x4000000\n" \
"v_mfma_f32_16x16x32_f16 v[80:83], v[136:139], v[184:187], v[80:83]\n" \
"v_and_b32 v190, s92, v190\n" \
"v_and_b32 v191, s92, v191\n" \
"buffer_load_dwordx4 v232, s[36:39], s81 offen sc1 lds\n" \
"v_mfma_f32_16x16x32_f16 v[104:107], v[136:139], v[144:147], v[104:107]\n" \
"buffer_store_dwordx4 v[188:191], v234, s[36:39], s82 offen offset:1024 sc1\n" \
"v_mfma_f32_16x16x32_f16 v[116:119], v[140:143], v[144:147], v[116:119]\n" \
"s_setprio 1\n" \
"s_waitcnt vmcnt(7) lgkmcnt(0)\n" \
"s_barrier\n" \
"v_mfma_f32_16x16x32_f16 v[0:3], v[120:123], v[148:151], v[0:3]\n" \
"v_pk_add_f16 v136, v120, v128\n" \
"v_pk_add_f16 v137, v121, v129\n" \
"v_mfma_f32_16x16x32_f16 v[12:15], v[124:127], v[148:151], v[12:15]\n" \
"v_pk_add_f16 v148, v148, v164\n" \
"v_pk_add_f16 v138, v122, v130\n" \
"v_mfma_f32_16x16x32_f16 v[36:39], v[124:127], v[152:155], v[36:39]\n" \
"v_pk_add_f16 v149, v149, v165\n" \
"v_pk_add_f16 v150, v150, v166\n" \
"v_pk_add_f16 v151, v151, v167\n" \
"buffer_store_dwordx4 v[228:231], v235, s[36:39], s68 offen sc1\n" \
"v_mfma_f32_16x16x32_f16 v[24:27], v[120:123], v[152:155], v[24:27]\n" \
"v_pk_add_f16 v152, v152, v168\n" \
"v_pk_add_f16 v139, v123, v131\n" \
"s_add_i32 m0, s44, 0xa000\n" \
"s_add_i32 s80, s46, 0x4400000\n" \
"v_mfma_f32_16x16x32_f16 v[48:51], v[120:123], v[208:211], v[48:51]\n" \
"v_pk_add_f16 v153, v153, v169\n" \
"v_pk_add_f16 v140, v124, v132\n" \
"buffer_load_dwordx4 v232, s[36:39], s80 offen sc1 lds\n" \
"v_mfma_f32_16x16x32_f16 v[60:63], v[124:127], v[208:211], v[60:63]\n" \
"v_pk_add_f16 v154, v154, v170\n" \
"v_pk_add_f16 v155, v155, v171\n" \
"ds_read_b128 v[144:147], v240 offset:4608\n" \
"v_mfma_f32_16x16x32_f16 v[84:87], v[124:127], v[156:159], v[84:87]\n" \
"v_pk_add_f16 v208, v208, v172\n" \
"v_pk_add_f16 v141, v125, v133\n" \
"v_mfma_f32_16x16x32_f16 v[72:75], v[120:123], v[156:159], v[72:75]\n" \
"v_pk_add_f16 v209, v209, v173\n" \
"v_pk_add_f16 v142, v126, v134\n" \
"v_mfma_f32_16x16x32_f16 v[96:99], v[120:123], v[160:163], v[96:99]\n" \
"v_pk_add_f16 v210, v210, v174\n" \
"v_pk_add_f16 v211, v211, v175\n" \
"ds_read_b64_tr_b16 v[188:189], v236 offset:28672\n" \
"v_mfma_f32_16x16x32_f16 v[108:111], v[124:127], v[160:163], v[108:111]\n" \
"v_pk_add_f16 v156, v156, v176\n" \
"v_pk_add_f16 v143, v127, v135\n" \
"ds_read_b64_tr_b16 v[192:193], v237 offset:28672\n" \
"v_mfma_f32_16x16x32_f16 v[4:7], v[128:131], v[164:167], v[4:7]\n" \
"v_pk_add_f16 v157, v157, v177\n" \
"ds_read_b64_tr_b16 v[120:121], v241 offset:0\n" \
"buffer_load_dwordx4 v232, s[36:39], s80 offen offset:1024 sc1 lds\n" \
"v_mfma_f32_16x16x32_f16 v[16:19], v[132:135], v[164:167], v[16:19]\n" \
"v_pk_add_f16 v158, v158, v178\n" \
"ds_read_b128 v[184:187], v240 offset:2560\n" \
"v_mfma_f32_16x16x32_f16 v[40:43], v[132:135], v[168:171], v[40:43]\n" \
"v_pk_add_f16 v159, v159, v179\n" \
"ds_read_b128 v[164:167], v240 offset:2048\n" \
"v_mfma_f32_16x16x32_f16 v[28:31], v[128:131], v[168:171], v[28:31]\n" \
"v_pk_add_f16 v160, v160, v180\n" \
"ds_read_b64_tr_b16 v[122:123], v241 offset:8192\n" \
"v_mfma_f32_16x16x32_f16 v[52:55], v[128:131], v[172:175], v[52:55]\n" \
"v_pk_add_f16 v161, v161, v181\n" \
"ds_read_b128 v[168:171], v240 offset:4096\n" \
"s_waitcnt lgkmcnt(6)\n" \
"v_mfma_f32_16x16x16_bf16 v[188:191], v[188:189], v[254:255], 0\n" \
"v_mfma_f32_16x16x32_f16 v[64:67], v[132:135], v[172:175], v[64:67]\n" \
"v_pk_add_f16 v162, v162, v182\n" \
"ds_read_b64_tr_b16 v[124:125], v242 offset:0\n" \
"s_waitcnt lgkmcnt(6)\n" \
"v_mfma_f32_16x16x16_bf16 v[192:195], v[192:193], v[254:255], 0\n" \
"v_mfma_f32_16x16x32_f16 v[88:91], v[132:135], v[176:179], v[88:91]\n" \
"v_pk_add_f16 v163, v163, v183\n" \
"ds_read_b128 v[172:175], v240 offset:6144\n" \
"buffer_load_dwordx4 v232, s[36:39], s80 offen offset:2048 sc1 lds\n" \
"v_mfma_f32_16x16x32_f16 v[76:79], v[128:131], v[176:179], v[76:79]\n" \
"s_setprio 0\n" \
"ds_read_b64_tr_b16 v[126:127], v242 offset:8192\n" \
"v_mfma_f32_16x16x32_f16 v[100:103], v[128:131], v[180:183], v[100:103]\n" \
"v_cvt_pk_f16_f32 v188, v188, v189\n" \
"ds_read_b128 v[176:179], v240 offset:8192\n" \
"v_mfma_f32_16x16x32_f16 v[112:115], v[132:135], v[180:183], v[112:115]\n" \
"v_cvt_pk_f16_f32 v189, v190, v191\n" \
"ds_read_b64_tr_b16 v[128:129], v241 offset:512\n" \
"v_mfma_f32_16x16x32_f16 v[8:11], v[136:139], v[148:151], v[8:11]\n" \
"v_cvt_pk_f16_f32 v190, v192, v193\n" \
"ds_read_b128 v[180:183], v240 offset:0\n" \
"v_mfma_f32_16x16x32_f16 v[20:23], v[140:143], v[148:151], v[20:23]\n" \
"v_cvt_pk_f16_f32 v191, v194, v195\n" \
"v_add_u32 v188, s91, v188\n" \
"ds_read_b128 v[148:151], v240 offset:6656\n" \
"v_mfma_f32_16x16x32_f16 v[44:47], v[140:143], v[152:155], v[44:47]\n" \
"ds_read_b64_tr_b16 v[130:131], v241 offset:8704\n" \
"ds_read_b64_tr_b16 v[132:133], v242 offset:512\n" \
"buffer_load_dwordx4 v232, s[36:39], s80 offen offset:3072 sc1 lds\n" \
"v_mfma_f32_16x16x32_f16 v[32:35], v[136:139], v[152:155], v[32:35]\n" \
"v_add_u32 v189, s91, v189\n" \
"v_add_u32 v190, s91, v190\n" \
"ds_read_b128 v[152:155], v240 offset:8704\n" \
"v_mfma_f32_16x16x32_f16 v[56:59], v[136:139], v[208:211], v[56:59]\n" \
"v_add_u32 v191, s91, v191\n" \
"ds_read_b64_tr_b16 v[134:135], v242 offset:8704\n" \
"v_mfma_f32_16x16x32_f16 v[68:71], v[140:143], v[208:211], v[68:71]\n" \
"v_and_b32 v188, s92, v188\n" \
"ds_read_b128 v[208:211], v240 offset:512\n" \
"v_mfma_f32_16x16x32_f16 v[92:95], v[140:143], v[156:159], v[92:95]\n" \
"v_and_b32 v189, s92, v189\n" \
"s_add_i32 m0, s45, 0xa000\n" \
"s_add_i32 s81, s47, 0x4400000\n" \
"s_add_i32 s82, s48, 0x6000000\n" \
"v_mfma_f32_16x16x32_f16 v[80:83], v[136:139], v[156:159], v[80:83]\n" \
"v_and_b32 v190, s92, v190\n" \
"v_and_b32 v191, s92, v191\n" \
"buffer_load_dwordx4 v232, s[36:39], s81 offen sc1 lds\n" \
"v_mfma_f32_16x16x32_f16 v[104:107], v[136:139], v[160:163], v[104:107]\n" \
"buffer_store_dwordx4 v[188:191], v234, s[36:39], s82 offen sc1\n" \
"v_mfma_f32_16x16x32_f16 v[116:119], v[140:143], v[160:163], v[116:119]\n" \
"s_setprio 1\n" \
"s_waitcnt vmcnt(7) lgkmcnt(0)\n" \
"s_barrier\n" \
"v_mfma_f32_16x16x32_f16 v[0:3], v[120:123], v[164:167], v[0:3]\n" \
"v_pk_add_f16 v136, v120, v128\n" \
"v_pk_add_f16 v137, v121, v129\n" \
"v_mfma_f32_16x16x32_f16 v[12:15], v[124:127], v[164:167], v[12:15]\n" \
"v_pk_add_f16 v164, v164, v184\n" \
"v_pk_add_f16 v138, v122, v130\n" \
"v_mfma_f32_16x16x32_f16 v[36:39], v[124:127], v[168:171], v[36:39]\n" \
"v_pk_add_f16 v165, v165, v185\n" \
"v_pk_add_f16 v166, v166, v186\n" \
"v_pk_add_f16 v167, v167, v187\n" \
"v_mfma_f32_16x16x32_f16 v[24:27], v[120:123], v[168:171], v[24:27]\n" \
"v_pk_add_f16 v168, v168, v144\n" \
"v_pk_add_f16 v139, v123, v131\n" \
"s_add_i32 m0, s44, 0x0\n" \
"s_add_i32 s80, s46, 0x4800000\n" \
"v_mfma_f32_16x16x32_f16 v[48:51], v[120:123], v[172:175], v[48:51]\n" \
"v_pk_add_f16 v169, v169, v145\n" \
"v_pk_add_f16 v140, v124, v132\n" \
"buffer_load_dwordx4 v232, s[36:39], s80 offen sc1 lds\n" \
"v_mfma_f32_16x16x32_f16 v[60:63], v[124:127], v[172:175], v[60:63]\n" \
"v_pk_add_f16 v170, v170, v146\n" \
"v_pk_add_f16 v171, v171, v147\n" \
"ds_read_b128 v[160:163], v240 offset:25088\n" \
"v_mfma_f32_16x16x32_f16 v[84:87], v[124:127], v[176:179], v[84:87]\n" \
"v_pk_add_f16 v172, v172, v148\n" \
"v_pk_add_f16 v141, v125, v133\n" \
"v_mfma_f32_16x16x32_f16 v[72:75], v[120:123], v[176:179], v[72:75]\n" \
"v_pk_add_f16 v173, v173, v149\n" \
"v_pk_add_f16 v142, v126, v134\n" \
"v_mfma_f32_16x16x32_f16 v[96:99], v[120:123], v[180:183], v[96:99]\n" \
"v_pk_add_f16 v174, v174, v150\n" \
"v_pk_add_f16 v175, v175, v151\n" \
"ds_read_b64_tr_b16 v[188:189], v238 offset:28672\n" \
"v_mfma_f32_16x16x32_f16 v[108:111], v[124:127], v[180:183], v[108:111]\n" \
"v_pk_add_f16 v176, v176, v152\n" \
"v_pk_add_f16 v143, v127, v135\n" \
"ds_read_b64_tr_b16 v[192:193], v239 offset:28672\n" \
"v_mfma_f32_16x16x32_f16 v[4:7], v[128:131], v[184:187], v[4:7]\n" \
"v_pk_add_f16 v177, v177, v153\n" \
"ds_read_b64_tr_b16 v[120:121], v241 offset:16384\n" \
"buffer_load_dwordx4 v232, s[36:39], s80 offen offset:1024 sc1 lds\n" \
"v_mfma_f32_16x16x32_f16 v[16:19], v[132:135], v[184:187], v[16:19]\n" \
"v_pk_add_f16 v178, v178, v154\n" \
"ds_read_b128 v[156:159], v240 offset:23040\n" \
"v_mfma_f32_16x16x32_f16 v[40:43], v[132:135], v[144:147], v[40:43]\n" \
"v_pk_add_f16 v179, v179, v155\n" \
"ds_read_b128 v[184:187], v240 offset:22528\n" \
"v_mfma_f32_16x16x32_f16 v[28:31], v[128:131], v[144:147], v[28:31]\n" \
"v_pk_add_f16 v180, v180, v208\n" \
"ds_read_b64_tr_b16 v[122:123], v241 offset:24576\n" \
"v_mfma_f32_16x16x32_f16 v[52:55], v[128:131], v[148:151], v[52:55]\n" \
"v_pk_add_f16 v181, v181, v209\n" \
"ds_read_b128 v[144:147], v240 offset:24576\n" \
"s_waitcnt lgkmcnt(6)\n" \
"v_mfma_f32_16x16x16_bf16 v[188:191], v[188:189], v[254:255], 0\n" \
"v_mfma_f32_16x16x32_f16 v[64:67], v[132:135], v[148:151], v[64:67]\n" \
"v_pk_add_f16 v182, v182, v210\n" \
"ds_read_b64_tr_b16 v[124:125], v242 offset:16384\n" \
"s_waitcnt lgkmcnt(6)\n" \
"v_mfma_f32_16x16x16_bf16 v[192:195], v[192:193], v[254:255], 0\n" \
"v_mfma_f32_16x16x32_f16 v[88:91], v[132:135], v[152:155], v[88:91]\n" \
"v_pk_add_f16 v183, v183, v211\n" \
"ds_read_b128 v[148:151], v240 offset:26624\n" \
"buffer_load_dwordx4 v232, s[36:39], s80 offen offset:2048 sc1 lds\n" \
"v_mfma_f32_16x16x32_f16 v[76:79], v[128:131], v[152:155], v[76:79]\n" \
"s_setprio 0\n" \
"ds_read_b64_tr_b16 v[126:127], v242 offset:24576\n" \
"v_mfma_f32_16x16x32_f16 v[100:103], v[128:131], v[208:211], v[100:103]\n" \
"v_cvt_pk_f16_f32 v188, v188, v189\n" \
"ds_read_b128 v[152:155], v240 offset:28672\n" \
"v_mfma_f32_16x16x32_f16 v[112:115], v[132:135], v[208:211], v[112:115]\n" \
"v_cvt_pk_f16_f32 v189, v190, v191\n" \
"ds_read_b64_tr_b16 v[128:129], v241 offset:16896\n" \
"v_mfma_f32_16x16x32_f16 v[8:11], v[136:139], v[164:167], v[8:11]\n" \
"v_cvt_pk_f16_f32 v190, v192, v193\n" \
"ds_read_b128 v[208:211], v240 offset:20480\n" \
"v_mfma_f32_16x16x32_f16 v[20:23], v[140:143], v[164:167], v[20:23]\n" \
"v_cvt_pk_f16_f32 v191, v194, v195\n" \
"v_add_u32 v188, s91, v188\n" \
"ds_read_b128 v[164:167], v240 offset:27136\n" \
"v_mfma_f32_16x16x32_f16 v[44:47], v[140:143], v[168:171], v[44:47]\n" \
"ds_read_b64_tr_b16 v[130:131], v241 offset:25088\n" \
"ds_read_b64_tr_b16 v[132:133], v242 offset:16896\n" \
"buffer_load_dwordx4 v232, s[36:39], s80 offen offset:3072 sc1 lds\n" \
"v_mfma_f32_16x16x32_f16 v[32:35], v[136:139], v[168:171], v[32:35]\n" \
"v_add_u32 v189, s91, v189\n" \
"v_add_u32 v190, s91, v190\n" \
"ds_read_b128 v[168:171], v240 offset:29184\n" \
"v_mfma_f32_16x16x32_f16 v[56:59], v[136:139], v[172:175], v[56:59]\n" \
"v_add_u32 v191, s91, v191\n" \
"ds_read_b64_tr_b16 v[134:135], v242 offset:25088\n" \
"v_mfma_f32_16x16x32_f16 v[68:71], v[140:143], v[172:175], v[68:71]\n" \
"v_and_b32 v188, s92, v188\n" \
"ds_read_b128 v[172:175], v240 offset:20992\n" \
"v_mfma_f32_16x16x32_f16 v[92:95], v[140:143], v[176:179], v[92:95]\n" \
"v_and_b32 v189, s92, v189\n" \
"s_add_i32 m0, s45, 0x0\n" \
"s_add_i32 s81, s47, 0x4800000\n" \
"v_mfma_f32_16x16x32_f16 v[80:83], v[136:139], v[176:179], v[80:83]\n" \
"v_and_b32 v190, s92, v190\n" \
"v_and_b32 v191, s92, v191\n" \
"buffer_load_dwordx4 v232, s[36:39], s81 offen sc1 lds\n" \
"v_mfma_f32_16x16x32_f16 v[104:107], v[136:139], v[180:183], v[104:107]\n" \
"buffer_store_dwordx4 v[188:191], v234, s[36:39], s82 offen offset:1024 sc1\n" \
"v_mfma_f32_16x16x32_f16 v[116:119], v[140:143], v[180:183], v[116:119]\n" \
"s_setprio 1\n" \
"s_waitcnt vmcnt(7) lgkmcnt(0)\n" \
"s_barrier\n" \
"v_mfma_f32_16x16x32_f16 v[0:3], v[120:123], v[184:187], v[0:3]\n" \
"v_pk_add_f16 v136, v120, v128\n" \
"v_pk_add_f16 v137, v121, v129\n" \
"s_add_i32 m0, s54, 0x0\n" \
"s_mov_b32 s83, 0x0\n" \
"v_mfma_f32_16x16x32_f16 v[12:15], v[124:127], v[184:187], v[12:15]\n" \
"v_pk_add_f16 v184, v184, v156\n" \
"v_pk_add_f16 v138, v122, v130\n" \
"buffer_load_dwordx4 v233, s[36:39], s83 offen sc1 lds\n" \
"v_mfma_f32_16x16x32_f16 v[36:39], v[124:127], v[144:147], v[36:39]\n" \
"v_pk_add_f16 v185, v185, v157\n" \
"v_pk_add_f16 v186, v186, v158\n" \
"v_pk_add_f16 v187, v187, v159\n" \
"v_mfma_f32_16x16x32_f16 v[24:27], v[120:123], v[144:147], v[24:27]\n" \
"v_pk_add_f16 v144, v144, v160\n" \
"v_pk_add_f16 v139, v123, v131\n" \
"s_add_i32 m0, s44, 0x5000\n" \
"s_add_i32 s80, s46, 0x4c00000\n" \
"v_mfma_f32_16x16x32_f16 v[48:51], v[120:123], v[148:151], v[48:51]\n" \
"v_pk_add_f16 v145, v145, v161\n" \
"v_pk_add_f16 v140, v124, v132\n" \
"buffer_load_dwordx4 v232, s[36:39], s80 offen sc1 lds\n" \
"v_mfma_f32_16x16x32_f16 v[60:63], v[124:127], v[148:151], v[60:63]\n" \
"v_pk_add_f16 v146, v146, v162\n" \
"v_pk_add_f16 v147, v147, v163\n" \
"ds_read_b128 v[180:183], v240 offset:45568\n" \
"v_mfma_f32_16x16x32_f16 v[84:87], v[124:127], v[152:155], v[84:87]\n" \
"v_pk_add_f16 v148, v148, v164\n" \
"v_pk_add_f16 v141, v125, v133\n" \
"v_mfma_f32_16x16x32_f16 v[72:75], v[120:123], v[152:155], v[72:75]\n" \
"v_pk_add_f16 v149, v149, v165\n" \
"v_pk_add_f16 v142, v126, v134\n" \
"v_mfma_f32_16x16x32_f16 v[96:99], v[120:123], v[208:211], v[96:99]\n" \
"v_pk_add_f16 v150, v150, v166\n" \
"v_pk_add_f16 v151, v151, v167\n" \
"ds_read_b64_tr_b16 v[188:189], v236 offset:32768\n" \
"v_mfma_f32_16x16x32_f16 v[108:111], v[124:127], v[208:211], v[108:111]\n" \
"v_pk_add_f16 v152, v152, v168\n" \
"v_pk_add_f16 v143, v127, v135\n" \
"ds_read_b64_tr_b16 v[192:193], v237 offset:32768\n" \
"v_mfma_f32_16x16x32_f16 v[4:7], v[128:131], v[156:159], v[4:7]\n" \
"v_pk_add_f16 v153, v153, v169\n" \
"ds_read_b64_tr_b16 v[120:121], v241 offset:32768\n" \
"buffer_load_dwordx4 v232, s[36:39], s80 offen offset:1024 sc1 lds\n" \
"v_mfma_f32_16x16x32_f16 v[16:19], v[132:135], v[156:159], v[16:19]\n" \
"v_pk_add_f16 v154, v154, v170\n" \
"ds_read_b128 v[176:179], v240 offset:43520\n" \
"v_mfma_f32_16x16x32_f16 v[40:43], v[132:135], v[160:163], v[40:43]\n" \
"v_pk_add_f16 v155, v155, v171\n" \
"ds_read_b128 v[156:159], v240 offset:43008\n" \
"v_mfma_f32_16x16x32_f16 v[28:31], v[128:131], v[160:163], v[28:31]\n" \
"v_pk_add_f16 v208, v208, v172\n" \
"ds_read_b64_tr_b16 v[122:123], v241 offset:40960\n" \
"v_mfma_f32_16x16x32_f16 v[52:55], v[128:131], v[164:167], v[52:55]\n" \
"v_pk_add_f16 v209, v209, v173\n" \
"ds_read_b128 v[160:163], v240 offset:45056\n" \
"s_waitcnt lgkmcnt(6)\n" \
"v_mfma_f32_16x16x16_bf16 v[188:191], v[188:189], v[254:255], 0\n" \
"v_mfma_f32_16x16x32_f16 v[64:67], v[132:135], v[164:167], v[64:67]\n" \
"v_pk_add_f16 v210, v210, v174\n" \
"ds_read_b64_tr_b16 v[124:125], v242 offset:32768\n" \
"s_waitcnt lgkmcnt(6)\n" \
"v_mfma_f32_16x16x16_bf16 v[192:195], v[192:193], v[254:255], 0\n" \
"v_mfma_f32_16x16x32_f16 v[88:91], v[132:135], v[168:171], v[88:91]\n" \
"v_pk_add_f16 v211, v211, v175\n" \
"ds_read_b128 v[164:167], v240 offset:47104\n" \
"buffer_load_dwordx4 v232, s[36:39], s80 offen offset:2048 sc1 lds\n" \
"v_mfma_f32_16x16x32_f16 v[76:79], v[128:131], v[168:171], v[76:79]\n" \
"s_setprio 0\n" \
"ds_read_b64_tr_b16 v[126:127], v242 offset:40960\n" \
"v_mfma_f32_16x16x32_f16 v[100:103], v[128:131], v[172:175], v[100:103]\n" \
"v_cvt_pk_f16_f32 v188, v188, v189\n" \
"ds_read_b128 v[168:171], v240 offset:49152\n" \
"v_mfma_f32_16x16x32_f16 v[112:115], v[132:135], v[172:175], v[112:115]\n" \
"v_cvt_pk_f16_f32 v189, v190, v191\n" \
"ds_read_b64_tr_b16 v[128:129], v241 offset:33280\n" \
"v_mfma_f32_16x16x32_f16 v[8:11], v[136:139], v[184:187], v[8:11]\n" \
"v_cvt_pk_f16_f32 v190, v192, v193\n" \
"ds_read_b128 v[172:175], v240 offset:40960\n" \
"v_mfma_f32_16x16x32_f16 v[20:23], v[140:143], v[184:187], v[20:23]\n" \
"v_cvt_pk_f16_f32 v191, v194, v195\n" \
"v_add_u32 v188, s91, v188\n" \
"ds_read_b128 v[184:187], v240 offset:47616\n" \
"v_mfma_f32_16x16x32_f16 v[44:47], v[140:143], v[144:147], v[44:47]\n" \
"ds_read_b64_tr_b16 v[130:131], v241 offset:41472\n" \
"ds_read_b64_tr_b16 v[132:133], v242 offset:33280\n" \
"buffer_load_dwordx4 v232, s[36:39], s80 offen offset:3072 sc1 lds\n" \
"v_mfma_f32_16x16x32_f16 v[32:35], v[136:139], v[144:147], v[32:35]\n" \
"v_add_u32 v189, s91, v189\n" \
"v_add_u32 v190, s91, v190\n" \
"ds_read_b128 v[144:147], v240 offset:49664\n" \
"v_mfma_f32_16x16x32_f16 v[56:59], v[136:139], v[148:151], v[56:59]\n" \
"v_add_u32 v191, s91, v191\n" \
"ds_read_b64_tr_b16 v[134:135], v242 offset:41472\n" \
"v_mfma_f32_16x16x32_f16 v[68:71], v[140:143], v[148:151], v[68:71]\n" \
"v_and_b32 v188, s92, v188\n" \
"ds_read_b128 v[148:151], v240 offset:41472\n" \
"v_mfma_f32_16x16x32_f16 v[92:95], v[140:143], v[152:155], v[92:95]\n" \
"v_and_b32 v189, s92, v189\n" \
"s_add_i32 m0, s45, 0x5000\n" \
"s_add_i32 s81, s47, 0x4c00000\n" \
"s_add_i32 s82, s49, 0x6800000\n" \
"v_mfma_f32_16x16x32_f16 v[80:83], v[136:139], v[152:155], v[80:83]\n" \
"v_and_b32 v190, s92, v190\n" \
"v_and_b32 v191, s92, v191\n" \
"buffer_load_dwordx4 v232, s[36:39], s81 offen sc1 lds\n" \
"v_mfma_f32_16x16x32_f16 v[104:107], v[136:139], v[208:211], v[104:107]\n" \
"buffer_store_dwordx4 v[188:191], v234, s[36:39], s82 offen sc1\n" \
"v_mfma_f32_16x16x32_f16 v[116:119], v[140:143], v[208:211], v[116:119]\n" \
"s_setprio 1\n" \
"s_waitcnt vmcnt(7) lgkmcnt(0)\n" \
"s_barrier\n" \
"v_mfma_f32_16x16x32_f16 v[0:3], v[120:123], v[156:159], v[0:3]\n" \
"v_pk_add_f16 v136, v120, v128\n" \
"v_pk_add_f16 v137, v121, v129\n" \
"v_mfma_f32_16x16x32_f16 v[12:15], v[124:127], v[156:159], v[12:15]\n" \
"v_pk_add_f16 v156, v156, v176\n" \
"v_pk_add_f16 v138, v122, v130\n" \
"v_mfma_f32_16x16x32_f16 v[36:39], v[124:127], v[160:163], v[36:39]\n" \
"v_pk_add_f16 v157, v157, v177\n" \
"v_pk_add_f16 v158, v158, v178\n" \
"v_pk_add_f16 v159, v159, v179\n" \
"v_mfma_f32_16x16x32_f16 v[24:27], v[120:123], v[160:163], v[24:27]\n" \
"v_pk_add_f16 v160, v160, v180\n" \
"v_pk_add_f16 v139, v123, v131\n" \
"s_add_i32 m0, s44, 0xa000\n" \
"s_add_i32 s80, s46, 0x5000000\n" \
"v_mfma_f32_16x16x32_f16 v[48:51], v[120:123], v[164:167], v[48:51]\n" \
"v_pk_add_f16 v161, v161, v181\n" \
"v_pk_add_f16 v140, v124, v132\n" \
"buffer_load_dwordx4 v232, s[36:39], s80 offen sc1 lds\n" \
"v_mfma_f32_16x16x32_f16 v[60:63], v[124:127], v[164:167], v[60:63]\n" \
"v_pk_add_f16 v162, v162, v182\n" \
"v_pk_add_f16 v163, v163, v183\n" \
"ds_read_b128 v[208:211], v240 offset:4608\n" \
"v_mfma_f32_16x16x32_f16 v[84:87], v[124:127], v[168:171], v[84:87]\n" \
"v_pk_add_f16 v164, v164, v184\n" \
"v_pk_add_f16 v141, v125, v133\n" \
"v_mfma_f32_16x16x32_f16 v[72:75], v[120:123], v[168:171], v[72:75]\n" \
"v_pk_add_f16 v165, v165, v185\n" \
"v_pk_add_f16 v142, v126, v134\n" \
"v_mfma_f32_16x16x32_f16 v[96:99], v[120:123], v[172:175], v[96:99]\n" \
"v_pk_add_f16 v166, v166, v186\n" \
"v_pk_add_f16 v167, v167, v187\n" \
"ds_read_b64_tr_b16 v[188:189], v238 offset:32768\n" \
"v_mfma_f32_16x16x32_f16 v[108:111], v[124:127], v[172:175], v[108:111]\n" \
"v_pk_add_f16 v168, v168, v144\n" \
"v_pk_add_f16 v143, v127, v135\n" \
"ds_read_b64_tr_b16 v[192:193], v239 offset:32768\n" \
"v_mfma_f32_16x16x32_f16 v[4:7], v[128:131], v[176:179], v[4:7]\n" \
"v_pk_add_f16 v169, v169, v145\n" \
"ds_read_b64_tr_b16 v[120:121], v241 offset:0\n" \
"buffer_load_dwordx4 v232, s[36:39], s80 offen offset:1024 sc1 lds\n" \
"v_mfma_f32_16x16x32_f16 v[16:19], v[132:135], v[176:179], v[16:19]\n" \
"v_pk_add_f16 v170, v170, v146\n" \
"ds_read_b128 v[152:155], v240 offset:2560\n" \
"v_mfma_f32_16x16x32_f16 v[40:43], v[132:135], v[180:183], v[40:43]\n" \
"v_pk_add_f16 v171, v171, v147\n" \
"ds_read_b128 v[176:179], v240 offset:2048\n" \
"v_mfma_f32_16x16x32_f16 v[28:31], v[128:131], v[180:183], v[28:31]\n" \
"v_pk_add_f16 v172, v172, v148\n" \
"ds_read_b64_tr_b16 v[122:123], v241 offset:8192\n" \
"v_mfma_f32_16x16x32_f16 v[52:55], v[128:131], v[184:187], v[52:55]\n" \
"v_pk_add_f16 v173, v173, v149\n" \
"ds_read_b128 v[180:183], v240 offset:4096\n" \
"s_waitcnt lgkmcnt(6)\n" \
"v_mfma_f32_16x16x16_bf16 v[188:191], v[188:189], v[254:255], 0\n" \
"v_mfma_f32_16x16x32_f16 v[64:67], v[132:135], v[184:187], v[64:67]\n" \
"v_pk_add_f16 v174, v174, v150\n" \
"ds_read_b64_tr_b16 v[124:125], v242 offset:0\n" \
"s_waitcnt lgkmcnt(6)\n" \
"v_mfma_f32_16x16x16_bf16 v[192:195], v[192:193], v[254:255], 0\n" \
"v_mfma_f32_16x16x32_f16 v[88:91], v[132:135], v[144:147], v[88:91]\n" \
"v_pk_add_f16 v175, v175, v151\n" \
"ds_read_b128 v[184:187], v240 offset:6144\n" \
"buffer_load_dwordx4 v232, s[36:39], s80 offen offset:2048 sc1 lds\n" \
"v_mfma_f32_16x16x32_f16 v[76:79], v[128:131], v[144:147], v[76:79]\n" \
"s_setprio 0\n" \
"ds_read_b64_tr_b16 v[126:127], v242 offset:8192\n" \
"v_mfma_f32_16x16x32_f16 v[100:103], v[128:131], v[148:151], v[100:103]\n" \
"v_cvt_pk_f16_f32 v188, v188, v189\n" \
"ds_read_b128 v[144:147], v240 offset:8192\n" \
"v_mfma_f32_16x16x32_f16 v[112:115], v[132:135], v[148:151], v[112:115]\n" \
"v_cvt_pk_f16_f32 v189, v190, v191\n" \
"ds_read_b64_tr_b16 v[128:129], v241 offset:512\n" \
"v_mfma_f32_16x16x32_f16 v[8:11], v[136:139], v[156:159], v[8:11]\n" \
"v_cvt_pk_f16_f32 v190, v192, v193\n" \
"ds_read_b128 v[148:151], v240 offset:0\n" \
"v_mfma_f32_16x16x32_f16 v[20:23], v[140:143], v[156:159], v[20:23]\n" \
"v_cvt_pk_f16_f32 v191, v194, v195\n" \
"v_add_u32 v188, s91, v188\n" \
"ds_read_b128 v[156:159], v240 offset:6656\n" \
"v_mfma_f32_16x16x32_f16 v[44:47], v[140:143], v[160:163], v[44:47]\n" \
"ds_read_b64_tr_b16 v[130:131], v241 offset:8704\n" \
"ds_read_b64_tr_b16 v[132:133], v242 offset:512\n" \
"buffer_load_dwordx4 v232, s[36:39], s80 offen offset:3072 sc1 lds\n" \
"v_mfma_f32_16x16x32_f16 v[32:35], v[136:139], v[160:163], v[32:35]\n" \
"v_add_u32 v189, s91, v189\n" \
"v_add_u32 v190, s91, v190\n" \
"ds_read_b128 v[160:163], v240 offset:8704\n" \
"v_mfma_f32_16x16x32_f16 v[56:59], v[136:139], v[164:167], v[56:59]\n" \
"v_add_u32 v191, s91, v191\n" \
"ds_read_b64_tr_b16 v[134:135], v242 offset:8704\n" \
"v_mfma_f32_16x16x32_f16 v[68:71], v[140:143], v[164:167], v[68:71]\n" \
"v_and_b32 v188, s92, v188\n" \
"ds_read_b128 v[164:167], v240 offset:512\n" \
"v_mfma_f32_16x16x32_f16 v[92:95], v[140:143], v[168:171], v[92:95]\n" \
"v_and_b32 v189, s92, v189\n" \
"s_add_i32 m0, s45, 0xa000\n" \
"s_add_i32 s81, s47, 0x5000000\n" \
"v_mfma_f32_16x16x32_f16 v[80:83], v[136:139], v[168:171], v[80:83]\n" \
"v_and_b32 v190, s92, v190\n" \
"v_and_b32 v191, s92, v191\n" \
"buffer_load_dwordx4 v232, s[36:39], s81 offen sc1 lds\n" \
"v_mfma_f32_16x16x32_f16 v[104:107], v[136:139], v[172:175], v[104:107]\n" \
"buffer_store_dwordx4 v[188:191], v234, s[36:39], s82 offen offset:1024 sc1\n" \
"v_mfma_f32_16x16x32_f16 v[116:119], v[140:143], v[172:175], v[116:119]\n" \
"s_setprio 1\n" \
"s_waitcnt vmcnt(7) lgkmcnt(0)\n" \
"s_barrier\n" \
"v_mfma_f32_16x16x32_f16 v[0:3], v[120:123], v[176:179], v[0:3]\n" \
"v_pk_add_f16 v136, v120, v128\n" \
"v_pk_add_f16 v137, v121, v129\n" \
"s_add_i32 m0, s54, 0x1000\n" \
"s_mov_b32 s83, 0x100\n" \
"v_mfma_f32_16x16x32_f16 v[12:15], v[124:127], v[176:179], v[12:15]\n" \
"v_pk_add_f16 v176, v176, v152\n" \
"v_pk_add_f16 v138, v122, v130\n" \
"buffer_load_dwordx4 v233, s[36:39], s83 offen sc1 lds\n" \
"v_mfma_f32_16x16x32_f16 v[36:39], v[124:127], v[180:183], v[36:39]\n" \
"v_pk_add_f16 v177, v177, v153\n" \
"v_pk_add_f16 v178, v178, v154\n" \
"v_pk_add_f16 v179, v179, v155\n" \
"v_mfma_f32_16x16x32_f16 v[24:27], v[120:123], v[180:183], v[24:27]\n" \
"v_pk_add_f16 v180, v180, v208\n" \
"v_pk_add_f16 v139, v123, v131\n" \
"s_add_i32 m0, s44, 0x0\n" \
"s_add_i32 s80, s46, 0x5400000\n" \
"v_mfma_f32_16x16x32_f16 v[48:51], v[120:123], v[184:187], v[48:51]\n" \
"v_pk_add_f16 v181, v181, v209\n" \
"v_pk_add_f16 v140, v124, v132\n" \
"buffer_load_dwordx4 v232, s[36:39], s80 offen sc1 lds\n" \
"v_mfma_f32_16x16x32_f16 v[60:63], v[124:127], v[184:187], v[60:63]\n" \
"v_pk_add_f16 v182, v182, v210\n" \
"v_pk_add_f16 v183, v183, v211\n" \
"ds_read_b128 v[172:175], v240 offset:25088\n" \
"v_mfma_f32_16x16x32_f16 v[84:87], v[124:127], v[144:147], v[84:87]\n" \
"v_pk_add_f16 v184, v184, v156\n" \
"v_pk_add_f16 v141, v125, v133\n" \
"v_mfma_f32_16x16x32_f16 v[72:75], v[120:123], v[144:147], v[72:75]\n" \
"v_pk_add_f16 v185, v185, v157\n" \
"v_pk_add_f16 v142, v126, v134\n" \
"v_mfma_f32_16x16x32_f16 v[96:99], v[120:123], v[148:151], v[96:99]\n" \
"v_pk_add_f16 v186, v186, v158\n" \
"v_pk_add_f16 v187, v187, v159\n" \
"ds_read_b64_tr_b16 v[188:189], v236 offset:36864\n" \
"v_mfma_f32_16x16x32_f16 v[108:111], v[124:127], v[148:151], v[108:111]\n" \
"v_pk_add_f16 v144, v144, v160\n" \
"v_pk_add_f16 v143, v127, v135\n" \
"ds_read_b64_tr_b16 v[192:193], v237 offset:36864\n" \
"v_mfma_f32_16x16x32_f16 v[4:7], v[128:131], v[152:155], v[4:7]\n" \
"v_pk_add_f16 v145, v145, v161\n" \
"ds_read_b64_tr_b16 v[120:121], v241 offset:16384\n" \
"buffer_load_dwordx4 v232, s[36:39], s80 offen offset:1024 sc1 lds\n" \
"v_mfma_f32_16x16x32_f16 v[16:19], v[132:135], v[152:155], v[16:19]\n" \
"v_pk_add_f16 v146, v146, v162\n" \
"ds_read_b128 v[168:171], v240 offset:23040\n" \
"v_mfma_f32_16x16x32_f16 v[40:43], v[132:135], v[208:211], v[40:43]\n" \
"v_pk_add_f16 v147, v147, v163\n" \
"ds_read_b128 v[152:155], v240 offset:22528\n" \
"v_mfma_f32_16x16x32_f16 v[28:31], v[128:131], v[208:211], v[28:31]\n" \
"v_pk_add_f16 v148, v148, v164\n" \
"ds_read_b64_tr_b16 v[122:123], v241 offset:24576\n" \
"v_mfma_f32_16x16x32_f16 v[52:55], v[128:131], v[156:159], v[52:55]\n" \
"v_pk_add_f16 v149, v149, v165\n" \
"ds_read_b128 v[208:211], v240 offset:24576\n" \
"s_waitcnt lgkmcnt(6)\n" \
"v_mfma_f32_16x16x16_bf16 v[188:191], v[188:189], v[254:255], 0\n" \
"v_mfma_f32_16x16x32_f16 v[64:67], v[132:135], v[156:159], v[64:67]\n" \
"v_pk_add_f16 v150, v150, v166\n" \
"ds_read_b64_tr_b16 v[124:125], v242 offset:16384\n" \
"s_waitcnt lgkmcnt(6)\n" \
"v_mfma_f32_16x16x16_bf16 v[192:195], v[192:193], v[254:255], 0\n" \
"v_mfma_f32_16x16x32_f16 v[88:91], v[132:135], v[160:163], v[88:91]\n" \
"v_pk_add_f16 v151, v151, v167\n" \
"ds_read_b128 v[156:159], v240 offset:26624\n" \
"buffer_load_dwordx4 v232, s[36:39], s80 offen offset:2048 sc1 lds\n" \
"v_mfma_f32_16x16x32_f16 v[76:79], v[128:131], v[160:163], v[76:79]\n" \
"s_setprio 0\n" \
"ds_read_b64_tr_b16 v[126:127], v242 offset:24576\n" \
"v_mfma_f32_16x16x32_f16 v[100:103], v[128:131], v[164:167], v[100:103]\n" \
"v_cvt_pk_f16_f32 v188, v188, v189\n" \
"ds_read_b128 v[160:163], v240 offset:28672\n" \
"v_mfma_f32_16x16x32_f16 v[112:115], v[132:135], v[164:167], v[112:115]\n" \
"v_cvt_pk_f16_f32 v189, v190, v191\n" \
"ds_read_b64_tr_b16 v[128:129], v241 offset:16896\n" \
"v_mfma_f32_16x16x32_f16 v[8:11], v[136:139], v[176:179], v[8:11]\n" \
"v_cvt_pk_f16_f32 v190, v192, v193\n" \
"ds_read_b128 v[164:167], v240 offset:20480\n" \
"v_mfma_f32_16x16x32_f16 v[20:23], v[140:143], v[176:179], v[20:23]\n" \
"v_cvt_pk_f16_f32 v191, v194, v195\n" \
"v_add_u32 v188, s91, v188\n" \
"ds_read_b128 v[176:179], v240 offset:27136\n" \
"v_mfma_f32_16x16x32_f16 v[44:47], v[140:143], v[180:183], v[44:47]\n" \
"ds_read_b64_tr_b16 v[130:131], v241 offset:25088\n" \
"ds_read_b64_tr_b16 v[132:133], v242 offset:16896\n" \
"buffer_load_dwordx4 v232, s[36:39], s80 offen offset:3072 sc1 lds\n" \
"v_mfma_f32_16x16x32_f16 v[32:35], v[136:139], v[180:183], v[32:35]\n" \
"v_add_u32 v189, s91, v189\n" \
"v_add_u32 v190, s91, v190\n" \
"ds_read_b128 v[180:183], v240 offset:29184\n" \
"v_mfma_f32_16x16x32_f16 v[56:59], v[136:139], v[184:187], v[56:59]\n" \
"v_add_u32 v191, s91, v191\n" \
"ds_read_b64_tr_b16 v[134:135], v242 offset:25088\n" \
"v_mfma_f32_16x16x32_f16 v[68:71], v[140:143], v[184:187], v[68:71]\n" \
"v_and_b32 v188, s92, v188\n" \
"ds_read_b128 v[184:187], v240 offset:20992\n" \
"v_mfma_f32_16x16x32_f16 v[92:95], v[140:143], v[144:147], v[92:95]\n" \
"v_and_b32 v189, s92, v189\n" \
"s_add_i32 m0, s45, 0x0\n" \
"s_add_i32 s81, s47, 0x5400000\n" \
"s_add_i32 s82, s50, 0x7000000\n" \
"v_mfma_f32_16x16x32_f16 v[80:83], v[136:139], v[144:147], v[80:83]\n" \
"v_and_b32 v190, s92, v190\n" \
"v_and_b32 v191, s92, v191\n" \
"buffer_load_dwordx4 v232, s[36:39], s81 offen sc1 lds\n" \
"v_mfma_f32_16x16x32_f16 v[104:107], v[136:139], v[148:151], v[104:107]\n" \
"buffer_store_dwordx4 v[188:191], v234, s[36:39], s82 offen sc1\n" \
"v_mfma_f32_16x16x32_f16 v[116:119], v[140:143], v[148:151], v[116:119]\n" \
"s_setprio 1\n" \
"s_waitcnt vmcnt(7) lgkmcnt(0)\n" \
"s_barrier\n" \
"v_mfma_f32_16x16x32_f16 v[0:3], v[120:123], v[152:155], v[0:3]\n" \
"v_pk_add_f16 v136, v120, v128\n" \
"v_pk_add_f16 v137, v121, v129\n" \
"v_mfma_f32_16x16x32_f16 v[12:15], v[124:127], v[152:155], v[12:15]\n" \
"v_pk_add_f16 v152, v152, v168\n" \
"v_pk_add_f16 v138, v122, v130\n" \
"v_mfma_f32_16x16x32_f16 v[36:39], v[124:127], v[208:211], v[36:39]\n" \
"v_pk_add_f16 v153, v153, v169\n" \
"v_pk_add_f16 v154, v154, v170\n" \
"v_pk_add_f16 v155, v155, v171\n" \
"v_mfma_f32_16x16x32_f16 v[24:27], v[120:123], v[208:211], v[24:27]\n" \
"v_pk_add_f16 v208, v208, v172\n" \
"v_pk_add_f16 v139, v123, v131\n" \
"s_add_i32 m0, s44, 0x5000\n" \
"s_add_i32 s80, s46, 0x5800000\n" \
"v_mfma_f32_16x16x32_f16 v[48:51], v[120:123], v[156:159], v[48:51]\n" \
"v_pk_add_f16 v209, v209, v173\n" \
"v_pk_add_f16 v140, v124, v132\n" \
"buffer_load_dwordx4 v232, s[36:39], s80 offen sc1 lds\n" \
"v_mfma_f32_16x16x32_f16 v[60:63], v[124:127], v[156:159], v[60:63]\n" \
"v_pk_add_f16 v210, v210, v174\n" \
"v_pk_add_f16 v211, v211, v175\n" \
"ds_read_b128 v[148:151], v240 offset:45568\n" \
"v_mfma_f32_16x16x32_f16 v[84:87], v[124:127], v[160:163], v[84:87]\n" \
"v_pk_add_f16 v156, v156, v176\n" \
"v_pk_add_f16 v141, v125, v133\n" \
"v_mfma_f32_16x16x32_f16 v[72:75], v[120:123], v[160:163], v[72:75]\n" \
"v_pk_add_f16 v157, v157, v177\n" \
"v_pk_add_f16 v142, v126, v134\n" \
"v_mfma_f32_16x16x32_f16 v[96:99], v[120:123], v[164:167], v[96:99]\n" \
"v_pk_add_f16 v158, v158, v178\n" \
"v_pk_add_f16 v159, v159, v179\n" \
"ds_read_b64_tr_b16 v[188:189], v238 offset:36864\n" \
"v_mfma_f32_16x16x32_f16 v[108:111], v[124:127], v[164:167], v[108:111]\n" \
"v_pk_add_f16 v160, v160, v180\n" \
"v_pk_add_f16 v143, v127, v135\n" \
"ds_read_b64_tr_b16 v[192:193], v239 offset:36864\n" \
"v_mfma_f32_16x16x32_f16 v[4:7], v[128:131], v[168:171], v[4:7]\n" \
"v_pk_add_f16 v161, v161, v181\n" \
"ds_read_b64_tr_b16 v[120:121], v241 offset:32768\n" \
"buffer_load_dwordx4 v232, s[36:39], s80 offen offset:1024 sc1 lds\n" \
"v_mfma_f32_16x16x32_f16 v[16:19], v[132:135], v[168:171], v[16:19]\n" \
"v_pk_add_f16 v162, v162, v182\n" \
"ds_read_b128 v[144:147], v240 offset:43520\n" \
"v_mfma_f32_16x16x32_f16 v[40:43], v[132:135], v[172:175], v[40:43]\n" \
"v_pk_add_f16 v163, v163, v183\n" \
"ds_read_b128 v[168:171], v240 offset:43008\n" \
"v_mfma_f32_16x16x32_f16 v[28:31], v[128:131], v[172:175], v[28:31]\n" \
"v_pk_add_f16 v164, v164, v184\n" \
"ds_read_b64_tr_b16 v[122:123], v241 offset:40960\n" \
"v_mfma_f32_16x16x32_f16 v[52:55], v[128:131], v[176:179], v[52:55]\n" \
"v_pk_add_f16 v165, v165, v185\n" \
"ds_read_b128 v[172:175], v240 offset:45056\n" \
"s_waitcnt lgkmcnt(6)\n" \
"v_mfma_f32_16x16x16_bf16 v[188:191], v[188:189], v[254:255], 0\n" \
"v_mfma_f32_16x16x32_f16 v[64:67], v[132:135], v[176:179], v[64:67]\n" \
"v_pk_add_f16 v166, v166, v186\n" \
"ds_read_b64_tr_b16 v[124:125], v242 offset:32768\n" \
"s_waitcnt lgkmcnt(6)\n" \
"v_mfma_f32_16x16x16_bf16 v[192:195], v[192:193], v[254:255], 0\n" \
"v_mfma_f32_16x16x32_f16 v[88:91], v[132:135], v[180:183], v[88:91]\n" \
"v_pk_add_f16 v167, v167, v187\n" \
"ds_read_b128 v[176:179], v240 offset:47104\n" \
"buffer_load_dwordx4 v232, s[36:39], s80 offen offset:2048 sc1 lds\n" \
"v_mfma_f32_16x16x32_f16 v[76:79], v[128:131], v[180:183], v[76:79]\n" \
"s_setprio 0\n" \
"ds_read_b64_tr_b16 v[126:127], v242 offset:40960\n" \
"v_mfma_f32_16x16x32_f16 v[100:103], v[128:131], v[184:187], v[100:103]\n" \
"v_cvt_pk_f16_f32 v188, v188, v189\n" \
"ds_read_b128 v[180:183], v240 offset:49152\n" \
"v_mfma_f32_16x16x32_f16 v[112:115], v[132:135], v[184:187], v[112:115]\n" \
"v_cvt_pk_f16_f32 v189, v190, v191\n" \
"ds_read_b64_tr_b16 v[128:129], v241 offset:33280\n" \
"v_mfma_f32_16x16x32_f16 v[8:11], v[136:139], v[152:155], v[8:11]\n" \
"v_cvt_pk_f16_f32 v190, v192, v193\n" \
"ds_read_b128 v[184:187], v240 offset:40960\n" \
"v_mfma_f32_16x16x32_f16 v[20:23], v[140:143], v[152:155], v[20:23]\n" \
"v_cvt_pk_f16_f32 v191, v194, v195\n" \
"v_add_u32 v188, s91, v188\n" \
"ds_read_b128 v[152:155], v240 offset:47616\n" \
"v_mfma_f32_16x16x32_f16 v[44:47], v[140:143], v[208:211], v[44:47]\n" \
"ds_read_b64_tr_b16 v[130:131], v241 offset:41472\n" \
"ds_read_b64_tr_b16 v[132:133], v242 offset:33280\n" \
"buffer_load_dwordx4 v232, s[36:39], s80 offen offset:3072 sc1 lds\n" \
"v_mfma_f32_16x16x32_f16 v[32:35], v[136:139], v[208:211], v[32:35]\n" \
"v_add_u32 v189, s91, v189\n" \
"v_add_u32 v190, s91, v190\n" \
"ds_read_b128 v[208:211], v240 offset:49664\n" \
"v_mfma_f32_16x16x32_f16 v[56:59], v[136:139], v[156:159], v[56:59]\n" \
"v_add_u32 v191, s91, v191\n" \
"ds_read_b64_tr_b16 v[134:135], v242 offset:41472\n" \
"v_mfma_f32_16x16x32_f16 v[68:71], v[140:143], v[156:159], v[68:71]\n" \
"v_and_b32 v188, s92, v188\n" \
"ds_read_b128 v[156:159], v240 offset:41472\n" \
"v_mfma_f32_16x16x32_f16 v[92:95], v[140:143], v[160:163], v[92:95]\n" \
"v_and_b32 v189, s92, v189\n" \
"s_add_i32 m0, s45, 0x5000\n" \
"s_add_i32 s81, s47, 0x5800000\n" \
"v_mfma_f32_16x16x32_f16 v[80:83], v[136:139], v[160:163], v[80:83]\n" \
"v_and_b32 v190, s92, v190\n" \
"v_and_b32 v191, s92, v191\n" \
"buffer_load_dwordx4 v232, s[36:39], s81 offen sc1 lds\n" \
"v_mfma_f32_16x16x32_f16 v[104:107], v[136:139], v[164:167], v[104:107]\n" \
"buffer_store_dwordx4 v[188:191], v234, s[36:39], s82 offen offset:1024 sc1\n" \
"v_mfma_f32_16x16x32_f16 v[116:119], v[140:143], v[164:167], v[116:119]\n" \
"s_setprio 1\n" \
"s_waitcnt vmcnt(7) lgkmcnt(0)\n" \
"s_barrier\n" \
"v_mfma_f32_16x16x32_f16 v[0:3], v[120:123], v[168:171], v[0:3]\n" \
"v_pk_add_f16 v136, v120, v128\n" \
"v_pk_add_f16 v137, v121, v129\n" \
"ds_read_b64_tr_b16 v[188:189], v236 offset:40960\n" \
"s_add_i32 m0, s54, 0x0\n" \
"s_mov_b32 s83, 0x200\n" \
"v_mfma_f32_16x16x32_f16 v[12:15], v[124:127], v[168:171], v[12:15]\n" \
"v_pk_add_f16 v168, v168, v144\n" \
"v_pk_add_f16 v138, v122, v130\n" \
"s_mov_b32 s84, 0x8014000\n" \
"ds_read_b64_tr_b16 v[192:193], v237 offset:40960\n" \
"buffer_load_dwordx4 v233, s[36:39], s83 offen sc1 lds\n" \
"v_mfma_f32_16x16x32_f16 v[36:39], v[124:127], v[172:175], v[36:39]\n" \
"v_pk_add_f16 v169, v169, v145\n" \
"v_pk_add_f16 v170, v170, v146\n" \
"v_pk_add_f16 v171, v171, v147\n" \
"buffer_load_dword v253, v252, s[36:39], s84 offen sc1\n" \
"v_mfma_f32_16x16x32_f16 v[24:27], v[120:123], v[172:175], v[24:27]\n" \
"v_pk_add_f16 v172, v172, v148\n" \
"v_pk_add_f16 v139, v123, v131\n" \
"s_add_i32 m0, s44, 0xa000\n" \
"s_add_i32 s80, s46, 0x5c00000\n" \
"v_mfma_f32_16x16x32_f16 v[48:51], v[120:123], v[176:179], v[48:51]\n" \
"v_pk_add_f16 v173, v173, v149\n" \
"v_pk_add_f16 v140, v124, v132\n" \
"buffer_load_dwordx4 v232, s[36:39], s80 offen sc1 lds\n" \
"v_mfma_f32_16x16x32_f16 v[60:63], v[124:127], v[176:179], v[60:63]\n" \
"v_pk_add_f16 v174, v174, v150\n" \
"v_pk_add_f16 v175, v175, v151\n" \
"ds_read_b128 v[164:167], v240 offset:4608\n" \
"s_waitcnt lgkmcnt(2)\n" \
"v_mfma_f32_16x16x16_bf16 v[188:191], v[188:189], v[254:255], 0\n" \
"v_mfma_f32_16x16x32_f16 v[84:87], v[124:127], v[180:183], v[84:87]\n" \
"v_pk_add_f16 v176, v176, v152\n" \
"v_pk_add_f16 v141, v125, v133\n" \
"s_waitcnt lgkmcnt(1)\n" \
"v_mfma_f32_16x16x16_bf16 v[192:195], v[192:193], v[254:255], 0\n" \
"v_mfma_f32_16x16x32_f16 v[72:75], v[120:123], v[180:183], v[72:75]\n" \
"v_pk_add_f16 v177, v177, v153\n" \
"v_pk_add_f16 v142, v126, v134\n" \
"v_mfma_f32_16x16x32_f16 v[96:99], v[120:123], v[184:187], v[96:99]\n" \
"v_pk_add_f16 v178, v178, v154\n" \
"v_pk_add_f16 v179, v179, v155\n" \
"v_mfma_f32_16x16x32_f16 v[108:111], v[124:127], v[184:187], v[108:111]\n" \
"v_pk_add_f16 v180, v180, v208\n" \
"v_pk_add_f16 v143, v127, v135\n" \
"v_cvt_pk_f16_f32 v188, v188, v189\n" \
"v_cvt_pk_f16_f32 v189, v190, v191\n" \
"v_mfma_f32_16x16x32_f16 v[4:7], v[128:131], v[144:147], v[4:7]\n" \
"v_pk_add_f16 v181, v181, v209\n" \
"v_cvt_pk_f16_f32 v190, v192, v193\n" \
"ds_read_b64_tr_b16 v[120:121], v241 offset:0\n" \
"buffer_load_dwordx4 v232, s[36:39], s80 offen offset:1024 sc1 lds\n" \
"v_mfma_f32_16x16x32_f16 v[16:19], v[132:135], v[144:147], v[16:19]\n" \
"v_pk_add_f16 v182, v182, v210\n" \
"v_cvt_pk_f16_f32 v191, v194, v195\n" \
"v_add_u32 v188, s91, v188\n" \
"ds_read_b128 v[160:163], v240 offset:2560\n" \
"v_mfma_f32_16x16x32_f16 v[40:43], v[132:135], v[148:151], v[40:43]\n" \
"v_pk_add_f16 v183, v183, v211\n" \
"v_add_u32 v189, s91, v189\n" \
"v_add_u32 v190, s91, v190\n" \
"ds_read_b128 v[144:147], v240 offset:2048\n" \
"v_mfma_f32_16x16x32_f16 v[28:31], v[128:131], v[148:151], v[28:31]\n" \
"v_pk_add_f16 v184, v184, v156\n" \
"v_add_u32 v191, s91, v191\n" \
"v_and_b32 v188, s92, v188\n" \
"ds_read_b64_tr_b16 v[122:123], v241 offset:8192\n" \
"s_add_i32 s82, s51, 0x7800000\n" \
"v_mfma_f32_16x16x32_f16 v[52:55], v[128:131], v[152:155], v[52:55]\n" \
"v_pk_add_f16 v185, v185, v157\n" \
"v_and_b32 v189, s92, v189\n" \
"v_and_b32 v190, s92, v190\n" \
"v_and_b32 v191, s92, v191\n" \
"ds_read_b128 v[148:151], v240 offset:4096\n" \
"v_mfma_f32_16x16x32_f16 v[64:67], v[132:135], v[152:155], v[64:67]\n" \
"v_pk_add_f16 v186, v186, v158\n" \
"ds_read_b64_tr_b16 v[124:125], v242 offset:0\n" \
"buffer_store_dwordx4 v[188:191], v234, s[36:39], s82 offen sc1\n" \
"v_mfma_f32_16x16x32_f16 v[88:91], v[132:135], v[208:211], v[88:91]\n" \
"v_pk_add_f16 v187, v187, v159\n" \
"ds_read_b128 v[152:155], v240 offset:6144\n" \
"buffer_load_dwordx4 v232, s[36:39], s80 offen offset:2048 sc1 lds\n" \
"v_mfma_f32_16x16x32_f16 v[76:79], v[128:131], v[208:211], v[76:79]\n" \
"s_setprio 0\n" \
"ds_read_b64_tr_b16 v[126:127], v242 offset:8192\n" \
"ds_read_b64_tr_b16 v[188:189], v238 offset:40960\n" \
"v_mfma_f32_16x16x32_f16 v[100:103], v[128:131], v[156:159], v[100:103]\n" \
"ds_read_b128 v[208:211], v240 offset:8192\n" \
"ds_read_b64_tr_b16 v[192:193], v239 offset:40960\n" \
"v_mfma_f32_16x16x32_f16 v[112:115], v[132:135], v[156:159], v[112:115]\n" \
"ds_read_b64_tr_b16 v[128:129], v241 offset:512\n" \
"v_mfma_f32_16x16x32_f16 v[8:11], v[136:139], v[168:171], v[8:11]\n" \
"ds_read_b128 v[156:159], v240 offset:0\n" \
"s_waitcnt lgkmcnt(4)\n" \
"v_mfma_f32_16x16x16_bf16 v[188:191], v[188:189], v[254:255], 0\n" \
"v_mfma_f32_16x16x32_f16 v[20:23], v[140:143], v[168:171], v[20:23]\n" \
"ds_read_b128 v[168:171], v240 offset:6656\n" \
"s_waitcnt lgkmcnt(3)\n" \
"v_mfma_f32_16x16x16_bf16 v[192:195], v[192:193], v[254:255], 0\n" \
"v_mfma_f32_16x16x32_f16 v[44:47], v[140:143], v[172:175], v[44:47]\n" \
"ds_read_b64_tr_b16 v[130:131], v241 offset:8704\n" \
"ds_read_b64_tr_b16 v[132:133], v242 offset:512\n" \
"buffer_load_dwordx4 v232, s[36:39], s80 offen offset:3072 sc1 lds\n" \
"v_mfma_f32_16x16x32_f16 v[32:35], v[136:139], v[172:175], v[32:35]\n" \
"ds_read_b128 v[172:175], v240 offset:8704\n" \
"v_mfma_f32_16x16x32_f16 v[56:59], v[136:139], v[176:179], v[56:59]\n" \
"v_cvt_pk_f16_f32 v188, v188, v189\n" \
"v_cvt_pk_f16_f32 v189, v190, v191\n" \
"ds_read_b64_tr_b16 v[134:135], v242 offset:8704\n" \
"v_mfma_f32_16x16x32_f16 v[68:71], v[140:143], v[176:179], v[68:71]\n" \
"v_cvt_pk_f16_f32 v190, v192, v193\n" \
"v_cvt_pk_f16_f32 v191, v194, v195\n" \
"v_add_u32 v188, s91, v188\n" \
"v_add_u32 v189, s91, v189\n" \
"v_add_u32 v190, s91, v190\n" \
"ds_read_b128 v[176:179], v240 offset:512\n" \
"v_mfma_f32_16x16x32_f16 v[92:95], v[140:143], v[180:183], v[92:95]\n" \
"s_waitcnt vmcnt(5)\n" \
"v_cmp_eq_u32 vcc, 0, v253\n" \
"s_cbranch_vccnz .Ls110_%=\n" \
".Lr110_%=:\n" \
"s_add_i32 m0, s45, 0xa000\n" \
"s_add_i32 s81, s47, 0x5c00000\n" \
"v_mfma_f32_16x16x32_f16 v[80:83], v[136:139], v[180:183], v[80:83]\n" \
"v_add_u32 v191, s91, v191\n" \
"v_and_b32 v188, s92, v188\n" \
"buffer_load_dwordx4 v232, s[36:39], s81 offen sc1 lds\n" \
"v_mfma_f32_16x16x32_f16 v[104:107], v[136:139], v[184:187], v[104:107]\n" \
"v_and_b32 v189, s92, v189\n" \
"v_and_b32 v190, s92, v190\n" \
"v_and_b32 v191, s92, v191\n" \
"v_mfma_f32_16x16x32_f16 v[116:119], v[140:143], v[184:187], v[116:119]\n" \
"s_setprio 1\n" \
"buffer_store_dwordx4 v[188:191], v234, s[36:39], s82 offen offset:1024 sc1\n" \
"s_waitcnt vmcnt(7) lgkmcnt(0)\n" \
"s_barrier\n" \
"v_mfma_f32_16x16x32_f16 v[0:3], v[120:123], v[144:147], v[0:3]\n" \
"v_pk_add_f16 v136, v120, v128\n" \
"v_pk_add_f16 v137, v121, v129\n" \
"v_mfma_f32_16x16x32_f16 v[12:15], v[124:127], v[144:147], v[12:15]\n" \
"v_pk_add_f16 v144, v144, v160\n" \
"v_pk_add_f16 v138, v122, v130\n" \
"v_mfma_f32_16x16x32_f16 v[36:39], v[124:127], v[148:151], v[36:39]\n" \
"v_pk_add_f16 v145, v145, v161\n" \
"v_pk_add_f16 v146, v146, v162\n" \
"v_pk_add_f16 v147, v147, v163\n" \
"v_mfma_f32_16x16x32_f16 v[24:27], v[120:123], v[148:151], v[24:27]\n" \
"v_pk_add_f16 v148, v148, v164\n" \
"v_pk_add_f16 v139, v123, v131\n" \
"s_add_i32 m0, s44, 0x0\n" \
"s_add_i32 s80, s46, 0x6000000\n" \
"v_mfma_f32_16x16x32_f16 v[48:51], v[120:123], v[152:155], v[48:51]\n" \
"v_pk_add_f16 v149, v149, v165\n" \
"v_pk_add_f16 v140, v124, v132\n" \
"buffer_load_dwordx4 v232, s[36:39], s80 offen sc1 lds\n" \
"v_mfma_f32_16x16x32_f16 v[60:63], v[124:127], v[152:155], v[60:63]\n" \
"v_pk_add_f16 v150, v150, v166\n" \
"v_pk_add_f16 v151, v151, v167\n" \
"ds_read_b128 v[184:187], v240 offset:25088\n" \
"v_mfma_f32_16x16x32_f16 v[84:87], v[124:127], v[208:211], v[84:87]\n" \
"v_pk_add_f16 v152, v152, v168\n" \
"v_pk_add_f16 v141, v125, v133\n" \
"v_mfma_f32_16x16x32_f16 v[72:75], v[120:123], v[208:211], v[72:75]\n" \
"v_pk_add_f16 v153, v153, v169\n" \
"v_pk_add_f16 v142, v126, v134\n" \
"v_mfma_f32_16x16x32_f16 v[96:99], v[120:123], v[156:159], v[96:99]\n" \
"v_pk_add_f16 v154, v154, v170\n" \
"v_pk_add_f16 v155, v155, v171\n" \
"v_mfma_f32_16x16x32_f16 v[108:111], v[124:127], v[156:159], v[108:111]\n" \
"v_pk_add_f16 v208, v208, v172\n" \
"v_pk_add_f16 v143, v127, v135\n" \
"v_mfma_f32_16x16x32_f16 v[4:7], v[128:131], v[160:163], v[4:7]\n" \
"v_pk_add_f16 v209, v209, v173\n" \
"ds_read_b64_tr_b16 v[120:121], v241 offset:16384\n" \
"buffer_load_dwordx4 v232, s[36:39], s80 offen offset:1024 sc1 lds\n" \
"v_mfma_f32_16x16x32_f16 v[16:19], v[132:135], v[160:163], v[16:19]\n" \
"v_pk_add_f16 v210, v210, v174\n" \
"ds_read_b128 v[180:183], v240 offset:23040\n" \
"v_mfma_f32_16x16x32_f16 v[40:43], v[132:135], v[164:167], v[40:43]\n" \
"v_pk_add_f16 v211, v211, v175\n" \
"ds_read_b128 v[160:163], v240 offset:22528\n" \
"v_mfma_f32_16x16x32_f16 v[28:31], v[128:131], v[164:167], v[28:31]\n" \
"v_pk_add_f16 v156, v156, v176\n" \
"ds_read_b64_tr_b16 v[122:123], v241 offset:24576\n" \
"v_mfma_f32_16x16x32_f16 v[52:55], v[128:131], v[168:171], v[52:55]\n" \
"v_pk_add_f16 v157, v157, v177\n" \
"ds_read_b128 v[164:167], v240 offset:24576\n" \
"v_mfma_f32_16x16x32_f16 v[64:67], v[132:135], v[168:171], v[64:67]\n" \
"v_pk_add_f16 v158, v158, v178\n" \
"ds_read_b64_tr_b16 v[124:125], v242 offset:16384\n" \
"v_mfma_f32_16x16x32_f16 v[88:91], v[132:135], v[172:175], v[88:91]\n" \
"v_pk_add_f16 v159, v159, v179\n" \
"ds_read_b128 v[168:171], v240 offset:26624\n" \
"buffer_load_dwordx4 v232, s[36:39], s80 offen offset:2048 sc1 lds\n" \
"v_mfma_f32_16x16x32_f16 v[76:79], v[128:131], v[172:175], v[76:79]\n" \
"s_setprio 0\n" \
"ds_read_b64_tr_b16 v[126:127], v242 offset:24576\n" \
"v_mfma_f32_16x16x32_f16 v[100:103], v[128:131], v[176:179], v[100:103]\n" \
"ds_read_b128 v[172:175], v240 offset:28672\n" \
"v_mfma_f32_16x16x32_f16 v[112:115], v[132:135], v[176:179], v[112:115]\n" \
"ds_read_b64_tr_b16 v[128:129], v241 offset:16896\n" \
"v_mfma_f32_16x16x32_f16 v[8:11], v[136:139], v[144:147], v[8:11]\n" \
"ds_read_b128 v[176:179], v240 offset:20480\n" \
"v_mfma_f32_16x16x32_f16 v[20:23], v[140:143], v[144:147], v[20:23]\n" \
"ds_read_b128 v[144:147], v240 offset:27136\n" \
"v_mfma_f32_16x16x32_f16 v[44:47], v[140:143], v[148:151], v[44:47]\n" \
"ds_read_b64_tr_b16 v[130:131], v241 offset:25088\n" \
"ds_read_b64_tr_b16 v[132:133], v242 offset:16896\n" \
"buffer_load_dwordx4 v232, s[36:39], s80 offen offset:3072 sc1 lds\n" \
"v_mfma_f32_16x16x32_f16 v[32:35], v[136:139], v[148:151], v[32:35]\n" \
"ds_read_b128 v[148:151], v240 offset:29184\n" \
"v_mfma_f32_16x16x32_f16 v[56:59], v[136:139], v[152:155], v[56:59]\n" \
"ds_read_b64_tr_b16 v[134:135], v242 offset:25088\n" \
"v_mfma_f32_16x16x32_f16 v[68:71], v[140:143], v[152:155], v[68:71]\n" \
"ds_read_b128 v[152:155], v240 offset:20992\n" \
"v_mfma_f32_16x16x32_f16 v[92:95], v[140:143], v[208:211], v[92:95]\n" \
"s_add_i32 m0, s45, 0x0\n" \
"s_add_i32 s81, s47, 0x6000000\n" \
"v_mfma_f32_16x16x32_f16 v[80:83], v[136:139], v[208:211], v[80:83]\n" \
"buffer_load_dwordx4 v232, s[36:39], s81 offen sc1 lds\n" \
"v_mfma_f32_16x16x32_f16 v[104:107], v[136:139], v[156:159], v[104:107]\n" \
"v_mfma_f32_16x16x32_f16 v[116:119], v[140:143], v[156:159], v[116:119]\n" \
"s_setprio 1\n" \
"s_waitcnt vmcnt(5) lgkmcnt(0)\n" \
"s_barrier\n" \
"v_mfma_f32_16x16x32_f16 v[0:3], v[120:123], v[160:163], v[0:3]\n" \
"v_pk_add_f16 v136, v120, v128\n" \
"v_pk_add_f16 v137, v121, v129\n" \
"s_add_i32 m0, s54, 0x1000\n" \
"s_mov_b32 s83, 0x300\n" \
"v_mfma_f32_16x16x32_f16 v[12:15], v[124:127], v[160:163], v[12:15]\n" \
"v_pk_add_f16 v160, v160, v180\n" \
"v_pk_add_f16 v138, v122, v130\n" \
"buffer_load_dwordx4 v233, s[36:39], s83 offen sc1 lds\n" \
"v_mfma_f32_16x16x32_f16 v[36:39], v[124:127], v[164:167], v[36:39]\n" \
"v_pk_add_f16 v161, v161, v181\n" \
"v_pk_add_f16 v162, v162, v182\n" \
"v_pk_add_f16 v163, v163, v183\n" \
"v_mfma_f32_16x16x32_f16 v[24:27], v[120:123], v[164:167], v[24:27]\n" \
"v_pk_add_f16 v164, v164, v184\n" \
"v_pk_add_f16 v139, v123, v131\n" \
"s_add_i32 m0, s44, 0x5000\n" \
"s_add_i32 s80, s46, 0x6400000\n" \
"v_mfma_f32_16x16x32_f16 v[48:51], v[120:123], v[168:171], v[48:51]\n" \
"v_pk_add_f16 v165, v165, v185\n" \
"v_pk_add_f16 v140, v124, v132\n" \
"buffer_load_dwordx4 v232, s[36:39], s80 offen sc1 lds\n" \
"v_mfma_f32_16x16x32_f16 v[60:63], v[124:127], v[168:171], v[60:63]\n" \
"v_pk_add_f16 v166, v166, v186\n" \
"v_pk_add_f16 v167, v167, v187\n" \
"ds_read_b128 v[156:159], v240 offset:45568\n" \
"v_mfma_f32_16x16x32_f16 v[84:87], v[124:127], v[172:175], v[84:87]\n" \
"v_pk_add_f16 v168, v168, v144\n" \
"v_pk_add_f16 v141, v125, v133\n" \
"v_mfma_f32_16x16x32_f16 v[72:75], v[120:123], v[172:175], v[72:75]\n" \
"v_pk_add_f16 v169, v169, v145\n" \
"v_pk_add_f16 v142, v126, v134\n" \
"v_mfma_f32_16x16x32_f16 v[96:99], v[120:123], v[176:179], v[96:99]\n" \
"v_pk_add_f16 v170, v170, v146\n" \
"v_pk_add_f16 v171, v171, v147\n" \
"v_mfma_f32_16x16x32_f16 v[108:111], v[124:127], v[176:179], v[108:111]\n" \
"v_pk_add_f16 v172, v172, v148\n" \
"v_pk_add_f16 v143, v127, v135\n" \
"v_mfma_f32_16x16x32_f16 v[4:7], v[128:131], v[180:183], v[4:7]\n" \
"v_pk_add_f16 v173, v173, v149\n" \
"ds_read_b64_tr_b16 v[120:121], v241 offset:32768\n" \
"buffer_load_dwordx4 v232, s[36:39], s80 offen offset:1024 sc1 lds\n" \
"v_mfma_f32_16x16x32_f16 v[16:19], v[132:135], v[180:183], v[16:19]\n" \
"v_pk_add_f16 v174, v174, v150\n" \
"ds_read_b128 v[208:211], v240 offset:43520\n" \
"v_mfma_f32_16x16x32_f16 v[40:43], v[132:135], v[184:187], v[40:43]\n" \
"v_pk_add_f16 v175, v175, v151\n" \
"ds_read_b128 v[180:183], v240 offset:43008\n" \
"v_mfma_f32_16x16x32_f16 v[28:31], v[128:131], v[184:187], v[28:31]\n" \
"v_pk_add_f16 v176, v176, v152\n" \
"ds_read_b64_tr_b16 v[122:123], v241 offset:40960\n" \
"v_mfma_f32_16x16x32_f16 v[52:55], v[128:131], v[144:147], v[52:55]\n" \
"v_pk_add_f16 v177, v177, v153\n" \
"ds_read_b128 v[184:187], v240 offset:45056\n" \
"v_mfma_f32_16x16x32_f16 v[64:67], v[132:135], v[144:147], v[64:67]\n" \
"v_pk_add_f16 v178, v178, v154\n" \
"ds_read_b64_tr_b16 v[124:125], v242 offset:32768\n" \
"v_mfma_f32_16x16x32_f16 v[88:91], v[132:135], v[148:151], v[88:91]\n" \
"v_pk_add_f16 v179, v179, v155\n" \
"ds_read_b128 v[144:147], v240 offset:47104\n" \
"buffer_load_dwordx4 v232, s[36:39], s80 offen offset:2048 sc1 lds\n" \
"v_mfma_f32_16x16x32_f16 v[76:79], v[128:131], v[148:151], v[76:79]\n" \
"s_setprio 0\n" \
"ds_read_b64_tr_b16 v[126:127], v242 offset:40960\n" \
"v_mfma_f32_16x16x32_f16 v[100:103], v[128:131], v[152:155], v[100:103]\n" \
"ds_read_b128 v[148:151], v240 offset:49152\n" \
"v_mfma_f32_16x16x32_f16 v[112:115], v[132:135], v[152:155], v[112:115]\n" \
"ds_read_b64_tr_b16 v[128:129], v241 offset:33280\n" \
"v_mfma_f32_16x16x32_f16 v[8:11], v[136:139], v[160:163], v[8:11]\n" \
"ds_read_b128 v[152:155], v240 offset:40960\n" \
"v_mfma_f32_16x16x32_f16 v[20:23], v[140:143], v[160:163], v[20:23]\n" \
"ds_read_b128 v[160:163], v240 offset:47616\n" \
"v_mfma_f32_16x16x32_f16 v[44:47], v[140:143], v[164:167], v[44:47]\n" \
"ds_read_b64_tr_b16 v[130:131], v241 offset:41472\n" \
"ds_read_b64_tr_b16 v[132:133], v242 offset:33280\n" \
"buffer_load_dwordx4 v232, s[36:39], s80 offen offset:3072 sc1 lds\n" \
"v_mfma_f32_16x16x32_f16 v[32:35], v[136:139], v[164:167], v[32:35]\n" \
"ds_read_b128 v[164:167], v240 offset:49664\n" \
"v_mfma_f32_16x16x32_f16 v[56:59], v[136:139], v[168:171], v[56:59]\n" \
"ds_read_b64_tr_b16 v[134:135], v242 offset:41472\n" \
"v_mfma_f32_16x16x32_f16 v[68:71], v[140:143], v[168:171], v[68:71]\n" \
"ds_read_b128 v[168:171], v240 offset:41472\n" \
"v_mfma_f32_16x16x32_f16 v[92:95], v[140:143], v[172:175], v[92:95]\n" \
"s_add_i32 m0, s45, 0x5000\n" \
"s_add_i32 s81, s47, 0x6400000\n" \
"v_mfma_f32_16x16x32_f16 v[80:83], v[136:139], v[172:175], v[80:83]\n" \
"buffer_load_dwordx4 v232, s[36:39], s81 offen sc1 lds\n" \
"v_mfma_f32_16x16x32_f16 v[104:107], v[136:139], v[176:179], v[104:107]\n" \
"v_mfma_f32_16x16x32_f16 v[116:119], v[140:143], v[176:179], v[116:119]\n" \
"s_setprio 1\n" \
"s_waitcnt vmcnt(6) lgkmcnt(0)\n" \
"s_barrier\n" \
"v_mfma_f32_16x16x32_f16 v[0:3], v[120:123], v[180:183], v[0:3]\n" \
"v_pk_add_f16 v136, v120, v128\n" \
"v_pk_add_f16 v137, v121, v129\n" \
"v_mfma_f32_16x16x32_f16 v[12:15], v[124:127], v[180:183], v[12:15]\n" \
"v_pk_add_f16 v180, v180, v208\n" \
"v_pk_add_f16 v138, v122, v130\n" \
"v_mfma_f32_16x16x32_f16 v[36:39], v[124:127], v[184:187], v[36:39]\n" \
"v_pk_add_f16 v181, v181, v209\n" \
"v_pk_add_f16 v182, v182, v210\n" \
"v_pk_add_f16 v183, v183, v211\n" \
"v_mfma_f32_16x16x32_f16 v[24:27], v[120:123], v[184:187], v[24:27]\n" \
"v_pk_add_f16 v184, v184, v156\n" \
"v_pk_add_f16 v139, v123, v131\n" \
"s_add_i32 m0, s44, 0xa000\n" \
"s_add_i32 s80, s46, 0x6800000\n" \
"v_mfma_f32_16x16x32_f16 v[48:51], v[120:123], v[144:147], v[48:51]\n" \
"v_pk_add_f16 v185, v185, v157\n" \
"v_pk_add_f16 v140, v124, v132\n" \
"buffer_load_dwordx4 v232, s[36:39], s80 offen sc1 lds\n" \
"v_mfma_f32_16x16x32_f16 v[60:63], v[124:127], v[144:147], v[60:63]\n" \
"v_pk_add_f16 v186, v186, v158\n" \
"v_pk_add_f16 v187, v187, v159\n" \
"ds_read_b128 v[176:179], v240 offset:4608\n" \
"v_mfma_f32_16x16x32_f16 v[84:87], v[124:127], v[148:151], v[84:87]\n" \
"v_pk_add_f16 v144, v144, v160\n" \
"v_pk_add_f16 v141, v125, v133\n" \
"v_mfma_f32_16x16x32_f16 v[72:75], v[120:123], v[148:151], v[72:75]\n" \
"v_pk_add_f16 v145, v145, v161\n" \
"v_pk_add_f16 v142, v126, v134\n" \
"v_mfma_f32_16x16x32_f16 v[96:99], v[120:123], v[152:155], v[96:99]\n" \
"v_pk_add_f16 v146, v146, v162\n" \
"v_pk_add_f16 v147, v147, v163\n" \
"v_mfma_f32_16x16x32_f16 v[108:111], v[124:127], v[152:155], v[108:111]\n" \
"v_pk_add_f16 v148, v148, v164\n" \
"v_pk_add_f16 v143, v127, v135\n" \
"v_mfma_f32_16x16x32_f16 v[4:7], v[128:131], v[208:211], v[4:7]\n" \
"v_pk_add_f16 v149, v149, v165\n" \
"ds_read_b64_tr_b16 v[120:121], v241 offset:0\n" \
"buffer_load_dwordx4 v232, s[36:39], s80 offen offset:1024 sc1 lds\n" \
"v_mfma_f32_16x16x32_f16 v[16:19], v[132:135], v[208:211], v[16:19]\n" \
"v_pk_add_f16 v150, v150, v166\n" \
"ds_read_b128 v[172:175], v240 offset:2560\n" \
"v_mfma_f32_16x16x32_f16 v[40:43], v[132:135], v[156:159], v[40:43]\n" \
"v_pk_add_f16 v151, v151, v167\n" \
"ds_read_b128 v[208:211], v240 offset:2048\n" \
"v_mfma_f32_16x16x32_f16 v[28:31], v[128:131], v[156:159], v[28:31]\n" \
"v_pk_add_f16 v152, v152, v168\n" \
"ds_read_b64_tr_b16 v[122:123], v241 offset:8192\n" \
"v_mfma_f32_16x16x32_f16 v[52:55], v[128:131], v[160:163], v[52:55]\n" \
"v_pk_add_f16 v153, v153, v169\n" \
"ds_read_b128 v[156:159], v240 offset:4096\n" \
"v_mfma_f32_16x16x32_f16 v[64:67], v[132:135], v[160:163], v[64:67]\n" \
"v_pk_add_f16 v154, v154, v170\n" \
"ds_read_b64_tr_b16 v[124:125], v242 offset:0\n" \
"v_mfma_f32_16x16x32_f16 v[88:91], v[132:135], v[164:167], v[88:91]\n" \
"v_pk_add_f16 v155, v155, v171\n" \
"ds_read_b128 v[160:163], v240 offset:6144\n" \
"buffer_load_dwordx4 v232, s[36:39], s80 offen offset:2048 sc1 lds\n" \
"v_mfma_f32_16x16x32_f16 v[76:79], v[128:131], v[164:167], v[76:79]\n" \
"s_setprio 0\n" \
"ds_read_b64_tr_b16 v[126:127], v242 offset:8192\n" \
"v_mfma_f32_16x16x32_f16 v[100:103], v[128:131], v[168:171], v[100:103]\n" \
"ds_read_b128 v[164:167], v240 offset:8192\n" \
"v_mfma_f32_16x16x32_f16 v[112:115], v[132:135], v[168:171], v[112:115]\n" \
"ds_read_b64_tr_b16 v[128:129], v241 offset:512\n" \
"v_mfma_f32_16x16x32_f16 v[8:11], v[136:139], v[180:183], v[8:11]\n" \
"ds_read_b128 v[168:171], v240 offset:0\n" \
"v_mfma_f32_16x16x32_f16 v[20:23], v[140:143], v[180:183], v[20:23]\n" \
"ds_read_b128 v[180:183], v240 offset:6656\n" \
"v_mfma_f32_16x16x32_f16 v[44:47], v[140:143], v[184:187], v[44:47]\n" \
"ds_read_b64_tr_b16 v[130:131], v241 offset:8704\n" \
"ds_read_b64_tr_b16 v[132:133], v242 offset:512\n" \
"buffer_load_dwordx4 v232, s[36:39], s80 offen offset:3072 sc1 lds\n" \
"v_mfma_f32_16x16x32_f16 v[32:35], v[136:139], v[184:187], v[32:35]\n" \
"ds_read_b128 v[184:187], v240 offset:8704\n" \
"v_mfma_f32_16x16x32_f16 v[56:59], v[136:139], v[144:147], v[56:59]\n" \
"ds_read_b64_tr_b16 v[134:135], v242 offset:8704\n" \
"v_mfma_f32_16x16x32_f16 v[68:71], v[140:143], v[144:147], v[68:71]\n" \
"ds_read_b128 v[144:147], v240 offset:512\n" \
"v_mfma_f32_16x16x32_f16 v[92:95], v[140:143], v[148:151], v[92:95]\n" \
"s_add_i32 m0, s45, 0xa000\n" \
"s_add_i32 s81, s47, 0x6800000\n" \
"v_mfma_f32_16x16x32_f16 v[80:83], v[136:139], v[148:151], v[80:83]\n" \
"buffer_load_dwordx4 v232, s[36:39], s81 offen sc1 lds\n" \
"v_mfma_f32_16x16x32_f16 v[104:107], v[136:139], v[152:155], v[104:107]\n" \
"v_mfma_f32_16x16x32_f16 v[116:119], v[140:143], v[152:155], v[116:119]\n" \
"s_setprio 1\n" \
"s_waitcnt vmcnt(5) lgkmcnt(0)\n" \
"s_barrier\n" \
"v_mfma_f32_16x16x32_f16 v[0:3], v[120:123], v[208:211], v[0:3]\n" \
"v_pk_add_f16 v136, v120, v128\n" \
"v_pk_add_f16 v137, v121, v129\n" \
"v_mfma_f32_16x16x32_f16 v[12:15], v[124:127], v[208:211], v[12:15]\n" \
"v_pk_add_f16 v208, v208, v172\n" \
"v_pk_add_f16 v138, v122, v130\n" \
"v_mfma_f32_16x16x32_f16 v[36:39], v[124:127], v[156:159], v[36:39]\n" \
"v_pk_add_f16 v209, v209, v173\n" \
"v_pk_add_f16 v210, v210, v174\n" \
"v_pk_add_f16 v211, v211, v175\n" \
"v_mfma_f32_16x16x32_f16 v[24:27], v[120:123], v[156:159], v[24:27]\n" \
"v_pk_add_f16 v156, v156, v176\n" \
"v_pk_add_f16 v139, v123, v131\n" \
"s_add_i32 m0, s44, 0x0\n" \
"s_add_i32 s80, s46, 0x6c00000\n" \
"v_mfma_f32_16x16x32_f16 v[48:51], v[120:123], v[160:163], v[48:51]\n" \
"v_pk_add_f16 v157, v157, v177\n" \
"v_pk_add_f16 v140, v124, v132\n" \
"buffer_load_dwordx4 v232, s[36:39], s80 offen sc1 lds\n" \
"v_mfma_f32_16x16x32_f16 v[60:63], v[124:127], v[160:163], v[60:63]\n" \
"v_pk_add_f16 v158, v158, v178\n" \
"v_pk_add_f16 v159, v159, v179\n" \
"ds_read_b128 v[152:155], v240 offset:25088\n" \
"v_mfma_f32_16x16x32_f16 v[84:87], v[124:127], v[164:167], v[84:87]\n" \
"v_pk_add_f16 v160, v160, v180\n" \
"v_pk_add_f16 v141, v125, v133\n" \
"v_mfma_f32_16x16x32_f16 v[72:75], v[120:123], v[164:167], v[72:75]\n" \
"v_pk_add_f16 v161, v161, v181\n" \
"v_pk_add_f16 v142, v126, v134\n" \
"v_mfma_f32_16x16x32_f16 v[96:99], v[120:123], v[168:171], v[96:99]\n" \
"v_pk_add_f16 v162, v162, v182\n" \
"v_pk_add_f16 v163, v163, v183\n" \
"v_mfma_f32_16x16x32_f16 v[108:111], v[124:127], v[168:171], v[108:111]\n" \
"v_pk_add_f16 v164, v164, v184\n" \
"v_pk_add_f16 v143, v127, v135\n" \
"v_mfma_f32_16x16x32_f16 v[4:7], v[128:131], v[172:175], v[4:7]\n" \
"v_pk_add_f16 v165, v165, v185\n" \
"ds_read_b64_tr_b16 v[120:121], v241 offset:16384\n" \
"buffer_load_dwordx4 v232, s[36:39], s80 offen offset:1024 sc1 lds\n" \
"v_mfma_f32_16x16x32_f16 v[16:19], v[132:135], v[172:175], v[16:19]\n" \
"v_pk_add_f16 v166, v166, v186\n" \
"ds_read_b128 v[148:151], v240 offset:23040\n" \
"v_mfma_f32_16x16x32_f16 v[40:43], v[132:135], v[176:179], v[40:43]\n" \
"v_pk_add_f16 v167, v167, v187\n" \
"ds_read_b128 v[172:175], v240 offset:22528\n" \
"v_mfma_f32_16x16x32_f16 v[28:31], v[128:131], v[176:179], v[28:31]\n" \
"v_pk_add_f16 v168, v168, v144\n" \
"ds_read_b64_tr_b16 v[122:123], v241 offset:24576\n" \
"v_mfma_f32_16x16x32_f16 v[52:55], v[128:131], v[180:183], v[52:55]\n" \
"v_pk_add_f16 v169, v169, v145\n" \
"ds_read_b128 v[176:179], v240 offset:24576\n" \
"v_mfma_f32_16x16x32_f16 v[64:67], v[132:135], v[180:183], v[64:67]\n" \
"v_pk_add_f16 v170, v170, v146\n" \
"ds_read_b64_tr_b16 v[124:125], v242 offset:16384\n" \
"v_mfma_f32_16x16x32_f16 v[88:91], v[132:135], v[184:187], v[88:91]\n" \
"v_pk_add_f16 v171, v171, v147\n" \
"ds_read_b128 v[180:183], v240 offset:26624\n" \
"buffer_load_dwordx4 v232, s[36:39], s80 offen offset:2048 sc1 lds\n" \
"v_mfma_f32_16x16x32_f16 v[76:79], v[128:131], v[184:187], v[76:79]\n" \
"s_setprio 0\n" \
"ds_read_b64_tr_b16 v[126:127], v242 offset:24576\n" \
"v_mfma_f32_16x16x32_f16 v[100:103], v[128:131], v[144:147], v[100:103]\n" \
"ds_read_b128 v[184:187], v240 offset:28672\n" \
"v_mfma_f32_16x16x32_f16 v[112:115], v[132:135], v[144:147], v[112:115]\n" \
"ds_read_b64_tr_b16 v[128:129], v241 offset:16896\n" \
"v_mfma_f32_16x16x32_f16 v[8:11], v[136:139], v[208:211], v[8:11]\n" \
"ds_read_b128 v[144:147], v240 offset:20480\n" \
"v_mfma_f32_16x16x32_f16 v[20:23], v[140:143], v[208:211], v[20:23]\n" \
"ds_read_b128 v[208:211], v240 offset:27136\n" \
"v_mfma_f32_16x16x32_f16 v[44:47], v[140:143], v[156:159], v[44:47]\n" \
"ds_read_b64_tr_b16 v[130:131], v241 offset:25088\n" \
"ds_read_b64_tr_b16 v[132:133], v242 offset:16896\n" \
"buffer_load_dwordx4 v232, s[36:39], s80 offen offset:3072 sc1 lds\n" \
"v_mfma_f32_16x16x32_f16 v[32:35], v[136:139], v[156:159], v[32:35]\n" \
"ds_read_b128 v[156:159], v240 offset:29184\n" \
"v_mfma_f32_16x16x32_f16 v[56:59], v[136:139], v[160:163], v[56:59]\n" \
"ds_read_b64_tr_b16 v[134:135], v242 offset:25088\n" \
"v_mfma_f32_16x16x32_f16 v[68:71], v[140:143], v[160:163], v[68:71]\n" \
"ds_read_b128 v[160:163], v240 offset:20992\n" \
"v_mfma_f32_16x16x32_f16 v[92:95], v[140:143], v[164:167], v[92:95]\n" \
"s_add_i32 m0, s45, 0x0\n" \
"s_add_i32 s81, s47, 0x6c00000\n" \
"v_mfma_f32_16x16x32_f16 v[80:83], v[136:139], v[164:167], v[80:83]\n" \
"buffer_load_dwordx4 v232, s[36:39], s81 offen sc1 lds\n" \
"v_mfma_f32_16x16x32_f16 v[104:107], v[136:139], v[168:171], v[104:107]\n" \
"v_mfma_f32_16x16x32_f16 v[116:119], v[140:143], v[168:171], v[116:119]\n" \
"s_setprio 1\n" \
"s_waitcnt vmcnt(5) lgkmcnt(0)\n" \
"s_barrier\n" \
"v_mfma_f32_16x16x32_f16 v[0:3], v[120:123], v[172:175], v[0:3]\n" \
"v_pk_add_f16 v136, v120, v128\n" \
"v_pk_add_f16 v137, v121, v129\n" \
"v_mfma_f32_16x16x32_f16 v[12:15], v[124:127], v[172:175], v[12:15]\n" \
"v_pk_add_f16 v172, v172, v148\n" \
"v_pk_add_f16 v138, v122, v130\n" \
"v_mfma_f32_16x16x32_f16 v[36:39], v[124:127], v[176:179], v[36:39]\n" \
"v_pk_add_f16 v173, v173, v149\n" \
"v_pk_add_f16 v174, v174, v150\n" \
"v_pk_add_f16 v175, v175, v151\n" \
"s_add_i32 m0, s44, 0x5000\n" \
"s_add_i32 s80, s46, 0x7000000\n" \
"v_mfma_f32_16x16x32_f16 v[24:27], v[120:123], v[176:179], v[24:27]\n" \
"v_pk_add_f16 v176, v176, v152\n" \
"v_pk_add_f16 v139, v123, v131\n" \
"buffer_load_dwordx4 v232, s[36:39], s80 offen sc1 lds\n" \
"v_mfma_f32_16x16x32_f16 v[48:51], v[120:123], v[180:183], v[48:51]\n" \
"v_pk_add_f16 v177, v177, v153\n" \
"v_pk_add_f16 v140, v124, v132\n" \
"v_mfma_f32_16x16x32_f16 v[60:63], v[124:127], v[180:183], v[60:63]\n" \
"v_pk_add_f16 v178, v178, v154\n" \
"v_pk_add_f16 v179, v179, v155\n" \
"ds_read_b128 v[168:171], v240 offset:45568\n" \
"v_mfma_f32_16x16x32_f16 v[84:87], v[124:127], v[184:187], v[84:87]\n" \
"v_pk_add_f16 v180, v180, v208\n" \
"v_pk_add_f16 v141, v125, v133\n" \
"buffer_load_dwordx4 v232, s[36:39], s80 offen offset:1024 sc1 lds\n" \
"v_mfma_f32_16x16x32_f16 v[72:75], v[120:123], v[184:187], v[72:75]\n" \
"v_pk_add_f16 v181, v181, v209\n" \
"v_pk_add_f16 v142, v126, v134\n" \
"v_mfma_f32_16x16x32_f16 v[96:99], v[120:123], v[144:147], v[96:99]\n" \
"v_pk_add_f16 v182, v182, v210\n" \
"v_pk_add_f16 v183, v183, v211\n" \
"v_mfma_f32_16x16x32_f16 v[108:111], v[124:127], v[144:147], v[108:111]\n" \
"v_pk_add_f16 v184, v184, v156\n" \
"v_pk_add_f16 v143, v127, v135\n" \
"buffer_load_dwordx4 v232, s[36:39], s80 offen offset:2048 sc1 lds\n" \
"v_mfma_f32_16x16x32_f16 v[4:7], v[128:131], v[148:151], v[4:7]\n" \
"v_pk_add_f16 v185, v185, v157\n" \
"ds_read_b64_tr_b16 v[120:121], v241 offset:32768\n" \
"v_mfma_f32_16x16x32_f16 v[16:19], v[132:135], v[148:151], v[16:19]\n" \
"v_pk_add_f16 v186, v186, v158\n" \
"ds_read_b128 v[164:167], v240 offset:43520\n" \
"v_mfma_f32_16x16x32_f16 v[40:43], v[132:135], v[152:155], v[40:43]\n" \
"v_pk_add_f16 v187, v187, v159\n" \
"ds_read_b128 v[148:151], v240 offset:43008\n" \
"buffer_load_dwordx4 v232, s[36:39], s80 offen offset:3072 sc1 lds\n" \
"v_mfma_f32_16x16x32_f16 v[28:31], v[128:131], v[152:155], v[28:31]\n" \
"v_pk_add_f16 v144, v144, v160\n" \
"ds_read_b64_tr_b16 v[122:123], v241 offset:40960\n" \
"v_mfma_f32_16x16x32_f16 v[52:55], v[128:131], v[208:211], v[52:55]\n" \
"v_pk_add_f16 v145, v145, v161\n" \
"ds_read_b128 v[152:155], v240 offset:45056\n" \
"s_add_i32 m0, s45, 0x5000\n" \
"s_add_i32 s81, s47, 0x7000000\n" \
"v_mfma_f32_16x16x32_f16 v[64:67], v[132:135], v[208:211], v[64:67]\n" \
"v_pk_add_f16 v146, v146, v162\n" \
"ds_read_b64_tr_b16 v[124:125], v242 offset:32768\n" \
"buffer_load_dwordx4 v232, s[36:39], s81 offen sc1 lds\n" \
"v_mfma_f32_16x16x32_f16 v[88:91], v[132:135], v[156:159], v[88:91]\n" \
"v_pk_add_f16 v147, v147, v163\n" \
"ds_read_b128 v[208:211], v240 offset:47104\n" \
"v_mfma_f32_16x16x32_f16 v[76:79], v[128:131], v[156:159], v[76:79]\n" \
"s_setprio 0\n" \
"ds_read_b64_tr_b16 v[126:127], v242 offset:40960\n" \
"s_add_i32 m0, s52, 0x0\n" \
"s_add_i32 s80, s55, 0x0\n" \
"v_mfma_f32_16x16x32_f16 v[100:103], v[128:131], v[160:163], v[100:103]\n" \
"ds_read_b128 v[156:159], v240 offset:49152\n" \
"buffer_load_dwordx4 v232, s[36:39], s80 offen sc1 lds\n" \
"v_mfma_f32_16x16x32_f16 v[112:115], v[132:135], v[160:163], v[112:115]\n" \
"ds_read_b64_tr_b16 v[128:129], v241 offset:33280\n" \
"v_mfma_f32_16x16x32_f16 v[8:11], v[136:139], v[172:175], v[8:11]\n" \
"ds_read_b128 v[160:163], v240 offset:40960\n" \
"v_mfma_f32_16x16x32_f16 v[20:23], v[140:143], v[172:175], v[20:23]\n" \
"ds_read_b128 v[172:175], v240 offset:47616\n" \
"buffer_load_dwordx4 v232, s[36:39], s80 offen offset:1024 sc1 lds\n" \
"v_mfma_f32_16x16x32_f16 v[44:47], v[140:143], v[176:179], v[44:47]\n" \
"ds_read_b64_tr_b16 v[130:131], v241 offset:41472\n" \
"ds_read_b64_tr_b16 v[132:133], v242 offset:33280\n" \
"v_mfma_f32_16x16x32_f16 v[32:35], v[136:139], v[176:179], v[32:35]\n" \
"ds_read_b128 v[176:179], v240 offset:49664\n" \
"v_mfma_f32_16x16x32_f16 v[56:59], v[136:139], v[180:183], v[56:59]\n" \
"ds_read_b64_tr_b16 v[134:135], v242 offset:41472\n" \
"buffer_load_dwordx4 v232, s[36:39], s80 offen offset:2048 sc1 lds\n" \
"v_mfma_f32_16x16x32_f16 v[68:71], v[140:143], v[180:183], v[68:71]\n" \
"ds_read_b128 v[180:183], v240 offset:41472\n" \
"v_mfma_f32_16x16x32_f16 v[92:95], v[140:143], v[184:187], v[92:95]\n" \
"v_mfma_f32_16x16x32_f16 v[80:83], v[136:139], v[184:187], v[80:83]\n" \
"buffer_load_dwordx4 v232, s[36:39], s80 offen offset:3072 sc1 lds\n" \
"v_mfma_f32_16x16x32_f16 v[104:107], v[136:139], v[144:147], v[104:107]\n" \
"v_mfma_f32_16x16x32_f16 v[116:119], v[140:143], v[144:147], v[116:119]\n" \
"s_setprio 1\n" \
"s_waitcnt vmcnt(9) lgkmcnt(0)\n" \
"s_barrier\n" \
"v_mfma_f32_16x16x32_f16 v[0:3], v[120:123], v[148:151], v[0:3]\n" \
"v_pk_add_f16 v136, v120, v128\n" \
"v_pk_add_f16 v137, v121, v129\n" \
"v_mfma_f32_16x16x32_f16 v[12:15], v[124:127], v[148:151], v[12:15]\n" \
"v_pk_add_f16 v148, v148, v164\n" \
"v_pk_add_f16 v138, v122, v130\n" \
"v_mfma_f32_16x16x32_f16 v[36:39], v[124:127], v[152:155], v[36:39]\n" \
"v_pk_add_f16 v149, v149, v165\n" \
"v_pk_add_f16 v150, v150, v166\n" \
"v_pk_add_f16 v151, v151, v167\n" \
"v_mfma_f32_16x16x32_f16 v[24:27], v[120:123], v[152:155], v[24:27]\n" \
"v_pk_add_f16 v152, v152, v168\n" \
"v_pk_add_f16 v139, v123, v131\n" \
"s_add_i32 m0, s44, 0xa000\n" \
"s_add_i32 s80, s46, 0x7400000\n" \
"v_mfma_f32_16x16x32_f16 v[48:51], v[120:123], v[208:211], v[48:51]\n" \
"v_pk_add_f16 v153, v153, v169\n" \
"v_pk_add_f16 v140, v124, v132\n" \
"buffer_load_dwordx4 v232, s[36:39], s80 offen sc1 lds\n" \
"v_mfma_f32_16x16x32_f16 v[60:63], v[124:127], v[208:211], v[60:63]\n" \
"v_pk_add_f16 v154, v154, v170\n" \
"v_pk_add_f16 v155, v155, v171\n" \
"ds_read_b128 v[144:147], v240 offset:4608\n" \
"v_mfma_f32_16x16x32_f16 v[84:87], v[124:127], v[156:159], v[84:87]\n" \
"v_pk_add_f16 v208, v208, v172\n" \
"v_pk_add_f16 v141, v125, v133\n" \
"v_mfma_f32_16x16x32_f16 v[72:75], v[120:123], v[156:159], v[72:75]\n" \
"v_pk_add_f16 v209, v209, v173\n" \
"v_pk_add_f16 v142, v126, v134\n" \
"v_mfma_f32_16x16x32_f16 v[96:99], v[120:123], v[160:163], v[96:99]\n" \
"v_pk_add_f16 v210, v210, v174\n" \
"v_pk_add_f16 v211, v211, v175\n" \
"v_mfma_f32_16x16x32_f16 v[108:111], v[124:127], v[160:163], v[108:111]\n" \
"v_pk_add_f16 v156, v156, v176\n" \
"v_pk_add_f16 v143, v127, v135\n" \
"v_mfma_f32_16x16x32_f16 v[4:7], v[128:131], v[164:167], v[4:7]\n" \
"v_pk_add_f16 v157, v157, v177\n" \
"ds_read_b64_tr_b16 v[120:121], v241 offset:0\n" \
"buffer_load_dwordx4 v232, s[36:39], s80 offen offset:1024 sc1 lds\n" \
"v_mfma_f32_16x16x32_f16 v[16:19], v[132:135], v[164:167], v[16:19]\n" \
"v_pk_add_f16 v158, v158, v178\n" \
"ds_read_b128 v[184:187], v240 offset:2560\n" \
"v_mfma_f32_16x16x32_f16 v[40:43], v[132:135], v[168:171], v[40:43]\n" \
"v_pk_add_f16 v159, v159, v179\n" \
"ds_read_b128 v[164:167], v240 offset:2048\n" \
"v_mfma_f32_16x16x32_f16 v[28:31], v[128:131], v[168:171], v[28:31]\n" \
"v_pk_add_f16 v160, v160, v180\n" \
"ds_read_b64_tr_b16 v[122:123], v241 offset:8192\n" \
"v_mfma_f32_16x16x32_f16 v[52:55], v[128:131], v[172:175], v[52:55]\n" \
"v_pk_add_f16 v161, v161, v181\n" \
"ds_read_b128 v[168:171], v240 offset:4096\n" \
"v_mfma_f32_16x16x32_f16 v[64:67], v[132:135], v[172:175], v[64:67]\n" \
"v_pk_add_f16 v162, v162, v182\n" \
"ds_read_b64_tr_b16 v[124:125], v242 offset:0\n" \
"v_mfma_f32_16x16x32_f16 v[88:91], v[132:135], v[176:179], v[88:91]\n" \
"v_pk_add_f16 v163, v163, v183\n" \
"ds_read_b128 v[172:175], v240 offset:6144\n" \
"buffer_load_dwordx4 v232, s[36:39], s80 offen offset:2048 sc1 lds\n" \
"v_mfma_f32_16x16x32_f16 v[76:79], v[128:131], v[176:179], v[76:79]\n" \
"s_setprio 0\n" \
"ds_read_b64_tr_b16 v[126:127], v242 offset:8192\n" \
"v_mfma_f32_16x16x32_f16 v[100:103], v[128:131], v[180:183], v[100:103]\n" \
"ds_read_b128 v[176:179], v240 offset:8192\n" \
"v_mfma_f32_16x16x32_f16 v[112:115], v[132:135], v[180:183], v[112:115]\n" \
"ds_read_b64_tr_b16 v[128:129], v241 offset:512\n" \
"v_mfma_f32_16x16x32_f16 v[8:11], v[136:139], v[148:151], v[8:11]\n" \
"ds_read_b128 v[180:183], v240 offset:0\n" \
"v_mfma_f32_16x16x32_f16 v[20:23], v[140:143], v[148:151], v[20:23]\n" \
"ds_read_b128 v[148:151], v240 offset:6656\n" \
"v_mfma_f32_16x16x32_f16 v[44:47], v[140:143], v[152:155], v[44:47]\n" \
"ds_read_b64_tr_b16 v[130:131], v241 offset:8704\n" \
"ds_read_b64_tr_b16 v[132:133], v242 offset:512\n" \
"buffer_load_dwordx4 v232, s[36:39], s80 offen offset:3072 sc1 lds\n" \
"v_mfma_f32_16x16x32_f16 v[32:35], v[136:139], v[152:155], v[32:35]\n" \
"ds_read_b128 v[152:155], v240 offset:8704\n" \
"v_mfma_f32_16x16x32_f16 v[56:59], v[136:139], v[208:211], v[56:59]\n" \
"ds_read_b64_tr_b16 v[134:135], v242 offset:8704\n" \
"v_mfma_f32_16x16x32_f16 v[68:71], v[140:143], v[208:211], v[68:71]\n" \
"ds_read_b128 v[208:211], v240 offset:512\n" \
"v_mfma_f32_16x16x32_f16 v[92:95], v[140:143], v[156:159], v[92:95]\n" \
"s_add_i32 m0, s45, 0xa000\n" \
"s_add_i32 s81, s47, 0x7400000\n" \
"v_mfma_f32_16x16x32_f16 v[80:83], v[136:139], v[156:159], v[80:83]\n" \
"buffer_load_dwordx4 v232, s[36:39], s81 offen sc1 lds\n" \
"v_mfma_f32_16x16x32_f16 v[104:107], v[136:139], v[160:163], v[104:107]\n" \
"v_mfma_f32_16x16x32_f16 v[116:119], v[140:143], v[160:163], v[116:119]\n" \
"s_setprio 1\n" \
"s_waitcnt vmcnt(9) lgkmcnt(0)\n" \
"s_barrier\n" \
"v_mfma_f32_16x16x32_f16 v[0:3], v[120:123], v[164:167], v[0:3]\n" \
"v_pk_add_f16 v136, v120, v128\n" \
"v_pk_add_f16 v137, v121, v129\n" \
"v_mfma_f32_16x16x32_f16 v[12:15], v[124:127], v[164:167], v[12:15]\n" \
"v_pk_add_f16 v164, v164, v184\n" \
"v_pk_add_f16 v138, v122, v130\n" \
"v_mfma_f32_16x16x32_f16 v[36:39], v[124:127], v[168:171], v[36:39]\n" \
"v_pk_add_f16 v165, v165, v185\n" \
"v_pk_add_f16 v166, v166, v186\n" \
"v_pk_add_f16 v167, v167, v187\n" \
"v_mfma_f32_16x16x32_f16 v[24:27], v[120:123], v[168:171], v[24:27]\n" \
"v_pk_add_f16 v168, v168, v144\n" \
"v_pk_add_f16 v139, v123, v131\n" \
"s_add_i32 m0, s44, 0x0\n" \
"s_add_i32 s80, s46, 0x7800000\n" \
"s_bitcmp0_b32 s47, 10\n" \
"s_cselect_b32 s80, 0x80000000, s80\n" \
"v_mfma_f32_16x16x32_f16 v[48:51], v[120:123], v[172:175], v[48:51]\n" \
"v_pk_add_f16 v169, v169, v145\n" \
"v_pk_add_f16 v140, v124, v132\n" \
"buffer_load_dwordx4 v232, s[36:39], s80 offen sc1 lds\n" \
"v_mfma_f32_16x16x32_f16 v[60:63], v[124:127], v[172:175], v[60:63]\n" \
"v_pk_add_f16 v170, v170, v146\n" \
"v_pk_add_f16 v171, v171, v147\n" \
"v_mfma_f32_16x16x32_f16 v[84:87], v[124:127], v[176:179], v[84:87]\n" \
"v_pk_add_f16 v172, v172, v148\n" \
"v_pk_add_f16 v141, v125, v133\n" \
"v_mfma_f32_16x16x32_f16 v[72:75], v[120:123], v[176:179], v[72:75]\n" \
"v_pk_add_f16 v173, v173, v149\n" \
"v_pk_add_f16 v142, v126, v134\n" \
"v_mfma_f32_16x16x32_f16 v[96:99], v[120:123], v[180:183], v[96:99]\n" \
"v_pk_add_f16 v174, v174, v150\n" \
"v_pk_add_f16 v175, v175, v151\n" \
"v_mfma_f32_16x16x32_f16 v[108:111], v[124:127], v[180:183], v[108:111]\n" \
"v_pk_add_f16 v176, v176, v152\n" \
"v_pk_add_f16 v143, v127, v135\n" \
"v_mfma_f32_16x16x32_f16 v[4:7], v[128:131], v[184:187], v[4:7]\n" \
"v_pk_add_f16 v177, v177, v153\n" \
"ds_read_b64_tr_b16 v[120:121], v241 offset:16384\n" \
"buffer_load_dwordx4 v232, s[36:39], s80 offen offset:1024 sc1 lds\n" \
"v_mfma_f32_16x16x32_f16 v[16:19], v[132:135], v[184:187], v[16:19]\n" \
"v_pk_add_f16 v178, v178, v154\n" \
"ds_read_b64_tr_b16 v[122:123], v241 offset:24576\n" \
"v_mfma_f32_16x16x32_f16 v[40:43], v[132:135], v[144:147], v[40:43]\n" \
"v_pk_add_f16 v179, v179, v155\n" \
"ds_read_b128 v[184:187], v240 offset:22528\n" \
"v_mfma_f32_16x16x32_f16 v[28:31], v[128:131], v[144:147], v[28:31]\n" \
"v_pk_add_f16 v180, v180, v208\n" \
"ds_read_b64_tr_b16 v[124:125], v242 offset:16384\n" \
"v_mfma_f32_16x16x32_f16 v[52:55], v[128:131], v[148:151], v[52:55]\n" \
"v_pk_add_f16 v181, v181, v209\n" \
"ds_read_b128 v[144:147], v240 offset:23040\n" \
"v_mfma_f32_16x16x32_f16 v[64:67], v[132:135], v[148:151], v[64:67]\n" \
"v_pk_add_f16 v182, v182, v210\n" \
"v_mfma_f32_16x16x32_f16 v[88:91], v[132:135], v[152:155], v[88:91]\n" \
"v_pk_add_f16 v183, v183, v211\n" \
"ds_read_b128 v[148:151], v240 offset:24576\n" \
"buffer_load_dwordx4 v232, s[36:39], s80 offen offset:2048 sc1 lds\n" \
"v_mfma_f32_16x16x32_f16 v[76:79], v[128:131], v[152:155], v[76:79]\n" \
"s_setprio 0\n" \
"ds_read_b64_tr_b16 v[126:127], v242 offset:24576\n" \
"v_mfma_f32_16x16x32_f16 v[100:103], v[128:131], v[208:211], v[100:103]\n" \
"ds_read_b128 v[152:155], v240 offset:25088\n" \
"v_mfma_f32_16x16x32_f16 v[112:115], v[132:135], v[208:211], v[112:115]\n" \
"ds_read_b64_tr_b16 v[128:129], v241 offset:16896\n" \
"v_mfma_f32_16x16x32_f16 v[8:11], v[136:139], v[164:167], v[8:11]\n" \
"ds_read_b64_tr_b16 v[130:131], v241 offset:25088\n" \
"v_mfma_f32_16x16x32_f16 v[20:23], v[140:143], v[164:167], v[20:23]\n" \
"ds_read_b64_tr_b16 v[132:133], v242 offset:16896\n" \
"v_mfma_f32_16x16x32_f16 v[44:47], v[140:143], v[168:171], v[44:47]\n" \
"v_add_u32 v243, 0x1b000, v241\n" \
"ds_read_b64_tr_b16 v[134:135], v242 offset:25088\n" \
"buffer_load_dwordx4 v232, s[36:39], s80 offen offset:3072 sc1 lds\n" \
"v_mfma_f32_16x16x32_f16 v[32:35], v[136:139], v[168:171], v[32:35]\n" \
"v_xor_b32 v244, 32, v243\n" \
"v_mfma_f32_16x16x32_f16 v[56:59], v[136:139], v[172:175], v[56:59]\n" \
"v_mfma_f32_16x16x32_f16 v[68:71], v[140:143], v[172:175], v[68:71]\n" \
"v_add_u32 v246, 0x15000, v240\n" \
"v_mfma_f32_16x16x32_f16 v[92:95], v[140:143], v[176:179], v[92:95]\n" \
"s_add_i32 m0, s45, 0x0\n" \
"s_add_i32 s81, s47, 0x7800000\n" \
"v_mfma_f32_16x16x32_f16 v[80:83], v[136:139], v[176:179], v[80:83]\n" \
"buffer_load_dwordx4 v232, s[36:39], s81 offen sc1 lds\n" \
"v_mfma_f32_16x16x32_f16 v[104:107], v[136:139], v[180:183], v[104:107]\n" \
"s_mov_b32 m0, 0x27c00\n" \
"v_mfma_f32_16x16x32_f16 v[116:119], v[140:143], v[180:183], v[116:119]\n" \
"global_load_lds_dword v[248:249], off\n" \
"s_waitcnt vmcnt(6) lgkmcnt(0)\n" \
"s_barrier\n" \
"v_mfma_f32_16x16x32_f16 v[0:3], v[120:123], v[184:187], v[0:3]\n" \
"v_pk_add_f16 v136, v120, v128\n" \
"v_pk_add_f16 v137, v121, v129\n" \
"v_pk_add_f16 v138, v122, v130\n" \
"v_pk_add_f16 v139, v123, v131\n" \
"ds_read_b128 v[156:159], v240 offset:43008\n" \
"ds_read_b64_tr_b16 v[164:165], v241 offset:32768\n" \
"v_mfma_f32_16x16x32_f16 v[12:15], v[124:127], v[184:187], v[12:15]\n" \
"v_pk_add_f16 v184, v184, v144\n" \
"v_pk_add_f16 v185, v185, v145\n" \
"v_pk_add_f16 v140, v124, v132\n" \
"ds_read_b64_tr_b16 v[166:167], v241 offset:40960\n" \
"ds_read_b64_tr_b16 v[168:169], v242 offset:32768\n" \
"v_mfma_f32_16x16x32_f16 v[4:7], v[128:131], v[144:147], v[4:7]\n" \
"v_pk_add_f16 v186, v186, v146\n" \
"v_pk_add_f16 v187, v187, v147\n" \
"v_pk_add_f16 v141, v125, v133\n" \
"s_add_i32 m0, s44, 0xffff7000\n" \
"s_add_i32 s80, s46, 0x7c00000\n" \
"s_bitcmp1_b32 s47, 10\n" \
"s_cselect_b64 exec, -1, 0\n" \
"buffer_load_dwordx4 v232, s[36:39], s80 offen sc1 lds\n" \
"buffer_load_dwordx4 v232, s[36:39], s80 offen offset:1024 sc1 lds\n" \
"buffer_load_dwordx4 v232, s[36:39], s80 offen offset:2048 sc1 lds\n" \
"buffer_load_dwordx4 v232, s[36:39], s80 offen offset:3072 sc1 lds\n" \
"s_mov_b64 exec, -1\n" \
"v_mfma_f32_16x16x32_f16 v[16:19], v[132:135], v[144:147], v[16:19]\n" \
"v_pk_add_f16 v142, v126, v134\n" \
"v_pk_add_f16 v143, v127, v135\n" \
"ds_read_b128 v[160:163], v240 offset:43520\n" \
"ds_read_b64_tr_b16 v[170:171], v242 offset:40960\n" \
"ds_read_b64_tr_b16 v[172:173], v241 offset:33280\n" \
"v_mfma_f32_16x16x32_f16 v[8:11], v[136:139], v[184:187], v[8:11]\n" \
"s_add_i32 m0, s45, 0xffff7000\n" \
"s_add_i32 s81, s47, 0x7c00000\n" \
"ds_read_b64_tr_b16 v[174:175], v241 offset:41472\n" \
"v_mfma_f32_16x16x32_f16 v[20:23], v[140:143], v[184:187], v[20:23]\n" \
"buffer_load_dwordx4 v232, s[36:39], s81 offen sc1 lds\n" \
"ds_read_b64_tr_b16 v[176:177], v242 offset:33280\n" \
"ds_read_b64_tr_b16 v[178:179], v242 offset:41472\n" \
"v_mfma_f32_16x16x32_f16 v[24:27], v[120:123], v[148:151], v[24:27]\n" \
"ds_read_b128 v[184:187], v246 offset:2048\n" \
"ds_read_b64_tr_b16 v[188:189], v243 offset:0\n" \
"ds_read_b64_tr_b16 v[190:191], v243 offset:8192\n" \
"v_mfma_f32_16x16x32_f16 v[36:39], v[124:127], v[148:151], v[36:39]\n" \
"v_pk_add_f16 v148, v148, v152\n" \
"v_pk_add_f16 v149, v149, v153\n" \
"ds_read_b64_tr_b16 v[192:193], v244 offset:0\n" \
"v_mfma_f32_16x16x32_f16 v[28:31], v[128:131], v[152:155], v[28:31]\n" \
"v_pk_add_f16 v150, v150, v154\n" \
"v_pk_add_f16 v151, v151, v155\n" \
"v_mfma_f32_16x16x32_f16 v[40:43], v[132:135], v[152:155], v[40:43]\n" \
"s_waitcnt lgkmcnt(7)\n" \
"v_pk_add_f16 v180, v164, v172\n" \
"ds_read_b128 v[144:147], v246 offset:2560\n" \
"ds_read_b64_tr_b16 v[194:195], v244 offset:8192\n" \
"ds_read_b64_tr_b16 v[196:197], v243 offset:512\n" \
"v_mfma_f32_16x16x32_f16 v[32:35], v[136:139], v[148:151], v[32:35]\n" \
"v_pk_add_f16 v181, v165, v173\n" \
"s_waitcnt lgkmcnt(9)\n" \
"v_pk_add_f16 v182, v166, v174\n" \
"ds_read_b64_tr_b16 v[198:199], v243 offset:8704\n" \
"v_mfma_f32_16x16x32_f16 v[44:47], v[140:143], v[148:151], v[44:47]\n" \
"v_pk_add_f16 v183, v167, v175\n" \
"s_waitcnt lgkmcnt(8)\n" \
"v_pk_add_f16 v236, v168, v176\n" \
"ds_read_b64_tr_b16 v[200:201], v244 offset:512\n" \
"ds_read_b64_tr_b16 v[202:203], v244 offset:8704\n" \
"v_mfma_f32_16x16x32_f16 v[0:3], v[164:167], v[156:159], v[0:3]\n" \
"v_pk_add_f16 v237, v169, v177\n" \
"ds_read_b128 v[148:151], v246 offset:10240\n" \
"ds_read_b64_tr_b16 v[212:213], v243 offset:16384\n" \
"ds_read_b64_tr_b16 v[214:215], v243 offset:24576\n" \
"v_mfma_f32_16x16x32_f16 v[12:15], v[168:171], v[156:159], v[12:15]\n" \
"v_pk_add_f16 v156, v156, v160\n" \
"v_pk_add_f16 v157, v157, v161\n" \
"ds_read_b64_tr_b16 v[216:217], v244 offset:16384\n" \
"v_mfma_f32_16x16x32_f16 v[4:7], v[172:175], v[160:163], v[4:7]\n" \
"v_pk_add_f16 v158, v158, v162\n" \
"v_pk_add_f16 v159, v159, v163\n" \
"v_pk_add_f16 v238, v170, v178\n" \
"v_pk_add_f16 v239, v171, v179\n" \
"v_mfma_f32_16x16x32_f16 v[16:19], v[176:179], v[160:163], v[16:19]\n" \
"s_waitcnt lgkmcnt(7)\n" \
"v_pk_add_f16 v204, v188, v196\n" \
"ds_read_b128 v[152:155], v246 offset:10752\n" \
"ds_read_b64_tr_b16 v[218:219], v244 offset:24576\n" \
"ds_read_b64_tr_b16 v[220:221], v243 offset:16896\n" \
"v_mfma_f32_16x16x32_f16 v[8:11], v[180:183], v[156:159], v[8:11]\n" \
"v_pk_add_f16 v205, v189, v197\n" \
"s_waitcnt lgkmcnt(8)\n" \
"v_pk_add_f16 v206, v190, v198\n" \
"ds_read_b64_tr_b16 v[222:223], v243 offset:25088\n" \
"v_mfma_f32_16x16x32_f16 v[20:23], v[236:239], v[156:159], v[20:23]\n" \
"ds_read_b64_tr_b16 v[224:225], v244 offset:16896\n" \
"ds_read_b64_tr_b16 v[226:227], v244 offset:25088\n" \
"v_mfma_f32_16x16x32_f16 v[0:3], v[188:191], v[184:187], v[0:3]\n" \
"v_pk_add_f16 v207, v191, v199\n" \
"v_pk_add_f16 v208, v192, v200\n" \
"ds_read_b128 v[156:159], v240 offset:45056\n" \
"v_mfma_f32_16x16x32_f16 v[12:15], v[192:195], v[184:187], v[12:15]\n" \
"v_pk_add_f16 v184, v184, v144\n" \
"v_pk_add_f16 v185, v185, v145\n" \
"v_pk_add_f16 v209, v193, v201\n" \
"v_mfma_f32_16x16x32_f16 v[4:7], v[196:199], v[144:147], v[4:7]\n" \
"v_pk_add_f16 v186, v186, v146\n" \
"v_pk_add_f16 v187, v187, v147\n" \
"s_waitcnt lgkmcnt(7)\n" \
"v_pk_add_f16 v210, v194, v202\n" \
"v_pk_add_f16 v211, v195, v203\n" \
"v_mfma_f32_16x16x32_f16 v[16:19], v[200:203], v[144:147], v[16:19]\n" \
"s_waitcnt lgkmcnt(4)\n" \
"v_pk_add_f16 v228, v212, v220\n" \
"ds_read_b128 v[160:163], v240 offset:45568\n" \
"v_mfma_f32_16x16x32_f16 v[8:11], v[204:207], v[184:187], v[8:11]\n" \
"v_pk_add_f16 v229, v213, v221\n" \
"v_mfma_f32_16x16x32_f16 v[20:23], v[208:211], v[184:187], v[20:23]\n" \
"s_waitcnt lgkmcnt(4)\n" \
"v_pk_add_f16 v230, v214, v222\n" \
"v_pk_add_f16 v231, v215, v223\n" \
"v_mfma_f32_16x16x32_f16 v[0:3], v[212:215], v[148:151], v[0:3]\n" \
"s_waitcnt lgkmcnt(2)\n" \
"v_pk_add_f16 v232, v216, v224\n" \
"v_pk_add_f16 v233, v217, v225\n" \
"ds_read_b128 v[184:187], v246 offset:4096\n" \
"v_mfma_f32_16x16x32_f16 v[12:15], v[216:219], v[148:151], v[12:15]\n" \
"v_pk_add_f16 v148, v148, v152\n" \
"v_pk_add_f16 v149, v149, v153\n" \
"v_pk_add_f16 v234, v218, v226\n" \
"v_mfma_f32_16x16x32_f16 v[4:7], v[220:223], v[152:155], v[4:7]\n" \
"v_pk_add_f16 v150, v150, v154\n" \
"v_pk_add_f16 v151, v151, v155\n" \
"v_pk_add_f16 v235, v219, v227\n" \
"v_mfma_f32_16x16x32_f16 v[16:19], v[224:227], v[152:155], v[16:19]\n" \
"ds_read_b128 v[144:147], v246 offset:4608\n" \
"v_mfma_f32_16x16x32_f16 v[8:11], v[228:231], v[148:151], v[8:11]\n" \
"v_mfma_f32_16x16x32_f16 v[20:23], v[232:235], v[148:151], v[20:23]\n" \
"v_add_u32 v241, 0xffff7000, v240\n" \
"s_waitcnt lgkmcnt(2)\n" \
"v_mfma_f32_16x16x32_f16 v[24:27], v[164:167], v[156:159], v[24:27]\n" \
"ds_read_b128 v[148:151], v246 offset:12288\n" \
"v_mfma_f32_16x16x32_f16 v[36:39], v[168:171], v[156:159], v[36:39]\n" \
"v_pk_add_f16 v156, v156, v160\n" \
"v_pk_add_f16 v157, v157, v161\n" \
"v_mfma_f32_16x16x32_f16 v[28:31], v[172:175], v[160:163], v[28:31]\n" \
"v_pk_add_f16 v158, v158, v162\n" \
"v_pk_add_f16 v159, v159, v163\n" \
"v_mfma_f32_16x16x32_f16 v[40:43], v[176:179], v[160:163], v[40:43]\n" \
"ds_read_b128 v[152:155], v246 offset:12800\n" \
"v_fmac_f32 v0, s56, v8\n" \
"v_fmac_f32 v1, s56, v9\n" \
"v_fmac_f32 v2, s56, v10\n" \
"v_fmac_f32 v3, s56, v11\n" \
"v_mfma_f32_16x16x32_f16 v[32:35], v[180:183], v[156:159], v[32:35]\n" \
"v_fmac_f32 v12, s56, v20\n" \
"v_fmac_f32 v13, s56, v21\n" \
"v_fmac_f32 v14, s56, v22\n" \
"v_fmac_f32 v15, s56, v23\n" \
"v_mfma_f32_16x16x32_f16 v[44:47], v[236:239], v[156:159], v[44:47]\n" \
"v_cvt_pk_bf16_f32 v0, v0, v1\n" \
"v_cvt_pk_bf16_f32 v1, v2, v3\n" \
"v_cvt_pk_bf16_f32 v2, v12, v13\n" \
"v_cvt_pk_bf16_f32 v3, v14, v15\n" \
"s_waitcnt lgkmcnt(2)\n" \
"v_mfma_f32_16x16x32_f16 v[24:27], v[188:191], v[184:187], v[24:27]\n" \
"ds_read_b128 v[156:159], v240 offset:26624\n" \
"buffer_store_dwordx4 v[0:3], v245, s[40:43], s69 offen sc1\n" \
"v_fmac_f32 v4, s56, v8\n" \
"v_fmac_f32 v5, s56, v9\n" \
"v_fmac_f32 v6, s56, v10\n" \
"v_fmac_f32 v7, s56, v11\n" \
"v_mfma_f32_16x16x32_f16 v[36:39], v[192:195], v[184:187], v[36:39]\n" \
"v_pk_add_f16 v184, v184, v144\n" \
"v_pk_add_f16 v185, v185, v145\n" \
"v_fmac_f32 v16, s56, v20\n" \
"v_fmac_f32 v17, s56, v21\n" \
"v_fmac_f32 v18, s56, v22\n" \
"v_fmac_f32 v19, s56, v23\n" \
"v_mfma_f32_16x16x32_f16 v[28:31], v[196:199], v[144:147], v[28:31]\n" \
"v_pk_add_f16 v186, v186, v146\n" \
"v_pk_add_f16 v187, v187, v147\n" \
"v_cvt_pk_bf16_f32 v4, v4, v5\n" \
"v_cvt_pk_bf16_f32 v5, v6, v7\n" \
"v_cvt_pk_bf16_f32 v6, v16, v17\n" \
"v_cvt_pk_bf16_f32 v7, v18, v19\n" \
"buffer_store_dwordx4 v[4:7], v245, s[40:43], s69 offen offset:128 sc1\n" \
"v_mfma_f32_16x16x32_f16 v[40:43], v[200:203], v[144:147], v[40:43]\n" \
"ds_read_b128 v[160:163], v240 offset:27136\n" \
"v_mfma_f32_16x16x32_f16 v[32:35], v[204:207], v[184:187], v[32:35]\n" \
"v_mfma_f32_16x16x32_f16 v[44:47], v[208:211], v[184:187], v[44:47]\n" \
"s_waitcnt lgkmcnt(1)\n" \
"v_mfma_f32_16x16x32_f16 v[24:27], v[212:215], v[148:151], v[24:27]\n" \
"ds_read_b128 v[184:187], v240 offset:47104\n" \
"v_mfma_f32_16x16x32_f16 v[36:39], v[216:219], v[148:151], v[36:39]\n" \
"v_pk_add_f16 v148, v148, v152\n" \
"v_pk_add_f16 v149, v149, v153\n" \
"v_mfma_f32_16x16x32_f16 v[28:31], v[220:223], v[152:155], v[28:31]\n" \
"v_pk_add_f16 v150, v150, v154\n" \
"v_pk_add_f16 v151, v151, v155\n" \
"v_mfma_f32_16x16x32_f16 v[40:43], v[224:227], v[152:155], v[40:43]\n" \
"ds_read_b128 v[144:147], v240 offset:47616\n" \
"v_mfma_f32_16x16x32_f16 v[32:35], v[228:231], v[148:151], v[32:35]\n" \
"s_waitcnt vmcnt(2)\n" \
"s_barrier\n" \
"v_mfma_f32_16x16x32_f16 v[44:47], v[232:235], v[148:151], v[44:47]\n" \
"v_mfma_f32_16x16x32_f16 v[48:51], v[120:123], v[156:159], v[48:51]\n" \
"ds_read_b128 v[148:151], v240 offset:6144\n" \
"v_mfma_f32_16x16x32_f16 v[60:63], v[124:127], v[156:159], v[60:63]\n" \
"s_waitcnt lgkmcnt(3)\n" \
"v_pk_add_f16 v156, v156, v160\n" \
"v_pk_add_f16 v157, v157, v161\n" \
"v_mfma_f32_16x16x32_f16 v[52:55], v[128:131], v[160:163], v[52:55]\n" \
"v_pk_add_f16 v158, v158, v162\n" \
"v_pk_add_f16 v159, v159, v163\n" \
"v_mfma_f32_16x16x32_f16 v[64:67], v[132:135], v[160:163], v[64:67]\n" \
"ds_read_b128 v[152:155], v240 offset:6656\n" \
"v_fmac_f32 v24, s56, v32\n" \
"v_fmac_f32 v25, s56, v33\n" \
"v_fmac_f32 v26, s56, v34\n" \
"v_mfma_f32_16x16x32_f16 v[56:59], v[136:139], v[156:159], v[56:59]\n" \
"v_fmac_f32 v27, s56, v35\n" \
"v_fmac_f32 v36, s56, v44\n" \
"v_fmac_f32 v37, s56, v45\n" \
"v_mfma_f32_16x16x32_f16 v[68:71], v[140:143], v[156:159], v[68:71]\n" \
"v_fmac_f32 v38, s56, v46\n" \
"v_fmac_f32 v39, s56, v47\n" \
"v_cvt_pk_bf16_f32 v24, v24, v25\n" \
"s_waitcnt lgkmcnt(2)\n" \
"v_mfma_f32_16x16x32_f16 v[48:51], v[164:167], v[184:187], v[48:51]\n" \
"ds_read_b128 v[156:159], v241 offset:6144\n" \
"v_cvt_pk_bf16_f32 v25, v26, v27\n" \
"v_cvt_pk_bf16_f32 v26, v36, v37\n" \
"v_cvt_pk_bf16_f32 v27, v38, v39\n" \
"v_mfma_f32_16x16x32_f16 v[60:63], v[168:171], v[184:187], v[60:63]\n" \
"v_pk_add_f16 v184, v184, v144\n" \
"v_pk_add_f16 v185, v185, v145\n" \
"buffer_store_dwordx4 v[24:27], v245, s[40:43], s70 offen sc1\n" \
"v_fmac_f32 v28, s56, v32\n" \
"v_fmac_f32 v29, s56, v33\n" \
"v_fmac_f32 v30, s56, v34\n" \
"v_mfma_f32_16x16x32_f16 v[52:55], v[172:175], v[144:147], v[52:55]\n" \
"v_pk_add_f16 v186, v186, v146\n" \
"v_pk_add_f16 v187, v187, v147\n" \
"v_fmac_f32 v31, s56, v35\n" \
"v_fmac_f32 v40, s56, v44\n" \
"v_fmac_f32 v41, s56, v45\n" \
"v_mfma_f32_16x16x32_f16 v[64:67], v[176:179], v[144:147], v[64:67]\n" \
"ds_read_b128 v[160:163], v241 offset:6656\n" \
"v_fmac_f32 v42, s56, v46\n" \
"v_fmac_f32 v43, s56, v47\n" \
"v_cvt_pk_bf16_f32 v28, v28, v29\n" \
"v_mfma_f32_16x16x32_f16 v[56:59], v[180:183], v[184:187], v[56:59]\n" \
"v_cvt_pk_bf16_f32 v29, v30, v31\n" \
"v_cvt_pk_bf16_f32 v30, v40, v41\n" \
"v_cvt_pk_bf16_f32 v31, v42, v43\n" \
"buffer_store_dwordx4 v[28:31], v245, s[40:43], s70 offen offset:128 sc1\n" \
"v_mfma_f32_16x16x32_f16 v[68:71], v[236:239], v[184:187], v[68:71]\n" \
"s_waitcnt lgkmcnt(1)\n" \
"v_mfma_f32_16x16x32_f16 v[48:51], v[188:191], v[148:151], v[48:51]\n" \
"ds_read_b128 v[184:187], v240 offset:28672\n" \
"v_mfma_f32_16x16x32_f16 v[60:63], v[192:195], v[148:151], v[60:63]\n" \
"v_pk_add_f16 v148, v148, v152\n" \
"v_pk_add_f16 v149, v149, v153\n" \
"v_mfma_f32_16x16x32_f16 v[52:55], v[196:199], v[152:155], v[52:55]\n" \
"v_pk_add_f16 v150, v150, v154\n" \
"v_pk_add_f16 v151, v151, v155\n" \
"v_mfma_f32_16x16x32_f16 v[64:67], v[200:203], v[152:155], v[64:67]\n" \
"ds_read_b128 v[144:147], v240 offset:29184\n" \
"v_mfma_f32_16x16x32_f16 v[56:59], v[204:207], v[148:151], v[56:59]\n" \
"v_mfma_f32_16x16x32_f16 v[68:71], v[208:211], v[148:151], v[68:71]\n" \
"v_mfma_f32_16x16x32_f16 v[48:51], v[212:215], v[156:159], v[48:51]\n" \
"ds_read_b128 v[148:151], v240 offset:49152\n" \
"v_mfma_f32_16x16x32_f16 v[60:63], v[216:219], v[156:159], v[60:63]\n" \
"s_waitcnt lgkmcnt(3)\n" \
"v_pk_add_f16 v156, v156, v160\n" \
"v_pk_add_f16 v157, v157, v161\n" \
"v_mfma_f32_16x16x32_f16 v[52:55], v[220:223], v[160:163], v[52:55]\n" \
"v_pk_add_f16 v158, v158, v162\n" \
"v_pk_add_f16 v159, v159, v163\n" \
"v_mfma_f32_16x16x32_f16 v[64:67], v[224:227], v[160:163], v[64:67]\n" \
"ds_read_b128 v[152:155], v240 offset:49664\n" \
"v_mfma_f32_16x16x32_f16 v[56:59], v[228:231], v[156:159], v[56:59]\n" \
"v_mfma_f32_16x16x32_f16 v[68:71], v[232:235], v[156:159], v[68:71]\n" \
"s_waitcnt lgkmcnt(3)\n" \
"v_mfma_f32_16x16x32_f16 v[72:75], v[120:123], v[184:187], v[72:75]\n" \
"ds_read_b128 v[156:159], v240 offset:8192\n" \
"v_mfma_f32_16x16x32_f16 v[84:87], v[124:127], v[184:187], v[84:87]\n" \
"s_waitcnt lgkmcnt(3)\n" \
"v_pk_add_f16 v184, v184, v144\n" \
"v_pk_add_f16 v185, v185, v145\n" \
"v_mfma_f32_16x16x32_f16 v[76:79], v[128:131], v[144:147], v[76:79]\n" \
"v_pk_add_f16 v186, v186, v146\n" \
"v_pk_add_f16 v187, v187, v147\n" \
"v_mfma_f32_16x16x32_f16 v[88:91], v[132:135], v[144:147], v[88:91]\n" \
"ds_read_b128 v[160:163], v240 offset:8704\n" \
"v_fmac_f32 v48, s56, v56\n" \
"v_fmac_f32 v49, s56, v57\n" \
"v_mfma_f32_16x16x32_f16 v[80:83], v[136:139], v[184:187], v[80:83]\n" \
"v_fmac_f32 v50, s56, v58\n" \
"v_fmac_f32 v51, s56, v59\n" \
"v_fmac_f32 v60, s56, v68\n" \
"v_mfma_f32_16x16x32_f16 v[92:95], v[140:143], v[184:187], v[92:95]\n" \
"v_fmac_f32 v61, s56, v69\n" \
"v_fmac_f32 v62, s56, v70\n" \
"s_waitcnt lgkmcnt(2)\n" \
"v_mfma_f32_16x16x32_f16 v[72:75], v[164:167], v[148:151], v[72:75]\n" \
"ds_read_b128 v[184:187], v241 offset:8192\n" \
"v_fmac_f32 v63, s56, v71\n" \
"v_cvt_pk_bf16_f32 v48, v48, v49\n" \
"v_cvt_pk_bf16_f32 v49, v50, v51\n" \
"v_mfma_f32_16x16x32_f16 v[84:87], v[168:171], v[148:151], v[84:87]\n" \
"v_pk_add_f16 v148, v148, v152\n" \
"v_pk_add_f16 v149, v149, v153\n" \
"v_cvt_pk_bf16_f32 v50, v60, v61\n" \
"v_cvt_pk_bf16_f32 v51, v62, v63\n" \
"v_mfma_f32_16x16x32_f16 v[76:79], v[172:175], v[152:155], v[76:79]\n" \
"v_pk_add_f16 v150, v150, v154\n" \
"v_pk_add_f16 v151, v151, v155\n" \
"buffer_store_dwordx4 v[48:51], v245, s[40:43], s71 offen sc1\n" \
"v_fmac_f32 v52, s56, v56\n" \
"v_fmac_f32 v53, s56, v57\n" \
"v_mfma_f32_16x16x32_f16 v[88:91], v[176:179], v[152:155], v[88:91]\n" \
"ds_read_b128 v[144:147], v241 offset:8704\n" \
"v_fmac_f32 v54, s56, v58\n" \
"v_fmac_f32 v55, s56, v59\n" \
"v_fmac_f32 v64, s56, v68\n" \
"v_mfma_f32_16x16x32_f16 v[80:83], v[180:183], v[148:151], v[80:83]\n" \
"v_fmac_f32 v65, s56, v69\n" \
"v_fmac_f32 v66, s56, v70\n" \
"v_mfma_f32_16x16x32_f16 v[92:95], v[236:239], v[148:151], v[92:95]\n" \
"v_fmac_f32 v67, s56, v71\n" \
"v_cvt_pk_bf16_f32 v52, v52, v53\n" \
"v_cvt_pk_bf16_f32 v53, v54, v55\n" \
"s_waitcnt lgkmcnt(1)\n" \
"v_mfma_f32_16x16x32_f16 v[72:75], v[188:191], v[156:159], v[72:75]\n" \
"ds_read_b128 v[148:151], v240 offset:20480\n" \
"v_cvt_pk_bf16_f32 v54, v64, v65\n" \
"v_cvt_pk_bf16_f32 v55, v66, v67\n" \
"buffer_store_dwordx4 v[52:55], v245, s[40:43], s71 offen offset:128 sc1\n" \
"v_mfma_f32_16x16x32_f16 v[84:87], v[192:195], v[156:159], v[84:87]\n" \
"v_pk_add_f16 v156, v156, v160\n" \
"v_pk_add_f16 v157, v157, v161\n" \
"v_mfma_f32_16x16x32_f16 v[76:79], v[196:199], v[160:163], v[76:79]\n" \
"v_pk_add_f16 v158, v158, v162\n" \
"v_pk_add_f16 v159, v159, v163\n" \
"v_mfma_f32_16x16x32_f16 v[88:91], v[200:203], v[160:163], v[88:91]\n" \
"ds_read_b128 v[152:155], v240 offset:20992\n" \
"v_mfma_f32_16x16x32_f16 v[80:83], v[204:207], v[156:159], v[80:83]\n" \
"v_mfma_f32_16x16x32_f16 v[92:95], v[208:211], v[156:159], v[92:95]\n" \
"v_mfma_f32_16x16x32_f16 v[72:75], v[212:215], v[184:187], v[72:75]\n" \
"ds_read_b128 v[156:159], v240 offset:40960\n" \
"v_mfma_f32_16x16x32_f16 v[84:87], v[216:219], v[184:187], v[84:87]\n" \
"s_waitcnt lgkmcnt(3)\n" \
"v_pk_add_f16 v184, v184, v144\n" \
"v_pk_add_f16 v185, v185, v145\n" \
"v_mfma_f32_16x16x32_f16 v[76:79], v[220:223], v[144:147], v[76:79]\n" \
"v_pk_add_f16 v186, v186, v146\n" \
"v_pk_add_f16 v187, v187, v147\n" \
"v_mfma_f32_16x16x32_f16 v[88:91], v[224:227], v[144:147], v[88:91]\n" \
"ds_read_b128 v[160:163], v240 offset:41472\n" \
"v_mfma_f32_16x16x32_f16 v[80:83], v[228:231], v[184:187], v[80:83]\n" \
"v_mfma_f32_16x16x32_f16 v[92:95], v[232:235], v[184:187], v[92:95]\n" \
"s_waitcnt lgkmcnt(3)\n" \
"v_mfma_f32_16x16x32_f16 v[96:99], v[120:123], v[148:151], v[96:99]\n" \
"ds_read_b128 v[184:187], v240 offset:0\n" \
"v_mfma_f32_16x16x32_f16 v[108:111], v[124:127], v[148:151], v[108:111]\n" \
"s_waitcnt lgkmcnt(3)\n" \
"v_pk_add_f16 v148, v148, v152\n" \
"v_pk_add_f16 v149, v149, v153\n" \
"v_mfma_f32_16x16x32_f16 v[100:103], v[128:131], v[152:155], v[100:103]\n" \
"v_pk_add_f16 v150, v150, v154\n" \
"v_pk_add_f16 v151, v151, v155\n" \
"v_mfma_f32_16x16x32_f16 v[112:115], v[132:135], v[152:155], v[112:115]\n" \
"ds_read_b128 v[144:147], v240 offset:512\n" \
"v_fmac_f32 v72, s56, v80\n" \
"v_fmac_f32 v73, s56, v81\n" \
"v_mfma_f32_16x16x32_f16 v[104:107], v[136:139], v[148:151], v[104:107]\n" \
"v_fmac_f32 v74, s56, v82\n" \
"v_fmac_f32 v75, s56, v83\n" \
"v_fmac_f32 v84, s56, v92\n" \
"v_mfma_f32_16x16x32_f16 v[116:119], v[140:143], v[148:151], v[116:119]\n" \
"v_fmac_f32 v85, s56, v93\n" \
"v_fmac_f32 v86, s56, v94\n" \
"s_waitcnt lgkmcnt(2)\n" \
"v_mfma_f32_16x16x32_f16 v[96:99], v[164:167], v[156:159], v[96:99]\n" \
"ds_read_b128 v[148:151], v241 offset:0\n" \
"v_fmac_f32 v87, s56, v95\n" \
"v_cvt_pk_bf16_f32 v72, v72, v73\n" \
"v_cvt_pk_bf16_f32 v73, v74, v75\n" \
"v_mfma_f32_16x16x32_f16 v[108:111], v[168:171], v[156:159], v[108:111]\n" \
"v_pk_add_f16 v156, v156, v160\n" \
"v_pk_add_f16 v157, v157, v161\n" \
"v_cvt_pk_bf16_f32 v74, v84, v85\n" \
"v_cvt_pk_bf16_f32 v75, v86, v87\n" \
"v_mfma_f32_16x16x32_f16 v[100:103], v[172:175], v[160:163], v[100:103]\n" \
"v_pk_add_f16 v158, v158, v162\n" \
"v_pk_add_f16 v159, v159, v163\n" \
"buffer_store_dwordx4 v[72:75], v245, s[40:43], s72 offen sc1\n" \
"v_fmac_f32 v76, s56, v80\n" \
"v_fmac_f32 v77, s56, v81\n" \
"v_mfma_f32_16x16x32_f16 v[112:115], v[176:179], v[160:163], v[112:115]\n" \
"ds_read_b128 v[152:155], v241 offset:512\n" \
"v_fmac_f32 v78, s56, v82\n" \
"v_fmac_f32 v79, s56, v83\n" \
"v_fmac_f32 v88, s56, v92\n" \
"v_mfma_f32_16x16x32_f16 v[104:107], v[180:183], v[156:159], v[104:107]\n" \
"v_fmac_f32 v89, s56, v93\n" \
"v_fmac_f32 v90, s56, v94\n" \
"v_mfma_f32_16x16x32_f16 v[116:119], v[236:239], v[156:159], v[116:119]\n" \
"v_fmac_f32 v91, s56, v95\n" \
"v_cvt_pk_bf16_f32 v76, v76, v77\n" \
"v_cvt_pk_bf16_f32 v77, v78, v79\n" \
"s_waitcnt lgkmcnt(1)\n" \
"v_mfma_f32_16x16x32_f16 v[96:99], v[188:191], v[184:187], v[96:99]\n" \
"v_cvt_pk_bf16_f32 v78, v88, v89\n" \
"v_cvt_pk_bf16_f32 v79, v90, v91\n" \
"buffer_store_dwordx4 v[76:79], v245, s[40:43], s72 offen offset:128 sc1\n" \
"v_mfma_f32_16x16x32_f16 v[108:111], v[192:195], v[184:187], v[108:111]\n" \
"v_pk_add_f16 v184, v184, v144\n" \
"v_pk_add_f16 v185, v185, v145\n" \
"v_mfma_f32_16x16x32_f16 v[100:103], v[196:199], v[144:147], v[100:103]\n" \
"v_pk_add_f16 v186, v186, v146\n" \
"v_pk_add_f16 v187, v187, v147\n" \
"v_mfma_f32_16x16x32_f16 v[112:115], v[200:203], v[144:147], v[112:115]\n" \
"s_nop 0\n" \
"v_mfma_f32_16x16x32_f16 v[104:107], v[204:207], v[184:187], v[104:107]\n" \
"v_mfma_f32_16x16x32_f16 v[116:119], v[208:211], v[184:187], v[116:119]\n" \
"v_mfma_f32_16x16x32_f16 v[96:99], v[212:215], v[148:151], v[96:99]\n" \
"v_mfma_f32_16x16x32_f16 v[108:111], v[216:219], v[148:151], v[108:111]\n" \
"s_waitcnt lgkmcnt(0)\n" \
"v_pk_add_f16 v148, v148, v152\n" \
"v_pk_add_f16 v149, v149, v153\n" \
"v_mfma_f32_16x16x32_f16 v[100:103], v[220:223], v[152:155], v[100:103]\n" \
"v_pk_add_f16 v150, v150, v154\n" \
"v_pk_add_f16 v151, v151, v155\n" \
"v_mfma_f32_16x16x32_f16 v[112:115], v[224:227], v[152:155], v[112:115]\n" \
"s_nop 0\n" \
"v_mfma_f32_16x16x32_f16 v[104:107], v[228:231], v[148:151], v[104:107]\n" \
"v_mfma_f32_16x16x32_f16 v[116:119], v[232:235], v[148:151], v[116:119]\n" \
"s_nop 7\n" \
"v_fmac_f32 v96, s56, v104\n" \
"v_fmac_f32 v97, s56, v105\n" \
"v_fmac_f32 v98, s56, v106\n" \
"v_fmac_f32 v99, s56, v107\n" \
"v_fmac_f32 v108, s56, v116\n" \
"v_fmac_f32 v109, s56, v117\n" \
"v_fmac_f32 v110, s56, v118\n" \
"v_fmac_f32 v111, s56, v119\n" \
"v_cvt_pk_bf16_f32 v96, v96, v97\n" \
"v_cvt_pk_bf16_f32 v97, v98, v99\n" \
"v_cvt_pk_bf16_f32 v98, v108, v109\n" \
"v_cvt_pk_bf16_f32 v99, v110, v111\n" \
"buffer_store_dwordx4 v[96:99], v245, s[40:43], s73 offen sc1\n" \
"v_fmac_f32 v100, s56, v104\n" \
"v_fmac_f32 v101, s56, v105\n" \
"v_fmac_f32 v102, s56, v106\n" \
"v_fmac_f32 v103, s56, v107\n" \
"v_fmac_f32 v112, s56, v116\n" \
"v_fmac_f32 v113, s56, v117\n" \
"v_fmac_f32 v114, s56, v118\n" \
"v_fmac_f32 v115, s56, v119\n" \
"v_cvt_pk_bf16_f32 v100, v100, v101\n" \
"v_cvt_pk_bf16_f32 v101, v102, v103\n" \
"v_cvt_pk_bf16_f32 v102, v112, v113\n" \
"v_cvt_pk_bf16_f32 v103, v114, v115\n" \
"buffer_store_dwordx4 v[100:103], v245, s[40:43], s73 offen offset:128 sc1\n" \
"s_waitcnt vmcnt(0)\n" \
"s_barrier\n" \
"s_branch .Lcf_e\n" \
".Ls12_%=:\n" \
"s_mov_b32 s87, 0x200000\n" \
"s_mov_b32 s86, 0x8004000\n" \
".Lt12_%=:\n" \
"s_sleep 3\n" \
"buffer_load_dword v253, v252, s[36:39], s86 offen sc1\n" \
"s_waitcnt vmcnt(0)\n" \
"s_sub_u32 s87, s87, 1\n" \
"s_cbranch_scc1 .Lr12_%=\n" \
"v_cmp_eq_u32 vcc, 0, v253\n" \
"s_cbranch_vccnz .Lt12_%=\n" \
"s_branch .Lr12_%=\n" \
".Ls16_%=:\n" \
"s_mov_b32 s87, 0x200000\n" \
"s_mov_b32 s86, 0x800c000\n" \
".Lt16_%=:\n" \
"s_sleep 3\n" \
"buffer_load_dword v253, v252, s[36:39], s86 offen sc1\n" \
"s_waitcnt vmcnt(0)\n" \
"s_sub_u32 s87, s87, 1\n" \
"s_cbranch_scc1 .Lr16_%=\n" \
"v_cmp_eq_u32 vcc, 0, v253\n" \
"s_cbranch_vccnz .Lt16_%=\n" \
"s_branch .Lr16_%=\n" \
".Ls110_%=:\n" \
"s_mov_b32 s87, 0x200000\n" \
"s_mov_b32 s86, 0x8014000\n" \
".Lt110_%=:\n" \
"s_sleep 3\n" \
"buffer_load_dword v253, v252, s[36:39], s86 offen sc1\n" \
"s_waitcnt vmcnt(0)\n" \
"s_sub_u32 s87, s87, 1\n" \
"s_cbranch_scc1 .Lr110_%=\n" \
"v_cmp_eq_u32 vcc, 0, v253\n" \
"s_cbranch_vccnz .Lt110_%=\n" \
"s_branch .Lr110_%=\n" \
""
#define CLOBBERS "v0", "v1", "v2", "v3", "v4", "v5", "v6", "v7", "v8", "v9", "v10", "v11", "v12", "v13", "v14", "v15", "v16", "v17", "v18", "v19", "v20", "v21", "v22", "v23", "v24", "v25", "v26", "v27", "v28", "v29", "v30", "v31", "v32", "v33", "v34", "v35", "v36", "v37", "v38", "v39", "v40", "v41", "v42", "v43", "v44", "v45", "v46", "v47", "v48", "v49", "v50", "v51", "v52", "v53", "v54", "v55", "v56", "v57", "v58", "v59", "v60", "v61", "v62", "v63", "v64", "v65", "v66", "v67", "v68", "v69", "v70", "v71", "v84", "v85", "v86", "v87", "v88", "v89", "v90", "v91", "v92", "v93", "v94", "v95", "v120", "v121", "v122", "v123", "v124", "v125", "v126", "v127", "v128", "v129", "v130", "v131", "v132", "v133", "v134", "v135", "v136", "v137", "v138", "v139", "v140", "v141", "v142", "v143", "v144", "v145", "v146", "v147", "v148", "v149", "v150", "v151", "v152", "v153", "v154", "v155", "v156", "v157", "v158", "v159", "v160", "v161", "v162", "v163", "v164", "v165", "v166", "v167", "v168", "v169", "v170", "v171", "v172", "v173", "v174", "v175", "v176", "v177", "v178", "v179", "v180", "v181", "v182", "v183", "v184", "v185", "v186", "v187", "v188", "v189", "v190", "v191", "v76", "v77", "v78", "v79", "v80", "v81", "v82", "v83", "v253", "v192", "v193", "v194", "v195", "s80", "s81", "s82", "s83", "s84", "s85", "s86", "s87", "s88", "s89", "s90", "s91", "s92", "s75", "s94", "s95"
template <int NM>
__device__ __forceinline__ void run1(Ctx& cx, const unsigned short* __restrict__ x, const unsigned short* __restrict__ w, RQ& rq) {
  unsigned xs;
  phase1<NM>(cx, x, w, rq, xs, rq.vb, [&]() __attribute__((always_inline)) {
  static_assert(NS - st_first(PG) == 3 && NH == 10 && NQW == 11 && RAWL == 110592, "constants of the generated code");
  const unsigned bid = cx.vb, idx = bid >> 3, slot = (bid & 7) * 4 + (idx >> 3), lane = cx.lane4 >> 2, h = (idx >> 2) & 1, wv = cx.wv;
  const unsigned lds0 = (unsigned)(uintptr_t)(lds_void*)cx.smem;
  unsigned* sa = rq.s, *sb = rq.s + 16;
  sa[0] = cx.ldg; sa[1] = cx.ldf; sa[2] = cx.sg; sa[3] = cx.sf;
#pragma unroll
  for (int j = 0; j < 4; ++j) {
    sa[4 + j] = ((bid + j) & 3) * 16384;
    sa[8 + j] = ((bid + j) & 3) * 256;
  }
  if (NM == 5) {
    sa[8] = lds0 + RAWL + 32768 + (wv & 1) * 8192 + (wv & 2) * 2048;
    sa[11] = (30 + (wv & 1)) * SBLK + (wv & 2) * 10240;
    const unsigned long long pace = wv == 7 ? (bid & 1 ? bytes(105, 100, 90, 88, 86, 84, 82) : bytes(150, 140, 120, 100, 90, 88, 86)) : 0ull;
    sa[9] = (unsigned)pace;
    sa[13] = (unsigned)(pace >> 32);
    sa[10] = lds0 + CORD + (wv & 3) * 1024;
  }
  sa[12] = min(slot, 1u) * 0xbf800000u;
  if (NM == 6) {
    const unsigned fg = WS_GRP - WS_S + (idx & 3) * 256 + h * 128;
    sa[13] = wv == 1 ? 255u : 0u;
    sa[14] = fg + slot * 4;
    sa[15] = wv == 0 ? fg + ((bid & 7) + (bid & 4)) * 1024 : 0x80000000u;
    sb[14] = slot * 4;
  }
#pragma unroll
  for (int n = 0; n < NH; ++n) {
    const unsigned blk = (xs & 255u) + n % 5;
    rq.s[n ? 15 + n : 31] = (xs & ~255u) + (n / 5) * (8 * SBLK) + (blk + (blk & 192u)) * 128u;
  }
#pragma unroll
  for (int m = 0; m < 5; ++m) {
    const unsigned t = NM == 5 ? (m + 1) % 5 : m;
    sb[9 + m] = t * (16 * 65536) + ((h * 10 + (wv >> 2) * 5 + (wv & 2) + t) & 2) * 4096;
  }
#pragma unroll
  for (int i = 0; i < 32; ++i) rq.s[i] = __builtin_amdgcn_readfirstlane(rq.s[i]);
  const unsigned prd = cx.prd + (RAWL - RAWB);
  const unsigned yo = (h * NBLK + (wv >> 2) * 80 + (lane & 14)) * 65536 + (idx & 3) * 32768 + (wv & 1) * 16384 + slot * 256 + (lane & 1) * 64 + (lane >> 4) * 16;
  unsigned* v = rq.v;
  v[0] = cx.voff; v[1] = cx.voff5; v[2] = cx.poff; v[3] = cx.xo; v[4] = prd; v[5] = prd ^ 32u; v[6] = prd ^ 64u; v[7] = prd ^ 96u;
  v[8] = cx.xbase; v[9] = cx.wbase; v[10] = cx.wbase ^ 32u; v[11] = cx.fvo; v[12] = lane * 16; v[13] = yo; v[14] = cx.pwr + (RAWL - RAWB);
  if (NM == 6) {
    v[15] = cx.crd;
    v[16] = CBUF + min(lane & 15, 10u) * 272 + wv * 64 + (cx.lane4 >> 6) * 16;
    v[17] = CORD + lane * 16;
    v[18] = (bid & 7) * 1024 + (idx & 3) * 256 + lane * 4;
  } else {
    const unsigned co = (((bid >> 3) & 3) * 2 + ((bid >> 2) & 1)) * 128u + (cx.lane4 & 127u);
    const unsigned long long p = (unsigned long long)((const char*)(cx.lane4 < 128 ? cx.gamma : cx.beta) + co);
    v[15] = 0u;
    v[16] = (unsigned)p;
    v[17] = (unsigned)(p >> 32);
    v[18] = cx.fvo;
  }
#pragma unroll
  for (int i = 0; i < 32; ++i) asm volatile("" : "+s"(rq.s[i]));
#pragma unroll
  for (int i = 0; i < 19; ++i) asm volatile("" : "+v"(rq.v[i]));
  SB0;
  });
}

template <int CTRL>
__device__ __forceinline__ float bfly(float a, float b, bool hi) {
  const float give = hi ? a : b, keep = hi ? b : a;
  return keep + __builtin_bit_cast(float, __builtin_amdgcn_update_dpp(0, __builtin_bit_cast(int, give), CTRL, 0xf, 0xf, false));
}
__device__ __forceinline__ float halves_sum(float v) {
  const auto r = __builtin_amdgcn_permlane32_swap(__float_as_uint(v), __float_as_uint(v), false, false);
  return __uint_as_float(r[0]) + __uint_as_float(r[1]);
}

extern "C" __global__ __launch_bounds__(512, 2) void conv_fft(char* ws, const unsigned short* __restrict__ x,
                                                   const unsigned short* __restrict__ w,
                                                   const unsigned short* __restrict__ gamma,
                                                   const unsigned short* __restrict__ beta,
                                                   unsigned short* __restrict__ out) {
  __shared__ __attribute__((aligned(16))) char smem[160 * 1024];
  const unsigned lds0 = (unsigned)(uintptr_t)(lds_void*)smem;
  const int wv = __builtin_amdgcn_readfirstlane(threadIdx.x >> 6);
  Ctx cx;
  {
    unsigned len;
    asm volatile("s_getpc_b64 %0\n.Lcf_a:\n\ts_mov_b32 %1, .Lcf_b-.Lcf_a" : "=s"(cx.pc), "=s"(len));
    cx.wv = wv;
    cx.pfo = len;
    unsigned xcc;
    asm volatile("s_getreg_b32 %0, hwreg(HW_REG_XCC_ID, 0, 3)" : "=s"(xcc));
    unsigned id = blockIdx.x;
    asm volatile("" : "+s"(id));
    cx.id = id;
    cx.vb = (int)((id & 248u) | (id & 256u ? (id >> 10) & 7u : xcc));
    unsigned xa = x_addr(cx.vb, 0, wv), xo = ((cx.vb >> 2) & 1) * (T_ * D_ * 2), l1 = lds0 + wv * 1024;
    asm volatile("" : "+v"(xa));
    asm volatile("" : "+s"(xo), "+s"(l1));
    SB0;
    const char* pb = ws + WS_PUB;
    u32x4 sx, sw;
    if (((id | wv) == 0)) {
      const unsigned l = threadIdx.x;
      const u32x4 z = {0u, 0u, 0u, 0u};
      unsigned d;
      asm volatile("s_load_dword %0, %1, 0x8080" : "=s"(d) : "s"(pb) : "memory");
      {
#pragma unroll
        for (int k = 0; k < 2; ++k)
          asm volatile("global_store_dwordx4 %0, %1, %2 sc1" :: "v"(32768 + (l & 7) * 16 + (l & 8) * 512 + (l & 16) * 1024 + (l & 32) * 32 + k * 8192), "v"(z), "s"(pb) : "memory");
      }
      {
        const unsigned so = l * (31u * 1024u + 16u) + (4u << 20);
        asm volatile("global_store_dwordx4 %2, %3, %4 sc1\n\tglobal_load_dwordx4 %0, %5, %6\n\tglobal_load_dwordx4 %1, %7, %8"
                     : "=&v"(sx), "=&v"(sw) : "v"(l * 16 + 1024), "v"(z), "s"(pb), "v"(so + (l & 3u) * (8u << 20)), "s"(x), "v"(so + (l & 1u) * (6u << 20)), "s"(w) : "memory");
      }
      asm volatile("s_waitcnt lgkmcnt(0)" : "+s"(d));
    }
    const u32x4 rx = make_rsrc((const char*)x + xo, T_ * D_ * 2);
    if (id | wv) issue_x(0, 0, xa, rx, l1, 0, 2);
    if (id) code_touch(cx, 0, 1 & 1 ? 1 : 2);
    SB0;
    if (id | wv) issue_x(0, wv, xa, rx, lds0, 2, 9);
    if (id == 0 && wv) {
      const unsigned x0 = x_addr(cx.vb, 0, 0);
      DMA16("nt", rx, lds0 + (wv - 1) * 8192, x0, (wv - 1) * 65536u);
      if (wv < 3) DMA16("nt", rx, lds0 + (wv + 6) * 8192, x0, (wv + 6) * 65536u);
    }
    SB0;
    if (((id | wv) == 0)) {
      wait_vm<0>();
      asm volatile("" : "+v"(sx), "+v"(sw));
      unsigned m = expo(amax(amax(amax(amax(0, sx[0]), sx[1]), sx[2]), sx[3])) | expo(amax(amax(amax(amax(0, sw[0]), sw[1]), sw[2]), sw[3])) << 16;
      m = amax(m, (unsigned)__builtin_amdgcn_update_dpp(0, (int)m, 0x128, 0xf, 0xf, false));
      m = amax(m, (unsigned)__builtin_amdgcn_update_dpp(0, (int)m, 0x124, 0xf, 0xf, false));
      m = amax(m, (unsigned)__builtin_amdgcn_update_dpp(0, (int)m, 0x122, 0xf, 0xf, false));
      m = amax(m, (unsigned)__builtin_amdgcn_update_dpp(0, (int)m, 0x121, 0xf, 0xf, false));
      unsigned ex = 28, ew = 28;
#pragma unroll
      for (int l = 0; l < 64; l += 16) {
        const unsigned v = (unsigned)__builtin_amdgcn_readlane((int)m, l);
        ex = max(ex + 2, v & 255u) - 2;
        ew = max(ew + 3, v >> 16) - 3;
      }
      const unsigned long long tg = __builtin_amdgcn_s_memrealtime();
      const unsigned lo = (unsigned)tg << 4 | xcc << 1, hi = ((unsigned)(tg >> 28) & 0xffffu) | min(ex, 220u) << 16 | min(ew, 220u) << 24;
      asm volatile("global_store_dwordx4 %0, %1, %2 sc1\n\ts_nop 1" :: "v"(threadIdx.x * 16), "v"(u32x4{lo, hi, ~lo, ~hi}), "s"(pb) : "memory");
      unsigned g[2] = {~0u, ~0u};
#pragma unroll 1
      for (int k = 0; k < 10 && ((k & 1) || min(g[0], g[1]) > 520u); ++k) {
        unsigned long long t0, t1;
        unsigned d;
        asm volatile("s_memtime %0\n\ts_waitcnt lgkmcnt(0)\n\ts_load_dword %2, %3, 0x0\n\ts_waitcnt lgkmcnt(0)\n\ts_memtime %1\n\ts_waitcnt lgkmcnt(0)"
                     : "=&s"(t0), "=&s"(t1), "=&s"(d) : "s"(pb + 32768 + (k & 1) * 8192 + (k & 2) * 2048 + (k & 4) * 4096 + (k & 8) * 128) : "memory");
        if (k & 1) g[1] = min(g[1], (unsigned)(t1 - t0)); else g[0] = min(g[0], (unsigned)(t1 - t0));
      }
      const unsigned ln = lo | (((g[0] < g[1] ? 0u : 1u) ^ (cx.vb >> 2)) & 1u);
      asm volatile("global_store_dwordx4 %0, %1, %2 sc1\n\ts_nop 1" :: "v"(threadIdx.x * 16 + 2048), "v"(u32x4{ln, hi, ln, hi}), "s"(pb) : "memory");
    }
  }
  cx.smem = smem;
  cx.ws = ws;
  cx.out = (const char*)out;
  cx.gamma = gamma;
  cx.beta = beta;
  bf16x8 gb[8];
  unsigned fv;
  {
    RQ rq;
    if (wv < 4) run1<6>(cx, x, w, rq); else run1<5>(cx, x, w, rq);
    __builtin_amdgcn_s_barrier();
    asm volatile("s_cmp_gt_u32 %[wv], 3\n\ts_cbranch_scc1 .Lcf_1\n" LOOP0 ".Lcf_1:\n" LOOP1 ".Lcf_e:"
      :
        "+{v[72:75]}"(rq.h[0]), "+{v232}"(rq.v[0]), "+{v233}"(rq.v[1]), "+{v234}"(rq.v[2]),
        "+{v235}"(rq.v[3]), "+{v236}"(rq.v[4]), "+{v237}"(rq.v[5]), "+{v238}"(rq.v[6]), "+{v239}"(rq.v[7]), "+{v[196:199]}"(rq.h[1]),
        "+{v[200:203]}"(rq.h[2]), "+{v[204:207]}"(rq.h[3]), "+{v[96:97]}"(rq.raw[0]), "+{v[98:99]}"(rq.raw[1]), "+{v[100:101]}"(rq.raw[2]),
        "+{v[102:103]}"(rq.raw[3]), "+{v[104:105]}"(rq.raw[4]), "+{v[106:107]}"(rq.raw[5]), "+{v[108:109]}"(rq.raw[6]), "+{v[110:111]}"(rq.raw[7]),
        "+{v[112:113]}"(rq.raw[8]), "+{v[114:115]}"(rq.raw[9]), "+{v[116:117]}"(rq.raw[10]), "+{v241}"(rq.v[9]), "+{v243}"(rq.v[11]), "+{v244}"(rq.v[12]), "+{v252}"(rq.v[18]), "+{v[254:255]}"(rq.vb),
        "+{v[208:211]}"(rq.h[4]), "+{v[212:215]}"(rq.h[5]), "+{v[216:219]}"(rq.h[6]), "+{v[220:223]}"(rq.h[7]), "+{v[224:227]}"(rq.h[8]), "+{v[228:231]}"(rq.h[9]),
        "+{v248}"(rq.v[16]), "+{v249}"(rq.v[17]), "+{v246}"(rq.v[14]), "+{v247}"(rq.v[15])
      :
        "{v240}"(rq.v[8]), "{v242}"(rq.v[10]), "{v245}"(rq.v[13]),

            "{s44}"(rq.s[0]), "{s45}"(rq.s[1]),
        "{s46}"(rq.s[2]), "{s47}"(rq.s[3]), "{s48}"(rq.s[4]), "{s49}"(rq.s[5]), "{s50}"(rq.s[6]), "{s51}"(rq.s[7]), "{s52}"(rq.s[8]), "{s53}"(rq.s[9]),
        "{s54}"(rq.s[10]), "{s55}"(rq.s[11]), "{s56}"(rq.s[12]), "{s57}"(rq.s[13]), "{s58}"(rq.s[14]), "{s59}"(rq.s[15]), "{s60}"(rq.s[16]),
        "{s61}"(rq.s[17]), "{s62}"(rq.s[18]), "{s63}"(rq.s[19]), "{s64}"(rq.s[20]), "{s65}"(rq.s[21]), "{s66}"(rq.s[22]), "{s67}"(rq.s[23]),
        "{s68}"(rq.s[24]), "{s69}"(rq.s[25]), "{s70}"(rq.s[26]), "{s71}"(rq.s[27]), "{s72}"(rq.s[28]), "{s73}"(rq.s[29]), "{s74}"(rq.s[30]), "{s93}"(rq.s[31]),
        "{s[36:39]}"(cx.rs), "{s[40:43]}"(make_rsrc(cx.ws + WS_Y, ROWS * 65536)), "{s[76:77]}"(&TAB.G[0][0][0][0]), [wv] "s"(wv)
      : "memory", "vcc", "scc", "v118", "v119", CLOBBERS);
    fv = rq.v[17];
  }
  ws = cx.ws;
  const int bid = cx.vb, idx = bid >> 3;
  const int slot = (bid & 7) * 4 + (idx >> 3), b = (idx >> 2) & 1, nt = idx & 3;
  const int lane = (int)__builtin_amdgcn_mbcnt_hi(~0u, __builtin_amdgcn_mbcnt_lo(~0u, 0u));
  const int c64i = nt * 2 + ((bid >> 2) & 1), rg = (bid & 1) * 8 + (slot & 1) * 4 + (bid & 2) + (idx >> 4);
  const int hw = wv >> 2, wp = hw ? 7 - wv : wv, cg = c64i * 2 + hw;
  const int bs = 10 * rg;
  const int ntk = wp < 2 ? 3 : 2;
  const int g = lane >> 4, qq = (lane >> 2) & 3, pp = lane & 3, fr = lane & 15;
  const int ch0 = cg * 32 + 8 * g;
  const unsigned cnt = lds0 + 8 * 12288 + 3072 + hw * 8;
  if (wv == 2) asm volatile("ds_write_b32 %0, %1\n\ts_waitcnt lgkmcnt(0)" :: "v"(cnt + (lane & 3) * 4), "v"(0u) : "memory");
#pragma unroll
  for (int i = 0; i < 8; ++i) gb[i] = *reinterpret_cast<const bf16x8*>(smem + i * 1024 + lane * 16);
  const int be_ = fr < 4 ? 2 * wp + (fr >> 1) : 8 + wp;
  const int l0 = HOP * (bs + 2 * wp) + fr, l2 = HOP * (8 - wp), le = (fr >> 1) < ntk ? HOP * (bs + be_) + 48 + (fr & 1) : 1 << 20;
  const int lend = min(HOP * (bs + 10), L_);
  f32x4 nk, rk;
  float rn = 1.0f / (float)(lend - HOP * bs);
#pragma unroll
  for (int k = 0; k < 4; ++k) {
    nk[k] = (float)(2 * HOP + min(max(lend - HOP * (bs + k + 8), 0), HOP));
    rk[k] = 1.0f / nk[k];
  }
  asm volatile("" : "+v"(nk), "+v"(rk), "+v"(rn));
  f32x4 cin[2];
#pragma unroll
    for (int s = 0; s < 2; ++s) {
      cin[s] = *reinterpret_cast<const f32x4*>(smem + CBUF + min(be_, 10) * 272 + hw * 128 + g * 32 + s * 16);
#pragma unroll
      for (int r = 0; r < 4; ++r) cin[s][r] = fr & 1 ? __builtin_amdgcn_ldexpf(cin[s][r], cx.es) : 0.f;
    }
    asm volatile("" : "+v"(cin[0]), "+v"(cin[1]));
#pragma unroll
  for (int i = 0; i < 8; i += 4) asm volatile("" : "+v"(gb[i]), "+v"(gb[i + 1]), "+v"(gb[i + 2]), "+v"(gb[i + 3]));
  SB0;
  unsigned so[2], vo[5];
#pragma unroll
  for (int j = 0; j < 4; ++j) vo[j] = (unsigned)((lane >> 2) * 128 + (((lane & 3) ^ ((lane >> 3) & 2)) << 4) + j * 1024);
  vo[4] = (unsigned)((lane ^ ((lane >> 3) & 6)) << 4);
#pragma unroll
  for (int i = 0; i < 2; ++i) {
    const unsigned yr = (unsigned)(b * NBLK + bs + (i ? 8 + wp : 2 * wp));
    so[i] = i + 2 <= ntk ? (yr & ~1u) * 65536 + (yr & 1) * 64 + nt * 32768 + hw * 16384 + ((c64i ^ (yr >> 5)) & 1) * 8192 : 0xf0000000u;
  }
  asm volatile("" : "+s"(so[0]), "+s"(so[1]));
  asm volatile("s_waitcnt vmcnt(0)" : "+v"(fv), "+v"(vo[0]), "+v"(vo[1]), "+v"(vo[2]), "+v"(vo[3]), "+v"(vo[4]) :: "memory");
  if (wv == 0 && !__all(fv != 0u)) spin_flags(ws + WS_GRP + nt * 256 + b * 128 + ((bid & 7) + (bid & 4)) * 1024 + ((lane & 31) ^ ((lane & 31) == slot)) * 4);
  __builtin_amdgcn_s_barrier();
  char* my = smem + wv * 12288;
  if (1) { if (1 & 4 && !hw) __builtin_amdgcn_s_setprio(3); else __builtin_amdgcn_s_setprio(2); }
  if (24 && hw) __builtin_amdgcn_s_sleep(24);
  if (12 && (nt & 1)) __builtin_amdgcn_s_sleep(12);
  if (0 && b) __builtin_amdgcn_s_sleep(0);
  {
    const u32x4 rs = make_rsrc(ws + WS_Y, ROWS * 65536);
#pragma unroll
    for (int i = 0; i < 3; ++i)
      asm volatile("s_mov_b32 m0, %0\n\ts_nop 0\n\tbuffer_load_dwordx4 %1, %2, %3 offen sc1 lds\n\tbuffer_load_dwordx4 %4, %2, %3 offen offset:1024 sc1 lds\n\t"
                   "buffer_load_dwordx4 %5, %2, %3 offen offset:2048 sc1 lds\n\tbuffer_load_dwordx4 %6, %2, %3 offen offset:3072 sc1 lds"
                   :: "s"(lds0 + wv * 12288 + i * 4096), "v"(vo[i < 2 ? 4 : 0]), "s"(rs), "s"(i < 2 ? so[0] + i * 4096 : so[1]), "v"(vo[i < 2 ? 4 : 1]), "v"(vo[i < 2 ? 4 : 2]),
                      "v"(vo[i < 2 ? 4 : 3]) : "memory");
  }
  f32x4 y[24], npv[2];
  float s1[8], s2[8];
  {
    bf16x8 a[12];
    unsigned sp = wv * 12288 + (4 * g + qq) * 128 + (((2 * g + (qq >> 1)) & 3) << 5) + pp * 8, st = wv * 12288 + 8192 + (4 * g + qq) * 64 + ((g & 1) << 5) + pp * 8;
    auto rd = [&](int q) __attribute__((always_inline)) {
      const char* p = smem + (q < 8 ? (sp ^ (unsigned)((q & 6) << 4)) + (q & 1) * 4096 : (st ^ (unsigned)((q & 2) << 4)) + (q & 1) * 2048);
      a[q] = cat(tr_read(p), tr_read(p + (q < 8 ? 2048 : 1024)));
    };
    auto sums = [&](int cp, int q) __attribute__((always_inline)) {
      const int e = (cp & 4) + q;
      if ((cp & 3) == 3) {
        if (cp >= 8) y[cp & 7][q] = (fr >> 1) == (cp >> 3) ? y[cp][q] : y[cp & 7][q];
        return;
      }
      float d = y[cp][q];
      if (cp >= 16 && l0 + l2 + 16 * (cp & 3) >= lend) d = 0.f;
      if (cp == 0 || cp == 4) { s1[e] = d; s2[e] = d * d; }
      else { s1[e] += d; s2[e] = __builtin_fmaf(d, d, s2[e]); }
      asm volatile("" : "+v"(s1[e]), "+v"(s2[e]));
    };
    unsigned sv = wv * 12288 + g * 8;
    asm volatile("" : "+v"(sp), "+v"(st), "+v"(sv));
    wait_vm<8>();
    SB0;
    u32x2 pv[2];
#pragma unroll
    for (int s = 0; s < 2; ++s) pv[s] = *reinterpret_cast<const u32x2*>(smem + sv + s * 32);
    SB0;
#pragma unroll
    for (int q = 0; q < 8; q += 2) { rd(q); SB0; }
#pragma unroll
    for (int s = 0; s < 2; ++s) {
      npv[s] = f32x4{bf_lo(pv[s][0]), bf_hi(pv[s][0]), bf_lo(pv[s][1]), bf_hi(pv[s][1])} * (-1.0f / 64.0f);
      cin[s] += npv[s];
    }
    SB0;
#pragma unroll
    for (int c = 0; c < 16; ++c) {
      if (c == 8) wait_vm<4>();
      y[c] = MFMA(a[2 * (c >> 2)], gb[2 * (c & 3)], (c & 3) == 3 ? cin[(c >> 2) & 1] : npv[(c >> 2) & 1]);
      SB0;
      if (c >= 8 && c < 12) rd(2 * (c - 8) + 1);
      SB0;
    }
    auto chain = [&](auto cc) __attribute__((always_inline)) {
      constexpr int c = cc.v, is = c >> 2, tt = c & 3;
      if (c == 14) wait_vm<0>();
#pragma unroll
      for (int q = 0; q < 4; ++q) {
        if (c < 24 && q == (c < 16 ? 2 : q & 2)) y[c % 24] = MFMA(a[(2 * is + (q >> 1)) % 12], gb[2 * tt + (q >> 1)], q ? y[c % 24] : tt == 3 ? cin[is & 1] : npv[is & 1]);
        SB0;
        if (c == 14 || c == 15) { if (q & 1) rd(8 + 2 * (c - 14) + (q >> 1)); }
        if (c >= 2) sums(c - 2, q);
        SB0;
      }
    };
    sfor<0, 16>(chain);
    if (ntk == 3) {
      sfor<16, 26>(chain);
    } else {
#pragma unroll
      for (int q = 0; q < 8; ++q) sums(14 + (q >> 2), q & 3);
    }
#pragma unroll
    for (int e = 0; e < 8; ++e) {
      const float d = le < lend ? y[3 + (e & 4)][e & 3] : 0.f;
      s1[e] += d;
      s2[e] = __builtin_fmaf(d, d, s2[e]);
    }
  }
  float* red = reinterpret_cast<float*>(smem + 8 * 12288);
  {
    float v[8];
#pragma unroll
    for (int j = 0; j < 8; ++j) v[j] = bfly<0x140>(s1[j], s2[j], fr & 8);
#pragma unroll
    for (int j = 0; j < 4; ++j) v[j] = bfly<0x141>(v[j], v[j + 4], fr & 4);
#pragma unroll
    for (int j = 0; j < 2; ++j) v[j] = bfly<0x4E>(v[j], v[j + 2], fr & 2);
    red[((fr >> 3) * 8 + 2 * wp + hw) * 32 + 8 * g + (fr & 7)] = bfly<0xB1>(v[0], v[1], fr & 1);
    if (fr < 2) *reinterpret_cast<f32x4*>(red + (16 + 2 * wp + hw) * 32 + 8 * g + 4 * fr) = fr ? npv[1] : npv[0];
  }
  unsigned arr = 1u;
  if (lane == 0) asm volatile("ds_add_rtn_u32 %0, %1, %0\n\ts_waitcnt lgkmcnt(0)" : "+v"(arr) : "v"(cnt) : "memory");
  unsigned long long* stats = reinterpret_cast<unsigned long long*>(ws + WS_STATS);
  if (__builtin_amdgcn_readfirstlane(arr) == 3u && lane < 32) {
    const int tid = hw * 32 + lane;
    float mk[4], sm = 0.f, m2 = 0.f;
#pragma unroll
    for (int k = 0; k < 4; ++k) {
      const float* e = red + 64 * k + tid;
      const float dm = e[0] * rk[k];
      mk[k] = dm - e[512];
      m2 += fmaxf(e[256] - e[0] * dm, 0.f);
      sm += nk[k] * mk[k];
    }
    const float mean = sm * rn;
#pragma unroll
    for (int k = 0; k < 4; ++k) m2 += nk[k] * (mk[k] - mean) * (mk[k] - mean);
    unsigned long long pk = (unsigned long long)__float_as_uint(mean) | ((unsigned long long)__float_as_uint(m2) << 32);
    if (pk == ~0ull) pk = ~1ull;
    __hip_atomic_store((gu64*)(stats + (b * 16 + rg + (rg & 12) + (c64i & 1) * 4 + b * 16) * 256 + nt * 64 + tid), pk, RLX_AGENT);
  }
  constexpr float GS = -0.4686686930532063f, GA = -2.0f * 0.7978845608028654f * 1.4426950408889634f / GS;
  const unsigned short* gp = reinterpret_cast<const unsigned short*>(smem + GAM + hw * 64) + (lane & 31);
  float gs = bf_lo(gp[0]) * GS, bt = bf_lo(gp[64]) * GS, eps = fmaxf(__builtin_amdgcn_ldexpf(1e-5f, 2 * cx.es), 1e-30f);
  float cl = lane < 32 ? 0.f : -15.f / 500.f;
  asm volatile("" : "+v"(gs), "+v"(bt), "+v"(eps), "+v"(cl));
  unsigned long long* gl = reinterpret_cast<unsigned long long*>(smem + 8 * 12288 + 4096);
  {
    unsigned long long pk[2];
    const int row = 4 * wp + (lane >> 5), col = hw * 32 + (lane & 31);
    for (unsigned spins = 0;; ++spins) {
#pragma unroll
      for (int r = 0; r < 2; ++r)
        pk[r] = __hip_atomic_load((gu64*)(stats + (b * 32 + row + 2 * r + 4 * wp + (c64i & 1) * 4) * 256 + nt * 64 + col), RLX_AGENT);
      if (__all(pk[0] != ~0ull && pk[1] != ~0ull) || spins > (1u << 20)) break;
      if (1) __builtin_amdgcn_s_sleep(1);
    }
    gl[row * 66 + col] = pk[0];
    gl[(row + 2) * 66 + col] = pk[1];
    if (lane == 0) asm volatile("ds_add_u32 %0, %1" :: "v"(cnt + 4), "v"(1u) : "memory");
    unsigned c;
    do asm volatile("ds_read_b32 %0, %1\n\ts_waitcnt lgkmcnt(0)" : "=v"(c) : "v"(cnt + 4) : "memory");
    while (__builtin_amdgcn_readfirstlane(c) < 4u);
  }
  f32x4 ga[2], be[2];
  {
    const f32x2* gr = reinterpret_cast<const f32x2*>(gl + hw * 32 + (lane & 31));
    const f32x2 p = {gr[0][0], 0.f};
    f32x2 d, sm = {0.f, 0.f};
    float q = 0.f;
#pragma unroll
    for (int k = 0; k < 8; ++k) {
      d = gr[((lane >> 5) * 8 + k) * 66] - p;
      sm += d;
      q = __builtin_fmaf(d[0], d[0], q);
    }
    const float dl = cl * d[0];
    const float dm = halves_sum(sm[0] + dl) * (500.f / L_), var = halves_sum(sm[1] + 500.f * __builtin_fmaf(dl, d[0], q)) * (1.f / L_) - dm * dm;
    gs *= __builtin_amdgcn_rsqf(fmaxf(var, 0.f) + eps);
    float* am = reinterpret_cast<float*>(my);
    am[lane] = lane < 32 ? gs : bt - (p[0] + dm) * gs;
#pragma unroll
    for (int s = 0; s < 2; ++s) {
      ga[s] = *reinterpret_cast<const f32x4*>(am + 8 * g + 4 * s);
      be[s] = *reinterpret_cast<const f32x4*>(am + 32 + 8 * g + 4 * s) - npv[s] * ga[s];
    }
  }
  {
    if (1) __builtin_amdgcn_s_setprio(0);
    f32x4 k1 = {GA, GA, GA, GA}, k2 = {GS, GS, GS, GS};
    asm volatile("" : "+v"(k1), "+v"(k2));
    const u32x4 ro = make_rsrc(out + (size_t)b * L_ * D_, lend * (D_ * 2));
    const unsigned oo = (unsigned)((l0 * D_ + ch0) * 2);
#pragma unroll
    for (int i = 0; i < 3; ++i)
      if (i < 2 || ntk == 3)
#pragma unroll
      for (int tt = 0; tt < 4; ++tt) {
        if (tt == 3 && i) break;
        f32x4 o[2];
#pragma unroll
        for (int s = 0; s < 2; ++s) {
          const f32x4 z = y[8 * i + 4 * s + tt] * ga[s] + be[s];
          const f32x4 a = z * (z * z + k1);
          f32x4 e;
#pragma unroll
          for (int r = 0; r < 4; ++r) e[r] = __builtin_amdgcn_exp2f(a[r]);
          e = e * k2 + k2;
#pragma unroll
          for (int r = 0; r < 4; ++r) e[r] = __builtin_amdgcn_rcpf(e[r]);
          o[s] = z * e;
        }
        st16(pack8(o[0], o[1]), ro, tt < 3 ? oo : (unsigned)((le * D_ + ch0) * 2), tt < 3 ? ((i < 2 ? HOP * i : l2) + 16 * tt) * (D_ * 2) : 0);
      }
  }
  asm volatile(".Lcf_b:\n\ts_nop 0" ::: "memory");
}

extern "C" void kernel_launch(void* const* d_in, const int* in_sizes, int n_in,
                              void* d_out, int out_size, void* d_ws, size_t ws_size,
                              hipStream_t stream) {
  const unsigned short* x = (const unsigned short*)d_in[0];
  const unsigned short* w = (const unsigned short*)d_in[1];
  const unsigned short* gamma = (const unsigned short*)d_in[2];
  const unsigned short* beta = (const unsigned short*)d_in[3];
  char* ws = (char*)(((uintptr_t)d_ws + (2u << 20) - 1) & ~(uintptr_t)((2u << 20) - 1));
  conv_fft<<<256, 512, 0, stream>>>(ws, x, w, gamma, beta, (unsigned short*)d_out);
}
```
